```python
import torch

def _softmax(x, axis=-1):
    return torch.softmax(x, dim=axis)

def _gelu(x):
    return torch.nn.functional.gelu(x, approximate='tanh')

def _silu(x):
    return torch.nn.functional.silu(x)

def _sigmoid(x):
    return torch.sigmoid(x)

def _layernorm(x, axis=-1, eps=1e-05):
    m = x.mean(dim=axis, keepdim=True)
    v = x.var(dim=axis, keepdim=True, unbiased=False)
    return (x - m) / torch.sqrt(v + eps)

def _scatter_add(tgt, idx, src, axis=0):
    return tgt.clone().index_add_(axis, idx, src)

def _segment_sum(src, seg, n):
    out = torch.zeros((n,) + src.shape[1:], dtype=src.dtype)
    return out.index_add_(0, seg, src)

def _pad(x, pw):
    flat = tuple((v for pair in reversed(pw) for v in pair))
    return torch.nn.functional.pad(x, flat)
kernel_name = 'concat_heads__ssm_conv_h3__memory_aug_recurrent_mem_tr__prompt'

def setup_inputs():

    _w = lambda a: (lambda t: t.to(torch.bfloat16) if t.is_floating_point() else t)(torch.as_tensor(a))

    def _A_setup():
        B, S, D = (1, 2048, 1024)
        ks = 4
        return {'q': _w((lambda *s: torch.randn(*s, dtype=torch.bfloat16) * 0.05)(B, S, D)), 'k': _w((lambda *s: torch.randn(*s, dtype=torch.bfloat16) * 0.4)(B, S, D)), 'v': _w((lambda *s: torch.randn(*s, dtype=torch.bfloat16) * 0.4)(B, S, D)), 'shift_k': _w((lambda *s: torch.randn(*s, dtype=torch.bfloat16) * 0.4)(D, ks) * 0.5), 'ssm_k': _w((lambda *s: torch.randn(*s, dtype=torch.bfloat16) * 0.4)(D, S) * (1.0 / S ** 0.5)), 'ssm_D': _w((lambda *s: torch.randn(*s, dtype=torch.bfloat16) * 0.4)(D))}
    _A_in = _A_setup()

    def _B_setup():
        B, S, Dm = (1, 2048, 1024)
        H, Dh = (32, 128)
        M, seg = (8, 32)
        seg = min(seg, S)
        S = S // seg * seg
        return {'x': _w((lambda *s: torch.randn(*s, dtype=torch.bfloat16) * 0.05)(B, S, Dm)), 'mem0': _w((lambda *s: torch.randn(*s, dtype=torch.bfloat16) * 0.4)(B, M, Dm)), 'Wq': _w((lambda *s: torch.randn(*s, dtype=torch.bfloat16) * 6.4)(Dm, H, Dh) * Dm ** (-0.5)), 'Wk': _w((lambda *s: torch.randn(*s, dtype=torch.bfloat16) * 6.4)(Dm, H, Dh) * Dm ** (-0.5)), 'Wv': _w((lambda *s: torch.randn(*s, dtype=torch.bfloat16) * 0.4)(Dm, H, Dh) * Dm ** (-0.5)), 'Wo': _w((lambda *s: torch.randn(*s, dtype=torch.bfloat16) * 0.4)(H, Dh, Dm) * (H * Dh) ** (-0.5)), 'seg_len': seg}
    _B_in = _B_setup()
    return {'A_q': _A_in['q'], 'A_k': _A_in['k'], 'A_v': _A_in['v'], 'A_shift_k': _A_in['shift_k'], 'A_ssm_k': _A_in['ssm_k'], 'A_ssm_D': _A_in['ssm_D'], 'B_x': _B_in['x'], 'B_mem0': _B_in['mem0'], 'B_Wq': _B_in['Wq'], 'B_Wk': _B_in['Wk'], 'B_Wv': _B_in['Wv'], 'B_Wo': _B_in['Wo'], 'B_seg_len': _B_in['seg_len']}

def reference(A_q, A_k, A_v, A_shift_k, A_ssm_k, A_ssm_D, B_x, B_mem0, B_Wq, B_Wk, B_Wv, B_Wo, B_seg_len):

    def _A_ref(q, k, v, shift_k, ssm_k, ssm_D):
        B, S, D = q.shape
        ks = shift_k.shape[-1]
        k_pad = _pad(k, ((0, 0), (ks - 1, 0), (0, 0)))
        k_shift = torch.zeros_like(k)
        for t in range(ks):
            k_shift = k_shift + k_pad[:, t:t + S, :] * shift_k[:, ks - 1 - t][None, None, :]
        kv = k_shift * v
        L = 2 * S
        kv_pad = _pad(kv, ((0, 0), (0, S), (0, 0)))
        kf_pad = _pad(ssm_k, ((0, 0), (0, S)))
        kv_f = (lambda x, axes=(-2, -1): torch.fft.rfft2(x.float(), dim=axes))(kv_pad[:, None, :, :], axes=(1, 2))
        kf_f = (lambda x, axes=(-2, -1): torch.fft.rfft2(x.float(), dim=axes))(kf_pad[:, None, :], axes=(1, 2))
        kf_f = (lambda x, *d: x.permute(*d))(kf_f, 1, 2, 0)
        y_f = kv_f * kf_f[None, :, :, :]
        y = (lambda x, s, axes=(-2, -1): torch.fft.irfft2(x, s=s, dim=axes).to(torch.bfloat16))(y_f, (1, L), axes=(1, 2))[:, 0, :S, :]
        y = y + kv * ssm_D[None, None, :]
        return q * y

    def _B_ref(x, mem0, Wq, Wk, Wv, Wo, seg_len):
        B, S, Dm = x.shape
        H, Dh = (Wq.shape[1], Wq.shape[2])
        M = mem0.shape[1]
        scale = Dh ** (-0.5)
        n_seg = S // seg_len
        L = M + seg_len
        idx = torch.arange(L)
        mask = (idx[:, None] < M) | (idx[:, None] >= idx[None, :])
        mem = mem0
        outs = []
        for s in range(n_seg):
            xs = x[:, s * seg_len:(s + 1) * seg_len, :].clone()
            h = torch.concatenate([mem, xs], axis=1)
            q = torch.einsum('btd,dhe->bhte', h, Wq)
            k = torch.einsum('btd,dhe->bhte', h, Wk)
            v = torch.einsum('btd,dhe->bhte', h, Wv)
            scores = torch.einsum('bhqe,bhke->bhqk', q, k) * scale
            scores = torch.where(mask[None, None], scores, -1000000000.0)
            p = _softmax(scores, axis=-1)
            o = torch.einsum('bhqk,bhke->bhqe', p, v)
            o = torch.einsum('bhte,hed->btd', o, Wo)
            mem = o[:, :M, :].clone()
            outs.append(o[:, M:, :])
        return torch.concatenate(outs, axis=1)
    a = _A_ref(A_q, A_k, A_v, A_shift_k, A_ssm_k, A_ssm_D)
    b = _B_ref(B_x, B_mem0, B_Wq, B_Wk, B_Wv, B_Wo, B_seg_len)
    return torch.concatenate([a, b], axis=-1)
```

```cpp
#include <hip/hip_runtime.h>
#include <hip/hip_bf16.h>
#include <stdint.h>
#include <type_traits>

constexpr int S_LEN = 2048;
constexpr int D_A   = 1024;
constexpr int DM    = 1024;
constexpr int NH    = 32;
constexpr int DH    = 128;
constexpr int HD    = NH * DH;
constexpr int MEMR  = 8;
constexpr int SEG   = 32;
constexpr int NSEG  = S_LEN / SEG;
constexpr int OUTW  = 2048;

typedef unsigned short u16;
typedef unsigned long long u64;
typedef short bf16x8 __attribute__((ext_vector_type(8)));
typedef short bf16x4 __attribute__((ext_vector_type(4)));
typedef float f32x4 __attribute__((ext_vector_type(4)));
typedef float f32x16 __attribute__((ext_vector_type(16)));
typedef unsigned u32x4 __attribute__((ext_vector_type(4)));
typedef unsigned u32x2 __attribute__((ext_vector_type(2)));
#define LAS __attribute__((address_space(3)))
#define GAS __attribute__((address_space(1)))

constexpr size_t MB = 1u << 20;
constexpr size_t OFF_QX  = 68 * MB;
constexpr size_t OFF_KX  = 84 * MB;
constexpr size_t OFF_VX  = 100 * MB;
constexpr size_t OFF_KVT = 116 * MB;
constexpr unsigned WS_USED = 120u << 20;
constexpr size_t PROF_AT = 0;

__device__ __forceinline__ float bf2f(u16 b) { return __uint_as_float(((unsigned)b) << 16); }
__device__ __forceinline__ u16 f2bf(float f) { __bf16 b = (__bf16)f; return __builtin_bit_cast(u16, b); }
__device__ __forceinline__ float rbf(float f) { return bf2f(f2bf(f)); }
__device__ __forceinline__ unsigned pk2(float a, float b) {
  typedef float f2 __attribute__((ext_vector_type(2)));
  typedef __bf16 b2 __attribute__((ext_vector_type(2)));
  f2 v = {a, b}; b2 r = __builtin_convertvector(v, b2); return __builtin_bit_cast(unsigned, r);
}

__device__ __forceinline__ int opaque_tid(int wv) {
  int l = __builtin_amdgcn_mbcnt_hi(~0u, __builtin_amdgcn_mbcnt_lo(~0u, 0u));
  asm volatile("" : "+v"(l));
  return wv * 64 + l;
}
__device__ __forceinline__ int swz_b(int row) { return ((row & 3) << 2) | ((row >> 2) & 3); }

#define KNOB(name, v) constexpr int name = v
KNOB(K_DEF, 80);
KNOB(W_STEP, 33); KNOB(W_OFF, 56);
KNOB(RHO, 3);
#define CALR 5
#define CALN 1
#define WRONG 0
#define OUTP "nt"
#define PREV 15
#define PDMA 1
#define PRD 1
#define XSW 1
#define P_EAG 3
#define FILL0 4
#define NOFL 0
#define NOFILL 0
#define XS_T 1000
#define XS_G 64
#define K_DLY 200
#define Q_DLY 450
#define U_STG 1
#define NTW 0
#define XS_N 32
#define U_AH 0
#define U_POS 11
#define F_SLP 0
#define A_DELAY 1500
#define LDNT(p) __builtin_nontemporal_load(p)
constexpr int CHK      = 16384;
constexpr int PAGE     = 2 << 20;
constexpr int SY_UM    = 0;
constexpr int SY_HOT   = 32 * PAGE;
constexpr int H_MEM    = 0;
constexpr int H_P      = 16 * CHK;
constexpr int H_ANN    = 28 * CHK;
constexpr int H_TR     = H_ANN + 4096;
constexpr int H_DLT    = 32 * CHK;
constexpr int H_FLG    = 33 * CHK;
constexpr int H_PX     = 40 * CHK;
constexpr int H_BYTES  = 72 * CHK;
static_assert(H_P == 0x40000 && H_ANN == 0x70000 && H_FLG == 0x84000, "generated text");
constexpr int SY_SET   = 33 * PAGE;
constexpr int SY_EPOCH = SY_SET + 128;
constexpr int SY_ABORT = SY_SET + 256;
constexpr int SY_SEL   = SY_ABORT + 128;
constexpr int SY_CT    = SY_ABORT + 640;
constexpr int CT_ARR = 0, CT_INIT = 8, CT_QUEUE = 9, CT_A1 = 10, CT_A2 = 18, CT_CAL = 26;
constexpr int CT_PR = CT_CAL + 33;
constexpr int SY_BYTES = SY_CT + (CT_PR + 128) * 128;
constexpr int URING = 16;
constexpr unsigned SENT = 0xFFFFFFFFu;
constexpr unsigned OOR = 0x7fffffffu;
constexpr unsigned SPIN_CAP = 1u << 19;
constexpr int NLF = 16;
constexpr int L_WTS = 0;
constexpr int L_MEM = L_WTS + 4 * NLF * 2048;
constexpr int L_XCH = L_MEM + 16384;
constexpr int L_XK  = L_XCH + 2048;
constexpr int L_KX  = L_XCH + 4096;
constexpr int L_PRF = L_KX + 8192;
constexpr int XRS = 40;
constexpr int CHAIN_LDS = 160 * 1024;
static_assert(L_XCH + 128 * XRS <= L_PRF && L_PRF + 4096 <= CHAIN_LDS, "lds");

typedef __bf16 bfv8 __attribute__((ext_vector_type(8)));
typedef __bf16 bfv16 __attribute__((ext_vector_type(16)));
typedef unsigned u32x8 __attribute__((ext_vector_type(8)));
typedef unsigned u32x12 __attribute__((ext_vector_type(12)));
typedef unsigned u32x16 __attribute__((ext_vector_type(16)));
typedef unsigned u32x32 __attribute__((ext_vector_type(32)));
struct Wts { u32x8 a[32]; u32x32 v[4]; };

#define STAMPL(i) do {} while (0)
#define PVAL(i, v) do {} while (0)
#define PDECL(base, str) do {} while (0)
#define PZERO do {} while (0)
#define PDUMP(mask) do {} while (0)
#define TA(x) do {} while (0)
#define TB(x) TS(x)
#define TS(x) do {} while (0)
#define SSTAMP(i) do {} while (0)
struct ChainArgs {
  u64* prof;
  const u16* mem0; const u16* Wq; const u16* Wk; const u16* Wv; const u16* Wo; const u16* X;
  u16* out; char* sync;
  const u16* Aq; const u16* Ak; const u16* Av; const u16* Ashift; const u16* Assm; const u16* AD; u16* kvT;
};
__device__ __forceinline__ GAS unsigned* ctr(const ChainArgs& a, int i) { return (GAS unsigned*)(a.sync + SY_CT + i * 128); }
__device__ __forceinline__ void ctr_add(const ChainArgs& a, int i, unsigned v) {
  __hip_atomic_fetch_add(ctr(a, i), v, __ATOMIC_RELAXED, __HIP_MEMORY_SCOPE_AGENT);
}

__device__ __forceinline__ __amdgpu_buffer_rsrc_t mk_rsrc(const void* p, unsigned bytes) {
  const u64 v = (u64)p;
  const unsigned lo = (unsigned)__builtin_amdgcn_readfirstlane((unsigned)v), hi = (unsigned)__builtin_amdgcn_readfirstlane((unsigned)(v >> 32));
  const u64 u = (u64)lo | ((u64)hi << 32);
  return __builtin_amdgcn_make_buffer_rsrc((void*)u, 0, bytes, 0x00020000);
}
__device__ __forceinline__ u32x4 own_rsrc(const void* p, unsigned bytes) {
  u32x4 r = {(unsigned)__builtin_amdgcn_readfirstlane((unsigned)(u64)p), (unsigned)__builtin_amdgcn_readfirstlane((unsigned)((u64)p >> 32)) & 0xffffu, bytes, 0x00020000u};
  asm volatile("s_nop 4" : "+s"(r));
  return r;
}
constexpr int LD_AUX = 16, ST_AUX = 16;
__device__ __forceinline__ u32x4 ld16(__amdgpu_buffer_rsrc_t rs, unsigned off) {
  return __builtin_amdgcn_raw_buffer_load_b128(rs, off, 0, LD_AUX);
}
__device__ __forceinline__ void st16(__amdgpu_buffer_rsrc_t rs, unsigned off, u32x4 v) {
  __builtin_amdgcn_raw_buffer_store_b128(v, rs, off, 0, ST_AUX);
}
__device__ __forceinline__ void st16s(u32x4 v, __amdgpu_buffer_rsrc_t rs, unsigned voff, unsigned soff) {
  __builtin_amdgcn_raw_buffer_store_b128(v, rs, voff, soff, ST_AUX);
  asm volatile("s_nop 1" :: "v"(v) : "memory");
}
__device__ __forceinline__ void st8(__amdgpu_buffer_rsrc_t rs, unsigned off, u32x2 v) {
  __builtin_amdgcn_raw_buffer_store_b64(v, rs, off, 0, ST_AUX);
}
__device__ __forceinline__ void st4(__amdgpu_buffer_rsrc_t rs, unsigned off, unsigned v) {
  __builtin_amdgcn_raw_buffer_store_b32(v, rs, off, 0, ST_AUX);
}
__device__ __forceinline__ unsigned ld4(__amdgpu_buffer_rsrc_t rs, unsigned off) {
  return __builtin_amdgcn_raw_buffer_load_b32(rs, off, 0, LD_AUX);
}
constexpr int TU = 16;
__device__ __forceinline__ unsigned cyc32() { return (unsigned)__builtin_amdgcn_s_memtime(); }
__device__ __forceinline__ unsigned to_sgpr(unsigned x) {
  unsigned r;
  asm volatile("s_nop 0\n\tv_readfirstlane_b32 %0, %1\n\ts_nop 4" : "=s"(r) : "v"(x));
  return r;
}
struct Clk {
  unsigned ce, te;
  unsigned c2t, t2c;
  __device__ __forceinline__ void edge(unsigned& c, unsigned& t) {
    u64 c0, r0, r1, r2, r3, r4, r5;
    asm volatile("s_memtime %0\n\ts_memrealtime %1\n\ts_nop 4\n\ts_memrealtime %2\n\ts_nop 4\n\ts_memrealtime %3\n\ts_nop 4\n\t"
                 "s_memrealtime %4\n\ts_nop 4\n\ts_memrealtime %5\n\ts_nop 4\n\ts_memrealtime %6\n\ts_waitcnt lgkmcnt(0)"
                 : "=&s"(c0), "=&s"(r0), "=&s"(r1), "=&s"(r2), "=&s"(r3), "=&s"(r4), "=&s"(r5) :: "memory");
    const unsigned x = (unsigned)r0;
    const unsigned n = ((unsigned)r1 == x) + ((unsigned)r2 == x) + ((unsigned)r3 == x) + ((unsigned)r4 == x) + ((unsigned)r5 == x);
    c = to_sgpr((unsigned)c0 + 24u * n); t = (x + 4u) * TU;
  }
  __device__ __forceinline__ void init() { edge(ce, te); c2t = 44000u; t2c = 97600u; }
  __device__ __forceinline__ void sync() {
    unsigned c, t;
    edge(c, t);
    set(c, t);
  }
  __device__ __forceinline__ void set(unsigned c, unsigned t) {
    if (c == ce) return;
    const float q = (float)(t - te) * __builtin_amdgcn_rcpf((float)(c - ce));
    const float qc = fminf(fmaxf(q, 0.51f), 1.99f);
    c2t = to_sgpr((unsigned)(qc * 65536.f));
    t2c = to_sgpr((unsigned)(__builtin_amdgcn_rcpf(qc) * 65536.f));
    ce = c; te = t;
  }
  __device__ __forceinline__ unsigned time(unsigned c) const { return te + ((min(c - ce, 30000u) * c2t) >> 16); }
  __device__ __forceinline__ unsigned now() const { return time(cyc32()); }
  __device__ __forceinline__ unsigned target(unsigned t) const {
    return to_sgpr(ce + (((unsigned)min(max((int)(t - te), 0), 20000) * t2c) >> 16));
  }
  __device__ __forceinline__ void wait(unsigned t) const { wait_c(target(t)); }
  __device__ __forceinline__ void wait_c(unsigned c) const {
    unsigned y;
    asm volatile("1:\n\ts_memtime vcc\n\ts_waitcnt lgkmcnt(0)\n\ts_sub_i32 %0, %1, vcc_lo\n\ts_cmp_gt_i32 %0, 128\n\ts_cbranch_scc1 1b\n\t"
                 "2:\n\ts_sub_i32 %0, %0, %2\n\ts_cmp_gt_i32 %0, %3\n\ts_cbranch_scc1 2b"
                 : "=&s"(y) : "s"(c), "n"(W_STEP), "n"(W_OFF) : "memory", "scc", "vcc");
  }
};
__device__ __forceinline__ unsigned nosent(unsigned d) { return min(d, SENT - 1u); }
__device__ __forceinline__ int arow(int row, int half) { return 4 * (row >> 1) + 2 * half + (row & 1); }
__device__ __forceinline__ unsigned umax4(u32x4 v) { return max(max(v[0], v[1]), max(v[2], v[3])); }
__device__ __forceinline__ void stamp_again(GAS u64* p) {
  __hip_atomic_store(p, (__builtin_amdgcn_s_memrealtime() & ~7ull) | (__hip_atomic_load(p, __ATOMIC_RELAXED, __HIP_MEMORY_SCOPE_AGENT) & 7ull),
                     __ATOMIC_RELAXED, __HIP_MEMORY_SCOPE_AGENT);
}
__device__ __forceinline__ bool poll_fail(unsigned& spins, GAS unsigned* abortw, int lane) {
  ++spins;
  if (spins > SPIN_CAP) {
    if (lane == 0) __hip_atomic_store(abortw, 1u, __ATOMIC_RELAXED, __HIP_MEMORY_SCOPE_AGENT);
    return true;
  }
  if ((spins & 511u) == 0u && lane == 0) {
    unsigned n = 0;
    for (int i = 0; i < 8; ++i) n += __hip_atomic_load(abortw + (SY_CT - SY_ABORT + (CT_ARR + i) * 128) / 4, __ATOMIC_RELAXED, __HIP_MEMORY_SCOPE_AGENT) + 1u;
    if (n < 256u) stamp_again((GAS u64*)(abortw - 32));
  }
  if ((spins & 63u) == 0u)
    return __builtin_amdgcn_readfirstlane(__hip_atomic_load(abortw, __ATOMIC_RELAXED, __HIP_MEMORY_SCOPE_AGENT)) != SENT;
  return false;
}
struct Gate {
  u64 entry; int arr; unsigned rot; bool open;
  __device__ __forceinline__ bool look(char* sync, u64 v = 0) {
    if (open) return true;
    for (unsigned spins = 0;; ++spins) {
      if (spins) v = __hip_atomic_load((GAS u64*)(sync + SY_EPOCH), __ATOMIC_RELAXED, __HIP_MEMORY_SCOPE_AGENT);
      v = (u64)(unsigned)__builtin_amdgcn_readfirstlane((unsigned)v) | ((u64)(unsigned)__builtin_amdgcn_readfirstlane((unsigned)(v >> 32)) << 32);
      if (v - entry + 2000u < (1ull << 31) || spins > 4 * SPIN_CAP) break;
      if (spins) __builtin_amdgcn_s_sleep(8);
    }
    asm volatile("" ::: "memory");
    const bool same = rot == ((unsigned)v & 7u);
    rot = (unsigned)v & 7u; open = true;
    return same;
  }
  __device__ __forceinline__ void arrive(char* sync) {
    if (arr >= 0 && opaque_tid(0) == 0) __hip_atomic_fetch_add((GAS unsigned*)(sync + SY_CT + (CT_ARR + arr) * 128), 1u, __ATOMIC_RELAXED, __HIP_MEMORY_SCOPE_AGENT);
    arr = -1;
  }
  __device__ __forceinline__ bool pass(char* sync, u64 v = 0) { const bool r = look(sync, v); arrive(sync); return r; }
};
__device__ __forceinline__ unsigned die_half() { return ((unsigned)__builtin_amdgcn_s_getreg(20 | (3 << 11)) >> 2) & 1u; }
typedef u32x2 Sel;
__device__ __forceinline__ Sel sel_wait(char* sync, GAS unsigned* abortw, int lane, bool& dead, bool all = false, int c0 = CT_INIT, int n0 = 0, int c1 = CT_INIT, int n1 = 0) {
  unsigned spins = 0;
  u64 v;
  const bool m = lane == 0 || lane > 3 || !all;
  const int off = m ? SY_SEL : SY_CT + (lane == 1 ? CT_INIT : lane == 2 ? c0 : c1) * 128;
  while (true) {
    v = __hip_atomic_load((GAS u64*)(sync + off), __ATOMIC_RELAXED, __HIP_MEMORY_SCOPE_AGENT);
    if (__all(m ? (v >> 63) == 0 : (int)v >= (lane == 1 ? 63 : lane == 2 ? n0 : n1))) break;
    __builtin_amdgcn_s_sleep(4);
    if (dead || poll_fail(spins, abortw, lane)) { dead = true; v = 0; break; }
  }
  return (Sel){(unsigned)__builtin_amdgcn_readfirstlane((unsigned)v), (unsigned)__builtin_amdgcn_readfirstlane((unsigned)(v >> 32))};
}
__device__ __forceinline__ __amdgpu_buffer_rsrc_t rsrc_u(char* sync) { return mk_rsrc((void*)sync, WS_USED); }
__device__ __forceinline__ unsigned um_slot(int s, Sel sel) { return (unsigned)s * (64 * CHK) + ((sel[0] >> (s >> 1)) & 1u) * 8192; }
constexpr unsigned UXH = 8 * CHK;
__device__ __forceinline__ unsigned ux_base(int s, int h, Sel sel) { return ((s & 15) * 32 + h) * UXH + ((~sel[0] >> (2 * (s & 15) + (h >> 4))) & 1u) * 8192; }
__device__ __forceinline__ constexpr unsigned ux_tile(int t) { return (t >> 3) * CHK + (t & 7) * 1024; }
__device__ __forceinline__ __amdgpu_buffer_rsrc_t rsrc_hot(char* sync, Sel sel) {
  return mk_rsrc((void*)(sync + SY_HOT + (sel[1] & 1u) * 8192), H_BYTES - 8192);
}
template <int SLP = 4>
__device__ __forceinline__ void ctr_wait(GAS unsigned* c, unsigned need, GAS unsigned* abortw, int lane, bool& dead) {
  unsigned spins = 0;
  while (true) {
    unsigned v = __builtin_amdgcn_readfirstlane(__hip_atomic_load(c, __ATOMIC_RELAXED, __HIP_MEMORY_SCOPE_AGENT)) + 1u;
    if (v >= need) break;
    __builtin_amdgcn_s_sleep(SLP);
    if (dead || poll_fail(spins, abortw, lane)) { dead = true; break; }
  }
}
template <int N> __device__ __forceinline__ void wait_vm() { asm volatile("s_waitcnt vmcnt(%0)" :: "n"(N) : "memory"); }
__device__ __forceinline__ void lds_barrier() {
  asm volatile("s_waitcnt lgkmcnt(0)" ::: "memory");
  __builtin_amdgcn_s_barrier();
  asm volatile("" ::: "memory");
}
__device__ __forceinline__ f32x4 vzero() { f32x4 z = {0.f, 0.f, 0.f, 0.f}; asm volatile("" : "+v"(z)); return z; }
constexpr bool blk_in_lds(int R, int b) {
  return R == 0 ? ((b & 7) == 1 || (b & 7) == 4) : (b < 16 ? (b & 3) == 0 : (b & 3) == 3);
}
constexpr int lds_index(int R, int b) { int n = 0; for (int i = 0; i < b; ++i) if (blk_in_lds(R, i)) ++n; return n; }
constexpr bool frag_in_lds(int R, int f) { return blk_in_lds(R, f >> 1); }
constexpr int frag_reg_slot(int R, int f) { int n = 0; for (int i = 0; i < f; ++i) if (!frag_in_lds(R, i)) ++n; return n; }

struct WSched { int ord[32]; int slot[32]; int issue[32]; int out[32]; int reg[32]; };
constexpr WSched make_wsched(int R) {
  WSched S{};
  int n = 0;
  for (int b = 0; b < 32; ++b) if (!blk_in_lds(R, b)) S.ord[n++] = b;
  for (int i = 0; i < n; ++i) S.reg[i] = frag_reg_slot(R, 2 * S.ord[i]);
  const int npure = n;
  for (int b = 0; b < 32; ++b) if (blk_in_lds(R, b)) S.ord[n++] = b;
  int seq[32] = {};
  int nissued = 0, next = 0;
  for (int i = 0; i < 10; ++i) { S.slot[i] = (i + 10 - npure % 10) % 10; S.issue[i] = -1; seq[i] = nissued++; }
  next = 10;
  S.out[0] = nissued - 1;
  for (int c = 0; c <= npure; ++c) {
    if (c >= 1) {
      const int k = S.slot[c - 1];
      if (next < npure) { S.slot[next] = k; S.issue[next] = c; seq[next] = nissued++; ++next; }
      else if (k < NLF / 2) { S.slot[npure + k] = k; S.issue[npure + k] = c; seq[npure + k] = nissued++; }
    }
    if (c + 1 < npure) S.out[c + 1] = nissued - 1 - seq[c + 1];
  }
  return S;
}
__device__ __forceinline__ u32x8 tr_frag(const char* sb, int tile, int lane) {
  u32x8 f;
#pragma unroll
  for (int q = 0; q < 4; ++q) {
    int row = (q >> 1) * 32 + 8 * (lane >> 4) + 4 * (q & 1) + ((lane & 15) >> 2);
    int p = lane & 3;
    int lc = 2 * tile + (p >> 1);
    int addr = row * 256 + ((lc ^ swz_b(row)) * 16) + 8 * (p & 1);
    bf16x4 v = __builtin_amdgcn_ds_read_tr16_b64_v4i16((LAS bf16x4*)(sb + addr));
    u32x2 u = __builtin_bit_cast(u32x2, v);
    f[2 * q] = u[0]; f[2 * q + 1] = u[1];
  }
  return f;
}
__device__ __forceinline__ void dma16(__amdgpu_buffer_rsrc_t rs, unsigned voff, unsigned soff, unsigned lds, bool nt = false, bool pad = true) {
  if (nt) asm volatile("s_mov_b32 m0, %0\n\ts_nop 3\n\tbuffer_load_dwordx4 %1, %2, %3 offen nt lds" :: "s"(lds), "v"(voff), "s"(rs), "s"(soff) : "m0", "memory");
  else if (pad) asm volatile("s_mov_b32 m0, %0\n\ts_nop 3\n\tbuffer_load_dwordx4 %1, %2, %3 offen lds" :: "s"(lds), "v"(voff), "s"(rs), "s"(soff) : "m0", "memory");
  else asm volatile("s_mov_b32 m0, %0\n\ts_nop 0\n\tbuffer_load_dwordx4 %1, %2, %3 offen lds" :: "s"(lds), "v"(voff), "s"(rs), "s"(soff) : "m0", "memory");
}
__device__ __forceinline__ u32x8 tr_frag2(const char* sb, int w, int f, int lane) {
  u32x8 r;
#pragma unroll
  for (int c = 0; c < 2; ++c)
#pragma unroll
    for (int t = 0; t < 2; ++t) {
      const int row = 32 * (w >> 1) + 16 * c + 4 * (lane >> 4) + ((lane & 15) >> 2);
      const int p = lane & 3, lc = 2 * (4 * (w & 1) + 2 * t + f) + (p >> 1);
      const u32x2 u = __builtin_bit_cast(u32x2, __builtin_amdgcn_ds_read_tr16_b64_v4i16((LAS bf16x4*)(sb + row * 256 + ((lc ^ swz_b(row)) * 16) + 8 * (p & 1))));
      r[4 * c + t] = u[0]; r[4 * c + 2 + t] = u[1];
    }
  return r;
}
template <int ROLE>
__device__ __forceinline__ void load_weights(const ChainArgs& a, int h, int w, int lane, char* smem, Wts& W) {
  u32x8 F[4];
#pragma unroll
  for (int i = 0; i < 32; ++i) asm volatile("" : "=a"(W.a[i]));
  constexpr WSched S = make_wsched(ROLE);
  constexpr int NP = 32 - NLF / 2;
  const __amdgpu_buffer_rsrc_t r0 = mk_rsrc(ROLE == 0 ? a.Wq : a.Wv, 8u << 20), r1 = mk_rsrc(ROLE == 0 ? a.Wk : a.Wo, 8u << 20);
  const unsigned lds0 = (unsigned)(uintptr_t)(LAS char*)smem;
  unsigned vo[2][4];
#pragma unroll
  for (int j = 0; j < 4; ++j) {
    const int row = (w * 4 + j) * 4 + (lane >> 4), lc = (lane & 15) ^ swz_b(row);
    vo[0][j] = row * HD * 2 + lc * 16; vo[1][j] = ((row & 15) + 2 * (row & 16)) * DM * 2 + (row >> 5) * 256 + lc * 16;
  }
  asm volatile("s_nop 4");
  auto stage = [&](int i) {
    const int blk = S.ord[i];
    const bool second = ROLE == 0 ? (blk & 1) : blk >= 16, wo = ROLE == 1 && second;
    const unsigned so = wo ? (unsigned)(((128 * h + 32 * (blk & 2) + 16 * (blk & 1)) * DM + 256 * (((blk - 16) >> 2) ^ (h & 3))) * 2)
                           : (unsigned)((64 * ((ROLE == 0 ? blk >> 1 : blk) ^ (h & RHO)) * HD + 128 * h) * 2);
#pragma unroll
    for (int j = 0; j < 4; ++j) dma16(second ? r1 : r0, vo[wo][j], so, lds0 + 16384 * S.slot[i] + (w * 4 + j) * 1024, (NTW >> (wo ? 1 : 0)) & 1);
  };
#pragma unroll
  for (int i = 0; i < 32; ++i) if (S.issue[i] == -1) stage(i);
#pragma unroll
  for (int c = 0; c < NP; ++c) {
    asm volatile("s_waitcnt vmcnt(%0)" :: "n"(4 * S.out[c]) : "memory");
    lds_barrier();
#pragma unroll
    for (int i = 0; i < 32; ++i) if (S.issue[i] == c) stage(i);
    const char* sb = smem + 16384 * S.slot[c];
#pragma unroll
    for (int t = 0; t < 2; ++t) {
      u32x8 f = (ROLE == 1 && S.ord[c] >= 16) ? tr_frag2(sb, w, t, lane) : tr_frag(sb, 2 * w + t, lane);
      if (S.reg[c] < 32) {
#pragma unroll
        for (int i = 0; i < 8; ++i) asm volatile("v_accvgpr_write_b32 a[%1], %0" :: "v"(f[i]), "n"(8 * ((S.reg[c] + t) & 31) + i));
      }
      F[(S.reg[c] + t) & 3] = f;
    }
    if (S.reg[c] >= 32 && (S.reg[c] & 2)) {
      const u32x16 lo = __builtin_shufflevector(F[0], F[1], 0, 1, 2, 3, 4, 5, 6, 7, 8, 9, 10, 11, 12, 13, 14, 15);
      const u32x16 hi = __builtin_shufflevector(F[2], F[3], 0, 1, 2, 3, 4, 5, 6, 7, 8, 9, 10, 11, 12, 13, 14, 15);
      u32x32 x = __builtin_shufflevector(lo, hi, 0, 1, 2, 3, 4, 5, 6, 7, 8, 9, 10, 11, 12, 13, 14, 15, 16, 17, 18, 19, 20, 21, 22, 23, 24, 25, 26, 27, 28, 29, 30, 31);
      asm volatile("" : "+v"(x));
      W.v[(S.reg[c] >> 2) & 3] = x;
    }
  }
  static_assert(S.issue[31] < NP && S.issue[NP] < S.issue[31], "resident blocks are issued in slot order inside the loop");
#pragma unroll
  for (int j = 0; j < NLF / 2; ++j) {
    asm volatile("s_waitcnt vmcnt(%0)" :: "n"(4 * (NLF / 2 - 1 - j)) : "memory");
    lds_barrier();
    const bool two = ROLE == 1 && S.ord[NP + j] >= 16;
    u32x8 f0 = two ? tr_frag2(smem + 16384 * j, w, 0, lane) : tr_frag(smem + 16384 * j, 2 * w, lane);
    u32x8 f1 = two ? tr_frag2(smem + 16384 * j, w, 1, lane) : tr_frag(smem + 16384 * j, 2 * w + 1, lane);
    lds_barrier();
    char* d = smem + 16384 * j + w * 4096 + lane * 16;
    *(u32x4*)d = (u32x4){f0[0], f0[1], f0[2], f0[3]};
    *(u32x4*)(d + 1024) = (u32x4){f0[4], f0[5], f0[6], f0[7]};
    *(u32x4*)(d + 2048) = (u32x4){f1[0], f1[1], f1[2], f1[3]};
    *(u32x4*)(d + 3072) = (u32x4){f1[4], f1[5], f1[6], f1[7]};
  }
  lds_barrier();
}
#define SB0 __builtin_amdgcn_sched_barrier(0)
#define QK_BODY \
  "s_cmp_eq_u32 %[wv], 1\n\t" \
  "s_cbranch_scc1 91f\n\t" \
  "s_cmp_eq_u32 %[wv], 2\n\t" \
  "s_cbranch_scc1 92f\n\t" \
  "s_cmp_eq_u32 %[wv], 3\n\t" \
  "s_cbranch_scc1 93f\n\t" \
  "90:\n\t" \
  "s_mov_b32 s58, s42\n\t" \
  "20:\n\t" \
  "s_and_b32 s76, s44, 7\n\t" \
  "s_lshl_b32 s76, s76, 15\n\t" \
  "s_add_i32 s40, s54, s76\n\t" \
  "s_and_b32 s76, s44, 3\n\t" \
  "s_lshl_b32 s45, s76, 14\n\t" \
  "s_mul_i32 s47, s76, 0xc000\n\t" \
  "s_add_i32 s47, s47, 0x40000\n\t" \
  "s_add_i32 s49, s44, -1\n\t" \
  "s_and_b32 s48, s49, 3\n\t" \
  "s_lshl_b32 s48, s48, 14\n\t" \
  "s_lshl_b32 s50, s53, 8\n\t" \
  "s_mov_b32 s59, 0\n\t" \
  "s_mov_b32 s92, s51\n\t" \
  "s_mov_b32 s93, s53\n\t" \
  "s_xor_b32 s76, s40, 0x400\n\t" \
  "s_xor_b32 s77, s40, 0x800\n\t" \
  "s_xor_b32 s78, s40, 0xc00\n\t" \
  "s_cmp_lg_u32 s58, 0\n\t" \
  "s_cbranch_scc1 11f\n\t" \
  "10:\n\t" \
  "ds_read_b128 v[48:51], v102 offset:0\n\t" \
  "ds_read_b128 v[52:55], v102 offset:1024\n\t" \
  "ds_read_b128 v[56:59], v102 offset:2048\n\t" \
  "ds_read_b128 v[60:63], v102 offset:3072\n\t" \
  "ds_read_b128 v[16:19], v102 offset:16384\n\t" \
  "ds_read_b128 v[20:23], v102 offset:17408\n\t" \
  "ds_read_b128 v[24:27], v102 offset:18432\n\t" \
  "ds_read_b128 v[28:31], v102 offset:19456\n\t" \
  "ds_read_b128 v[84:87], v102 offset:49152\n\t" \
  "ds_read_b128 v[88:91], v102 offset:50176\n\t" \
  "ds_read_b128 v[92:95], v102 offset:51200\n\t" \
  "ds_read_b128 v[96:99], v102 offset:52224\n\t" \
  "s_waitcnt lgkmcnt(4)\n\t" \
  "ds_read_b128 v[0:3], v103 offset:16384\n\t" \
  "ds_read_b128 v[4:7], v103 offset:17408\n\t" \
  "ds_read_b128 v[8:11], v103 offset:18432\n\t" \
  "ds_read_b128 v[12:15], v103 offset:19456\n\t" \
  "s_bitcmp1_b32 s58, 1\n\t" \
  "s_cselect_b64 s[80:81], -1, 0\n\t" \
  "s_cmp_lg_u32 s58, 0\n\t" \
  "s_cbranch_scc1 17f\n\t" \
  "s_mov_b32 exec_hi, 0\n\t" \
  "16:\n\t" \
  "s_memtime vcc\n\t" \
  "s_waitcnt lgkmcnt(0)\n\t" \
  "s_sub_i32 s79, s41, vcc_lo\n\t" \
  "s_cmp_gt_i32 s79, 64\n\t" \
  "s_cbranch_scc1 16b\n\t" \
  "13:\n\t" \
  "buffer_load_dword v116, v110, %[rh], s48 offen sc1\n\t" \
  "s_waitcnt vmcnt(0)\n\t" \
  "15:\n\t" \
  "v_mov_b32 v117, 20000\n\t" \
  "v_cmp_ne_u32_sdwa vcc, v116, s49 src0_sel:BYTE_0 src1_sel:DWORD\n\t" \
  "v_subrev_u32 v116, s50, v116\n\t" \
  "v_ashrrev_i32 v116, 8, v116\n\t" \
  "s_nop 1\n\t" \
  "v_max_i32_dpp v116, v116, v116 quad_perm:[1,0,3,2] row_mask:0xf bank_mask:0xf\n\t" \
  "s_cbranch_vccnz 14f\n\t" \
  "s_nop 0\n\t" \
  "v_max_i32_dpp v116, v116, v116 quad_perm:[2,3,0,1] row_mask:0xf bank_mask:0xf\n\t" \
  "s_nop 1\n\t" \
  "v_max_i32_dpp v116, v116, v116 row_half_mirror row_mask:0xf bank_mask:0xf\n\t" \
  "s_nop 1\n\t" \
  "v_max_i32_dpp v116, v116, v116 row_mirror row_mask:0xf bank_mask:0xf\n\t" \
  "v_med3_i32 v116, v116, 0, v117\n\t" \
  "v_mul_u32_u24 v116, s52, v116\n\t" \
  "v_lshrrev_b32 v116, 16, v116\n\t" \
  "s_mov_b64 exec, -1\n\t" \
  "v_readlane_b32 s84, v116, 0\n\t" \
  "v_readlane_b32 s85, v116, 16\n\t" \
  "s_max_u32 s84, s84, s85\n\t" \
  "s_add_i32 s84, s84, s51\n\t" \
  "1:\n\t" \
  "s_memtime vcc\n\t" \
  "s_waitcnt lgkmcnt(0)\n\t" \
  "s_sub_i32 s79, s84, vcc_lo\n\t" \
  "s_cmp_gt_i32 s79, 128\n\t" \
  "s_cbranch_scc1 1b\n\t" \
  "2:\n\t" \
  "s_sub_i32 s79, s79, 33\n\t" \
  "s_cmp_gt_i32 s79, 56\n\t" \
  "s_cbranch_scc1 2b\n\t" \
  "buffer_load_dwordx4 v[32:35], v100, %[rh], s40 offen sc1\n\t" \
  "s_nop 12\n\t" \
  "buffer_load_dwordx4 v[36:39], v100, %[rh], s76 offen sc1\n\t" \
  "s_nop 12\n\t" \
  "buffer_load_dwordx4 v[40:43], v100, %[rh], s77 offen sc1\n\t" \
  "s_nop 12\n\t" \
  "buffer_load_dwordx4 v[44:47], v100, %[rh], s78 offen sc1\n\t" \
  "s_memtime vcc\n\t" \
  "s_memrealtime s[82:83]\n\t" \
  "s_nop 4\n\t" \
  "s_memrealtime s[84:85]\n\t" \
  "s_nop 4\n\t" \
  "s_memrealtime s[94:95]\n\t" \
  "s_nop 4\n\t" \
  "s_memrealtime s[86:87]\n\t" \
  "s_nop 4\n\t" \
  "s_memrealtime s[88:89]\n\t" \
  "s_nop 4\n\t" \
  "s_memrealtime s[90:91]\n\t" \
  "s_mov_b32 s92, 0\n\t" \
  "s_waitcnt lgkmcnt(0)\n\t" \
  "s_cmp_eq_u32 s82, s84\n\t" \
  "s_addc_u32 s92, s92, 0\n\t" \
  "s_cmp_eq_u32 s82, s94\n\t" \
  "s_addc_u32 s92, s92, 0\n\t" \
  "s_cmp_eq_u32 s82, s86\n\t" \
  "s_addc_u32 s92, s92, 0\n\t" \
  "s_cmp_eq_u32 s82, s88\n\t" \
  "s_addc_u32 s92, s92, 0\n\t" \
  "s_cmp_eq_u32 s82, s90\n\t" \
  "s_addc_u32 s92, s92, 0\n\t" \
  "s_mul_i32 s92, s92, 24\n\t" \
  "s_add_i32 s92, s92, vcc_lo\n\t" \
  "s_lshl4_add_u32 s93, s82, 64\n\t" \
  "3:\n\t" \
  "v_mov_b64 v[68:69], 0\n\t" \
  "v_mov_b64 v[70:71], 0\n\t" \
  "v_mov_b64 v[72:73], 0\n\t" \
  "v_mov_b64 v[74:75], 0\n\t" \
  "v_mov_b64 v[76:77], 0\n\t" \
  "v_mov_b64 v[78:79], 0\n\t" \
  "v_mov_b64 v[80:81], 0\n\t" \
  "v_mov_b64 v[82:83], 0\n\t" \
  "s_waitcnt lgkmcnt(0)\n\t" \
  "s_waitcnt vmcnt(3)\n\t" \
  "v_smfmac_f32_16x16x64_bf16 v[68:71], v[32:35], a[0:7], v104\n\t" \
  "ds_write_b128 v101, v[32:35] offset:0\n\t" \
  "v_smfmac_f32_16x16x64_bf16 v[72:75], v[32:35], a[8:15], v104\n\t" \
  "v_smfmac_f32_16x16x64_bf16 v[76:79], v[32:35], v[48:55], v104\n\t" \
  "v_smfmac_f32_16x16x64_bf16 v[80:83], v[32:35], v[56:63], v104\n\t" \
  "s_waitcnt vmcnt(2)\n\t" \
  "v_smfmac_f32_16x16x64_bf16 v[68:71], v[36:39], a[16:23], v104\n\t" \
  "ds_write_b128 v101, v[36:39] offset:1024\n\t" \
  "v_smfmac_f32_16x16x64_bf16 v[72:75], v[36:39], a[24:31], v104\n\t" \
  "v_smfmac_f32_16x16x64_bf16 v[76:79], v[36:39], a[32:39], v104\n\t" \
  "v_cmp_u_f32 vcc, v68, v69\n\t" \
  "v_smfmac_f32_16x16x64_bf16 v[80:83], v[36:39], a[40:47], v104\n\t" \
  "v_cmp_u_f32 s[82:83], v70, v71\n\t" \
  "s_waitcnt vmcnt(1)\n\t" \
  "v_smfmac_f32_16x16x64_bf16 v[68:71], v[40:43], v[16:23], v104\n\t" \
  "ds_write_b128 v101, v[40:43] offset:2048\n\t" \
  "v_smfmac_f32_16x16x64_bf16 v[72:75], v[40:43], v[24:31], v104\n\t" \
  "s_or_b64 vcc, vcc, s[82:83]\n\t" \
  "s_or_b64 vcc, vcc, s[80:81]\n\t" \
  "s_waitcnt lgkmcnt(1)\n\t" \
  "v_smfmac_f32_16x16x64_bf16 v[76:79], v[40:43], a[48:55], v104\n\t" \
  "s_cbranch_scc1 8f\n\t" \
  "5:\n\t" \
  "s_barrier\n\t" \
  "4:\n\t" \
  "s_memtime s[56:57]\n\t" \
  "ds_read_b128 v[48:51], v101 offset:4096\n\t" \
  "v_smfmac_f32_16x16x64_bf16 v[80:83], v[40:43], a[56:63], v104\n\t" \
  "ds_read_b128 v[52:55], v101 offset:5120\n\t" \
  "ds_read_b128 v[56:59], v101 offset:8192\n\t" \
  "s_waitcnt vmcnt(0)\n\t" \
  "v_smfmac_f32_16x16x64_bf16 v[68:71], v[44:47], a[64:71], v104\n\t" \
  "ds_write_b128 v101, v[44:47] offset:3072\n\t" \
  "v_smfmac_f32_16x16x64_bf16 v[72:75], v[44:47], a[72:79], v104\n\t" \
  "v_smfmac_f32_16x16x64_bf16 v[76:79], v[44:47], a[80:87], v104\n\t" \
  "v_cmp_u_f32 vcc, v68, v69\n\t" \
  "v_smfmac_f32_16x16x64_bf16 v[80:83], v[44:47], a[88:95], v104\n\t" \
  "v_cmp_u_f32 s[82:83], v70, v71\n\t" \
  "s_waitcnt lgkmcnt(3)\n\t" \
  "v_smfmac_f32_16x16x64_bf16 v[68:71], v[48:51], a[96:103], v104\n\t" \
  "ds_read_b128 v[60:63], v101 offset:9216\n\t" \
  "ds_read_b128 v[64:67], v101 offset:12288\n\t" \
  "v_smfmac_f32_16x16x64_bf16 v[72:75], v[48:51], a[104:111], v104\n\t" \
  "s_or_b64 vcc, vcc, s[82:83]\n\t" \
  "s_waitcnt lgkmcnt(2)\n\t" \
  "v_smfmac_f32_16x16x64_bf16 v[68:71], v[52:55], a[112:119], v104\n\t" \
  "s_cbranch_scc1 9f\n\t" \
  "6:\n\t" \
  "s_barrier\n\t" \
  "v_smfmac_f32_16x16x64_bf16 v[72:75], v[52:55], a[120:127], v104\n\t" \
  "ds_read_b128 v[48:51], v101 offset:13312\n\t" \
  "v_smfmac_f32_16x16x64_bf16 v[68:71], v[56:59], a[192:199], v104\n\t" \
  "ds_read_b128 v[52:55], v101 offset:6144\n\t" \
  "v_smfmac_f32_16x16x64_bf16 v[72:75], v[56:59], a[200:207], v104\n\t" \
  "s_waitcnt lgkmcnt(3)\n\t" \
  "v_smfmac_f32_16x16x64_bf16 v[68:71], v[60:63], a[208:215], v104\n\t" \
  "ds_read_b128 v[56:59], v101 offset:7168\n\t" \
  "v_smfmac_f32_16x16x64_bf16 v[72:75], v[60:63], a[216:223], v104\n\t" \
  "s_waitcnt lgkmcnt(3)\n\t" \
  "v_smfmac_f32_16x16x64_bf16 v[68:71], v[64:67], v[152:159], v104\n\t" \
  "ds_read_b128 v[60:63], v101 offset:10240\n\t" \
  "ds_read_b128 v[32:35], v119 offset:0\n\t" \
  "v_smfmac_f32_16x16x64_bf16 v[72:75], v[64:67], v[160:167], v104\n\t" \
  "ds_read_b128 v[16:19], v103 offset:49152\n\t" \
  "s_waitcnt lgkmcnt(5)\n\t" \
  "v_smfmac_f32_16x16x64_bf16 v[68:71], v[48:51], v[168:175], v104\n\t" \
  "ds_read_b128 v[64:67], v101 offset:11264\n\t" \
  "ds_read_b128 v[36:39], v119 offset:1024\n\t" \
  "v_smfmac_f32_16x16x64_bf16 v[72:75], v[48:51], v[176:183], v104\n\t" \
  "ds_read_b128 v[20:23], v103 offset:50176\n\t" \
  "s_waitcnt lgkmcnt(7)\n\t" \
  "v_smfmac_f32_16x16x64_bf16 v[68:71], v[52:55], v[84:91], v104\n\t" \
  "ds_read_b128 v[48:51], v101 offset:15360\n\t" \
  "ds_read_b128 v[40:43], v119 offset:16\n\t" \
  "v_smfmac_f32_16x16x64_bf16 v[72:75], v[52:55], v[92:99], v104\n\t" \
  "ds_read_b128 v[24:27], v103 offset:51200\n\t" \
  "s_waitcnt lgkmcnt(9)\n\t" \
  "v_smfmac_f32_16x16x64_bf16 v[68:71], v[56:59], a[160:167], v104\n\t" \
  "ds_read_b128 v[52:55], v101 offset:14336\n\t" \
  "ds_read_b128 v[44:47], v119 offset:1040\n\t" \
  "v_smfmac_f32_16x16x64_bf16 v[72:75], v[56:59], a[168:175], v104\n\t" \
  "ds_read_b128 v[28:31], v103 offset:52224\n\t" \
  "s_waitcnt lgkmcnt(11)\n\t" \
  "v_smfmac_f32_16x16x64_bf16 v[68:71], v[60:63], v[0:7], v104\n\t" \
  "ds_read_b128 v[92:95], v119 offset:2048\n\t" \
  "v_smfmac_f32_16x16x64_bf16 v[72:75], v[60:63], v[8:15], v104\n\t" \
  "ds_read_b128 v[96:99], v119 offset:3072\n\t" \
  "s_waitcnt lgkmcnt(10)\n\t" \
  "v_smfmac_f32_16x16x64_bf16 v[68:71], v[64:67], v[120:127], v104\n\t" \
  "ds_read_b128 v[0:3], v102 offset:32768\n\t" \
  "v_smfmac_f32_16x16x64_bf16 v[72:75], v[64:67], v[128:135], v104\n\t" \
  "ds_read_b128 v[4:7], v102 offset:33792\n\t" \
  "s_waitcnt lgkmcnt(9)\n\t" \
  "v_smfmac_f32_16x16x64_bf16 v[68:71], v[48:51], v[216:223], v104\n\t" \
  "ds_read_b128 v[8:11], v102 offset:34816\n\t" \
  "s_waitcnt lgkmcnt(7)\n\t" \
  "v_smfmac_f32_16x16x64_bf16 v[68:71], v[52:55], v[16:23], v104\n\t" \
  "ds_read_b128 v[12:15], v102 offset:35840\n\t" \
  "v_smfmac_f32_16x16x64_bf16 v[72:75], v[48:51], v[224:231], v104\n\t" \
  "ds_read_b128 v[16:19], v119 offset:2064\n\t" \
  "ds_read_b128 v[20:23], v119 offset:3088\n\t" \
  "s_waitcnt lgkmcnt(8)\n\t" \
  "v_smfmac_f32_16x16x64_bf16 v[72:75], v[52:55], v[24:31], v104\n\t" \
  "v_add_f32 v116, v68, v70\n\t" \
  "v_add_f32 v117, v69, v71\n\t" \
  "v_smfmac_f32_16x16x64_bf16 v[76:79], v[56:59], a[176:183], v104\n\t" \
  "v_cvt_pk_bf16_f32 v116, v116, v117\n\t" \
  "ds_read_b128 v[24:27], v103 offset:2048\n\t" \
  "v_smfmac_f32_16x16x64_bf16 v[80:83], v[56:59], a[184:191], v104\n\t" \
  "v_add_f32 v117, v72, v74\n\t" \
  "v_add_f32 v118, v73, v75\n\t" \
  "v_cvt_pk_bf16_f32 v117, v117, v118\n\t" \
  "ds_write2st64_b32 v105, v116, v117 offset1:1\n\t" \
  "v_smfmac_f32_16x16x64_bf16 v[76:79], v[60:63], a[240:247], v104\n\t" \
  "v_mov_b64 v[68:69], 0\n\t" \
  "v_smfmac_f32_16x16x64_bf16 v[80:83], v[60:63], a[248:255], v104\n\t" \
  "v_mov_b64 v[70:71], 0\n\t" \
  "v_smfmac_f32_16x16x64_bf16 v[76:79], v[64:67], v[136:143], v104\n\t" \
  "v_mov_b64 v[72:73], 0\n\t" \
  "v_smfmac_f32_16x16x64_bf16 v[80:83], v[64:67], v[144:151], v104\n\t" \
  "s_waitcnt lgkmcnt(0)\n\t" \
  "s_barrier\n\t" \
  "ds_read_b64_tr_b16 v[84:85], v107 offset:0\n\t" \
  "ds_read_b64_tr_b16 v[86:87], v107 offset:128\n\t" \
  "v_smfmac_f32_16x16x64_bf16 v[76:79], v[48:51], v[232:239], v104\n\t" \
  "ds_read_b64_tr_b16 v[88:89], v107 offset:1024\n\t" \
  "ds_read_b64_tr_b16 v[90:91], v107 offset:1152\n\t" \
  "v_smfmac_f32_16x16x64_bf16 v[80:83], v[48:51], v[240:247], v104\n\t" \
  "ds_read_b128 v[56:59], v101 offset:4096\n\t" \
  "v_mov_b64 v[74:75], 0\n\t" \
  "v_smfmac_f32_16x16x64_bf16 v[76:79], v[52:55], v[200:207], v104\n\t" \
  "ds_read_b128 v[60:63], v101 offset:13312\n\t" \
  "ds_read_b128 v[28:31], v103 offset:3072\n\t" \
  "v_smfmac_f32_16x16x64_bf16 v[80:83], v[52:55], v[208:215], v104\n\t" \
  "s_waitcnt lgkmcnt(5)\n\t" \
  "v_smfmac_f32_16x16x64_bf16 v[68:71], v[84:87], v[32:39], v104\n\t" \
  "v_smfmac_f32_16x16x64_bf16 v[72:75], v[84:87], v[40:47], v104\n\t" \
  "ds_read_b128 v[64:67], v101 offset:6144\n\t" \
  "s_waitcnt lgkmcnt(4)\n\t" \
  "v_smfmac_f32_16x16x64_bf16 v[68:71], v[88:91], v[92:99], v104\n\t" \
  "v_smfmac_f32_16x16x64_bf16 v[72:75], v[88:91], v[16:23], v104\n\t" \
  "ds_read_b128 v[48:51], v101 offset:8192\n\t" \
  "s_waitcnt lgkmcnt(4)\n\t" \
  "v_smfmac_f32_16x16x64_bf16 v[76:79], v[56:59], v[0:7], v104\n\t" \
  "ds_read_b128 v[16:19], v103 offset:0\n\t" \
  "v_add_f32 v40, v68, v70\n\t" \
  "v_smfmac_f32_16x16x64_bf16 v[80:83], v[56:59], v[8:15], v104\n\t" \
  "v_add_f32 v41, v72, v74\n\t" \
  "v_max_f32 v42, v40, v41\n\t" \
  "s_waitcnt lgkmcnt(4)\n\t" \
  "v_smfmac_f32_16x16x64_bf16 v[76:79], v[60:63], v[184:191], v104\n\t" \
  "ds_read_b128 v[20:23], v103 offset:1024\n\t" \
  "v_max_f32_dpp v42, v42, v42 quad_perm:[1,0,3,2] row_mask:0xf bank_mask:0xf\n\t" \
  "v_smfmac_f32_16x16x64_bf16 v[80:83], v[60:63], v[192:199], v104\n\t" \
  "ds_read_b128 v[52:55], v101 offset:5120\n\t" \
  "v_max_f32_dpp v42, v42, v42 quad_perm:[2,3,0,1] row_mask:0xf bank_mask:0xf\n\t" \
  "s_waitcnt lgkmcnt(4)\n\t" \
  "v_smfmac_f32_16x16x64_bf16 v[76:79], v[64:67], a[144:151], v104\n\t" \
  "ds_read_b128 v[56:59], v101 offset:9216\n\t" \
  "v_max_f32_dpp v42, v42, v42 row_half_mirror row_mask:0xf bank_mask:0xf\n\t" \
  "v_smfmac_f32_16x16x64_bf16 v[80:83], v[64:67], a[152:159], v104\n\t" \
  "ds_read_b128 v[60:63], v101 offset:12288\n\t" \
  "v_max_f32_dpp v42, v42, v42 row_mirror row_mask:0xf bank_mask:0xf\n\t" \
  "v_mul_f32 v42, %[cs], v42\n\t" \
  "s_waitcnt lgkmcnt(3)\n\t" \
  "v_smfmac_f32_16x16x64_bf16 v[76:79], v[48:51], v[16:23], v104\n\t" \
  "ds_read_b128 v[0:3], v103 offset:32768\n\t" \
  "v_fma_f32 v40, v40, %[cs], -v42\n\t" \
  "v_fma_f32 v41, v41, %[cs], -v42\n\t" \
  "v_smfmac_f32_16x16x64_bf16 v[80:83], v[48:51], v[24:31], v104\n\t" \
  "ds_read_b128 v[4:7], v103 offset:33792\n\t" \
  "v_exp_f32 v43, v40\n\t" \
  "v_exp_f32 v44, v41\n\t" \
  "s_waitcnt lgkmcnt(4)\n\t" \
  "v_smfmac_f32_16x16x64_bf16 v[76:79], v[52:55], a[128:135], v104\n\t" \
  "ds_read_b128 v[8:11], v103 offset:34816\n\t" \
  "v_cvt_pk_bf16_f32 v45, v43, v44\n\t" \
  "buffer_store_dword v45, v112, %[rh], s47 offen sc1\n\t" \
  "v_smfmac_f32_16x16x64_bf16 v[80:83], v[52:55], a[136:143], v104\n\t" \
  "ds_read_b128 v[12:15], v103 offset:35840\n\t" \
  "s_waitcnt lgkmcnt(5)\n\t" \
  "v_smfmac_f32_16x16x64_bf16 v[76:79], v[56:59], a[224:231], v104\n\t" \
  "s_waitcnt lgkmcnt(2)\n\t" \
  "v_smfmac_f32_16x16x64_bf16 v[76:79], v[60:63], v[0:7], v104\n\t" \
  "v_smfmac_f32_16x16x64_bf16 v[80:83], v[56:59], a[232:239], v104\n\t" \
  "s_waitcnt lgkmcnt(0)\n\t" \
  "v_smfmac_f32_16x16x64_bf16 v[80:83], v[60:63], v[8:15], v104\n\t" \
  "v_add_f32 v20, v76, v78\n\t" \
  "v_add_f32 v21, v77, v79\n\t" \
  "v_cvt_pk_bf16_f32 v20, v20, v21\n\t" \
  "ds_write_b32 v106, v20\n\t" \
  "v_add_f32 v92, v43, v44\n\t" \
  "v_mov_b32 v25, v115\n\t" \
  "s_nop 1\n\t" \
  "v_add_f32 v23, v80, v82\n\t" \
  "v_add_f32 v21, v81, v83\n\t" \
  "v_cvt_pk_bf16_f32 v21, v23, v21\n\t" \
  "ds_write_b32 v106, v21 offset:256\n\t" \
  "v_add_f32_dpp v92, v92, v92 row_ror:8 row_mask:0xf bank_mask:0xf\n\t" \
  "v_mov_b64 v[0:1], 0\n\t" \
  "v_mov_b64 v[2:3], 0\n\t" \
  "v_mov_b32 v27, v114\n\t" \
  "s_waitcnt lgkmcnt(0)\n\t" \
  "s_barrier\n\t" \
  "ds_read_b64_tr_b16 v[4:5], v108 offset:0\n\t" \
  "ds_read_b64_tr_b16 v[6:7], v108 offset:64\n\t" \
  "ds_read_b64_tr_b16 v[8:9], v108 offset:512\n\t" \
  "ds_read_b64_tr_b16 v[10:11], v108 offset:576\n\t" \
  "ds_read_b64_tr_b16 v[12:13], v108 offset:1024\n\t" \
  "ds_read_b64_tr_b16 v[14:15], v108 offset:1088\n\t" \
  "ds_read_b64_tr_b16 v[16:17], v108 offset:1536\n\t" \
  "ds_read_b64_tr_b16 v[18:19], v108 offset:1600\n\t" \
  "s_waitcnt lgkmcnt(4)\n\t" \
  "v_smfmac_f32_16x16x64_bf16 v[0:3], v[84:87], v[4:11], v104\n\t" \
  "s_waitcnt lgkmcnt(0)\n\t" \
  "v_smfmac_f32_16x16x64_bf16 v[0:3], v[88:91], v[12:19], v104\n\t" \
  "s_nop 7\n\t" \
  "v_add_f32 v96, v0, v2\n\t" \
  "v_fma_f32 v96, v96, %[cs], -v42\n\t" \
  "v_exp_f32 v24, v96\n\t" \
  "s_nop 0\n\t" \
  "v_add_f32 v29, v92, v24\n\t" \
  "s_nop 0\n\t" \
  "v_mov_b32_dpp v25, v24 quad_perm:[1,0,3,2] row_mask:0xf bank_mask:0x3\n\t" \
  "v_add_f32_dpp v29, v29, v29 quad_perm:[1,0,3,2] row_mask:0xf bank_mask:0xf\n\t" \
  "v_mov_b32_dpp v27, v24 quad_perm:[0,1,2,3] row_mask:0xf bank_mask:0x3\n\t" \
  "s_nop 0\n\t" \
  "v_add_f32_dpp v29, v29, v29 quad_perm:[2,3,0,1] row_mask:0xf bank_mask:0xf\n\t" \
  "s_nop 1\n\t" \
  "v_add_f32_dpp v29, v29, v29 row_half_mirror row_mask:0xf bank_mask:0xf\n\t" \
  "v_rcp_f32 v26, v29\n\t" \
  "v_cmpx_gt_f32 vcc, 0x5d5e0b6b, v29\n\t" \
  "v_mul_f32 v24, v27, v26\n\t" \
  "v_mul_f32 v25, v25, v26\n\t" \
  "v_cvt_pk_bf16_f32 v24, v24, v25\n\t" \
  "buffer_store_dword v24, v113, %[rh], s47 offen sc1\n\t" \
  "s_memtime s[76:77]\n\t" \
  "s_mov_b64 exec, -1\n\t" \
  "v_mov_b32 v97, v29\n\t" \
  "s_cmp_eq_u64 vcc, -1\n\t" \
  "s_cselect_b32 s42, 0, 8\n\t" \
  "s_waitcnt lgkmcnt(0)\n\t" \
  "s_mov_b32 s57, s76\n\t" \
  "s_add_i32 s41, s56, 2050\n\t" \
  "s_sub_i32 s76, s92, s51\n\t" \
  "s_sub_i32 s77, s93, s53\n\t" \
  "s_cmp_eq_u32 s76, 0\n\t" \
  "s_cbranch_scc1 21f\n\t" \
  "v_cvt_f32_u32 v117, s76\n\t" \
  "v_cvt_f32_u32 v116, s77\n\t" \
  "v_rcp_f32 v117, v117\n\t" \
  "s_mov_b32 s51, s92\n\t" \
  "v_mul_f32 v117, v117, v116\n\t" \
  "v_max_f32 v117, 0x3f028f5c, v117\n\t" \
  "v_min_f32 v117, 0x3ffeb852, v117\n\t" \
  "v_rcp_f32 v116, v117\n\t" \
  "v_mul_f32 v117, 0x4f000000, v117\n\t" \
  "v_mul_f32 v116, 0x47800000, v116\n\t" \
  "v_cvt_u32_f32 v117, v117\n\t" \
  "v_cvt_u32_f32 v116, v116\n\t" \
  "s_mov_b32 s53, s93\n\t" \
  "v_readfirstlane_b32 s46, v117\n\t" \
  "v_readfirstlane_b32 s52, v116\n\t" \
  "21:\n\t" \
  "s_add_i32 s44, s44, 1\n\t" \
  "s_and_b32 s58, s58, 4\n\t" \
  "s_and_b32 s76, s44, 64\n\t" \
  "s_or_b32 s42, s42, s76\n\t" \
  "s_cbranch_scc0 20b\n\t" \
  "s_branch 99f\n\t" \
  "14:\n\t" \
  "s_add_u32 s59, s59, 0x10000\n\t" \
  "s_cmp_lt_u32 s59, 0x100000\n\t" \
  "s_cbranch_scc1 13b\n\t" \
  "s_mov_b64 exec, -1\n\t" \
  "s_mov_b32 s42, 16\n\t" \
  "s_branch 99f\n\t" \
  "8:\n\t" \
  "s_bitcmp1_b32 s58, 1\n\t" \
  "s_cbranch_scc1 4b\n\t" \
  "s_bitcmp1_b32 s58, 2\n\t" \
  "s_cbranch_scc1 5b\n\t" \
  "v_max3_u32 v116, v32, v33, v34\n\t" \
  "v_max3_u32 v117, v35, v36, v37\n\t" \
  "v_max3_u32 v116, v116, v38, v39\n\t" \
  "v_max_u32 v116, v116, v117\n\t" \
  "v_cmp_eq_u32 vcc, -1, v116\n\t" \
  "s_cbranch_vccz 5b\n\t" \
  "s_branch 0f\n\t" \
  "9:\n\t" \
  "s_bitcmp1_b32 s58, 2\n\t" \
  "s_cbranch_scc1 6b\n\t" \
  "v_max3_u32 v116, v40, v41, v42\n\t" \
  "v_max3_u32 v117, v43, v44, v45\n\t" \
  "v_max3_u32 v116, v116, v46, v47\n\t" \
  "v_max_u32 v116, v116, v117\n\t" \
  "v_cmp_eq_u32 vcc, -1, v116\n\t" \
  "s_cbranch_vccz 6b\n\t" \
  "s_or_b32 s58, s58, 2\n\t" \
  "0:\n\t" \
  "s_or_b32 s58, s58, 1\n\t" \
  "11:\n\t" \
  "buffer_load_dwordx4 v[32:35], v100, %[rh], s40 offen sc1\n\t" \
  "buffer_load_dwordx4 v[36:39], v100, %[rh], s76 offen sc1\n\t" \
  "buffer_load_dwordx4 v[40:43], v100, %[rh], s77 offen sc1\n\t" \
  "buffer_load_dwordx4 v[44:47], v100, %[rh], s78 offen sc1\n\t" \
  "s_waitcnt lgkmcnt(0)\n\t" \
  "s_branch 10b\n\t" \
  "17:\n\t" \
  "s_add_u32 s59, s59, 1\n\t" \
  "s_bitcmp1_b32 s58, 2\n\t" \
  "s_cbranch_scc1 3b\n\t" \
  "s_waitcnt vmcnt(0)\n\t" \
  "v_max_u32 v116, v32, v33\n\t" \
  "v_max3_u32 v116, v116, v34, v35\n\t" \
  "v_max3_u32 v116, v116, v36, v37\n\t" \
  "v_max3_u32 v116, v116, v38, v39\n\t" \
  "v_max3_u32 v116, v116, v40, v41\n\t" \
  "v_max3_u32 v116, v116, v42, v43\n\t" \
  "v_max3_u32 v116, v116, v44, v45\n\t" \
  "v_max3_u32 v116, v116, v46, v47\n\t" \
  "v_cmp_eq_u32 vcc, -1, v116\n\t" \
  "s_cbranch_vccz 3b\n\t" \
  "s_bitcmp1_b32 s59, 6\n\t" \
  "s_cbranch_scc1 12f\n\t" \
  "buffer_load_dwordx4 v[32:35], v100, %[rh], s40 offen sc1\n\t" \
  "buffer_load_dwordx4 v[36:39], v100, %[rh], s76 offen sc1\n\t" \
  "buffer_load_dwordx4 v[40:43], v100, %[rh], s77 offen sc1\n\t" \
  "buffer_load_dwordx4 v[44:47], v100, %[rh], s78 offen sc1\n\t" \
  "s_branch 17b\n\t" \
  "12:\n\t" \
  "s_mov_b32 s42, s58\n\t" \
  "s_branch 99f\n\t" \
  "91:\n\t" \
  "s_mov_b32 s58, s42\n\t" \
  "20:\n\t" \
  "s_and_b32 s76, s44, 7\n\t" \
  "s_lshl_b32 s76, s76, 15\n\t" \
  "s_add_i32 s40, s54, s76\n\t" \
  "s_and_b32 s76, s44, 3\n\t" \
  "s_lshl_b32 s45, s76, 14\n\t" \
  "s_mul_i32 s47, s76, 0xc000\n\t" \
  "s_add_i32 s47, s47, 0x40000\n\t" \
  "s_add_i32 s49, s44, -1\n\t" \
  "s_and_b32 s48, s49, 3\n\t" \
  "s_lshl_b32 s48, s48, 14\n\t" \
  "s_lshl_b32 s50, s53, 8\n\t" \
  "s_mov_b32 s59, 0\n\t" \
  "s_mov_b32 s92, s51\n\t" \
  "s_mov_b32 s93, s53\n\t" \
  "s_xor_b32 s76, s40, 0x400\n\t" \
  "s_xor_b32 s77, s40, 0x800\n\t" \
  "s_xor_b32 s78, s40, 0xc00\n\t" \
  "s_cmp_lg_u32 s58, 0\n\t" \
  "s_cbranch_scc1 11f\n\t" \
  "10:\n\t" \
  "ds_read_b128 v[48:51], v102 offset:32768\n\t" \
  "ds_read_b128 v[52:55], v102 offset:33792\n\t" \
  "ds_read_b128 v[56:59], v102 offset:34816\n\t" \
  "ds_read_b128 v[60:63], v102 offset:35840\n\t" \
  "ds_read_b128 v[16:19], v102 offset:49152\n\t" \
  "ds_read_b128 v[20:23], v102 offset:50176\n\t" \
  "ds_read_b128 v[24:27], v102 offset:51200\n\t" \
  "ds_read_b128 v[28:31], v102 offset:52224\n\t" \
  "ds_read_b128 v[84:87], v103 offset:16384\n\t" \
  "ds_read_b128 v[88:91], v103 offset:17408\n\t" \
  "ds_read_b128 v[92:95], v103 offset:18432\n\t" \
  "ds_read_b128 v[96:99], v103 offset:19456\n\t" \
  "s_waitcnt lgkmcnt(4)\n\t" \
  "ds_read_b128 v[0:3], v103 offset:49152\n\t" \
  "ds_read_b128 v[4:7], v103 offset:50176\n\t" \
  "ds_read_b128 v[8:11], v103 offset:51200\n\t" \
  "ds_read_b128 v[12:15], v103 offset:52224\n\t" \
  "s_bitcmp1_b32 s58, 1\n\t" \
  "s_cselect_b64 s[80:81], -1, 0\n\t" \
  "s_cmp_lg_u32 s58, 0\n\t" \
  "s_cbranch_scc1 17f\n\t" \
  "s_mov_b32 exec_hi, 0\n\t" \
  "16:\n\t" \
  "s_memtime vcc\n\t" \
  "s_waitcnt lgkmcnt(0)\n\t" \
  "s_sub_i32 s79, s41, vcc_lo\n\t" \
  "s_cmp_gt_i32 s79, 64\n\t" \
  "s_cbranch_scc1 16b\n\t" \
  "13:\n\t" \
  "buffer_load_dword v116, v110, %[rh], s48 offen sc1\n\t" \
  "s_waitcnt vmcnt(0)\n\t" \
  "15:\n\t" \
  "v_mov_b32 v117, 20000\n\t" \
  "v_cmp_ne_u32_sdwa vcc, v116, s49 src0_sel:BYTE_0 src1_sel:DWORD\n\t" \
  "v_subrev_u32 v116, s50, v116\n\t" \
  "v_ashrrev_i32 v116, 8, v116\n\t" \
  "s_nop 1\n\t" \
  "v_max_i32_dpp v116, v116, v116 quad_perm:[1,0,3,2] row_mask:0xf bank_mask:0xf\n\t" \
  "s_cbranch_vccnz 14f\n\t" \
  "s_nop 0\n\t" \
  "v_max_i32_dpp v116, v116, v116 quad_perm:[2,3,0,1] row_mask:0xf bank_mask:0xf\n\t" \
  "s_nop 1\n\t" \
  "v_max_i32_dpp v116, v116, v116 row_half_mirror row_mask:0xf bank_mask:0xf\n\t" \
  "s_nop 1\n\t" \
  "v_max_i32_dpp v116, v116, v116 row_mirror row_mask:0xf bank_mask:0xf\n\t" \
  "v_med3_i32 v116, v116, 0, v117\n\t" \
  "v_mul_u32_u24 v116, s52, v116\n\t" \
  "v_lshrrev_b32 v116, 16, v116\n\t" \
  "s_mov_b64 exec, -1\n\t" \
  "v_readlane_b32 s84, v116, 0\n\t" \
  "v_readlane_b32 s85, v116, 16\n\t" \
  "s_max_u32 s84, s84, s85\n\t" \
  "s_add_i32 s84, s84, s51\n\t" \
  "1:\n\t" \
  "s_memtime vcc\n\t" \
  "s_waitcnt lgkmcnt(0)\n\t" \
  "s_sub_i32 s79, s84, vcc_lo\n\t" \
  "s_cmp_gt_i32 s79, 128\n\t" \
  "s_cbranch_scc1 1b\n\t" \
  "2:\n\t" \
  "s_sub_i32 s79, s79, 33\n\t" \
  "s_cmp_gt_i32 s79, 56\n\t" \
  "s_cbranch_scc1 2b\n\t" \
  "buffer_load_dwordx4 v[32:35], v100, %[rh], s40 offen sc1\n\t" \
  "s_nop 12\n\t" \
  "buffer_load_dwordx4 v[36:39], v100, %[rh], s76 offen sc1\n\t" \
  "s_nop 12\n\t" \
  "buffer_load_dwordx4 v[40:43], v100, %[rh], s77 offen sc1\n\t" \
  "s_nop 12\n\t" \
  "buffer_load_dwordx4 v[44:47], v100, %[rh], s78 offen sc1\n\t" \
  "s_memtime vcc\n\t" \
  "s_memrealtime s[82:83]\n\t" \
  "s_nop 4\n\t" \
  "s_memrealtime s[84:85]\n\t" \
  "s_nop 4\n\t" \
  "s_memrealtime s[94:95]\n\t" \
  "s_nop 4\n\t" \
  "s_memrealtime s[86:87]\n\t" \
  "s_nop 4\n\t" \
  "s_memrealtime s[88:89]\n\t" \
  "s_nop 4\n\t" \
  "s_memrealtime s[90:91]\n\t" \
  "s_mov_b32 s92, 0\n\t" \
  "s_waitcnt lgkmcnt(0)\n\t" \
  "s_cmp_eq_u32 s82, s84\n\t" \
  "s_addc_u32 s92, s92, 0\n\t" \
  "s_cmp_eq_u32 s82, s94\n\t" \
  "s_addc_u32 s92, s92, 0\n\t" \
  "s_cmp_eq_u32 s82, s86\n\t" \
  "s_addc_u32 s92, s92, 0\n\t" \
  "s_cmp_eq_u32 s82, s88\n\t" \
  "s_addc_u32 s92, s92, 0\n\t" \
  "s_cmp_eq_u32 s82, s90\n\t" \
  "s_addc_u32 s92, s92, 0\n\t" \
  "s_mul_i32 s92, s92, 24\n\t" \
  "s_add_i32 s92, s92, vcc_lo\n\t" \
  "s_lshl4_add_u32 s93, s82, 64\n\t" \
  "3:\n\t" \
  "v_mov_b64 v[68:69], 0\n\t" \
  "v_mov_b64 v[70:71], 0\n\t" \
  "v_mov_b64 v[72:73], 0\n\t" \
  "v_mov_b64 v[74:75], 0\n\t" \
  "v_mov_b64 v[76:77], 0\n\t" \
  "v_mov_b64 v[78:79], 0\n\t" \
  "v_mov_b64 v[80:81], 0\n\t" \
  "v_mov_b64 v[82:83], 0\n\t" \
  "s_waitcnt lgkmcnt(0)\n\t" \
  "s_waitcnt vmcnt(3)\n\t" \
  "v_smfmac_f32_16x16x64_bf16 v[68:71], v[32:35], a[96:103], v104\n\t" \
  "ds_write_b128 v101, v[32:35] offset:4096\n\t" \
  "v_smfmac_f32_16x16x64_bf16 v[72:75], v[32:35], a[104:111], v104\n\t" \
  "v_smfmac_f32_16x16x64_bf16 v[76:79], v[32:35], v[48:55], v104\n\t" \
  "v_smfmac_f32_16x16x64_bf16 v[80:83], v[32:35], v[56:63], v104\n\t" \
  "s_waitcnt vmcnt(2)\n\t" \
  "v_smfmac_f32_16x16x64_bf16 v[68:71], v[36:39], a[112:119], v104\n\t" \
  "ds_write_b128 v101, v[36:39] offset:5120\n\t" \
  "v_smfmac_f32_16x16x64_bf16 v[72:75], v[36:39], a[120:127], v104\n\t" \
  "v_smfmac_f32_16x16x64_bf16 v[76:79], v[36:39], a[128:135], v104\n\t" \
  "v_cmp_u_f32 vcc, v68, v69\n\t" \
  "v_smfmac_f32_16x16x64_bf16 v[80:83], v[36:39], a[136:143], v104\n\t" \
  "v_cmp_u_f32 s[82:83], v70, v71\n\t" \
  "s_waitcnt vmcnt(1)\n\t" \
  "v_smfmac_f32_16x16x64_bf16 v[68:71], v[40:43], v[16:23], v104\n\t" \
  "ds_write_b128 v101, v[40:43] offset:6144\n\t" \
  "v_smfmac_f32_16x16x64_bf16 v[72:75], v[40:43], v[24:31], v104\n\t" \
  "s_or_b64 vcc, vcc, s[82:83]\n\t" \
  "s_or_b64 vcc, vcc, s[80:81]\n\t" \
  "s_waitcnt lgkmcnt(1)\n\t" \
  "v_smfmac_f32_16x16x64_bf16 v[76:79], v[40:43], a[144:151], v104\n\t" \
  "s_cbranch_scc1 8f\n\t" \
  "5:\n\t" \
  "s_barrier\n\t" \
  "4:\n\t" \
  "s_memtime s[56:57]\n\t" \
  "ds_read_b128 v[48:51], v101 offset:8192\n\t" \
  "v_smfmac_f32_16x16x64_bf16 v[80:83], v[40:43], a[152:159], v104\n\t" \
  "ds_read_b128 v[52:55], v101 offset:9216\n\t" \
  "ds_read_b128 v[56:59], v101 offset:12288\n\t" \
  "s_waitcnt vmcnt(0)\n\t" \
  "v_smfmac_f32_16x16x64_bf16 v[68:71], v[44:47], a[160:167], v104\n\t" \
  "ds_write_b128 v101, v[44:47] offset:7168\n\t" \
  "v_smfmac_f32_16x16x64_bf16 v[72:75], v[44:47], a[168:175], v104\n\t" \
  "v_smfmac_f32_16x16x64_bf16 v[76:79], v[44:47], a[176:183], v104\n\t" \
  "v_cmp_u_f32 vcc, v68, v69\n\t" \
  "v_smfmac_f32_16x16x64_bf16 v[80:83], v[44:47], a[184:191], v104\n\t" \
  "v_cmp_u_f32 s[82:83], v70, v71\n\t" \
  "s_waitcnt lgkmcnt(3)\n\t" \
  "v_smfmac_f32_16x16x64_bf16 v[68:71], v[48:51], a[192:199], v104\n\t" \
  "ds_read_b128 v[60:63], v101 offset:13312\n\t" \
  "ds_read_b128 v[64:67], v101 offset:0\n\t" \
  "v_smfmac_f32_16x16x64_bf16 v[72:75], v[48:51], a[200:207], v104\n\t" \
  "s_or_b64 vcc, vcc, s[82:83]\n\t" \
  "s_waitcnt lgkmcnt(2)\n\t" \
  "v_smfmac_f32_16x16x64_bf16 v[68:71], v[52:55], a[208:215], v104\n\t" \
  "s_cbranch_scc1 9f\n\t" \
  "6:\n\t" \
  "s_barrier\n\t" \
  "v_smfmac_f32_16x16x64_bf16 v[72:75], v[52:55], a[216:223], v104\n\t" \
  "ds_read_b128 v[48:51], v101 offset:1024\n\t" \
  "v_smfmac_f32_16x16x64_bf16 v[68:71], v[56:59], v[152:159], v104\n\t" \
  "ds_read_b128 v[52:55], v101 offset:10240\n\t" \
  "v_smfmac_f32_16x16x64_bf16 v[72:75], v[56:59], v[160:167], v104\n\t" \
  "s_waitcnt lgkmcnt(3)\n\t" \
  "v_smfmac_f32_16x16x64_bf16 v[68:71], v[60:63], v[168:175], v104\n\t" \
  "ds_read_b128 v[56:59], v101 offset:11264\n\t" \
  "v_smfmac_f32_16x16x64_bf16 v[72:75], v[60:63], v[176:183], v104\n\t" \
  "s_waitcnt lgkmcnt(3)\n\t" \
  "v_smfmac_f32_16x16x64_bf16 v[68:71], v[64:67], a[0:7], v104\n\t" \
  "ds_read_b128 v[60:63], v101 offset:14336\n\t" \
  "ds_read_b128 v[32:35], v119 offset:0\n\t" \
  "v_smfmac_f32_16x16x64_bf16 v[72:75], v[64:67], a[8:15], v104\n\t" \
  "ds_read_b128 v[16:19], v102 offset:16384\n\t" \
  "s_waitcnt lgkmcnt(5)\n\t" \
  "v_smfmac_f32_16x16x64_bf16 v[68:71], v[48:51], a[16:23], v104\n\t" \
  "ds_read_b128 v[64:67], v101 offset:15360\n\t" \
  "ds_read_b128 v[36:39], v119 offset:1024\n\t" \
  "v_smfmac_f32_16x16x64_bf16 v[72:75], v[48:51], a[24:31], v104\n\t" \
  "ds_read_b128 v[20:23], v102 offset:17408\n\t" \
  "s_waitcnt lgkmcnt(7)\n\t" \
  "v_smfmac_f32_16x16x64_bf16 v[68:71], v[52:55], v[84:91], v104\n\t" \
  "ds_read_b128 v[48:51], v101 offset:3072\n\t" \
  "ds_read_b128 v[40:43], v119 offset:16\n\t" \
  "v_smfmac_f32_16x16x64_bf16 v[72:75], v[52:55], v[92:99], v104\n\t" \
  "ds_read_b128 v[24:27], v102 offset:18432\n\t" \
  "s_waitcnt lgkmcnt(9)\n\t" \
  "v_smfmac_f32_16x16x64_bf16 v[68:71], v[56:59], v[120:127], v104\n\t" \
  "ds_read_b128 v[52:55], v101 offset:2048\n\t" \
  "ds_read_b128 v[44:47], v119 offset:1040\n\t" \
  "v_smfmac_f32_16x16x64_bf16 v[72:75], v[56:59], v[128:135], v104\n\t" \
  "ds_read_b128 v[28:31], v102 offset:19456\n\t" \
  "s_waitcnt lgkmcnt(11)\n\t" \
  "v_smfmac_f32_16x16x64_bf16 v[68:71], v[60:63], v[0:7], v104\n\t" \
  "ds_read_b128 v[92:95], v119 offset:2048\n\t" \
  "v_smfmac_f32_16x16x64_bf16 v[72:75], v[60:63], v[8:15], v104\n\t" \
  "ds_read_b128 v[96:99], v119 offset:3072\n\t" \
  "s_waitcnt lgkmcnt(10)\n\t" \
  "v_smfmac_f32_16x16x64_bf16 v[68:71], v[64:67], v[216:223], v104\n\t" \
  "ds_read_b128 v[0:3], v103 offset:0\n\t" \
  "v_smfmac_f32_16x16x64_bf16 v[72:75], v[64:67], v[224:231], v104\n\t" \
  "ds_read_b128 v[4:7], v103 offset:1024\n\t" \
  "s_waitcnt lgkmcnt(9)\n\t" \
  "v_smfmac_f32_16x16x64_bf16 v[68:71], v[48:51], a[64:71], v104\n\t" \
  "ds_read_b128 v[8:11], v103 offset:2048\n\t" \
  "s_waitcnt lgkmcnt(7)\n\t" \
  "v_smfmac_f32_16x16x64_bf16 v[68:71], v[52:55], v[16:23], v104\n\t" \
  "ds_read_b128 v[12:15], v103 offset:3072\n\t" \
  "v_smfmac_f32_16x16x64_bf16 v[72:75], v[48:51], a[72:79], v104\n\t" \
  "ds_read_b128 v[16:19], v119 offset:2064\n\t" \
  "ds_read_b128 v[20:23], v119 offset:3088\n\t" \
  "s_waitcnt lgkmcnt(8)\n\t" \
  "v_smfmac_f32_16x16x64_bf16 v[72:75], v[52:55], v[24:31], v104\n\t" \
  "v_add_f32 v116, v68, v70\n\t" \
  "v_add_f32 v117, v69, v71\n\t" \
  "v_smfmac_f32_16x16x64_bf16 v[76:79], v[56:59], v[136:143], v104\n\t" \
  "v_cvt_pk_bf16_f32 v116, v116, v117\n\t" \
  "ds_read_b128 v[24:27], v103 offset:34816\n\t" \
  "v_smfmac_f32_16x16x64_bf16 v[80:83], v[56:59], v[144:151], v104\n\t" \
  "v_add_f32 v117, v72, v74\n\t" \
  "v_add_f32 v118, v73, v75\n\t" \
  "v_cvt_pk_bf16_f32 v117, v117, v118\n\t" \
  "ds_write2st64_b32 v105, v116, v117 offset1:1\n\t" \
  "v_smfmac_f32_16x16x64_bf16 v[76:79], v[60:63], v[200:207], v104\n\t" \
  "v_mov_b64 v[68:69], 0\n\t" \
  "v_smfmac_f32_16x16x64_bf16 v[80:83], v[60:63], v[208:215], v104\n\t" \
  "v_mov_b64 v[70:71], 0\n\t" \
  "v_smfmac_f32_16x16x64_bf16 v[76:79], v[64:67], v[232:239], v104\n\t" \
  "v_mov_b64 v[72:73], 0\n\t" \
  "v_smfmac_f32_16x16x64_bf16 v[80:83], v[64:67], v[240:247], v104\n\t" \
  "s_waitcnt lgkmcnt(0)\n\t" \
  "s_barrier\n\t" \
  "ds_read_b64_tr_b16 v[84:85], v107 offset:0\n\t" \
  "ds_read_b64_tr_b16 v[86:87], v107 offset:128\n\t" \
  "v_smfmac_f32_16x16x64_bf16 v[76:79], v[48:51], a[80:87], v104\n\t" \
  "ds_read_b64_tr_b16 v[88:89], v107 offset:1024\n\t" \
  "ds_read_b64_tr_b16 v[90:91], v107 offset:1152\n\t" \
  "v_smfmac_f32_16x16x64_bf16 v[80:83], v[48:51], a[88:95], v104\n\t" \
  "ds_read_b128 v[56:59], v101 offset:8192\n\t" \
  "v_mov_b64 v[74:75], 0\n\t" \
  "v_smfmac_f32_16x16x64_bf16 v[76:79], v[52:55], a[48:55], v104\n\t" \
  "ds_read_b128 v[60:63], v101 offset:1024\n\t" \
  "ds_read_b128 v[28:31], v103 offset:35840\n\t" \
  "v_smfmac_f32_16x16x64_bf16 v[80:83], v[52:55], a[56:63], v104\n\t" \
  "s_waitcnt lgkmcnt(5)\n\t" \
  "v_smfmac_f32_16x16x64_bf16 v[68:71], v[84:87], v[32:39], v104\n\t" \
  "v_smfmac_f32_16x16x64_bf16 v[72:75], v[84:87], v[40:47], v104\n\t" \
  "ds_read_b128 v[64:67], v101 offset:10240\n\t" \
  "s_waitcnt lgkmcnt(4)\n\t" \
  "v_smfmac_f32_16x16x64_bf16 v[68:71], v[88:91], v[92:99], v104\n\t" \
  "v_smfmac_f32_16x16x64_bf16 v[72:75], v[88:91], v[16:23], v104\n\t" \
  "ds_read_b128 v[48:51], v101 offset:12288\n\t" \
  "s_waitcnt lgkmcnt(4)\n\t" \
  "v_smfmac_f32_16x16x64_bf16 v[76:79], v[56:59], v[0:7], v104\n\t" \
  "ds_read_b128 v[16:19], v103 offset:32768\n\t" \
  "v_add_f32 v40, v69, v71\n\t" \
  "v_smfmac_f32_16x16x64_bf16 v[80:83], v[56:59], v[8:15], v104\n\t" \
  "v_add_f32 v41, v73, v75\n\t" \
  "v_max_f32 v42, v40, v41\n\t" \
  "s_waitcnt lgkmcnt(4)\n\t" \
  "v_smfmac_f32_16x16x64_bf16 v[76:79], v[60:63], a[32:39], v104\n\t" \
  "ds_read_b128 v[20:23], v103 offset:33792\n\t" \
  "v_max_f32_dpp v42, v42, v42 quad_perm:[1,0,3,2] row_mask:0xf bank_mask:0xf\n\t" \
  "v_smfmac_f32_16x16x64_bf16 v[80:83], v[60:63], a[40:47], v104\n\t" \
  "ds_read_b128 v[52:55], v101 offset:9216\n\t" \
  "v_max_f32_dpp v42, v42, v42 quad_perm:[2,3,0,1] row_mask:0xf bank_mask:0xf\n\t" \
  "s_waitcnt lgkmcnt(4)\n\t" \
  "v_smfmac_f32_16x16x64_bf16 v[76:79], v[64:67], a[240:247], v104\n\t" \
  "ds_read_b128 v[56:59], v101 offset:13312\n\t" \
  "v_max_f32_dpp v42, v42, v42 row_half_mirror row_mask:0xf bank_mask:0xf\n\t" \
  "v_smfmac_f32_16x16x64_bf16 v[80:83], v[64:67], a[248:255], v104\n\t" \
  "ds_read_b128 v[60:63], v101 offset:0\n\t" \
  "v_max_f32_dpp v42, v42, v42 row_mirror row_mask:0xf bank_mask:0xf\n\t" \
  "v_mul_f32 v42, %[cs], v42\n\t" \
  "s_waitcnt lgkmcnt(3)\n\t" \
  "v_smfmac_f32_16x16x64_bf16 v[76:79], v[48:51], v[16:23], v104\n\t" \
  "ds_read_b128 v[0:3], v102 offset:0\n\t" \
  "v_fma_f32 v40, v40, %[cs], -v42\n\t" \
  "v_fma_f32 v41, v41, %[cs], -v42\n\t" \
  "v_smfmac_f32_16x16x64_bf16 v[80:83], v[48:51], v[24:31], v104\n\t" \
  "ds_read_b128 v[4:7], v102 offset:1024\n\t" \
  "v_exp_f32 v43, v40\n\t" \
  "v_exp_f32 v44, v41\n\t" \
  "s_waitcnt lgkmcnt(4)\n\t" \
  "v_smfmac_f32_16x16x64_bf16 v[76:79], v[52:55], a[224:231], v104\n\t" \
  "ds_read_b128 v[8:11], v102 offset:2048\n\t" \
  "v_cvt_pk_bf16_f32 v45, v43, v44\n\t" \
  "buffer_store_dword v45, v112, %[rh], s47 offen sc1\n\t" \
  "v_smfmac_f32_16x16x64_bf16 v[80:83], v[52:55], a[232:239], v104\n\t" \
  "ds_read_b128 v[12:15], v102 offset:3072\n\t" \
  "s_waitcnt lgkmcnt(5)\n\t" \
  "v_smfmac_f32_16x16x64_bf16 v[76:79], v[56:59], v[184:191], v104\n\t" \
  "s_waitcnt lgkmcnt(2)\n\t" \
  "v_smfmac_f32_16x16x64_bf16 v[76:79], v[60:63], v[0:7], v104\n\t" \
  "v_smfmac_f32_16x16x64_bf16 v[80:83], v[56:59], v[192:199], v104\n\t" \
  "s_waitcnt lgkmcnt(0)\n\t" \
  "v_smfmac_f32_16x16x64_bf16 v[80:83], v[60:63], v[8:15], v104\n\t" \
  "v_add_f32 v20, v76, v78\n\t" \
  "v_add_f32 v21, v77, v79\n\t" \
  "v_cvt_pk_bf16_f32 v20, v20, v21\n\t" \
  "ds_write_b32 v106, v20\n\t" \
  "v_add_f32 v92, v43, v44\n\t" \
  "v_mov_b32 v25, v115\n\t" \
  "s_nop 1\n\t" \
  "v_add_f32 v23, v80, v82\n\t" \
  "v_add_f32 v21, v81, v83\n\t" \
  "v_cvt_pk_bf16_f32 v21, v23, v21\n\t" \
  "ds_write_b32 v106, v21 offset:256\n\t" \
  "v_add_f32_dpp v92, v92, v92 row_ror:8 row_mask:0xf bank_mask:0xf\n\t" \
  "v_mov_b64 v[0:1], 0\n\t" \
  "v_mov_b64 v[2:3], 0\n\t" \
  "v_mov_b32 v27, v114\n\t" \
  "s_waitcnt lgkmcnt(0)\n\t" \
  "s_barrier\n\t" \
  "ds_read_b64_tr_b16 v[4:5], v108 offset:0\n\t" \
  "ds_read_b64_tr_b16 v[6:7], v108 offset:64\n\t" \
  "ds_read_b64_tr_b16 v[8:9], v108 offset:512\n\t" \
  "ds_read_b64_tr_b16 v[10:11], v108 offset:576\n\t" \
  "ds_read_b64_tr_b16 v[12:13], v108 offset:1024\n\t" \
  "ds_read_b64_tr_b16 v[14:15], v108 offset:1088\n\t" \
  "ds_read_b64_tr_b16 v[16:17], v108 offset:1536\n\t" \
  "ds_read_b64_tr_b16 v[18:19], v108 offset:1600\n\t" \
  "s_waitcnt lgkmcnt(4)\n\t" \
  "v_smfmac_f32_16x16x64_bf16 v[0:3], v[84:87], v[4:11], v104\n\t" \
  "s_waitcnt lgkmcnt(0)\n\t" \
  "v_smfmac_f32_16x16x64_bf16 v[0:3], v[88:91], v[12:19], v104\n\t" \
  "s_nop 7\n\t" \
  "v_add_f32 v96, v1, v3\n\t" \
  "v_fma_f32 v96, v96, %[cs], -v42\n\t" \
  "v_exp_f32 v24, v96\n\t" \
  "s_nop 0\n\t" \
  "v_add_f32 v29, v92, v24\n\t" \
  "s_nop 0\n\t" \
  "v_mov_b32_dpp v25, v24 quad_perm:[1,0,3,2] row_mask:0xf bank_mask:0x3\n\t" \
  "v_add_f32_dpp v29, v29, v29 quad_perm:[1,0,3,2] row_mask:0xf bank_mask:0xf\n\t" \
  "v_mov_b32_dpp v27, v24 quad_perm:[0,1,2,3] row_mask:0xf bank_mask:0x3\n\t" \
  "s_nop 0\n\t" \
  "v_add_f32_dpp v29, v29, v29 quad_perm:[2,3,0,1] row_mask:0xf bank_mask:0xf\n\t" \
  "s_nop 1\n\t" \
  "v_add_f32_dpp v29, v29, v29 row_half_mirror row_mask:0xf bank_mask:0xf\n\t" \
  "v_rcp_f32 v26, v29\n\t" \
  "v_cmpx_gt_f32 vcc, 0x5d5e0b6b, v29\n\t" \
  "v_mul_f32 v24, v27, v26\n\t" \
  "v_mul_f32 v25, v25, v26\n\t" \
  "v_cvt_pk_bf16_f32 v24, v24, v25\n\t" \
  "buffer_store_dword v24, v113, %[rh], s47 offen sc1\n\t" \
  "s_memtime s[76:77]\n\t" \
  "s_mov_b64 exec, -1\n\t" \
  "v_mov_b32 v97, v29\n\t" \
  "s_cmp_eq_u64 vcc, -1\n\t" \
  "s_cselect_b32 s42, 0, 8\n\t" \
  "s_waitcnt lgkmcnt(0)\n\t" \
  "s_mov_b32 s57, s76\n\t" \
  "s_add_i32 s41, s56, 2050\n\t" \
  "s_sub_i32 s76, s92, s51\n\t" \
  "s_sub_i32 s77, s93, s53\n\t" \
  "s_cmp_eq_u32 s76, 0\n\t" \
  "s_cbranch_scc1 21f\n\t" \
  "v_cvt_f32_u32 v117, s76\n\t" \
  "v_cvt_f32_u32 v116, s77\n\t" \
  "v_rcp_f32 v117, v117\n\t" \
  "s_mov_b32 s51, s92\n\t" \
  "v_mul_f32 v117, v117, v116\n\t" \
  "v_max_f32 v117, 0x3f028f5c, v117\n\t" \
  "v_min_f32 v117, 0x3ffeb852, v117\n\t" \
  "v_rcp_f32 v116, v117\n\t" \
  "v_mul_f32 v117, 0x4f000000, v117\n\t" \
  "v_mul_f32 v116, 0x47800000, v116\n\t" \
  "v_cvt_u32_f32 v117, v117\n\t" \
  "v_cvt_u32_f32 v116, v116\n\t" \
  "s_mov_b32 s53, s93\n\t" \
  "v_readfirstlane_b32 s46, v117\n\t" \
  "v_readfirstlane_b32 s52, v116\n\t" \
  "21:\n\t" \
  "s_add_i32 s44, s44, 1\n\t" \
  "s_and_b32 s58, s58, 4\n\t" \
  "s_and_b32 s76, s44, 64\n\t" \
  "s_or_b32 s42, s42, s76\n\t" \
  "s_cbranch_scc0 20b\n\t" \
  "s_branch 99f\n\t" \
  "14:\n\t" \
  "s_add_u32 s59, s59, 0x10000\n\t" \
  "s_cmp_lt_u32 s59, 0x100000\n\t" \
  "s_cbranch_scc1 13b\n\t" \
  "s_mov_b64 exec, -1\n\t" \
  "s_mov_b32 s42, 16\n\t" \
  "s_branch 99f\n\t" \
  "8:\n\t" \
  "s_bitcmp1_b32 s58, 1\n\t" \
  "s_cbranch_scc1 4b\n\t" \
  "s_bitcmp1_b32 s58, 2\n\t" \
  "s_cbranch_scc1 5b\n\t" \
  "v_max3_u32 v116, v32, v33, v34\n\t" \
  "v_max3_u32 v117, v35, v36, v37\n\t" \
  "v_max3_u32 v116, v116, v38, v39\n\t" \
  "v_max_u32 v116, v116, v117\n\t" \
  "v_cmp_eq_u32 vcc, -1, v116\n\t" \
  "s_cbranch_vccz 5b\n\t" \
  "s_branch 0f\n\t" \
  "9:\n\t" \
  "s_bitcmp1_b32 s58, 2\n\t" \
  "s_cbranch_scc1 6b\n\t" \
  "v_max3_u32 v116, v40, v41, v42\n\t" \
  "v_max3_u32 v117, v43, v44, v45\n\t" \
  "v_max3_u32 v116, v116, v46, v47\n\t" \
  "v_max_u32 v116, v116, v117\n\t" \
  "v_cmp_eq_u32 vcc, -1, v116\n\t" \
  "s_cbranch_vccz 6b\n\t" \
  "s_or_b32 s58, s58, 2\n\t" \
  "0:\n\t" \
  "s_or_b32 s58, s58, 1\n\t" \
  "11:\n\t" \
  "buffer_load_dwordx4 v[32:35], v100, %[rh], s40 offen sc1\n\t" \
  "buffer_load_dwordx4 v[36:39], v100, %[rh], s76 offen sc1\n\t" \
  "buffer_load_dwordx4 v[40:43], v100, %[rh], s77 offen sc1\n\t" \
  "buffer_load_dwordx4 v[44:47], v100, %[rh], s78 offen sc1\n\t" \
  "s_waitcnt lgkmcnt(0)\n\t" \
  "s_branch 10b\n\t" \
  "17:\n\t" \
  "s_add_u32 s59, s59, 1\n\t" \
  "s_bitcmp1_b32 s58, 2\n\t" \
  "s_cbranch_scc1 3b\n\t" \
  "s_waitcnt vmcnt(0)\n\t" \
  "v_max_u32 v116, v32, v33\n\t" \
  "v_max3_u32 v116, v116, v34, v35\n\t" \
  "v_max3_u32 v116, v116, v36, v37\n\t" \
  "v_max3_u32 v116, v116, v38, v39\n\t" \
  "v_max3_u32 v116, v116, v40, v41\n\t" \
  "v_max3_u32 v116, v116, v42, v43\n\t" \
  "v_max3_u32 v116, v116, v44, v45\n\t" \
  "v_max3_u32 v116, v116, v46, v47\n\t" \
  "v_cmp_eq_u32 vcc, -1, v116\n\t" \
  "s_cbranch_vccz 3b\n\t" \
  "s_bitcmp1_b32 s59, 6\n\t" \
  "s_cbranch_scc1 12f\n\t" \
  "buffer_load_dwordx4 v[32:35], v100, %[rh], s40 offen sc1\n\t" \
  "buffer_load_dwordx4 v[36:39], v100, %[rh], s76 offen sc1\n\t" \
  "buffer_load_dwordx4 v[40:43], v100, %[rh], s77 offen sc1\n\t" \
  "buffer_load_dwordx4 v[44:47], v100, %[rh], s78 offen sc1\n\t" \
  "s_branch 17b\n\t" \
  "12:\n\t" \
  "s_mov_b32 s42, s58\n\t" \
  "s_branch 99f\n\t" \
  "92:\n\t" \
  "s_mov_b32 s58, s42\n\t" \
  "20:\n\t" \
  "s_and_b32 s76, s44, 7\n\t" \
  "s_lshl_b32 s76, s76, 15\n\t" \
  "s_add_i32 s40, s54, s76\n\t" \
  "s_and_b32 s76, s44, 3\n\t" \
  "s_lshl_b32 s45, s76, 14\n\t" \
  "s_add_i32 s47, s44, 0\n\t" \
  "s_min_u32 s47, s47, 63\n\t" \
  "s_lshl_b32 s47, s47, 18\n\t" \
  "s_add_i32 s47, s47, s55\n\t" \
  "s_add_i32 s97, s47, 0x1000\n\t" \
  "s_add_i32 s49, s44, -1\n\t" \
  "s_and_b32 s48, s49, 3\n\t" \
  "s_lshl_b32 s48, s48, 14\n\t" \
  "s_lshl_b32 s50, s53, 8\n\t" \
  "s_mov_b32 s59, 0\n\t" \
  "s_mov_b32 s92, s51\n\t" \
  "s_mov_b32 s93, s53\n\t" \
  "s_xor_b32 s76, s40, 0x400\n\t" \
  "s_xor_b32 s77, s40, 0x800\n\t" \
  "s_xor_b32 s78, s40, 0xc00\n\t" \
  "s_cmp_lg_u32 s58, 0\n\t" \
  "s_cbranch_scc1 11f\n\t" \
  "10:\n\t" \
  "ds_read_b128 v[48:51], v103 offset:0\n\t" \
  "ds_read_b128 v[52:55], v103 offset:1024\n\t" \
  "ds_read_b128 v[56:59], v103 offset:2048\n\t" \
  "ds_read_b128 v[60:63], v103 offset:3072\n\t" \
  "ds_read_b128 v[16:19], v103 offset:16384\n\t" \
  "ds_read_b128 v[20:23], v103 offset:17408\n\t" \
  "ds_read_b128 v[24:27], v103 offset:18432\n\t" \
  "ds_read_b128 v[28:31], v103 offset:19456\n\t" \
  "ds_read_b128 v[84:87], v103 offset:49152\n\t" \
  "ds_read_b128 v[88:91], v103 offset:50176\n\t" \
  "ds_read_b128 v[92:95], v103 offset:51200\n\t" \
  "ds_read_b128 v[96:99], v103 offset:52224\n\t" \
  "s_waitcnt lgkmcnt(4)\n\t" \
  "ds_read_b128 v[0:3], v102 offset:16384\n\t" \
  "ds_read_b128 v[4:7], v102 offset:17408\n\t" \
  "ds_read_b128 v[8:11], v102 offset:18432\n\t" \
  "ds_read_b128 v[12:15], v102 offset:19456\n\t" \
  "s_bitcmp1_b32 s58, 1\n\t" \
  "s_cselect_b64 s[80:81], -1, 0\n\t" \
  "s_cmp_lg_u32 s58, 0\n\t" \
  "s_cbranch_scc1 17f\n\t" \
  "s_mov_b32 exec_hi, 0\n\t" \
  "s_waitcnt vmcnt(2)\n\t" \
  "v_mov_b32 v116, v118\n\t" \
  "s_branch 15f\n\t" \
  "13:\n\t" \
  "buffer_load_dword v116, v110, %[rh], s48 offen sc1\n\t" \
  "s_waitcnt vmcnt(0)\n\t" \
  "15:\n\t" \
  "v_mov_b32 v117, 20000\n\t" \
  "v_cmp_ne_u32_sdwa vcc, v116, s49 src0_sel:BYTE_0 src1_sel:DWORD\n\t" \
  "v_subrev_u32 v116, s50, v116\n\t" \
  "v_ashrrev_i32 v116, 8, v116\n\t" \
  "s_nop 1\n\t" \
  "v_max_i32_dpp v116, v116, v116 quad_perm:[1,0,3,2] row_mask:0xf bank_mask:0xf\n\t" \
  "s_cbranch_vccnz 14f\n\t" \
  "s_nop 0\n\t" \
  "v_max_i32_dpp v116, v116, v116 quad_perm:[2,3,0,1] row_mask:0xf bank_mask:0xf\n\t" \
  "s_nop 1\n\t" \
  "v_max_i32_dpp v116, v116, v116 row_half_mirror row_mask:0xf bank_mask:0xf\n\t" \
  "s_nop 1\n\t" \
  "v_max_i32_dpp v116, v116, v116 row_mirror row_mask:0xf bank_mask:0xf\n\t" \
  "v_med3_i32 v116, v116, 0, v117\n\t" \
  "v_mul_u32_u24 v116, s52, v116\n\t" \
  "v_lshrrev_b32 v116, 16, v116\n\t" \
  "s_mov_b64 exec, -1\n\t" \
  "v_readlane_b32 s84, v116, 0\n\t" \
  "v_readlane_b32 s85, v116, 16\n\t" \
  "s_max_u32 s84, s84, s85\n\t" \
  "s_add_i32 s84, s84, s51\n\t" \
  "1:\n\t" \
  "s_memtime vcc\n\t" \
  "s_waitcnt lgkmcnt(0)\n\t" \
  "s_sub_i32 s79, s84, vcc_lo\n\t" \
  "s_cmp_gt_i32 s79, 128\n\t" \
  "s_cbranch_scc1 1b\n\t" \
  "2:\n\t" \
  "s_sub_i32 s79, s79, 33\n\t" \
  "s_cmp_gt_i32 s79, 56\n\t" \
  "s_cbranch_scc1 2b\n\t" \
  "buffer_load_dwordx4 v[32:35], v100, %[rh], s40 offen sc1\n\t" \
  "s_nop 12\n\t" \
  "buffer_load_dwordx4 v[36:39], v100, %[rh], s76 offen sc1\n\t" \
  "s_nop 12\n\t" \
  "buffer_load_dwordx4 v[40:43], v100, %[rh], s77 offen sc1\n\t" \
  "s_nop 12\n\t" \
  "buffer_load_dwordx4 v[44:47], v100, %[rh], s78 offen sc1\n\t" \
  "s_memtime vcc\n\t" \
  "s_memrealtime s[82:83]\n\t" \
  "s_nop 4\n\t" \
  "s_memrealtime s[84:85]\n\t" \
  "s_nop 4\n\t" \
  "s_memrealtime s[94:95]\n\t" \
  "s_nop 4\n\t" \
  "s_memrealtime s[86:87]\n\t" \
  "s_nop 4\n\t" \
  "s_memrealtime s[88:89]\n\t" \
  "s_nop 4\n\t" \
  "s_memrealtime s[90:91]\n\t" \
  "s_mov_b32 s92, 0\n\t" \
  "s_waitcnt lgkmcnt(0)\n\t" \
  "s_cmp_eq_u32 s82, s84\n\t" \
  "s_addc_u32 s92, s92, 0\n\t" \
  "s_cmp_eq_u32 s82, s94\n\t" \
  "s_addc_u32 s92, s92, 0\n\t" \
  "s_cmp_eq_u32 s82, s86\n\t" \
  "s_addc_u32 s92, s92, 0\n\t" \
  "s_cmp_eq_u32 s82, s88\n\t" \
  "s_addc_u32 s92, s92, 0\n\t" \
  "s_cmp_eq_u32 s82, s90\n\t" \
  "s_addc_u32 s92, s92, 0\n\t" \
  "s_mul_i32 s92, s92, 24\n\t" \
  "s_add_i32 s92, s92, vcc_lo\n\t" \
  "s_lshl4_add_u32 s93, s82, 64\n\t" \
  "3:\n\t" \
  "v_mov_b64 v[68:69], 0\n\t" \
  "v_mov_b64 v[70:71], 0\n\t" \
  "v_mov_b64 v[72:73], 0\n\t" \
  "v_mov_b64 v[74:75], 0\n\t" \
  "v_mov_b64 v[76:77], 0\n\t" \
  "v_mov_b64 v[78:79], 0\n\t" \
  "v_mov_b64 v[80:81], 0\n\t" \
  "v_mov_b64 v[82:83], 0\n\t" \
  "s_waitcnt lgkmcnt(0)\n\t" \
  "s_waitcnt vmcnt(3)\n\t" \
  "v_smfmac_f32_16x16x64_bf16 v[68:71], v[32:35], a[192:199], v104\n\t" \
  "ds_write_b128 v101, v[32:35] offset:8192\n\t" \
  "v_smfmac_f32_16x16x64_bf16 v[72:75], v[32:35], a[200:207], v104\n\t" \
  "v_smfmac_f32_16x16x64_bf16 v[76:79], v[32:35], v[48:55], v104\n\t" \
  "v_mov_b32 v116, s49\n\t" \
  "buffer_store_dword v116, v109, %[rh], 0 offen sc1\n\t" \
  "v_smfmac_f32_16x16x64_bf16 v[80:83], v[32:35], v[56:63], v104\n\t" \
  "s_waitcnt vmcnt(3)\n\t" \
  "v_smfmac_f32_16x16x64_bf16 v[68:71], v[36:39], a[208:215], v104\n\t" \
  "ds_write_b128 v101, v[36:39] offset:9216\n\t" \
  "v_smfmac_f32_16x16x64_bf16 v[72:75], v[36:39], a[216:223], v104\n\t" \
  "v_smfmac_f32_16x16x64_bf16 v[76:79], v[36:39], a[224:231], v104\n\t" \
  "v_cmp_u_f32 vcc, v68, v69\n\t" \
  "v_smfmac_f32_16x16x64_bf16 v[80:83], v[36:39], a[232:239], v104\n\t" \
  "v_cmp_u_f32 s[82:83], v70, v71\n\t" \
  "s_waitcnt vmcnt(2)\n\t" \
  "v_smfmac_f32_16x16x64_bf16 v[68:71], v[40:43], v[16:23], v104\n\t" \
  "ds_write_b128 v101, v[40:43] offset:10240\n\t" \
  "v_smfmac_f32_16x16x64_bf16 v[72:75], v[40:43], v[24:31], v104\n\t" \
  "s_or_b64 vcc, vcc, s[82:83]\n\t" \
  "s_or_b64 vcc, vcc, s[80:81]\n\t" \
  "s_waitcnt lgkmcnt(1)\n\t" \
  "v_smfmac_f32_16x16x64_bf16 v[76:79], v[40:43], a[240:247], v104\n\t" \
  "s_cbranch_scc1 8f\n\t" \
  "5:\n\t" \
  "s_barrier\n\t" \
  "4:\n\t" \
  "s_memtime s[56:57]\n\t" \
  "ds_read_b128 v[48:51], v101 offset:12288\n\t" \
  "v_smfmac_f32_16x16x64_bf16 v[80:83], v[40:43], a[248:255], v104\n\t" \
  "ds_read_b128 v[52:55], v101 offset:13312\n\t" \
  "ds_read_b128 v[56:59], v101 offset:0\n\t" \
  "s_waitcnt vmcnt(1)\n\t" \
  "v_smfmac_f32_16x16x64_bf16 v[68:71], v[44:47], v[120:127], v104\n\t" \
  "v_smfmac_f32_16x16x64_bf16 v[72:75], v[44:47], v[128:135], v104\n\t" \
  "ds_write_b128 v101, v[44:47] offset:11264\n\t" \
  "v_smfmac_f32_16x16x64_bf16 v[76:79], v[44:47], v[136:143], v104\n\t" \
  "v_cmp_u_f32 vcc, v68, v69\n\t" \
  "v_smfmac_f32_16x16x64_bf16 v[80:83], v[44:47], v[144:151], v104\n\t" \
  "v_cmp_u_f32 s[82:83], v70, v71\n\t" \
  "s_waitcnt lgkmcnt(3)\n\t" \
  "v_smfmac_f32_16x16x64_bf16 v[68:71], v[48:51], v[152:159], v104\n\t" \
  "ds_read_b128 v[60:63], v101 offset:1024\n\t" \
  "ds_read_b128 v[64:67], v101 offset:4096\n\t" \
  "v_smfmac_f32_16x16x64_bf16 v[72:75], v[48:51], v[160:167], v104\n\t" \
  "s_or_b64 vcc, vcc, s[82:83]\n\t" \
  "s_waitcnt lgkmcnt(2)\n\t" \
  "v_smfmac_f32_16x16x64_bf16 v[68:71], v[52:55], v[168:175], v104\n\t" \
  "s_cbranch_scc1 9f\n\t" \
  "6:\n\t" \
  "s_barrier\n\t" \
  "v_smfmac_f32_16x16x64_bf16 v[72:75], v[52:55], v[176:183], v104\n\t" \
  "ds_read_b128 v[48:51], v101 offset:5120\n\t" \
  "v_smfmac_f32_16x16x64_bf16 v[68:71], v[56:59], a[0:7], v104\n\t" \
  "ds_read_b128 v[52:55], v101 offset:14336\n\t" \
  "v_smfmac_f32_16x16x64_bf16 v[72:75], v[56:59], a[8:15], v104\n\t" \
  "s_waitcnt lgkmcnt(3)\n\t" \
  "v_smfmac_f32_16x16x64_bf16 v[68:71], v[60:63], a[16:23], v104\n\t" \
  "ds_read_b128 v[56:59], v101 offset:15360\n\t" \
  "v_smfmac_f32_16x16x64_bf16 v[72:75], v[60:63], a[24:31], v104\n\t" \
  "s_waitcnt lgkmcnt(3)\n\t" \
  "v_smfmac_f32_16x16x64_bf16 v[68:71], v[64:67], a[96:103], v104\n\t" \
  "ds_read_b128 v[60:63], v101 offset:2048\n\t" \
  "v_smfmac_f32_16x16x64_bf16 v[72:75], v[64:67], a[104:111], v104\n\t" \
  "ds_read_b128 v[16:19], v102 offset:49152\n\t" \
  "s_waitcnt lgkmcnt(4)\n\t" \
  "v_smfmac_f32_16x16x64_bf16 v[68:71], v[48:51], a[112:119], v104\n\t" \
  "ds_read_b128 v[64:67], v101 offset:3072\n\t" \
  "v_smfmac_f32_16x16x64_bf16 v[72:75], v[48:51], a[120:127], v104\n\t" \
  "ds_read_b128 v[20:23], v102 offset:50176\n\t" \
  "s_waitcnt lgkmcnt(5)\n\t" \
  "v_smfmac_f32_16x16x64_bf16 v[68:71], v[52:55], v[84:91], v104\n\t" \
  "ds_read_b128 v[48:51], v101 offset:7168\n\t" \
  "v_smfmac_f32_16x16x64_bf16 v[72:75], v[52:55], v[92:99], v104\n\t" \
  "ds_read_b128 v[24:27], v102 offset:51200\n\t" \
  "s_waitcnt lgkmcnt(6)\n\t" \
  "v_smfmac_f32_16x16x64_bf16 v[68:71], v[56:59], v[216:223], v104\n\t" \
  "ds_read_b128 v[52:55], v101 offset:6144\n\t" \
  "v_smfmac_f32_16x16x64_bf16 v[72:75], v[56:59], v[224:231], v104\n\t" \
  "ds_read_b128 v[28:31], v102 offset:52224\n\t" \
  "s_waitcnt lgkmcnt(7)\n\t" \
  "v_smfmac_f32_16x16x64_bf16 v[68:71], v[60:63], v[0:7], v104\n\t" \
  "buffer_load_dword v117, v107, %[ru], 0 offen sc1\n\t" \
  "v_smfmac_f32_16x16x64_bf16 v[72:75], v[60:63], v[8:15], v104\n\t" \
  "s_waitcnt lgkmcnt(5)\n\t" \
  "v_smfmac_f32_16x16x64_bf16 v[68:71], v[64:67], a[64:71], v104\n\t" \
  "ds_read_b128 v[0:3], v103 offset:32768\n\t" \
  "v_smfmac_f32_16x16x64_bf16 v[72:75], v[64:67], a[72:79], v104\n\t" \
  "ds_read_b128 v[4:7], v103 offset:33792\n\t" \
  "s_waitcnt lgkmcnt(5)\n\t" \
  "v_smfmac_f32_16x16x64_bf16 v[68:71], v[48:51], a[160:167], v104\n\t" \
  "ds_read_b128 v[8:11], v103 offset:34816\n\t" \
  "s_waitcnt lgkmcnt(4)\n\t" \
  "v_smfmac_f32_16x16x64_bf16 v[68:71], v[52:55], v[16:23], v104\n\t" \
  "ds_read_b128 v[12:15], v103 offset:35840\n\t" \
  "v_smfmac_f32_16x16x64_bf16 v[72:75], v[48:51], a[168:175], v104\n\t" \
  "s_waitcnt lgkmcnt(4)\n\t" \
  "v_smfmac_f32_16x16x64_bf16 v[72:75], v[52:55], v[24:31], v104\n\t" \
  "v_add_f32 v96, v68, v70\n\t" \
  "v_add_f32 v97, v69, v71\n\t" \
  "v_smfmac_f32_16x16x64_bf16 v[76:79], v[56:59], v[232:239], v104\n\t" \
  "v_cvt_pk_bf16_f32 v96, v96, v97\n\t" \
  "ds_read_b128 v[24:27], v102 offset:2048\n\t" \
  "v_smfmac_f32_16x16x64_bf16 v[80:83], v[56:59], v[240:247], v104\n\t" \
  "v_add_f32 v97, v72, v74\n\t" \
  "v_add_f32 v98, v73, v75\n\t" \
  "v_cvt_pk_bf16_f32 v97, v97, v98\n\t" \
  "ds_write2st64_b32 v105, v96, v97 offset1:1\n\t" \
  "v_smfmac_f32_16x16x64_bf16 v[76:79], v[60:63], a[48:55], v104\n\t" \
  "v_smfmac_f32_16x16x64_bf16 v[80:83], v[60:63], a[56:63], v104\n\t" \
  "v_smfmac_f32_16x16x64_bf16 v[76:79], v[64:67], a[80:87], v104\n\t" \
  "v_smfmac_f32_16x16x64_bf16 v[80:83], v[64:67], a[88:95], v104\n\t" \
  "s_waitcnt lgkmcnt(0)\n\t" \
  "s_barrier\n\t" \
  "ds_read_b128 v[56:59], v101 offset:12288\n\t" \
  "v_smfmac_f32_16x16x64_bf16 v[76:79], v[48:51], a[176:183], v104\n\t" \
  "buffer_load_dwordx4 v[84:87], v100, %[ru], s47 offen offset:0 sc1\n\t" \
  "v_smfmac_f32_16x16x64_bf16 v[80:83], v[48:51], a[184:191], v104\n\t" \
  "ds_read_b128 v[60:63], v101 offset:5120\n\t" \
  "v_smfmac_f32_16x16x64_bf16 v[76:79], v[52:55], a[144:151], v104\n\t" \
  "ds_read_b128 v[16:19], v102 offset:0\n\t" \
  "buffer_load_dwordx4 v[88:91], v100, %[ru], s47 offen offset:1024 sc1\n\t" \
  "v_smfmac_f32_16x16x64_bf16 v[80:83], v[52:55], a[152:159], v104\n\t" \
  "ds_read_b128 v[64:67], v101 offset:14336\n\t" \
  "s_waitcnt lgkmcnt(3)\n\t" \
  "v_smfmac_f32_16x16x64_bf16 v[76:79], v[56:59], v[0:7], v104\n\t" \
  "ds_read_b128 v[20:23], v102 offset:1024\n\t" \
  "buffer_load_dwordx4 v[92:95], v100, %[ru], s47 offen offset:2048 sc1\n\t" \
  "v_smfmac_f32_16x16x64_bf16 v[80:83], v[56:59], v[8:15], v104\n\t" \
  "ds_read_b128 v[48:51], v101 offset:0\n\t" \
  "s_waitcnt lgkmcnt(4)\n\t" \
  "v_smfmac_f32_16x16x64_bf16 v[76:79], v[60:63], a[128:135], v104\n\t" \
  "ds_read_b128 v[28:31], v102 offset:3072\n\t" \
  "buffer_load_dwordx4 v[96:99], v100, %[ru], s47 offen offset:3072 sc1\n\t" \
  "v_smfmac_f32_16x16x64_bf16 v[80:83], v[60:63], a[136:143], v104\n\t" \
  "ds_read_b128 v[52:55], v101 offset:13312\n\t" \
  "s_waitcnt lgkmcnt(4)\n\t" \
  "v_smfmac_f32_16x16x64_bf16 v[76:79], v[64:67], v[200:207], v104\n\t" \
  "ds_read_b128 v[0:3], v102 offset:32768\n\t" \
  "buffer_load_dwordx4 v[32:35], v100, %[ru], s97 offen offset:0 sc1\n\t" \
  "v_smfmac_f32_16x16x64_bf16 v[80:83], v[64:67], v[208:215], v104\n\t" \
  "ds_read_b128 v[56:59], v101 offset:1024\n\t" \
  "ds_read_b128 v[4:7], v102 offset:33792\n\t" \
  "s_waitcnt lgkmcnt(5)\n\t" \
  "v_smfmac_f32_16x16x64_bf16 v[76:79], v[48:51], v[16:23], v104\n\t" \
  "ds_read_b128 v[60:63], v101 offset:4096\n\t" \
  "buffer_load_dwordx4 v[36:39], v100, %[ru], s97 offen offset:1024 sc1\n\t" \
  "s_waitcnt lgkmcnt(5)\n\t" \
  "v_smfmac_f32_16x16x64_bf16 v[80:83], v[48:51], v[24:31], v104\n\t" \
  "ds_read_b128 v[8:11], v102 offset:34816\n\t" \
  "s_waitcnt lgkmcnt(5)\n\t" \
  "v_smfmac_f32_16x16x64_bf16 v[76:79], v[52:55], v[184:191], v104\n\t" \
  "ds_read_b128 v[12:15], v102 offset:35840\n\t" \
  "buffer_load_dwordx4 v[40:43], v100, %[ru], s97 offen offset:2048 sc1\n\t" \
  "v_smfmac_f32_16x16x64_bf16 v[80:83], v[52:55], v[192:199], v104\n\t" \
  "s_waitcnt lgkmcnt(4)\n\t" \
  "v_smfmac_f32_16x16x64_bf16 v[76:79], v[56:59], a[32:39], v104\n\t" \
  "buffer_load_dwordx4 v[44:47], v100, %[ru], s97 offen offset:3072 sc1\n\t" \
  "s_waitcnt lgkmcnt(2)\n\t" \
  "v_smfmac_f32_16x16x64_bf16 v[76:79], v[60:63], v[0:7], v104\n\t" \
  "v_smfmac_f32_16x16x64_bf16 v[80:83], v[56:59], a[40:47], v104\n\t" \
  "s_waitcnt lgkmcnt(0)\n\t" \
  "v_smfmac_f32_16x16x64_bf16 v[80:83], v[60:63], v[8:15], v104\n\t" \
  "v_add_f32 v68, v76, v78\n\t" \
  "v_add_f32 v69, v77, v79\n\t" \
  "v_cvt_pk_bf16_f32 v68, v68, v69\n\t" \
  "ds_write_b32 v106, v68\n\t" \
  "ds_read_b128 v[0:3], v101 offset:20480\n\t" \
  "ds_read_b128 v[4:7], v101 offset:21504\n\t" \
  "ds_read_b128 v[8:11], v101 offset:22528\n\t" \
  "ds_read_b128 v[12:15], v101 offset:23552\n\t" \
  "v_add_f32 v70, v80, v82\n\t" \
  "v_add_f32 v71, v81, v83\n\t" \
  "v_cvt_pk_bf16_f32 v70, v70, v71\n\t" \
  "ds_write_b32 v106, v70 offset:256\n\t" \
  "ds_read_b128 v[16:19], v101 offset:24576\n\t" \
  "ds_read_b128 v[20:23], v101 offset:25600\n\t" \
  "ds_read_b128 v[24:27], v101 offset:26624\n\t" \
  "ds_read_b128 v[28:31], v101 offset:27648\n\t" \
  "s_waitcnt lgkmcnt(0)\n\t" \
  "s_barrier\n\t" \
  "s_waitcnt vmcnt(0)\n\t" \
  "v_mfma_f32_16x16x32_bf16 v[64:67], v[0:3], v[32:35], 0\n\t" \
  "v_mfma_f32_16x16x32_bf16 v[68:71], v[16:19], v[32:35], v[112:115]\n\t" \
  "v_mfma_f32_16x16x32_bf16 v[72:75], v[0:3], v[84:87], v[112:115]\n\t" \
  "v_readfirstlane_b32 s76, v117\n\t" \
  "s_add_i32 s77, s44, 1\n\t" \
  "s_min_i32 s77, s77, 63\n\t" \
  "s_cmp_lt_i32 s76, s77\n\t" \
  "s_cselect_b32 s42, 32, 0\n\t" \
  "v_mfma_f32_16x16x32_bf16 v[64:67], v[4:7], v[36:39], v[64:67]\n\t" \
  "v_mfma_f32_16x16x32_bf16 v[68:71], v[20:23], v[36:39], v[68:71]\n\t" \
  "v_mfma_f32_16x16x32_bf16 v[72:75], v[4:7], v[88:91], v[72:75]\n\t" \
  "ds_read_b64_tr_b16 v[48:49], v108 offset:0\n\t" \
  "ds_read_b64_tr_b16 v[50:51], v108 offset:64\n\t" \
  "ds_read_b64_tr_b16 v[52:53], v108 offset:512\n\t" \
  "ds_read_b64_tr_b16 v[54:55], v108 offset:576\n\t" \
  "ds_read_b64_tr_b16 v[56:57], v108 offset:1024\n\t" \
  "ds_read_b64_tr_b16 v[58:59], v108 offset:1088\n\t" \
  "ds_read_b64_tr_b16 v[60:61], v108 offset:1536\n\t" \
  "ds_read_b64_tr_b16 v[62:63], v108 offset:1600\n\t" \
  "v_mfma_f32_16x16x32_bf16 v[64:67], v[8:11], v[40:43], v[64:67]\n\t" \
  "v_mfma_f32_16x16x32_bf16 v[68:71], v[24:27], v[40:43], v[68:71]\n\t" \
  "v_mfma_f32_16x16x32_bf16 v[72:75], v[8:11], v[92:95], v[72:75]\n\t" \
  "v_mfma_f32_16x16x32_bf16 v[64:67], v[12:15], v[44:47], v[64:67]\n\t" \
  "v_mfma_f32_16x16x32_bf16 v[68:71], v[28:31], v[44:47], v[68:71]\n\t" \
  "v_mfma_f32_16x16x32_bf16 v[72:75], v[12:15], v[96:99], v[72:75]\n\t" \
  "s_waitcnt lgkmcnt(0)\n\t" \
  "v_mfma_f32_16x16x32_bf16 v[76:79], v[48:51], v[32:35], 0\n\t" \
  "v_mfma_f32_16x16x32_bf16 v[80:83], v[48:51], v[84:87], 0\n\t" \
  "v_mfma_f32_16x16x32_bf16 v[76:79], v[52:55], v[36:39], v[76:79]\n\t" \
  "v_max3_f32 v1, v64, v65, v66\n\t" \
  "v_mfma_f32_16x16x32_bf16 v[80:83], v[52:55], v[88:91], v[80:83]\n\t" \
  "v_max3_f32 v1, v1, v67, v68\n\t" \
  "v_mfma_f32_16x16x32_bf16 v[76:79], v[56:59], v[40:43], v[76:79]\n\t" \
  "v_max3_f32 v0, v72, v73, v74\n\t" \
  "v_mfma_f32_16x16x32_bf16 v[80:83], v[56:59], v[92:95], v[80:83]\n\t" \
  "v_max3_f32 v1, v1, v69, v70\n\t" \
  "v_mfma_f32_16x16x32_bf16 v[76:79], v[60:63], v[44:47], v[76:79]\n\t" \
  "v_max_f32 v0, v0, v75\n\t" \
  "v_mfma_f32_16x16x32_bf16 v[80:83], v[60:63], v[96:99], v[80:83]\n\t" \
  "v_max_f32 v1, v1, v71\n\t" \
  "s_nop 1\n\t" \
  "v_permlane32_swap_b32 v0, v1\n\t" \
  "s_lshl_b32 s76, s45, 3\n\t" \
  "v_max_f32 v0, v0, v1\n\t" \
  "s_nop 2\n\t" \
  "v_mov_b32_dpp v80, v76 quad_perm:[0,1,2,3] row_mask:0xc bank_mask:0xf\n\t" \
  "v_mov_b32_dpp v81, v77 quad_perm:[0,1,2,3] row_mask:0xc bank_mask:0xf\n\t" \
  "v_mov_b32_dpp v82, v78 quad_perm:[0,1,2,3] row_mask:0xc bank_mask:0xf\n\t" \
  "v_mov_b32_dpp v83, v79 quad_perm:[0,1,2,3] row_mask:0xc bank_mask:0xf\n\t" \
  "v_max3_f32 v0, v0, v80, v81\n\t" \
  "v_max3_f32 v0, v0, v82, v83\n\t" \
  "v_mov_b32 v1, v0\n\t" \
  "s_nop 1\n\t" \
  "v_permlane16_swap_b32 v0, v1\n\t" \
  "s_nop 0\n\t" \
  "v_max_f32 v0, v0, v1\n\t" \
  "v_mul_f32 v0, %[cs], v0\n\t" \
  "v_mov_b32 v1, v0\n\t" \
  "v_mov_b32 v2, v0\n\t" \
  "s_nop 1\n\t" \
  "v_permlane32_swap_b32 v1, v2\n\t" \
  "s_nop 0\n\t" \
  "v_fma_f32 v80, v80, %[cs], -v0\n\t" \
  "v_fma_f32 v81, v81, %[cs], -v0\n\t" \
  "v_fma_f32 v82, v82, %[cs], -v0\n\t" \
  "v_fma_f32 v83, v83, %[cs], -v0\n\t" \
  "v_fma_f32 v72, v72, %[cs], -v1\n\t" \
  "v_fma_f32 v73, v73, %[cs], -v1\n\t" \
  "v_fma_f32 v74, v74, %[cs], -v1\n\t" \
  "v_fma_f32 v75, v75, %[cs], -v1\n\t" \
  "v_fma_f32 v64, v64, %[cs], -v2\n\t" \
  "v_fma_f32 v65, v65, %[cs], -v2\n\t" \
  "v_fma_f32 v66, v66, %[cs], -v2\n\t" \
  "v_fma_f32 v67, v67, %[cs], -v2\n\t" \
  "v_fma_f32 v68, v68, %[cs], -v2\n\t" \
  "v_fma_f32 v69, v69, %[cs], -v2\n\t" \
  "v_fma_f32 v70, v70, %[cs], -v2\n\t" \
  "v_fma_f32 v71, v71, %[cs], -v2\n\t" \
  "v_exp_f32 v80, v80\n\t" \
  "v_exp_f32 v81, v81\n\t" \
  "v_exp_f32 v82, v82\n\t" \
  "v_exp_f32 v83, v83\n\t" \
  "v_exp_f32 v72, v72\n\t" \
  "v_exp_f32 v73, v73\n\t" \
  "v_exp_f32 v74, v74\n\t" \
  "v_exp_f32 v75, v75\n\t" \
  "v_exp_f32 v64, v64\n\t" \
  "v_exp_f32 v65, v65\n\t" \
  "v_exp_f32 v66, v66\n\t" \
  "v_exp_f32 v67, v67\n\t" \
  "v_exp_f32 v68, v68\n\t" \
  "v_exp_f32 v69, v69\n\t" \
  "v_exp_f32 v70, v70\n\t" \
  "v_exp_f32 v71, v71\n\t" \
  "v_add_f32 v3, v80, v81\n\t" \
  "v_add_f32 v0, v72, v73\n\t" \
  "v_add_f32 v1, v64, v65\n\t" \
  "v_add_f32 v2, v68, v69\n\t" \
  "v_add_f32 v3, v3, v82\n\t" \
  "v_add_f32 v0, v0, v74\n\t" \
  "v_add_f32 v1, v1, v66\n\t" \
  "v_add_f32 v2, v2, v70\n\t" \
  "v_add_f32 v0, v0, v75\n\t" \
  "v_add_f32 v1, v1, v67\n\t" \
  "v_add_f32 v2, v2, v71\n\t" \
  "v_add_f32 v1, v1, v2\n\t" \
  "v_add_f32 v3, v3, v83\n\t" \
  "s_nop 0\n\t" \
  "v_permlane32_swap_b32 v0, v1\n\t" \
  "s_nop 0\n\t" \
  "v_add_f32 v0, v0, v1\n\t" \
  "v_add_f32 v0, v0, v3\n\t" \
  "v_mov_b32 v1, v0\n\t" \
  "s_nop 1\n\t" \
  "v_permlane16_swap_b32 v0, v1\n\t" \
  "s_nop 0\n\t" \
  "v_add_f32 v0, v0, v1\n\t" \
  "v_rcp_f32 v0, v0\n\t" \
  "buffer_load_dword v118, v110, %[rh], s45 offen sc1\n\t" \
  "v_mov_b32 v1, v0\n\t" \
  "v_mov_b32 v2, v0\n\t" \
  "s_nop 1\n\t" \
  "v_permlane32_swap_b32 v1, v2\n\t" \
  "s_nop 0\n\t" \
  "v_mul_f32 v80, v80, v0\n\t" \
  "v_mul_f32 v81, v81, v0\n\t" \
  "v_mul_f32 v82, v82, v0\n\t" \
  "v_mul_f32 v83, v83, v0\n\t" \
  "v_mul_f32 v72, v72, v1\n\t" \
  "v_mul_f32 v73, v73, v1\n\t" \
  "v_mul_f32 v74, v74, v1\n\t" \
  "v_mul_f32 v75, v75, v1\n\t" \
  "v_mul_f32 v64, v64, v2\n\t" \
  "v_mul_f32 v65, v65, v2\n\t" \
  "v_mul_f32 v66, v66, v2\n\t" \
  "v_mul_f32 v67, v67, v2\n\t" \
  "v_mul_f32 v68, v68, v2\n\t" \
  "v_mul_f32 v69, v69, v2\n\t" \
  "v_mul_f32 v70, v70, v2\n\t" \
  "v_mul_f32 v71, v71, v2\n\t" \
  "v_cvt_pk_bf16_f32 v0, v72, v73\n\t" \
  "v_cvt_pk_bf16_f32 v1, v64, v65\n\t" \
  "v_cvt_pk_bf16_f32 v2, v74, v75\n\t" \
  "v_cvt_pk_bf16_f32 v3, v66, v67\n\t" \
  "v_cvt_pk_bf16_f32 v4, v68, v69\n\t" \
  "v_cvt_pk_bf16_f32 v5, v80, v81\n\t" \
  "v_cvt_pk_bf16_f32 v6, v70, v71\n\t" \
  "v_cvt_pk_bf16_f32 v7, v82, v83\n\t" \
  "buffer_store_dwordx4 v[0:3], v119, %[rh], s76 offen sc1\n\t" \
  "buffer_store_dwordx4 v[4:7], v119, %[rh], s76 offen offset:1024 sc1\n\t" \
  "s_add_i32 s41, s56, 2050\n\t" \
  "s_sub_i32 s76, s92, s51\n\t" \
  "s_sub_i32 s77, s93, s53\n\t" \
  "s_cmp_eq_u32 s76, 0\n\t" \
  "s_cbranch_scc1 21f\n\t" \
  "v_cvt_f32_u32 v117, s76\n\t" \
  "v_cvt_f32_u32 v116, s77\n\t" \
  "v_rcp_f32 v117, v117\n\t" \
  "s_mov_b32 s51, s92\n\t" \
  "v_mul_f32 v117, v117, v116\n\t" \
  "v_max_f32 v117, 0x3f028f5c, v117\n\t" \
  "v_min_f32 v117, 0x3ffeb852, v117\n\t" \
  "v_rcp_f32 v116, v117\n\t" \
  "v_mul_f32 v117, 0x4f000000, v117\n\t" \
  "v_mul_f32 v116, 0x47800000, v116\n\t" \
  "v_cvt_u32_f32 v117, v117\n\t" \
  "v_cvt_u32_f32 v116, v116\n\t" \
  "s_mov_b32 s53, s93\n\t" \
  "v_readfirstlane_b32 s46, v117\n\t" \
  "v_readfirstlane_b32 s52, v116\n\t" \
  "21:\n\t" \
  "s_add_i32 s44, s44, 1\n\t" \
  "s_and_b32 s58, s58, 4\n\t" \
  "s_and_b32 s76, s44, 64\n\t" \
  "s_or_b32 s42, s42, s76\n\t" \
  "s_cbranch_scc0 20b\n\t" \
  "s_branch 99f\n\t" \
  "14:\n\t" \
  "s_add_u32 s59, s59, 0x10000\n\t" \
  "s_cmp_lt_u32 s59, 0x100000\n\t" \
  "s_cbranch_scc1 13b\n\t" \
  "s_mov_b64 exec, -1\n\t" \
  "s_mov_b32 s42, 16\n\t" \
  "s_branch 99f\n\t" \
  "8:\n\t" \
  "s_bitcmp1_b32 s58, 1\n\t" \
  "s_cbranch_scc1 4b\n\t" \
  "s_bitcmp1_b32 s58, 2\n\t" \
  "s_cbranch_scc1 5b\n\t" \
  "v_max3_u32 v116, v32, v33, v34\n\t" \
  "v_max3_u32 v117, v35, v36, v37\n\t" \
  "v_max3_u32 v116, v116, v38, v39\n\t" \
  "v_max_u32 v116, v116, v117\n\t" \
  "v_cmp_eq_u32 vcc, -1, v116\n\t" \
  "s_cbranch_vccz 5b\n\t" \
  "s_branch 0f\n\t" \
  "9:\n\t" \
  "s_bitcmp1_b32 s58, 2\n\t" \
  "s_cbranch_scc1 6b\n\t" \
  "v_max3_u32 v116, v40, v41, v42\n\t" \
  "v_max3_u32 v117, v43, v44, v45\n\t" \
  "v_max3_u32 v116, v116, v46, v47\n\t" \
  "v_max_u32 v116, v116, v117\n\t" \
  "v_cmp_eq_u32 vcc, -1, v116\n\t" \
  "s_cbranch_vccz 6b\n\t" \
  "s_or_b32 s58, s58, 2\n\t" \
  "0:\n\t" \
  "s_or_b32 s58, s58, 1\n\t" \
  "11:\n\t" \
  "buffer_load_dwordx4 v[32:35], v100, %[rh], s40 offen sc1\n\t" \
  "buffer_load_dwordx4 v[36:39], v100, %[rh], s76 offen sc1\n\t" \
  "buffer_load_dwordx4 v[40:43], v100, %[rh], s77 offen sc1\n\t" \
  "buffer_load_dwordx4 v[44:47], v100, %[rh], s78 offen sc1\n\t" \
  "s_waitcnt lgkmcnt(0)\n\t" \
  "s_branch 10b\n\t" \
  "17:\n\t" \
  "s_add_u32 s59, s59, 1\n\t" \
  "s_bitcmp1_b32 s58, 2\n\t" \
  "s_cbranch_scc1 3b\n\t" \
  "s_waitcnt vmcnt(0)\n\t" \
  "v_max_u32 v116, v32, v33\n\t" \
  "v_max3_u32 v116, v116, v34, v35\n\t" \
  "v_max3_u32 v116, v116, v36, v37\n\t" \
  "v_max3_u32 v116, v116, v38, v39\n\t" \
  "v_max3_u32 v116, v116, v40, v41\n\t" \
  "v_max3_u32 v116, v116, v42, v43\n\t" \
  "v_max3_u32 v116, v116, v44, v45\n\t" \
  "v_max3_u32 v116, v116, v46, v47\n\t" \
  "v_cmp_eq_u32 vcc, -1, v116\n\t" \
  "s_cbranch_vccz 3b\n\t" \
  "s_bitcmp1_b32 s59, 6\n\t" \
  "s_cbranch_scc1 12f\n\t" \
  "buffer_load_dwordx4 v[32:35], v100, %[rh], s40 offen sc1\n\t" \
  "buffer_load_dwordx4 v[36:39], v100, %[rh], s76 offen sc1\n\t" \
  "buffer_load_dwordx4 v[40:43], v100, %[rh], s77 offen sc1\n\t" \
  "buffer_load_dwordx4 v[44:47], v100, %[rh], s78 offen sc1\n\t" \
  "s_branch 17b\n\t" \
  "12:\n\t" \
  "s_mov_b32 s42, s58\n\t" \
  "s_branch 99f\n\t" \
  "93:\n\t" \
  "s_mov_b32 s58, s42\n\t" \
  "20:\n\t" \
  "s_and_b32 s76, s44, 7\n\t" \
  "s_lshl_b32 s76, s76, 15\n\t" \
  "s_add_i32 s40, s54, s76\n\t" \
  "s_and_b32 s76, s44, 3\n\t" \
  "s_lshl_b32 s45, s76, 14\n\t" \
  "s_add_i32 s47, s44, 1\n\t" \
  "s_min_u32 s47, s47, 63\n\t" \
  "s_lshl_b32 s47, s47, 18\n\t" \
  "s_add_i32 s47, s47, s55\n\t" \
  "s_add_i32 s97, s47, 0x1000\n\t" \
  "s_add_i32 s49, s44, -1\n\t" \
  "s_and_b32 s48, s49, 3\n\t" \
  "s_lshl_b32 s48, s48, 14\n\t" \
  "s_lshl_b32 s50, s53, 8\n\t" \
  "s_mov_b32 s59, 0\n\t" \
  "s_mov_b32 s92, s51\n\t" \
  "s_mov_b32 s93, s53\n\t" \
  "s_xor_b32 s76, s40, 0x400\n\t" \
  "s_xor_b32 s77, s40, 0x800\n\t" \
  "s_xor_b32 s78, s40, 0xc00\n\t" \
  "s_cmp_lg_u32 s58, 0\n\t" \
  "s_cbranch_scc1 11f\n\t" \
  "10:\n\t" \
  "ds_read_b128 v[48:51], v103 offset:32768\n\t" \
  "ds_read_b128 v[52:55], v103 offset:33792\n\t" \
  "ds_read_b128 v[56:59], v103 offset:34816\n\t" \
  "ds_read_b128 v[60:63], v103 offset:35840\n\t" \
  "ds_read_b128 v[16:19], v103 offset:49152\n\t" \
  "ds_read_b128 v[20:23], v103 offset:50176\n\t" \
  "ds_read_b128 v[24:27], v103 offset:51200\n\t" \
  "ds_read_b128 v[28:31], v103 offset:52224\n\t" \
  "ds_read_b128 v[84:87], v102 offset:16384\n\t" \
  "ds_read_b128 v[88:91], v102 offset:17408\n\t" \
  "ds_read_b128 v[92:95], v102 offset:18432\n\t" \
  "ds_read_b128 v[96:99], v102 offset:19456\n\t" \
  "s_waitcnt lgkmcnt(4)\n\t" \
  "ds_read_b128 v[0:3], v102 offset:49152\n\t" \
  "ds_read_b128 v[4:7], v102 offset:50176\n\t" \
  "ds_read_b128 v[8:11], v102 offset:51200\n\t" \
  "ds_read_b128 v[12:15], v102 offset:52224\n\t" \
  "s_bitcmp1_b32 s58, 1\n\t" \
  "s_cselect_b64 s[80:81], -1, 0\n\t" \
  "s_cmp_lg_u32 s58, 0\n\t" \
  "s_cbranch_scc1 17f\n\t" \
  "s_mov_b32 exec_hi, 0\n\t" \
  "16:\n\t" \
  "s_memtime vcc\n\t" \
  "s_waitcnt lgkmcnt(0)\n\t" \
  "s_sub_i32 s79, s41, vcc_lo\n\t" \
  "s_cmp_gt_i32 s79, 64\n\t" \
  "s_cbranch_scc1 16b\n\t" \
  "13:\n\t" \
  "buffer_load_dword v116, v110, %[rh], s48 offen sc1\n\t" \
  "s_waitcnt vmcnt(0)\n\t" \
  "15:\n\t" \
  "v_mov_b32 v117, 20000\n\t" \
  "v_cmp_ne_u32_sdwa vcc, v116, s49 src0_sel:BYTE_0 src1_sel:DWORD\n\t" \
  "v_subrev_u32 v116, s50, v116\n\t" \
  "v_ashrrev_i32 v116, 8, v116\n\t" \
  "s_nop 1\n\t" \
  "v_max_i32_dpp v116, v116, v116 quad_perm:[1,0,3,2] row_mask:0xf bank_mask:0xf\n\t" \
  "s_cbranch_vccnz 14f\n\t" \
  "s_nop 0\n\t" \
  "v_max_i32_dpp v116, v116, v116 quad_perm:[2,3,0,1] row_mask:0xf bank_mask:0xf\n\t" \
  "s_nop 1\n\t" \
  "v_max_i32_dpp v116, v116, v116 row_half_mirror row_mask:0xf bank_mask:0xf\n\t" \
  "s_nop 1\n\t" \
  "v_max_i32_dpp v116, v116, v116 row_mirror row_mask:0xf bank_mask:0xf\n\t" \
  "v_med3_i32 v116, v116, 0, v117\n\t" \
  "v_mul_u32_u24 v116, s52, v116\n\t" \
  "v_lshrrev_b32 v116, 16, v116\n\t" \
  "s_mov_b64 exec, -1\n\t" \
  "v_readlane_b32 s84, v116, 0\n\t" \
  "v_readlane_b32 s85, v116, 16\n\t" \
  "s_max_u32 s84, s84, s85\n\t" \
  "s_add_i32 s84, s84, s51\n\t" \
  "1:\n\t" \
  "s_memtime vcc\n\t" \
  "s_waitcnt lgkmcnt(0)\n\t" \
  "s_sub_i32 s79, s84, vcc_lo\n\t" \
  "s_cmp_gt_i32 s79, 128\n\t" \
  "s_cbranch_scc1 1b\n\t" \
  "2:\n\t" \
  "s_sub_i32 s79, s79, 33\n\t" \
  "s_cmp_gt_i32 s79, 56\n\t" \
  "s_cbranch_scc1 2b\n\t" \
  "buffer_load_dwordx4 v[32:35], v100, %[rh], s40 offen sc1\n\t" \
  "s_nop 12\n\t" \
  "buffer_load_dwordx4 v[36:39], v100, %[rh], s76 offen sc1\n\t" \
  "s_nop 12\n\t" \
  "buffer_load_dwordx4 v[40:43], v100, %[rh], s77 offen sc1\n\t" \
  "s_nop 12\n\t" \
  "buffer_load_dwordx4 v[44:47], v100, %[rh], s78 offen sc1\n\t" \
  "s_memtime vcc\n\t" \
  "s_memrealtime s[82:83]\n\t" \
  "s_nop 4\n\t" \
  "s_memrealtime s[84:85]\n\t" \
  "s_nop 4\n\t" \
  "s_memrealtime s[94:95]\n\t" \
  "s_nop 4\n\t" \
  "s_memrealtime s[86:87]\n\t" \
  "s_nop 4\n\t" \
  "s_memrealtime s[88:89]\n\t" \
  "s_nop 4\n\t" \
  "s_memrealtime s[90:91]\n\t" \
  "s_mov_b32 s92, 0\n\t" \
  "s_waitcnt lgkmcnt(0)\n\t" \
  "s_cmp_eq_u32 s82, s84\n\t" \
  "s_addc_u32 s92, s92, 0\n\t" \
  "s_cmp_eq_u32 s82, s94\n\t" \
  "s_addc_u32 s92, s92, 0\n\t" \
  "s_cmp_eq_u32 s82, s86\n\t" \
  "s_addc_u32 s92, s92, 0\n\t" \
  "s_cmp_eq_u32 s82, s88\n\t" \
  "s_addc_u32 s92, s92, 0\n\t" \
  "s_cmp_eq_u32 s82, s90\n\t" \
  "s_addc_u32 s92, s92, 0\n\t" \
  "s_mul_i32 s92, s92, 24\n\t" \
  "s_add_i32 s92, s92, vcc_lo\n\t" \
  "s_lshl4_add_u32 s93, s82, 64\n\t" \
  "3:\n\t" \
  "s_sub_i32 s84, s92, s43\n\t" \
  "s_lshl_b32 s85, s93, 8\n\t" \
  "s_or_b32 s85, s85, s44\n\t" \
  "v_mov_b32 v118, s85\n\t" \
  "v_mov_b64 v[68:69], 0\n\t" \
  "v_mov_b64 v[70:71], 0\n\t" \
  "v_mov_b64 v[72:73], 0\n\t" \
  "v_mov_b64 v[74:75], 0\n\t" \
  "v_mov_b64 v[76:77], 0\n\t" \
  "v_mov_b64 v[78:79], 0\n\t" \
  "v_mov_b64 v[80:81], 0\n\t" \
  "v_mov_b64 v[82:83], 0\n\t" \
  "s_waitcnt lgkmcnt(0)\n\t" \
  "s_waitcnt vmcnt(3)\n\t" \
  "v_smfmac_f32_16x16x64_bf16 v[68:71], v[32:35], v[152:159], v104\n\t" \
  "ds_write_b128 v101, v[32:35] offset:12288\n\t" \
  "v_smfmac_f32_16x16x64_bf16 v[72:75], v[32:35], v[160:167], v104\n\t" \
  "v_smfmac_f32_16x16x64_bf16 v[76:79], v[32:35], v[48:55], v104\n\t" \
  "v_smfmac_f32_16x16x64_bf16 v[80:83], v[32:35], v[56:63], v104\n\t" \
  "s_waitcnt vmcnt(2)\n\t" \
  "v_smfmac_f32_16x16x64_bf16 v[68:71], v[36:39], v[168:175], v104\n\t" \
  "ds_write_b128 v101, v[36:39] offset:13312\n\t" \
  "v_smfmac_f32_16x16x64_bf16 v[72:75], v[36:39], v[176:183], v104\n\t" \
  "v_smfmac_f32_16x16x64_bf16 v[76:79], v[36:39], v[184:191], v104\n\t" \
  "v_cmp_u_f32 vcc, v68, v69\n\t" \
  "v_smfmac_f32_16x16x64_bf16 v[80:83], v[36:39], v[192:199], v104\n\t" \
  "v_cmp_u_f32 s[82:83], v70, v71\n\t" \
  "s_waitcnt vmcnt(1)\n\t" \
  "v_smfmac_f32_16x16x64_bf16 v[68:71], v[40:43], v[16:23], v104\n\t" \
  "ds_write_b128 v101, v[40:43] offset:14336\n\t" \
  "v_smfmac_f32_16x16x64_bf16 v[72:75], v[40:43], v[24:31], v104\n\t" \
  "s_or_b64 vcc, vcc, s[82:83]\n\t" \
  "s_or_b64 vcc, vcc, s[80:81]\n\t" \
  "s_waitcnt lgkmcnt(1)\n\t" \
  "v_smfmac_f32_16x16x64_bf16 v[76:79], v[40:43], v[200:207], v104\n\t" \
  "s_cbranch_scc1 8f\n\t" \
  "5:\n\t" \
  "s_barrier\n\t" \
  "4:\n\t" \
  "s_memtime s[56:57]\n\t" \
  "ds_read_b128 v[48:51], v101 offset:0\n\t" \
  "v_smfmac_f32_16x16x64_bf16 v[80:83], v[40:43], v[208:215], v104\n\t" \
  "ds_read_b128 v[52:55], v101 offset:1024\n\t" \
  "ds_read_b128 v[56:59], v101 offset:4096\n\t" \
  "s_waitcnt vmcnt(0)\n\t" \
  "v_smfmac_f32_16x16x64_bf16 v[68:71], v[44:47], v[216:223], v104\n\t" \
  "v_smfmac_f32_16x16x64_bf16 v[72:75], v[44:47], v[224:231], v104\n\t" \
  "ds_write_b128 v101, v[44:47] offset:15360\n\t" \
  "v_smfmac_f32_16x16x64_bf16 v[76:79], v[44:47], v[232:239], v104\n\t" \
  "v_cmp_u_f32 vcc, v68, v69\n\t" \
  "v_smfmac_f32_16x16x64_bf16 v[80:83], v[44:47], v[240:247], v104\n\t" \
  "v_cmp_u_f32 s[82:83], v70, v71\n\t" \
  "s_waitcnt lgkmcnt(3)\n\t" \
  "v_smfmac_f32_16x16x64_bf16 v[68:71], v[48:51], a[0:7], v104\n\t" \
  "ds_read_b128 v[60:63], v101 offset:5120\n\t" \
  "ds_read_b128 v[64:67], v101 offset:8192\n\t" \
  "v_smfmac_f32_16x16x64_bf16 v[72:75], v[48:51], a[8:15], v104\n\t" \
  "s_or_b64 vcc, vcc, s[82:83]\n\t" \
  "s_waitcnt lgkmcnt(2)\n\t" \
  "v_smfmac_f32_16x16x64_bf16 v[68:71], v[52:55], a[16:23], v104\n\t" \
  "s_cbranch_scc1 9f\n\t" \
  "6:\n\t" \
  "s_barrier\n\t" \
  "v_smfmac_f32_16x16x64_bf16 v[72:75], v[52:55], a[24:31], v104\n\t" \
  "s_waitcnt lgkmcnt(0)\n\t" \
  "ds_read_b128 v[48:51], v101 offset:9216\n\t" \
  "v_smfmac_f32_16x16x64_bf16 v[68:71], v[56:59], a[96:103], v104\n\t" \
  "ds_read_b128 v[52:55], v101 offset:2048\n\t" \
  "v_smfmac_f32_16x16x64_bf16 v[72:75], v[56:59], a[104:111], v104\n\t" \
  "s_sub_i32 s79, s56, s84\n\t" \
  "s_mul_hi_u32 s79, s79, s46\n\t" \
  "v_smfmac_f32_16x16x64_bf16 v[68:71], v[60:63], a[112:119], v104\n\t" \
  "ds_read_b128 v[56:59], v101 offset:3072\n\t" \
  "v_smfmac_f32_16x16x64_bf16 v[72:75], v[60:63], a[120:127], v104\n\t" \
  "v_lshl_add_u32 v116, s79, 9, v118\n\t" \
  "buffer_store_dword v116, v112, %[rh], s45 offen sc1\n\t" \
  "v_smfmac_f32_16x16x64_bf16 v[68:71], v[64:67], a[192:199], v104\n\t" \
  "ds_read_b128 v[60:63], v101 offset:6144\n\t" \
  "v_smfmac_f32_16x16x64_bf16 v[72:75], v[64:67], a[200:207], v104\n\t" \
  "ds_read_b128 v[16:19], v103 offset:16384\n\t" \
  "s_waitcnt lgkmcnt(4)\n\t" \
  "v_smfmac_f32_16x16x64_bf16 v[68:71], v[48:51], a[208:215], v104\n\t" \
  "ds_read_b128 v[64:67], v101 offset:7168\n\t" \
  "v_smfmac_f32_16x16x64_bf16 v[72:75], v[48:51], a[216:223], v104\n\t" \
  "ds_read_b128 v[20:23], v103 offset:17408\n\t" \
  "s_waitcnt lgkmcnt(5)\n\t" \
  "v_smfmac_f32_16x16x64_bf16 v[68:71], v[52:55], v[84:91], v104\n\t" \
  "ds_read_b128 v[48:51], v101 offset:11264\n\t" \
  "v_smfmac_f32_16x16x64_bf16 v[72:75], v[52:55], v[92:99], v104\n\t" \
  "ds_read_b128 v[24:27], v103 offset:18432\n\t" \
  "s_waitcnt lgkmcnt(6)\n\t" \
  "v_smfmac_f32_16x16x64_bf16 v[68:71], v[56:59], a[64:71], v104\n\t" \
  "ds_read_b128 v[52:55], v101 offset:10240\n\t" \
  "v_smfmac_f32_16x16x64_bf16 v[72:75], v[56:59], a[72:79], v104\n\t" \
  "ds_read_b128 v[28:31], v103 offset:19456\n\t" \
  "s_waitcnt lgkmcnt(7)\n\t" \
  "v_smfmac_f32_16x16x64_bf16 v[68:71], v[60:63], v[0:7], v104\n\t" \
  "v_smfmac_f32_16x16x64_bf16 v[72:75], v[60:63], v[8:15], v104\n\t" \
  "buffer_load_dword v117, v107, %[ru], 0 offen sc1\n\t" \
  "s_waitcnt lgkmcnt(5)\n\t" \
  "v_smfmac_f32_16x16x64_bf16 v[68:71], v[64:67], a[160:167], v104\n\t" \
  "ds_read_b128 v[0:3], v102 offset:0\n\t" \
  "v_smfmac_f32_16x16x64_bf16 v[72:75], v[64:67], a[168:175], v104\n\t" \
  "ds_read_b128 v[4:7], v102 offset:1024\n\t" \
  "s_waitcnt lgkmcnt(5)\n\t" \
  "v_smfmac_f32_16x16x64_bf16 v[68:71], v[48:51], v[120:127], v104\n\t" \
  "ds_read_b128 v[8:11], v102 offset:2048\n\t" \
  "s_waitcnt lgkmcnt(4)\n\t" \
  "v_smfmac_f32_16x16x64_bf16 v[68:71], v[52:55], v[16:23], v104\n\t" \
  "ds_read_b128 v[12:15], v102 offset:3072\n\t" \
  "v_smfmac_f32_16x16x64_bf16 v[72:75], v[48:51], v[128:135], v104\n\t" \
  "s_waitcnt lgkmcnt(4)\n\t" \
  "v_smfmac_f32_16x16x64_bf16 v[72:75], v[52:55], v[24:31], v104\n\t" \
  "v_add_f32 v96, v68, v70\n\t" \
  "v_add_f32 v97, v69, v71\n\t" \
  "v_smfmac_f32_16x16x64_bf16 v[76:79], v[56:59], a[80:87], v104\n\t" \
  "v_cvt_pk_bf16_f32 v96, v96, v97\n\t" \
  "ds_read_b128 v[24:27], v102 offset:34816\n\t" \
  "v_smfmac_f32_16x16x64_bf16 v[80:83], v[56:59], a[88:95], v104\n\t" \
  "v_add_f32 v97, v72, v74\n\t" \
  "v_add_f32 v98, v73, v75\n\t" \
  "v_cvt_pk_bf16_f32 v97, v97, v98\n\t" \
  "ds_write2st64_b32 v105, v96, v97 offset1:1\n\t" \
  "v_smfmac_f32_16x16x64_bf16 v[76:79], v[60:63], a[144:151], v104\n\t" \
  "v_smfmac_f32_16x16x64_bf16 v[80:83], v[60:63], a[152:159], v104\n\t" \
  "v_smfmac_f32_16x16x64_bf16 v[76:79], v[64:67], a[176:183], v104\n\t" \
  "v_smfmac_f32_16x16x64_bf16 v[80:83], v[64:67], a[184:191], v104\n\t" \
  "s_waitcnt lgkmcnt(0)\n\t" \
  "s_barrier\n\t" \
  "ds_read_b128 v[56:59], v101 offset:0\n\t" \
  "v_smfmac_f32_16x16x64_bf16 v[76:79], v[48:51], v[136:143], v104\n\t" \
  "v_smfmac_f32_16x16x64_bf16 v[80:83], v[48:51], v[144:151], v104\n\t" \
  "ds_read_b128 v[60:63], v101 offset:9216\n\t" \
  "buffer_load_dwordx4 v[84:87], v100, %[ru], s47 offen offset:0 sc1\n\t" \
  "v_smfmac_f32_16x16x64_bf16 v[76:79], v[52:55], a[240:247], v104\n\t" \
  "ds_read_b128 v[16:19], v102 offset:32768\n\t" \
  "v_smfmac_f32_16x16x64_bf16 v[80:83], v[52:55], a[248:255], v104\n\t" \
  "ds_read_b128 v[64:67], v101 offset:2048\n\t" \
  "buffer_load_dwordx4 v[88:91], v100, %[ru], s47 offen offset:1024 sc1\n\t" \
  "s_waitcnt lgkmcnt(3)\n\t" \
  "v_smfmac_f32_16x16x64_bf16 v[76:79], v[56:59], v[0:7], v104\n\t" \
  "ds_read_b128 v[20:23], v102 offset:33792\n\t" \
  "v_smfmac_f32_16x16x64_bf16 v[80:83], v[56:59], v[8:15], v104\n\t" \
  "ds_read_b128 v[48:51], v101 offset:4096\n\t" \
  "buffer_load_dwordx4 v[92:95], v100, %[ru], s47 offen offset:2048 sc1\n\t" \
  "s_waitcnt lgkmcnt(4)\n\t" \
  "v_smfmac_f32_16x16x64_bf16 v[76:79], v[60:63], a[224:231], v104\n\t" \
  "ds_read_b128 v[28:31], v102 offset:35840\n\t" \
  "v_smfmac_f32_16x16x64_bf16 v[80:83], v[60:63], a[232:239], v104\n\t" \
  "ds_read_b128 v[52:55], v101 offset:1024\n\t" \
  "buffer_load_dwordx4 v[96:99], v100, %[ru], s47 offen offset:3072 sc1\n\t" \
  "s_waitcnt lgkmcnt(4)\n\t" \
  "v_smfmac_f32_16x16x64_bf16 v[76:79], v[64:67], a[48:55], v104\n\t" \
  "ds_read_b128 v[0:3], v103 offset:0\n\t" \
  "v_smfmac_f32_16x16x64_bf16 v[80:83], v[64:67], a[56:63], v104\n\t" \
  "ds_read_b128 v[56:59], v101 offset:5120\n\t" \
  "ds_read_b128 v[4:7], v103 offset:1024\n\t" \
  "buffer_load_dwordx4 v[32:35], v100, %[ru], s97 offen offset:0 sc1\n\t" \
  "s_waitcnt lgkmcnt(5)\n\t" \
  "v_smfmac_f32_16x16x64_bf16 v[76:79], v[48:51], v[16:23], v104\n\t" \
  "ds_read_b128 v[60:63], v101 offset:8192\n\t" \
  "s_waitcnt lgkmcnt(5)\n\t" \
  "v_smfmac_f32_16x16x64_bf16 v[80:83], v[48:51], v[24:31], v104\n\t" \
  "ds_read_b128 v[8:11], v103 offset:2048\n\t" \
  "buffer_load_dwordx4 v[36:39], v100, %[ru], s97 offen offset:1024 sc1\n\t" \
  "s_waitcnt lgkmcnt(5)\n\t" \
  "v_smfmac_f32_16x16x64_bf16 v[76:79], v[52:55], a[32:39], v104\n\t" \
  "ds_read_b128 v[12:15], v103 offset:3072\n\t" \
  "v_smfmac_f32_16x16x64_bf16 v[80:83], v[52:55], a[40:47], v104\n\t" \
  "buffer_load_dwordx4 v[40:43], v100, %[ru], s97 offen offset:2048 sc1\n\t" \
  "s_waitcnt lgkmcnt(4)\n\t" \
  "v_smfmac_f32_16x16x64_bf16 v[76:79], v[56:59], a[128:135], v104\n\t" \
  "s_waitcnt lgkmcnt(2)\n\t" \
  "v_smfmac_f32_16x16x64_bf16 v[76:79], v[60:63], v[0:7], v104\n\t" \
  "buffer_load_dwordx4 v[44:47], v100, %[ru], s97 offen offset:3072 sc1\n\t" \
  "v_smfmac_f32_16x16x64_bf16 v[80:83], v[56:59], a[136:143], v104\n\t" \
  "s_waitcnt lgkmcnt(0)\n\t" \
  "v_smfmac_f32_16x16x64_bf16 v[80:83], v[60:63], v[8:15], v104\n\t" \
  "v_add_f32 v68, v76, v78\n\t" \
  "v_add_f32 v69, v77, v79\n\t" \
  "v_cvt_pk_bf16_f32 v68, v68, v69\n\t" \
  "ds_write_b32 v106, v68\n\t" \
  "s_nop 3\n\t" \
  "v_add_f32 v70, v80, v82\n\t" \
  "v_add_f32 v71, v81, v83\n\t" \
  "v_cvt_pk_bf16_f32 v70, v70, v71\n\t" \
  "ds_write_b32 v106, v70 offset:256\n\t" \
  "s_waitcnt lgkmcnt(0)\n\t" \
  "s_barrier\n\t" \
  "s_memtime s[76:77]\n\t" \
  "s_waitcnt lgkmcnt(0)\n\t" \
  "s_add_i32 s57, s76, 240\n\t" \
  "s_sleep 5\n\t" \
  "s_waitcnt vmcnt(8)\n\t" \
  "v_readfirstlane_b32 s76, v117\n\t" \
  "s_add_i32 s77, s44, 2\n\t" \
  "s_min_i32 s77, s77, 63\n\t" \
  "s_cmp_lt_i32 s76, s77\n\t" \
  "s_cselect_b32 s42, 32, 0\n\t" \
  "s_add_i32 s41, s56, 2050\n\t" \
  "s_sub_i32 s76, s57, s56\n\t" \
  "s_add_i32 s77, s43, 8\n\t" \
  "s_min_u32 s76, s76, s77\n\t" \
  "s_sub_i32 s43, s43, 4\n\t" \
  "s_max_u32 s43, s43, s76\n\t" \
  "s_add_i32 s76, s45, 0x4000\n\t" \
  "s_and_b32 s76, s76, 0xc000\n\t" \
  "v_mov_b32 v116, -1\n\t" \
  "buffer_store_dword v116, v112, %[rh], s76 offen sc1\n\t" \
  "s_sub_i32 s76, s92, s51\n\t" \
  "s_sub_i32 s77, s93, s53\n\t" \
  "s_cmp_eq_u32 s76, 0\n\t" \
  "s_cbranch_scc1 21f\n\t" \
  "v_cvt_f32_u32 v117, s76\n\t" \
  "v_cvt_f32_u32 v116, s77\n\t" \
  "v_rcp_f32 v117, v117\n\t" \
  "s_mov_b32 s51, s92\n\t" \
  "v_mul_f32 v117, v117, v116\n\t" \
  "v_max_f32 v117, 0x3f028f5c, v117\n\t" \
  "v_min_f32 v117, 0x3ffeb852, v117\n\t" \
  "v_rcp_f32 v116, v117\n\t" \
  "v_mul_f32 v117, 0x4f000000, v117\n\t" \
  "v_mul_f32 v116, 0x47800000, v116\n\t" \
  "v_cvt_u32_f32 v117, v117\n\t" \
  "v_cvt_u32_f32 v116, v116\n\t" \
  "s_mov_b32 s53, s93\n\t" \
  "v_readfirstlane_b32 s46, v117\n\t" \
  "v_readfirstlane_b32 s52, v116\n\t" \
  "21:\n\t" \
  "s_waitcnt vmcnt(0)\n\t" \
  "ds_write_b128 v101, v[84:87] offset:20480\n\t" \
  "ds_write_b128 v101, v[88:91] offset:21504\n\t" \
  "ds_write_b128 v101, v[92:95] offset:22528\n\t" \
  "ds_write_b128 v101, v[96:99] offset:23552\n\t" \
  "ds_write_b128 v101, v[32:35] offset:24576\n\t" \
  "ds_write_b128 v101, v[36:39] offset:25600\n\t" \
  "ds_write_b128 v101, v[40:43] offset:26624\n\t" \
  "ds_write_b128 v101, v[44:47] offset:27648\n\t" \
  "v_mov_b32 v0, -1\n\t" \
  "v_mov_b32 v1, -1\n\t" \
  "v_mov_b32 v2, -1\n\t" \
  "v_mov_b32 v3, -1\n\t" \
  "s_and_b32 s76, s49, 3\n\t" \
  "s_mul_i32 s76, s76, 0xc000\n\t" \
  "s_add_i32 s76, s76, 0x40000\n\t" \
  "buffer_store_dwordx4 v[0:3], v109, %[rh], s76 offen sc1\n\t" \
  "s_add_i32 s44, s44, 1\n\t" \
  "s_and_b32 s58, s58, 4\n\t" \
  "s_and_b32 s76, s44, 64\n\t" \
  "s_or_b32 s42, s42, s76\n\t" \
  "s_cbranch_scc0 20b\n\t" \
  "s_branch 99f\n\t" \
  "14:\n\t" \
  "s_add_u32 s59, s59, 0x10000\n\t" \
  "s_cmp_lt_u32 s59, 0x100000\n\t" \
  "s_cbranch_scc1 13b\n\t" \
  "s_mov_b64 exec, -1\n\t" \
  "s_mov_b32 s42, 16\n\t" \
  "s_branch 99f\n\t" \
  "8:\n\t" \
  "s_bitcmp1_b32 s58, 1\n\t" \
  "s_cbranch_scc1 4b\n\t" \
  "s_bitcmp1_b32 s58, 2\n\t" \
  "s_cbranch_scc1 5b\n\t" \
  "v_max3_u32 v116, v32, v33, v34\n\t" \
  "v_max3_u32 v117, v35, v36, v37\n\t" \
  "v_max3_u32 v116, v116, v38, v39\n\t" \
  "v_max_u32 v116, v116, v117\n\t" \
  "v_cmp_eq_u32 vcc, -1, v116\n\t" \
  "s_cbranch_vccz 5b\n\t" \
  "s_branch 0f\n\t" \
  "9:\n\t" \
  "s_bitcmp1_b32 s58, 2\n\t" \
  "s_cbranch_scc1 6b\n\t" \
  "v_max3_u32 v116, v40, v41, v42\n\t" \
  "v_max3_u32 v117, v43, v44, v45\n\t" \
  "v_max3_u32 v116, v116, v46, v47\n\t" \
  "v_max_u32 v116, v116, v117\n\t" \
  "v_cmp_eq_u32 vcc, -1, v116\n\t" \
  "s_cbranch_vccz 6b\n\t" \
  "s_or_b32 s58, s58, 2\n\t" \
  "0:\n\t" \
  "s_or_b32 s58, s58, 1\n\t" \
  "11:\n\t" \
  "buffer_load_dwordx4 v[32:35], v100, %[rh], s40 offen sc1\n\t" \
  "buffer_load_dwordx4 v[36:39], v100, %[rh], s76 offen sc1\n\t" \
  "buffer_load_dwordx4 v[40:43], v100, %[rh], s77 offen sc1\n\t" \
  "buffer_load_dwordx4 v[44:47], v100, %[rh], s78 offen sc1\n\t" \
  "s_waitcnt lgkmcnt(0)\n\t" \
  "s_branch 10b\n\t" \
  "17:\n\t" \
  "s_add_u32 s59, s59, 1\n\t" \
  "s_bitcmp1_b32 s58, 2\n\t" \
  "s_cbranch_scc1 3b\n\t" \
  "s_waitcnt vmcnt(0)\n\t" \
  "v_max_u32 v116, v32, v33\n\t" \
  "v_max3_u32 v116, v116, v34, v35\n\t" \
  "v_max3_u32 v116, v116, v36, v37\n\t" \
  "v_max3_u32 v116, v116, v38, v39\n\t" \
  "v_max3_u32 v116, v116, v40, v41\n\t" \
  "v_max3_u32 v116, v116, v42, v43\n\t" \
  "v_max3_u32 v116, v116, v44, v45\n\t" \
  "v_max3_u32 v116, v116, v46, v47\n\t" \
  "v_cmp_eq_u32 vcc, -1, v116\n\t" \
  "s_cbranch_vccz 3b\n\t" \
  "s_bitcmp1_b32 s59, 6\n\t" \
  "s_cbranch_scc1 12f\n\t" \
  "buffer_load_dwordx4 v[32:35], v100, %[rh], s40 offen sc1\n\t" \
  "buffer_load_dwordx4 v[36:39], v100, %[rh], s76 offen sc1\n\t" \
  "buffer_load_dwordx4 v[40:43], v100, %[rh], s77 offen sc1\n\t" \
  "buffer_load_dwordx4 v[44:47], v100, %[rh], s78 offen sc1\n\t" \
  "s_branch 17b\n\t" \
  "12:\n\t" \
  "s_mov_b32 s42, s58\n\t" \
  "s_branch 99f\n\t" \
  "99:\n\t" \
  "s_waitcnt vmcnt(0) lgkmcnt(0)\n\t" \
  ""
#define VO_BODY \
  "s_cmp_eq_u32 %[wv], 1\n\t" \
  "s_cbranch_scc1 91f\n\t" \
  "s_cmp_eq_u32 %[wv], 2\n\t" \
  "s_cbranch_scc1 92f\n\t" \
  "s_cmp_eq_u32 %[wv], 3\n\t" \
  "s_cbranch_scc1 93f\n\t" \
  "90:\n\t" \
  "s_mov_b32 s58, s42\n\t" \
  "20:\n\t" \
  "s_and_b32 s76, s44, 7\n\t" \
  "s_lshl_b32 s76, s76, 15\n\t" \
  "s_add_i32 s40, s54, s76\n\t" \
  "s_and_b32 s76, s44, 3\n\t" \
  "s_lshl_b32 s45, s76, 14\n\t" \
  "s_and_b32 s47, s44, 3\n\t" \
  "s_lshr_b32 s76, s47, 1\n\t" \
  "s_lshl_b32 s47, s47, 20\n\t" \
  "s_add_i32 s47, s47, s55\n\t" \
  "s_bitcmp1_b32 %[sel], s76\n\t" \
  "s_cselect_b32 s76, 0x2000, 0\n\t" \
  "s_add_i32 s47, s47, s76\n\t" \
  "s_add_i32 s49, s44, -1\n\t" \
  "s_and_b32 s48, s49, 3\n\t" \
  "s_lshl_b32 s48, s48, 14\n\t" \
  "s_lshl_b32 s50, s53, 8\n\t" \
  "s_mov_b32 s59, 0\n\t" \
  "s_mov_b32 s92, s51\n\t" \
  "s_mov_b32 s93, s53\n\t" \
  "s_xor_b32 s76, s40, 0x400\n\t" \
  "s_xor_b32 s77, s40, 0x800\n\t" \
  "s_xor_b32 s78, s40, 0xc00\n\t" \
  "s_cmp_lg_u32 s58, 0\n\t" \
  "s_cbranch_scc1 11f\n\t" \
  "10:\n\t" \
  "ds_read_b128 v[48:51], v102 offset:0\n\t" \
  "ds_read_b128 v[52:55], v102 offset:1024\n\t" \
  "ds_read_b128 v[56:59], v102 offset:2048\n\t" \
  "ds_read_b128 v[60:63], v102 offset:3072\n\t" \
  "ds_read_b128 v[0:3], v102 offset:16384\n\t" \
  "ds_read_b128 v[4:7], v102 offset:17408\n\t" \
  "ds_read_b128 v[8:11], v102 offset:18432\n\t" \
  "ds_read_b128 v[12:15], v102 offset:19456\n\t" \
  "ds_read_b128 v[16:19], v102 offset:32768\n\t" \
  "ds_read_b128 v[20:23], v102 offset:33792\n\t" \
  "ds_read_b128 v[24:27], v102 offset:34816\n\t" \
  "ds_read_b128 v[28:31], v102 offset:35840\n\t" \
  "s_waitcnt lgkmcnt(4)\n\t" \
  "ds_read_b128 v[80:83], v102 offset:49152\n\t" \
  "ds_read_b128 v[84:87], v102 offset:50176\n\t" \
  "ds_read_b128 v[88:91], v102 offset:51200\n\t" \
  "ds_read_b128 v[92:95], v102 offset:52224\n\t" \
  "s_bitcmp1_b32 s58, 1\n\t" \
  "s_cselect_b64 s[80:81], -1, 0\n\t" \
  "s_cmp_lg_u32 s58, 0\n\t" \
  "s_cbranch_scc1 17f\n\t" \
  "s_mov_b32 exec_hi, 0\n\t" \
  "16:\n\t" \
  "s_memtime vcc\n\t" \
  "s_waitcnt lgkmcnt(0)\n\t" \
  "s_sub_i32 s79, s41, vcc_lo\n\t" \
  "s_cmp_gt_i32 s79, 64\n\t" \
  "s_cbranch_scc1 16b\n\t" \
  "13:\n\t" \
  "buffer_load_dword v116, v110, %[rh], s48 offen sc1\n\t" \
  "s_waitcnt vmcnt(0)\n\t" \
  "15:\n\t" \
  "v_mov_b32 v117, 20000\n\t" \
  "v_cmp_ne_u32_sdwa vcc, v116, s49 src0_sel:BYTE_0 src1_sel:DWORD\n\t" \
  "v_subrev_u32 v116, s50, v116\n\t" \
  "v_ashrrev_i32 v116, 8, v116\n\t" \
  "s_nop 1\n\t" \
  "v_max_i32_dpp v116, v116, v116 quad_perm:[1,0,3,2] row_mask:0xf bank_mask:0xf\n\t" \
  "s_cbranch_vccnz 14f\n\t" \
  "s_nop 0\n\t" \
  "v_max_i32_dpp v116, v116, v116 quad_perm:[2,3,0,1] row_mask:0xf bank_mask:0xf\n\t" \
  "s_nop 1\n\t" \
  "v_max_i32_dpp v116, v116, v116 row_half_mirror row_mask:0xf bank_mask:0xf\n\t" \
  "s_nop 1\n\t" \
  "v_max_i32_dpp v116, v116, v116 row_mirror row_mask:0xf bank_mask:0xf\n\t" \
  "v_med3_i32 v116, v116, 0, v117\n\t" \
  "v_mul_u32_u24 v116, s52, v116\n\t" \
  "v_lshrrev_b32 v116, 16, v116\n\t" \
  "s_mov_b64 exec, -1\n\t" \
  "v_readlane_b32 s84, v116, 0\n\t" \
  "v_readlane_b32 s85, v116, 16\n\t" \
  "s_max_u32 s84, s84, s85\n\t" \
  "s_add_i32 s84, s84, s51\n\t" \
  "1:\n\t" \
  "s_memtime vcc\n\t" \
  "s_waitcnt lgkmcnt(0)\n\t" \
  "s_sub_i32 s79, s84, vcc_lo\n\t" \
  "s_cmp_gt_i32 s79, 128\n\t" \
  "s_cbranch_scc1 1b\n\t" \
  "2:\n\t" \
  "s_sub_i32 s79, s79, 33\n\t" \
  "s_cmp_gt_i32 s79, 56\n\t" \
  "s_cbranch_scc1 2b\n\t" \
  "buffer_load_dwordx4 v[32:35], v100, %[rh], s40 offen sc1\n\t" \
  "s_nop 12\n\t" \
  "buffer_load_dwordx4 v[36:39], v100, %[rh], s76 offen sc1\n\t" \
  "s_nop 12\n\t" \
  "buffer_load_dwordx4 v[40:43], v100, %[rh], s77 offen sc1\n\t" \
  "s_nop 12\n\t" \
  "buffer_load_dwordx4 v[44:47], v100, %[rh], s78 offen sc1\n\t" \
  "s_memtime vcc\n\t" \
  "s_memrealtime s[82:83]\n\t" \
  "s_nop 4\n\t" \
  "s_memrealtime s[84:85]\n\t" \
  "s_nop 4\n\t" \
  "s_memrealtime s[94:95]\n\t" \
  "s_nop 4\n\t" \
  "s_memrealtime s[86:87]\n\t" \
  "s_nop 4\n\t" \
  "s_memrealtime s[88:89]\n\t" \
  "s_nop 4\n\t" \
  "s_memrealtime s[90:91]\n\t" \
  "s_mov_b32 s92, 0\n\t" \
  "s_waitcnt lgkmcnt(0)\n\t" \
  "s_cmp_eq_u32 s82, s84\n\t" \
  "s_addc_u32 s92, s92, 0\n\t" \
  "s_cmp_eq_u32 s82, s94\n\t" \
  "s_addc_u32 s92, s92, 0\n\t" \
  "s_cmp_eq_u32 s82, s86\n\t" \
  "s_addc_u32 s92, s92, 0\n\t" \
  "s_cmp_eq_u32 s82, s88\n\t" \
  "s_addc_u32 s92, s92, 0\n\t" \
  "s_cmp_eq_u32 s82, s90\n\t" \
  "s_addc_u32 s92, s92, 0\n\t" \
  "s_mul_i32 s92, s92, 24\n\t" \
  "s_add_i32 s92, s92, vcc_lo\n\t" \
  "s_lshl4_add_u32 s93, s82, 64\n\t" \
  "3:\n\t" \
  "v_mov_b64 v[72:73], 0\n\t" \
  "v_mov_b64 v[74:75], 0\n\t" \
  "v_mov_b64 v[76:77], 0\n\t" \
  "v_mov_b64 v[78:79], 0\n\t" \
  "s_waitcnt lgkmcnt(0)\n\t" \
  "s_waitcnt vmcnt(3)\n\t" \
  "v_smfmac_f32_16x16x64_bf16 v[72:75], v[32:35], v[48:55], v104\n\t" \
  "ds_write_b128 v101, v[32:35] offset:0\n\t" \
  "v_smfmac_f32_16x16x64_bf16 v[76:79], v[32:35], v[56:63], v104\n\t" \
  "s_waitcnt vmcnt(2)\n\t" \
  "ds_write_b128 v101, v[36:39] offset:1024\n\t" \
  "v_smfmac_f32_16x16x64_bf16 v[72:75], v[36:39], a[0:7], v104\n\t" \
  "v_smfmac_f32_16x16x64_bf16 v[76:79], v[36:39], a[8:15], v104\n\t" \
  "s_nop 3\n\t" \
  "v_cmp_u_f32 vcc, v72, v73\n\t" \
  "v_cmp_u_f32 s[82:83], v74, v75\n\t" \
  "s_waitcnt vmcnt(1)\n\t" \
  "v_smfmac_f32_16x16x64_bf16 v[72:75], v[40:43], a[16:23], v104\n\t" \
  "s_or_b64 vcc, vcc, s[82:83]\n\t" \
  "s_or_b64 vcc, vcc, s[80:81]\n\t" \
  "v_smfmac_f32_16x16x64_bf16 v[76:79], v[40:43], a[24:31], v104\n\t" \
  "s_waitcnt lgkmcnt(0)\n\t" \
  "s_cbranch_scc1 8f\n\t" \
  "5:\n\t" \
  "s_barrier\n\t" \
  "4:\n\t" \
  "s_memtime s[56:57]\n\t" \
  "ds_read_b128 v[48:51], v101 offset:4096\n\t" \
  "ds_read_b128 v[52:55], v101 offset:5120\n\t" \
  "ds_read_b128 v[56:59], v101 offset:9216\n\t" \
  "s_waitcnt vmcnt(0)\n\t" \
  "v_smfmac_f32_16x16x64_bf16 v[72:75], v[44:47], a[32:39], v104\n\t" \
  "ds_read_b128 v[60:63], v101 offset:8192\n\t" \
  "ds_read_b128 v[64:67], v101 offset:13312\n\t" \
  "v_smfmac_f32_16x16x64_bf16 v[76:79], v[44:47], a[40:47], v104\n\t" \
  "ds_read_b128 v[68:71], v101 offset:12288\n\t" \
  "ds_write_b128 v101, v[40:43] offset:2048\n\t" \
  "s_nop 1\n\t" \
  "v_cmp_u_f32 vcc, v72, v73\n\t" \
  "v_cmp_u_f32 s[82:83], v74, v75\n\t" \
  "s_waitcnt lgkmcnt(6)\n\t" \
  "v_smfmac_f32_16x16x64_bf16 v[72:75], v[48:51], v[0:7], v104\n\t" \
  "s_or_b64 vcc, vcc, s[82:83]\n\t" \
  "v_smfmac_f32_16x16x64_bf16 v[76:79], v[48:51], v[8:15], v104\n\t" \
  "s_cbranch_scc1 9f\n\t" \
  "6:\n\t" \
  "s_waitcnt lgkmcnt(5)\n\t" \
  "v_smfmac_f32_16x16x64_bf16 v[72:75], v[52:55], a[48:55], v104\n\t" \
  "ds_write_b128 v101, v[44:47] offset:3072\n\t" \
  "v_smfmac_f32_16x16x64_bf16 v[76:79], v[52:55], a[56:63], v104\n\t" \
  "v_mov_b64 v[40:41], 0\n\t" \
  "s_waitcnt lgkmcnt(5)\n\t" \
  "v_smfmac_f32_16x16x64_bf16 v[72:75], v[56:59], a[96:103], v104\n\t" \
  "v_mov_b64 v[42:43], 0\n\t" \
  "v_smfmac_f32_16x16x64_bf16 v[76:79], v[56:59], a[104:111], v104\n\t" \
  "s_waitcnt lgkmcnt(4)\n\t" \
  "v_smfmac_f32_16x16x64_bf16 v[72:75], v[60:63], v[16:23], v104\n\t" \
  "s_waitcnt lgkmcnt(0)\n\t" \
  "s_barrier\n\t" \
  "v_smfmac_f32_16x16x64_bf16 v[76:79], v[60:63], v[24:31], v104\n\t" \
  "ds_read_b128 v[48:51], v101 offset:6144\n\t" \
  "ds_read_b128 v[52:55], v101 offset:10240\n\t" \
  "v_smfmac_f32_16x16x64_bf16 v[72:75], v[64:67], a[144:151], v104\n\t" \
  "ds_read_b128 v[56:59], v101 offset:14336\n\t" \
  "ds_read_b128 v[60:63], v101 offset:7168\n\t" \
  "v_smfmac_f32_16x16x64_bf16 v[76:79], v[64:67], a[152:159], v104\n\t" \
  "ds_read_b128 v[32:35], v101 offset:11264\n\t" \
  "ds_read_b128 v[36:39], v101 offset:15360\n\t" \
  "v_smfmac_f32_16x16x64_bf16 v[72:75], v[68:71], v[80:87], v104\n\t" \
  "ds_read_b128 v[0:3], v103 offset:0\n\t" \
  "v_mov_b64 v[44:45], 0\n\t" \
  "v_smfmac_f32_16x16x64_bf16 v[76:79], v[68:71], v[88:95], v104\n\t" \
  "v_mov_b64 v[46:47], 0\n\t" \
  "s_waitcnt lgkmcnt(5)\n\t" \
  "v_smfmac_f32_16x16x64_bf16 v[72:75], v[48:51], a[64:71], v104\n\t" \
  "ds_read_b128 v[4:7], v103 offset:1024\n\t" \
  "v_mov_b64 v[80:81], 0\n\t" \
  "v_smfmac_f32_16x16x64_bf16 v[72:75], v[52:55], a[112:119], v104\n\t" \
  "v_mov_b64 v[82:83], 0\n\t" \
  "s_waitcnt lgkmcnt(2)\n\t" \
  "v_smfmac_f32_16x16x64_bf16 v[72:75], v[56:59], a[160:167], v104\n\t" \
  "ds_read_b128 v[8:11], v103 offset:2048\n\t" \
  "v_mov_b64 v[84:85], 0\n\t" \
  "v_smfmac_f32_16x16x64_bf16 v[72:75], v[60:63], a[80:87], v104\n\t" \
  "ds_read_b128 v[12:15], v103 offset:3072\n\t" \
  "v_mov_b64 v[86:87], 0\n\t" \
  "v_smfmac_f32_16x16x64_bf16 v[72:75], v[32:35], a[128:135], v104\n\t" \
  "ds_read_b128 v[16:19], v103 offset:16384\n\t" \
  "v_mov_b64 v[88:89], 0\n\t" \
  "v_smfmac_f32_16x16x64_bf16 v[72:75], v[36:39], a[176:183], v104\n\t" \
  "ds_read_b128 v[20:23], v103 offset:17408\n\t" \
  "v_mov_b64 v[90:91], 0\n\t" \
  "v_smfmac_f32_16x16x64_bf16 v[76:79], v[48:51], a[72:79], v104\n\t" \
  "ds_read_b128 v[24:27], v103 offset:18432\n\t" \
  "v_mov_b64 v[92:93], 0\n\t" \
  "v_smfmac_f32_16x16x64_bf16 v[76:79], v[52:55], a[120:127], v104\n\t" \
  "v_add_f32 v96, v72, v74\n\t" \
  "v_add_f32 v97, v73, v75\n\t" \
  "v_cvt_pk_bf16_f32 v96, v96, v97\n\t" \
  "ds_write_b32 v105, v96\n\t" \
  "v_smfmac_f32_16x16x64_bf16 v[76:79], v[56:59], a[168:175], v104\n\t" \
  "ds_read_b128 v[28:31], v103 offset:19456\n\t" \
  "v_mov_b64 v[94:95], 0\n\t" \
  "v_smfmac_f32_16x16x64_bf16 v[76:79], v[60:63], a[88:95], v104\n\t" \
  "v_smfmac_f32_16x16x64_bf16 v[76:79], v[32:35], a[136:143], v104\n\t" \
  "v_smfmac_f32_16x16x64_bf16 v[76:79], v[36:39], a[184:191], v104\n\t" \
  "s_waitcnt lgkmcnt(1)\n\t" \
  "s_barrier\n\t" \
  "ds_read_b64_tr_b16 v[96:97], v107 offset:0\n\t" \
  "ds_read_b64_tr_b16 v[98:99], v107 offset:64\n\t" \
  "ds_read_b64_tr_b16 v[56:57], v107 offset:1024\n\t" \
  "ds_read_b64_tr_b16 v[58:59], v107 offset:1088\n\t" \
  "s_nop 1\n\t" \
  "v_add_f32 v116, v76, v78\n\t" \
  "v_add_f32 v117, v77, v79\n\t" \
  "v_cvt_pk_bf16_f32 v116, v116, v117\n\t" \
  "ds_write_b32 v105, v116 offset:256\n\t" \
  "s_waitcnt lgkmcnt(3)\n\t" \
  "v_smfmac_f32_16x16x64_bf16 v[40:43], v[96:99], a[192:199], v106\n\t" \
  "v_smfmac_f32_16x16x64_bf16 v[44:47], v[96:99], a[200:207], v106\n\t" \
  "s_waitcnt lgkmcnt(0)\n\t" \
  "s_barrier\n\t" \
  "ds_read_b64_tr_b16 v[52:53], v107 offset:256\n\t" \
  "ds_read_b64_tr_b16 v[54:55], v107 offset:320\n\t" \
  "v_smfmac_f32_16x16x64_bf16 v[40:43], v[56:59], a[224:231], v106\n\t" \
  "ds_read_b64_tr_b16 v[60:61], v107 offset:1280\n\t" \
  "ds_read_b64_tr_b16 v[62:63], v107 offset:1344\n\t" \
  "v_smfmac_f32_16x16x64_bf16 v[44:47], v[56:59], a[232:239], v106\n\t" \
  "v_smfmac_f32_16x16x64_bf16 v[80:83], v[96:99], a[240:247], v106\n\t" \
  "v_mov_b64 v[32:33], 0\n\t" \
  "v_smfmac_f32_16x16x64_bf16 v[84:87], v[96:99], a[248:255], v106\n\t" \
  "v_mov_b64 v[34:35], 0\n\t" \
  "v_smfmac_f32_16x16x64_bf16 v[80:83], v[56:59], v[136:143], v106\n\t" \
  "v_mov_b64 v[36:37], 0\n\t" \
  "v_smfmac_f32_16x16x64_bf16 v[84:87], v[56:59], v[144:151], v106\n\t" \
  "s_waitcnt lgkmcnt(2)\n\t" \
  "v_smfmac_f32_16x16x64_bf16 v[40:43], v[52:55], a[208:215], v106\n\t" \
  "v_smfmac_f32_16x16x64_bf16 v[44:47], v[52:55], a[216:223], v106\n\t" \
  "s_xor_b32 s76, s47, 0x40000\n\t" \
  "s_waitcnt lgkmcnt(0)\n\t" \
  "v_smfmac_f32_16x16x64_bf16 v[40:43], v[60:63], v[0:7], v106\n\t" \
  "v_mov_b64 v[38:39], 0\n\t" \
  "s_xor_b32 s77, s47, 0x80000\n\t" \
  "v_smfmac_f32_16x16x64_bf16 v[44:47], v[60:63], v[8:15], v106\n\t" \
  "s_xor_b32 s78, s47, 0xc0000\n\t" \
  "v_smfmac_f32_16x16x64_bf16 v[80:83], v[52:55], v[120:127], v106\n\t" \
  "ds_read_b128 v[0:3], v103 offset:32768\n\t" \
  "v_cvt_pk_bf16_f32 v64, v40, v41\n\t" \
  "v_smfmac_f32_16x16x64_bf16 v[84:87], v[52:55], v[128:135], v106\n\t" \
  "ds_read_b128 v[4:7], v103 offset:33792\n\t" \
  "v_cvt_pk_bf16_f32 v66, v42, v43\n\t" \
  "v_smfmac_f32_16x16x64_bf16 v[80:83], v[60:63], v[16:23], v106\n\t" \
  "ds_read_b128 v[8:11], v103 offset:34816\n\t" \
  "v_cvt_pk_bf16_f32 v65, v44, v45\n\t" \
  "v_smfmac_f32_16x16x64_bf16 v[84:87], v[60:63], v[24:31], v106\n\t" \
  "ds_read_b128 v[12:15], v103 offset:35840\n\t" \
  "v_cvt_pk_bf16_f32 v67, v46, v47\n\t" \
  "v_smfmac_f32_16x16x64_bf16 v[88:91], v[96:99], v[152:159], v106\n\t" \
  "buffer_store_dwordx4 v[64:67], v108, %[ru], s47 offen sc1\n\t" \
  "v_cvt_pk_bf16_f32 v68, v80, v81\n\t" \
  "v_smfmac_f32_16x16x64_bf16 v[92:95], v[96:99], v[160:167], v106\n\t" \
  "v_cvt_pk_bf16_f32 v70, v82, v83\n\t" \
  "v_smfmac_f32_16x16x64_bf16 v[88:91], v[52:55], v[168:175], v106\n\t" \
  "v_cvt_pk_bf16_f32 v69, v84, v85\n\t" \
  "v_smfmac_f32_16x16x64_bf16 v[92:95], v[52:55], v[176:183], v106\n\t" \
  "v_cvt_pk_bf16_f32 v71, v86, v87\n\t" \
  "v_smfmac_f32_16x16x64_bf16 v[88:91], v[56:59], v[184:191], v106\n\t" \
  "ds_read_b128 v[16:19], v103 offset:49152\n\t" \
  "buffer_store_dwordx4 v[68:71], v108, %[ru], s76 offen sc1\n\t" \
  "v_smfmac_f32_16x16x64_bf16 v[92:95], v[56:59], v[192:199], v106\n\t" \
  "ds_read_b128 v[20:23], v103 offset:50176\n\t" \
  "s_waitcnt lgkmcnt(4)\n\t" \
  "v_smfmac_f32_16x16x64_bf16 v[88:91], v[60:63], v[0:7], v106\n\t" \
  "ds_read_b128 v[24:27], v103 offset:51200\n\t" \
  "s_waitcnt lgkmcnt(3)\n\t" \
  "v_smfmac_f32_16x16x64_bf16 v[92:95], v[60:63], v[8:15], v106\n\t" \
  "ds_read_b128 v[28:31], v103 offset:52224\n\t" \
  "v_smfmac_f32_16x16x64_bf16 v[32:35], v[96:99], v[200:207], v106\n\t" \
  "v_cvt_pk_bf16_f32 v72, v88, v89\n\t" \
  "v_smfmac_f32_16x16x64_bf16 v[36:39], v[96:99], v[208:215], v106\n\t" \
  "v_cvt_pk_bf16_f32 v74, v90, v91\n\t" \
  "v_smfmac_f32_16x16x64_bf16 v[32:35], v[52:55], v[216:223], v106\n\t" \
  "v_cvt_pk_bf16_f32 v73, v92, v93\n\t" \
  "v_smfmac_f32_16x16x64_bf16 v[36:39], v[52:55], v[224:231], v106\n\t" \
  "v_cvt_pk_bf16_f32 v75, v94, v95\n\t" \
  "v_smfmac_f32_16x16x64_bf16 v[32:35], v[56:59], v[232:239], v106\n\t" \
  "buffer_store_dwordx4 v[72:75], v108, %[ru], s77 offen sc1\n\t" \
  "s_waitcnt lgkmcnt(2)\n\t" \
  "v_smfmac_f32_16x16x64_bf16 v[32:35], v[60:63], v[16:23], v106\n\t" \
  "v_smfmac_f32_16x16x64_bf16 v[36:39], v[56:59], v[240:247], v106\n\t" \
  "s_waitcnt lgkmcnt(0)\n\t" \
  "v_smfmac_f32_16x16x64_bf16 v[36:39], v[60:63], v[24:31], v106\n\t" \
  "v_cvt_pk_bf16_f32 v76, v32, v33\n\t" \
  "v_cvt_pk_bf16_f32 v78, v34, v35\n\t" \
  "s_nop 5\n\t" \
  "v_cvt_pk_bf16_f32 v77, v36, v37\n\t" \
  "v_cvt_pk_bf16_f32 v79, v38, v39\n\t" \
  "buffer_store_dwordx4 v[76:79], v108, %[ru], s78 offen sc1\n\t" \
  "s_memtime s[76:77]\n\t" \
  "s_mov_b32 s42, 0\n\t" \
  "s_waitcnt lgkmcnt(0)\n\t" \
  "s_mov_b32 s57, s76\n\t" \
  "s_add_i32 s41, s56, 2050\n\t" \
  "s_sub_i32 s76, s92, s51\n\t" \
  "s_sub_i32 s77, s93, s53\n\t" \
  "s_cmp_eq_u32 s76, 0\n\t" \
  "s_cbranch_scc1 21f\n\t" \
  "v_cvt_f32_u32 v117, s76\n\t" \
  "v_cvt_f32_u32 v116, s77\n\t" \
  "v_rcp_f32 v117, v117\n\t" \
  "s_mov_b32 s51, s92\n\t" \
  "v_mul_f32 v117, v117, v116\n\t" \
  "v_max_f32 v117, 0x3f028f5c, v117\n\t" \
  "v_min_f32 v117, 0x3ffeb852, v117\n\t" \
  "v_rcp_f32 v116, v117\n\t" \
  "v_mul_f32 v117, 0x4f000000, v117\n\t" \
  "v_mul_f32 v116, 0x47800000, v116\n\t" \
  "v_cvt_u32_f32 v117, v117\n\t" \
  "v_cvt_u32_f32 v116, v116\n\t" \
  "s_mov_b32 s53, s93\n\t" \
  "v_readfirstlane_b32 s46, v117\n\t" \
  "v_readfirstlane_b32 s52, v116\n\t" \
  "21:\n\t" \
  "s_add_i32 s44, s44, 1\n\t" \
  "s_and_b32 s58, s58, 4\n\t" \
  "s_and_b32 s76, s44, 64\n\t" \
  "s_or_b32 s42, s42, s76\n\t" \
  "s_cbranch_scc0 20b\n\t" \
  "s_branch 99f\n\t" \
  "14:\n\t" \
  "s_add_u32 s59, s59, 0x10000\n\t" \
  "s_cmp_lt_u32 s59, 0x100000\n\t" \
  "s_cbranch_scc1 13b\n\t" \
  "s_mov_b64 exec, -1\n\t" \
  "s_mov_b32 s42, 16\n\t" \
  "s_branch 99f\n\t" \
  "8:\n\t" \
  "s_bitcmp1_b32 s58, 1\n\t" \
  "s_cbranch_scc1 4b\n\t" \
  "s_bitcmp1_b32 s58, 2\n\t" \
  "s_cbranch_scc1 5b\n\t" \
  "v_max3_u32 v116, v32, v33, v34\n\t" \
  "v_max3_u32 v117, v35, v36, v37\n\t" \
  "v_max3_u32 v116, v116, v38, v39\n\t" \
  "v_max_u32 v116, v116, v117\n\t" \
  "v_cmp_eq_u32 vcc, -1, v116\n\t" \
  "s_cbranch_vccz 5b\n\t" \
  "s_branch 0f\n\t" \
  "9:\n\t" \
  "s_bitcmp1_b32 s58, 2\n\t" \
  "s_cbranch_scc1 6b\n\t" \
  "v_max3_u32 v116, v40, v41, v42\n\t" \
  "v_max3_u32 v117, v43, v44, v45\n\t" \
  "v_max3_u32 v116, v116, v46, v47\n\t" \
  "v_max_u32 v116, v116, v117\n\t" \
  "v_cmp_eq_u32 vcc, -1, v116\n\t" \
  "s_cbranch_vccz 6b\n\t" \
  "s_or_b32 s58, s58, 2\n\t" \
  "0:\n\t" \
  "s_or_b32 s58, s58, 1\n\t" \
  "11:\n\t" \
  "buffer_load_dwordx4 v[32:35], v100, %[rh], s40 offen sc1\n\t" \
  "buffer_load_dwordx4 v[36:39], v100, %[rh], s76 offen sc1\n\t" \
  "buffer_load_dwordx4 v[40:43], v100, %[rh], s77 offen sc1\n\t" \
  "buffer_load_dwordx4 v[44:47], v100, %[rh], s78 offen sc1\n\t" \
  "s_waitcnt lgkmcnt(0)\n\t" \
  "s_branch 10b\n\t" \
  "17:\n\t" \
  "s_add_u32 s59, s59, 1\n\t" \
  "s_bitcmp1_b32 s58, 2\n\t" \
  "s_cbranch_scc1 3b\n\t" \
  "s_waitcnt vmcnt(0)\n\t" \
  "v_max_u32 v116, v32, v33\n\t" \
  "v_max3_u32 v116, v116, v34, v35\n\t" \
  "v_max3_u32 v116, v116, v36, v37\n\t" \
  "v_max3_u32 v116, v116, v38, v39\n\t" \
  "v_max3_u32 v116, v116, v40, v41\n\t" \
  "v_max3_u32 v116, v116, v42, v43\n\t" \
  "v_max3_u32 v116, v116, v44, v45\n\t" \
  "v_max3_u32 v116, v116, v46, v47\n\t" \
  "v_cmp_eq_u32 vcc, -1, v116\n\t" \
  "s_cbranch_vccz 3b\n\t" \
  "s_bitcmp1_b32 s59, 6\n\t" \
  "s_cbranch_scc1 12f\n\t" \
  "buffer_load_dwordx4 v[32:35], v100, %[rh], s40 offen sc1\n\t" \
  "buffer_load_dwordx4 v[36:39], v100, %[rh], s76 offen sc1\n\t" \
  "buffer_load_dwordx4 v[40:43], v100, %[rh], s77 offen sc1\n\t" \
  "buffer_load_dwordx4 v[44:47], v100, %[rh], s78 offen sc1\n\t" \
  "s_branch 17b\n\t" \
  "12:\n\t" \
  "s_mov_b32 s42, s58\n\t" \
  "s_branch 99f\n\t" \
  "91:\n\t" \
  "s_mov_b32 s58, s42\n\t" \
  "20:\n\t" \
  "s_and_b32 s76, s44, 7\n\t" \
  "s_lshl_b32 s76, s76, 15\n\t" \
  "s_add_i32 s40, s54, s76\n\t" \
  "s_and_b32 s76, s44, 3\n\t" \
  "s_lshl_b32 s45, s76, 14\n\t" \
  "s_and_b32 s47, s44, 3\n\t" \
  "s_lshr_b32 s76, s47, 1\n\t" \
  "s_lshl_b32 s47, s47, 20\n\t" \
  "s_add_i32 s47, s47, s55\n\t" \
  "s_bitcmp1_b32 %[sel], s76\n\t" \
  "s_cselect_b32 s76, 0x2000, 0\n\t" \
  "s_add_i32 s47, s47, s76\n\t" \
  "s_add_i32 s49, s44, -1\n\t" \
  "s_and_b32 s48, s49, 3\n\t" \
  "s_lshl_b32 s48, s48, 14\n\t" \
  "s_lshl_b32 s50, s53, 8\n\t" \
  "s_mov_b32 s59, 0\n\t" \
  "s_mov_b32 s92, s51\n\t" \
  "s_mov_b32 s93, s53\n\t" \
  "s_xor_b32 s76, s40, 0x400\n\t" \
  "s_xor_b32 s77, s40, 0x800\n\t" \
  "s_xor_b32 s78, s40, 0xc00\n\t" \
  "s_cmp_lg_u32 s58, 0\n\t" \
  "s_cbranch_scc1 11f\n\t" \
  "10:\n\t" \
  "ds_read_b128 v[48:51], v102 offset:16384\n\t" \
  "ds_read_b128 v[52:55], v102 offset:17408\n\t" \
  "ds_read_b128 v[56:59], v102 offset:18432\n\t" \
  "ds_read_b128 v[60:63], v102 offset:19456\n\t" \
  "ds_read_b128 v[0:3], v102 offset:32768\n\t" \
  "ds_read_b128 v[4:7], v102 offset:33792\n\t" \
  "ds_read_b128 v[8:11], v102 offset:34816\n\t" \
  "ds_read_b128 v[12:15], v102 offset:35840\n\t" \
  "ds_read_b128 v[16:19], v102 offset:49152\n\t" \
  "ds_read_b128 v[20:23], v102 offset:50176\n\t" \
  "ds_read_b128 v[24:27], v102 offset:51200\n\t" \
  "ds_read_b128 v[28:31], v102 offset:52224\n\t" \
  "s_waitcnt lgkmcnt(4)\n\t" \
  "ds_read_b128 v[80:83], v102 offset:0\n\t" \
  "ds_read_b128 v[84:87], v102 offset:1024\n\t" \
  "ds_read_b128 v[88:91], v102 offset:2048\n\t" \
  "ds_read_b128 v[92:95], v102 offset:3072\n\t" \
  "s_bitcmp1_b32 s58, 1\n\t" \
  "s_cselect_b64 s[80:81], -1, 0\n\t" \
  "s_cmp_lg_u32 s58, 0\n\t" \
  "s_cbranch_scc1 17f\n\t" \
  "s_mov_b32 exec_hi, 0\n\t" \
  "16:\n\t" \
  "s_memtime vcc\n\t" \
  "s_waitcnt lgkmcnt(0)\n\t" \
  "s_sub_i32 s79, s41, vcc_lo\n\t" \
  "s_cmp_gt_i32 s79, 64\n\t" \
  "s_cbranch_scc1 16b\n\t" \
  "13:\n\t" \
  "buffer_load_dword v116, v110, %[rh], s48 offen sc1\n\t" \
  "s_waitcnt vmcnt(0)\n\t" \
  "15:\n\t" \
  "v_mov_b32 v117, 20000\n\t" \
  "v_cmp_ne_u32_sdwa vcc, v116, s49 src0_sel:BYTE_0 src1_sel:DWORD\n\t" \
  "v_subrev_u32 v116, s50, v116\n\t" \
  "v_ashrrev_i32 v116, 8, v116\n\t" \
  "s_nop 1\n\t" \
  "v_max_i32_dpp v116, v116, v116 quad_perm:[1,0,3,2] row_mask:0xf bank_mask:0xf\n\t" \
  "s_cbranch_vccnz 14f\n\t" \
  "s_nop 0\n\t" \
  "v_max_i32_dpp v116, v116, v116 quad_perm:[2,3,0,1] row_mask:0xf bank_mask:0xf\n\t" \
  "s_nop 1\n\t" \
  "v_max_i32_dpp v116, v116, v116 row_half_mirror row_mask:0xf bank_mask:0xf\n\t" \
  "s_nop 1\n\t" \
  "v_max_i32_dpp v116, v116, v116 row_mirror row_mask:0xf bank_mask:0xf\n\t" \
  "v_med3_i32 v116, v116, 0, v117\n\t" \
  "v_mul_u32_u24 v116, s52, v116\n\t" \
  "v_lshrrev_b32 v116, 16, v116\n\t" \
  "s_mov_b64 exec, -1\n\t" \
  "v_readlane_b32 s84, v116, 0\n\t" \
  "v_readlane_b32 s85, v116, 16\n\t" \
  "s_max_u32 s84, s84, s85\n\t" \
  "s_add_i32 s84, s84, s51\n\t" \
  "1:\n\t" \
  "s_memtime vcc\n\t" \
  "s_waitcnt lgkmcnt(0)\n\t" \
  "s_sub_i32 s79, s84, vcc_lo\n\t" \
  "s_cmp_gt_i32 s79, 128\n\t" \
  "s_cbranch_scc1 1b\n\t" \
  "2:\n\t" \
  "s_sub_i32 s79, s79, 33\n\t" \
  "s_cmp_gt_i32 s79, 56\n\t" \
  "s_cbranch_scc1 2b\n\t" \
  "buffer_load_dwordx4 v[32:35], v100, %[rh], s40 offen sc1\n\t" \
  "s_nop 12\n\t" \
  "buffer_load_dwordx4 v[36:39], v100, %[rh], s76 offen sc1\n\t" \
  "s_nop 12\n\t" \
  "buffer_load_dwordx4 v[40:43], v100, %[rh], s77 offen sc1\n\t" \
  "s_nop 12\n\t" \
  "buffer_load_dwordx4 v[44:47], v100, %[rh], s78 offen sc1\n\t" \
  "s_memtime vcc\n\t" \
  "s_memrealtime s[82:83]\n\t" \
  "s_nop 4\n\t" \
  "s_memrealtime s[84:85]\n\t" \
  "s_nop 4\n\t" \
  "s_memrealtime s[94:95]\n\t" \
  "s_nop 4\n\t" \
  "s_memrealtime s[86:87]\n\t" \
  "s_nop 4\n\t" \
  "s_memrealtime s[88:89]\n\t" \
  "s_nop 4\n\t" \
  "s_memrealtime s[90:91]\n\t" \
  "s_mov_b32 s92, 0\n\t" \
  "s_waitcnt lgkmcnt(0)\n\t" \
  "s_cmp_eq_u32 s82, s84\n\t" \
  "s_addc_u32 s92, s92, 0\n\t" \
  "s_cmp_eq_u32 s82, s94\n\t" \
  "s_addc_u32 s92, s92, 0\n\t" \
  "s_cmp_eq_u32 s82, s86\n\t" \
  "s_addc_u32 s92, s92, 0\n\t" \
  "s_cmp_eq_u32 s82, s88\n\t" \
  "s_addc_u32 s92, s92, 0\n\t" \
  "s_cmp_eq_u32 s82, s90\n\t" \
  "s_addc_u32 s92, s92, 0\n\t" \
  "s_mul_i32 s92, s92, 24\n\t" \
  "s_add_i32 s92, s92, vcc_lo\n\t" \
  "s_lshl4_add_u32 s93, s82, 64\n\t" \
  "3:\n\t" \
  "v_mov_b64 v[72:73], 0\n\t" \
  "v_mov_b64 v[74:75], 0\n\t" \
  "v_mov_b64 v[76:77], 0\n\t" \
  "v_mov_b64 v[78:79], 0\n\t" \
  "s_waitcnt lgkmcnt(0)\n\t" \
  "s_waitcnt vmcnt(3)\n\t" \
  "v_smfmac_f32_16x16x64_bf16 v[72:75], v[32:35], v[48:55], v104\n\t" \
  "ds_write_b128 v101, v[32:35] offset:4096\n\t" \
  "v_smfmac_f32_16x16x64_bf16 v[76:79], v[32:35], v[56:63], v104\n\t" \
  "s_waitcnt vmcnt(2)\n\t" \
  "ds_write_b128 v101, v[36:39] offset:5120\n\t" \
  "v_smfmac_f32_16x16x64_bf16 v[72:75], v[36:39], a[48:55], v104\n\t" \
  "v_smfmac_f32_16x16x64_bf16 v[76:79], v[36:39], a[56:63], v104\n\t" \
  "s_nop 3\n\t" \
  "v_cmp_u_f32 vcc, v72, v73\n\t" \
  "v_cmp_u_f32 s[82:83], v74, v75\n\t" \
  "s_waitcnt vmcnt(1)\n\t" \
  "v_smfmac_f32_16x16x64_bf16 v[72:75], v[40:43], a[64:71], v104\n\t" \
  "s_or_b64 vcc, vcc, s[82:83]\n\t" \
  "s_or_b64 vcc, vcc, s[80:81]\n\t" \
  "v_smfmac_f32_16x16x64_bf16 v[76:79], v[40:43], a[72:79], v104\n\t" \
  "s_waitcnt lgkmcnt(0)\n\t" \
  "s_cbranch_scc1 8f\n\t" \
  "5:\n\t" \
  "s_barrier\n\t" \
  "4:\n\t" \
  "s_memtime s[56:57]\n\t" \
  "ds_read_b128 v[48:51], v101 offset:8192\n\t" \
  "ds_read_b128 v[52:55], v101 offset:9216\n\t" \
  "ds_read_b128 v[56:59], v101 offset:13312\n\t" \
  "s_waitcnt vmcnt(0)\n\t" \
  "v_smfmac_f32_16x16x64_bf16 v[72:75], v[44:47], a[80:87], v104\n\t" \
  "ds_read_b128 v[60:63], v101 offset:12288\n\t" \
  "ds_read_b128 v[64:67], v101 offset:1024\n\t" \
  "v_smfmac_f32_16x16x64_bf16 v[76:79], v[44:47], a[88:95], v104\n\t" \
  "ds_read_b128 v[68:71], v101 offset:0\n\t" \
  "ds_write_b128 v101, v[40:43] offset:6144\n\t" \
  "s_nop 1\n\t" \
  "v_cmp_u_f32 vcc, v72, v73\n\t" \
  "v_cmp_u_f32 s[82:83], v74, v75\n\t" \
  "s_waitcnt lgkmcnt(6)\n\t" \
  "v_smfmac_f32_16x16x64_bf16 v[72:75], v[48:51], v[0:7], v104\n\t" \
  "s_or_b64 vcc, vcc, s[82:83]\n\t" \
  "v_smfmac_f32_16x16x64_bf16 v[76:79], v[48:51], v[8:15], v104\n\t" \
  "s_cbranch_scc1 9f\n\t" \
  "6:\n\t" \
  "s_waitcnt lgkmcnt(5)\n\t" \
  "v_smfmac_f32_16x16x64_bf16 v[72:75], v[52:55], a[96:103], v104\n\t" \
  "ds_write_b128 v101, v[44:47] offset:7168\n\t" \
  "v_smfmac_f32_16x16x64_bf16 v[76:79], v[52:55], a[104:111], v104\n\t" \
  "v_mov_b64 v[40:41], 0\n\t" \
  "s_waitcnt lgkmcnt(5)\n\t" \
  "v_smfmac_f32_16x16x64_bf16 v[72:75], v[56:59], a[144:151], v104\n\t" \
  "v_mov_b64 v[42:43], 0\n\t" \
  "v_smfmac_f32_16x16x64_bf16 v[76:79], v[56:59], a[152:159], v104\n\t" \
  "s_waitcnt lgkmcnt(4)\n\t" \
  "v_smfmac_f32_16x16x64_bf16 v[72:75], v[60:63], v[16:23], v104\n\t" \
  "s_waitcnt lgkmcnt(0)\n\t" \
  "s_barrier\n\t" \
  "v_smfmac_f32_16x16x64_bf16 v[76:79], v[60:63], v[24:31], v104\n\t" \
  "ds_read_b128 v[48:51], v101 offset:10240\n\t" \
  "ds_read_b128 v[52:55], v101 offset:14336\n\t" \
  "v_smfmac_f32_16x16x64_bf16 v[72:75], v[64:67], a[0:7], v104\n\t" \
  "ds_read_b128 v[56:59], v101 offset:2048\n\t" \
  "ds_read_b128 v[60:63], v101 offset:11264\n\t" \
  "v_smfmac_f32_16x16x64_bf16 v[76:79], v[64:67], a[8:15], v104\n\t" \
  "ds_read_b128 v[32:35], v101 offset:15360\n\t" \
  "ds_read_b128 v[36:39], v101 offset:3072\n\t" \
  "v_smfmac_f32_16x16x64_bf16 v[72:75], v[68:71], v[80:87], v104\n\t" \
  "ds_read_b128 v[0:3], v103 offset:0\n\t" \
  "v_mov_b64 v[44:45], 0\n\t" \
  "v_smfmac_f32_16x16x64_bf16 v[76:79], v[68:71], v[88:95], v104\n\t" \
  "v_mov_b64 v[46:47], 0\n\t" \
  "s_waitcnt lgkmcnt(5)\n\t" \
  "v_smfmac_f32_16x16x64_bf16 v[72:75], v[48:51], a[112:119], v104\n\t" \
  "ds_read_b128 v[4:7], v103 offset:1024\n\t" \
  "v_mov_b64 v[80:81], 0\n\t" \
  "v_smfmac_f32_16x16x64_bf16 v[72:75], v[52:55], a[160:167], v104\n\t" \
  "v_mov_b64 v[82:83], 0\n\t" \
  "s_waitcnt lgkmcnt(2)\n\t" \
  "v_smfmac_f32_16x16x64_bf16 v[72:75], v[56:59], a[16:23], v104\n\t" \
  "ds_read_b128 v[8:11], v103 offset:2048\n\t" \
  "v_mov_b64 v[84:85], 0\n\t" \
  "v_smfmac_f32_16x16x64_bf16 v[72:75], v[60:63], a[128:135], v104\n\t" \
  "ds_read_b128 v[12:15], v103 offset:3072\n\t" \
  "v_mov_b64 v[86:87], 0\n\t" \
  "v_smfmac_f32_16x16x64_bf16 v[72:75], v[32:35], a[176:183], v104\n\t" \
  "ds_read_b128 v[16:19], v103 offset:16384\n\t" \
  "v_mov_b64 v[88:89], 0\n\t" \
  "v_smfmac_f32_16x16x64_bf16 v[72:75], v[36:39], a[32:39], v104\n\t" \
  "ds_read_b128 v[20:23], v103 offset:17408\n\t" \
  "v_mov_b64 v[90:91], 0\n\t" \
  "v_smfmac_f32_16x16x64_bf16 v[76:79], v[48:51], a[120:127], v104\n\t" \
  "ds_read_b128 v[24:27], v103 offset:18432\n\t" \
  "v_mov_b64 v[92:93], 0\n\t" \
  "v_smfmac_f32_16x16x64_bf16 v[76:79], v[52:55], a[168:175], v104\n\t" \
  "v_add_f32 v96, v72, v74\n\t" \
  "v_add_f32 v97, v73, v75\n\t" \
  "v_cvt_pk_bf16_f32 v96, v96, v97\n\t" \
  "ds_write_b32 v105, v96\n\t" \
  "v_smfmac_f32_16x16x64_bf16 v[76:79], v[56:59], a[24:31], v104\n\t" \
  "ds_read_b128 v[28:31], v103 offset:19456\n\t" \
  "v_mov_b64 v[94:95], 0\n\t" \
  "v_smfmac_f32_16x16x64_bf16 v[76:79], v[60:63], a[136:143], v104\n\t" \
  "v_smfmac_f32_16x16x64_bf16 v[76:79], v[32:35], a[184:191], v104\n\t" \
  "v_smfmac_f32_16x16x64_bf16 v[76:79], v[36:39], a[40:47], v104\n\t" \
  "s_waitcnt lgkmcnt(1)\n\t" \
  "s_barrier\n\t" \
  "ds_read_b64_tr_b16 v[96:97], v107 offset:0\n\t" \
  "ds_read_b64_tr_b16 v[98:99], v107 offset:64\n\t" \
  "ds_read_b64_tr_b16 v[56:57], v107 offset:1024\n\t" \
  "ds_read_b64_tr_b16 v[58:59], v107 offset:1088\n\t" \
  "s_nop 1\n\t" \
  "v_add_f32 v116, v76, v78\n\t" \
  "v_add_f32 v117, v77, v79\n\t" \
  "v_cvt_pk_bf16_f32 v116, v116, v117\n\t" \
  "ds_write_b32 v105, v116 offset:256\n\t" \
  "s_waitcnt lgkmcnt(3)\n\t" \
  "v_smfmac_f32_16x16x64_bf16 v[40:43], v[96:99], a[192:199], v106\n\t" \
  "v_smfmac_f32_16x16x64_bf16 v[44:47], v[96:99], a[200:207], v106\n\t" \
  "s_waitcnt lgkmcnt(0)\n\t" \
  "s_barrier\n\t" \
  "ds_read_b64_tr_b16 v[52:53], v107 offset:256\n\t" \
  "ds_read_b64_tr_b16 v[54:55], v107 offset:320\n\t" \
  "v_smfmac_f32_16x16x64_bf16 v[40:43], v[56:59], a[224:231], v106\n\t" \
  "ds_read_b64_tr_b16 v[60:61], v107 offset:1280\n\t" \
  "ds_read_b64_tr_b16 v[62:63], v107 offset:1344\n\t" \
  "v_smfmac_f32_16x16x64_bf16 v[44:47], v[56:59], a[232:239], v106\n\t" \
  "v_smfmac_f32_16x16x64_bf16 v[80:83], v[96:99], a[240:247], v106\n\t" \
  "v_mov_b64 v[32:33], 0\n\t" \
  "v_smfmac_f32_16x16x64_bf16 v[84:87], v[96:99], a[248:255], v106\n\t" \
  "v_mov_b64 v[34:35], 0\n\t" \
  "v_smfmac_f32_16x16x64_bf16 v[80:83], v[56:59], v[136:143], v106\n\t" \
  "v_mov_b64 v[36:37], 0\n\t" \
  "v_smfmac_f32_16x16x64_bf16 v[84:87], v[56:59], v[144:151], v106\n\t" \
  "s_waitcnt lgkmcnt(2)\n\t" \
  "v_smfmac_f32_16x16x64_bf16 v[40:43], v[52:55], a[208:215], v106\n\t" \
  "v_smfmac_f32_16x16x64_bf16 v[44:47], v[52:55], a[216:223], v106\n\t" \
  "s_xor_b32 s76, s47, 0x40000\n\t" \
  "s_waitcnt lgkmcnt(0)\n\t" \
  "v_smfmac_f32_16x16x64_bf16 v[40:43], v[60:63], v[0:7], v106\n\t" \
  "v_mov_b64 v[38:39], 0\n\t" \
  "s_xor_b32 s77, s47, 0x80000\n\t" \
  "v_smfmac_f32_16x16x64_bf16 v[44:47], v[60:63], v[8:15], v106\n\t" \
  "s_xor_b32 s78, s47, 0xc0000\n\t" \
  "v_smfmac_f32_16x16x64_bf16 v[80:83], v[52:55], v[120:127], v106\n\t" \
  "ds_read_b128 v[0:3], v103 offset:32768\n\t" \
  "v_cvt_pk_bf16_f32 v64, v40, v41\n\t" \
  "v_smfmac_f32_16x16x64_bf16 v[84:87], v[52:55], v[128:135], v106\n\t" \
  "ds_read_b128 v[4:7], v103 offset:33792\n\t" \
  "v_cvt_pk_bf16_f32 v66, v42, v43\n\t" \
  "v_smfmac_f32_16x16x64_bf16 v[80:83], v[60:63], v[16:23], v106\n\t" \
  "ds_read_b128 v[8:11], v103 offset:34816\n\t" \
  "v_cvt_pk_bf16_f32 v65, v44, v45\n\t" \
  "v_smfmac_f32_16x16x64_bf16 v[84:87], v[60:63], v[24:31], v106\n\t" \
  "ds_read_b128 v[12:15], v103 offset:35840\n\t" \
  "v_cvt_pk_bf16_f32 v67, v46, v47\n\t" \
  "v_smfmac_f32_16x16x64_bf16 v[88:91], v[96:99], v[152:159], v106\n\t" \
  "v_cvt_pk_bf16_f32 v68, v80, v81\n\t" \
  "v_smfmac_f32_16x16x64_bf16 v[92:95], v[96:99], v[160:167], v106\n\t" \
  "buffer_store_dwordx4 v[64:67], v108, %[ru], s47 offen sc1\n\t" \
  "v_cvt_pk_bf16_f32 v70, v82, v83\n\t" \
  "v_smfmac_f32_16x16x64_bf16 v[88:91], v[52:55], v[168:175], v106\n\t" \
  "v_cvt_pk_bf16_f32 v69, v84, v85\n\t" \
  "v_smfmac_f32_16x16x64_bf16 v[92:95], v[52:55], v[176:183], v106\n\t" \
  "v_cvt_pk_bf16_f32 v71, v86, v87\n\t" \
  "v_smfmac_f32_16x16x64_bf16 v[88:91], v[56:59], v[184:191], v106\n\t" \
  "ds_read_b128 v[16:19], v103 offset:49152\n\t" \
  "v_smfmac_f32_16x16x64_bf16 v[92:95], v[56:59], v[192:199], v106\n\t" \
  "ds_read_b128 v[20:23], v103 offset:50176\n\t" \
  "buffer_store_dwordx4 v[68:71], v108, %[ru], s76 offen sc1\n\t" \
  "s_waitcnt lgkmcnt(4)\n\t" \
  "v_smfmac_f32_16x16x64_bf16 v[88:91], v[60:63], v[0:7], v106\n\t" \
  "ds_read_b128 v[24:27], v103 offset:51200\n\t" \
  "s_waitcnt lgkmcnt(3)\n\t" \
  "v_smfmac_f32_16x16x64_bf16 v[92:95], v[60:63], v[8:15], v106\n\t" \
  "ds_read_b128 v[28:31], v103 offset:52224\n\t" \
  "v_smfmac_f32_16x16x64_bf16 v[32:35], v[96:99], v[200:207], v106\n\t" \
  "v_cvt_pk_bf16_f32 v72, v88, v89\n\t" \
  "v_smfmac_f32_16x16x64_bf16 v[36:39], v[96:99], v[208:215], v106\n\t" \
  "v_cvt_pk_bf16_f32 v74, v90, v91\n\t" \
  "v_smfmac_f32_16x16x64_bf16 v[32:35], v[52:55], v[216:223], v106\n\t" \
  "v_cvt_pk_bf16_f32 v73, v92, v93\n\t" \
  "v_smfmac_f32_16x16x64_bf16 v[36:39], v[52:55], v[224:231], v106\n\t" \
  "v_cvt_pk_bf16_f32 v75, v94, v95\n\t" \
  "v_smfmac_f32_16x16x64_bf16 v[32:35], v[56:59], v[232:239], v106\n\t" \
  "s_waitcnt lgkmcnt(2)\n\t" \
  "v_smfmac_f32_16x16x64_bf16 v[32:35], v[60:63], v[16:23], v106\n\t" \
  "buffer_store_dwordx4 v[72:75], v108, %[ru], s77 offen sc1\n\t" \
  "v_smfmac_f32_16x16x64_bf16 v[36:39], v[56:59], v[240:247], v106\n\t" \
  "s_waitcnt lgkmcnt(0)\n\t" \
  "v_smfmac_f32_16x16x64_bf16 v[36:39], v[60:63], v[24:31], v106\n\t" \
  "v_cvt_pk_bf16_f32 v76, v32, v33\n\t" \
  "v_cvt_pk_bf16_f32 v78, v34, v35\n\t" \
  "s_nop 5\n\t" \
  "v_cvt_pk_bf16_f32 v77, v36, v37\n\t" \
  "v_cvt_pk_bf16_f32 v79, v38, v39\n\t" \
  "buffer_store_dwordx4 v[76:79], v108, %[ru], s78 offen sc1\n\t" \
  "s_memtime s[76:77]\n\t" \
  "s_mov_b32 s42, 0\n\t" \
  "s_waitcnt lgkmcnt(0)\n\t" \
  "s_mov_b32 s57, s76\n\t" \
  "s_add_i32 s41, s56, 2050\n\t" \
  "s_sub_i32 s76, s92, s51\n\t" \
  "s_sub_i32 s77, s93, s53\n\t" \
  "s_cmp_eq_u32 s76, 0\n\t" \
  "s_cbranch_scc1 21f\n\t" \
  "v_cvt_f32_u32 v117, s76\n\t" \
  "v_cvt_f32_u32 v116, s77\n\t" \
  "v_rcp_f32 v117, v117\n\t" \
  "s_mov_b32 s51, s92\n\t" \
  "v_mul_f32 v117, v117, v116\n\t" \
  "v_max_f32 v117, 0x3f028f5c, v117\n\t" \
  "v_min_f32 v117, 0x3ffeb852, v117\n\t" \
  "v_rcp_f32 v116, v117\n\t" \
  "v_mul_f32 v117, 0x4f000000, v117\n\t" \
  "v_mul_f32 v116, 0x47800000, v116\n\t" \
  "v_cvt_u32_f32 v117, v117\n\t" \
  "v_cvt_u32_f32 v116, v116\n\t" \
  "s_mov_b32 s53, s93\n\t" \
  "v_readfirstlane_b32 s46, v117\n\t" \
  "v_readfirstlane_b32 s52, v116\n\t" \
  "21:\n\t" \
  "s_add_i32 s44, s44, 1\n\t" \
  "s_and_b32 s58, s58, 4\n\t" \
  "s_and_b32 s76, s44, 64\n\t" \
  "s_or_b32 s42, s42, s76\n\t" \
  "s_cbranch_scc0 20b\n\t" \
  "s_branch 99f\n\t" \
  "14:\n\t" \
  "s_add_u32 s59, s59, 0x10000\n\t" \
  "s_cmp_lt_u32 s59, 0x100000\n\t" \
  "s_cbranch_scc1 13b\n\t" \
  "s_mov_b64 exec, -1\n\t" \
  "s_mov_b32 s42, 16\n\t" \
  "s_branch 99f\n\t" \
  "8:\n\t" \
  "s_bitcmp1_b32 s58, 1\n\t" \
  "s_cbranch_scc1 4b\n\t" \
  "s_bitcmp1_b32 s58, 2\n\t" \
  "s_cbranch_scc1 5b\n\t" \
  "v_max3_u32 v116, v32, v33, v34\n\t" \
  "v_max3_u32 v117, v35, v36, v37\n\t" \
  "v_max3_u32 v116, v116, v38, v39\n\t" \
  "v_max_u32 v116, v116, v117\n\t" \
  "v_cmp_eq_u32 vcc, -1, v116\n\t" \
  "s_cbranch_vccz 5b\n\t" \
  "s_branch 0f\n\t" \
  "9:\n\t" \
  "s_bitcmp1_b32 s58, 2\n\t" \
  "s_cbranch_scc1 6b\n\t" \
  "v_max3_u32 v116, v40, v41, v42\n\t" \
  "v_max3_u32 v117, v43, v44, v45\n\t" \
  "v_max3_u32 v116, v116, v46, v47\n\t" \
  "v_max_u32 v116, v116, v117\n\t" \
  "v_cmp_eq_u32 vcc, -1, v116\n\t" \
  "s_cbranch_vccz 6b\n\t" \
  "s_or_b32 s58, s58, 2\n\t" \
  "0:\n\t" \
  "s_or_b32 s58, s58, 1\n\t" \
  "11:\n\t" \
  "buffer_load_dwordx4 v[32:35], v100, %[rh], s40 offen sc1\n\t" \
  "buffer_load_dwordx4 v[36:39], v100, %[rh], s76 offen sc1\n\t" \
  "buffer_load_dwordx4 v[40:43], v100, %[rh], s77 offen sc1\n\t" \
  "buffer_load_dwordx4 v[44:47], v100, %[rh], s78 offen sc1\n\t" \
  "s_waitcnt lgkmcnt(0)\n\t" \
  "s_branch 10b\n\t" \
  "17:\n\t" \
  "s_add_u32 s59, s59, 1\n\t" \
  "s_bitcmp1_b32 s58, 2\n\t" \
  "s_cbranch_scc1 3b\n\t" \
  "s_waitcnt vmcnt(0)\n\t" \
  "v_max_u32 v116, v32, v33\n\t" \
  "v_max3_u32 v116, v116, v34, v35\n\t" \
  "v_max3_u32 v116, v116, v36, v37\n\t" \
  "v_max3_u32 v116, v116, v38, v39\n\t" \
  "v_max3_u32 v116, v116, v40, v41\n\t" \
  "v_max3_u32 v116, v116, v42, v43\n\t" \
  "v_max3_u32 v116, v116, v44, v45\n\t" \
  "v_max3_u32 v116, v116, v46, v47\n\t" \
  "v_cmp_eq_u32 vcc, -1, v116\n\t" \
  "s_cbranch_vccz 3b\n\t" \
  "s_bitcmp1_b32 s59, 6\n\t" \
  "s_cbranch_scc1 12f\n\t" \
  "buffer_load_dwordx4 v[32:35], v100, %[rh], s40 offen sc1\n\t" \
  "buffer_load_dwordx4 v[36:39], v100, %[rh], s76 offen sc1\n\t" \
  "buffer_load_dwordx4 v[40:43], v100, %[rh], s77 offen sc1\n\t" \
  "buffer_load_dwordx4 v[44:47], v100, %[rh], s78 offen sc1\n\t" \
  "s_branch 17b\n\t" \
  "12:\n\t" \
  "s_mov_b32 s42, s58\n\t" \
  "s_branch 99f\n\t" \
  "92:\n\t" \
  "s_mov_b32 s58, s42\n\t" \
  "20:\n\t" \
  "s_and_b32 s76, s44, 7\n\t" \
  "s_lshl_b32 s76, s76, 15\n\t" \
  "s_add_i32 s40, s54, s76\n\t" \
  "s_and_b32 s76, s44, 3\n\t" \
  "s_lshl_b32 s45, s76, 14\n\t" \
  "s_and_b32 s47, s44, 3\n\t" \
  "s_lshr_b32 s76, s47, 1\n\t" \
  "s_lshl_b32 s47, s47, 20\n\t" \
  "s_add_i32 s47, s47, s55\n\t" \
  "s_bitcmp1_b32 %[sel], s76\n\t" \
  "s_cselect_b32 s76, 0x2000, 0\n\t" \
  "s_add_i32 s47, s47, s76\n\t" \
  "s_add_i32 s49, s44, -1\n\t" \
  "s_and_b32 s48, s49, 3\n\t" \
  "s_lshl_b32 s48, s48, 14\n\t" \
  "s_lshl_b32 s50, s53, 8\n\t" \
  "s_mov_b32 s59, 0\n\t" \
  "s_mov_b32 s92, s51\n\t" \
  "s_mov_b32 s93, s53\n\t" \
  "s_xor_b32 s76, s40, 0x400\n\t" \
  "s_xor_b32 s77, s40, 0x800\n\t" \
  "s_xor_b32 s78, s40, 0xc00\n\t" \
  "s_cmp_lg_u32 s58, 0\n\t" \
  "s_cbranch_scc1 11f\n\t" \
  "10:\n\t" \
  "ds_read_b128 v[48:51], v102 offset:32768\n\t" \
  "ds_read_b128 v[52:55], v102 offset:33792\n\t" \
  "ds_read_b128 v[56:59], v102 offset:34816\n\t" \
  "ds_read_b128 v[60:63], v102 offset:35840\n\t" \
  "ds_read_b128 v[0:3], v102 offset:49152\n\t" \
  "ds_read_b128 v[4:7], v102 offset:50176\n\t" \
  "ds_read_b128 v[8:11], v102 offset:51200\n\t" \
  "ds_read_b128 v[12:15], v102 offset:52224\n\t" \
  "ds_read_b128 v[16:19], v102 offset:0\n\t" \
  "ds_read_b128 v[20:23], v102 offset:1024\n\t" \
  "ds_read_b128 v[24:27], v102 offset:2048\n\t" \
  "ds_read_b128 v[28:31], v102 offset:3072\n\t" \
  "s_waitcnt lgkmcnt(4)\n\t" \
  "ds_read_b128 v[80:83], v102 offset:16384\n\t" \
  "ds_read_b128 v[84:87], v102 offset:17408\n\t" \
  "ds_read_b128 v[88:91], v102 offset:18432\n\t" \
  "ds_read_b128 v[92:95], v102 offset:19456\n\t" \
  "s_bitcmp1_b32 s58, 1\n\t" \
  "s_cselect_b64 s[80:81], -1, 0\n\t" \
  "s_cmp_lg_u32 s58, 0\n\t" \
  "s_cbranch_scc1 17f\n\t" \
  "s_mov_b32 exec_hi, 0\n\t" \
  "16:\n\t" \
  "s_memtime vcc\n\t" \
  "s_waitcnt lgkmcnt(0)\n\t" \
  "s_sub_i32 s79, s41, vcc_lo\n\t" \
  "s_cmp_gt_i32 s79, 64\n\t" \
  "s_cbranch_scc1 16b\n\t" \
  "13:\n\t" \
  "buffer_load_dword v116, v110, %[rh], s48 offen sc1\n\t" \
  "s_waitcnt vmcnt(0)\n\t" \
  "15:\n\t" \
  "v_mov_b32 v117, 20000\n\t" \
  "v_cmp_ne_u32_sdwa vcc, v116, s49 src0_sel:BYTE_0 src1_sel:DWORD\n\t" \
  "v_subrev_u32 v116, s50, v116\n\t" \
  "v_ashrrev_i32 v116, 8, v116\n\t" \
  "s_nop 1\n\t" \
  "v_max_i32_dpp v116, v116, v116 quad_perm:[1,0,3,2] row_mask:0xf bank_mask:0xf\n\t" \
  "s_cbranch_vccnz 14f\n\t" \
  "s_nop 0\n\t" \
  "v_max_i32_dpp v116, v116, v116 quad_perm:[2,3,0,1] row_mask:0xf bank_mask:0xf\n\t" \
  "s_nop 1\n\t" \
  "v_max_i32_dpp v116, v116, v116 row_half_mirror row_mask:0xf bank_mask:0xf\n\t" \
  "s_nop 1\n\t" \
  "v_max_i32_dpp v116, v116, v116 row_mirror row_mask:0xf bank_mask:0xf\n\t" \
  "v_med3_i32 v116, v116, 0, v117\n\t" \
  "v_mul_u32_u24 v116, s52, v116\n\t" \
  "v_lshrrev_b32 v116, 16, v116\n\t" \
  "s_mov_b64 exec, -1\n\t" \
  "v_readlane_b32 s84, v116, 0\n\t" \
  "v_readlane_b32 s85, v116, 16\n\t" \
  "s_max_u32 s84, s84, s85\n\t" \
  "s_add_i32 s84, s84, s51\n\t" \
  "1:\n\t" \
  "s_memtime vcc\n\t" \
  "s_waitcnt lgkmcnt(0)\n\t" \
  "s_sub_i32 s79, s84, vcc_lo\n\t" \
  "s_cmp_gt_i32 s79, 128\n\t" \
  "s_cbranch_scc1 1b\n\t" \
  "2:\n\t" \
  "s_sub_i32 s79, s79, 33\n\t" \
  "s_cmp_gt_i32 s79, 56\n\t" \
  "s_cbranch_scc1 2b\n\t" \
  "buffer_load_dwordx4 v[32:35], v100, %[rh], s40 offen sc1\n\t" \
  "s_nop 12\n\t" \
  "buffer_load_dwordx4 v[36:39], v100, %[rh], s76 offen sc1\n\t" \
  "s_nop 12\n\t" \
  "buffer_load_dwordx4 v[40:43], v100, %[rh], s77 offen sc1\n\t" \
  "s_nop 12\n\t" \
  "buffer_load_dwordx4 v[44:47], v100, %[rh], s78 offen sc1\n\t" \
  "s_memtime vcc\n\t" \
  "s_memrealtime s[82:83]\n\t" \
  "s_nop 4\n\t" \
  "s_memrealtime s[84:85]\n\t" \
  "s_nop 4\n\t" \
  "s_memrealtime s[94:95]\n\t" \
  "s_nop 4\n\t" \
  "s_memrealtime s[86:87]\n\t" \
  "s_nop 4\n\t" \
  "s_memrealtime s[88:89]\n\t" \
  "s_nop 4\n\t" \
  "s_memrealtime s[90:91]\n\t" \
  "s_mov_b32 s92, 0\n\t" \
  "s_waitcnt lgkmcnt(0)\n\t" \
  "s_cmp_eq_u32 s82, s84\n\t" \
  "s_addc_u32 s92, s92, 0\n\t" \
  "s_cmp_eq_u32 s82, s94\n\t" \
  "s_addc_u32 s92, s92, 0\n\t" \
  "s_cmp_eq_u32 s82, s86\n\t" \
  "s_addc_u32 s92, s92, 0\n\t" \
  "s_cmp_eq_u32 s82, s88\n\t" \
  "s_addc_u32 s92, s92, 0\n\t" \
  "s_cmp_eq_u32 s82, s90\n\t" \
  "s_addc_u32 s92, s92, 0\n\t" \
  "s_mul_i32 s92, s92, 24\n\t" \
  "s_add_i32 s92, s92, vcc_lo\n\t" \
  "s_lshl4_add_u32 s93, s82, 64\n\t" \
  "3:\n\t" \
  "v_mov_b64 v[72:73], 0\n\t" \
  "v_mov_b64 v[74:75], 0\n\t" \
  "v_mov_b64 v[76:77], 0\n\t" \
  "v_mov_b64 v[78:79], 0\n\t" \
  "s_waitcnt lgkmcnt(0)\n\t" \
  "s_waitcnt vmcnt(3)\n\t" \
  "v_smfmac_f32_16x16x64_bf16 v[72:75], v[32:35], v[48:55], v104\n\t" \
  "ds_write_b128 v101, v[32:35] offset:8192\n\t" \
  "v_smfmac_f32_16x16x64_bf16 v[76:79], v[32:35], v[56:63], v104\n\t" \
  "s_waitcnt vmcnt(2)\n\t" \
  "ds_write_b128 v101, v[36:39] offset:9216\n\t" \
  "v_smfmac_f32_16x16x64_bf16 v[72:75], v[36:39], a[96:103], v104\n\t" \
  "v_smfmac_f32_16x16x64_bf16 v[76:79], v[36:39], a[104:111], v104\n\t" \
  "s_nop 3\n\t" \
  "v_cmp_u_f32 vcc, v72, v73\n\t" \
  "v_cmp_u_f32 s[82:83], v74, v75\n\t" \
  "s_waitcnt vmcnt(1)\n\t" \
  "v_smfmac_f32_16x16x64_bf16 v[72:75], v[40:43], a[112:119], v104\n\t" \
  "s_or_b64 vcc, vcc, s[82:83]\n\t" \
  "s_or_b64 vcc, vcc, s[80:81]\n\t" \
  "v_smfmac_f32_16x16x64_bf16 v[76:79], v[40:43], a[120:127], v104\n\t" \
  "s_waitcnt lgkmcnt(0)\n\t" \
  "s_cbranch_scc1 8f\n\t" \
  "5:\n\t" \
  "s_barrier\n\t" \
  "4:\n\t" \
  "s_memtime s[56:57]\n\t" \
  "ds_read_b128 v[48:51], v101 offset:12288\n\t" \
  "ds_read_b128 v[52:55], v101 offset:13312\n\t" \
  "ds_read_b128 v[56:59], v101 offset:1024\n\t" \
  "s_waitcnt vmcnt(0)\n\t" \
  "v_smfmac_f32_16x16x64_bf16 v[72:75], v[44:47], a[128:135], v104\n\t" \
  "ds_read_b128 v[60:63], v101 offset:0\n\t" \
  "ds_read_b128 v[64:67], v101 offset:5120\n\t" \
  "v_smfmac_f32_16x16x64_bf16 v[76:79], v[44:47], a[136:143], v104\n\t" \
  "ds_read_b128 v[68:71], v101 offset:4096\n\t" \
  "ds_write_b128 v101, v[40:43] offset:10240\n\t" \
  "s_nop 1\n\t" \
  "v_cmp_u_f32 vcc, v72, v73\n\t" \
  "v_cmp_u_f32 s[82:83], v74, v75\n\t" \
  "s_waitcnt lgkmcnt(6)\n\t" \
  "v_smfmac_f32_16x16x64_bf16 v[72:75], v[48:51], v[0:7], v104\n\t" \
  "s_or_b64 vcc, vcc, s[82:83]\n\t" \
  "v_smfmac_f32_16x16x64_bf16 v[76:79], v[48:51], v[8:15], v104\n\t" \
  "s_cbranch_scc1 9f\n\t" \
  "6:\n\t" \
  "s_waitcnt lgkmcnt(5)\n\t" \
  "v_smfmac_f32_16x16x64_bf16 v[72:75], v[52:55], a[144:151], v104\n\t" \
  "ds_write_b128 v101, v[44:47] offset:11264\n\t" \
  "v_smfmac_f32_16x16x64_bf16 v[76:79], v[52:55], a[152:159], v104\n\t" \
  "v_mov_b64 v[40:41], 0\n\t" \
  "s_waitcnt lgkmcnt(5)\n\t" \
  "v_smfmac_f32_16x16x64_bf16 v[72:75], v[56:59], a[0:7], v104\n\t" \
  "v_mov_b64 v[42:43], 0\n\t" \
  "v_smfmac_f32_16x16x64_bf16 v[76:79], v[56:59], a[8:15], v104\n\t" \
  "s_waitcnt lgkmcnt(4)\n\t" \
  "v_smfmac_f32_16x16x64_bf16 v[72:75], v[60:63], v[16:23], v104\n\t" \
  "s_waitcnt lgkmcnt(0)\n\t" \
  "s_barrier\n\t" \
  "v_smfmac_f32_16x16x64_bf16 v[76:79], v[60:63], v[24:31], v104\n\t" \
  "ds_read_b128 v[48:51], v101 offset:14336\n\t" \
  "ds_read_b128 v[52:55], v101 offset:2048\n\t" \
  "v_smfmac_f32_16x16x64_bf16 v[72:75], v[64:67], a[48:55], v104\n\t" \
  "ds_read_b128 v[56:59], v101 offset:6144\n\t" \
  "ds_read_b128 v[60:63], v101 offset:15360\n\t" \
  "v_smfmac_f32_16x16x64_bf16 v[76:79], v[64:67], a[56:63], v104\n\t" \
  "ds_read_b128 v[32:35], v101 offset:3072\n\t" \
  "ds_read_b128 v[36:39], v101 offset:7168\n\t" \
  "v_smfmac_f32_16x16x64_bf16 v[72:75], v[68:71], v[80:87], v104\n\t" \
  "ds_read_b128 v[0:3], v103 offset:0\n\t" \
  "v_mov_b64 v[44:45], 0\n\t" \
  "v_smfmac_f32_16x16x64_bf16 v[76:79], v[68:71], v[88:95], v104\n\t" \
  "v_mov_b64 v[46:47], 0\n\t" \
  "s_waitcnt lgkmcnt(5)\n\t" \
  "v_smfmac_f32_16x16x64_bf16 v[72:75], v[48:51], a[160:167], v104\n\t" \
  "ds_read_b128 v[4:7], v103 offset:1024\n\t" \
  "v_mov_b64 v[80:81], 0\n\t" \
  "v_smfmac_f32_16x16x64_bf16 v[72:75], v[52:55], a[16:23], v104\n\t" \
  "v_mov_b64 v[82:83], 0\n\t" \
  "s_waitcnt lgkmcnt(2)\n\t" \
  "v_smfmac_f32_16x16x64_bf16 v[72:75], v[56:59], a[64:71], v104\n\t" \
  "ds_read_b128 v[8:11], v103 offset:2048\n\t" \
  "v_mov_b64 v[84:85], 0\n\t" \
  "v_smfmac_f32_16x16x64_bf16 v[72:75], v[60:63], a[176:183], v104\n\t" \
  "ds_read_b128 v[12:15], v103 offset:3072\n\t" \
  "v_mov_b64 v[86:87], 0\n\t" \
  "v_smfmac_f32_16x16x64_bf16 v[72:75], v[32:35], a[32:39], v104\n\t" \
  "ds_read_b128 v[16:19], v103 offset:16384\n\t" \
  "v_mov_b64 v[88:89], 0\n\t" \
  "v_smfmac_f32_16x16x64_bf16 v[72:75], v[36:39], a[80:87], v104\n\t" \
  "ds_read_b128 v[20:23], v103 offset:17408\n\t" \
  "v_mov_b64 v[90:91], 0\n\t" \
  "v_smfmac_f32_16x16x64_bf16 v[76:79], v[48:51], a[168:175], v104\n\t" \
  "ds_read_b128 v[24:27], v103 offset:18432\n\t" \
  "v_mov_b64 v[92:93], 0\n\t" \
  "v_smfmac_f32_16x16x64_bf16 v[76:79], v[52:55], a[24:31], v104\n\t" \
  "v_add_f32 v96, v72, v74\n\t" \
  "v_add_f32 v97, v73, v75\n\t" \
  "v_cvt_pk_bf16_f32 v96, v96, v97\n\t" \
  "ds_write_b32 v105, v96\n\t" \
  "v_smfmac_f32_16x16x64_bf16 v[76:79], v[56:59], a[72:79], v104\n\t" \
  "ds_read_b128 v[28:31], v103 offset:19456\n\t" \
  "v_mov_b64 v[94:95], 0\n\t" \
  "v_smfmac_f32_16x16x64_bf16 v[76:79], v[60:63], a[184:191], v104\n\t" \
  "v_smfmac_f32_16x16x64_bf16 v[76:79], v[32:35], a[40:47], v104\n\t" \
  "v_smfmac_f32_16x16x64_bf16 v[76:79], v[36:39], a[88:95], v104\n\t" \
  "s_waitcnt lgkmcnt(1)\n\t" \
  "s_barrier\n\t" \
  "ds_read_b64_tr_b16 v[96:97], v107 offset:0\n\t" \
  "ds_read_b64_tr_b16 v[98:99], v107 offset:64\n\t" \
  "ds_read_b64_tr_b16 v[56:57], v107 offset:1024\n\t" \
  "ds_read_b64_tr_b16 v[58:59], v107 offset:1088\n\t" \
  "s_nop 1\n\t" \
  "v_add_f32 v116, v76, v78\n\t" \
  "v_add_f32 v117, v77, v79\n\t" \
  "v_cvt_pk_bf16_f32 v116, v116, v117\n\t" \
  "ds_write_b32 v105, v116 offset:256\n\t" \
  "s_waitcnt lgkmcnt(3)\n\t" \
  "v_smfmac_f32_16x16x64_bf16 v[40:43], v[96:99], a[192:199], v106\n\t" \
  "v_smfmac_f32_16x16x64_bf16 v[44:47], v[96:99], a[200:207], v106\n\t" \
  "s_waitcnt lgkmcnt(0)\n\t" \
  "s_barrier\n\t" \
  "ds_read_b64_tr_b16 v[52:53], v107 offset:256\n\t" \
  "ds_read_b64_tr_b16 v[54:55], v107 offset:320\n\t" \
  "v_smfmac_f32_16x16x64_bf16 v[40:43], v[56:59], a[224:231], v106\n\t" \
  "ds_read_b64_tr_b16 v[60:61], v107 offset:1280\n\t" \
  "ds_read_b64_tr_b16 v[62:63], v107 offset:1344\n\t" \
  "v_smfmac_f32_16x16x64_bf16 v[44:47], v[56:59], a[232:239], v106\n\t" \
  "v_smfmac_f32_16x16x64_bf16 v[80:83], v[96:99], a[240:247], v106\n\t" \
  "v_mov_b64 v[32:33], 0\n\t" \
  "v_smfmac_f32_16x16x64_bf16 v[84:87], v[96:99], a[248:255], v106\n\t" \
  "v_mov_b64 v[34:35], 0\n\t" \
  "v_smfmac_f32_16x16x64_bf16 v[80:83], v[56:59], v[136:143], v106\n\t" \
  "v_mov_b64 v[36:37], 0\n\t" \
  "v_smfmac_f32_16x16x64_bf16 v[84:87], v[56:59], v[144:151], v106\n\t" \
  "s_waitcnt lgkmcnt(2)\n\t" \
  "v_smfmac_f32_16x16x64_bf16 v[40:43], v[52:55], a[208:215], v106\n\t" \
  "v_smfmac_f32_16x16x64_bf16 v[44:47], v[52:55], a[216:223], v106\n\t" \
  "s_xor_b32 s76, s47, 0x40000\n\t" \
  "s_waitcnt lgkmcnt(0)\n\t" \
  "v_smfmac_f32_16x16x64_bf16 v[40:43], v[60:63], v[0:7], v106\n\t" \
  "v_mov_b64 v[38:39], 0\n\t" \
  "s_xor_b32 s77, s47, 0x80000\n\t" \
  "v_smfmac_f32_16x16x64_bf16 v[44:47], v[60:63], v[8:15], v106\n\t" \
  "s_xor_b32 s78, s47, 0xc0000\n\t" \
  "v_smfmac_f32_16x16x64_bf16 v[80:83], v[52:55], v[120:127], v106\n\t" \
  "ds_read_b128 v[0:3], v103 offset:32768\n\t" \
  "v_cvt_pk_bf16_f32 v64, v40, v41\n\t" \
  "v_smfmac_f32_16x16x64_bf16 v[84:87], v[52:55], v[128:135], v106\n\t" \
  "ds_read_b128 v[4:7], v103 offset:33792\n\t" \
  "v_cvt_pk_bf16_f32 v66, v42, v43\n\t" \
  "v_smfmac_f32_16x16x64_bf16 v[80:83], v[60:63], v[16:23], v106\n\t" \
  "ds_read_b128 v[8:11], v103 offset:34816\n\t" \
  "v_cvt_pk_bf16_f32 v65, v44, v45\n\t" \
  "v_smfmac_f32_16x16x64_bf16 v[84:87], v[60:63], v[24:31], v106\n\t" \
  "ds_read_b128 v[12:15], v103 offset:35840\n\t" \
  "v_cvt_pk_bf16_f32 v67, v46, v47\n\t" \
  "v_smfmac_f32_16x16x64_bf16 v[88:91], v[96:99], v[152:159], v106\n\t" \
  "v_cvt_pk_bf16_f32 v68, v80, v81\n\t" \
  "v_smfmac_f32_16x16x64_bf16 v[92:95], v[96:99], v[160:167], v106\n\t" \
  "v_cvt_pk_bf16_f32 v70, v82, v83\n\t" \
  "v_smfmac_f32_16x16x64_bf16 v[88:91], v[52:55], v[168:175], v106\n\t" \
  "buffer_store_dwordx4 v[64:67], v108, %[ru], s47 offen sc1\n\t" \
  "v_cvt_pk_bf16_f32 v69, v84, v85\n\t" \
  "v_smfmac_f32_16x16x64_bf16 v[92:95], v[52:55], v[176:183], v106\n\t" \
  "v_cvt_pk_bf16_f32 v71, v86, v87\n\t" \
  "v_smfmac_f32_16x16x64_bf16 v[88:91], v[56:59], v[184:191], v106\n\t" \
  "ds_read_b128 v[16:19], v103 offset:49152\n\t" \
  "v_smfmac_f32_16x16x64_bf16 v[92:95], v[56:59], v[192:199], v106\n\t" \
  "ds_read_b128 v[20:23], v103 offset:50176\n\t" \
  "s_waitcnt lgkmcnt(4)\n\t" \
  "v_smfmac_f32_16x16x64_bf16 v[88:91], v[60:63], v[0:7], v106\n\t" \
  "ds_read_b128 v[24:27], v103 offset:51200\n\t" \
  "buffer_store_dwordx4 v[68:71], v108, %[ru], s76 offen sc1\n\t" \
  "s_waitcnt lgkmcnt(3)\n\t" \
  "v_smfmac_f32_16x16x64_bf16 v[92:95], v[60:63], v[8:15], v106\n\t" \
  "ds_read_b128 v[28:31], v103 offset:52224\n\t" \
  "v_smfmac_f32_16x16x64_bf16 v[32:35], v[96:99], v[200:207], v106\n\t" \
  "v_cvt_pk_bf16_f32 v72, v88, v89\n\t" \
  "v_smfmac_f32_16x16x64_bf16 v[36:39], v[96:99], v[208:215], v106\n\t" \
  "v_cvt_pk_bf16_f32 v74, v90, v91\n\t" \
  "v_smfmac_f32_16x16x64_bf16 v[32:35], v[52:55], v[216:223], v106\n\t" \
  "v_cvt_pk_bf16_f32 v73, v92, v93\n\t" \
  "v_smfmac_f32_16x16x64_bf16 v[36:39], v[52:55], v[224:231], v106\n\t" \
  "v_cvt_pk_bf16_f32 v75, v94, v95\n\t" \
  "v_smfmac_f32_16x16x64_bf16 v[32:35], v[56:59], v[232:239], v106\n\t" \
  "s_waitcnt lgkmcnt(2)\n\t" \
  "v_smfmac_f32_16x16x64_bf16 v[32:35], v[60:63], v[16:23], v106\n\t" \
  "v_smfmac_f32_16x16x64_bf16 v[36:39], v[56:59], v[240:247], v106\n\t" \
  "buffer_store_dwordx4 v[72:75], v108, %[ru], s77 offen sc1\n\t" \
  "s_waitcnt lgkmcnt(0)\n\t" \
  "v_smfmac_f32_16x16x64_bf16 v[36:39], v[60:63], v[24:31], v106\n\t" \
  "v_cvt_pk_bf16_f32 v76, v32, v33\n\t" \
  "v_cvt_pk_bf16_f32 v78, v34, v35\n\t" \
  "s_nop 5\n\t" \
  "v_cvt_pk_bf16_f32 v77, v36, v37\n\t" \
  "v_cvt_pk_bf16_f32 v79, v38, v39\n\t" \
  "buffer_store_dwordx4 v[76:79], v108, %[ru], s78 offen sc1\n\t" \
  "s_memtime s[76:77]\n\t" \
  "s_mov_b32 s42, 0\n\t" \
  "s_waitcnt lgkmcnt(0)\n\t" \
  "s_mov_b32 s57, s76\n\t" \
  "s_add_i32 s41, s56, 2050\n\t" \
  "s_sub_i32 s76, s92, s51\n\t" \
  "s_sub_i32 s77, s93, s53\n\t" \
  "s_cmp_eq_u32 s76, 0\n\t" \
  "s_cbranch_scc1 21f\n\t" \
  "v_cvt_f32_u32 v117, s76\n\t" \
  "v_cvt_f32_u32 v116, s77\n\t" \
  "v_rcp_f32 v117, v117\n\t" \
  "s_mov_b32 s51, s92\n\t" \
  "v_mul_f32 v117, v117, v116\n\t" \
  "v_max_f32 v117, 0x3f028f5c, v117\n\t" \
  "v_min_f32 v117, 0x3ffeb852, v117\n\t" \
  "v_rcp_f32 v116, v117\n\t" \
  "v_mul_f32 v117, 0x4f000000, v117\n\t" \
  "v_mul_f32 v116, 0x47800000, v116\n\t" \
  "v_cvt_u32_f32 v117, v117\n\t" \
  "v_cvt_u32_f32 v116, v116\n\t" \
  "s_mov_b32 s53, s93\n\t" \
  "v_readfirstlane_b32 s46, v117\n\t" \
  "v_readfirstlane_b32 s52, v116\n\t" \
  "21:\n\t" \
  "s_add_i32 s44, s44, 1\n\t" \
  "s_and_b32 s58, s58, 4\n\t" \
  "s_and_b32 s76, s44, 64\n\t" \
  "s_or_b32 s42, s42, s76\n\t" \
  "s_cbranch_scc0 20b\n\t" \
  "s_branch 99f\n\t" \
  "14:\n\t" \
  "s_add_u32 s59, s59, 0x10000\n\t" \
  "s_cmp_lt_u32 s59, 0x100000\n\t" \
  "s_cbranch_scc1 13b\n\t" \
  "s_mov_b64 exec, -1\n\t" \
  "s_mov_b32 s42, 16\n\t" \
  "s_branch 99f\n\t" \
  "8:\n\t" \
  "s_bitcmp1_b32 s58, 1\n\t" \
  "s_cbranch_scc1 4b\n\t" \
  "s_bitcmp1_b32 s58, 2\n\t" \
  "s_cbranch_scc1 5b\n\t" \
  "v_max3_u32 v116, v32, v33, v34\n\t" \
  "v_max3_u32 v117, v35, v36, v37\n\t" \
  "v_max3_u32 v116, v116, v38, v39\n\t" \
  "v_max_u32 v116, v116, v117\n\t" \
  "v_cmp_eq_u32 vcc, -1, v116\n\t" \
  "s_cbranch_vccz 5b\n\t" \
  "s_branch 0f\n\t" \
  "9:\n\t" \
  "s_bitcmp1_b32 s58, 2\n\t" \
  "s_cbranch_scc1 6b\n\t" \
  "v_max3_u32 v116, v40, v41, v42\n\t" \
  "v_max3_u32 v117, v43, v44, v45\n\t" \
  "v_max3_u32 v116, v116, v46, v47\n\t" \
  "v_max_u32 v116, v116, v117\n\t" \
  "v_cmp_eq_u32 vcc, -1, v116\n\t" \
  "s_cbranch_vccz 6b\n\t" \
  "s_or_b32 s58, s58, 2\n\t" \
  "0:\n\t" \
  "s_or_b32 s58, s58, 1\n\t" \
  "11:\n\t" \
  "buffer_load_dwordx4 v[32:35], v100, %[rh], s40 offen sc1\n\t" \
  "buffer_load_dwordx4 v[36:39], v100, %[rh], s76 offen sc1\n\t" \
  "buffer_load_dwordx4 v[40:43], v100, %[rh], s77 offen sc1\n\t" \
  "buffer_load_dwordx4 v[44:47], v100, %[rh], s78 offen sc1\n\t" \
  "s_waitcnt lgkmcnt(0)\n\t" \
  "s_branch 10b\n\t" \
  "17:\n\t" \
  "s_add_u32 s59, s59, 1\n\t" \
  "s_bitcmp1_b32 s58, 2\n\t" \
  "s_cbranch_scc1 3b\n\t" \
  "s_waitcnt vmcnt(0)\n\t" \
  "v_max_u32 v116, v32, v33\n\t" \
  "v_max3_u32 v116, v116, v34, v35\n\t" \
  "v_max3_u32 v116, v116, v36, v37\n\t" \
  "v_max3_u32 v116, v116, v38, v39\n\t" \
  "v_max3_u32 v116, v116, v40, v41\n\t" \
  "v_max3_u32 v116, v116, v42, v43\n\t" \
  "v_max3_u32 v116, v116, v44, v45\n\t" \
  "v_max3_u32 v116, v116, v46, v47\n\t" \
  "v_cmp_eq_u32 vcc, -1, v116\n\t" \
  "s_cbranch_vccz 3b\n\t" \
  "s_bitcmp1_b32 s59, 6\n\t" \
  "s_cbranch_scc1 12f\n\t" \
  "buffer_load_dwordx4 v[32:35], v100, %[rh], s40 offen sc1\n\t" \
  "buffer_load_dwordx4 v[36:39], v100, %[rh], s76 offen sc1\n\t" \
  "buffer_load_dwordx4 v[40:43], v100, %[rh], s77 offen sc1\n\t" \
  "buffer_load_dwordx4 v[44:47], v100, %[rh], s78 offen sc1\n\t" \
  "s_branch 17b\n\t" \
  "12:\n\t" \
  "s_mov_b32 s42, s58\n\t" \
  "s_branch 99f\n\t" \
  "93:\n\t" \
  "s_mov_b32 s58, s42\n\t" \
  "20:\n\t" \
  "s_and_b32 s76, s44, 7\n\t" \
  "s_lshl_b32 s76, s76, 15\n\t" \
  "s_add_i32 s40, s54, s76\n\t" \
  "s_and_b32 s76, s44, 3\n\t" \
  "s_lshl_b32 s45, s76, 14\n\t" \
  "s_and_b32 s47, s44, 3\n\t" \
  "s_lshr_b32 s76, s47, 1\n\t" \
  "s_lshl_b32 s47, s47, 20\n\t" \
  "s_add_i32 s47, s47, s55\n\t" \
  "s_bitcmp1_b32 %[sel], s76\n\t" \
  "s_cselect_b32 s76, 0x2000, 0\n\t" \
  "s_add_i32 s47, s47, s76\n\t" \
  "s_add_i32 s49, s44, -1\n\t" \
  "s_and_b32 s48, s49, 3\n\t" \
  "s_lshl_b32 s48, s48, 14\n\t" \
  "s_lshl_b32 s50, s53, 8\n\t" \
  "s_mov_b32 s59, 0\n\t" \
  "s_mov_b32 s92, s51\n\t" \
  "s_mov_b32 s93, s53\n\t" \
  "s_xor_b32 s76, s40, 0x400\n\t" \
  "s_xor_b32 s77, s40, 0x800\n\t" \
  "s_xor_b32 s78, s40, 0xc00\n\t" \
  "s_cmp_lg_u32 s58, 0\n\t" \
  "s_cbranch_scc1 11f\n\t" \
  "10:\n\t" \
  "ds_read_b128 v[48:51], v102 offset:49152\n\t" \
  "ds_read_b128 v[52:55], v102 offset:50176\n\t" \
  "ds_read_b128 v[56:59], v102 offset:51200\n\t" \
  "ds_read_b128 v[60:63], v102 offset:52224\n\t" \
  "ds_read_b128 v[0:3], v102 offset:0\n\t" \
  "ds_read_b128 v[4:7], v102 offset:1024\n\t" \
  "ds_read_b128 v[8:11], v102 offset:2048\n\t" \
  "ds_read_b128 v[12:15], v102 offset:3072\n\t" \
  "ds_read_b128 v[16:19], v102 offset:16384\n\t" \
  "ds_read_b128 v[20:23], v102 offset:17408\n\t" \
  "ds_read_b128 v[24:27], v102 offset:18432\n\t" \
  "ds_read_b128 v[28:31], v102 offset:19456\n\t" \
  "s_waitcnt lgkmcnt(4)\n\t" \
  "ds_read_b128 v[80:83], v102 offset:32768\n\t" \
  "ds_read_b128 v[84:87], v102 offset:33792\n\t" \
  "ds_read_b128 v[88:91], v102 offset:34816\n\t" \
  "ds_read_b128 v[92:95], v102 offset:35840\n\t" \
  "s_bitcmp1_b32 s58, 1\n\t" \
  "s_cselect_b64 s[80:81], -1, 0\n\t" \
  "s_cmp_lg_u32 s58, 0\n\t" \
  "s_cbranch_scc1 17f\n\t" \
  "s_mov_b32 exec_hi, 0\n\t" \
  "16:\n\t" \
  "s_memtime vcc\n\t" \
  "s_waitcnt lgkmcnt(0)\n\t" \
  "s_sub_i32 s79, s41, vcc_lo\n\t" \
  "s_cmp_gt_i32 s79, 64\n\t" \
  "s_cbranch_scc1 16b\n\t" \
  "13:\n\t" \
  "buffer_load_dword v116, v110, %[rh], s48 offen sc1\n\t" \
  "s_waitcnt vmcnt(0)\n\t" \
  "15:\n\t" \
  "v_mov_b32 v117, 20000\n\t" \
  "v_cmp_ne_u32_sdwa vcc, v116, s49 src0_sel:BYTE_0 src1_sel:DWORD\n\t" \
  "v_subrev_u32 v116, s50, v116\n\t" \
  "v_ashrrev_i32 v116, 8, v116\n\t" \
  "s_nop 1\n\t" \
  "v_max_i32_dpp v116, v116, v116 quad_perm:[1,0,3,2] row_mask:0xf bank_mask:0xf\n\t" \
  "s_cbranch_vccnz 14f\n\t" \
  "s_nop 0\n\t" \
  "v_max_i32_dpp v116, v116, v116 quad_perm:[2,3,0,1] row_mask:0xf bank_mask:0xf\n\t" \
  "s_nop 1\n\t" \
  "v_max_i32_dpp v116, v116, v116 row_half_mirror row_mask:0xf bank_mask:0xf\n\t" \
  "s_nop 1\n\t" \
  "v_max_i32_dpp v116, v116, v116 row_mirror row_mask:0xf bank_mask:0xf\n\t" \
  "v_med3_i32 v116, v116, 0, v117\n\t" \
  "v_mul_u32_u24 v116, s52, v116\n\t" \
  "v_lshrrev_b32 v116, 16, v116\n\t" \
  "s_mov_b64 exec, -1\n\t" \
  "v_readlane_b32 s84, v116, 0\n\t" \
  "v_readlane_b32 s85, v116, 16\n\t" \
  "s_max_u32 s84, s84, s85\n\t" \
  "s_add_i32 s84, s84, s51\n\t" \
  "1:\n\t" \
  "s_memtime vcc\n\t" \
  "s_waitcnt lgkmcnt(0)\n\t" \
  "s_sub_i32 s79, s84, vcc_lo\n\t" \
  "s_cmp_gt_i32 s79, 128\n\t" \
  "s_cbranch_scc1 1b\n\t" \
  "2:\n\t" \
  "s_sub_i32 s79, s79, 33\n\t" \
  "s_cmp_gt_i32 s79, 56\n\t" \
  "s_cbranch_scc1 2b\n\t" \
  "buffer_load_dwordx4 v[32:35], v100, %[rh], s40 offen sc1\n\t" \
  "s_nop 12\n\t" \
  "buffer_load_dwordx4 v[36:39], v100, %[rh], s76 offen sc1\n\t" \
  "s_nop 12\n\t" \
  "buffer_load_dwordx4 v[40:43], v100, %[rh], s77 offen sc1\n\t" \
  "s_nop 12\n\t" \
  "buffer_load_dwordx4 v[44:47], v100, %[rh], s78 offen sc1\n\t" \
  "s_memtime vcc\n\t" \
  "s_memrealtime s[82:83]\n\t" \
  "s_nop 4\n\t" \
  "s_memrealtime s[84:85]\n\t" \
  "s_nop 4\n\t" \
  "s_memrealtime s[94:95]\n\t" \
  "s_nop 4\n\t" \
  "s_memrealtime s[86:87]\n\t" \
  "s_nop 4\n\t" \
  "s_memrealtime s[88:89]\n\t" \
  "s_nop 4\n\t" \
  "s_memrealtime s[90:91]\n\t" \
  "s_mov_b32 s92, 0\n\t" \
  "s_waitcnt lgkmcnt(0)\n\t" \
  "s_cmp_eq_u32 s82, s84\n\t" \
  "s_addc_u32 s92, s92, 0\n\t" \
  "s_cmp_eq_u32 s82, s94\n\t" \
  "s_addc_u32 s92, s92, 0\n\t" \
  "s_cmp_eq_u32 s82, s86\n\t" \
  "s_addc_u32 s92, s92, 0\n\t" \
  "s_cmp_eq_u32 s82, s88\n\t" \
  "s_addc_u32 s92, s92, 0\n\t" \
  "s_cmp_eq_u32 s82, s90\n\t" \
  "s_addc_u32 s92, s92, 0\n\t" \
  "s_mul_i32 s92, s92, 24\n\t" \
  "s_add_i32 s92, s92, vcc_lo\n\t" \
  "s_lshl4_add_u32 s93, s82, 64\n\t" \
  "3:\n\t" \
  "v_mov_b64 v[72:73], 0\n\t" \
  "v_mov_b64 v[74:75], 0\n\t" \
  "v_mov_b64 v[76:77], 0\n\t" \
  "v_mov_b64 v[78:79], 0\n\t" \
  "s_waitcnt lgkmcnt(0)\n\t" \
  "s_waitcnt vmcnt(3)\n\t" \
  "v_smfmac_f32_16x16x64_bf16 v[72:75], v[32:35], v[48:55], v104\n\t" \
  "ds_write_b128 v101, v[32:35] offset:12288\n\t" \
  "v_smfmac_f32_16x16x64_bf16 v[76:79], v[32:35], v[56:63], v104\n\t" \
  "s_waitcnt vmcnt(2)\n\t" \
  "ds_write_b128 v101, v[36:39] offset:13312\n\t" \
  "v_smfmac_f32_16x16x64_bf16 v[72:75], v[36:39], a[144:151], v104\n\t" \
  "v_smfmac_f32_16x16x64_bf16 v[76:79], v[36:39], a[152:159], v104\n\t" \
  "s_nop 3\n\t" \
  "v_cmp_u_f32 vcc, v72, v73\n\t" \
  "v_cmp_u_f32 s[82:83], v74, v75\n\t" \
  "s_waitcnt vmcnt(1)\n\t" \
  "v_smfmac_f32_16x16x64_bf16 v[72:75], v[40:43], a[160:167], v104\n\t" \
  "s_or_b64 vcc, vcc, s[82:83]\n\t" \
  "s_or_b64 vcc, vcc, s[80:81]\n\t" \
  "v_smfmac_f32_16x16x64_bf16 v[76:79], v[40:43], a[168:175], v104\n\t" \
  "s_waitcnt lgkmcnt(0)\n\t" \
  "s_cbranch_scc1 8f\n\t" \
  "5:\n\t" \
  "s_barrier\n\t" \
  "4:\n\t" \
  "s_memtime s[56:57]\n\t" \
  "ds_read_b128 v[48:51], v101 offset:0\n\t" \
  "ds_read_b128 v[52:55], v101 offset:1024\n\t" \
  "ds_read_b128 v[56:59], v101 offset:5120\n\t" \
  "s_waitcnt vmcnt(0)\n\t" \
  "v_smfmac_f32_16x16x64_bf16 v[72:75], v[44:47], a[176:183], v104\n\t" \
  "ds_read_b128 v[60:63], v101 offset:4096\n\t" \
  "ds_read_b128 v[64:67], v101 offset:9216\n\t" \
  "v_smfmac_f32_16x16x64_bf16 v[76:79], v[44:47], a[184:191], v104\n\t" \
  "ds_read_b128 v[68:71], v101 offset:8192\n\t" \
  "ds_write_b128 v101, v[40:43] offset:14336\n\t" \
  "s_nop 1\n\t" \
  "v_cmp_u_f32 vcc, v72, v73\n\t" \
  "v_cmp_u_f32 s[82:83], v74, v75\n\t" \
  "s_waitcnt lgkmcnt(6)\n\t" \
  "v_smfmac_f32_16x16x64_bf16 v[72:75], v[48:51], v[0:7], v104\n\t" \
  "s_or_b64 vcc, vcc, s[82:83]\n\t" \
  "v_smfmac_f32_16x16x64_bf16 v[76:79], v[48:51], v[8:15], v104\n\t" \
  "s_cbranch_scc1 9f\n\t" \
  "6:\n\t" \
  "s_waitcnt lgkmcnt(5)\n\t" \
  "v_smfmac_f32_16x16x64_bf16 v[72:75], v[52:55], a[0:7], v104\n\t" \
  "ds_write_b128 v101, v[44:47] offset:15360\n\t" \
  "v_smfmac_f32_16x16x64_bf16 v[76:79], v[52:55], a[8:15], v104\n\t" \
  "v_mov_b64 v[40:41], 0\n\t" \
  "s_waitcnt lgkmcnt(5)\n\t" \
  "v_smfmac_f32_16x16x64_bf16 v[72:75], v[56:59], a[48:55], v104\n\t" \
  "v_mov_b64 v[42:43], 0\n\t" \
  "v_smfmac_f32_16x16x64_bf16 v[76:79], v[56:59], a[56:63], v104\n\t" \
  "s_waitcnt lgkmcnt(4)\n\t" \
  "v_smfmac_f32_16x16x64_bf16 v[72:75], v[60:63], v[16:23], v104\n\t" \
  "s_waitcnt lgkmcnt(0)\n\t" \
  "s_barrier\n\t" \
  "v_smfmac_f32_16x16x64_bf16 v[76:79], v[60:63], v[24:31], v104\n\t" \
  "ds_read_b128 v[48:51], v101 offset:2048\n\t" \
  "ds_read_b128 v[52:55], v101 offset:6144\n\t" \
  "v_smfmac_f32_16x16x64_bf16 v[72:75], v[64:67], a[96:103], v104\n\t" \
  "ds_read_b128 v[56:59], v101 offset:10240\n\t" \
  "ds_read_b128 v[60:63], v101 offset:3072\n\t" \
  "v_smfmac_f32_16x16x64_bf16 v[76:79], v[64:67], a[104:111], v104\n\t" \
  "ds_read_b128 v[32:35], v101 offset:7168\n\t" \
  "ds_read_b128 v[36:39], v101 offset:11264\n\t" \
  "v_smfmac_f32_16x16x64_bf16 v[72:75], v[68:71], v[80:87], v104\n\t" \
  "ds_read_b128 v[0:3], v103 offset:0\n\t" \
  "v_mov_b64 v[44:45], 0\n\t" \
  "v_smfmac_f32_16x16x64_bf16 v[76:79], v[68:71], v[88:95], v104\n\t" \
  "v_mov_b64 v[46:47], 0\n\t" \
  "s_waitcnt lgkmcnt(5)\n\t" \
  "v_smfmac_f32_16x16x64_bf16 v[72:75], v[48:51], a[16:23], v104\n\t" \
  "ds_read_b128 v[4:7], v103 offset:1024\n\t" \
  "v_mov_b64 v[80:81], 0\n\t" \
  "v_smfmac_f32_16x16x64_bf16 v[72:75], v[52:55], a[64:71], v104\n\t" \
  "v_mov_b64 v[82:83], 0\n\t" \
  "s_waitcnt lgkmcnt(2)\n\t" \
  "v_smfmac_f32_16x16x64_bf16 v[72:75], v[56:59], a[112:119], v104\n\t" \
  "ds_read_b128 v[8:11], v103 offset:2048\n\t" \
  "v_mov_b64 v[84:85], 0\n\t" \
  "v_smfmac_f32_16x16x64_bf16 v[72:75], v[60:63], a[32:39], v104\n\t" \
  "ds_read_b128 v[12:15], v103 offset:3072\n\t" \
  "v_mov_b64 v[86:87], 0\n\t" \
  "v_smfmac_f32_16x16x64_bf16 v[72:75], v[32:35], a[80:87], v104\n\t" \
  "ds_read_b128 v[16:19], v103 offset:16384\n\t" \
  "v_mov_b64 v[88:89], 0\n\t" \
  "v_smfmac_f32_16x16x64_bf16 v[72:75], v[36:39], a[128:135], v104\n\t" \
  "ds_read_b128 v[20:23], v103 offset:17408\n\t" \
  "v_mov_b64 v[90:91], 0\n\t" \
  "v_smfmac_f32_16x16x64_bf16 v[76:79], v[48:51], a[24:31], v104\n\t" \
  "ds_read_b128 v[24:27], v103 offset:18432\n\t" \
  "v_mov_b64 v[92:93], 0\n\t" \
  "v_smfmac_f32_16x16x64_bf16 v[76:79], v[52:55], a[72:79], v104\n\t" \
  "v_add_f32 v96, v72, v74\n\t" \
  "v_add_f32 v97, v73, v75\n\t" \
  "v_cvt_pk_bf16_f32 v96, v96, v97\n\t" \
  "ds_write_b32 v105, v96\n\t" \
  "v_smfmac_f32_16x16x64_bf16 v[76:79], v[56:59], a[120:127], v104\n\t" \
  "ds_read_b128 v[28:31], v103 offset:19456\n\t" \
  "v_mov_b64 v[94:95], 0\n\t" \
  "v_smfmac_f32_16x16x64_bf16 v[76:79], v[60:63], a[40:47], v104\n\t" \
  "v_smfmac_f32_16x16x64_bf16 v[76:79], v[32:35], a[88:95], v104\n\t" \
  "v_smfmac_f32_16x16x64_bf16 v[76:79], v[36:39], a[136:143], v104\n\t" \
  "s_waitcnt lgkmcnt(1)\n\t" \
  "s_barrier\n\t" \
  "ds_read_b64_tr_b16 v[96:97], v107 offset:0\n\t" \
  "ds_read_b64_tr_b16 v[98:99], v107 offset:64\n\t" \
  "ds_read_b64_tr_b16 v[56:57], v107 offset:1024\n\t" \
  "ds_read_b64_tr_b16 v[58:59], v107 offset:1088\n\t" \
  "s_nop 1\n\t" \
  "v_add_f32 v116, v76, v78\n\t" \
  "v_add_f32 v117, v77, v79\n\t" \
  "v_cvt_pk_bf16_f32 v116, v116, v117\n\t" \
  "ds_write_b32 v105, v116 offset:256\n\t" \
  "s_waitcnt lgkmcnt(3)\n\t" \
  "v_smfmac_f32_16x16x64_bf16 v[40:43], v[96:99], a[192:199], v106\n\t" \
  "v_smfmac_f32_16x16x64_bf16 v[44:47], v[96:99], a[200:207], v106\n\t" \
  "s_waitcnt lgkmcnt(0)\n\t" \
  "s_barrier\n\t" \
  "ds_read_b64_tr_b16 v[52:53], v107 offset:256\n\t" \
  "ds_read_b64_tr_b16 v[54:55], v107 offset:320\n\t" \
  "v_smfmac_f32_16x16x64_bf16 v[40:43], v[56:59], a[224:231], v106\n\t" \
  "ds_read_b64_tr_b16 v[60:61], v107 offset:1280\n\t" \
  "ds_read_b64_tr_b16 v[62:63], v107 offset:1344\n\t" \
  "v_smfmac_f32_16x16x64_bf16 v[44:47], v[56:59], a[232:239], v106\n\t" \
  "v_smfmac_f32_16x16x64_bf16 v[80:83], v[96:99], a[240:247], v106\n\t" \
  "v_mov_b64 v[32:33], 0\n\t" \
  "v_smfmac_f32_16x16x64_bf16 v[84:87], v[96:99], a[248:255], v106\n\t" \
  "v_mov_b64 v[34:35], 0\n\t" \
  "v_smfmac_f32_16x16x64_bf16 v[80:83], v[56:59], v[136:143], v106\n\t" \
  "v_mov_b64 v[36:37], 0\n\t" \
  "v_smfmac_f32_16x16x64_bf16 v[84:87], v[56:59], v[144:151], v106\n\t" \
  "s_waitcnt lgkmcnt(2)\n\t" \
  "v_smfmac_f32_16x16x64_bf16 v[40:43], v[52:55], a[208:215], v106\n\t" \
  "v_smfmac_f32_16x16x64_bf16 v[44:47], v[52:55], a[216:223], v106\n\t" \
  "s_xor_b32 s76, s47, 0x40000\n\t" \
  "s_waitcnt lgkmcnt(0)\n\t" \
  "v_smfmac_f32_16x16x64_bf16 v[40:43], v[60:63], v[0:7], v106\n\t" \
  "v_mov_b64 v[38:39], 0\n\t" \
  "s_xor_b32 s77, s47, 0x80000\n\t" \
  "v_smfmac_f32_16x16x64_bf16 v[44:47], v[60:63], v[8:15], v106\n\t" \
  "s_xor_b32 s78, s47, 0xc0000\n\t" \
  "v_smfmac_f32_16x16x64_bf16 v[80:83], v[52:55], v[120:127], v106\n\t" \
  "ds_read_b128 v[0:3], v103 offset:32768\n\t" \
  "v_cvt_pk_bf16_f32 v64, v40, v41\n\t" \
  "v_smfmac_f32_16x16x64_bf16 v[84:87], v[52:55], v[128:135], v106\n\t" \
  "ds_read_b128 v[4:7], v103 offset:33792\n\t" \
  "v_cvt_pk_bf16_f32 v66, v42, v43\n\t" \
  "v_smfmac_f32_16x16x64_bf16 v[80:83], v[60:63], v[16:23], v106\n\t" \
  "ds_read_b128 v[8:11], v103 offset:34816\n\t" \
  "v_cvt_pk_bf16_f32 v65, v44, v45\n\t" \
  "v_smfmac_f32_16x16x64_bf16 v[84:87], v[60:63], v[24:31], v106\n\t" \
  "ds_read_b128 v[12:15], v103 offset:35840\n\t" \
  "v_cvt_pk_bf16_f32 v67, v46, v47\n\t" \
  "v_smfmac_f32_16x16x64_bf16 v[88:91], v[96:99], v[152:159], v106\n\t" \
  "v_cvt_pk_bf16_f32 v68, v80, v81\n\t" \
  "v_smfmac_f32_16x16x64_bf16 v[92:95], v[96:99], v[160:167], v106\n\t" \
  "v_cvt_pk_bf16_f32 v70, v82, v83\n\t" \
  "v_smfmac_f32_16x16x64_bf16 v[88:91], v[52:55], v[168:175], v106\n\t" \
  "v_cvt_pk_bf16_f32 v69, v84, v85\n\t" \
  "v_smfmac_f32_16x16x64_bf16 v[92:95], v[52:55], v[176:183], v106\n\t" \
  "buffer_store_dwordx4 v[64:67], v108, %[ru], s47 offen sc1\n\t" \
  "v_cvt_pk_bf16_f32 v71, v86, v87\n\t" \
  "v_smfmac_f32_16x16x64_bf16 v[88:91], v[56:59], v[184:191], v106\n\t" \
  "ds_read_b128 v[16:19], v103 offset:49152\n\t" \
  "v_smfmac_f32_16x16x64_bf16 v[92:95], v[56:59], v[192:199], v106\n\t" \
  "ds_read_b128 v[20:23], v103 offset:50176\n\t" \
  "s_waitcnt lgkmcnt(4)\n\t" \
  "v_smfmac_f32_16x16x64_bf16 v[88:91], v[60:63], v[0:7], v106\n\t" \
  "ds_read_b128 v[24:27], v103 offset:51200\n\t" \
  "s_waitcnt lgkmcnt(3)\n\t" \
  "v_smfmac_f32_16x16x64_bf16 v[92:95], v[60:63], v[8:15], v106\n\t" \
  "ds_read_b128 v[28:31], v103 offset:52224\n\t" \
  "buffer_store_dwordx4 v[68:71], v108, %[ru], s76 offen sc1\n\t" \
  "v_smfmac_f32_16x16x64_bf16 v[32:35], v[96:99], v[200:207], v106\n\t" \
  "v_cvt_pk_bf16_f32 v72, v88, v89\n\t" \
  "v_smfmac_f32_16x16x64_bf16 v[36:39], v[96:99], v[208:215], v106\n\t" \
  "v_cvt_pk_bf16_f32 v74, v90, v91\n\t" \
  "v_smfmac_f32_16x16x64_bf16 v[32:35], v[52:55], v[216:223], v106\n\t" \
  "v_cvt_pk_bf16_f32 v73, v92, v93\n\t" \
  "v_smfmac_f32_16x16x64_bf16 v[36:39], v[52:55], v[224:231], v106\n\t" \
  "v_cvt_pk_bf16_f32 v75, v94, v95\n\t" \
  "v_smfmac_f32_16x16x64_bf16 v[32:35], v[56:59], v[232:239], v106\n\t" \
  "s_waitcnt lgkmcnt(2)\n\t" \
  "v_smfmac_f32_16x16x64_bf16 v[32:35], v[60:63], v[16:23], v106\n\t" \
  "v_smfmac_f32_16x16x64_bf16 v[36:39], v[56:59], v[240:247], v106\n\t" \
  "s_waitcnt lgkmcnt(0)\n\t" \
  "v_smfmac_f32_16x16x64_bf16 v[36:39], v[60:63], v[24:31], v106\n\t" \
  "buffer_store_dwordx4 v[72:75], v108, %[ru], s77 offen sc1\n\t" \
  "v_cvt_pk_bf16_f32 v76, v32, v33\n\t" \
  "v_cvt_pk_bf16_f32 v78, v34, v35\n\t" \
  "s_nop 5\n\t" \
  "v_cvt_pk_bf16_f32 v77, v36, v37\n\t" \
  "v_cvt_pk_bf16_f32 v79, v38, v39\n\t" \
  "buffer_store_dwordx4 v[76:79], v108, %[ru], s78 offen sc1\n\t" \
  "s_memtime s[76:77]\n\t" \
  "s_mov_b32 s42, 0\n\t" \
  "s_waitcnt lgkmcnt(0)\n\t" \
  "s_mov_b32 s57, s76\n\t" \
  "s_add_i32 s41, s56, 2050\n\t" \
  "s_sub_i32 s76, s92, s51\n\t" \
  "s_sub_i32 s77, s93, s53\n\t" \
  "s_cmp_eq_u32 s76, 0\n\t" \
  "s_cbranch_scc1 21f\n\t" \
  "v_cvt_f32_u32 v117, s76\n\t" \
  "v_cvt_f32_u32 v116, s77\n\t" \
  "v_rcp_f32 v117, v117\n\t" \
  "s_mov_b32 s51, s92\n\t" \
  "v_mul_f32 v117, v117, v116\n\t" \
  "v_max_f32 v117, 0x3f028f5c, v117\n\t" \
  "v_min_f32 v117, 0x3ffeb852, v117\n\t" \
  "v_rcp_f32 v116, v117\n\t" \
  "v_mul_f32 v117, 0x4f000000, v117\n\t" \
  "v_mul_f32 v116, 0x47800000, v116\n\t" \
  "v_cvt_u32_f32 v117, v117\n\t" \
  "v_cvt_u32_f32 v116, v116\n\t" \
  "s_mov_b32 s53, s93\n\t" \
  "v_readfirstlane_b32 s46, v117\n\t" \
  "v_readfirstlane_b32 s52, v116\n\t" \
  "21:\n\t" \
  "s_add_i32 s44, s44, 1\n\t" \
  "s_and_b32 s58, s58, 4\n\t" \
  "s_and_b32 s76, s44, 64\n\t" \
  "s_or_b32 s42, s42, s76\n\t" \
  "s_cbranch_scc0 20b\n\t" \
  "s_branch 99f\n\t" \
  "14:\n\t" \
  "s_add_u32 s59, s59, 0x10000\n\t" \
  "s_cmp_lt_u32 s59, 0x100000\n\t" \
  "s_cbranch_scc1 13b\n\t" \
  "s_mov_b64 exec, -1\n\t" \
  "s_mov_b32 s42, 16\n\t" \
  "s_branch 99f\n\t" \
  "8:\n\t" \
  "s_bitcmp1_b32 s58, 1\n\t" \
  "s_cbranch_scc1 4b\n\t" \
  "s_bitcmp1_b32 s58, 2\n\t" \
  "s_cbranch_scc1 5b\n\t" \
  "v_max3_u32 v116, v32, v33, v34\n\t" \
  "v_max3_u32 v117, v35, v36, v37\n\t" \
  "v_max3_u32 v116, v116, v38, v39\n\t" \
  "v_max_u32 v116, v116, v117\n\t" \
  "v_cmp_eq_u32 vcc, -1, v116\n\t" \
  "s_cbranch_vccz 5b\n\t" \
  "s_branch 0f\n\t" \
  "9:\n\t" \
  "s_bitcmp1_b32 s58, 2\n\t" \
  "s_cbranch_scc1 6b\n\t" \
  "v_max3_u32 v116, v40, v41, v42\n\t" \
  "v_max3_u32 v117, v43, v44, v45\n\t" \
  "v_max3_u32 v116, v116, v46, v47\n\t" \
  "v_max_u32 v116, v116, v117\n\t" \
  "v_cmp_eq_u32 vcc, -1, v116\n\t" \
  "s_cbranch_vccz 6b\n\t" \
  "s_or_b32 s58, s58, 2\n\t" \
  "0:\n\t" \
  "s_or_b32 s58, s58, 1\n\t" \
  "11:\n\t" \
  "buffer_load_dwordx4 v[32:35], v100, %[rh], s40 offen sc1\n\t" \
  "buffer_load_dwordx4 v[36:39], v100, %[rh], s76 offen sc1\n\t" \
  "buffer_load_dwordx4 v[40:43], v100, %[rh], s77 offen sc1\n\t" \
  "buffer_load_dwordx4 v[44:47], v100, %[rh], s78 offen sc1\n\t" \
  "s_waitcnt lgkmcnt(0)\n\t" \
  "s_branch 10b\n\t" \
  "17:\n\t" \
  "s_add_u32 s59, s59, 1\n\t" \
  "s_bitcmp1_b32 s58, 2\n\t" \
  "s_cbranch_scc1 3b\n\t" \
  "s_waitcnt vmcnt(0)\n\t" \
  "v_max_u32 v116, v32, v33\n\t" \
  "v_max3_u32 v116, v116, v34, v35\n\t" \
  "v_max3_u32 v116, v116, v36, v37\n\t" \
  "v_max3_u32 v116, v116, v38, v39\n\t" \
  "v_max3_u32 v116, v116, v40, v41\n\t" \
  "v_max3_u32 v116, v116, v42, v43\n\t" \
  "v_max3_u32 v116, v116, v44, v45\n\t" \
  "v_max3_u32 v116, v116, v46, v47\n\t" \
  "v_cmp_eq_u32 vcc, -1, v116\n\t" \
  "s_cbranch_vccz 3b\n\t" \
  "s_bitcmp1_b32 s59, 6\n\t" \
  "s_cbranch_scc1 12f\n\t" \
  "buffer_load_dwordx4 v[32:35], v100, %[rh], s40 offen sc1\n\t" \
  "buffer_load_dwordx4 v[36:39], v100, %[rh], s76 offen sc1\n\t" \
  "buffer_load_dwordx4 v[40:43], v100, %[rh], s77 offen sc1\n\t" \
  "buffer_load_dwordx4 v[44:47], v100, %[rh], s78 offen sc1\n\t" \
  "s_branch 17b\n\t" \
  "12:\n\t" \
  "s_mov_b32 s42, s58\n\t" \
  "s_branch 99f\n\t" \
  "99:\n\t" \
  "s_waitcnt vmcnt(0) lgkmcnt(0)\n\t" \
  ".Lchain_last:\n\t" \
  ""
#define CB_BODY \
  "s_mov_b32 s58, 8\n\t" \
  "s_mov_b32 s59, 0\n\t" \
  "s_sub_i32 s56, s51, 8000\n\t" \
  "s_lshl_b32 s50, s53, 8\n\t" \
  "s_mov_b32 s95, 0x84000\n\t" \
  "s_branch 40f\n\t" \
  "20:\n\t" \
  "s_mov_b64 exec, 0xff\n\t" \
  "s_mov_b32 s88, s51\n\t" \
  "s_add_i32 s85, s44, -1\n\t" \
  "s_add_i32 s79, s44, 2\n\t" \
  "v_cmp_gt_i32 vcc, s79, v210\n\t" \
  "s_and_b32 s96, s79, 15\n\t" \
  "s_lshl_b32 s96, s96, 22\n\t" \
  "s_add_i32 s96, s96, s42\n\t" \
  "s_lshr_b32 s82, s96, 21\n\t" \
  "s_bitcmp0_b32 %[sel], s82\n\t" \
  "s_cselect_b32 s82, 0x2000, 0\n\t" \
  "s_add_i32 s96, s96, s82\n\t" \
  "s_cmp_lt_u32 s79, 64\n\t" \
  "s_cselect_b32 s79, vcc_lo, -1\n\t" \
  "s_cmp_eq_u32 s79, 0\n\t" \
  "s_cselect_b32 s96, s96, 0x7f000000\n\t" \
  "s_cmp_eq_u32 s44, 0\n\t" \
  "s_cbranch_scc1 23f\n\t" \
  "21:\n\t" \
  "v_cmp_ne_u32_sdwa vcc, v208, s85 src0_sel:BYTE_0 src1_sel:DWORD\n\t" \
  "v_subrev_u32 v208, s50, v208\n\t" \
  "v_ashrrev_i32 v208, 8, v208\n\t" \
  "s_nop 1\n\t" \
  "v_max_i32_dpp v208, v208, v208 quad_perm:[1,0,3,2] row_mask:0xf bank_mask:0xf\n\t" \
  "s_cbranch_vccnz 22f\n\t" \
  "24:\n\t" \
  "s_nop 0\n\t" \
  "v_max_i32_dpp v208, v208, v208 quad_perm:[2,3,0,1] row_mask:0xf bank_mask:0xf\n\t" \
  "s_nop 1\n\t" \
  "v_max_i32_dpp v208, v208, v208 row_half_mirror row_mask:0xf bank_mask:0xf\n\t" \
  "v_add_u32 v208, 1120, v208\n\t" \
  "v_med3_i32 v208, v208, 0, v246\n\t" \
  "v_mul_u32_u24 v208, s52, v208\n\t" \
  "v_lshrrev_b32 v208, 16, v208\n\t" \
  "s_nop 0\n\t" \
  "v_readlane_b32 s88, v208, 0\n\t" \
  "s_add_i32 s88, s88, s51\n\t" \
  "23:\n\t" \
  "s_mov_b32 exec_lo, -1\n\t" \
  "v_min_u32 v209, 1520, v209\n\t" \
  "s_nop 1\n\t" \
  "v_max_u32_dpp v209, v209, v209 quad_perm:[1,0,3,2] row_mask:0xf bank_mask:0xf\n\t" \
  "s_nop 1\n\t" \
  "v_max_u32_dpp v209, v209, v209 quad_perm:[2,3,0,1] row_mask:0xf bank_mask:0xf\n\t" \
  "s_nop 1\n\t" \
  "v_max_u32_dpp v209, v209, v209 row_half_mirror row_mask:0xf bank_mask:0xf\n\t" \
  "s_nop 1\n\t" \
  "v_max_u32_dpp v209, v209, v209 row_mirror row_mask:0xf bank_mask:0xf\n\t" \
  "s_nop 1\n\t" \
  "v_readlane_b32 s89, v209, 0\n\t" \
  "v_readlane_b32 s90, v209, 16\n\t" \
  "s_max_u32 s57, s89, s90\n\t" \
  "s_add_i32 s57, s57, 330\n\t" \
  "s_and_b32 s81, s44, 3\n\t" \
  "s_lshl_b32 s81, s81, 14\n\t" \
  "s_add_i32 s81, s81, 0x70000\n\t" \
  "s_mov_b64 exec, 0xff\n\t" \
  "1:\n\t" \
  "s_memtime vcc\n\t" \
  "s_waitcnt lgkmcnt(0)\n\t" \
  "s_sub_i32 s79, s88, vcc_lo\n\t" \
  "s_cmp_gt_i32 s79, 128\n\t" \
  "s_cbranch_scc1 1b\n\t" \
  "2:\n\t" \
  "s_sub_i32 s79, s79, 33\n\t" \
  "s_cmp_gt_i32 s79, 56\n\t" \
  "s_cbranch_scc1 2b\n\t" \
  "buffer_load_dword v208, v234, %[rh], s81 offen sc1\n\t" \
  "s_memtime vcc\n\t" \
  "s_memrealtime s[82:83]\n\t" \
  "s_nop 4\n\t" \
  "s_memrealtime s[84:85]\n\t" \
  "s_nop 4\n\t" \
  "s_memrealtime s[94:95]\n\t" \
  "s_nop 4\n\t" \
  "s_memrealtime s[86:87]\n\t" \
  "s_nop 4\n\t" \
  "s_memrealtime s[88:89]\n\t" \
  "s_nop 4\n\t" \
  "s_memrealtime s[90:91]\n\t" \
  "s_mov_b32 s92, 0\n\t" \
  "s_waitcnt lgkmcnt(0)\n\t" \
  "s_cmp_eq_u32 s82, s84\n\t" \
  "s_addc_u32 s92, s92, 0\n\t" \
  "s_cmp_eq_u32 s82, s94\n\t" \
  "s_addc_u32 s92, s92, 0\n\t" \
  "s_cmp_eq_u32 s82, s86\n\t" \
  "s_addc_u32 s92, s92, 0\n\t" \
  "s_cmp_eq_u32 s82, s88\n\t" \
  "s_addc_u32 s92, s92, 0\n\t" \
  "s_cmp_eq_u32 s82, s90\n\t" \
  "s_addc_u32 s92, s92, 0\n\t" \
  "s_mul_i32 s92, s92, 24\n\t" \
  "s_add_i32 s92, s92, vcc_lo\n\t" \
  "s_lshl4_add_u32 s93, s82, 64\n\t" \
  "s_sub_i32 s82, s92, s51\n\t" \
  "s_sub_i32 s83, s93, s53\n\t" \
  "s_cmp_eq_u32 s82, 0\n\t" \
  "s_cbranch_scc1 41f\n\t" \
  "v_cvt_f32_u32 v210, s82\n\t" \
  "v_cvt_f32_u32 v209, s83\n\t" \
  "v_rcp_f32 v210, v210\n\t" \
  "s_mov_b32 s51, s92\n\t" \
  "v_mul_f32 v210, v210, v209\n\t" \
  "v_max_f32 v210, 0x3f028f5c, v210\n\t" \
  "v_min_f32 v210, 0x3ffeb852, v210\n\t" \
  "v_rcp_f32 v209, v210\n\t" \
  "v_mul_f32 v210, 0x4f000000, v210\n\t" \
  "v_mul_f32 v209, 0x47800000, v209\n\t" \
  "v_cvt_u32_f32 v210, v210\n\t" \
  "v_cvt_u32_f32 v209, v209\n\t" \
  "s_mov_b32 s53, s93\n\t" \
  "v_readfirstlane_b32 s46, v210\n\t" \
  "v_readfirstlane_b32 s52, v209\n\t" \
  "41:\n\t" \
  "s_lshl_b32 s50, s53, 8\n\t" \
  "s_add_i32 s89, s57, s53\n\t" \
  "s_mov_b32 s84, 600\n\t" \
  "s_mov_b32 s95, 0x84000\n\t" \
  "13:\n\t" \
  "s_waitcnt vmcnt(0)\n\t" \
  "s_memtime s[86:87]\n\t" \
  "v_cmp_ne_u32_sdwa vcc, v208, s44 src0_sel:BYTE_0 src1_sel:DWORD\n\t" \
  "v_subrev_u32 v208, s50, v208\n\t" \
  "v_ashrrev_i32 v208, 8, v208\n\t" \
  "s_nop 1\n\t" \
  "v_max_i32_dpp v208, v208, v208 quad_perm:[1,0,3,2] row_mask:0xf bank_mask:0xf\n\t" \
  "s_nop 1\n\t" \
  "v_max_i32_dpp v208, v208, v208 quad_perm:[2,3,0,1] row_mask:0xf bank_mask:0xf\n\t" \
  "s_cbranch_vccnz 14f\n\t" \
  "15:\n\t" \
  "s_nop 0\n\t" \
  "v_max_i32_dpp v208, v208, v208 row_half_mirror row_mask:0xf bank_mask:0xf\n\t" \
  "v_add_u32 v209, 255, v208\n\t" \
  "v_med3_i32 v209, v209, 0, v246\n\t" \
  "v_mul_u32_u24 v209, s52, v209\n\t" \
  "v_readlane_b32 s82, v208, 0\n\t" \
  "v_lshrrev_b32 v209, 16, v209\n\t" \
  "s_waitcnt lgkmcnt(0)\n\t" \
  "s_sub_i32 s79, s51, s86\n\t" \
  "v_readlane_b32 s83, v209, 0\n\t" \
  "s_mov_b64 exec, -1\n\t" \
  "s_add_i32 s79, s79, s83\n\t" \
  "s_sub_i32 s85, s84, s79\n\t" \
  "s_max_i32 s85, s85, 0\n\t" \
  "s_add_i32 s79, s79, -670\n\t" \
  "s_mul_hi_u32 s85, s85, s46\n\t" \
  "s_lshl1_add_u32 s85, s85, s82\n\t" \
  "s_add_i32 s85, s85, s89\n\t" \
  "s_lshl_b32 s85, s85, 8\n\t" \
  "s_or_b32 s85, s85, s44\n\t" \
  "v_mov_b32 v209, s85\n\t" \
  "buffer_store_dword v209, v235, %[rh], s81 offen sc1\n\t" \
  "s_add_i32 s85, s53, s82\n\t" \
  "v_mov_b32 v208, s85\n\t" \
  "ds_write_b32 v245, v208\n\t" \
  "3:\n\t" \
  "s_sub_i32 s79, s79, 33\n\t" \
  "s_cmp_gt_i32 s79, 56\n\t" \
  "s_cbranch_scc1 3b\n\t" \
  "buffer_load_dwordx4 v[192:195], v226, %[rh], s49 offen offset:0 sc1\n\t" \
  "s_nop 6\n\t" \
  "buffer_load_dwordx4 v[196:199], v226, %[rh], s49 offen offset:2048 sc1\n\t" \
  "s_nop 6\n\t" \
  "buffer_load_dwordx4 v[200:203], v226, %[rh], s80 offen offset:0 sc1\n\t" \
  "s_nop 6\n\t" \
  "buffer_load_dwordx4 v[204:207], v226, %[rh], s80 offen offset:2048 sc1\n\t" \
  "s_nop 6\n\t" \
  "buffer_load_dwordx4 v[64:67], v225, %[ru], s48 offen offset:0 sc1\n\t" \
  "s_nop 6\n\t" \
  "buffer_load_dwordx4 v[72:75], v225, %[ru], s76 offen offset:0 sc1\n\t" \
  "s_add_i32 s79, s79, 218\n\t" \
  "4:\n\t" \
  "s_sub_i32 s79, s79, 33\n\t" \
  "s_cmp_gt_i32 s79, 56\n\t" \
  "s_cbranch_scc1 4b\n\t" \
  "buffer_load_dwordx4 v[68:71], v225, %[ru], s77 offen offset:0 sc1\n\t" \
  "s_nop 6\n\t" \
  "buffer_load_dwordx4 v[76:79], v225, %[ru], s78 offen offset:0 sc1\n\t" \
  "s_nop 6\n\t" \
  "buffer_load_dwordx4 v[212:215], v228, %[rh], s54 offen offset:0 sc1\n\t" \
  "s_nop 6\n\t" \
  "buffer_load_dwordx4 v[208:211], v227, %[rh], s54 offen offset:0 sc1\n\t" \
  "10:\n\t" \
  "s_waitcnt vmcnt(9)\n\t" \
  "v_smfmac_f32_16x16x64_bf16 v[144:147], v[192:195], v[0:7], v229\n\t" \
  "v_smfmac_f32_16x16x64_bf16 v[148:151], v[192:195], v[8:15], v229\n\t" \
  "s_waitcnt vmcnt(8)\n\t" \
  "v_smfmac_f32_16x16x64_bf16 v[152:155], v[196:199], v[16:23], v229\n\t" \
  "v_cvt_pk_bf16_f32 v176, v144, v145\n\t" \
  "v_cvt_pk_bf16_f32 v177, v146, v147\n\t" \
  "v_smfmac_f32_16x16x64_bf16 v[156:159], v[196:199], v[24:31], v229\n\t" \
  "v_cvt_pk_bf16_f32 v184, v148, v149\n\t" \
  "v_cvt_pk_bf16_f32 v185, v150, v151\n\t" \
  "s_waitcnt vmcnt(7)\n\t" \
  "v_smfmac_f32_16x16x64_bf16 v[160:163], v[200:203], v[32:39], v229\n\t" \
  "v_cvt_pk_bf16_f32 v178, v152, v153\n\t" \
  "v_cvt_pk_bf16_f32 v179, v154, v155\n\t" \
  "v_smfmac_f32_16x16x64_bf16 v[164:167], v[200:203], v[40:47], v229\n\t" \
  "v_cvt_pk_bf16_f32 v186, v156, v157\n\t" \
  "v_cvt_pk_bf16_f32 v187, v158, v159\n\t" \
  "s_waitcnt vmcnt(6)\n\t" \
  "v_smfmac_f32_16x16x64_bf16 v[168:171], v[204:207], v[48:55], v229\n\t" \
  "v_cvt_pk_bf16_f32 v180, v160, v161\n\t" \
  "v_cvt_pk_bf16_f32 v181, v162, v163\n\t" \
  "v_smfmac_f32_16x16x64_bf16 v[172:175], v[204:207], v[56:63], v229\n\t" \
  "v_cvt_pk_bf16_f32 v188, v164, v165\n\t" \
  "v_cvt_pk_bf16_f32 v189, v166, v167\n\t" \
  "s_waitcnt vmcnt(5)\n\t" \
  "s_waitcnt vmcnt(4)\n\t" \
  "v_swap_b32 v65, v72\n\t" \
  "v_swap_b32 v67, v74\n\t" \
  "s_nop 1\n\t" \
  "v_cvt_pk_bf16_f32 v182, v168, v169\n\t" \
  "v_cvt_pk_bf16_f32 v183, v170, v171\n\t" \
  "v_cvt_pk_bf16_f32 v190, v172, v173\n\t" \
  "v_cvt_pk_bf16_f32 v191, v174, v175\n\t" \
  "s_waitcnt vmcnt(1)\n\t" \
  "v_smfmac_f32_16x16x64_bf16 v[216:219], v[212:215], v[176:183], v230\n\t" \
  "v_smfmac_f32_16x16x64_bf16 v[220:223], v[212:215], v[184:191], v230\n\t" \
  "v_swap_b32 v69, v76\n\t" \
  "v_swap_b32 v71, v78\n\t" \
  "s_waitcnt vmcnt(0)\n\t" \
  "s_nop 0\n\t" \
  "v_smfmac_f32_16x16x64_bf16 v[216:219], v[208:211], v[64:71], v229\n\t" \
  "v_smfmac_f32_16x16x64_bf16 v[220:223], v[208:211], v[72:79], v229\n\t" \
  "s_nop 3\n\t" \
  "ds_write2st64_b32 v231, v216, v217 offset0:0 offset1:1\n\t" \
  "ds_write2st64_b32 v231, v218, v219 offset0:2 offset1:3\n\t" \
  "v_cmp_u_f32 vcc, v216, v217\n\t" \
  "v_cmp_u_f32 s[82:83], v218, v219\n\t" \
  "ds_write2st64_b32 v231, v220, v221 offset0:16 offset1:17\n\t" \
  "ds_write2st64_b32 v231, v222, v223 offset0:18 offset1:19\n\t" \
  "v_cmp_u_f32 s[84:85], v220, v222\n\t" \
  "s_or_b64 vcc, vcc, s[82:83]\n\t" \
  "s_or_b64 vcc, vcc, s[84:85]\n\t" \
  "s_cbranch_scc1 8f\n\t" \
  "6:\n\t" \
  "s_waitcnt lgkmcnt(0)\n\t" \
  "s_barrier\n\t" \
  "ds_read_b64 v[136:137], v232 offset:0\n\t" \
  "ds_read_b64 v[138:139], v232 offset:1024\n\t" \
  "ds_read_b64 v[140:141], v232 offset:2048\n\t" \
  "ds_read_b64 v[142:143], v232 offset:3072\n\t" \
  "s_add_i32 s79, s44, 1\n\t" \
  "s_and_b32 s79, s79, 7\n\t" \
  "s_lshl_b32 s79, s79, 15\n\t" \
  "s_waitcnt lgkmcnt(2)\n\t" \
  "v_add_f32 v136, v136, v138\n\t" \
  "v_add_f32 v137, v137, v139\n\t" \
  "s_waitcnt lgkmcnt(0)\n\t" \
  "v_add_f32 v140, v140, v142\n\t" \
  "v_add_f32 v141, v141, v143\n\t" \
  "v_add_f32 v136, v136, v140\n\t" \
  "v_add_f32 v137, v137, v141\n\t" \
  "s_nop 0\n\t" \
  "v_add_f32_dpp v136, v136, v136 quad_perm:[1,0,3,2] row_mask:0xf bank_mask:0xf\n\t" \
  "v_add_f32_dpp v137, v137, v137 quad_perm:[1,0,3,2] row_mask:0xf bank_mask:0xf\n\t" \
  "v_cvt_pk_bf16_f32 v136, v136, v137\n\t" \
  "buffer_store_dword v136, v233, %[rh], s79 offen sc1\n\t" \
  "s_memtime s[56:57]\n\t" \
  "s_cmp_eq_u32 s96, 0x7f000000\n\t" \
  "s_cbranch_scc1 40f\n\t" \
  "s_nop 8\n\t" \
  "39:\n\t" \
  "s_xor_b32 s81, s96, 0x0\n\t" \
  "buffer_load_dwordx4 a[0:3], v224, %[ru], s81 offen offset:0 sc1\n\t" \
  "ds_read_b128 v[208:211], v247\n\t" \
  "v_mov_b64 v[212:213], -1\n\t" \
  "v_mov_b64 v[214:215], -1\n\t" \
  "s_add_i32 s79, s44, -1\n\t" \
  "s_and_b32 s79, s79, 7\n\t" \
  "s_lshl_b32 s79, s79, 15\n\t" \
  "buffer_store_dwordx4 v[212:215], v243, %[rh], s79 offen sc1\n\t" \
  "s_waitcnt lgkmcnt(0)\n\t" \
  "s_sub_i32 s76, s56, s51\n\t" \
  "s_mul_hi_u32 s76, s76, s46\n\t" \
  "s_lshl1_add_u32 s76, s76, s53\n\t" \
  "v_sub_u32 v208, s76, v208\n\t" \
  "v_sub_u32 v209, s76, v209\n\t" \
  "v_sub_u32 v210, s76, v210\n\t" \
  "v_sub_u32 v211, s76, v211\n\t" \
  "v_min3_u32 v208, v208, v209, v210\n\t" \
  "buffer_load_dwordx4 a[4:7], v224, %[ru], s81 offen offset:1024 sc1\n\t" \
  "v_min_u32 v208, v208, v211\n\t" \
  "s_nop 0\n\t" \
  "v_readfirstlane_b32 s76, v208\n\t" \
  "s_bitcmp1_b32 s58, 3\n\t" \
  "s_cselect_b32 s76, s45, s76\n\t" \
  "s_min_u32 s77, s45, s47\n\t" \
  "s_min_u32 s77, s77, s76\n\t" \
  "s_mov_b32 s47, s45\n\t" \
  "s_mov_b32 s45, s76\n\t" \
  "v_mov_b32 v208, s77\n\t" \
  "s_and_b32 s58, s58, 4\n\t" \
  "buffer_store_dword v208, v244, %[rh], s55 offen sc1\n\t" \
  "s_and_b32 s97, s44, 3\n\t" \
  "s_lshl_b32 s97, s97, 17\n\t" \
  "s_add_i32 s97, s97, s43\n\t" \
  "s_add_i32 s79, s44, 4\n\t" \
  "s_and_b32 s79, s79, 3\n\t" \
  "s_add_i32 s82, s56, 260\n\t" \
  "60:\n\t" \
  "s_memtime vcc\n\t" \
  "s_waitcnt lgkmcnt(0)\n\t" \
  "s_sub_i32 s83, s82, vcc_lo\n\t" \
  "s_cmp_gt_i32 s83, 30\n\t" \
  "s_cbranch_scc1 60b\n\t" \
  "s_xor_b32 s81, s96, 0x20000\n\t" \
  "buffer_load_dwordx4 a[8:11], v224, %[ru], s81 offen offset:0 sc1\n\t" \
  "s_lshr_b32 s76, s79, 1\n\t" \
  "s_lshl_b32 s84, s79, 20\n\t" \
  "s_add_i32 s84, s84, s40\n\t" \
  "s_bitcmp1_b32 %[sel], s76\n\t" \
  "s_cselect_b32 s76, 0x2000, 0\n\t" \
  "s_add_i32 s84, s84, s76\n\t" \
  "s_andn2_b32 s84, s84, 0xfff\n\t" \
  "s_cmp_lt_i32 s44, 60\n\t" \
  "s_cselect_b32 s84, s84, 0x7f000000\n\t" \
  "s_add_i32 s79, s44, 2\n\t" \
  "s_and_b32 s48, s79, 3\n\t" \
  "s_lshr_b32 s76, s48, 1\n\t" \
  "s_lshl_b32 s48, s48, 20\n\t" \
  "s_add_i32 s48, s48, s40\n\t" \
  "s_bitcmp1_b32 %[sel], s76\n\t" \
  "s_cselect_b32 s76, 0x2000, 0\n\t" \
  "s_add_i32 s48, s48, s76\n\t" \
  "s_xor_b32 s76, s48, 0x200\n\t" \
  "s_xor_b32 s77, s48, 0x400\n\t" \
  "s_add_i32 s82, s56, 390\n\t" \
  "61:\n\t" \
  "s_memtime vcc\n\t" \
  "s_waitcnt lgkmcnt(0)\n\t" \
  "s_sub_i32 s83, s82, vcc_lo\n\t" \
  "s_cmp_gt_i32 s83, 30\n\t" \
  "s_cbranch_scc1 61b\n\t" \
  "buffer_load_dwordx4 a[12:15], v224, %[ru], s81 offen offset:1024 sc1\n\t" \
  "s_xor_b32 s78, s48, 0x600\n\t" \
  "s_and_b32 s79, s79, 3\n\t" \
  "s_mul_i32 s54, s79, 0xc000\n\t" \
  "s_add_i32 s49, s54, s41\n\t" \
  "s_xor_b32 s80, s49, 0x400\n\t" \
  "s_add_i32 s82, s56, 520\n\t" \
  "62:\n\t" \
  "s_memtime vcc\n\t" \
  "s_waitcnt lgkmcnt(0)\n\t" \
  "s_sub_i32 s83, s82, vcc_lo\n\t" \
  "s_cmp_gt_i32 s83, 30\n\t" \
  "s_cbranch_scc1 62b\n\t" \
  "s_xor_b32 s81, s96, 0x40000\n\t" \
  "buffer_load_dwordx4 a[16:19], v224, %[ru], s81 offen offset:0 sc1\n\t" \
  "s_add_i32 s82, s56, 650\n\t" \
  "63:\n\t" \
  "s_memtime vcc\n\t" \
  "s_waitcnt lgkmcnt(0)\n\t" \
  "s_sub_i32 s83, s82, vcc_lo\n\t" \
  "s_cmp_gt_i32 s83, 30\n\t" \
  "s_cbranch_scc1 63b\n\t" \
  "buffer_load_dwordx4 a[20:23], v224, %[ru], s81 offen offset:1024 sc1\n\t" \
  "s_add_i32 s82, s56, 780\n\t" \
  "64:\n\t" \
  "s_memtime vcc\n\t" \
  "s_waitcnt lgkmcnt(0)\n\t" \
  "s_sub_i32 s83, s82, vcc_lo\n\t" \
  "s_cmp_gt_i32 s83, 30\n\t" \
  "s_cbranch_scc1 64b\n\t" \
  "s_xor_b32 s81, s96, 0x60000\n\t" \
  "buffer_load_dwordx4 a[24:27], v224, %[ru], s81 offen offset:0 sc1\n\t" \
  "s_add_i32 s82, s56, 910\n\t" \
  "65:\n\t" \
  "s_memtime vcc\n\t" \
  "s_waitcnt lgkmcnt(0)\n\t" \
  "s_sub_i32 s83, s82, vcc_lo\n\t" \
  "s_cmp_gt_i32 s83, 30\n\t" \
  "s_cbranch_scc1 65b\n\t" \
  "buffer_load_dwordx4 a[28:31], v224, %[ru], s81 offen offset:1024 sc1\n\t" \
  "buffer_store_dwordx4 v[212:215], v224, %[ru], s84 offen offset:0 sc1\n\t" \
  "s_add_i32 s82, s56, 1040\n\t" \
  "66:\n\t" \
  "s_memtime vcc\n\t" \
  "s_waitcnt lgkmcnt(0)\n\t" \
  "s_sub_i32 s83, s82, vcc_lo\n\t" \
  "s_cmp_gt_i32 s83, 30\n\t" \
  "s_cbranch_scc1 66b\n\t" \
  "s_xor_b32 s81, s96, 0x80000\n\t" \
  "buffer_load_dwordx4 a[32:35], v224, %[ru], s81 offen offset:0 sc1\n\t" \
  "s_add_i32 s82, s56, 1170\n\t" \
  "67:\n\t" \
  "s_memtime vcc\n\t" \
  "s_waitcnt lgkmcnt(0)\n\t" \
  "s_sub_i32 s83, s82, vcc_lo\n\t" \
  "s_cmp_gt_i32 s83, 30\n\t" \
  "s_cbranch_scc1 67b\n\t" \
  "buffer_load_dwordx4 a[36:39], v224, %[ru], s81 offen offset:1024 sc1\n\t" \
  "buffer_store_dwordx4 v[212:215], v224, %[ru], s84 offen offset:1024 sc1\n\t" \
  "s_add_i32 s82, s56, 1300\n\t" \
  "68:\n\t" \
  "s_memtime vcc\n\t" \
  "s_waitcnt lgkmcnt(0)\n\t" \
  "s_sub_i32 s83, s82, vcc_lo\n\t" \
  "s_cmp_gt_i32 s83, 30\n\t" \
  "s_cbranch_scc1 68b\n\t" \
  "s_xor_b32 s81, s96, 0xa0000\n\t" \
  "buffer_load_dwordx4 a[40:43], v224, %[ru], s81 offen offset:0 sc1\n\t" \
  "s_add_i32 s82, s56, 1430\n\t" \
  "69:\n\t" \
  "s_memtime vcc\n\t" \
  "s_waitcnt lgkmcnt(0)\n\t" \
  "s_sub_i32 s83, s82, vcc_lo\n\t" \
  "s_cmp_gt_i32 s83, 30\n\t" \
  "s_cbranch_scc1 69b\n\t" \
  "buffer_load_dwordx4 a[44:47], v224, %[ru], s81 offen offset:1024 sc1\n\t" \
  "buffer_store_dwordx4 v[212:215], v224, %[ru], s84 offen offset:2048 sc1\n\t" \
  "s_add_i32 s82, s56, 1560\n\t" \
  "70:\n\t" \
  "s_memtime vcc\n\t" \
  "s_waitcnt lgkmcnt(0)\n\t" \
  "s_sub_i32 s83, s82, vcc_lo\n\t" \
  "s_cmp_gt_i32 s83, 30\n\t" \
  "s_cbranch_scc1 70b\n\t" \
  "s_xor_b32 s81, s96, 0xc0000\n\t" \
  "buffer_load_dwordx4 a[48:51], v224, %[ru], s81 offen offset:0 sc1\n\t" \
  "s_add_i32 s82, s56, 1690\n\t" \
  "71:\n\t" \
  "s_memtime vcc\n\t" \
  "s_waitcnt lgkmcnt(0)\n\t" \
  "s_sub_i32 s83, s82, vcc_lo\n\t" \
  "s_cmp_gt_i32 s83, 30\n\t" \
  "s_cbranch_scc1 71b\n\t" \
  "buffer_load_dwordx4 a[52:55], v224, %[ru], s81 offen offset:1024 sc1\n\t" \
  "buffer_store_dwordx4 v[212:215], v224, %[ru], s84 offen offset:3072 sc1\n\t" \
  "s_add_i32 s82, s56, 1820\n\t" \
  "72:\n\t" \
  "s_memtime vcc\n\t" \
  "s_waitcnt lgkmcnt(0)\n\t" \
  "s_sub_i32 s83, s82, vcc_lo\n\t" \
  "s_cmp_gt_i32 s83, 30\n\t" \
  "s_cbranch_scc1 72b\n\t" \
  "s_xor_b32 s81, s96, 0xe0000\n\t" \
  "buffer_load_dwordx4 a[56:59], v224, %[ru], s81 offen offset:0 sc1\n\t" \
  "s_add_i32 s82, s56, 1950\n\t" \
  "73:\n\t" \
  "s_memtime vcc\n\t" \
  "s_waitcnt lgkmcnt(0)\n\t" \
  "s_sub_i32 s83, s82, vcc_lo\n\t" \
  "s_cmp_gt_i32 s83, 30\n\t" \
  "s_cbranch_scc1 73b\n\t" \
  "buffer_load_dwordx4 a[60:63], v224, %[ru], s81 offen offset:1024 sc1\n\t" \
  "s_cmp_lt_i32 s44, 0\n\t" \
  "s_cbranch_scc1 51f\n\t" \
  "s_add_i32 s82, s56, 2700\n\t" \
  "74:\n\t" \
  "s_memtime vcc\n\t" \
  "s_waitcnt lgkmcnt(0)\n\t" \
  "s_sub_i32 s83, s82, vcc_lo\n\t" \
  "s_cmp_gt_i32 s83, 30\n\t" \
  "s_cbranch_scc1 74b\n\t" \
  "45:\n\t" \
  "s_mov_b64 exec, 0xff\n\t" \
  "buffer_load_dword v211, v234, %[rh], s95 offen sc1\n\t" \
  "s_waitcnt vmcnt(0)\n\t" \
  "v_cmp_gt_i32 vcc, s44, v211\n\t" \
  "s_mov_b64 exec, -1\n\t" \
  "s_cbranch_vccnz 49f\n\t" \
  "46:\n\t" \
  "s_xor_b32 s81, s97, 0x0\n\t" \
  "buffer_load_dwordx4 v[80:83], v224, %[rh], s81 offen offset:0 sc1\n\t" \
  "s_xor_b32 s81, s97, 0x800\n\t" \
  "buffer_load_dwordx4 v[84:87], v224, %[rh], s81 offen offset:0 sc1\n\t" \
  "s_xor_b32 s81, s97, 0x0\n\t" \
  "buffer_load_dwordx4 v[88:91], v224, %[rh], s81 offen offset:1024 sc1\n\t" \
  "s_xor_b32 s81, s97, 0x800\n\t" \
  "buffer_load_dwordx4 v[92:95], v224, %[rh], s81 offen offset:1024 sc1\n\t" \
  "s_xor_b32 s81, s97, 0x4000\n\t" \
  "buffer_load_dwordx4 v[96:99], v224, %[rh], s81 offen offset:0 sc1\n\t" \
  "s_xor_b32 s81, s97, 0x4800\n\t" \
  "buffer_load_dwordx4 v[100:103], v224, %[rh], s81 offen offset:0 sc1\n\t" \
  "s_xor_b32 s81, s97, 0x4000\n\t" \
  "buffer_load_dwordx4 v[104:107], v224, %[rh], s81 offen offset:1024 sc1\n\t" \
  "s_xor_b32 s81, s97, 0x4800\n\t" \
  "buffer_load_dwordx4 v[108:111], v224, %[rh], s81 offen offset:1024 sc1\n\t" \
  "s_waitcnt vmcnt(6)\n\t" \
  "v_swap_b32 v81, v84\n\t" \
  "v_swap_b32 v83, v86\n\t" \
  "s_nop 1\n\t" \
  "v_mfma_f32_16x16x32_bf16 v[192:195], v[0:3], v[80:83], 0\n\t" \
  "v_mfma_f32_16x16x32_bf16 v[200:203], v[8:11], v[80:83], 0\n\t" \
  "s_xor_b32 s81, s97, 0x1000\n\t" \
  "buffer_load_dwordx4 v[112:115], v224, %[rh], s81 offen offset:0 sc1\n\t" \
  "v_mfma_f32_16x16x32_bf16 v[196:199], v[0:3], v[84:87], 0\n\t" \
  "v_mfma_f32_16x16x32_bf16 v[204:207], v[8:11], v[84:87], 0\n\t" \
  "s_xor_b32 s81, s97, 0x1800\n\t" \
  "buffer_load_dwordx4 v[116:119], v224, %[rh], s81 offen offset:0 sc1\n\t" \
  "s_waitcnt vmcnt(6)\n\t" \
  "v_swap_b32 v89, v92\n\t" \
  "v_swap_b32 v91, v94\n\t" \
  "s_nop 1\n\t" \
  "v_mfma_f32_16x16x32_bf16 v[196:199], v[4:7], v[88:91], v[196:199]\n\t" \
  "v_mfma_f32_16x16x32_bf16 v[204:207], v[12:15], v[88:91], v[204:207]\n\t" \
  "s_xor_b32 s81, s97, 0x1000\n\t" \
  "buffer_load_dwordx4 v[120:123], v224, %[rh], s81 offen offset:1024 sc1\n\t" \
  "s_xor_b32 s81, s97, 0x1800\n\t" \
  "buffer_load_dwordx4 v[124:127], v224, %[rh], s81 offen offset:1024 sc1\n\t" \
  "v_accvgpr_read_b32 v0, a0\n\t" \
  "v_accvgpr_read_b32 v1, a8\n\t" \
  "v_accvgpr_read_b32 v2, a2\n\t" \
  "v_accvgpr_read_b32 v3, a10\n\t" \
  "v_accvgpr_read_b32 v4, a4\n\t" \
  "v_accvgpr_read_b32 v5, a12\n\t" \
  "v_accvgpr_read_b32 v6, a6\n\t" \
  "v_accvgpr_read_b32 v7, a14\n\t" \
  "v_accvgpr_read_b32 v8, a1\n\t" \
  "v_accvgpr_read_b32 v9, a9\n\t" \
  "v_accvgpr_read_b32 v10, a3\n\t" \
  "v_accvgpr_read_b32 v11, a11\n\t" \
  "v_accvgpr_read_b32 v12, a5\n\t" \
  "v_accvgpr_read_b32 v13, a13\n\t" \
  "v_accvgpr_read_b32 v14, a7\n\t" \
  "v_accvgpr_read_b32 v15, a15\n\t" \
  "s_waitcnt vmcnt(6)\n\t" \
  "v_swap_b32 v97, v100\n\t" \
  "v_swap_b32 v99, v102\n\t" \
  "s_nop 1\n\t" \
  "v_mfma_f32_16x16x32_bf16 v[192:195], v[16:19], v[96:99], v[192:195]\n\t" \
  "v_mfma_f32_16x16x32_bf16 v[200:203], v[24:27], v[96:99], v[200:203]\n\t" \
  "s_xor_b32 s81, s97, 0x5000\n\t" \
  "buffer_load_dwordx4 v[128:131], v224, %[rh], s81 offen offset:0 sc1\n\t" \
  "v_mfma_f32_16x16x32_bf16 v[196:199], v[16:19], v[100:103], v[196:199]\n\t" \
  "v_mfma_f32_16x16x32_bf16 v[204:207], v[24:27], v[100:103], v[204:207]\n\t" \
  "s_xor_b32 s81, s97, 0x5800\n\t" \
  "buffer_load_dwordx4 v[132:135], v224, %[rh], s81 offen offset:0 sc1\n\t" \
  "s_waitcnt vmcnt(6)\n\t" \
  "v_swap_b32 v105, v108\n\t" \
  "v_swap_b32 v107, v110\n\t" \
  "s_nop 1\n\t" \
  "v_mfma_f32_16x16x32_bf16 v[196:199], v[20:23], v[104:107], v[196:199]\n\t" \
  "v_mfma_f32_16x16x32_bf16 v[204:207], v[28:31], v[104:107], v[204:207]\n\t" \
  "s_xor_b32 s81, s97, 0x5000\n\t" \
  "buffer_load_dwordx4 v[136:139], v224, %[rh], s81 offen offset:1024 sc1\n\t" \
  "s_xor_b32 s81, s97, 0x5800\n\t" \
  "buffer_load_dwordx4 v[140:143], v224, %[rh], s81 offen offset:1024 sc1\n\t" \
  "s_add_i32 s79, s44, 1\n\t" \
  "s_and_b32 s79, s79, 3\n\t" \
  "s_lshl_b32 s79, s79, 14\n\t" \
  "s_add_i32 s94, s79, 0x71000\n\t" \
  "s_mov_b32 exec_hi, 0\n\t" \
  "buffer_load_dword v209, v237, %[rh], s55 offen offset:0 sc1\n\t" \
  "s_mov_b64 exec, 0xff\n\t" \
  "buffer_load_dword v210, v242, %[ru], 0 offen offset:0 sc1\n\t" \
  "buffer_load_dword v208, v236, %[rh], s94 offen offset:0 sc1\n\t" \
  "s_mov_b64 exec, -1\n\t" \
  "v_accvgpr_read_b32 v16, a32\n\t" \
  "v_accvgpr_read_b32 v17, a40\n\t" \
  "v_accvgpr_read_b32 v18, a34\n\t" \
  "v_accvgpr_read_b32 v19, a42\n\t" \
  "v_accvgpr_read_b32 v20, a36\n\t" \
  "v_accvgpr_read_b32 v21, a44\n\t" \
  "v_accvgpr_read_b32 v22, a38\n\t" \
  "v_accvgpr_read_b32 v23, a46\n\t" \
  "v_accvgpr_read_b32 v24, a33\n\t" \
  "v_accvgpr_read_b32 v25, a41\n\t" \
  "v_accvgpr_read_b32 v26, a35\n\t" \
  "v_accvgpr_read_b32 v27, a43\n\t" \
  "v_accvgpr_read_b32 v28, a37\n\t" \
  "v_accvgpr_read_b32 v29, a45\n\t" \
  "v_accvgpr_read_b32 v30, a39\n\t" \
  "v_accvgpr_read_b32 v31, a47\n\t" \
  "v_permlane32_swap_b32 v92, v108\n\t" \
  "v_permlane32_swap_b32 v93, v109\n\t" \
  "v_permlane32_swap_b32 v94, v110\n\t" \
  "v_permlane32_swap_b32 v95, v111\n\t" \
  "s_nop 1\n\t" \
  "v_mfma_f32_16x16x32_bf16 v[192:195], v[64:67], v[92:95], v[192:195]\n\t" \
  "v_mfma_f32_16x16x32_bf16 v[200:203], v[72:75], v[92:95], v[200:203]\n\t" \
  "v_mfma_f32_16x16x32_bf16 v[196:199], v[64:67], v[108:111], v[196:199]\n\t" \
  "v_mfma_f32_16x16x32_bf16 v[204:207], v[72:75], v[108:111], v[204:207]\n\t" \
  "s_waitcnt vmcnt(9)\n\t" \
  "v_swap_b32 v113, v116\n\t" \
  "v_swap_b32 v115, v118\n\t" \
  "s_nop 1\n\t" \
  "v_mfma_f32_16x16x32_bf16 v[192:195], v[32:35], v[112:115], v[192:195]\n\t" \
  "v_mfma_f32_16x16x32_bf16 v[200:203], v[40:43], v[112:115], v[200:203]\n\t" \
  "v_mfma_f32_16x16x32_bf16 v[196:199], v[32:35], v[116:119], v[196:199]\n\t" \
  "v_mfma_f32_16x16x32_bf16 v[204:207], v[40:43], v[116:119], v[204:207]\n\t" \
  "s_waitcnt vmcnt(7)\n\t" \
  "v_swap_b32 v121, v124\n\t" \
  "v_swap_b32 v123, v126\n\t" \
  "s_nop 1\n\t" \
  "v_mfma_f32_16x16x32_bf16 v[196:199], v[36:39], v[120:123], v[196:199]\n\t" \
  "v_mfma_f32_16x16x32_bf16 v[204:207], v[44:47], v[120:123], v[204:207]\n\t" \
  "v_accvgpr_read_b32 v32, a16\n\t" \
  "v_accvgpr_read_b32 v33, a24\n\t" \
  "v_accvgpr_read_b32 v34, a18\n\t" \
  "v_accvgpr_read_b32 v35, a26\n\t" \
  "v_accvgpr_read_b32 v36, a20\n\t" \
  "v_accvgpr_read_b32 v37, a28\n\t" \
  "v_accvgpr_read_b32 v38, a22\n\t" \
  "v_accvgpr_read_b32 v39, a30\n\t" \
  "v_accvgpr_read_b32 v40, a17\n\t" \
  "v_accvgpr_read_b32 v41, a25\n\t" \
  "v_accvgpr_read_b32 v42, a19\n\t" \
  "v_accvgpr_read_b32 v43, a27\n\t" \
  "v_accvgpr_read_b32 v44, a21\n\t" \
  "v_accvgpr_read_b32 v45, a29\n\t" \
  "v_accvgpr_read_b32 v46, a23\n\t" \
  "v_accvgpr_read_b32 v47, a31\n\t" \
  "s_waitcnt vmcnt(5)\n\t" \
  "v_swap_b32 v129, v132\n\t" \
  "v_swap_b32 v131, v134\n\t" \
  "s_nop 1\n\t" \
  "v_mfma_f32_16x16x32_bf16 v[192:195], v[48:51], v[128:131], v[192:195]\n\t" \
  "v_mfma_f32_16x16x32_bf16 v[200:203], v[56:59], v[128:131], v[200:203]\n\t" \
  "v_mfma_f32_16x16x32_bf16 v[196:199], v[48:51], v[132:135], v[196:199]\n\t" \
  "v_mfma_f32_16x16x32_bf16 v[204:207], v[56:59], v[132:135], v[204:207]\n\t" \
  "s_waitcnt vmcnt(3)\n\t" \
  "v_swap_b32 v137, v140\n\t" \
  "v_swap_b32 v139, v142\n\t" \
  "s_nop 1\n\t" \
  "v_mfma_f32_16x16x32_bf16 v[196:199], v[52:55], v[136:139], v[196:199]\n\t" \
  "v_mfma_f32_16x16x32_bf16 v[204:207], v[60:63], v[136:139], v[204:207]\n\t" \
  "v_permlane32_swap_b32 v124, v140\n\t" \
  "v_permlane32_swap_b32 v125, v141\n\t" \
  "v_permlane32_swap_b32 v126, v142\n\t" \
  "v_permlane32_swap_b32 v127, v143\n\t" \
  "s_nop 1\n\t" \
  "v_mfma_f32_16x16x32_bf16 v[192:195], v[68:71], v[124:127], v[192:195]\n\t" \
  "v_mfma_f32_16x16x32_bf16 v[200:203], v[76:79], v[124:127], v[200:203]\n\t" \
  "v_mfma_f32_16x16x32_bf16 v[196:199], v[68:71], v[140:143], v[196:199]\n\t" \
  "v_mfma_f32_16x16x32_bf16 v[204:207], v[76:79], v[140:143], v[204:207]\n\t" \
  "v_mov_b64 v[144:145], 0\n\t" \
  "v_mov_b64 v[146:147], 0\n\t" \
  "v_mov_b64 v[148:149], 0\n\t" \
  "v_mov_b64 v[150:151], 0\n\t" \
  "v_mov_b64 v[152:153], 0\n\t" \
  "v_mov_b64 v[154:155], 0\n\t" \
  "v_mov_b64 v[156:157], 0\n\t" \
  "v_mov_b64 v[158:159], 0\n\t" \
  "ds_write_b128 v239, v[192:195] offset:0\n\t" \
  "ds_write_b128 v239, v[200:203] offset:2048\n\t" \
  "ds_write_b128 v239, v[196:199] offset:1024\n\t" \
  "ds_write_b128 v239, v[204:207] offset:3072\n\t" \
  "s_lshl_b32 s79, s44, 17\n\t" \
  "s_waitcnt lgkmcnt(0)\n\t" \
  "s_barrier\n\t" \
  "ds_read_b128 v[80:83], v240 offset:0\n\t" \
  "ds_read_b128 v[84:87], v240 offset:4096\n\t" \
  "ds_read_b128 v[88:91], v240 offset:8192\n\t" \
  "ds_read_b128 v[92:95], v240 offset:12288\n\t" \
  "v_mov_b64 v[160:161], 0\n\t" \
  "v_mov_b64 v[162:163], 0\n\t" \
  "v_mov_b64 v[164:165], 0\n\t" \
  "v_mov_b64 v[166:167], 0\n\t" \
  "v_mov_b64 v[168:169], 0\n\t" \
  "v_mov_b64 v[170:171], 0\n\t" \
  "v_mov_b64 v[172:173], 0\n\t" \
  "v_mov_b64 v[174:175], 0\n\t" \
  "v_mov_b64 v[216:217], 0\n\t" \
  "v_mov_b64 v[218:219], 0\n\t" \
  "v_mov_b64 v[220:221], 0\n\t" \
  "v_mov_b64 v[222:223], 0\n\t" \
  "s_waitcnt lgkmcnt(2)\n\t" \
  "v_add_f32 v80, v80, v84\n\t" \
  "v_add_f32 v81, v81, v85\n\t" \
  "v_add_f32 v82, v82, v86\n\t" \
  "v_add_f32 v83, v83, v87\n\t" \
  "s_waitcnt lgkmcnt(0)\n\t" \
  "v_add_f32 v88, v88, v92\n\t" \
  "v_add_f32 v89, v89, v93\n\t" \
  "v_add_f32 v90, v90, v94\n\t" \
  "v_add_f32 v91, v91, v95\n\t" \
  "v_add_f32 v80, v80, v88\n\t" \
  "v_add_f32 v81, v81, v89\n\t" \
  "v_add_f32 v82, v82, v90\n\t" \
  "v_add_f32 v83, v83, v91\n\t" \
  "v_cvt_pk_bf16_f32 v80, v80, v81\n\t" \
  "v_cvt_pk_bf16_f32 v81, v82, v83\n\t" \
  "s_waitcnt vmcnt(0)\n\t" \
  "buffer_store_dwordx2 v[80:81], v241, %[ro], s79 offen nt\n\t" \
  "s_cmp_gt_i32 s44, 61\n\t" \
  "s_cbranch_scc1 99f\n\t" \
  "52:\n\t" \
  "v_accvgpr_read_b32 v48, a48\n\t" \
  "v_accvgpr_read_b32 v49, a56\n\t" \
  "v_accvgpr_read_b32 v50, a50\n\t" \
  "v_accvgpr_read_b32 v51, a58\n\t" \
  "v_accvgpr_read_b32 v52, a52\n\t" \
  "v_accvgpr_read_b32 v53, a60\n\t" \
  "v_accvgpr_read_b32 v54, a54\n\t" \
  "v_accvgpr_read_b32 v55, a62\n\t" \
  "v_accvgpr_read_b32 v56, a49\n\t" \
  "v_accvgpr_read_b32 v57, a57\n\t" \
  "v_accvgpr_read_b32 v58, a51\n\t" \
  "v_accvgpr_read_b32 v59, a59\n\t" \
  "v_accvgpr_read_b32 v60, a53\n\t" \
  "v_accvgpr_read_b32 v61, a61\n\t" \
  "v_accvgpr_read_b32 v62, a55\n\t" \
  "v_accvgpr_read_b32 v63, a63\n\t" \
  "s_add_i32 s44, s44, 2\n\t" \
  "s_branch 20b\n\t" \
  "22:\n\t" \
  "s_bitcmp1_b32 s58, 2\n\t" \
  "s_cbranch_scc1 24b\n\t" \
  "s_sleep 2\n\t" \
  "buffer_load_dword v208, v236, %[rh], s94 offen sc1\n\t" \
  "s_waitcnt vmcnt(0)\n\t" \
  "s_add_u32 s59, s59, 1\n\t" \
  "s_bitcmp0_b32 s59, 20\n\t" \
  "s_cbranch_scc1 21b\n\t" \
  "s_or_b32 s58, s58, 4\n\t" \
  "s_branch 21b\n\t" \
  "14:\n\t" \
  "s_bitcmp1_b32 s58, 2\n\t" \
  "s_cbranch_scc1 15b\n\t" \
  "s_mov_b32 s84, 900\n\t" \
  "buffer_load_dword v208, v234, %[rh], s81 offen sc1\n\t" \
  "s_add_u32 s59, s59, 1\n\t" \
  "s_bitcmp0_b32 s59, 20\n\t" \
  "s_cbranch_scc1 13b\n\t" \
  "s_or_b32 s58, s58, 4\n\t" \
  "s_branch 13b\n\t" \
  "8:\n\t" \
  "s_mov_b32 s79, 0\n\t" \
  "17:\n\t" \
  "s_bitcmp1_b32 s58, 2\n\t" \
  "s_cbranch_scc1 18f\n\t" \
  "v_max_u32 v136, v64, v65\n\t" \
  "v_max3_u32 v136, v136, v66, v67\n\t" \
  "v_max3_u32 v136, v136, v68, v69\n\t" \
  "v_max3_u32 v136, v136, v70, v71\n\t" \
  "v_max3_u32 v136, v136, v72, v73\n\t" \
  "v_max3_u32 v136, v136, v74, v75\n\t" \
  "v_max3_u32 v136, v136, v76, v77\n\t" \
  "v_max3_u32 v136, v136, v78, v79\n\t" \
  "v_max3_u32 v136, v136, v192, v193\n\t" \
  "v_max3_u32 v136, v136, v194, v195\n\t" \
  "v_max3_u32 v136, v136, v196, v197\n\t" \
  "v_max3_u32 v136, v136, v198, v199\n\t" \
  "v_max3_u32 v136, v136, v200, v201\n\t" \
  "v_max3_u32 v136, v136, v202, v203\n\t" \
  "v_max3_u32 v136, v136, v204, v205\n\t" \
  "v_max3_u32 v136, v136, v206, v207\n\t" \
  "v_max3_u32 v136, v136, v208, v209\n\t" \
  "v_max3_u32 v136, v136, v210, v211\n\t" \
  "v_max3_u32 v136, v136, v212, v213\n\t" \
  "v_max3_u32 v136, v136, v214, v215\n\t" \
  "v_cmp_eq_u32 vcc, -1, v136\n\t" \
  "s_cbranch_vccz 18f\n\t" \
  "s_mov_b32 s79, 1\n\t" \
  "buffer_load_dwordx4 v[192:195], v226, %[rh], s49 offen offset:0 sc1\n\t" \
  "buffer_load_dwordx4 v[64:67], v225, %[ru], s48 offen offset:0 sc1\n\t" \
  "buffer_load_dwordx4 v[196:199], v226, %[rh], s49 offen offset:2048 sc1\n\t" \
  "buffer_load_dwordx4 v[72:75], v225, %[ru], s76 offen offset:0 sc1\n\t" \
  "buffer_load_dwordx4 v[200:203], v226, %[rh], s80 offen offset:0 sc1\n\t" \
  "buffer_load_dwordx4 v[68:71], v225, %[ru], s77 offen offset:0 sc1\n\t" \
  "buffer_load_dwordx4 v[204:207], v226, %[rh], s80 offen offset:2048 sc1\n\t" \
  "buffer_load_dwordx4 v[76:79], v225, %[ru], s78 offen offset:0 sc1\n\t" \
  "buffer_load_dwordx4 v[208:211], v227, %[rh], s54 offen offset:0 sc1\n\t" \
  "buffer_load_dwordx4 v[212:215], v228, %[rh], s54 offen offset:0 sc1\n\t" \
  "s_waitcnt vmcnt(0)\n\t" \
  "s_add_u32 s59, s59, 1\n\t" \
  "s_bitcmp0_b32 s59, 20\n\t" \
  "s_cbranch_scc1 17b\n\t" \
  "s_or_b32 s58, s58, 4\n\t" \
  "s_branch 17b\n\t" \
  "18:\n\t" \
  "s_cmp_eq_u32 s79, 0\n\t" \
  "s_cbranch_scc1 6b\n\t" \
  "v_mov_b64 v[144:145], 0\n\t" \
  "v_mov_b64 v[146:147], 0\n\t" \
  "v_mov_b64 v[148:149], 0\n\t" \
  "v_mov_b64 v[150:151], 0\n\t" \
  "v_mov_b64 v[152:153], 0\n\t" \
  "v_mov_b64 v[154:155], 0\n\t" \
  "v_mov_b64 v[156:157], 0\n\t" \
  "v_mov_b64 v[158:159], 0\n\t" \
  "v_mov_b64 v[160:161], 0\n\t" \
  "v_mov_b64 v[162:163], 0\n\t" \
  "v_mov_b64 v[164:165], 0\n\t" \
  "v_mov_b64 v[166:167], 0\n\t" \
  "v_mov_b64 v[168:169], 0\n\t" \
  "v_mov_b64 v[170:171], 0\n\t" \
  "v_mov_b64 v[172:173], 0\n\t" \
  "v_mov_b64 v[174:175], 0\n\t" \
  "v_mov_b64 v[216:217], 0\n\t" \
  "v_mov_b64 v[218:219], 0\n\t" \
  "v_mov_b64 v[220:221], 0\n\t" \
  "v_mov_b64 v[222:223], 0\n\t" \
  "s_branch 10b\n\t" \
  "40:\n\t" \
  "s_add_i32 s79, s44, 2\n\t" \
  "s_cmp_lt_i32 s79, 64\n\t" \
  "s_cbranch_scc0 44f\n\t" \
  "42:\n\t" \
  "s_mov_b64 exec, 0xff\n\t" \
  "buffer_load_dword v210, v242, %[ru], 0 offen sc1\n\t" \
  "s_waitcnt vmcnt(0)\n\t" \
  "v_cmp_gt_i32 vcc, s79, v210\n\t" \
  "s_mov_b64 exec, -1\n\t" \
  "s_cbranch_vccz 43f\n\t" \
  "s_sleep 3\n\t" \
  "s_bitcmp1_b32 s58, 2\n\t" \
  "s_cbranch_scc1 43f\n\t" \
  "s_add_u32 s59, s59, 1\n\t" \
  "s_bitcmp0_b32 s59, 20\n\t" \
  "s_cbranch_scc1 42b\n\t" \
  "s_or_b32 s58, s58, 4\n\t" \
  "s_branch 43f\n\t" \
  "43:\n\t" \
  "s_and_b32 s96, s79, 15\n\t" \
  "s_lshl_b32 s96, s96, 22\n\t" \
  "s_add_i32 s96, s96, s42\n\t" \
  "s_lshr_b32 s82, s96, 21\n\t" \
  "s_bitcmp0_b32 %[sel], s82\n\t" \
  "s_cselect_b32 s82, 0x2000, 0\n\t" \
  "s_add_i32 s96, s96, s82\n\t" \
  "s_branch 39b\n\t" \
  "51:\n\t" \
  "s_add_i32 s79, s44, 1\n\t" \
  "s_and_b32 s79, s79, 3\n\t" \
  "s_lshl_b32 s79, s79, 14\n\t" \
  "s_add_i32 s94, s79, 0x71000\n\t" \
  "s_mov_b32 exec_hi, 0\n\t" \
  "buffer_load_dword v209, v237, %[rh], s55 offen offset:0 sc1\n\t" \
  "s_mov_b64 exec, 0xff\n\t" \
  "buffer_load_dword v210, v242, %[ru], 0 offen offset:0 sc1\n\t" \
  "buffer_load_dword v208, v236, %[rh], s94 offen offset:0 sc1\n\t" \
  "s_mov_b64 exec, -1\n\t" \
  "v_mov_b64 v[144:145], 0\n\t" \
  "v_mov_b64 v[146:147], 0\n\t" \
  "v_mov_b64 v[148:149], 0\n\t" \
  "v_mov_b64 v[150:151], 0\n\t" \
  "v_mov_b64 v[152:153], 0\n\t" \
  "v_mov_b64 v[154:155], 0\n\t" \
  "v_mov_b64 v[156:157], 0\n\t" \
  "v_mov_b64 v[158:159], 0\n\t" \
  "v_mov_b64 v[160:161], 0\n\t" \
  "v_mov_b64 v[162:163], 0\n\t" \
  "v_mov_b64 v[164:165], 0\n\t" \
  "v_mov_b64 v[166:167], 0\n\t" \
  "v_mov_b64 v[168:169], 0\n\t" \
  "v_mov_b64 v[170:171], 0\n\t" \
  "v_mov_b64 v[172:173], 0\n\t" \
  "v_mov_b64 v[174:175], 0\n\t" \
  "v_mov_b64 v[216:217], 0\n\t" \
  "v_mov_b64 v[218:219], 0\n\t" \
  "v_mov_b64 v[220:221], 0\n\t" \
  "v_mov_b64 v[222:223], 0\n\t" \
  "s_waitcnt vmcnt(0)\n\t" \
  "v_accvgpr_read_b32 v0, a0\n\t" \
  "v_accvgpr_read_b32 v1, a8\n\t" \
  "v_accvgpr_read_b32 v2, a2\n\t" \
  "v_accvgpr_read_b32 v3, a10\n\t" \
  "v_accvgpr_read_b32 v4, a4\n\t" \
  "v_accvgpr_read_b32 v5, a12\n\t" \
  "v_accvgpr_read_b32 v6, a6\n\t" \
  "v_accvgpr_read_b32 v7, a14\n\t" \
  "v_accvgpr_read_b32 v8, a1\n\t" \
  "v_accvgpr_read_b32 v9, a9\n\t" \
  "v_accvgpr_read_b32 v10, a3\n\t" \
  "v_accvgpr_read_b32 v11, a11\n\t" \
  "v_accvgpr_read_b32 v12, a5\n\t" \
  "v_accvgpr_read_b32 v13, a13\n\t" \
  "v_accvgpr_read_b32 v14, a7\n\t" \
  "v_accvgpr_read_b32 v15, a15\n\t" \
  "v_accvgpr_read_b32 v16, a32\n\t" \
  "v_accvgpr_read_b32 v17, a40\n\t" \
  "v_accvgpr_read_b32 v18, a34\n\t" \
  "v_accvgpr_read_b32 v19, a42\n\t" \
  "v_accvgpr_read_b32 v20, a36\n\t" \
  "v_accvgpr_read_b32 v21, a44\n\t" \
  "v_accvgpr_read_b32 v22, a38\n\t" \
  "v_accvgpr_read_b32 v23, a46\n\t" \
  "v_accvgpr_read_b32 v24, a33\n\t" \
  "v_accvgpr_read_b32 v25, a41\n\t" \
  "v_accvgpr_read_b32 v26, a35\n\t" \
  "v_accvgpr_read_b32 v27, a43\n\t" \
  "v_accvgpr_read_b32 v28, a37\n\t" \
  "v_accvgpr_read_b32 v29, a45\n\t" \
  "v_accvgpr_read_b32 v30, a39\n\t" \
  "v_accvgpr_read_b32 v31, a47\n\t" \
  "v_accvgpr_read_b32 v32, a16\n\t" \
  "v_accvgpr_read_b32 v33, a24\n\t" \
  "v_accvgpr_read_b32 v34, a18\n\t" \
  "v_accvgpr_read_b32 v35, a26\n\t" \
  "v_accvgpr_read_b32 v36, a20\n\t" \
  "v_accvgpr_read_b32 v37, a28\n\t" \
  "v_accvgpr_read_b32 v38, a22\n\t" \
  "v_accvgpr_read_b32 v39, a30\n\t" \
  "v_accvgpr_read_b32 v40, a17\n\t" \
  "v_accvgpr_read_b32 v41, a25\n\t" \
  "v_accvgpr_read_b32 v42, a19\n\t" \
  "v_accvgpr_read_b32 v43, a27\n\t" \
  "v_accvgpr_read_b32 v44, a21\n\t" \
  "v_accvgpr_read_b32 v45, a29\n\t" \
  "v_accvgpr_read_b32 v46, a23\n\t" \
  "v_accvgpr_read_b32 v47, a31\n\t" \
  "s_branch 52b\n\t" \
  "44:\n\t" \
  "s_waitcnt lgkmcnt(0)\n\t" \
  "s_and_b32 s97, s44, 3\n\t" \
  "s_lshl_b32 s97, s97, 17\n\t" \
  "s_add_i32 s97, s97, s43\n\t" \
  "s_branch 45b\n\t" \
  "49:\n\t" \
  "s_bitcmp1_b32 s58, 2\n\t" \
  "s_cbranch_scc1 46b\n\t" \
  "s_sleep 2\n\t" \
  "s_add_u32 s59, s59, 1\n\t" \
  "s_bitcmp0_b32 s59, 20\n\t" \
  "s_cbranch_scc1 45b\n\t" \
  "s_or_b32 s58, s58, 4\n\t" \
  "s_branch 46b\n\t" \
  "99:\n\t" \
  "s_waitcnt vmcnt(0) lgkmcnt(0)\n\t" \
  ""
#define P_BODY \
  "s_mov_b32 s46, -1\n\t" \
  "s_cmp_eq_u32 %[wv], 1\n\t" \
  "s_cbranch_scc1 91f\n\t" \
  "s_cmp_eq_u32 %[wv], 2\n\t" \
  "s_cbranch_scc1 92f\n\t" \
  "s_cmp_eq_u32 %[wv], 3\n\t" \
  "s_cbranch_scc1 93f\n\t" \
  "90:\n\t" \
  "ds_read_b128 v[0:3], v164 offset:0\n\t" \
  "ds_read_b128 v[4:7], v164 offset:8192\n\t" \
  "ds_read_b128 v[8:11], v165 offset:0\n\t" \
  "ds_read_b128 v[12:15], v165 offset:8192\n\t" \
  "ds_read_b128 v[16:19], v166 offset:0\n\t" \
  "ds_read_b128 v[20:23], v166 offset:8192\n\t" \
  "ds_read_b128 v[24:27], v167 offset:0\n\t" \
  "ds_read_b128 v[28:31], v167 offset:8192\n\t" \
  "ds_read_b128 v[32:35], v164 offset:256\n\t" \
  "ds_read_b128 v[36:39], v164 offset:8448\n\t" \
  "ds_read_b128 v[40:43], v165 offset:256\n\t" \
  "ds_read_b128 v[44:47], v165 offset:8448\n\t" \
  "ds_read_b128 v[48:51], v166 offset:256\n\t" \
  "ds_read_b128 v[52:55], v166 offset:8448\n\t" \
  "ds_read_b128 v[56:59], v167 offset:256\n\t" \
  "ds_read_b128 v[60:63], v167 offset:8448\n\t" \
  "v_mov_b64 v[152:153], -1\n\t" \
  "v_mov_b64 v[154:155], -1\n\t" \
  "s_cmp_le_u32 s44, 6\n\t" \
  "20:\n\t" \
  "s_waitcnt vmcnt(28) lgkmcnt(0)\n\t" \
  "v_mfma_f32_16x16x32_bf16 v[128:131], a[0:3], v[0:3], 0\n\t" \
  "s_cbranch_scc1 30f\n\t" \
  "40:\n\t" \
  "s_barrier\n\t" \
  "v_cvt_pk_bf16_f32 v148, v136, v137\n\t" \
  "v_cvt_pk_bf16_f32 v149, v138, v139\n\t" \
  "v_mfma_f32_16x16x32_bf16 v[132:135], a[4:7], v[0:3], 0\n\t" \
  "v_cvt_pk_bf16_f32 v150, v140, v141\n\t" \
  "v_cvt_pk_bf16_f32 v151, v142, v143\n\t" \
  "v_mfma_f32_16x16x32_bf16 v[136:139], a[0:3], v[4:7], 0\n\t" \
  "s_add_i32 m0, s41, 0\n\t" \
  "v_mfma_f32_16x16x32_bf16 v[140:143], a[4:7], v[4:7], 0\n\t" \
  "buffer_load_dwordx4 v160, %[rx], s40 offen lds\n\t" \
  "v_mfma_f32_16x16x32_bf16 v[128:131], a[8:11], v[8:11], v[128:131]\n\t" \
  "ds_read_b128 v[64:67], v164 offset:16384\n\t" \
  "v_mfma_f32_16x16x32_bf16 v[132:135], a[12:15], v[8:11], v[132:135]\n\t" \
  "ds_read_b128 v[68:71], v164 offset:24576\n\t" \
  "v_mfma_f32_16x16x32_bf16 v[136:139], a[8:11], v[12:15], v[136:139]\n\t" \
  "ds_read_b128 v[72:75], v165 offset:16384\n\t" \
  "v_mfma_f32_16x16x32_bf16 v[140:143], a[12:15], v[12:15], v[140:143]\n\t" \
  "buffer_store_dwordx4 v[144:147], v168, %[ru], s42 offen sc1\n\t" \
  "v_mfma_f32_16x16x32_bf16 v[128:131], a[16:19], v[16:19], v[128:131]\n\t" \
  "ds_read_b128 v[76:79], v165 offset:24576\n\t" \
  "v_mfma_f32_16x16x32_bf16 v[132:135], a[20:23], v[16:19], v[132:135]\n\t" \
  "ds_read_b128 v[80:83], v166 offset:16384\n\t" \
  "v_mfma_f32_16x16x32_bf16 v[136:139], a[16:19], v[20:23], v[136:139]\n\t" \
  "s_add_i32 m0, s41, 1024\n\t" \
  "v_mfma_f32_16x16x32_bf16 v[140:143], a[20:23], v[20:23], v[140:143]\n\t" \
  "buffer_load_dwordx4 v161, %[rx], s40 offen lds\n\t" \
  "v_mfma_f32_16x16x32_bf16 v[128:131], a[24:27], v[24:27], v[128:131]\n\t" \
  "ds_read_b128 v[84:87], v166 offset:24576\n\t" \
  "v_mfma_f32_16x16x32_bf16 v[132:135], a[28:31], v[24:27], v[132:135]\n\t" \
  "ds_read_b128 v[88:91], v167 offset:16384\n\t" \
  "v_mfma_f32_16x16x32_bf16 v[136:139], a[24:27], v[28:31], v[136:139]\n\t" \
  "ds_read_b128 v[92:95], v167 offset:24576\n\t" \
  "v_mfma_f32_16x16x32_bf16 v[140:143], a[28:31], v[28:31], v[140:143]\n\t" \
  "buffer_store_dwordx4 v[148:151], v169, %[ru], s42 offen sc1\n\t" \
  "v_mfma_f32_16x16x32_bf16 v[128:131], a[32:35], v[32:35], v[128:131]\n\t" \
  "ds_read_b128 v[96:99], v164 offset:16640\n\t" \
  "v_mfma_f32_16x16x32_bf16 v[132:135], a[36:39], v[32:35], v[132:135]\n\t" \
  "ds_read_b128 v[100:103], v164 offset:24832\n\t" \
  "v_mfma_f32_16x16x32_bf16 v[136:139], a[32:35], v[36:39], v[136:139]\n\t" \
  "s_add_i32 m0, s41, 2048\n\t" \
  "v_mfma_f32_16x16x32_bf16 v[140:143], a[36:39], v[36:39], v[140:143]\n\t" \
  "buffer_load_dwordx4 v162, %[rx], s40 offen lds\n\t" \
  "v_mfma_f32_16x16x32_bf16 v[128:131], a[40:43], v[40:43], v[128:131]\n\t" \
  "ds_read_b128 v[104:107], v165 offset:16640\n\t" \
  "v_mfma_f32_16x16x32_bf16 v[132:135], a[44:47], v[40:43], v[132:135]\n\t" \
  "ds_read_b128 v[108:111], v165 offset:24832\n\t" \
  "v_mfma_f32_16x16x32_bf16 v[136:139], a[40:43], v[44:47], v[136:139]\n\t" \
  "s_add_i32 m0, s41, 3072\n\t" \
  "v_mfma_f32_16x16x32_bf16 v[140:143], a[44:47], v[44:47], v[140:143]\n\t" \
  "buffer_load_dwordx4 v163, %[rx], s40 offen lds\n\t" \
  "v_mfma_f32_16x16x32_bf16 v[128:131], a[48:51], v[48:51], v[128:131]\n\t" \
  "ds_read_b128 v[112:115], v166 offset:16640\n\t" \
  "v_mfma_f32_16x16x32_bf16 v[132:135], a[52:55], v[48:51], v[132:135]\n\t" \
  "ds_read_b128 v[116:119], v166 offset:24832\n\t" \
  "v_mfma_f32_16x16x32_bf16 v[136:139], a[48:51], v[52:55], v[136:139]\n\t" \
  "ds_read_b128 v[120:123], v167 offset:16640\n\t" \
  "v_mfma_f32_16x16x32_bf16 v[140:143], a[52:55], v[52:55], v[140:143]\n\t" \
  "ds_read_b128 v[124:127], v167 offset:24832\n\t" \
  "v_mfma_f32_16x16x32_bf16 v[128:131], a[56:59], v[56:59], v[128:131]\n\t" \
  "v_mfma_f32_16x16x32_bf16 v[132:135], a[60:63], v[56:59], v[132:135]\n\t" \
  "v_mfma_f32_16x16x32_bf16 v[136:139], a[56:59], v[60:63], v[136:139]\n\t" \
  "v_mfma_f32_16x16x32_bf16 v[140:143], a[60:63], v[60:63], v[140:143]\n\t" \
  "s_cmp_le_u32 s44, 6\n\t" \
  "s_waitcnt vmcnt(30) lgkmcnt(0)\n\t" \
  "v_mfma_f32_16x16x32_bf16 v[128:131], a[64:67], v[64:67], v[128:131]\n\t" \
  "s_cbranch_scc1 31f\n\t" \
  "41:\n\t" \
  "s_barrier\n\t" \
  "v_mfma_f32_16x16x32_bf16 v[132:135], a[68:71], v[64:67], v[132:135]\n\t" \
  "s_add_i32 s76, s40, 512\n\t" \
  "v_mfma_f32_16x16x32_bf16 v[136:139], a[64:67], v[68:71], v[136:139]\n\t" \
  "s_add_i32 m0, s41, 16384\n\t" \
  "v_mfma_f32_16x16x32_bf16 v[140:143], a[68:71], v[68:71], v[140:143]\n\t" \
  "buffer_load_dwordx4 v160, %[rx], s76 offen lds\n\t" \
  "v_mfma_f32_16x16x32_bf16 v[128:131], a[72:75], v[72:75], v[128:131]\n\t" \
  "ds_read_b128 v[0:3], v164 offset:32768\n\t" \
  "v_mfma_f32_16x16x32_bf16 v[132:135], a[76:79], v[72:75], v[132:135]\n\t" \
  "ds_read_b128 v[4:7], v164 offset:40960\n\t" \
  "v_mfma_f32_16x16x32_bf16 v[136:139], a[72:75], v[76:79], v[136:139]\n\t" \
  "ds_read_b128 v[8:11], v165 offset:32768\n\t" \
  "v_mfma_f32_16x16x32_bf16 v[140:143], a[76:79], v[76:79], v[140:143]\n\t" \
  "v_mfma_f32_16x16x32_bf16 v[128:131], a[80:83], v[80:83], v[128:131]\n\t" \
  "ds_read_b128 v[12:15], v165 offset:40960\n\t" \
  "v_mfma_f32_16x16x32_bf16 v[132:135], a[84:87], v[80:83], v[132:135]\n\t" \
  "ds_read_b128 v[16:19], v166 offset:32768\n\t" \
  "v_mfma_f32_16x16x32_bf16 v[136:139], a[80:83], v[84:87], v[136:139]\n\t" \
  "s_add_i32 m0, s41, 17408\n\t" \
  "v_mfma_f32_16x16x32_bf16 v[140:143], a[84:87], v[84:87], v[140:143]\n\t" \
  "buffer_load_dwordx4 v161, %[rx], s76 offen lds\n\t" \
  "v_mfma_f32_16x16x32_bf16 v[128:131], a[88:91], v[88:91], v[128:131]\n\t" \
  "ds_read_b128 v[20:23], v166 offset:40960\n\t" \
  "v_mfma_f32_16x16x32_bf16 v[132:135], a[92:95], v[88:91], v[132:135]\n\t" \
  "ds_read_b128 v[24:27], v167 offset:32768\n\t" \
  "v_mfma_f32_16x16x32_bf16 v[136:139], a[88:91], v[92:95], v[136:139]\n\t" \
  "ds_read_b128 v[28:31], v167 offset:40960\n\t" \
  "v_mfma_f32_16x16x32_bf16 v[140:143], a[92:95], v[92:95], v[140:143]\n\t" \
  "v_mfma_f32_16x16x32_bf16 v[128:131], a[96:99], v[96:99], v[128:131]\n\t" \
  "ds_read_b128 v[32:35], v164 offset:33024\n\t" \
  "v_mfma_f32_16x16x32_bf16 v[132:135], a[100:103], v[96:99], v[132:135]\n\t" \
  "ds_read_b128 v[36:39], v164 offset:41216\n\t" \
  "v_mfma_f32_16x16x32_bf16 v[136:139], a[96:99], v[100:103], v[136:139]\n\t" \
  "s_add_i32 m0, s41, 18432\n\t" \
  "v_mfma_f32_16x16x32_bf16 v[140:143], a[100:103], v[100:103], v[140:143]\n\t" \
  "buffer_load_dwordx4 v162, %[rx], s76 offen lds\n\t" \
  "v_mfma_f32_16x16x32_bf16 v[128:131], a[104:107], v[104:107], v[128:131]\n\t" \
  "ds_read_b128 v[40:43], v165 offset:33024\n\t" \
  "v_mfma_f32_16x16x32_bf16 v[132:135], a[108:111], v[104:107], v[132:135]\n\t" \
  "ds_read_b128 v[44:47], v165 offset:41216\n\t" \
  "v_mfma_f32_16x16x32_bf16 v[136:139], a[104:107], v[108:111], v[136:139]\n\t" \
  "s_add_i32 m0, s41, 19456\n\t" \
  "v_mfma_f32_16x16x32_bf16 v[140:143], a[108:111], v[108:111], v[140:143]\n\t" \
  "buffer_load_dwordx4 v163, %[rx], s76 offen lds\n\t" \
  "v_mfma_f32_16x16x32_bf16 v[128:131], a[112:115], v[112:115], v[128:131]\n\t" \
  "ds_read_b128 v[48:51], v166 offset:33024\n\t" \
  "v_mfma_f32_16x16x32_bf16 v[132:135], a[116:119], v[112:115], v[132:135]\n\t" \
  "ds_read_b128 v[52:55], v166 offset:41216\n\t" \
  "v_mfma_f32_16x16x32_bf16 v[136:139], a[112:115], v[116:119], v[136:139]\n\t" \
  "ds_read_b128 v[56:59], v167 offset:33024\n\t" \
  "v_mfma_f32_16x16x32_bf16 v[140:143], a[116:119], v[116:119], v[140:143]\n\t" \
  "ds_read_b128 v[60:63], v167 offset:41216\n\t" \
  "v_mfma_f32_16x16x32_bf16 v[128:131], a[120:123], v[120:123], v[128:131]\n\t" \
  "v_mfma_f32_16x16x32_bf16 v[132:135], a[124:127], v[120:123], v[132:135]\n\t" \
  "v_mfma_f32_16x16x32_bf16 v[136:139], a[120:123], v[124:127], v[136:139]\n\t" \
  "v_mfma_f32_16x16x32_bf16 v[140:143], a[124:127], v[124:127], v[140:143]\n\t" \
  "s_cmp_le_u32 s44, 6\n\t" \
  "s_waitcnt vmcnt(29) lgkmcnt(0)\n\t" \
  "v_mfma_f32_16x16x32_bf16 v[128:131], a[128:131], v[0:3], v[128:131]\n\t" \
  "s_cbranch_scc1 32f\n\t" \
  "42:\n\t" \
  "s_barrier\n\t" \
  "v_mfma_f32_16x16x32_bf16 v[132:135], a[132:135], v[0:3], v[132:135]\n\t" \
  "s_add_i32 s76, s40, 1024\n\t" \
  "v_mfma_f32_16x16x32_bf16 v[136:139], a[128:131], v[4:7], v[136:139]\n\t" \
  "s_add_i32 m0, s41, 32768\n\t" \
  "v_mfma_f32_16x16x32_bf16 v[140:143], a[132:135], v[4:7], v[140:143]\n\t" \
  "buffer_load_dwordx4 v160, %[rx], s76 offen lds\n\t" \
  "v_mfma_f32_16x16x32_bf16 v[128:131], a[136:139], v[8:11], v[128:131]\n\t" \
  "ds_read_b128 v[64:67], v164 offset:49152\n\t" \
  "v_mfma_f32_16x16x32_bf16 v[132:135], a[140:143], v[8:11], v[132:135]\n\t" \
  "ds_read_b128 v[68:71], v164 offset:57344\n\t" \
  "v_mfma_f32_16x16x32_bf16 v[136:139], a[136:139], v[12:15], v[136:139]\n\t" \
  "ds_read_b128 v[72:75], v165 offset:49152\n\t" \
  "v_mfma_f32_16x16x32_bf16 v[140:143], a[140:143], v[12:15], v[140:143]\n\t" \
  "v_mfma_f32_16x16x32_bf16 v[128:131], a[144:147], v[16:19], v[128:131]\n\t" \
  "ds_read_b128 v[76:79], v165 offset:57344\n\t" \
  "v_mfma_f32_16x16x32_bf16 v[132:135], a[148:151], v[16:19], v[132:135]\n\t" \
  "ds_read_b128 v[80:83], v166 offset:49152\n\t" \
  "v_mfma_f32_16x16x32_bf16 v[136:139], a[144:147], v[20:23], v[136:139]\n\t" \
  "s_add_i32 m0, s41, 33792\n\t" \
  "v_mfma_f32_16x16x32_bf16 v[140:143], a[148:151], v[20:23], v[140:143]\n\t" \
  "buffer_load_dwordx4 v161, %[rx], s76 offen lds\n\t" \
  "v_mfma_f32_16x16x32_bf16 v[128:131], a[152:155], v[24:27], v[128:131]\n\t" \
  "ds_read_b128 v[84:87], v166 offset:57344\n\t" \
  "v_mfma_f32_16x16x32_bf16 v[132:135], a[156:159], v[24:27], v[132:135]\n\t" \
  "ds_read_b128 v[88:91], v167 offset:49152\n\t" \
  "v_mfma_f32_16x16x32_bf16 v[136:139], a[152:155], v[28:31], v[136:139]\n\t" \
  "ds_read_b128 v[92:95], v167 offset:57344\n\t" \
  "v_mfma_f32_16x16x32_bf16 v[140:143], a[156:159], v[28:31], v[140:143]\n\t" \
  "v_mfma_f32_16x16x32_bf16 v[128:131], a[160:163], v[32:35], v[128:131]\n\t" \
  "ds_read_b128 v[96:99], v164 offset:49408\n\t" \
  "v_mfma_f32_16x16x32_bf16 v[132:135], a[164:167], v[32:35], v[132:135]\n\t" \
  "ds_read_b128 v[100:103], v164 offset:57600\n\t" \
  "v_mfma_f32_16x16x32_bf16 v[136:139], a[160:163], v[36:39], v[136:139]\n\t" \
  "s_add_i32 m0, s41, 34816\n\t" \
  "v_mfma_f32_16x16x32_bf16 v[140:143], a[164:167], v[36:39], v[140:143]\n\t" \
  "buffer_load_dwordx4 v162, %[rx], s76 offen lds\n\t" \
  "v_mfma_f32_16x16x32_bf16 v[128:131], a[168:171], v[40:43], v[128:131]\n\t" \
  "ds_read_b128 v[104:107], v165 offset:49408\n\t" \
  "v_mfma_f32_16x16x32_bf16 v[132:135], a[172:175], v[40:43], v[132:135]\n\t" \
  "ds_read_b128 v[108:111], v165 offset:57600\n\t" \
  "v_mfma_f32_16x16x32_bf16 v[136:139], a[168:171], v[44:47], v[136:139]\n\t" \
  "s_add_i32 m0, s41, 35840\n\t" \
  "v_mfma_f32_16x16x32_bf16 v[140:143], a[172:175], v[44:47], v[140:143]\n\t" \
  "buffer_load_dwordx4 v163, %[rx], s76 offen lds\n\t" \
  "v_mfma_f32_16x16x32_bf16 v[128:131], a[176:179], v[48:51], v[128:131]\n\t" \
  "ds_read_b128 v[112:115], v166 offset:49408\n\t" \
  "v_mfma_f32_16x16x32_bf16 v[132:135], a[180:183], v[48:51], v[132:135]\n\t" \
  "ds_read_b128 v[116:119], v166 offset:57600\n\t" \
  "v_mfma_f32_16x16x32_bf16 v[136:139], a[176:179], v[52:55], v[136:139]\n\t" \
  "ds_read_b128 v[120:123], v167 offset:49408\n\t" \
  "v_mfma_f32_16x16x32_bf16 v[140:143], a[180:183], v[52:55], v[140:143]\n\t" \
  "ds_read_b128 v[124:127], v167 offset:57600\n\t" \
  "v_mfma_f32_16x16x32_bf16 v[128:131], a[184:187], v[56:59], v[128:131]\n\t" \
  "s_add_i32 s78, s44, -2\n\t" \
  "v_mfma_f32_16x16x32_bf16 v[132:135], a[188:191], v[56:59], v[132:135]\n\t" \
  "s_max_i32 s78, s78, s46\n\t" \
  "v_mfma_f32_16x16x32_bf16 v[136:139], a[184:187], v[60:63], v[136:139]\n\t" \
  "v_mov_b32 v156, s78\n\t" \
  "v_mfma_f32_16x16x32_bf16 v[140:143], a[188:191], v[60:63], v[140:143]\n\t" \
  "s_cmp_le_u32 s44, 6\n\t" \
  "s_waitcnt vmcnt(27) lgkmcnt(0)\n\t" \
  "v_mfma_f32_16x16x32_bf16 v[128:131], a[192:195], v[64:67], v[128:131]\n\t" \
  "s_cbranch_scc1 33f\n\t" \
  "43:\n\t" \
  "s_barrier\n\t" \
  "v_xor_b32 v164, 0x10000, v164\n\t" \
  "v_mfma_f32_16x16x32_bf16 v[132:135], a[196:199], v[64:67], v[132:135]\n\t" \
  "s_add_i32 s76, s40, 1536\n\t" \
  "buffer_store_dword v156, v171, %[ru], 0 offen sc1\n\t" \
  "v_mfma_f32_16x16x32_bf16 v[136:139], a[192:195], v[68:71], v[136:139]\n\t" \
  "s_add_i32 m0, s41, 49152\n\t" \
  "v_xor_b32 v165, 0x10000, v165\n\t" \
  "v_mfma_f32_16x16x32_bf16 v[140:143], a[196:199], v[68:71], v[140:143]\n\t" \
  "buffer_load_dwordx4 v160, %[rx], s76 offen lds\n\t" \
  "v_xor_b32 v166, 0x10000, v166\n\t" \
  "v_mfma_f32_16x16x32_bf16 v[128:131], a[200:203], v[72:75], v[128:131]\n\t" \
  "v_xor_b32 v167, 0x10000, v167\n\t" \
  "ds_read_b128 v[0:3], v164 offset:0\n\t" \
  "v_mfma_f32_16x16x32_bf16 v[132:135], a[204:207], v[72:75], v[132:135]\n\t" \
  "ds_read_b128 v[4:7], v164 offset:8192\n\t" \
  "v_mfma_f32_16x16x32_bf16 v[136:139], a[200:203], v[76:79], v[136:139]\n\t" \
  "ds_read_b128 v[8:11], v165 offset:0\n\t" \
  "v_mfma_f32_16x16x32_bf16 v[140:143], a[204:207], v[76:79], v[140:143]\n\t" \
  "v_mfma_f32_16x16x32_bf16 v[128:131], a[208:211], v[80:83], v[128:131]\n\t" \
  "ds_read_b128 v[12:15], v165 offset:8192\n\t" \
  "v_mfma_f32_16x16x32_bf16 v[132:135], a[212:215], v[80:83], v[132:135]\n\t" \
  "ds_read_b128 v[16:19], v166 offset:0\n\t" \
  "v_mfma_f32_16x16x32_bf16 v[136:139], a[208:211], v[84:87], v[136:139]\n\t" \
  "s_add_i32 m0, s41, 50176\n\t" \
  "ds_read_b128 v[20:23], v166 offset:8192\n\t" \
  "v_mfma_f32_16x16x32_bf16 v[140:143], a[212:215], v[84:87], v[140:143]\n\t" \
  "buffer_load_dwordx4 v161, %[rx], s76 offen lds\n\t" \
  "v_mfma_f32_16x16x32_bf16 v[128:131], a[216:219], v[88:91], v[128:131]\n\t" \
  "ds_read_b128 v[24:27], v167 offset:0\n\t" \
  "v_mfma_f32_16x16x32_bf16 v[132:135], a[220:223], v[88:91], v[132:135]\n\t" \
  "ds_read_b128 v[28:31], v167 offset:8192\n\t" \
  "v_mfma_f32_16x16x32_bf16 v[136:139], a[216:219], v[92:95], v[136:139]\n\t" \
  "ds_read_b128 v[32:35], v164 offset:256\n\t" \
  "v_mfma_f32_16x16x32_bf16 v[140:143], a[220:223], v[92:95], v[140:143]\n\t" \
  "v_mfma_f32_16x16x32_bf16 v[128:131], a[224:227], v[96:99], v[128:131]\n\t" \
  "ds_read_b128 v[36:39], v164 offset:8448\n\t" \
  "v_mfma_f32_16x16x32_bf16 v[132:135], a[228:231], v[96:99], v[132:135]\n\t" \
  "ds_read_b128 v[40:43], v165 offset:256\n\t" \
  "v_mfma_f32_16x16x32_bf16 v[136:139], a[224:227], v[100:103], v[136:139]\n\t" \
  "s_add_i32 m0, s41, 51200\n\t" \
  "v_mfma_f32_16x16x32_bf16 v[140:143], a[228:231], v[100:103], v[140:143]\n\t" \
  "buffer_load_dwordx4 v162, %[rx], s76 offen lds\n\t" \
  "v_mfma_f32_16x16x32_bf16 v[128:131], a[232:235], v[104:107], v[128:131]\n\t" \
  "ds_read_b128 v[44:47], v165 offset:8448\n\t" \
  "v_mfma_f32_16x16x32_bf16 v[132:135], a[236:239], v[104:107], v[132:135]\n\t" \
  "ds_read_b128 v[48:51], v166 offset:256\n\t" \
  "v_mfma_f32_16x16x32_bf16 v[136:139], a[232:235], v[108:111], v[136:139]\n\t" \
  "s_add_i32 m0, s41, 52224\n\t" \
  "v_mfma_f32_16x16x32_bf16 v[140:143], a[236:239], v[108:111], v[140:143]\n\t" \
  "buffer_load_dwordx4 v163, %[rx], s76 offen lds\n\t" \
  "v_mfma_f32_16x16x32_bf16 v[128:131], a[240:243], v[112:115], v[128:131]\n\t" \
  "ds_read_b128 v[52:55], v166 offset:8448\n\t" \
  "v_mfma_f32_16x16x32_bf16 v[132:135], a[244:247], v[112:115], v[132:135]\n\t" \
  "ds_read_b128 v[56:59], v167 offset:256\n\t" \
  "v_mfma_f32_16x16x32_bf16 v[136:139], a[240:243], v[116:119], v[136:139]\n\t" \
  "ds_read_b128 v[60:63], v167 offset:8448\n\t" \
  "v_mfma_f32_16x16x32_bf16 v[140:143], a[244:247], v[116:119], v[140:143]\n\t" \
  "s_add_i32 s44, s44, 1\n\t" \
  "s_mov_b32 s42, s43\n\t" \
  "v_mfma_f32_16x16x32_bf16 v[128:131], a[248:251], v[120:123], v[128:131]\n\t" \
  "s_add_i32 s43, s43, 0x40000\n\t" \
  "s_add_i32 s40, s40, 0x10000\n\t" \
  "v_mfma_f32_16x16x32_bf16 v[132:135], a[252:255], v[120:123], v[132:135]\n\t" \
  "s_xor_b32 s41, s41, 0x10000\n\t" \
  "v_mfma_f32_16x16x32_bf16 v[136:139], a[248:251], v[124:127], v[136:139]\n\t" \
  "v_cvt_pk_bf16_f32 v144, v128, v129\n\t" \
  "v_cvt_pk_bf16_f32 v145, v130, v131\n\t" \
  "v_mfma_f32_16x16x32_bf16 v[140:143], a[252:255], v[124:127], v[140:143]\n\t" \
  "v_cvt_pk_bf16_f32 v146, v132, v133\n\t" \
  "v_cvt_pk_bf16_f32 v147, v134, v135\n\t" \
  "s_cmp_eq_u32 s44, 64\n\t" \
  "s_cbranch_scc1 98f\n\t" \
  "s_cmp_le_u32 s44, 6\n\t" \
  "s_branch 20b\n\t" \
  "30:\n\t" \
  "s_waitcnt vmcnt(24)\n\t" \
  "s_branch 40b\n\t" \
  "31:\n\t" \
  "s_waitcnt vmcnt(24)\n\t" \
  "s_branch 41b\n\t" \
  "32:\n\t" \
  "s_waitcnt vmcnt(6)\n\t" \
  "s_barrier\n\t" \
  "s_add_i32 s46, s44, -1\n\t" \
  "v_mov_b32 v156, s46\n\t" \
  "buffer_store_dword v156, v171, %[ru], 0 offen sc1\n\t" \
  "s_branch 42b\n\t" \
  "33:\n\t" \
  "s_waitcnt vmcnt(24)\n\t" \
  "s_branch 43b\n\t" \
  "91:\n\t" \
  "ds_read_b128 v[0:3], v164 offset:0\n\t" \
  "ds_read_b128 v[4:7], v164 offset:8192\n\t" \
  "ds_read_b128 v[8:11], v165 offset:0\n\t" \
  "ds_read_b128 v[12:15], v165 offset:8192\n\t" \
  "ds_read_b128 v[16:19], v166 offset:0\n\t" \
  "ds_read_b128 v[20:23], v166 offset:8192\n\t" \
  "ds_read_b128 v[24:27], v167 offset:0\n\t" \
  "ds_read_b128 v[28:31], v167 offset:8192\n\t" \
  "ds_read_b128 v[32:35], v164 offset:256\n\t" \
  "ds_read_b128 v[36:39], v164 offset:8448\n\t" \
  "ds_read_b128 v[40:43], v165 offset:256\n\t" \
  "ds_read_b128 v[44:47], v165 offset:8448\n\t" \
  "ds_read_b128 v[48:51], v166 offset:256\n\t" \
  "ds_read_b128 v[52:55], v166 offset:8448\n\t" \
  "ds_read_b128 v[56:59], v167 offset:256\n\t" \
  "ds_read_b128 v[60:63], v167 offset:8448\n\t" \
  "v_mov_b64 v[152:153], -1\n\t" \
  "v_mov_b64 v[154:155], -1\n\t" \
  "s_cmp_le_u32 s44, 6\n\t" \
  "20:\n\t" \
  "s_waitcnt vmcnt(28) lgkmcnt(0)\n\t" \
  "v_mfma_f32_16x16x32_bf16 v[128:131], a[0:3], v[0:3], 0\n\t" \
  "s_cbranch_scc1 30f\n\t" \
  "40:\n\t" \
  "s_barrier\n\t" \
  "v_cvt_pk_bf16_f32 v148, v136, v137\n\t" \
  "v_cvt_pk_bf16_f32 v149, v138, v139\n\t" \
  "v_mfma_f32_16x16x32_bf16 v[132:135], a[4:7], v[0:3], 0\n\t" \
  "v_cvt_pk_bf16_f32 v150, v140, v141\n\t" \
  "v_cvt_pk_bf16_f32 v151, v142, v143\n\t" \
  "v_mfma_f32_16x16x32_bf16 v[136:139], a[0:3], v[4:7], 0\n\t" \
  "ds_read_b128 v[64:67], v164 offset:16384\n\t" \
  "v_mfma_f32_16x16x32_bf16 v[140:143], a[4:7], v[4:7], 0\n\t" \
  "s_add_i32 m0, s41, 0\n\t" \
  "v_mfma_f32_16x16x32_bf16 v[128:131], a[8:11], v[8:11], v[128:131]\n\t" \
  "buffer_load_dwordx4 v160, %[rx], s40 offen lds\n\t" \
  "v_mfma_f32_16x16x32_bf16 v[132:135], a[12:15], v[8:11], v[132:135]\n\t" \
  "ds_read_b128 v[68:71], v164 offset:24576\n\t" \
  "v_mfma_f32_16x16x32_bf16 v[136:139], a[8:11], v[12:15], v[136:139]\n\t" \
  "ds_read_b128 v[72:75], v165 offset:16384\n\t" \
  "v_mfma_f32_16x16x32_bf16 v[140:143], a[12:15], v[12:15], v[140:143]\n\t" \
  "ds_read_b128 v[76:79], v165 offset:24576\n\t" \
  "v_mfma_f32_16x16x32_bf16 v[128:131], a[16:19], v[16:19], v[128:131]\n\t" \
  "buffer_store_dwordx4 v[144:147], v168, %[ru], s42 offen sc1\n\t" \
  "v_mfma_f32_16x16x32_bf16 v[132:135], a[20:23], v[16:19], v[132:135]\n\t" \
  "ds_read_b128 v[80:83], v166 offset:16384\n\t" \
  "v_mfma_f32_16x16x32_bf16 v[136:139], a[16:19], v[20:23], v[136:139]\n\t" \
  "ds_read_b128 v[84:87], v166 offset:24576\n\t" \
  "v_mfma_f32_16x16x32_bf16 v[140:143], a[20:23], v[20:23], v[140:143]\n\t" \
  "s_add_i32 m0, s41, 1024\n\t" \
  "v_mfma_f32_16x16x32_bf16 v[128:131], a[24:27], v[24:27], v[128:131]\n\t" \
  "buffer_load_dwordx4 v161, %[rx], s40 offen lds\n\t" \
  "v_mfma_f32_16x16x32_bf16 v[132:135], a[28:31], v[24:27], v[132:135]\n\t" \
  "ds_read_b128 v[88:91], v167 offset:16384\n\t" \
  "v_mfma_f32_16x16x32_bf16 v[136:139], a[24:27], v[28:31], v[136:139]\n\t" \
  "ds_read_b128 v[92:95], v167 offset:24576\n\t" \
  "v_mfma_f32_16x16x32_bf16 v[140:143], a[28:31], v[28:31], v[140:143]\n\t" \
  "ds_read_b128 v[96:99], v164 offset:16640\n\t" \
  "v_mfma_f32_16x16x32_bf16 v[128:131], a[32:35], v[32:35], v[128:131]\n\t" \
  "buffer_store_dwordx4 v[148:151], v169, %[ru], s42 offen sc1\n\t" \
  "v_mfma_f32_16x16x32_bf16 v[132:135], a[36:39], v[32:35], v[132:135]\n\t" \
  "ds_read_b128 v[100:103], v164 offset:24832\n\t" \
  "v_mfma_f32_16x16x32_bf16 v[136:139], a[32:35], v[36:39], v[136:139]\n\t" \
  "ds_read_b128 v[104:107], v165 offset:16640\n\t" \
  "v_mfma_f32_16x16x32_bf16 v[140:143], a[36:39], v[36:39], v[140:143]\n\t" \
  "s_add_i32 m0, s41, 2048\n\t" \
  "v_mfma_f32_16x16x32_bf16 v[128:131], a[40:43], v[40:43], v[128:131]\n\t" \
  "buffer_load_dwordx4 v162, %[rx], s40 offen lds\n\t" \
  "v_mfma_f32_16x16x32_bf16 v[132:135], a[44:47], v[40:43], v[132:135]\n\t" \
  "ds_read_b128 v[108:111], v165 offset:24832\n\t" \
  "v_mfma_f32_16x16x32_bf16 v[136:139], a[40:43], v[44:47], v[136:139]\n\t" \
  "ds_read_b128 v[112:115], v166 offset:16640\n\t" \
  "v_mfma_f32_16x16x32_bf16 v[140:143], a[44:47], v[44:47], v[140:143]\n\t" \
  "s_add_i32 m0, s41, 3072\n\t" \
  "v_mfma_f32_16x16x32_bf16 v[128:131], a[48:51], v[48:51], v[128:131]\n\t" \
  "buffer_load_dwordx4 v163, %[rx], s40 offen lds\n\t" \
  "v_mfma_f32_16x16x32_bf16 v[132:135], a[52:55], v[48:51], v[132:135]\n\t" \
  "ds_read_b128 v[116:119], v166 offset:24832\n\t" \
  "v_mfma_f32_16x16x32_bf16 v[136:139], a[48:51], v[52:55], v[136:139]\n\t" \
  "ds_read_b128 v[120:123], v167 offset:16640\n\t" \
  "v_mfma_f32_16x16x32_bf16 v[140:143], a[52:55], v[52:55], v[140:143]\n\t" \
  "ds_read_b128 v[124:127], v167 offset:24832\n\t" \
  "v_mfma_f32_16x16x32_bf16 v[128:131], a[56:59], v[56:59], v[128:131]\n\t" \
  "v_mfma_f32_16x16x32_bf16 v[132:135], a[60:63], v[56:59], v[132:135]\n\t" \
  "v_mfma_f32_16x16x32_bf16 v[136:139], a[56:59], v[60:63], v[136:139]\n\t" \
  "v_mfma_f32_16x16x32_bf16 v[140:143], a[60:63], v[60:63], v[140:143]\n\t" \
  "s_cmp_le_u32 s44, 6\n\t" \
  "s_waitcnt vmcnt(30) lgkmcnt(0)\n\t" \
  "v_mfma_f32_16x16x32_bf16 v[128:131], a[64:67], v[64:67], v[128:131]\n\t" \
  "s_cbranch_scc1 31f\n\t" \
  "41:\n\t" \
  "s_barrier\n\t" \
  "v_mfma_f32_16x16x32_bf16 v[132:135], a[68:71], v[64:67], v[132:135]\n\t" \
  "s_add_i32 s76, s40, 512\n\t" \
  "v_mfma_f32_16x16x32_bf16 v[136:139], a[64:67], v[68:71], v[136:139]\n\t" \
  "ds_read_b128 v[0:3], v164 offset:32768\n\t" \
  "v_mfma_f32_16x16x32_bf16 v[140:143], a[68:71], v[68:71], v[140:143]\n\t" \
  "s_add_i32 m0, s41, 16384\n\t" \
  "v_mfma_f32_16x16x32_bf16 v[128:131], a[72:75], v[72:75], v[128:131]\n\t" \
  "buffer_load_dwordx4 v160, %[rx], s76 offen lds\n\t" \
  "v_mfma_f32_16x16x32_bf16 v[132:135], a[76:79], v[72:75], v[132:135]\n\t" \
  "ds_read_b128 v[4:7], v164 offset:40960\n\t" \
  "v_mfma_f32_16x16x32_bf16 v[136:139], a[72:75], v[76:79], v[136:139]\n\t" \
  "ds_read_b128 v[8:11], v165 offset:32768\n\t" \
  "v_mfma_f32_16x16x32_bf16 v[140:143], a[76:79], v[76:79], v[140:143]\n\t" \
  "ds_read_b128 v[12:15], v165 offset:40960\n\t" \
  "v_mfma_f32_16x16x32_bf16 v[128:131], a[80:83], v[80:83], v[128:131]\n\t" \
  "v_mfma_f32_16x16x32_bf16 v[132:135], a[84:87], v[80:83], v[132:135]\n\t" \
  "ds_read_b128 v[16:19], v166 offset:32768\n\t" \
  "v_mfma_f32_16x16x32_bf16 v[136:139], a[80:83], v[84:87], v[136:139]\n\t" \
  "ds_read_b128 v[20:23], v166 offset:40960\n\t" \
  "v_mfma_f32_16x16x32_bf16 v[140:143], a[84:87], v[84:87], v[140:143]\n\t" \
  "s_add_i32 m0, s41, 17408\n\t" \
  "v_mfma_f32_16x16x32_bf16 v[128:131], a[88:91], v[88:91], v[128:131]\n\t" \
  "buffer_load_dwordx4 v161, %[rx], s76 offen lds\n\t" \
  "v_mfma_f32_16x16x32_bf16 v[132:135], a[92:95], v[88:91], v[132:135]\n\t" \
  "ds_read_b128 v[24:27], v167 offset:32768\n\t" \
  "v_mfma_f32_16x16x32_bf16 v[136:139], a[88:91], v[92:95], v[136:139]\n\t" \
  "ds_read_b128 v[28:31], v167 offset:40960\n\t" \
  "v_mfma_f32_16x16x32_bf16 v[140:143], a[92:95], v[92:95], v[140:143]\n\t" \
  "ds_read_b128 v[32:35], v164 offset:33024\n\t" \
  "v_mfma_f32_16x16x32_bf16 v[128:131], a[96:99], v[96:99], v[128:131]\n\t" \
  "v_mfma_f32_16x16x32_bf16 v[132:135], a[100:103], v[96:99], v[132:135]\n\t" \
  "ds_read_b128 v[36:39], v164 offset:41216\n\t" \
  "v_mfma_f32_16x16x32_bf16 v[136:139], a[96:99], v[100:103], v[136:139]\n\t" \
  "ds_read_b128 v[40:43], v165 offset:33024\n\t" \
  "v_mfma_f32_16x16x32_bf16 v[140:143], a[100:103], v[100:103], v[140:143]\n\t" \
  "s_add_i32 m0, s41, 18432\n\t" \
  "v_mfma_f32_16x16x32_bf16 v[128:131], a[104:107], v[104:107], v[128:131]\n\t" \
  "buffer_load_dwordx4 v162, %[rx], s76 offen lds\n\t" \
  "v_mfma_f32_16x16x32_bf16 v[132:135], a[108:111], v[104:107], v[132:135]\n\t" \
  "ds_read_b128 v[44:47], v165 offset:41216\n\t" \
  "v_mfma_f32_16x16x32_bf16 v[136:139], a[104:107], v[108:111], v[136:139]\n\t" \
  "ds_read_b128 v[48:51], v166 offset:33024\n\t" \
  "v_mfma_f32_16x16x32_bf16 v[140:143], a[108:111], v[108:111], v[140:143]\n\t" \
  "s_add_i32 m0, s41, 19456\n\t" \
  "v_mfma_f32_16x16x32_bf16 v[128:131], a[112:115], v[112:115], v[128:131]\n\t" \
  "buffer_load_dwordx4 v163, %[rx], s76 offen lds\n\t" \
  "v_mfma_f32_16x16x32_bf16 v[132:135], a[116:119], v[112:115], v[132:135]\n\t" \
  "ds_read_b128 v[52:55], v166 offset:41216\n\t" \
  "v_mfma_f32_16x16x32_bf16 v[136:139], a[112:115], v[116:119], v[136:139]\n\t" \
  "ds_read_b128 v[56:59], v167 offset:33024\n\t" \
  "v_mfma_f32_16x16x32_bf16 v[140:143], a[116:119], v[116:119], v[140:143]\n\t" \
  "ds_read_b128 v[60:63], v167 offset:41216\n\t" \
  "v_mfma_f32_16x16x32_bf16 v[128:131], a[120:123], v[120:123], v[128:131]\n\t" \
  "v_mfma_f32_16x16x32_bf16 v[132:135], a[124:127], v[120:123], v[132:135]\n\t" \
  "v_mfma_f32_16x16x32_bf16 v[136:139], a[120:123], v[124:127], v[136:139]\n\t" \
  "v_mfma_f32_16x16x32_bf16 v[140:143], a[124:127], v[124:127], v[140:143]\n\t" \
  "s_cmp_le_u32 s44, 6\n\t" \
  "s_waitcnt vmcnt(29) lgkmcnt(0)\n\t" \
  "v_mfma_f32_16x16x32_bf16 v[128:131], a[128:131], v[0:3], v[128:131]\n\t" \
  "s_cbranch_scc1 32f\n\t" \
  "42:\n\t" \
  "s_barrier\n\t" \
  "v_mfma_f32_16x16x32_bf16 v[132:135], a[132:135], v[0:3], v[132:135]\n\t" \
  "s_add_i32 s76, s40, 1024\n\t" \
  "v_mfma_f32_16x16x32_bf16 v[136:139], a[128:131], v[4:7], v[136:139]\n\t" \
  "ds_read_b128 v[64:67], v164 offset:49152\n\t" \
  "v_mfma_f32_16x16x32_bf16 v[140:143], a[132:135], v[4:7], v[140:143]\n\t" \
  "s_add_i32 m0, s41, 32768\n\t" \
  "v_mfma_f32_16x16x32_bf16 v[128:131], a[136:139], v[8:11], v[128:131]\n\t" \
  "buffer_load_dwordx4 v160, %[rx], s76 offen lds\n\t" \
  "v_mfma_f32_16x16x32_bf16 v[132:135], a[140:143], v[8:11], v[132:135]\n\t" \
  "ds_read_b128 v[68:71], v164 offset:57344\n\t" \
  "v_mfma_f32_16x16x32_bf16 v[136:139], a[136:139], v[12:15], v[136:139]\n\t" \
  "ds_read_b128 v[72:75], v165 offset:49152\n\t" \
  "v_mfma_f32_16x16x32_bf16 v[140:143], a[140:143], v[12:15], v[140:143]\n\t" \
  "ds_read_b128 v[76:79], v165 offset:57344\n\t" \
  "v_mfma_f32_16x16x32_bf16 v[128:131], a[144:147], v[16:19], v[128:131]\n\t" \
  "v_mfma_f32_16x16x32_bf16 v[132:135], a[148:151], v[16:19], v[132:135]\n\t" \
  "ds_read_b128 v[80:83], v166 offset:49152\n\t" \
  "v_mfma_f32_16x16x32_bf16 v[136:139], a[144:147], v[20:23], v[136:139]\n\t" \
  "ds_read_b128 v[84:87], v166 offset:57344\n\t" \
  "v_mfma_f32_16x16x32_bf16 v[140:143], a[148:151], v[20:23], v[140:143]\n\t" \
  "s_add_i32 m0, s41, 33792\n\t" \
  "v_mfma_f32_16x16x32_bf16 v[128:131], a[152:155], v[24:27], v[128:131]\n\t" \
  "buffer_load_dwordx4 v161, %[rx], s76 offen lds\n\t" \
  "v_mfma_f32_16x16x32_bf16 v[132:135], a[156:159], v[24:27], v[132:135]\n\t" \
  "ds_read_b128 v[88:91], v167 offset:49152\n\t" \
  "v_mfma_f32_16x16x32_bf16 v[136:139], a[152:155], v[28:31], v[136:139]\n\t" \
  "ds_read_b128 v[92:95], v167 offset:57344\n\t" \
  "v_mfma_f32_16x16x32_bf16 v[140:143], a[156:159], v[28:31], v[140:143]\n\t" \
  "ds_read_b128 v[96:99], v164 offset:49408\n\t" \
  "v_mfma_f32_16x16x32_bf16 v[128:131], a[160:163], v[32:35], v[128:131]\n\t" \
  "v_mfma_f32_16x16x32_bf16 v[132:135], a[164:167], v[32:35], v[132:135]\n\t" \
  "ds_read_b128 v[100:103], v164 offset:57600\n\t" \
  "v_mfma_f32_16x16x32_bf16 v[136:139], a[160:163], v[36:39], v[136:139]\n\t" \
  "ds_read_b128 v[104:107], v165 offset:49408\n\t" \
  "v_mfma_f32_16x16x32_bf16 v[140:143], a[164:167], v[36:39], v[140:143]\n\t" \
  "s_add_i32 m0, s41, 34816\n\t" \
  "v_mfma_f32_16x16x32_bf16 v[128:131], a[168:171], v[40:43], v[128:131]\n\t" \
  "buffer_load_dwordx4 v162, %[rx], s76 offen lds\n\t" \
  "v_mfma_f32_16x16x32_bf16 v[132:135], a[172:175], v[40:43], v[132:135]\n\t" \
  "ds_read_b128 v[108:111], v165 offset:57600\n\t" \
  "v_mfma_f32_16x16x32_bf16 v[136:139], a[168:171], v[44:47], v[136:139]\n\t" \
  "ds_read_b128 v[112:115], v166 offset:49408\n\t" \
  "v_mfma_f32_16x16x32_bf16 v[140:143], a[172:175], v[44:47], v[140:143]\n\t" \
  "s_add_i32 m0, s41, 35840\n\t" \
  "v_mfma_f32_16x16x32_bf16 v[128:131], a[176:179], v[48:51], v[128:131]\n\t" \
  "buffer_load_dwordx4 v163, %[rx], s76 offen lds\n\t" \
  "v_mfma_f32_16x16x32_bf16 v[132:135], a[180:183], v[48:51], v[132:135]\n\t" \
  "ds_read_b128 v[116:119], v166 offset:57600\n\t" \
  "v_mfma_f32_16x16x32_bf16 v[136:139], a[176:179], v[52:55], v[136:139]\n\t" \
  "ds_read_b128 v[120:123], v167 offset:49408\n\t" \
  "v_mfma_f32_16x16x32_bf16 v[140:143], a[180:183], v[52:55], v[140:143]\n\t" \
  "ds_read_b128 v[124:127], v167 offset:57600\n\t" \
  "v_mfma_f32_16x16x32_bf16 v[128:131], a[184:187], v[56:59], v[128:131]\n\t" \
  "s_add_i32 s78, s44, -2\n\t" \
  "v_mfma_f32_16x16x32_bf16 v[132:135], a[188:191], v[56:59], v[132:135]\n\t" \
  "s_max_i32 s78, s78, s46\n\t" \
  "v_mfma_f32_16x16x32_bf16 v[136:139], a[184:187], v[60:63], v[136:139]\n\t" \
  "v_mov_b32 v156, s78\n\t" \
  "v_mfma_f32_16x16x32_bf16 v[140:143], a[188:191], v[60:63], v[140:143]\n\t" \
  "s_cmp_le_u32 s44, 6\n\t" \
  "s_waitcnt vmcnt(27) lgkmcnt(0)\n\t" \
  "v_mfma_f32_16x16x32_bf16 v[128:131], a[192:195], v[64:67], v[128:131]\n\t" \
  "s_cbranch_scc1 33f\n\t" \
  "43:\n\t" \
  "s_barrier\n\t" \
  "v_xor_b32 v164, 0x10000, v164\n\t" \
  "v_mfma_f32_16x16x32_bf16 v[132:135], a[196:199], v[64:67], v[132:135]\n\t" \
  "s_add_i32 s76, s40, 1536\n\t" \
  "buffer_store_dword v156, v171, %[ru], 0 offen sc1\n\t" \
  "v_mfma_f32_16x16x32_bf16 v[136:139], a[192:195], v[68:71], v[136:139]\n\t" \
  "v_xor_b32 v165, 0x10000, v165\n\t" \
  "ds_read_b128 v[0:3], v164 offset:0\n\t" \
  "v_mfma_f32_16x16x32_bf16 v[140:143], a[196:199], v[68:71], v[140:143]\n\t" \
  "s_add_i32 m0, s41, 49152\n\t" \
  "v_xor_b32 v166, 0x10000, v166\n\t" \
  "v_mfma_f32_16x16x32_bf16 v[128:131], a[200:203], v[72:75], v[128:131]\n\t" \
  "buffer_load_dwordx4 v160, %[rx], s76 offen lds\n\t" \
  "v_xor_b32 v167, 0x10000, v167\n\t" \
  "v_mfma_f32_16x16x32_bf16 v[132:135], a[204:207], v[72:75], v[132:135]\n\t" \
  "ds_read_b128 v[4:7], v164 offset:8192\n\t" \
  "v_mfma_f32_16x16x32_bf16 v[136:139], a[200:203], v[76:79], v[136:139]\n\t" \
  "ds_read_b128 v[8:11], v165 offset:0\n\t" \
  "v_mfma_f32_16x16x32_bf16 v[140:143], a[204:207], v[76:79], v[140:143]\n\t" \
  "ds_read_b128 v[12:15], v165 offset:8192\n\t" \
  "v_mfma_f32_16x16x32_bf16 v[128:131], a[208:211], v[80:83], v[128:131]\n\t" \
  "v_mfma_f32_16x16x32_bf16 v[132:135], a[212:215], v[80:83], v[132:135]\n\t" \
  "ds_read_b128 v[16:19], v166 offset:0\n\t" \
  "v_mfma_f32_16x16x32_bf16 v[136:139], a[208:211], v[84:87], v[136:139]\n\t" \
  "ds_read_b128 v[20:23], v166 offset:8192\n\t" \
  "v_mfma_f32_16x16x32_bf16 v[140:143], a[212:215], v[84:87], v[140:143]\n\t" \
  "s_add_i32 m0, s41, 50176\n\t" \
  "ds_read_b128 v[24:27], v167 offset:0\n\t" \
  "v_mfma_f32_16x16x32_bf16 v[128:131], a[216:219], v[88:91], v[128:131]\n\t" \
  "buffer_load_dwordx4 v161, %[rx], s76 offen lds\n\t" \
  "v_mfma_f32_16x16x32_bf16 v[132:135], a[220:223], v[88:91], v[132:135]\n\t" \
  "ds_read_b128 v[28:31], v167 offset:8192\n\t" \
  "v_mfma_f32_16x16x32_bf16 v[136:139], a[216:219], v[92:95], v[136:139]\n\t" \
  "ds_read_b128 v[32:35], v164 offset:256\n\t" \
  "v_mfma_f32_16x16x32_bf16 v[140:143], a[220:223], v[92:95], v[140:143]\n\t" \
  "ds_read_b128 v[36:39], v164 offset:8448\n\t" \
  "v_mfma_f32_16x16x32_bf16 v[128:131], a[224:227], v[96:99], v[128:131]\n\t" \
  "v_mfma_f32_16x16x32_bf16 v[132:135], a[228:231], v[96:99], v[132:135]\n\t" \
  "ds_read_b128 v[40:43], v165 offset:256\n\t" \
  "v_mfma_f32_16x16x32_bf16 v[136:139], a[224:227], v[100:103], v[136:139]\n\t" \
  "ds_read_b128 v[44:47], v165 offset:8448\n\t" \
  "v_mfma_f32_16x16x32_bf16 v[140:143], a[228:231], v[100:103], v[140:143]\n\t" \
  "s_add_i32 m0, s41, 51200\n\t" \
  "v_mfma_f32_16x16x32_bf16 v[128:131], a[232:235], v[104:107], v[128:131]\n\t" \
  "buffer_load_dwordx4 v162, %[rx], s76 offen lds\n\t" \
  "v_mfma_f32_16x16x32_bf16 v[132:135], a[236:239], v[104:107], v[132:135]\n\t" \
  "ds_read_b128 v[48:51], v166 offset:256\n\t" \
  "v_mfma_f32_16x16x32_bf16 v[136:139], a[232:235], v[108:111], v[136:139]\n\t" \
  "ds_read_b128 v[52:55], v166 offset:8448\n\t" \
  "v_mfma_f32_16x16x32_bf16 v[140:143], a[236:239], v[108:111], v[140:143]\n\t" \
  "s_add_i32 m0, s41, 52224\n\t" \
  "v_mfma_f32_16x16x32_bf16 v[128:131], a[240:243], v[112:115], v[128:131]\n\t" \
  "buffer_load_dwordx4 v163, %[rx], s76 offen lds\n\t" \
  "v_mfma_f32_16x16x32_bf16 v[132:135], a[244:247], v[112:115], v[132:135]\n\t" \
  "ds_read_b128 v[56:59], v167 offset:256\n\t" \
  "v_mfma_f32_16x16x32_bf16 v[136:139], a[240:243], v[116:119], v[136:139]\n\t" \
  "ds_read_b128 v[60:63], v167 offset:8448\n\t" \
  "v_mfma_f32_16x16x32_bf16 v[140:143], a[244:247], v[116:119], v[140:143]\n\t" \
  "s_add_i32 s44, s44, 1\n\t" \
  "s_mov_b32 s42, s43\n\t" \
  "v_mfma_f32_16x16x32_bf16 v[128:131], a[248:251], v[120:123], v[128:131]\n\t" \
  "s_add_i32 s43, s43, 0x40000\n\t" \
  "s_add_i32 s40, s40, 0x10000\n\t" \
  "v_mfma_f32_16x16x32_bf16 v[132:135], a[252:255], v[120:123], v[132:135]\n\t" \
  "s_xor_b32 s41, s41, 0x10000\n\t" \
  "v_mfma_f32_16x16x32_bf16 v[136:139], a[248:251], v[124:127], v[136:139]\n\t" \
  "v_cvt_pk_bf16_f32 v144, v128, v129\n\t" \
  "v_cvt_pk_bf16_f32 v145, v130, v131\n\t" \
  "v_mfma_f32_16x16x32_bf16 v[140:143], a[252:255], v[124:127], v[140:143]\n\t" \
  "v_cvt_pk_bf16_f32 v146, v132, v133\n\t" \
  "v_cvt_pk_bf16_f32 v147, v134, v135\n\t" \
  "s_cmp_eq_u32 s44, 64\n\t" \
  "s_cbranch_scc1 98f\n\t" \
  "s_cmp_le_u32 s44, 6\n\t" \
  "s_branch 20b\n\t" \
  "30:\n\t" \
  "s_waitcnt vmcnt(24)\n\t" \
  "s_branch 40b\n\t" \
  "31:\n\t" \
  "s_waitcnt vmcnt(24)\n\t" \
  "s_branch 41b\n\t" \
  "32:\n\t" \
  "s_waitcnt vmcnt(6)\n\t" \
  "s_barrier\n\t" \
  "s_add_i32 s46, s44, -1\n\t" \
  "v_mov_b32 v156, s46\n\t" \
  "buffer_store_dword v156, v171, %[ru], 0 offen sc1\n\t" \
  "s_branch 42b\n\t" \
  "33:\n\t" \
  "s_waitcnt vmcnt(24)\n\t" \
  "s_branch 43b\n\t" \
  "92:\n\t" \
  "ds_read_b128 v[0:3], v164 offset:0\n\t" \
  "ds_read_b128 v[4:7], v164 offset:8192\n\t" \
  "ds_read_b128 v[8:11], v165 offset:0\n\t" \
  "ds_read_b128 v[12:15], v165 offset:8192\n\t" \
  "ds_read_b128 v[16:19], v166 offset:0\n\t" \
  "ds_read_b128 v[20:23], v166 offset:8192\n\t" \
  "ds_read_b128 v[24:27], v167 offset:0\n\t" \
  "ds_read_b128 v[28:31], v167 offset:8192\n\t" \
  "ds_read_b128 v[32:35], v164 offset:256\n\t" \
  "ds_read_b128 v[36:39], v164 offset:8448\n\t" \
  "ds_read_b128 v[40:43], v165 offset:256\n\t" \
  "ds_read_b128 v[44:47], v165 offset:8448\n\t" \
  "ds_read_b128 v[48:51], v166 offset:256\n\t" \
  "ds_read_b128 v[52:55], v166 offset:8448\n\t" \
  "ds_read_b128 v[56:59], v167 offset:256\n\t" \
  "ds_read_b128 v[60:63], v167 offset:8448\n\t" \
  "v_mov_b64 v[152:153], -1\n\t" \
  "v_mov_b64 v[154:155], -1\n\t" \
  "s_cmp_le_u32 s44, 6\n\t" \
  "20:\n\t" \
  "s_waitcnt vmcnt(28) lgkmcnt(0)\n\t" \
  "v_mfma_f32_16x16x32_bf16 v[128:131], a[0:3], v[0:3], 0\n\t" \
  "s_cbranch_scc1 30f\n\t" \
  "40:\n\t" \
  "s_barrier\n\t" \
  "v_cvt_pk_bf16_f32 v148, v136, v137\n\t" \
  "v_cvt_pk_bf16_f32 v149, v138, v139\n\t" \
  "v_mfma_f32_16x16x32_bf16 v[132:135], a[4:7], v[0:3], 0\n\t" \
  "v_cvt_pk_bf16_f32 v150, v140, v141\n\t" \
  "v_cvt_pk_bf16_f32 v151, v142, v143\n\t" \
  "v_mfma_f32_16x16x32_bf16 v[136:139], a[0:3], v[4:7], 0\n\t" \
  "ds_read_b128 v[64:67], v164 offset:16384\n\t" \
  "v_mfma_f32_16x16x32_bf16 v[140:143], a[4:7], v[4:7], 0\n\t" \
  "ds_read_b128 v[68:71], v164 offset:24576\n\t" \
  "v_mfma_f32_16x16x32_bf16 v[128:131], a[8:11], v[8:11], v[128:131]\n\t" \
  "s_add_i32 m0, s41, 0\n\t" \
  "v_mfma_f32_16x16x32_bf16 v[132:135], a[12:15], v[8:11], v[132:135]\n\t" \
  "buffer_load_dwordx4 v160, %[rx], s40 offen lds\n\t" \
  "v_mfma_f32_16x16x32_bf16 v[136:139], a[8:11], v[12:15], v[136:139]\n\t" \
  "ds_read_b128 v[72:75], v165 offset:16384\n\t" \
  "v_mfma_f32_16x16x32_bf16 v[140:143], a[12:15], v[12:15], v[140:143]\n\t" \
  "ds_read_b128 v[76:79], v165 offset:24576\n\t" \
  "v_mfma_f32_16x16x32_bf16 v[128:131], a[16:19], v[16:19], v[128:131]\n\t" \
  "ds_read_b128 v[80:83], v166 offset:16384\n\t" \
  "v_mfma_f32_16x16x32_bf16 v[132:135], a[20:23], v[16:19], v[132:135]\n\t" \
  "buffer_store_dwordx4 v[144:147], v168, %[ru], s42 offen sc1\n\t" \
  "v_mfma_f32_16x16x32_bf16 v[136:139], a[16:19], v[20:23], v[136:139]\n\t" \
  "ds_read_b128 v[84:87], v166 offset:24576\n\t" \
  "v_mfma_f32_16x16x32_bf16 v[140:143], a[20:23], v[20:23], v[140:143]\n\t" \
  "ds_read_b128 v[88:91], v167 offset:16384\n\t" \
  "v_mfma_f32_16x16x32_bf16 v[128:131], a[24:27], v[24:27], v[128:131]\n\t" \
  "s_add_i32 m0, s41, 1024\n\t" \
  "v_mfma_f32_16x16x32_bf16 v[132:135], a[28:31], v[24:27], v[132:135]\n\t" \
  "buffer_load_dwordx4 v161, %[rx], s40 offen lds\n\t" \
  "v_mfma_f32_16x16x32_bf16 v[136:139], a[24:27], v[28:31], v[136:139]\n\t" \
  "ds_read_b128 v[92:95], v167 offset:24576\n\t" \
  "v_mfma_f32_16x16x32_bf16 v[140:143], a[28:31], v[28:31], v[140:143]\n\t" \
  "ds_read_b128 v[96:99], v164 offset:16640\n\t" \
  "v_mfma_f32_16x16x32_bf16 v[128:131], a[32:35], v[32:35], v[128:131]\n\t" \
  "ds_read_b128 v[100:103], v164 offset:24832\n\t" \
  "v_mfma_f32_16x16x32_bf16 v[132:135], a[36:39], v[32:35], v[132:135]\n\t" \
  "buffer_store_dwordx4 v[148:151], v169, %[ru], s42 offen sc1\n\t" \
  "v_mfma_f32_16x16x32_bf16 v[136:139], a[32:35], v[36:39], v[136:139]\n\t" \
  "ds_read_b128 v[104:107], v165 offset:16640\n\t" \
  "v_mfma_f32_16x16x32_bf16 v[140:143], a[36:39], v[36:39], v[140:143]\n\t" \
  "ds_read_b128 v[108:111], v165 offset:24832\n\t" \
  "v_mfma_f32_16x16x32_bf16 v[128:131], a[40:43], v[40:43], v[128:131]\n\t" \
  "s_add_i32 m0, s41, 2048\n\t" \
  "v_mfma_f32_16x16x32_bf16 v[132:135], a[44:47], v[40:43], v[132:135]\n\t" \
  "buffer_load_dwordx4 v162, %[rx], s40 offen lds\n\t" \
  "v_mfma_f32_16x16x32_bf16 v[136:139], a[40:43], v[44:47], v[136:139]\n\t" \
  "ds_read_b128 v[112:115], v166 offset:16640\n\t" \
  "v_mfma_f32_16x16x32_bf16 v[140:143], a[44:47], v[44:47], v[140:143]\n\t" \
  "ds_read_b128 v[116:119], v166 offset:24832\n\t" \
  "v_mfma_f32_16x16x32_bf16 v[128:131], a[48:51], v[48:51], v[128:131]\n\t" \
  "s_add_i32 m0, s41, 3072\n\t" \
  "v_mfma_f32_16x16x32_bf16 v[132:135], a[52:55], v[48:51], v[132:135]\n\t" \
  "buffer_load_dwordx4 v163, %[rx], s40 offen lds\n\t" \
  "v_mfma_f32_16x16x32_bf16 v[136:139], a[48:51], v[52:55], v[136:139]\n\t" \
  "ds_read_b128 v[120:123], v167 offset:16640\n\t" \
  "v_mfma_f32_16x16x32_bf16 v[140:143], a[52:55], v[52:55], v[140:143]\n\t" \
  "ds_read_b128 v[124:127], v167 offset:24832\n\t" \
  "v_mfma_f32_16x16x32_bf16 v[128:131], a[56:59], v[56:59], v[128:131]\n\t" \
  "v_mfma_f32_16x16x32_bf16 v[132:135], a[60:63], v[56:59], v[132:135]\n\t" \
  "v_mfma_f32_16x16x32_bf16 v[136:139], a[56:59], v[60:63], v[136:139]\n\t" \
  "v_mfma_f32_16x16x32_bf16 v[140:143], a[60:63], v[60:63], v[140:143]\n\t" \
  "s_cmp_le_u32 s44, 6\n\t" \
  "s_waitcnt vmcnt(30) lgkmcnt(0)\n\t" \
  "v_mfma_f32_16x16x32_bf16 v[128:131], a[64:67], v[64:67], v[128:131]\n\t" \
  "s_cbranch_scc1 31f\n\t" \
  "41:\n\t" \
  "s_barrier\n\t" \
  "v_mfma_f32_16x16x32_bf16 v[132:135], a[68:71], v[64:67], v[132:135]\n\t" \
  "s_add_i32 s76, s40, 512\n\t" \
  "v_mfma_f32_16x16x32_bf16 v[136:139], a[64:67], v[68:71], v[136:139]\n\t" \
  "ds_read_b128 v[0:3], v164 offset:32768\n\t" \
  "v_mfma_f32_16x16x32_bf16 v[140:143], a[68:71], v[68:71], v[140:143]\n\t" \
  "ds_read_b128 v[4:7], v164 offset:40960\n\t" \
  "v_mfma_f32_16x16x32_bf16 v[128:131], a[72:75], v[72:75], v[128:131]\n\t" \
  "s_add_i32 m0, s41, 16384\n\t" \
  "v_mfma_f32_16x16x32_bf16 v[132:135], a[76:79], v[72:75], v[132:135]\n\t" \
  "buffer_load_dwordx4 v160, %[rx], s76 offen lds\n\t" \
  "v_mfma_f32_16x16x32_bf16 v[136:139], a[72:75], v[76:79], v[136:139]\n\t" \
  "ds_read_b128 v[8:11], v165 offset:32768\n\t" \
  "v_mfma_f32_16x16x32_bf16 v[140:143], a[76:79], v[76:79], v[140:143]\n\t" \
  "ds_read_b128 v[12:15], v165 offset:40960\n\t" \
  "v_mfma_f32_16x16x32_bf16 v[128:131], a[80:83], v[80:83], v[128:131]\n\t" \
  "ds_read_b128 v[16:19], v166 offset:32768\n\t" \
  "v_mfma_f32_16x16x32_bf16 v[132:135], a[84:87], v[80:83], v[132:135]\n\t" \
  "v_mfma_f32_16x16x32_bf16 v[136:139], a[80:83], v[84:87], v[136:139]\n\t" \
  "ds_read_b128 v[20:23], v166 offset:40960\n\t" \
  "v_mfma_f32_16x16x32_bf16 v[140:143], a[84:87], v[84:87], v[140:143]\n\t" \
  "ds_read_b128 v[24:27], v167 offset:32768\n\t" \
  "v_mfma_f32_16x16x32_bf16 v[128:131], a[88:91], v[88:91], v[128:131]\n\t" \
  "s_add_i32 m0, s41, 17408\n\t" \
  "v_mfma_f32_16x16x32_bf16 v[132:135], a[92:95], v[88:91], v[132:135]\n\t" \
  "buffer_load_dwordx4 v161, %[rx], s76 offen lds\n\t" \
  "v_mfma_f32_16x16x32_bf16 v[136:139], a[88:91], v[92:95], v[136:139]\n\t" \
  "ds_read_b128 v[28:31], v167 offset:40960\n\t" \
  "v_mfma_f32_16x16x32_bf16 v[140:143], a[92:95], v[92:95], v[140:143]\n\t" \
  "ds_read_b128 v[32:35], v164 offset:33024\n\t" \
  "v_mfma_f32_16x16x32_bf16 v[128:131], a[96:99], v[96:99], v[128:131]\n\t" \
  "ds_read_b128 v[36:39], v164 offset:41216\n\t" \
  "v_mfma_f32_16x16x32_bf16 v[132:135], a[100:103], v[96:99], v[132:135]\n\t" \
  "v_mfma_f32_16x16x32_bf16 v[136:139], a[96:99], v[100:103], v[136:139]\n\t" \
  "ds_read_b128 v[40:43], v165 offset:33024\n\t" \
  "v_mfma_f32_16x16x32_bf16 v[140:143], a[100:103], v[100:103], v[140:143]\n\t" \
  "ds_read_b128 v[44:47], v165 offset:41216\n\t" \
  "v_mfma_f32_16x16x32_bf16 v[128:131], a[104:107], v[104:107], v[128:131]\n\t" \
  "s_add_i32 m0, s41, 18432\n\t" \
  "v_mfma_f32_16x16x32_bf16 v[132:135], a[108:111], v[104:107], v[132:135]\n\t" \
  "buffer_load_dwordx4 v162, %[rx], s76 offen lds\n\t" \
  "v_mfma_f32_16x16x32_bf16 v[136:139], a[104:107], v[108:111], v[136:139]\n\t" \
  "ds_read_b128 v[48:51], v166 offset:33024\n\t" \
  "v_mfma_f32_16x16x32_bf16 v[140:143], a[108:111], v[108:111], v[140:143]\n\t" \
  "ds_read_b128 v[52:55], v166 offset:41216\n\t" \
  "v_mfma_f32_16x16x32_bf16 v[128:131], a[112:115], v[112:115], v[128:131]\n\t" \
  "s_add_i32 m0, s41, 19456\n\t" \
  "v_mfma_f32_16x16x32_bf16 v[132:135], a[116:119], v[112:115], v[132:135]\n\t" \
  "buffer_load_dwordx4 v163, %[rx], s76 offen lds\n\t" \
  "v_mfma_f32_16x16x32_bf16 v[136:139], a[112:115], v[116:119], v[136:139]\n\t" \
  "ds_read_b128 v[56:59], v167 offset:33024\n\t" \
  "v_mfma_f32_16x16x32_bf16 v[140:143], a[116:119], v[116:119], v[140:143]\n\t" \
  "ds_read_b128 v[60:63], v167 offset:41216\n\t" \
  "v_mfma_f32_16x16x32_bf16 v[128:131], a[120:123], v[120:123], v[128:131]\n\t" \
  "v_mfma_f32_16x16x32_bf16 v[132:135], a[124:127], v[120:123], v[132:135]\n\t" \
  "v_mfma_f32_16x16x32_bf16 v[136:139], a[120:123], v[124:127], v[136:139]\n\t" \
  "v_mfma_f32_16x16x32_bf16 v[140:143], a[124:127], v[124:127], v[140:143]\n\t" \
  "s_cmp_le_u32 s44, 6\n\t" \
  "s_waitcnt vmcnt(29) lgkmcnt(0)\n\t" \
  "v_mfma_f32_16x16x32_bf16 v[128:131], a[128:131], v[0:3], v[128:131]\n\t" \
  "s_cbranch_scc1 32f\n\t" \
  "42:\n\t" \
  "s_barrier\n\t" \
  "v_mfma_f32_16x16x32_bf16 v[132:135], a[132:135], v[0:3], v[132:135]\n\t" \
  "s_add_i32 s76, s40, 1024\n\t" \
  "v_mfma_f32_16x16x32_bf16 v[136:139], a[128:131], v[4:7], v[136:139]\n\t" \
  "ds_read_b128 v[64:67], v164 offset:49152\n\t" \
  "v_mfma_f32_16x16x32_bf16 v[140:143], a[132:135], v[4:7], v[140:143]\n\t" \
  "ds_read_b128 v[68:71], v164 offset:57344\n\t" \
  "v_mfma_f32_16x16x32_bf16 v[128:131], a[136:139], v[8:11], v[128:131]\n\t" \
  "s_add_i32 m0, s41, 32768\n\t" \
  "v_mfma_f32_16x16x32_bf16 v[132:135], a[140:143], v[8:11], v[132:135]\n\t" \
  "buffer_load_dwordx4 v160, %[rx], s76 offen lds\n\t" \
  "v_mfma_f32_16x16x32_bf16 v[136:139], a[136:139], v[12:15], v[136:139]\n\t" \
  "ds_read_b128 v[72:75], v165 offset:49152\n\t" \
  "v_mfma_f32_16x16x32_bf16 v[140:143], a[140:143], v[12:15], v[140:143]\n\t" \
  "ds_read_b128 v[76:79], v165 offset:57344\n\t" \
  "v_mfma_f32_16x16x32_bf16 v[128:131], a[144:147], v[16:19], v[128:131]\n\t" \
  "ds_read_b128 v[80:83], v166 offset:49152\n\t" \
  "v_mfma_f32_16x16x32_bf16 v[132:135], a[148:151], v[16:19], v[132:135]\n\t" \
  "v_mfma_f32_16x16x32_bf16 v[136:139], a[144:147], v[20:23], v[136:139]\n\t" \
  "ds_read_b128 v[84:87], v166 offset:57344\n\t" \
  "v_mfma_f32_16x16x32_bf16 v[140:143], a[148:151], v[20:23], v[140:143]\n\t" \
  "ds_read_b128 v[88:91], v167 offset:49152\n\t" \
  "v_mfma_f32_16x16x32_bf16 v[128:131], a[152:155], v[24:27], v[128:131]\n\t" \
  "s_add_i32 m0, s41, 33792\n\t" \
  "v_mfma_f32_16x16x32_bf16 v[132:135], a[156:159], v[24:27], v[132:135]\n\t" \
  "buffer_load_dwordx4 v161, %[rx], s76 offen lds\n\t" \
  "v_mfma_f32_16x16x32_bf16 v[136:139], a[152:155], v[28:31], v[136:139]\n\t" \
  "ds_read_b128 v[92:95], v167 offset:57344\n\t" \
  "v_mfma_f32_16x16x32_bf16 v[140:143], a[156:159], v[28:31], v[140:143]\n\t" \
  "ds_read_b128 v[96:99], v164 offset:49408\n\t" \
  "v_mfma_f32_16x16x32_bf16 v[128:131], a[160:163], v[32:35], v[128:131]\n\t" \
  "ds_read_b128 v[100:103], v164 offset:57600\n\t" \
  "v_mfma_f32_16x16x32_bf16 v[132:135], a[164:167], v[32:35], v[132:135]\n\t" \
  "v_mfma_f32_16x16x32_bf16 v[136:139], a[160:163], v[36:39], v[136:139]\n\t" \
  "ds_read_b128 v[104:107], v165 offset:49408\n\t" \
  "v_mfma_f32_16x16x32_bf16 v[140:143], a[164:167], v[36:39], v[140:143]\n\t" \
  "ds_read_b128 v[108:111], v165 offset:57600\n\t" \
  "v_mfma_f32_16x16x32_bf16 v[128:131], a[168:171], v[40:43], v[128:131]\n\t" \
  "s_add_i32 m0, s41, 34816\n\t" \
  "v_mfma_f32_16x16x32_bf16 v[132:135], a[172:175], v[40:43], v[132:135]\n\t" \
  "buffer_load_dwordx4 v162, %[rx], s76 offen lds\n\t" \
  "v_mfma_f32_16x16x32_bf16 v[136:139], a[168:171], v[44:47], v[136:139]\n\t" \
  "ds_read_b128 v[112:115], v166 offset:49408\n\t" \
  "v_mfma_f32_16x16x32_bf16 v[140:143], a[172:175], v[44:47], v[140:143]\n\t" \
  "ds_read_b128 v[116:119], v166 offset:57600\n\t" \
  "v_mfma_f32_16x16x32_bf16 v[128:131], a[176:179], v[48:51], v[128:131]\n\t" \
  "s_add_i32 m0, s41, 35840\n\t" \
  "v_mfma_f32_16x16x32_bf16 v[132:135], a[180:183], v[48:51], v[132:135]\n\t" \
  "buffer_load_dwordx4 v163, %[rx], s76 offen lds\n\t" \
  "v_mfma_f32_16x16x32_bf16 v[136:139], a[176:179], v[52:55], v[136:139]\n\t" \
  "ds_read_b128 v[120:123], v167 offset:49408\n\t" \
  "v_mfma_f32_16x16x32_bf16 v[140:143], a[180:183], v[52:55], v[140:143]\n\t" \
  "ds_read_b128 v[124:127], v167 offset:57600\n\t" \
  "v_mfma_f32_16x16x32_bf16 v[128:131], a[184:187], v[56:59], v[128:131]\n\t" \
  "s_add_i32 s78, s44, -2\n\t" \
  "v_mfma_f32_16x16x32_bf16 v[132:135], a[188:191], v[56:59], v[132:135]\n\t" \
  "s_max_i32 s78, s78, s46\n\t" \
  "v_mfma_f32_16x16x32_bf16 v[136:139], a[184:187], v[60:63], v[136:139]\n\t" \
  "v_mov_b32 v156, s78\n\t" \
  "v_mfma_f32_16x16x32_bf16 v[140:143], a[188:191], v[60:63], v[140:143]\n\t" \
  "s_cmp_le_u32 s44, 6\n\t" \
  "s_waitcnt vmcnt(27) lgkmcnt(0)\n\t" \
  "v_mfma_f32_16x16x32_bf16 v[128:131], a[192:195], v[64:67], v[128:131]\n\t" \
  "s_cbranch_scc1 33f\n\t" \
  "43:\n\t" \
  "s_barrier\n\t" \
  "v_xor_b32 v164, 0x10000, v164\n\t" \
  "v_mfma_f32_16x16x32_bf16 v[132:135], a[196:199], v[64:67], v[132:135]\n\t" \
  "s_add_i32 s76, s40, 1536\n\t" \
  "buffer_store_dword v156, v171, %[ru], 0 offen sc1\n\t" \
  "v_mfma_f32_16x16x32_bf16 v[136:139], a[192:195], v[68:71], v[136:139]\n\t" \
  "v_xor_b32 v165, 0x10000, v165\n\t" \
  "ds_read_b128 v[0:3], v164 offset:0\n\t" \
  "v_mfma_f32_16x16x32_bf16 v[140:143], a[196:199], v[68:71], v[140:143]\n\t" \
  "v_xor_b32 v166, 0x10000, v166\n\t" \
  "ds_read_b128 v[4:7], v164 offset:8192\n\t" \
  "v_mfma_f32_16x16x32_bf16 v[128:131], a[200:203], v[72:75], v[128:131]\n\t" \
  "s_add_i32 m0, s41, 49152\n\t" \
  "v_xor_b32 v167, 0x10000, v167\n\t" \
  "v_mfma_f32_16x16x32_bf16 v[132:135], a[204:207], v[72:75], v[132:135]\n\t" \
  "buffer_load_dwordx4 v160, %[rx], s76 offen lds\n\t" \
  "v_mfma_f32_16x16x32_bf16 v[136:139], a[200:203], v[76:79], v[136:139]\n\t" \
  "ds_read_b128 v[8:11], v165 offset:0\n\t" \
  "v_mfma_f32_16x16x32_bf16 v[140:143], a[204:207], v[76:79], v[140:143]\n\t" \
  "ds_read_b128 v[12:15], v165 offset:8192\n\t" \
  "v_mfma_f32_16x16x32_bf16 v[128:131], a[208:211], v[80:83], v[128:131]\n\t" \
  "ds_read_b128 v[16:19], v166 offset:0\n\t" \
  "v_mfma_f32_16x16x32_bf16 v[132:135], a[212:215], v[80:83], v[132:135]\n\t" \
  "v_mfma_f32_16x16x32_bf16 v[136:139], a[208:211], v[84:87], v[136:139]\n\t" \
  "ds_read_b128 v[20:23], v166 offset:8192\n\t" \
  "v_mfma_f32_16x16x32_bf16 v[140:143], a[212:215], v[84:87], v[140:143]\n\t" \
  "ds_read_b128 v[24:27], v167 offset:0\n\t" \
  "v_mfma_f32_16x16x32_bf16 v[128:131], a[216:219], v[88:91], v[128:131]\n\t" \
  "s_add_i32 m0, s41, 50176\n\t" \
  "ds_read_b128 v[28:31], v167 offset:8192\n\t" \
  "v_mfma_f32_16x16x32_bf16 v[132:135], a[220:223], v[88:91], v[132:135]\n\t" \
  "buffer_load_dwordx4 v161, %[rx], s76 offen lds\n\t" \
  "v_mfma_f32_16x16x32_bf16 v[136:139], a[216:219], v[92:95], v[136:139]\n\t" \
  "ds_read_b128 v[32:35], v164 offset:256\n\t" \
  "v_mfma_f32_16x16x32_bf16 v[140:143], a[220:223], v[92:95], v[140:143]\n\t" \
  "ds_read_b128 v[36:39], v164 offset:8448\n\t" \
  "v_mfma_f32_16x16x32_bf16 v[128:131], a[224:227], v[96:99], v[128:131]\n\t" \
  "ds_read_b128 v[40:43], v165 offset:256\n\t" \
  "v_mfma_f32_16x16x32_bf16 v[132:135], a[228:231], v[96:99], v[132:135]\n\t" \
  "v_mfma_f32_16x16x32_bf16 v[136:139], a[224:227], v[100:103], v[136:139]\n\t" \
  "ds_read_b128 v[44:47], v165 offset:8448\n\t" \
  "v_mfma_f32_16x16x32_bf16 v[140:143], a[228:231], v[100:103], v[140:143]\n\t" \
  "ds_read_b128 v[48:51], v166 offset:256\n\t" \
  "v_mfma_f32_16x16x32_bf16 v[128:131], a[232:235], v[104:107], v[128:131]\n\t" \
  "s_add_i32 m0, s41, 51200\n\t" \
  "v_mfma_f32_16x16x32_bf16 v[132:135], a[236:239], v[104:107], v[132:135]\n\t" \
  "buffer_load_dwordx4 v162, %[rx], s76 offen lds\n\t" \
  "v_mfma_f32_16x16x32_bf16 v[136:139], a[232:235], v[108:111], v[136:139]\n\t" \
  "ds_read_b128 v[52:55], v166 offset:8448\n\t" \
  "v_mfma_f32_16x16x32_bf16 v[140:143], a[236:239], v[108:111], v[140:143]\n\t" \
  "ds_read_b128 v[56:59], v167 offset:256\n\t" \
  "v_mfma_f32_16x16x32_bf16 v[128:131], a[240:243], v[112:115], v[128:131]\n\t" \
  "s_add_i32 m0, s41, 52224\n\t" \
  "v_mfma_f32_16x16x32_bf16 v[132:135], a[244:247], v[112:115], v[132:135]\n\t" \
  "buffer_load_dwordx4 v163, %[rx], s76 offen lds\n\t" \
  "v_mfma_f32_16x16x32_bf16 v[136:139], a[240:243], v[116:119], v[136:139]\n\t" \
  "ds_read_b128 v[60:63], v167 offset:8448\n\t" \
  "v_mfma_f32_16x16x32_bf16 v[140:143], a[244:247], v[116:119], v[140:143]\n\t" \
  "s_add_i32 s44, s44, 1\n\t" \
  "s_mov_b32 s42, s43\n\t" \
  "v_mfma_f32_16x16x32_bf16 v[128:131], a[248:251], v[120:123], v[128:131]\n\t" \
  "s_add_i32 s43, s43, 0x40000\n\t" \
  "s_add_i32 s40, s40, 0x10000\n\t" \
  "v_mfma_f32_16x16x32_bf16 v[132:135], a[252:255], v[120:123], v[132:135]\n\t" \
  "s_xor_b32 s41, s41, 0x10000\n\t" \
  "v_mfma_f32_16x16x32_bf16 v[136:139], a[248:251], v[124:127], v[136:139]\n\t" \
  "v_cvt_pk_bf16_f32 v144, v128, v129\n\t" \
  "v_cvt_pk_bf16_f32 v145, v130, v131\n\t" \
  "v_mfma_f32_16x16x32_bf16 v[140:143], a[252:255], v[124:127], v[140:143]\n\t" \
  "v_cvt_pk_bf16_f32 v146, v132, v133\n\t" \
  "v_cvt_pk_bf16_f32 v147, v134, v135\n\t" \
  "s_cmp_eq_u32 s44, 64\n\t" \
  "s_cbranch_scc1 98f\n\t" \
  "s_cmp_le_u32 s44, 6\n\t" \
  "s_branch 20b\n\t" \
  "30:\n\t" \
  "s_waitcnt vmcnt(24)\n\t" \
  "s_branch 40b\n\t" \
  "31:\n\t" \
  "s_waitcnt vmcnt(24)\n\t" \
  "s_branch 41b\n\t" \
  "32:\n\t" \
  "s_waitcnt vmcnt(6)\n\t" \
  "s_barrier\n\t" \
  "s_add_i32 s46, s44, -1\n\t" \
  "v_mov_b32 v156, s46\n\t" \
  "buffer_store_dword v156, v171, %[ru], 0 offen sc1\n\t" \
  "s_branch 42b\n\t" \
  "33:\n\t" \
  "s_waitcnt vmcnt(24)\n\t" \
  "s_branch 43b\n\t" \
  "93:\n\t" \
  "ds_read_b128 v[0:3], v164 offset:0\n\t" \
  "ds_read_b128 v[4:7], v164 offset:8192\n\t" \
  "ds_read_b128 v[8:11], v165 offset:0\n\t" \
  "ds_read_b128 v[12:15], v165 offset:8192\n\t" \
  "ds_read_b128 v[16:19], v166 offset:0\n\t" \
  "ds_read_b128 v[20:23], v166 offset:8192\n\t" \
  "ds_read_b128 v[24:27], v167 offset:0\n\t" \
  "ds_read_b128 v[28:31], v167 offset:8192\n\t" \
  "ds_read_b128 v[32:35], v164 offset:256\n\t" \
  "ds_read_b128 v[36:39], v164 offset:8448\n\t" \
  "ds_read_b128 v[40:43], v165 offset:256\n\t" \
  "ds_read_b128 v[44:47], v165 offset:8448\n\t" \
  "ds_read_b128 v[48:51], v166 offset:256\n\t" \
  "ds_read_b128 v[52:55], v166 offset:8448\n\t" \
  "ds_read_b128 v[56:59], v167 offset:256\n\t" \
  "ds_read_b128 v[60:63], v167 offset:8448\n\t" \
  "v_mov_b64 v[152:153], -1\n\t" \
  "v_mov_b64 v[154:155], -1\n\t" \
  "s_cmp_le_u32 s44, 6\n\t" \
  "20:\n\t" \
  "s_waitcnt vmcnt(28) lgkmcnt(0)\n\t" \
  "v_mfma_f32_16x16x32_bf16 v[128:131], a[0:3], v[0:3], 0\n\t" \
  "s_cbranch_scc1 30f\n\t" \
  "40:\n\t" \
  "s_barrier\n\t" \
  "v_cvt_pk_bf16_f32 v148, v136, v137\n\t" \
  "v_cvt_pk_bf16_f32 v149, v138, v139\n\t" \
  "v_mfma_f32_16x16x32_bf16 v[132:135], a[4:7], v[0:3], 0\n\t" \
  "v_cvt_pk_bf16_f32 v150, v140, v141\n\t" \
  "v_cvt_pk_bf16_f32 v151, v142, v143\n\t" \
  "v_mfma_f32_16x16x32_bf16 v[136:139], a[0:3], v[4:7], 0\n\t" \
  "ds_read_b128 v[64:67], v164 offset:16384\n\t" \
  "v_mfma_f32_16x16x32_bf16 v[140:143], a[4:7], v[4:7], 0\n\t" \
  "ds_read_b128 v[68:71], v164 offset:24576\n\t" \
  "v_mfma_f32_16x16x32_bf16 v[128:131], a[8:11], v[8:11], v[128:131]\n\t" \
  "ds_read_b128 v[72:75], v165 offset:16384\n\t" \
  "v_mfma_f32_16x16x32_bf16 v[132:135], a[12:15], v[8:11], v[132:135]\n\t" \
  "s_add_i32 m0, s41, 0\n\t" \
  "v_mfma_f32_16x16x32_bf16 v[136:139], a[8:11], v[12:15], v[136:139]\n\t" \
  "buffer_load_dwordx4 v160, %[rx], s40 offen lds\n\t" \
  "v_mfma_f32_16x16x32_bf16 v[140:143], a[12:15], v[12:15], v[140:143]\n\t" \
  "ds_read_b128 v[76:79], v165 offset:24576\n\t" \
  "v_mfma_f32_16x16x32_bf16 v[128:131], a[16:19], v[16:19], v[128:131]\n\t" \
  "ds_read_b128 v[80:83], v166 offset:16384\n\t" \
  "v_mfma_f32_16x16x32_bf16 v[132:135], a[20:23], v[16:19], v[132:135]\n\t" \
  "ds_read_b128 v[84:87], v166 offset:24576\n\t" \
  "v_mfma_f32_16x16x32_bf16 v[136:139], a[16:19], v[20:23], v[136:139]\n\t" \
  "buffer_store_dwordx4 v[144:147], v168, %[ru], s42 offen sc1\n\t" \
  "v_mfma_f32_16x16x32_bf16 v[140:143], a[20:23], v[20:23], v[140:143]\n\t" \
  "ds_read_b128 v[88:91], v167 offset:16384\n\t" \
  "v_mfma_f32_16x16x32_bf16 v[128:131], a[24:27], v[24:27], v[128:131]\n\t" \
  "ds_read_b128 v[92:95], v167 offset:24576\n\t" \
  "v_mfma_f32_16x16x32_bf16 v[132:135], a[28:31], v[24:27], v[132:135]\n\t" \
  "s_add_i32 m0, s41, 1024\n\t" \
  "v_mfma_f32_16x16x32_bf16 v[136:139], a[24:27], v[28:31], v[136:139]\n\t" \
  "buffer_load_dwordx4 v161, %[rx], s40 offen lds\n\t" \
  "v_mfma_f32_16x16x32_bf16 v[140:143], a[28:31], v[28:31], v[140:143]\n\t" \
  "ds_read_b128 v[96:99], v164 offset:16640\n\t" \
  "v_mfma_f32_16x16x32_bf16 v[128:131], a[32:35], v[32:35], v[128:131]\n\t" \
  "ds_read_b128 v[100:103], v164 offset:24832\n\t" \
  "v_mfma_f32_16x16x32_bf16 v[132:135], a[36:39], v[32:35], v[132:135]\n\t" \
  "ds_read_b128 v[104:107], v165 offset:16640\n\t" \
  "v_mfma_f32_16x16x32_bf16 v[136:139], a[32:35], v[36:39], v[136:139]\n\t" \
  "buffer_store_dwordx4 v[148:151], v169, %[ru], s42 offen sc1\n\t" \
  "v_mfma_f32_16x16x32_bf16 v[140:143], a[36:39], v[36:39], v[140:143]\n\t" \
  "ds_read_b128 v[108:111], v165 offset:24832\n\t" \
  "v_mfma_f32_16x16x32_bf16 v[128:131], a[40:43], v[40:43], v[128:131]\n\t" \
  "ds_read_b128 v[112:115], v166 offset:16640\n\t" \
  "v_mfma_f32_16x16x32_bf16 v[132:135], a[44:47], v[40:43], v[132:135]\n\t" \
  "s_add_i32 m0, s41, 2048\n\t" \
  "v_mfma_f32_16x16x32_bf16 v[136:139], a[40:43], v[44:47], v[136:139]\n\t" \
  "buffer_load_dwordx4 v162, %[rx], s40 offen lds\n\t" \
  "v_mfma_f32_16x16x32_bf16 v[140:143], a[44:47], v[44:47], v[140:143]\n\t" \
  "ds_read_b128 v[116:119], v166 offset:24832\n\t" \
  "v_mfma_f32_16x16x32_bf16 v[128:131], a[48:51], v[48:51], v[128:131]\n\t" \
  "ds_read_b128 v[120:123], v167 offset:16640\n\t" \
  "v_mfma_f32_16x16x32_bf16 v[132:135], a[52:55], v[48:51], v[132:135]\n\t" \
  "s_add_i32 m0, s41, 3072\n\t" \
  "v_mfma_f32_16x16x32_bf16 v[136:139], a[48:51], v[52:55], v[136:139]\n\t" \
  "buffer_load_dwordx4 v163, %[rx], s40 offen lds\n\t" \
  "v_mfma_f32_16x16x32_bf16 v[140:143], a[52:55], v[52:55], v[140:143]\n\t" \
  "ds_read_b128 v[124:127], v167 offset:24832\n\t" \
  "v_mfma_f32_16x16x32_bf16 v[128:131], a[56:59], v[56:59], v[128:131]\n\t" \
  "v_mfma_f32_16x16x32_bf16 v[132:135], a[60:63], v[56:59], v[132:135]\n\t" \
  "v_mfma_f32_16x16x32_bf16 v[136:139], a[56:59], v[60:63], v[136:139]\n\t" \
  "v_mfma_f32_16x16x32_bf16 v[140:143], a[60:63], v[60:63], v[140:143]\n\t" \
  "s_cmp_le_u32 s44, 6\n\t" \
  "s_waitcnt vmcnt(30) lgkmcnt(0)\n\t" \
  "v_mfma_f32_16x16x32_bf16 v[128:131], a[64:67], v[64:67], v[128:131]\n\t" \
  "s_cbranch_scc1 31f\n\t" \
  "41:\n\t" \
  "s_barrier\n\t" \
  "v_mfma_f32_16x16x32_bf16 v[132:135], a[68:71], v[64:67], v[132:135]\n\t" \
  "s_add_i32 s76, s40, 512\n\t" \
  "v_mfma_f32_16x16x32_bf16 v[136:139], a[64:67], v[68:71], v[136:139]\n\t" \
  "ds_read_b128 v[0:3], v164 offset:32768\n\t" \
  "v_mfma_f32_16x16x32_bf16 v[140:143], a[68:71], v[68:71], v[140:143]\n\t" \
  "ds_read_b128 v[4:7], v164 offset:40960\n\t" \
  "v_mfma_f32_16x16x32_bf16 v[128:131], a[72:75], v[72:75], v[128:131]\n\t" \
  "ds_read_b128 v[8:11], v165 offset:32768\n\t" \
  "v_mfma_f32_16x16x32_bf16 v[132:135], a[76:79], v[72:75], v[132:135]\n\t" \
  "s_add_i32 m0, s41, 16384\n\t" \
  "v_mfma_f32_16x16x32_bf16 v[136:139], a[72:75], v[76:79], v[136:139]\n\t" \
  "buffer_load_dwordx4 v160, %[rx], s76 offen lds\n\t" \
  "v_mfma_f32_16x16x32_bf16 v[140:143], a[76:79], v[76:79], v[140:143]\n\t" \
  "ds_read_b128 v[12:15], v165 offset:40960\n\t" \
  "v_mfma_f32_16x16x32_bf16 v[128:131], a[80:83], v[80:83], v[128:131]\n\t" \
  "ds_read_b128 v[16:19], v166 offset:32768\n\t" \
  "v_mfma_f32_16x16x32_bf16 v[132:135], a[84:87], v[80:83], v[132:135]\n\t" \
  "ds_read_b128 v[20:23], v166 offset:40960\n\t" \
  "v_mfma_f32_16x16x32_bf16 v[136:139], a[80:83], v[84:87], v[136:139]\n\t" \
  "v_mfma_f32_16x16x32_bf16 v[140:143], a[84:87], v[84:87], v[140:143]\n\t" \
  "ds_read_b128 v[24:27], v167 offset:32768\n\t" \
  "v_mfma_f32_16x16x32_bf16 v[128:131], a[88:91], v[88:91], v[128:131]\n\t" \
  "ds_read_b128 v[28:31], v167 offset:40960\n\t" \
  "v_mfma_f32_16x16x32_bf16 v[132:135], a[92:95], v[88:91], v[132:135]\n\t" \
  "s_add_i32 m0, s41, 17408\n\t" \
  "v_mfma_f32_16x16x32_bf16 v[136:139], a[88:91], v[92:95], v[136:139]\n\t" \
  "buffer_load_dwordx4 v161, %[rx], s76 offen lds\n\t" \
  "v_mfma_f32_16x16x32_bf16 v[140:143], a[92:95], v[92:95], v[140:143]\n\t" \
  "ds_read_b128 v[32:35], v164 offset:33024\n\t" \
  "v_mfma_f32_16x16x32_bf16 v[128:131], a[96:99], v[96:99], v[128:131]\n\t" \
  "ds_read_b128 v[36:39], v164 offset:41216\n\t" \
  "v_mfma_f32_16x16x32_bf16 v[132:135], a[100:103], v[96:99], v[132:135]\n\t" \
  "ds_read_b128 v[40:43], v165 offset:33024\n\t" \
  "v_mfma_f32_16x16x32_bf16 v[136:139], a[96:99], v[100:103], v[136:139]\n\t" \
  "v_mfma_f32_16x16x32_bf16 v[140:143], a[100:103], v[100:103], v[140:143]\n\t" \
  "ds_read_b128 v[44:47], v165 offset:41216\n\t" \
  "v_mfma_f32_16x16x32_bf16 v[128:131], a[104:107], v[104:107], v[128:131]\n\t" \
  "ds_read_b128 v[48:51], v166 offset:33024\n\t" \
  "v_mfma_f32_16x16x32_bf16 v[132:135], a[108:111], v[104:107], v[132:135]\n\t" \
  "s_add_i32 m0, s41, 18432\n\t" \
  "v_mfma_f32_16x16x32_bf16 v[136:139], a[104:107], v[108:111], v[136:139]\n\t" \
  "buffer_load_dwordx4 v162, %[rx], s76 offen lds\n\t" \
  "v_mfma_f32_16x16x32_bf16 v[140:143], a[108:111], v[108:111], v[140:143]\n\t" \
  "ds_read_b128 v[52:55], v166 offset:41216\n\t" \
  "v_mfma_f32_16x16x32_bf16 v[128:131], a[112:115], v[112:115], v[128:131]\n\t" \
  "ds_read_b128 v[56:59], v167 offset:33024\n\t" \
  "v_mfma_f32_16x16x32_bf16 v[132:135], a[116:119], v[112:115], v[132:135]\n\t" \
  "s_add_i32 m0, s41, 19456\n\t" \
  "v_mfma_f32_16x16x32_bf16 v[136:139], a[112:115], v[116:119], v[136:139]\n\t" \
  "buffer_load_dwordx4 v163, %[rx], s76 offen lds\n\t" \
  "v_mfma_f32_16x16x32_bf16 v[140:143], a[116:119], v[116:119], v[140:143]\n\t" \
  "ds_read_b128 v[60:63], v167 offset:41216\n\t" \
  "v_mfma_f32_16x16x32_bf16 v[128:131], a[120:123], v[120:123], v[128:131]\n\t" \
  "v_mfma_f32_16x16x32_bf16 v[132:135], a[124:127], v[120:123], v[132:135]\n\t" \
  "v_mfma_f32_16x16x32_bf16 v[136:139], a[120:123], v[124:127], v[136:139]\n\t" \
  "v_mfma_f32_16x16x32_bf16 v[140:143], a[124:127], v[124:127], v[140:143]\n\t" \
  "s_cmp_le_u32 s44, 6\n\t" \
  "s_waitcnt vmcnt(29) lgkmcnt(0)\n\t" \
  "v_mfma_f32_16x16x32_bf16 v[128:131], a[128:131], v[0:3], v[128:131]\n\t" \
  "s_cbranch_scc1 32f\n\t" \
  "42:\n\t" \
  "s_barrier\n\t" \
  "v_mfma_f32_16x16x32_bf16 v[132:135], a[132:135], v[0:3], v[132:135]\n\t" \
  "s_add_i32 s76, s40, 1024\n\t" \
  "v_mfma_f32_16x16x32_bf16 v[136:139], a[128:131], v[4:7], v[136:139]\n\t" \
  "ds_read_b128 v[64:67], v164 offset:49152\n\t" \
  "v_mfma_f32_16x16x32_bf16 v[140:143], a[132:135], v[4:7], v[140:143]\n\t" \
  "ds_read_b128 v[68:71], v164 offset:57344\n\t" \
  "v_mfma_f32_16x16x32_bf16 v[128:131], a[136:139], v[8:11], v[128:131]\n\t" \
  "ds_read_b128 v[72:75], v165 offset:49152\n\t" \
  "v_mfma_f32_16x16x32_bf16 v[132:135], a[140:143], v[8:11], v[132:135]\n\t" \
  "s_add_i32 m0, s41, 32768\n\t" \
  "v_mfma_f32_16x16x32_bf16 v[136:139], a[136:139], v[12:15], v[136:139]\n\t" \
  "buffer_load_dwordx4 v160, %[rx], s76 offen lds\n\t" \
  "v_mfma_f32_16x16x32_bf16 v[140:143], a[140:143], v[12:15], v[140:143]\n\t" \
  "ds_read_b128 v[76:79], v165 offset:57344\n\t" \
  "v_mfma_f32_16x16x32_bf16 v[128:131], a[144:147], v[16:19], v[128:131]\n\t" \
  "ds_read_b128 v[80:83], v166 offset:49152\n\t" \
  "v_mfma_f32_16x16x32_bf16 v[132:135], a[148:151], v[16:19], v[132:135]\n\t" \
  "ds_read_b128 v[84:87], v166 offset:57344\n\t" \
  "v_mfma_f32_16x16x32_bf16 v[136:139], a[144:147], v[20:23], v[136:139]\n\t" \
  "v_mfma_f32_16x16x32_bf16 v[140:143], a[148:151], v[20:23], v[140:143]\n\t" \
  "ds_read_b128 v[88:91], v167 offset:49152\n\t" \
  "v_mfma_f32_16x16x32_bf16 v[128:131], a[152:155], v[24:27], v[128:131]\n\t" \
  "ds_read_b128 v[92:95], v167 offset:57344\n\t" \
  "v_mfma_f32_16x16x32_bf16 v[132:135], a[156:159], v[24:27], v[132:135]\n\t" \
  "s_add_i32 m0, s41, 33792\n\t" \
  "v_mfma_f32_16x16x32_bf16 v[136:139], a[152:155], v[28:31], v[136:139]\n\t" \
  "buffer_load_dwordx4 v161, %[rx], s76 offen lds\n\t" \
  "v_mfma_f32_16x16x32_bf16 v[140:143], a[156:159], v[28:31], v[140:143]\n\t" \
  "ds_read_b128 v[96:99], v164 offset:49408\n\t" \
  "v_mfma_f32_16x16x32_bf16 v[128:131], a[160:163], v[32:35], v[128:131]\n\t" \
  "ds_read_b128 v[100:103], v164 offset:57600\n\t" \
  "v_mfma_f32_16x16x32_bf16 v[132:135], a[164:167], v[32:35], v[132:135]\n\t" \
  "ds_read_b128 v[104:107], v165 offset:49408\n\t" \
  "v_mfma_f32_16x16x32_bf16 v[136:139], a[160:163], v[36:39], v[136:139]\n\t" \
  "v_mfma_f32_16x16x32_bf16 v[140:143], a[164:167], v[36:39], v[140:143]\n\t" \
  "ds_read_b128 v[108:111], v165 offset:57600\n\t" \
  "v_mfma_f32_16x16x32_bf16 v[128:131], a[168:171], v[40:43], v[128:131]\n\t" \
  "ds_read_b128 v[112:115], v166 offset:49408\n\t" \
  "v_mfma_f32_16x16x32_bf16 v[132:135], a[172:175], v[40:43], v[132:135]\n\t" \
  "s_add_i32 m0, s41, 34816\n\t" \
  "v_mfma_f32_16x16x32_bf16 v[136:139], a[168:171], v[44:47], v[136:139]\n\t" \
  "buffer_load_dwordx4 v162, %[rx], s76 offen lds\n\t" \
  "v_mfma_f32_16x16x32_bf16 v[140:143], a[172:175], v[44:47], v[140:143]\n\t" \
  "ds_read_b128 v[116:119], v166 offset:57600\n\t" \
  "v_mfma_f32_16x16x32_bf16 v[128:131], a[176:179], v[48:51], v[128:131]\n\t" \
  "ds_read_b128 v[120:123], v167 offset:49408\n\t" \
  "v_mfma_f32_16x16x32_bf16 v[132:135], a[180:183], v[48:51], v[132:135]\n\t" \
  "s_add_i32 m0, s41, 35840\n\t" \
  "v_mfma_f32_16x16x32_bf16 v[136:139], a[176:179], v[52:55], v[136:139]\n\t" \
  "buffer_load_dwordx4 v163, %[rx], s76 offen lds\n\t" \
  "v_mfma_f32_16x16x32_bf16 v[140:143], a[180:183], v[52:55], v[140:143]\n\t" \
  "ds_read_b128 v[124:127], v167 offset:57600\n\t" \
  "v_mfma_f32_16x16x32_bf16 v[128:131], a[184:187], v[56:59], v[128:131]\n\t" \
  "s_add_i32 s78, s44, -2\n\t" \
  "v_mfma_f32_16x16x32_bf16 v[132:135], a[188:191], v[56:59], v[132:135]\n\t" \
  "s_max_i32 s78, s78, s46\n\t" \
  "v_mfma_f32_16x16x32_bf16 v[136:139], a[184:187], v[60:63], v[136:139]\n\t" \
  "v_mov_b32 v156, s78\n\t" \
  "v_mfma_f32_16x16x32_bf16 v[140:143], a[188:191], v[60:63], v[140:143]\n\t" \
  "s_cmp_le_u32 s44, 6\n\t" \
  "s_waitcnt vmcnt(27) lgkmcnt(0)\n\t" \
  "v_mfma_f32_16x16x32_bf16 v[128:131], a[192:195], v[64:67], v[128:131]\n\t" \
  "s_cbranch_scc1 33f\n\t" \
  "43:\n\t" \
  "s_barrier\n\t" \
  "v_xor_b32 v164, 0x10000, v164\n\t" \
  "v_mfma_f32_16x16x32_bf16 v[132:135], a[196:199], v[64:67], v[132:135]\n\t" \
  "s_add_i32 s76, s40, 1536\n\t" \
  "buffer_store_dword v156, v171, %[ru], 0 offen sc1\n\t" \
  "v_mfma_f32_16x16x32_bf16 v[136:139], a[192:195], v[68:71], v[136:139]\n\t" \
  "v_xor_b32 v165, 0x10000, v165\n\t" \
  "ds_read_b128 v[0:3], v164 offset:0\n\t" \
  "v_mfma_f32_16x16x32_bf16 v[140:143], a[196:199], v[68:71], v[140:143]\n\t" \
  "v_xor_b32 v166, 0x10000, v166\n\t" \
  "ds_read_b128 v[4:7], v164 offset:8192\n\t" \
  "v_mfma_f32_16x16x32_bf16 v[128:131], a[200:203], v[72:75], v[128:131]\n\t" \
  "v_xor_b32 v167, 0x10000, v167\n\t" \
  "ds_read_b128 v[8:11], v165 offset:0\n\t" \
  "v_mfma_f32_16x16x32_bf16 v[132:135], a[204:207], v[72:75], v[132:135]\n\t" \
  "s_add_i32 m0, s41, 49152\n\t" \
  "ds_read_b128 v[12:15], v165 offset:8192\n\t" \
  "v_mfma_f32_16x16x32_bf16 v[136:139], a[200:203], v[76:79], v[136:139]\n\t" \
  "buffer_load_dwordx4 v160, %[rx], s76 offen lds\n\t" \
  "v_mfma_f32_16x16x32_bf16 v[140:143], a[204:207], v[76:79], v[140:143]\n\t" \
  "ds_read_b128 v[16:19], v166 offset:0\n\t" \
  "v_mfma_f32_16x16x32_bf16 v[128:131], a[208:211], v[80:83], v[128:131]\n\t" \
  "ds_read_b128 v[20:23], v166 offset:8192\n\t" \
  "v_mfma_f32_16x16x32_bf16 v[132:135], a[212:215], v[80:83], v[132:135]\n\t" \
  "ds_read_b128 v[24:27], v167 offset:0\n\t" \
  "v_mfma_f32_16x16x32_bf16 v[136:139], a[208:211], v[84:87], v[136:139]\n\t" \
  "v_mfma_f32_16x16x32_bf16 v[140:143], a[212:215], v[84:87], v[140:143]\n\t" \
  "ds_read_b128 v[28:31], v167 offset:8192\n\t" \
  "v_mfma_f32_16x16x32_bf16 v[128:131], a[216:219], v[88:91], v[128:131]\n\t" \
  "ds_read_b128 v[32:35], v164 offset:256\n\t" \
  "v_mfma_f32_16x16x32_bf16 v[132:135], a[220:223], v[88:91], v[132:135]\n\t" \
  "s_add_i32 m0, s41, 50176\n\t" \
  "v_mfma_f32_16x16x32_bf16 v[136:139], a[216:219], v[92:95], v[136:139]\n\t" \
  "buffer_load_dwordx4 v161, %[rx], s76 offen lds\n\t" \
  "v_mfma_f32_16x16x32_bf16 v[140:143], a[220:223], v[92:95], v[140:143]\n\t" \
  "ds_read_b128 v[36:39], v164 offset:8448\n\t" \
  "v_mfma_f32_16x16x32_bf16 v[128:131], a[224:227], v[96:99], v[128:131]\n\t" \
  "ds_read_b128 v[40:43], v165 offset:256\n\t" \
  "v_mfma_f32_16x16x32_bf16 v[132:135], a[228:231], v[96:99], v[132:135]\n\t" \
  "ds_read_b128 v[44:47], v165 offset:8448\n\t" \
  "v_mfma_f32_16x16x32_bf16 v[136:139], a[224:227], v[100:103], v[136:139]\n\t" \
  "v_mfma_f32_16x16x32_bf16 v[140:143], a[228:231], v[100:103], v[140:143]\n\t" \
  "ds_read_b128 v[48:51], v166 offset:256\n\t" \
  "v_mfma_f32_16x16x32_bf16 v[128:131], a[232:235], v[104:107], v[128:131]\n\t" \
  "ds_read_b128 v[52:55], v166 offset:8448\n\t" \
  "v_mfma_f32_16x16x32_bf16 v[132:135], a[236:239], v[104:107], v[132:135]\n\t" \
  "s_add_i32 m0, s41, 51200\n\t" \
  "v_mfma_f32_16x16x32_bf16 v[136:139], a[232:235], v[108:111], v[136:139]\n\t" \
  "buffer_load_dwordx4 v162, %[rx], s76 offen lds\n\t" \
  "v_mfma_f32_16x16x32_bf16 v[140:143], a[236:239], v[108:111], v[140:143]\n\t" \
  "ds_read_b128 v[56:59], v167 offset:256\n\t" \
  "v_mfma_f32_16x16x32_bf16 v[128:131], a[240:243], v[112:115], v[128:131]\n\t" \
  "ds_read_b128 v[60:63], v167 offset:8448\n\t" \
  "v_mfma_f32_16x16x32_bf16 v[132:135], a[244:247], v[112:115], v[132:135]\n\t" \
  "s_add_i32 m0, s41, 52224\n\t" \
  "v_mfma_f32_16x16x32_bf16 v[136:139], a[240:243], v[116:119], v[136:139]\n\t" \
  "buffer_load_dwordx4 v163, %[rx], s76 offen lds\n\t" \
  "v_mfma_f32_16x16x32_bf16 v[140:143], a[244:247], v[116:119], v[140:143]\n\t" \
  "s_add_i32 s44, s44, 1\n\t" \
  "s_mov_b32 s42, s43\n\t" \
  "v_mfma_f32_16x16x32_bf16 v[128:131], a[248:251], v[120:123], v[128:131]\n\t" \
  "s_add_i32 s43, s43, 0x40000\n\t" \
  "s_add_i32 s40, s40, 0x10000\n\t" \
  "v_mfma_f32_16x16x32_bf16 v[132:135], a[252:255], v[120:123], v[132:135]\n\t" \
  "s_xor_b32 s41, s41, 0x10000\n\t" \
  "v_mfma_f32_16x16x32_bf16 v[136:139], a[248:251], v[124:127], v[136:139]\n\t" \
  "v_cvt_pk_bf16_f32 v144, v128, v129\n\t" \
  "v_cvt_pk_bf16_f32 v145, v130, v131\n\t" \
  "v_mfma_f32_16x16x32_bf16 v[140:143], a[252:255], v[124:127], v[140:143]\n\t" \
  "v_cvt_pk_bf16_f32 v146, v132, v133\n\t" \
  "v_cvt_pk_bf16_f32 v147, v134, v135\n\t" \
  "s_cmp_eq_u32 s44, 64\n\t" \
  "s_cbranch_scc1 98f\n\t" \
  "s_cmp_le_u32 s44, 6\n\t" \
  "s_branch 20b\n\t" \
  "30:\n\t" \
  "s_waitcnt vmcnt(24)\n\t" \
  "s_branch 40b\n\t" \
  "31:\n\t" \
  "s_waitcnt vmcnt(24)\n\t" \
  "s_branch 41b\n\t" \
  "32:\n\t" \
  "s_waitcnt vmcnt(6)\n\t" \
  "s_barrier\n\t" \
  "s_add_i32 s46, s44, -1\n\t" \
  "v_mov_b32 v156, s46\n\t" \
  "buffer_store_dword v156, v171, %[ru], 0 offen sc1\n\t" \
  "s_branch 42b\n\t" \
  "33:\n\t" \
  "s_waitcnt vmcnt(24)\n\t" \
  "s_branch 43b\n\t" \
  "98:\n\t" \
  "s_nop 7\n\t" \
  "v_cvt_pk_bf16_f32 v148, v136, v137\n\t" \
  "v_cvt_pk_bf16_f32 v149, v138, v139\n\t" \
  "v_cvt_pk_bf16_f32 v150, v140, v141\n\t" \
  "v_cvt_pk_bf16_f32 v151, v142, v143\n\t" \
  "buffer_store_dwordx4 v[144:147], v168, %[ru], s42 offen sc1\n\t" \
  "buffer_store_dwordx4 v[148:151], v169, %[ru], s42 offen sc1\n\t" \
  "v_mov_b32 v156, 63\n\t" \
  "s_waitcnt vmcnt(0) lgkmcnt(0)\n\t" \
  "s_barrier\n\t" \
  "buffer_store_dword v156, v171, %[ru], 0 offen sc1\n\t" \
  "99:\n\t" \
  ""

#define V10(x) "v" #x "0", "v" #x "1", "v" #x "2", "v" #x "3", "v" #x "4", "v" #x "5", "v" #x "6", "v" #x "7", "v" #x "8", "v" #x "9"
#define A10(x) "a" #x "0", "a" #x "1", "a" #x "2", "a" #x "3", "a" #x "4", "a" #x "5", "a" #x "6", "a" #x "7", "a" #x "8", "a" #x "9"
#define V90 V10(), V10(1), V10(2), V10(3), V10(4), V10(5), V10(6), V10(7), V10(8)
#define S22 "s56", "s57", "s58", "s59", "s76", "s77", "s78", "s79", "s80", "s81", "s82", "s83", "s84", "s85", "s86", "s87", "s88", "s89", \
  "s90", "s91", "s92", "s93", "s94", "s95", "s96", "s97"
#define STEP_PP
#define STEP_CL
#define STEP_PV
#define CB_PV
#define STEPS(BODY, ...) asm volatile(BODY : "=&{v[92:99]}"(X), "+{s[40:55]}"(P) STEP_PV \
  : "{v[120:151]}"(W.v[0]), "{v[152:183]}"(W.v[1]), "{v[184:215]}"(W.v[2]), "{v[216:247]}"(W.v[3]), "{v[100:115]}"(C), [rh] "s"(rh), [wv] "s"(w), [ru] "s"(rum) STEP_PP __VA_ARGS__ \
  : "memory", "vcc", "scc", S22, STEP_CL V90, "v90", "v91", "v116", "v117", "v118")
__device__ __forceinline__ void keep_a(const Wts& W) {
#pragma unroll
  for (int i = 0; i < 32; ++i) asm volatile("" :: "a"(W.a[i]));
}

__device__ __forceinline__ void steps_init(u32x16& P, int h, int w, unsigned place) {
#pragma unroll
  for (int i = 0; i < 16; ++i) P[i] = 0;
  P[2] = 1; P[3] = 1420; P[14] = H_MEM + (w >> 1) * CHK + (w & 1) * 4096 + (h & RHO) * 1024; P[15] = place;
}
__device__ __forceinline__ void steps_enter(u32x16& P, Clk& ck, bool dead) {
  ck.sync();
  P[2] = (P[2] & 3u) | (dead ? 4u : 0u); P[6] = ck.c2t << 15; P[11] = ck.ce; P[12] = ck.t2c; P[13] = ck.te;
#pragma unroll
  for (int i = 0; i < 16; ++i) P[i] = __builtin_amdgcn_readfirstlane(P[i]);
}

__device__ __forceinline__ void role_qk(const ChainArgs& a, int h, char* smem, const int w, Gate& G) {
  int lane = opaque_tid(0);
  const int b = h; (void)b;
  u64* prof = a.prof; (void)prof;
  PDECL(L_PRF, 4);
  const float CS = 0.08838834764831845f * 1.4426950408889634f;
  GAS unsigned* abortw = (GAS unsigned*)(a.sync + SY_ABORT);
  bool dead = false;
  Wts W;
  Clk ck; ck.init();
  load_weights<0>(a, h, w, lane, smem, W);
  if (!G.pass(a.sync)) return;
  PZERO;
  const Sel sel = sel_wait(a.sync, abortw, lane, dead, true, CT_PR + h, 0, CT_PR + 32 + h, 1);
  const __amdgpu_buffer_rsrc_t rh = rsrc_hot(a.sync, sel), rum = rsrc_u(a.sync);
#pragma unroll
  for (int i = 0; i < 2; ++i) *(u32x4*)(smem + L_KX + (2 * w + i) * 1024 + lane * 16) = ld16(rum, OFF_KX + h * 8192 + (2 * w + i) * 1024 + lane * 16);
  auto pz_o = [&](int l) { return (l & 1) ? OOR : 2 * CHK + h * 256 + w * 128 + (l & 8) * 8 + (l >> 4) * 16 + (l & 6) * 2; };
  auto z_at = [&](int l, int half) { return ((l & 6) >> 1) == 2 * w + ((h >> 2) & 1) && ((l >> 4) & 1) == half ? 1.f : 0.f; };
  u32x16 C, P;
  unsigned C16;
  {
    const int r = lane & 15, g = lane >> 4;
    const unsigned l0 = (unsigned)(uintptr_t)(LAS char*)smem;
    C[0] = lane * 16; C[1] = l0 + L_MEM + lane * 16; C[2] = l0 + L_WTS + w * 4096 + lane * 16; C[3] = C[2] + 65536;
    C[4] = (lane & 2) ? 0xEEEE : 0x4444;
    C[5] = l0 + L_XCH + (16 * w + 2 * (r >> 2) + (r & 1)) * 32 + g * 8 + ((r >> 1) & 1) * 4;
    C[6] = l0 + L_XK + (32 * w + r) * 16 + g * 4;
    C[7] = w < 2 ? l0 + L_XCH + (8 * g + (r >> 2)) * 32 + (r & 3) * 8 : SY_CT + (CT_PR + 32 * (w - 2) + h) * 128;
    C[8] = l0 + L_XK + (8 * g + (r >> 2)) * 16 + (r & 1) * 8;
    C[9] = w < 3 ? (lane == 0 ? H_FLG + h * 128 : OOR) :
           lane < 32 ? (h >> 4) * CHK + (h & 15) * 512 + lane * 16 : (lane < 48 ? 2 * CHK + h * 256 + (lane - 32) * 16 : OOR);
    C[10] = H_TR + (lane & 31) * 128; C[11] = l0 + L_PRF + w * 1024 + (lane >> 6);
    C[12] = (h >> 4) * CHK + (h & 15) * 512 + w * 256 + (r >> 2) * 64 + g * 16 + (r & 3) * 4; C[13] = pz_o(lane);
    C[14] = __float_as_uint(z_at(lane, 0)); C[15] = __float_as_uint(z_at(lane, 1));
    C16 = l0 + L_KX + (r >> 3) * 4096 + (g * 16 + 2 * (r & 7)) * 16;
    if (w == 3) C[12] = lane == 0 ? H_ANN + h * 128 : OOR;
    if (w == 2) {
#pragma unroll
      for (int i = 0; i < 4; ++i) C[12 + i] = 4 * g + i <= r ? 0u : 0xff800000u;
      C16 = H_PX + (h >> 2) * CHK + (h & 3) * 2048 + lane * 16;
    }
    asm volatile("" : "+v"(C), "+v"(C16));
  }
  SSTAMP(96 + h);
  steps_init(P, h, w, (w == 2 ? OFF_QX : OFF_KX) + h * 8192);
  unsigned spins = 0;
  while (true) {
    u32x8 X; u32x16 pv; (void)pv;
    steps_enter(P, ck, dead);
    STEPS(QK_BODY, , [cs] "s"(CS), "{v119}"(C16));
    keep_a(W);
    const unsigned res = __builtin_amdgcn_readfirstlane(P[2]), s = __builtin_amdgcn_readfirstlane(P[4]);
    lane = opaque_tid(0);
    if (res & 8u) {
      auto sw = [](float v, int k) { return __int_as_float(k == 0 ? __builtin_amdgcn_mov_dpp(__float_as_int(v), 0xB1, 0xf, 0xf, false) :
                                                           k == 1 ? __builtin_amdgcn_mov_dpp(__float_as_int(v), 0x4E, 0xf, 0xf, false) :
                                                                    __builtin_amdgcn_mov_dpp(__float_as_int(v), 0x141, 0xf, 0xf, false)); };
      float d = fmaxf(__uint_as_float(X[4]), 0.f);
      d = fmaxf(d, sw(d, 0)); d = fmaxf(d, sw(d, 1)); d = fmaxf(d, sw(d, 2));
      const float x = __builtin_amdgcn_exp2f(__uint_as_float(X[4]) - d), y = __builtin_amdgcn_exp2f(-d), xn = sw(x, 0);
      float z = fmaf(__uint_as_float(X[0]), y, x);
      z += sw(z, 0); z += sw(z, 1); z += sw(z, 2);
      z = __builtin_amdgcn_rcpf(z);
      const unsigned pm = nosent((lane & 8) ? pk2(z_at(lane, 0) * y * z, z_at(lane, 1) * y * z) : pk2(x * z, xn * z));
      if (!(__uint_as_float(X[5]) < 1.0e18f))
        __builtin_amdgcn_raw_buffer_store_b32(pm, rh, pz_o(lane), H_P + ((s - 1) & 3) * 3 * CHK, ST_AUX);
    }
    if (res & 64u) break;
    if (res & 32u) ctr_wait(ctr(a, CT_PR + 32 * (w - 2) + h), min(s + w - 1, (unsigned)NSEG), abortw, lane, dead);
    if (res & 17u) {
      spins |= 63u;
      if (dead || poll_fail(spins, abortw, lane)) dead = true;
    }
  }
  if (w == 2 && lane == 0) st4(rh, H_FLG + h * 128, NSEG - 1);
  PDUMP(0);
}

__device__ __forceinline__ void role_vo(const ChainArgs& a, int h, char* smem, const int w, Gate& G) {
  int lane = opaque_tid(0);
  const int b = 32 + h; (void)b;
  u64* prof = a.prof; (void)prof;
  PDECL(L_PRF, 4);
  GAS unsigned* abortw = (GAS unsigned*)(a.sync + SY_ABORT);
  bool dead = false;
  Wts W;
  Clk ck; ck.init();
  load_weights<1>(a, h, w, lane, smem, W);
  if (!G.pass(a.sync)) return;
  PZERO;
  const Sel sel = sel_wait(a.sync, abortw, lane, dead, true);
  const __amdgpu_buffer_rsrc_t rh = rsrc_hot(a.sync, sel), rum = rsrc_u(a.sync);
  u32x16 C, P;
  {
    const int r = lane & 15, g = lane >> 4;
    const unsigned l0 = (unsigned)(uintptr_t)(LAS char*)smem;
#pragma unroll
    for (int i = 0; i < 16; ++i) C[i] = H_TR + (lane & 31) * 128;
    C[0] = lane * 16; C[1] = l0 + L_MEM + lane * 16; C[2] = l0 + L_WTS + w * 4096 + lane * 16; C[3] = C[2] + 65536;
    C[4] = (lane & 2) ? 0xEEEE : 0x4444;
    C[5] = l0 + L_XCH + (32 * w + r) * 16 + g * 4;
    C[6] = (lane & 8) ? 0xEEEE : 0x4444;
    C[7] = l0 + L_XCH + (32 * (g >> 1) + 8 * (g & 1) + (r >> 2)) * 16 + (r & 1) * 8;
    C[8] = (lane >> 5) * 2 * CHK + (lane & 31) * 16;
    C[11] = l0 + L_PRF + w * 1024 + (lane >> 6);
    asm volatile("" : "+v"(C));
  }
  SSTAMP(128 + h);
  steps_init(P, h, w, (16 * (h & 3) + 4 * w + (h >> 4)) * CHK + (h & 15) * 512);
  unsigned spins = 0;
  while (true) {
    u32x8 X; u32x16 pv; (void)pv;
    steps_enter(P, ck, dead);
    STEPS(VO_BODY, , [sel] "s"(sel[0]));
    keep_a(W);
    lane = opaque_tid(0);
    if (__builtin_amdgcn_readfirstlane(P[2]) & 64u) break;
    spins |= 63u;
    if (dead || poll_fail(spins, abortw, lane)) dead = true;
  }
  PDUMP(0);
}

__device__ __forceinline__ unsigned calibrate(const ChainArgs& a, int cal) {
  const __amdgpu_buffer_rsrc_t rs = mk_rsrc((void*)a.sync, SY_SET);
  unsigned best[2] = {1u << 30, 1u << 30};
  bool sure = false;
  for (int i = 0; i < CALR && !sure; ++i) {
#pragma unroll
    for (int hf = 0; hf < 2; ++hf) {
      u64 c0, c1;
      asm volatile("s_waitcnt vmcnt(0)\n\ts_memtime %0\n\ts_waitcnt lgkmcnt(0)" : "=s"(c0) :: "memory");
      unsigned v = __builtin_amdgcn_raw_buffer_load_b32(rs, 0, cal * PAGE + hf * 8192 + i * 256, LD_AUX);
      asm volatile("s_waitcnt vmcnt(0)\n\ts_memtime %0\n\ts_waitcnt lgkmcnt(0)" : "=s"(c1), "+v"(v) :: "memory");
      best[hf] = min(best[hf], (unsigned)(c1 - c0));
    }
    sure = i >= CALN && min(best[0], best[1]) < 560u && max(best[0], best[1]) - min(best[0], best[1]) > 120u;
  }
  return ((best[1] < best[0] ? 1u : 0u) ^ die_half()) + (sure ? 2u : 0u);
}
__device__ __forceinline__ void bg_drain() {
  asm volatile("s_waitcnt vmcnt(0)" ::: "memory");
  __builtin_amdgcn_s_barrier();
  asm volatile("" ::: "memory");
}

constexpr int LC_SUM = 65536, LC_TQ = LC_SUM + 8192, LC_OUT = 81920, LC_PRF = 98304;
__device__ __forceinline__ void role_cb(const ChainArgs& a, int ci, int par, char* smem, const int w, Gate& G) {
  int lane = opaque_tid(0);
  const int b = 64 + 32 * par + ci, j = ci >> 3; (void)b;
  u64* prof = a.prof; (void)prof;
  PDECL(LC_PRF, 8);
  GAS unsigned* abortw = (GAS unsigned*)(a.sync + SY_ABORT);
  bool dead = false;
  const unsigned l0 = (unsigned)(uintptr_t)(LAS char*)smem;
  const int tq = 2 * ci + par, cal = par ? 32 : ci;
  const bool cw = w == 0 && (par == 0 || ci == 3);
  unsigned m0 = 0, hs = 0;
  if (par && w == 1) {
    const unsigned x = __builtin_amdgcn_raw_buffer_load_b32(mk_rsrc((void*)a.sync, SY_SET), lane < (ci == 1 ? 32 : 16) ? (lane < 16 ? ci ^ 2 : 32) * PAGE + (lane & 1) * 8192 + (lane & 14) * 128 : OOR, 0, 0);
    asm volatile("" :: "v"(x));
  }
  if (cw) hs = calibrate(a, cal);
  __builtin_amdgcn_s_barrier();
  m0 = nosent(*(const unsigned*)(a.mem0 + (2 * (lane >> 4) + (lane & 1)) * DM + 16 * tq + (lane & 14)));
  Clk ck; ck.init();
  if (!G.pass(a.sync)) return;
  SSTAMP(0 + ci);
  if (cw && lane == 0) __hip_atomic_store(ctr(a, CT_CAL + cal), hs, __ATOMIC_RELAXED, __HIP_MEMORY_SCOPE_AGENT);
  if (b == 97 && w == 0) {
    unsigned spins = 0, v;
    while (true) {
      v = __hip_atomic_load(ctr(a, CT_CAL + min(lane, 32)), __ATOMIC_RELAXED, __HIP_MEMORY_SCOPE_AGENT);
      const int n0 = __builtin_popcountll(__builtin_amdgcn_ballot_w64((v ^ (lane & 1)) == 2u && lane < 33)), n1 = __builtin_popcountll(__builtin_amdgcn_ballot_w64((v ^ (lane & 1)) == 3u && lane < 33));
      const bool all = __all(v != SENT);
      const int d = n1 - n0 + 2 * __builtin_popcountll(__builtin_amdgcn_ballot_w64(((v ^ lane) & 1) && lane < 33)) - 33;
      if (max(n0, n1) >= 8 + 4 * min(n0, n1)) { v = lane ^ (n1 > n0); break; }
      if (all && abs(d) >= 13) v = lane ^ (d > 0);
      if (all || poll_fail(spins, abortw, lane)) break;
      __builtin_amdgcn_s_sleep(2);
    }
    const u64 m = __builtin_amdgcn_ballot_w64(((v ^ die_half()) & 1u) != 0 && lane < 33);
    if (lane == 0) __hip_atomic_store((GAS u64*)(a.sync + SY_SEL), m, __ATOMIC_RELAXED, __HIP_MEMORY_SCOPE_AGENT);
  }
  const Sel sel = sel_wait(a.sync, abortw, lane, dead);
  const __amdgpu_buffer_rsrc_t rh = rsrc_hot(a.sync, sel), rum = rsrc_u(a.sync), ro = mk_rsrc(a.out, 8u << 20);
  {
    SSTAMP(64 + ci);
    const unsigned s1v = ~(unsigned)(lane >> 6);
    const u32x4 sv = {s1v, s1v, s1v, s1v};
    st16(rh, (ci + 2) * CHK + par * 4096 + w * 1024 + lane * 16, sv);
    if (w == 1) st4(rh, H_MEM + (tq >> 5) * CHK + (tq & 31) * 256 + (4 * (lane >> 4) + (lane & 3)) * 16 + ((lane >> 2) & 3) * 4, m0);
#pragma unroll
    for (int i = 0; i < 4; ++i) st16(rum, um_slot(par, sel) + (2 * ci + (w >> 1)) * CHK + (w & 1) * 4096 + i * 1024 + lane * 16, sv);
    bg_drain();
    if (w == 0 && lane == 0) ctr_add(a, CT_INIT, 1u);
  }
  if (par) {
    while (__builtin_amdgcn_s_memrealtime() - G.entry < XS_T) __builtin_amdgcn_s_sleep(8);
    const __amdgpu_buffer_rsrc_t rx = mk_rsrc((void*)a.X, 4u << 20);
    unsigned x[8];
#pragma unroll
    for (int i = 0; i < 8; ++i) x[i] = __builtin_amdgcn_raw_buffer_load_b32(rx, (ci * 256 + w * 64 + lane) * XS_G, i * 8192 * XS_G, 0);
#pragma unroll
    for (int i = 0; i < 8; ++i) asm volatile("" :: "v"(x[i]));
  }
  ctr_wait(ctr(a, CT_INIT), 64u, abortw, lane, dead);
  lds_barrier();
  PZERO;
  SSTAMP(32 + ci);
  u32x16 C, P; u32x8 C2;
  {
    const int i = lane & 15, g = lane >> 4, tw = 2 * ci + (w >> 1);
    const unsigned mo = H_MEM + (tw >> 5) * CHK + (tw & 31) * 256 + (w & 1) * 128;
    C[0] = lane * 16; C[1] = (lane >> 5) * 2048 + (lane & 31) * 16;
    C[2] = ((i >> 3) ^ (j & 1)) * 512 + (i & 4) * 64 + g * 64 + (i & 3) * 16;
    C[3] = H_P + 2 * CHK + (8 * w + (((g & 2) + (i >> 3) + 4 * (g & 1)) ^ j)) * 256 + (i & 4) * 32 + (i & 3) * 16;
    C[4] = H_P + 2 * CHK + (8 * w + ((g + 4 * (i >> 3)) ^ j)) * 256 + (i & 4) * 32 + 64 + (i & 3) * 16;
    C[5] = (lane & 8) ? 0xEEEE : 0x4444; C[6] = (lane & 2) ? 0xEEEE : 0x4444;
    C[7] = l0 + LC_SUM + w * 1024 + lane * 4;
    C[8] = l0 + LC_SUM + (w >> 1) * 4096 + (2 * (w & 1) + (lane >> 5)) * 256 + ((2 * (lane & 1) + ((lane >> 3) & 1)) * 16 + 2 * (lane & 6) + 2 * ((lane >> 4) & 1)) * 4;
    C[9] = (lane & 1) ? OOR : mo + lane * 2;
    C[10] = (8 * w + (lane & 7)) * 128;
    C[11] = ((ci & 3) == w && lane == 0) ? 4096 + ci * 128 : OOR;
    C[12] = ((lane + 8 * w) & 31) * 128; C[13] = (ci & 3) == w ? C[12] : OOR;
    C[14] = 0;
    C[15] = l0 + LC_OUT + w * 4096 + (g * 16 + ((i + 4 * g) & 15)) * 16;
    C2[0] = l0 + LC_OUT + (((lane >> 1) & 2) + (w >> 1)) * 1024 + ((lane & 3) * 16 + ((8 * w + (lane >> 3) + 4 * (lane & 3)) & 15)) * 16;
    C2[1] = (8 * w + (lane >> 3)) * OUTW * 2 + DM * 2 + ci * 64 + (lane & 7) * 8;
    C2[2] = SY_CT + (CT_PR + 96 + 8 * w + (lane & 7)) * 128;
    C2[3] = lane < 8 ? mo + lane * 16 : OOR;
    C2[4] = (w | lane) == 0 ? ci * 128 : OOR;
    C2[5] = l0 + LC_TQ + 4 * w + (lane >> 6); C2[6] = 20000 + (lane >> 6); C2[7] = l0 + LC_TQ + (lane >> 6);
  }
#pragma unroll
  for (int i = 0; i < 16; ++i) P[i] = 0;
  ck.sync();
  P[0] = (2 * ci + (w >> 1)) * CHK + (w & 1) * 4096 + j * 512; P[1] = H_P + (w >> 1) * CHK + (w & 1) * 4096 + (j & 2) * 512;
  P[2] = (8 * w + j) * UXH + ux_tile(2 * ci); P[3] = H_PX + 2 * w * CHK + j * 2048; P[4] = par - 2; P[5] = P[7] = K_DEF * TU;
  P[6] = ck.c2t << 15; P[11] = ck.ce; P[12] = ck.t2c; P[13] = ck.te; P[15] = H_DLT + par * 4096;
#pragma unroll
  for (int i = 0; i < 16; ++i) P[i] = __builtin_amdgcn_readfirstlane(P[i]);
  u32x16 pv; (void)pv;
  asm volatile(CB_BODY : "+{s[40:55]}"(P) STEP_PV : "{v[224:239]}"(C), "{v[240:247]}"(C2) CB_PV, [rh] "s"(rh), [ru] "s"(rum), [ro] "s"(ro), [sel] "s"(sel[0])
               : "memory", "vcc", "scc", S22, V90, V10(9), V10(10), V10(11), V10(12), V10(13), V10(14), V10(15), V10(16), V10(17), V10(18), V10(19), V10(20), V10(21),
                 "v220", "v221", "v222", "v223", "s98", "s99", "s100", A10(), A10(1), A10(2), A10(3), A10(4), A10(5), "a60", "a61", "a62", "a63");
  lane = opaque_tid(0);
  PDUMP(2);
}

__device__ __forceinline__ void st16g(u16* p, u32x4 v, bool sc1) {
  if (sc1) asm volatile("global_store_dwordx4 %0, %1, off " OUTP "\n\ts_nop 1" :: "v"(p), "v"(v) : "memory");
  else *(u32x4*)p = v;
}
__device__ __forceinline__ u32x4 pk8(f32x4 a, f32x4 b) { return (u32x4){pk2(a[0], a[1]), pk2(a[2], a[3]), pk2(b[0], b[1]), pk2(b[2], b[3])}; }
__device__ __forceinline__ void role_p(const ChainArgs& a, int kind, int h, char* smem, const int w, Gate& G) {
  const int lane = opaque_tid(0), r = lane & 15, g = lane >> 4;
  const u32x4 rw = own_rsrc(kind == 0 ? a.Wq : (kind == 1 ? a.Wk : a.Wv), 8u << 20), rx = own_rsrc(a.X, 4u << 20);
  const unsigned lds0 = (unsigned)(uintptr_t)(LAS char*)smem + w * 4096;
  unsigned vw[4], vx[4];
#pragma unroll
  for (int i = 0; i < 4; ++i) {
    const int row = (w * 4 + i) * 4 + g, xr = 8 * w + 2 * i + (lane >> 5);
    vw[i] = row * HD * 2 + ((r ^ swz_b(row)) * 16);
    vx[i] = xr * DM * 2 + (((lane & 31) ^ (xr & 15)) * 16);
  }
  auto dma = [&](u32x4 rs, unsigned v, unsigned so, unsigned ld, bool nt = false) {
    if (nt) asm volatile("s_mov_b32 m0, %0\n\ts_nop 3\n\tbuffer_load_dwordx4 %1, %2, %3 offen nt lds" :: "s"(ld), "v"(v), "s"(rs), "s"(so) : "m0", "memory");
    else asm volatile("s_mov_b32 m0, %0\n\ts_nop 3\n\tbuffer_load_dwordx4 %1, %2, %3 offen lds" :: "s"(ld), "v"(v), "s"(rs), "s"(so) : "m0", "memory");
  };
  auto item = [&](int t) {
#pragma unroll
    for (int i = 0; i < 4; ++i) {
      if (t < 16) dma(rw, vw[i], (unsigned)((64 * (t ^ PREV) * HD + 128 * h) * 2), lds0 + (t & 7) * 16384 + i * 1024, (NTW >> 2) & 1);
      else dma(rx, vx[i], (unsigned)(((t - 16) >> 2) * 65536 + (t & 3) * 512), lds0 + (t & 7) * 16384 + i * 1024);
    }
  };
  Wts W;
#pragma unroll
  for (int i = 0; i < 32; ++i) asm volatile("" : "=a"(W.a[i]));
  if (kind < 2) while (__builtin_amdgcn_s_memrealtime() - G.entry < (kind ? K_DLY : Q_DLY)) __builtin_amdgcn_s_sleep(8);
#pragma unroll
  for (int t = 0; t < 8; ++t) item(t);
#pragma unroll
  for (int t = 0; t < 16; ++t) {
    if (t) wait_vm<24>(); else wait_vm<28>();
    lds_barrier();
    if (t) item(t + 7);
#pragma unroll
    for (int n = 0; n < 8; ++n) {
      const int row = 32 * (n >> 2) + 8 * g + 4 * (n & 1) + (r >> 2);
      const u32x2 u = __builtin_bit_cast(u32x2, __builtin_amdgcn_ds_read_tr16_b64_v4i16((LAS bf16x4*)(smem + (t & 7) * 16384 + row * 256 + (((4 * w + (r & 3)) ^ swz_b(row)) * 16) + 4 * (n & 2))));
      asm volatile("v_accvgpr_write_b32 a[%2], %0\n\tv_accvgpr_write_b32 a[%3], %1" :: "v"(u[0]), "v"(u[1]), "n"(16 * (t ^ PREV) + 2 * n), "n"(16 * (t ^ PREV) + 2 * n + 1));
    }
  }
  SSTAMP(160 + h);
  if (!G.pass(a.sync)) { wait_vm<0>(); return; }
  wait_vm<24>();
  lds_barrier();
  item(23);
  u32x16 C;
#pragma unroll
  for (int i = 0; i < 4; ++i) { C[i] = vx[i]; C[4 + i] = lds0 - w * 4096 + r * 512 + (((4 * i + g) ^ r) * 16); C[12 + i] = 0; }
  C[8] = lane * 16; C[9] = lane * 16 + 4096;
  C[10] = 0;
  C[11] = (w | lane) == 0 ? SY_CT + (CT_PR + 32 * kind + h) * 128 : OOR;
  u32x8 P = {131072u, lds0, 0x7f000000u, (unsigned)(OFF_QX + kind * (16u << 20) + h * 8192 + w * 1024), 0u, 0u, 0u, 0u};
#pragma unroll
  for (int i = 0; i < 8; ++i) P[i] = __builtin_amdgcn_readfirstlane(P[i]);
  asm volatile(P_BODY : "+{s[40:47]}"(P), "+{v[160:175]}"(C) : [rx] "s"(rx), [ru] "s"(rsrc_u(a.sync)), [wv] "s"(w), [pp] "s"(a.prof + 296000 + (32 * kind + h) * 16), [pq] "s"(a.prof + 300000 + (32 * kind + h) * 48)
               : "memory", "vcc", "scc", "m0", S22, STEP_CL V90, V10(9), V10(10), V10(11), V10(12), V10(13), V10(14), V10(15), "v176", "v177", "v178", "v179", "v180");
  keep_a(W);
}
#define BG_SLP 64
#define A2N 2
constexpr int A2_NCH = A2N;
__device__ __forceinline__ float blo(unsigned d) { return __uint_as_float(d << 16); }
__device__ __forceinline__ float bhi(unsigned d) { return __uint_as_float(d & 0xffff0000u); }
constexpr int A2_CSTR = 4160;
constexpr int A2_UOFF = 8 * A2_CSTR + 16 * 80;
constexpr int A2_WLDS = A2_UOFF + 64 * 80;
static_assert(4 * A2_WLDS + 256 <= CHAIN_LDS - 16, "lds");
__device__ __forceinline__ void a2_wave(const ChainArgs& a, int d0, int d1, char* smem, int w) {
  const int lane = opaque_tid(0);
  char* Wb = smem + w * A2_WLDS;
  char* U = Wb + A2_UOFF;
  const __amdgpu_buffer_rsrc_t rf = mk_rsrc((void*)a.Assm, 4u << 20);
  const __amdgpu_buffer_rsrc_t rkv = mk_rsrc((void*)a.kvT, 4u << 20);
  u32x4 hi[4], lo[4], uv[4];
  auto ld_h = [&](int d) {
#pragma unroll
    for (int i = 0; i < 4; ++i) {
      hi[i] = __builtin_amdgcn_raw_buffer_load_b128(rf, (unsigned)((255 - 64 * i - lane) * 16), (unsigned)(d * 4096), 2);
      lo[i] = __builtin_amdgcn_raw_buffer_load_b128(rf, (unsigned)(max(254 - 64 * i - lane, 0) * 16), (unsigned)(d * 4096), 2);
    }
  };
  auto ld_u = [&](int d) {
#pragma unroll
    for (int i = 0; i < 4; ++i) uv[i] = __builtin_amdgcn_raw_buffer_load_b128(rkv, (unsigned)((i * 64 + lane) * 16), (unsigned)(d * 4096), LD_AUX);
  };
  {
    GAS unsigned* abortw = (GAS unsigned*)(a.sync + SY_ABORT);
    bool dead = false;
    ctr_wait<BG_SLP>(ctr(a, CT_A1 + (min(d0, D_A - 1) >> 7)), d0 < d1 ? 8u : 0u, abortw, lane, dead);
  }
  ld_h(d0); ld_u(d0);
  if (lane < 24) *(f32x4*)(Wb + (lane / 3) * A2_CSTR + (256 + lane % 3) * 16) = vzero();
#pragma unroll
  for (int i = 0; i < 2; ++i) if (64 * i + lane < 80) *(f32x4*)(U - 16 * 80 + (64 * i + lane) * 16) = vzero();
  const int r = lane & 15, g = lane >> 4;
#pragma unroll 1
  for (int d = d0; d < d1; ++d) {
#pragma unroll
    for (int i = 0; i < 4; ++i) {
      u32x4 l = lo[i];
      if (i == 3 && lane == 63) l = (u32x4){0u, 0u, 0u, 0u};
      const unsigned src[8] = {l[0], l[1], l[2], l[3], hi[i][0], hi[i][1], hi[i][2], hi[i][3]};
      unsigned f[15];
#pragma unroll
      for (int x = 0; x < 15; ++x) {
        if ((x & 1) == 0) f[x] = __builtin_amdgcn_alignbit(src[7 - x / 2], src[7 - x / 2], 16);
        else f[x] = __builtin_amdgcn_perm(src[7 - (x + 1) / 2], src[7 - (x - 1) / 2], 0x07060100u);
      }
#pragma unroll
      for (int m = 0; m < 8; ++m) *(u32x4*)(Wb + m * A2_CSTR + (64 * i + lane) * 16) = (u32x4){f[7 - m], f[9 - m], f[11 - m], f[13 - m]};
    }
#pragma unroll
    for (int i = 0; i < 4; ++i) {
      const int ch = i * 64 + lane;
      *(u32x4*)(U + (ch >> 2) * 80 + (ch & 3) * 16) = uv[i];
    }
    const float Dd = bf2f(a.AD[d]);
    asm volatile("" ::: "memory");
    ld_h(min(d + 1, d1 - 1)); ld_u(min(d + 1, d1 - 1));
    f32x4 acc[4][2];
#pragma unroll
    for (int i = 0; i < 8; ++i) acc[i >> 1][i & 1] = vzero();
    const char* pa = Wb + (r & 7) * A2_CSTR + 2 * (2040 - (r & 8) + 8 * g) - 3 * 1024 - 32 - 64;
    const char* pb = U + (r - 1) * 80 + 16 * g;
    bf16x8 fa[2][8], fb[2][4];
    auto rd = [&](int b, int n, int k) {
      if (n < 4) fb[b][n] = *(const bf16x8*)(pb + 80 * (16 * n + 1 - k));
      else fa[b][n - 4] = *(const bf16x8*)(pa + 1024 * (3 - ((n - 4) >> 1)) + 32 * (1 - (n & 1)) + 64 * (1 - k));
    };
#pragma unroll
    for (int n = 0; n < 12; ++n) rd(0, n, 0);
#pragma unroll 1
    for (int k = 0; k < 16; k += 2) {
#pragma unroll
      for (int b = 0; b < 2; ++b) {
        int q = 0;
#pragma unroll
        for (int j = 0; j < 4; ++j)
#pragma unroll
          for (int i = j; i < 4; ++i)
#pragma unroll
            for (int mt = 0; mt < 2; ++mt) {
              asm volatile("v_mfma_f32_16x16x32_bf16 %0, %1, %2, %0" : "+v"(acc[i][mt]) : "v"(fa[b][2 * j + mt]), "v"(fb[b][i - j]));
              SB0;
              if (q < 12) rd(b ^ 1, q, 1);
              if (q == 12) { pa -= 64; pb -= 80; }
              ++q;
              SB0;
            }
      }
    }
    asm volatile("s_nop 7" ::: "memory");
#pragma unroll
    for (int i = 0; i < 8; ++i) asm volatile("" : "+v"(acc[i >> 1][i & 1]));
#pragma unroll
    for (int i = 0; i < 8; ++i) {
      char* pz = U + (16 * (i >> 1) + r) * 80 + 32 * (i & 1) + 8 * g;
      const u32x2 kvv = *(const u32x2*)pz;
      const f32x4 y = acc[i >> 1][i & 1];
      *(u32x2*)pz = (u32x2){pk2(fmaf(blo(kvv[0]), Dd, y[0]), fmaf(bhi(kvv[0]), Dd, y[1])), pk2(fmaf(blo(kvv[1]), Dd, y[2]), fmaf(bhi(kvv[1]), Dd, y[3]))};
    }
#pragma unroll
    for (int i = 0; i < 4; ++i) {
      const int ch = i * 64 + lane;
      st16s(*(const u32x4*)(U + (ch >> 2) * 80 + (ch & 3) * 16), rkv, (unsigned)(ch * 16), (unsigned)(d * 4096));
    }
  }
  wait_vm<0>();
  if (lane == 0 && d0 < d1) ctr_add(a, CT_A2 + (d0 >> 7), d1 - d0);
}

__device__ __forceinline__ void task_a1(const ChainArgs& a, int dp, int tq, char* smem, int wv) {
  const int lane = opaque_tid(0);
  const int dg = 2 * dp + (wv & 1), t0 = tq * 256 + (wv >> 1) * 128;
  const int d = dg * 64 + 8 * (lane & 7);
  const int tb = t0 + 8 * (lane >> 3);
  char* T = smem + wv * 16384;
  u32x4 kk[2][11], vv[2][8], sh[4];
#pragma unroll
  for (int i = 0; i < 4; ++i) sh[i] = *(const u32x4*)(a.Ashift + d * 4 + 8 * i);
  SB0;
#pragma unroll
  for (int gq = 0; gq < 2; ++gq) {
#pragma unroll
    for (int i = 0; i < 11; ++i) {
      const int t = tb + 64 * gq - 3 + i;
      kk[gq][i] = LDNT((const u32x4*)(a.Ak + (long)(t < 0 ? 0 : t) * D_A + d));
    }
#pragma unroll
    for (int i = 0; i < 8; ++i) vv[gq][i] = LDNT((const u32x4*)(a.Av + (long)(tb + 64 * gq + i) * D_A + d));
    SB0;
  }
#pragma unroll
  for (int i = 0; i < 3; ++i) if (tb == 0) kk[0][i] = (u32x4){0u, 0u, 0u, 0u};
#pragma unroll
  for (int gq = 0; gq < 2; ++gq) {
#pragma unroll
    for (int c = 0; c < 8; ++c) {
      const unsigned t01 = sh[c >> 1][2 * (c & 1)], t23 = sh[c >> 1][2 * (c & 1) + 1];
      const float s0 = blo(t01), s1 = bhi(t01), s2 = blo(t23), s3 = bhi(t23);
      float kf[11];
#pragma unroll
      for (int i = 0; i < 11; ++i) kf[i] = (c & 1) ? bhi(kk[gq][i][c >> 1]) : blo(kk[gq][i][c >> 1]);
      u32x4 o;
#pragma unroll
      for (int i = 0; i < 8; i += 2) {
        float r[2];
#pragma unroll
        for (int j = 0; j < 2; ++j)
          r[j] = fmaf(kf[i + j + 3], s0, fmaf(kf[i + j + 2], s1, fmaf(kf[i + j + 1], s2, kf[i + j] * s3))) * ((c & 1) ? bhi(vv[gq][i + j][c >> 1]) : blo(vv[gq][i + j][c >> 1]));
        o[i >> 1] = pk2(r[0], r[1]);
      }
      *(u32x4*)(T + (8 * (lane & 7) + c) * 256 + (((gq * 8 + (lane >> 3)) ^ (lane & 7)) * 16)) = o;
    }
    SB0;
  }
  const __amdgpu_buffer_rsrc_t rkv = mk_rsrc((void*)a.kvT, 4u << 20);
#pragma unroll
  for (int i = 0; i < 16; ++i) {
    const int row = 4 * i + (lane >> 4);
    const u32x4 o = *(const u32x4*)(T + row * 256 + (((lane & 15) ^ (row >> 3)) * 16));
    st16(rkv, (unsigned)(((dg * 64 + row) * S_LEN + t0 + (lane & 15) * 8) * 2), o);
  }
  bg_drain();
  if (opaque_tid(wv) == 0) ctr_add(a, CT_A1 + dp, 1u);
}

__device__ __forceinline__ void task_a3(const ChainArgs& a, int dp, int tq, char* smem, int wv) {
  const int lane = opaque_tid(0);
  const int dg = 2 * dp + (wv & 1), t0 = tq * 256 + (wv >> 1) * 128;
  const int d = dg * 64 + 8 * (lane & 7);
  const int tb = t0 + 8 * (lane >> 3);
  const __amdgpu_buffer_rsrc_t rz = mk_rsrc((void*)a.kvT, 4u << 20);
  u32x4 qq[16], zz[8][2];
#pragma unroll
  for (int i = 0; i < 16; ++i) qq[i] = LDNT((const u32x4*)(a.Aq + (long)(tb + 64 * (i >> 3) + (i & 7)) * D_A + d));
  char* T = smem + wv * 16384;
  {
    u32x4 zr[16];
    bool dead = false;
    ctr_wait<BG_SLP>(ctr(a, CT_A2 + dp), 128u, (GAS unsigned*)(a.sync + SY_ABORT), lane, dead);
#pragma unroll
    for (int i = 0; i < 16; ++i) zr[i] = ld16(rz, (unsigned)(((dg * 64 + 4 * i + (lane >> 4)) * S_LEN + t0 + (lane & 15) * 8) * 2));
#pragma unroll
    for (int i = 0; i < 16; ++i) {
      const int row = 4 * i + (lane >> 4);
      *(u32x4*)(T + row * 256 + (((lane & 15) ^ (row >> 3)) * 16)) = zr[i];
    }
  }
#pragma unroll
  for (int c = 0; c < 8; ++c)
#pragma unroll
    for (int gq = 0; gq < 2; ++gq) zz[c][gq] = *(const u32x4*)(T + (8 * (lane & 7) + c) * 256 + (((gq * 8 + (lane >> 3)) ^ (lane & 7)) * 16));
#pragma unroll
  for (int i = 0; i < 16; ++i) {
    u32x4 o;
#pragma unroll
    for (int c = 0; c < 8; c += 2) {
      const unsigned z0 = zz[c][i >> 3][(i >> 1) & 3], z1 = zz[c + 1][i >> 3][(i >> 1) & 3];
      o[c >> 1] = pk2(blo(qq[i][c >> 1]) * ((i & 1) ? bhi(z0) : blo(z0)), bhi(qq[i][c >> 1]) * ((i & 1) ? bhi(z1) : blo(z1)));
    }
    st16g(a.out + (long)(tb + 64 * (i >> 3) + (i & 7)) * OUTW + d, o, true);
  }
}

__device__ __forceinline__ void role_a(const ChainArgs& a, char* smem, int wv) {
  constexpr int N2 = 256 / A2_NCH;
  while (true) {
    asm volatile("" : "+s"(wv));
    int toff = CHAIN_LDS - 16;
    asm volatile("" : "+v"(toff));
    volatile LAS int* tslot = (volatile LAS int*)(smem + toff);
    if (opaque_tid(wv) == 0) *tslot = (int)__hip_atomic_fetch_add(ctr(a, CT_QUEUE), 1u, __ATOMIC_RELAXED, __HIP_MEMORY_SCOPE_AGENT) + 1;
    __syncthreads();
    const int t = __builtin_amdgcn_readfirstlane(*tslot);
    __syncthreads();
    if (t >= 128 + N2) break;
    if (t < 64) task_a1(a, t >> 3, t & 7, smem, wv);
    else if (t < 64 + N2) a2_wave(a, A2_NCH * (4 * (t - 64) + wv), A2_NCH * (4 * (t - 64) + wv + 1), smem, wv);
    else task_a3(a, (t - 64 - N2) >> 3, t & 7, smem, wv);
  }
}
__device__ __forceinline__ char* lds_base(char* smem, int off) { asm volatile("" : "+v"(off)); return smem + off; }
__device__ __forceinline__ void role_u(const ChainArgs& a, int h, char* smem, int w, Gate& G) {
  const int lane = opaque_tid(0);
  GAS unsigned* abortw = (GAS unsigned*)(a.sync + SY_ABORT);
  bool dead = false;
  char* sb = smem + w * 32768;
  const __amdgpu_buffer_rsrc_t rwo = mk_rsrc(a.Wo, 8u << 20);
  const unsigned lds0 = (unsigned)(uintptr_t)(LAS char*)sb;
  unsigned wo4[4];
#pragma unroll
  for (int i = 0; i < 4; ++i) { const int row = i * 4 + (lane >> 4); wo4[i] = row * DM * 2 + (((lane & 15) ^ swz_b(row)) * 16); }
  asm volatile("s_nop 4");
  auto stage = [&](int p) {
#pragma unroll
    for (int i = 0; i < 16; ++i)
      dma16(rwo, wo4[i & 3], (unsigned)(((128 * h + 64 * (p & 1) + 16 * (i >> 2)) * DM + 256 * w + 128 * (p >> 1)) * 2), lds0 + (p & 1) * 16384 + i * 1024, (NTW >> 3) & 1);
  };
  __builtin_amdgcn_s_waitcnt(0x0F70);
  u32x4 W[16][4];
  stage(0); stage(1);
#pragma unroll
  for (int p = 0; p < 4; ++p) {
    if (p < 3) wait_vm<16>(); else wait_vm<0>();
#pragma unroll
    for (int t = 0; t < 8; ++t) {
      const u32x8 f = tr_frag(sb + (p & 1) * 16384, t, lane);
      u32x4 f0 = {f[0], f[1], f[2], f[3]}, f1 = {f[4], f[5], f[6], f[7]};
      asm volatile("" : "+a"(f0), "+a"(f1));
      W[8 * (p >> 1) + t][2 * (p & 1)] = f0; W[8 * (p >> 1) + t][2 * (p & 1) + 1] = f1;
    }
    asm volatile("s_waitcnt lgkmcnt(0)" ::: "memory");
    if (p + 2 < 4) stage(p + 2);
  }
  SSTAMP(192 + h);
  if (!G.pass(a.sync)) return;
  const Sel sel = sel_wait(a.sync, abortw, lane, dead, true);
  const __amdgpu_buffer_rsrc_t ru = rsrc_u(a.sync);
  u32x4 V[2][8], pc[2];
  char* vl = lds_base(smem, lane * 16);
  volatile LAS int* fl = (volatile LAS int*)(smem + 16384);
  auto ld_v = [&](int s) {
#pragma unroll
    for (int i = 0; i < 2; ++i) pc[i] = __builtin_amdgcn_raw_buffer_load_b128(ru, (unsigned)(lane * 16 + i * 1024), (unsigned)(OFF_VX + (min(s, NSEG - 1) * NH + h) * 8192 + w * 2048), LD_AUX);
  };
  auto put_v = [&](int b) {
#pragma unroll
    for (int i = 0; i < 2; ++i) *(u32x4*)(vl + b * 8192 + w * 2048 + i * 1024) = pc[i];
  };
  auto signal = [&](int s) {
    st4(ru, (w | lane) == 0 ? SY_CT + (CT_PR + 96 + h) * 128 : OOR, s);
  };
  const unsigned lo = w == 0 && lane < 2 ? (lane ? SY_HOT + (sel[1] & 1u) * 8192 + H_FLG + h * 128 : SY_CT + (CT_PR + 64 + h) * 128) : OOR;
  auto upto = [&](unsigned cv) { return min(__builtin_amdgcn_readlane(cv, 0), __builtin_amdgcn_readlane(cv, 1) + URING - 1); };
  int can = -1, got = -1;
  auto seg = [&](int s, int b, u32x4 (&v)[8], u32x4 (&vn)[8]) {
    ld_v(s + 1);
    got = can;
    const unsigned cv = ld4(ru, can < s + 6 ? lo : OOR);
    const unsigned ub = ux_base(s, h, sel) + ux_tile(16) * w;
    f32x4 c[2][2];
    u32x4 o[2];
    int cn;
    SB0;
#pragma unroll
    for (int n = 0; n < 16; ++n) {
#pragma unroll
      for (int kp = 0; kp < 4; ++kp)
#pragma unroll
        for (int m = 0; m < 2; ++m) {
          if (kp == 0) asm volatile("v_mfma_f32_16x16x32_bf16 %0, %1, %2, 0" : "=&v"(c[n & 1][m]) : "v"(v[4 * m + kp]), "a"(W[n][kp]));
          else if (n == 15 && kp == 3 && m == 1) asm volatile("v_mfma_f32_16x16x32_bf16 %0, %1, %2, %0\n\ts_nop 7\n\ts_nop 3" : "+v"(c[n & 1][m]) : "v"(v[4 * m + kp]), "a"(W[n][kp]));
          else asm volatile("v_mfma_f32_16x16x32_bf16 %0, %1, %2, %0" : "+v"(c[n & 1][m]) : "v"(v[4 * m + kp]), "a"(W[n][kp]));
          SB0;
          const int q = 2 * kp + m - 5, t = (n - 1) & 1;
          if (n > 0 && q >= 0 && q < 2) { const f32x4 x = c[t][q]; o[q][t] = pk2(x[0], x[1]); o[q][2 + t] = pk2(x[2], x[3]); }
          if (n > 1 && q == (t ? 2 : -3)) __builtin_amdgcn_raw_buffer_store_b128(o[t ^ 1], ru, (unsigned)(lane * 16 + ((n - 2) & 3) * 1024), ub + ux_tile((n - 2) & 12), ST_AUX);
          if (n == U_POS && q == 2) {
            wait_vm<U_POS - 1>();
            put_v(b ^ 1);
            if (w == 0) fl[b] = upto(cv);
          }
          if (n == U_POS + 1 && q == -3) { lds_barrier(); cn = fl[b]; signal(s - 1); for (int i = 0; i < w * U_STG; ++i) asm volatile("s_nop 1"); }
          if ((n == U_POS + 2 || n == U_POS + 3) && q < -1) vn[4 * (n - U_POS - 2) + q + 5] = *(const u32x4*)(vl + (b ^ 1) * 8192 + (4 * (n - U_POS - 2) + q + 5) * 1024);
          SB0;
        }
    }
#pragma unroll
    for (int m = 0; m < 2; ++m) {
      o[m][1] = pk2(c[1][m][0], c[1][m][1]); o[m][3] = pk2(c[1][m][2], c[1][m][3]);
      st16s(o[m], ru, (unsigned)(lane * 16 + (2 + m) * 1024), ub + ux_tile(12));
    }
    can = max(can, __builtin_amdgcn_readfirstlane(cn));
  };
  for (int s = 0; s < NSEG;) {
    if (got < s) {
      bg_drain();
      signal(s - 1);
      if (w == 0) {
        unsigned spins = 0;
        while ((can = upto(ld4(ru, lo))) < min(s + U_AH, NSEG - 1)) {
          asm volatile("s_sleep 3" ::: "memory");
          if (dead || poll_fail(spins, abortw, lane)) { dead = true; can = NSEG; break; }
        }
        fl[2] = can;
      }
      lds_barrier();
      can = __builtin_amdgcn_readfirstlane(fl[2]);
      ld_v(s);
      put_v(0);
      lds_barrier();
#pragma unroll
      for (int i = 0; i < 8; ++i) V[0][i] = *(const u32x4*)(vl + i * 1024);
    }
    seg(s, 0, V[0], V[1]); ++s;
    if (got >= s && s < NSEG) { seg(s, 1, V[1], V[0]); ++s; }
  }
  bg_drain();
  signal(NSEG - 1);
}

__global__ __launch_bounds__(256, 1) void chain_kernel(ChainArgs a) {
  extern __shared__ __attribute__((aligned(16))) char smem[];
  unsigned again = blockIdx.x;
  asm volatile("" : "+s"(again));
  const int b = again & 255;
  again >>= 8;
  {
    u32x4 rc;
    asm volatile("s_getpc_b64 s[60:61]\n\t.Lchain_here:\n\ts_sub_u32 s60, s60, .Lchain_here-_Z12chain_kernel9ChainArgs\n\ts_subb_u32 s61, s61, 0\n\ts_and_b32 s61, s61, 0xffff\n\t"
                 "s_mov_b32 s62, .Lchain_end-_Z12chain_kernel9ChainArgs\n\ts_mov_b32 s63, 0x20000\n\t"
                 "buffer_load_dwordx4 a[252:255], %1, s[60:63], 0 offen\n\tbuffer_load_dwordx4 a[252:255], %1, s[60:63], s63 offen"
                 : "={s[60:63]}"(rc) : "v"((b >> 3) * 4096 + threadIdx.x * 16) : "scc", "a252", "a253", "a254", "a255");
  }
  int w = __builtin_amdgcn_readfirstlane(threadIdx.x >> 6);
  Gate G;
  G.entry = __builtin_amdgcn_s_memrealtime(); G.open = again != 0; G.arr = w == 0 && !again ? b & 7 : -1;
  G.rot = (again ? again : (unsigned)__builtin_amdgcn_s_getreg(20 | (3 << 11)) - b + WRONG) & 7u;
  {
    volatile LAS unsigned* t = (volatile LAS unsigned*)smem;
    if (w == 0) { t[0] = (unsigned)G.entry; t[1] = (unsigned)(G.entry >> 32); }
    __syncthreads();
    G.entry = (u64)(unsigned)__builtin_amdgcn_readfirstlane(t[0]) | ((u64)(unsigned)__builtin_amdgcn_readfirstlane(t[1]) << 32);
    __syncthreads();
  }
  if (b == 0 && !again) {
    const int lane = opaque_tid(0);
    const __amdgpu_buffer_rsrc_t rs = mk_rsrc(a.sync + SY_ABORT, SY_BYTES - SY_ABORT);
    for (int i = w; i * 1024 < SY_BYTES - SY_ABORT; i += 4) st16(rs, i * 1024 + lane * 16, (u32x4){SENT, SENT, SENT, SENT});
    bg_drain();
    if (w == 0 && lane == 0) __hip_atomic_store((GAS u64*)(a.sync + SY_EPOCH), (__builtin_amdgcn_s_memrealtime() & ~7ull) | G.rot, __ATOMIC_RELAXED, __HIP_MEMORY_SCOPE_AGENT);
  }
  {
    const unsigned rot = G.rot;
    const int x = (b + rot) & 7, j = ((b >> 3) + 3) & 31, i = j >> 2, role = (x & 4) + (j & 3);
    if ((role == 3 || role == 4) && w >= 2) {
      const int n = opaque_tid(0);
      const unsigned x0 = __builtin_amdgcn_raw_buffer_load_b32(w == 2 ? mk_rsrc(a.sync, WS_USED) : mk_rsrc(a.out, 8u << 20), n * PAGE + 4096 + j * 128, 0, 2);
      asm volatile("" :: "v"(x0));
    }
    if (role == 0) role_qk(a, 8 * x + i, smem, w, G);
    else if (role == 1) role_vo(a, 8 * x + i, smem, w, G);
    else if (role < 4) role_cb(a, 8 * (i >> 1) + 2 * x + (i & 1), role - 2, smem, w, G);
    else if (role < 7) role_p(a, role - 4, 8 * (x - 4) + i, smem, w, G);
    else role_u(a, 8 * (x - 4) + i, smem, w, G);
    if (G.rot != rot) {
      __syncthreads();
      asm volatile("s_getpc_b64 s[60:61]\n\t.Lchain_back:\n\ts_sub_u32 s60, s60, .Lchain_back-_Z12chain_kernel9ChainArgs\n\ts_subb_u32 s61, s61, 0\n\ts_setpc_b64 s[60:61]"
                   :: "{s[0:1]}"(__builtin_amdgcn_kernarg_segment_ptr()), "{s2}"(b | (8u | G.rot) << 8), "{v0}"(opaque_tid(w)) : "s60", "s61", "scc", "memory");
      __builtin_unreachable();
    }
  }
  if ((b + G.rot) & 4) role_a(a, smem, w);
  asm volatile(".Lchain_end:");
}

extern "C" void kernel_launch(void* const* d_in, const int* in_sizes, int n_in,
                              void* d_out, int out_size, void* d_ws, size_t ws_size,
                              hipStream_t stream) {
  char* ws = (char*)(((uintptr_t)d_ws + (2u << 20) - 1) & ~(uintptr_t)((2u << 20) - 1));
  hipFuncSetAttribute((const void*)chain_kernel, hipFuncAttributeMaxDynamicSharedMemorySize, CHAIN_LDS);
  ChainArgs ca;
  ca.prof = (u64*)((char*)d_ws + PROF_AT);
  ca.Aq = (const u16*)d_in[0]; ca.Ak = (const u16*)d_in[1]; ca.Av = (const u16*)d_in[2];
  ca.Ashift = (const u16*)d_in[3]; ca.Assm = (const u16*)d_in[4]; ca.AD = (const u16*)d_in[5];
  ca.X = (const u16*)d_in[6]; ca.mem0 = (const u16*)d_in[7];
  ca.Wq = (const u16*)d_in[8]; ca.Wk = (const u16*)d_in[9]; ca.Wv = (const u16*)d_in[10]; ca.Wo = (const u16*)d_in[11];
  ca.out = (u16*)d_out; ca.sync = ws; ca.kvT = (u16*)(ws + OFF_KVT);
  chain_kernel<<<256, 256, CHAIN_LDS, stream>>>(ca);
}
```

```cpp
#include <hip/hip_runtime.h>
#include <hip/hip_bf16.h>
#include <stdint.h>
#include <type_traits>

constexpr int S_LEN = 2048;
constexpr int D_A   = 1024;
constexpr int DM    = 1024;
constexpr int NH    = 32;
constexpr int DH    = 128;
constexpr int HD    = NH * DH;
constexpr int MEMR  = 8;
constexpr int SEG   = 32;
constexpr int NSEG  = S_LEN / SEG;
constexpr int OUTW  = 2048;

typedef unsigned short u16;
typedef unsigned long long u64;
typedef short bf16x8 __attribute__((ext_vector_type(8)));
typedef short bf16x4 __attribute__((ext_vector_type(4)));
typedef float f32x4 __attribute__((ext_vector_type(4)));
typedef float f32x16 __attribute__((ext_vector_type(16)));
typedef unsigned u32x4 __attribute__((ext_vector_type(4)));
typedef unsigned u32x2 __attribute__((ext_vector_type(2)));
#define LAS __attribute__((address_space(3)))
#define GAS __attribute__((address_space(1)))

constexpr size_t MB = 1u << 20;
constexpr size_t OFF_QX  = 68 * MB;
constexpr size_t OFF_KX  = 100 * MB;
constexpr size_t OFF_VX  = 132 * MB;
constexpr size_t OFF_KVT = 164 * MB;
constexpr unsigned WS_USED = 172u << 20;
constexpr size_t PROF_AT = 0;

__device__ __forceinline__ float bf2f(u16 b) { return __uint_as_float(((unsigned)b) << 16); }
__device__ __forceinline__ u16 f2bf(float f) { __bf16 b = (__bf16)f; return __builtin_bit_cast(u16, b); }
__device__ __forceinline__ float rbf(float f) { return bf2f(f2bf(f)); }
__device__ __forceinline__ unsigned pk2(float a, float b) {
  typedef float f2 __attribute__((ext_vector_type(2)));
  typedef __bf16 b2 __attribute__((ext_vector_type(2)));
  f2 v = {a, b}; b2 r = __builtin_convertvector(v, b2); return __builtin_bit_cast(unsigned, r);
}

__device__ __forceinline__ int opaque_tid(int wv) {
  int l = __builtin_amdgcn_mbcnt_hi(~0u, __builtin_amdgcn_mbcnt_lo(~0u, 0u));
  asm volatile("" : "+v"(l));
  return wv * 64 + l;
}
__device__ __forceinline__ int swz_b(int row) { return ((row & 3) << 2) | ((row >> 2) & 3); }

#define KNOB(name, v) constexpr int name = v
KNOB(K_DEF, 80);
KNOB(W_STEP, 33); KNOB(W_OFF, 56);
#define ORD 0
#define RHO(h) (((h) >> 3) ^ ORD)
#define CAL_A 800
#define CAL_T 1350
#define CAL_N 4
#define WRONG 0
#define OUTP "nt"
#define PREV 15
#define PDMA 1
#define PRD 1
#define XSW 1
#define P_EAG 3
#define FILL0 4
#define NOFL 0
#define NOFILL 0
#define XS_T 1000
#define XS_G 64
#define K_DLY 200
#define Q_DLY 450
#define U_STG 1
#define NTW 0
#define XS_N 32
#define U_AH 0
#define U_POS 11
#define FAR1 0
#define FAR2 0
#define KEYX 0
#define FARX 3
#define U_SLP 40
#define U_AH2 0
#define F_SLP 0
#define A_DELAY 1500
#define LDNT(p) __builtin_nontemporal_load(p)
constexpr int CHK      = 16384;
constexpr int PAGE     = 2 << 20;
constexpr int SY_UM    = 0;
constexpr int SY_HOT   = 32 * PAGE;
constexpr int H_MEM    = 0;
constexpr int H_P      = 16 * CHK;
constexpr int H_PZ     = H_P + 2 * CHK;
constexpr int H_ANN    = H_PZ + 2048;
constexpr int H_TR     = H_PZ + 3072;
constexpr int H_DLT    = 32 * CHK;
constexpr int H_FLG    = 33 * CHK;
constexpr int H_PX     = 48 * CHK;
constexpr int H_BYTES  = 80 * CHK;
constexpr int SY_SET   = 33 * PAGE;
constexpr int SY_EPOCH = SY_SET + 128;
constexpr int SY_ABORT = SY_SET + 256;
constexpr int SY_SEL   = SY_ABORT + 128;
constexpr int SY_CT    = SY_ABORT + 640;
constexpr int CT_ARR = 0, CT_INIT = 8, CT_QUEUE = 9, CT_A1 = 10, CT_A2 = 18, CT_CAL = 26;
constexpr int CT_PR = CT_CAL + 64;
constexpr int SY_BYTES = SY_CT + (CT_PR + 160) * 128;
constexpr int URING = 16;
constexpr unsigned SENT = 0xFFFFFFFFu;
constexpr unsigned OOR = 0x7fffffffu;
constexpr unsigned SPIN_CAP = 1u << 19;
constexpr int NLF = 16;
constexpr int L_WTS = 0;
constexpr int L_MEM = L_WTS + 4 * NLF * 2048;
constexpr int L_XCH = L_MEM + 16384;
constexpr int L_XK  = L_XCH + 2048;
constexpr int L_KX  = L_XCH + 4096;
constexpr int L_PRF = L_KX + 8192;
constexpr int XRS = 40;
constexpr int CHAIN_LDS = 160 * 1024;
static_assert(L_XCH + 128 * XRS <= L_PRF && L_PRF + 4096 <= CHAIN_LDS, "lds");

typedef __bf16 bfv8 __attribute__((ext_vector_type(8)));
typedef __bf16 bfv16 __attribute__((ext_vector_type(16)));
typedef unsigned u32x8 __attribute__((ext_vector_type(8)));
typedef unsigned u32x12 __attribute__((ext_vector_type(12)));
typedef unsigned u32x16 __attribute__((ext_vector_type(16)));
typedef unsigned u32x32 __attribute__((ext_vector_type(32)));
struct Wts { u32x8 a[32]; u32x32 v[4]; };

#define STAMPL(i) do {} while (0)
#define PVAL(i, v) do {} while (0)
#define PDECL(base, str) do {} while (0)
#define PZERO do {} while (0)
#define PDUMP(mask) do {} while (0)
#define TA(x) do {} while (0)
#define TB(x) TS(x)
#define TS(x) do {} while (0)
#define SSTAMP(i) do {} while (0)
struct ChainArgs {
  u64* prof;
  const u16* mem0; const u16* Wq; const u16* Wk; const u16* Wv; const u16* Wo; const u16* X;
  u16* out; char* sync;
  const u16* Aq; const u16* Ak; const u16* Av; const u16* Ashift; const u16* Assm; const u16* AD; u16* kvT;
};
__device__ __forceinline__ GAS unsigned* ctr(const ChainArgs& a, int i) { return (GAS unsigned*)(a.sync + SY_CT + i * 128); }
__device__ __forceinline__ void ctr_add(const ChainArgs& a, int i, unsigned v) {
  __hip_atomic_fetch_add(ctr(a, i), v, __ATOMIC_RELAXED, __HIP_MEMORY_SCOPE_AGENT);
}

__device__ __forceinline__ __amdgpu_buffer_rsrc_t mk_rsrc(const void* p, unsigned bytes) {
  const u64 v = (u64)p;
  const unsigned lo = (unsigned)__builtin_amdgcn_readfirstlane((unsigned)v), hi = (unsigned)__builtin_amdgcn_readfirstlane((unsigned)(v >> 32));
  const u64 u = (u64)lo | ((u64)hi << 32);
  return __builtin_amdgcn_make_buffer_rsrc((void*)u, 0, bytes, 0x00020000);
}
__device__ __forceinline__ u32x4 own_rsrc(const void* p, unsigned bytes) {
  u32x4 r = {(unsigned)__builtin_amdgcn_readfirstlane((unsigned)(u64)p), (unsigned)__builtin_amdgcn_readfirstlane((unsigned)((u64)p >> 32)) & 0xffffu, bytes, 0x00020000u};
  asm volatile("s_nop 4" : "+s"(r));
  return r;
}
constexpr int LD_AUX = 16, ST_AUX = 16;
__device__ __forceinline__ u32x4 ld16(__amdgpu_buffer_rsrc_t rs, unsigned off) {
  return __builtin_amdgcn_raw_buffer_load_b128(rs, off, 0, LD_AUX);
}
__device__ __forceinline__ void st16(__amdgpu_buffer_rsrc_t rs, unsigned off, u32x4 v) {
  __builtin_amdgcn_raw_buffer_store_b128(v, rs, off, 0, ST_AUX);
}
__device__ __forceinline__ void st16s(u32x4 v, __amdgpu_buffer_rsrc_t rs, unsigned voff, unsigned soff) {
  __builtin_amdgcn_raw_buffer_store_b128(v, rs, voff, soff, ST_AUX);
  asm volatile("s_nop 1" :: "v"(v) : "memory");
}
__device__ __forceinline__ void st8(__amdgpu_buffer_rsrc_t rs, unsigned off, u32x2 v) {
  __builtin_amdgcn_raw_buffer_store_b64(v, rs, off, 0, ST_AUX);
}
__device__ __forceinline__ void st4(__amdgpu_buffer_rsrc_t rs, unsigned off, unsigned v) {
  __builtin_amdgcn_raw_buffer_store_b32(v, rs, off, 0, ST_AUX);
}
__device__ __forceinline__ unsigned ld4(__amdgpu_buffer_rsrc_t rs, unsigned off) {
  return __builtin_amdgcn_raw_buffer_load_b32(rs, off, 0, LD_AUX);
}
constexpr int TU = 16;
__device__ __forceinline__ unsigned cyc32() { return (unsigned)__builtin_amdgcn_s_memtime(); }
__device__ __forceinline__ unsigned to_sgpr(unsigned x) {
  unsigned r;
  asm volatile("s_nop 0\n\tv_readfirstlane_b32 %0, %1\n\ts_nop 4" : "=s"(r) : "v"(x));
  return r;
}
struct Clk {
  unsigned ce, te;
  unsigned c2t, t2c;
  __device__ __forceinline__ void edge(unsigned& c, unsigned& t) {
    u64 c0, r0, r1, r2, r3, r4, r5;
    asm volatile("s_memtime %0\n\ts_memrealtime %1\n\ts_nop 4\n\ts_memrealtime %2\n\ts_nop 4\n\ts_memrealtime %3\n\ts_nop 4\n\t"
                 "s_memrealtime %4\n\ts_nop 4\n\ts_memrealtime %5\n\ts_nop 4\n\ts_memrealtime %6\n\ts_waitcnt lgkmcnt(0)"
                 : "=&s"(c0), "=&s"(r0), "=&s"(r1), "=&s"(r2), "=&s"(r3), "=&s"(r4), "=&s"(r5) :: "memory");
    const unsigned x = (unsigned)r0;
    const unsigned n = ((unsigned)r1 == x) + ((unsigned)r2 == x) + ((unsigned)r3 == x) + ((unsigned)r4 == x) + ((unsigned)r5 == x);
    c = to_sgpr((unsigned)c0 + 24u * n); t = (x + 4u) * TU;
  }
  __device__ __forceinline__ void init() { edge(ce, te); c2t = 44000u; t2c = 97600u; }
  __device__ __forceinline__ void sync() {
    unsigned c, t;
    edge(c, t);
    set(c, t);
  }
  __device__ __forceinline__ void set(unsigned c, unsigned t) {
    if (c == ce) return;
    const float q = (float)(t - te) * __builtin_amdgcn_rcpf((float)(c - ce));
    const float qc = fminf(fmaxf(q, 0.51f), 1.99f);
    c2t = to_sgpr((unsigned)(qc * 65536.f));
    t2c = to_sgpr((unsigned)(__builtin_amdgcn_rcpf(qc) * 65536.f));
    ce = c; te = t;
  }
  __device__ __forceinline__ unsigned time(unsigned c) const { return te + ((min(c - ce, 30000u) * c2t) >> 16); }
  __device__ __forceinline__ unsigned now() const { return time(cyc32()); }
  __device__ __forceinline__ unsigned target(unsigned t) const {
    return to_sgpr(ce + (((unsigned)min(max((int)(t - te), 0), 20000) * t2c) >> 16));
  }
  __device__ __forceinline__ void wait(unsigned t) const { wait_c(target(t)); }
  __device__ __forceinline__ void wait_c(unsigned c) const {
    unsigned y;
    asm volatile("1:\n\ts_memtime vcc\n\ts_waitcnt lgkmcnt(0)\n\ts_sub_i32 %0, %1, vcc_lo\n\ts_cmp_gt_i32 %0, 128\n\ts_cbranch_scc1 1b\n\t"
                 "2:\n\ts_sub_i32 %0, %0, %2\n\ts_cmp_gt_i32 %0, %3\n\ts_cbranch_scc1 2b"
                 : "=&s"(y) : "s"(c), "n"(W_STEP), "n"(W_OFF) : "memory", "scc", "vcc");
  }
};
__device__ __forceinline__ unsigned nosent(unsigned d) { return min(d, SENT - 1u); }
__device__ __forceinline__ int arow(int row, int half) { return 4 * (row >> 1) + 2 * half + (row & 1); }
__device__ __forceinline__ unsigned umax4(u32x4 v) { return max(max(v[0], v[1]), max(v[2], v[3])); }
__device__ __forceinline__ void stamp_again(GAS u64* p) {
  __hip_atomic_store(p, (__builtin_amdgcn_s_memrealtime() & ~7ull) | (__hip_atomic_load(p, __ATOMIC_RELAXED, __HIP_MEMORY_SCOPE_AGENT) & 7ull),
                     __ATOMIC_RELAXED, __HIP_MEMORY_SCOPE_AGENT);
}
__device__ __forceinline__ bool poll_fail(unsigned& spins, GAS unsigned* abortw, int lane) {
  ++spins;
  if (spins > SPIN_CAP) {
    if (lane == 0) __hip_atomic_store(abortw, 1u, __ATOMIC_RELAXED, __HIP_MEMORY_SCOPE_AGENT);
    return true;
  }
  if ((spins & 511u) == 0u && lane == 0) {
    unsigned n = 0;
    for (int i = 0; i < 8; ++i) n += __hip_atomic_load(abortw + (SY_CT - SY_ABORT + (CT_ARR + i) * 128) / 4, __ATOMIC_RELAXED, __HIP_MEMORY_SCOPE_AGENT) + 1u;
    if (n < 256u) stamp_again((GAS u64*)(abortw - 32));
  }
  if ((spins & 63u) == 0u)
    return __builtin_amdgcn_readfirstlane(__hip_atomic_load(abortw, __ATOMIC_RELAXED, __HIP_MEMORY_SCOPE_AGENT)) != SENT;
  return false;
}
struct Gate {
  u64 entry; int arr; unsigned rot; bool open;
  __device__ __forceinline__ bool look(char* sync, u64 v = 0) {
    if (open) return true;
    for (unsigned spins = 0;; ++spins) {
      if (spins) v = __hip_atomic_load((GAS u64*)(sync + SY_EPOCH), __ATOMIC_RELAXED, __HIP_MEMORY_SCOPE_AGENT);
      v = (u64)(unsigned)__builtin_amdgcn_readfirstlane((unsigned)v) | ((u64)(unsigned)__builtin_amdgcn_readfirstlane((unsigned)(v >> 32)) << 32);
      if (v - entry + 2000u < (1ull << 31) || spins > 4 * SPIN_CAP) break;
      if (spins) __builtin_amdgcn_s_sleep(8);
    }
    asm volatile("" ::: "memory");
    const bool same = rot == ((unsigned)v & 7u);
    rot = (unsigned)v & 7u; open = true;
    return same;
  }
  __device__ __forceinline__ void arrive(char* sync) {
    if (arr >= 0 && opaque_tid(0) == 0) __hip_atomic_fetch_add((GAS unsigned*)(sync + SY_CT + (CT_ARR + arr) * 128), 1u, __ATOMIC_RELAXED, __HIP_MEMORY_SCOPE_AGENT);
    arr = -1;
  }
  __device__ __forceinline__ bool pass(char* sync, u64 v = 0) { const bool r = look(sync, v); arrive(sync); return r; }
};
__device__ __forceinline__ unsigned die_half() { return ((unsigned)__builtin_amdgcn_s_getreg(20 | (3 << 11)) >> 2) & 1u; }
typedef u32x2 Sel;
__device__ __forceinline__ Sel sel_wait(char* sync, GAS unsigned* abortw, int lane, bool& dead, bool all = false, int c0 = CT_INIT, int n0 = 0, int c1 = CT_INIT, int n1 = 0) {
  unsigned spins = 0;
  u64 v;
  const bool m = lane == 0 || lane > 3 || !all;
  const int off = m ? SY_SEL : SY_CT + (lane == 1 ? CT_INIT : lane == 2 ? c0 : c1) * 128;
  while (true) {
    v = __hip_atomic_load((GAS u64*)(sync + off), __ATOMIC_RELAXED, __HIP_MEMORY_SCOPE_AGENT);
    if (__all(m ? (v >> (all ? 62 : 63)) == 0 : (int)v >= (lane == 1 ? 31 : lane == 2 ? n0 : n1))) break;
    __builtin_amdgcn_s_sleep(4);
    if (dead || poll_fail(spins, abortw, lane)) { dead = true; v = 0; break; }
  }
  return (Sel){(unsigned)__builtin_amdgcn_readfirstlane((unsigned)v), (unsigned)__builtin_amdgcn_readfirstlane((unsigned)(v >> 32))};
}
__device__ __forceinline__ __amdgpu_buffer_rsrc_t rsrc_u(char* sync) { return mk_rsrc((void*)sync, WS_USED); }
__device__ __forceinline__ unsigned far_blk(int n, Sel sel, int x = 0) { return n * CHK + ((~sel[0] ^ (n >> 7) ^ x) & 1u) * 8192; }
__device__ __forceinline__ constexpr unsigned quarter(int q) { return (q >> 1) * CHK + (q & 1) * 4096; }
__device__ __forceinline__ unsigned hot_q(int q, Sel sel) { return quarter(q ^ ((sel[1] >> 4) & 3u)); }
__device__ __forceinline__ unsigned um_at(int s, unsigned o, Sel sel) { return ((s & 3) * (64 * CHK) + o) ^ (((sel[1] >> (8 + 4 * (s & 3))) & 7u) << 12); }
__device__ __forceinline__ constexpr unsigned um_place(int ci, int g) { return (ci >> 3) * 16 * CHK + (ci & 1) * 8 * CHK + g * 2 * CHK + quarter(((ci >> 1) & 3) ^ ((ci >> 3) & 2)); }
constexpr unsigned UXH = 8 * CHK;
__device__ __forceinline__ unsigned ux_base(int s, int h, Sel sel) { return ((s & 15) * 32 + h) * UXH + ((~sel[0] >> (2 * (s & 15) + (h >> 4))) & 1u) * 8192; }
__device__ __forceinline__ constexpr unsigned ux_tile(int t) { return (t >> 3) * CHK + (t & 7) * 1024; }
__device__ __forceinline__ __amdgpu_buffer_rsrc_t rsrc_hot(char* sync, Sel sel) {
  return mk_rsrc((void*)(sync + SY_HOT + (sel[1] & 1u) * 8192), H_BYTES - 8192);
}
template <int SLP = 4>
__device__ __forceinline__ void ctr_wait(GAS unsigned* c, unsigned need, GAS unsigned* abortw, int lane, bool& dead) {
  unsigned spins = 0;
  while (true) {
    unsigned v = __builtin_amdgcn_readfirstlane(__hip_atomic_load(c, __ATOMIC_RELAXED, __HIP_MEMORY_SCOPE_AGENT)) + 1u;
    if (v >= need) break;
    __builtin_amdgcn_s_sleep(SLP);
    if (dead || poll_fail(spins, abortw, lane)) { dead = true; break; }
  }
}
template <int N> __device__ __forceinline__ void wait_vm() { asm volatile("s_waitcnt vmcnt(%0)" :: "n"(N) : "memory"); }
__device__ __forceinline__ void lds_barrier() {
  asm volatile("s_waitcnt lgkmcnt(0)" ::: "memory");
  __builtin_amdgcn_s_barrier();
  asm volatile("" ::: "memory");
}
__device__ __forceinline__ f32x4 vzero() { f32x4 z = {0.f, 0.f, 0.f, 0.f}; asm volatile("" : "+v"(z)); return z; }
constexpr bool blk_in_lds(int R, int b) {
  return R == 0 ? ((b & 7) == 1 || (b & 7) == 4) : (b < 16 ? (b & 3) == 0 : (b & 3) == 3);
}
constexpr int lds_index(int R, int b) { int n = 0; for (int i = 0; i < b; ++i) if (blk_in_lds(R, i)) ++n; return n; }
constexpr bool frag_in_lds(int R, int f) { return blk_in_lds(R, f >> 1); }
constexpr int frag_reg_slot(int R, int f) { int n = 0; for (int i = 0; i < f; ++i) if (!frag_in_lds(R, i)) ++n; return n; }

struct WSched { int ord[32]; int slot[32]; int issue[32]; int out[32]; int reg[32]; };
constexpr WSched make_wsched(int R) {
  WSched S{};
  int n = 0;
  for (int b = 0; b < 32; ++b) if (!blk_in_lds(R, b)) S.ord[n++] = b;
  for (int i = 0; i < n; ++i) S.reg[i] = frag_reg_slot(R, 2 * S.ord[i]);
  const int npure = n;
  for (int b = 0; b < 32; ++b) if (blk_in_lds(R, b)) S.ord[n++] = b;
  int seq[32] = {};
  int nissued = 0, next = 0;
  for (int i = 0; i < 10; ++i) { S.slot[i] = (i + 10 - npure % 10) % 10; S.issue[i] = -1; seq[i] = nissued++; }
  next = 10;
  S.out[0] = nissued - 1;
  for (int c = 0; c <= npure; ++c) {
    if (c >= 1) {
      const int k = S.slot[c - 1];
      if (next < npure) { S.slot[next] = k; S.issue[next] = c; seq[next] = nissued++; ++next; }
      else if (k < NLF / 2) { S.slot[npure + k] = k; S.issue[npure + k] = c; seq[npure + k] = nissued++; }
    }
    if (c + 1 < npure) S.out[c + 1] = nissued - 1 - seq[c + 1];
  }
  return S;
}
__device__ __forceinline__ u32x8 tr_frag(const char* sb, int tile, int lane) {
  u32x8 f;
#pragma unroll
  for (int q = 0; q < 4; ++q) {
    int row = (q >> 1) * 32 + 8 * (lane >> 4) + 4 * (q & 1) + ((lane & 15) >> 2);
    int p = lane & 3;
    int lc = 2 * tile + (p >> 1);
    int addr = row * 256 + ((lc ^ swz_b(row)) * 16) + 8 * (p & 1);
    bf16x4 v = __builtin_amdgcn_ds_read_tr16_b64_v4i16((LAS bf16x4*)(sb + addr));
    u32x2 u = __builtin_bit_cast(u32x2, v);
    f[2 * q] = u[0]; f[2 * q + 1] = u[1];
  }
  return f;
}
__device__ __forceinline__ void dma16(__amdgpu_buffer_rsrc_t rs, unsigned voff, unsigned soff, unsigned lds, bool nt = false, bool pad = true) {
  if (nt) asm volatile("s_mov_b32 m0, %0\n\ts_nop 3\n\tbuffer_load_dwordx4 %1, %2, %3 offen nt lds" :: "s"(lds), "v"(voff), "s"(rs), "s"(soff) : "m0", "memory");
  else if (pad) asm volatile("s_mov_b32 m0, %0\n\ts_nop 3\n\tbuffer_load_dwordx4 %1, %2, %3 offen lds" :: "s"(lds), "v"(voff), "s"(rs), "s"(soff) : "m0", "memory");
  else asm volatile("s_mov_b32 m0, %0\n\ts_nop 0\n\tbuffer_load_dwordx4 %1, %2, %3 offen lds" :: "s"(lds), "v"(voff), "s"(rs), "s"(soff) : "m0", "memory");
}
__device__ __forceinline__ u32x8 tr_frag2(const char* sb, int w, int f, int lane) {
  u32x8 r;
#pragma unroll
  for (int c = 0; c < 2; ++c)
#pragma unroll
    for (int t = 0; t < 2; ++t) {
      const int row = 32 * (w >> 1) + 16 * c + 4 * (lane >> 4) + ((lane & 15) >> 2);
      const int p = lane & 3, lc = 2 * (4 * (w & 1) + 2 * t + f) + (p >> 1);
      const u32x2 u = __builtin_bit_cast(u32x2, __builtin_amdgcn_ds_read_tr16_b64_v4i16((LAS bf16x4*)(sb + row * 256 + ((lc ^ swz_b(row)) * 16) + 8 * (p & 1))));
      r[4 * c + t] = u[0]; r[4 * c + 2 + t] = u[1];
    }
  return r;
}
template <int ROLE>
__device__ __forceinline__ void load_weights(const ChainArgs& a, int h, int w, int lane, char* smem, Wts& W) {
  u32x8 F[4];
#pragma unroll
  for (int i = 0; i < 32; ++i) asm volatile("" : "=a"(W.a[i]));
  constexpr WSched S = make_wsched(ROLE);
  constexpr int NP = 32 - NLF / 2;
  const __amdgpu_buffer_rsrc_t r0 = mk_rsrc(ROLE == 0 ? a.Wq : a.Wv, 8u << 20), r1 = mk_rsrc(ROLE == 0 ? a.Wk : a.Wo, 8u << 20);
  const unsigned lds0 = (unsigned)(uintptr_t)(LAS char*)smem;
  unsigned vo[2][4];
#pragma unroll
  for (int j = 0; j < 4; ++j) {
    const int row = (w * 4 + j) * 4 + (lane >> 4), lc = (lane & 15) ^ swz_b(row);
    vo[0][j] = row * HD * 2 + lc * 16; vo[1][j] = ((row & 15) + 2 * (row & 16)) * DM * 2 + (row >> 5) * 256 + lc * 16;
  }
  asm volatile("s_nop 4");
  auto stage = [&](int i) {
    const int blk = S.ord[i];
    const bool second = ROLE == 0 ? (blk & 1) : blk >= 16, wo = ROLE == 1 && second;
    const unsigned so = wo ? (unsigned)(((128 * h + 32 * (blk & 2) + 16 * (blk & 1)) * DM + 256 * (((blk - 16) >> 2) ^ (h & 3))) * 2)
                           : (unsigned)((64 * ((ROLE == 0 ? blk >> 1 : blk) ^ RHO(h)) * HD + 128 * h) * 2);
#pragma unroll
    for (int j = 0; j < 4; ++j) dma16(second ? r1 : r0, vo[wo][j], so, lds0 + 16384 * S.slot[i] + (w * 4 + j) * 1024, (NTW >> (wo ? 1 : 0)) & 1);
  };
#pragma unroll
  for (int i = 0; i < 32; ++i) if (S.issue[i] == -1) stage(i);
#pragma unroll
  for (int c = 0; c < NP; ++c) {
    asm volatile("s_waitcnt vmcnt(%0)" :: "n"(4 * S.out[c]) : "memory");
    lds_barrier();
#pragma unroll
    for (int i = 0; i < 32; ++i) if (S.issue[i] == c) stage(i);
    const char* sb = smem + 16384 * S.slot[c];
#pragma unroll
    for (int t = 0; t < 2; ++t) {
      u32x8 f = (ROLE == 1 && S.ord[c] >= 16) ? tr_frag2(sb, w, t, lane) : tr_frag(sb, 2 * w + t, lane);
      if (S.reg[c] < 32) {
#pragma unroll
        for (int i = 0; i < 8; ++i) asm volatile("v_accvgpr_write_b32 a[%1], %0" :: "v"(f[i]), "n"(8 * ((S.reg[c] + t) & 31) + i));
      }
      F[(S.reg[c] + t) & 3] = f;
    }
    if (S.reg[c] >= 32 && (S.reg[c] & 2)) {
      const u32x16 lo = __builtin_shufflevector(F[0], F[1], 0, 1, 2, 3, 4, 5, 6, 7, 8, 9, 10, 11, 12, 13, 14, 15);
      const u32x16 hi = __builtin_shufflevector(F[2], F[3], 0, 1, 2, 3, 4, 5, 6, 7, 8, 9, 10, 11, 12, 13, 14, 15);
      u32x32 x = __builtin_shufflevector(lo, hi, 0, 1, 2, 3, 4, 5, 6, 7, 8, 9, 10, 11, 12, 13, 14, 15, 16, 17, 18, 19, 20, 21, 22, 23, 24, 25, 26, 27, 28, 29, 30, 31);
      asm volatile("" : "+v"(x));
      W.v[(S.reg[c] >> 2) & 3] = x;
    }
  }
  static_assert(S.issue[31] < NP && S.issue[NP] < S.issue[31], "resident blocks are issued in slot order inside the loop");
#pragma unroll
  for (int j = 0; j < NLF / 2; ++j) {
    asm volatile("s_waitcnt vmcnt(%0)" :: "n"(4 * (NLF / 2 - 1 - j)) : "memory");
    lds_barrier();
    const bool two = ROLE == 1 && S.ord[NP + j] >= 16;
    u32x8 f0 = two ? tr_frag2(smem + 16384 * j, w, 0, lane) : tr_frag(smem + 16384 * j, 2 * w, lane);
    u32x8 f1 = two ? tr_frag2(smem + 16384 * j, w, 1, lane) : tr_frag(smem + 16384 * j, 2 * w + 1, lane);
    lds_barrier();
    char* d = smem + 16384 * j + w * 4096 + lane * 16;
    *(u32x4*)d = (u32x4){f0[0], f0[1], f0[2], f0[3]};
    *(u32x4*)(d + 1024) = (u32x4){f0[4], f0[5], f0[6], f0[7]};
    *(u32x4*)(d + 2048) = (u32x4){f1[0], f1[1], f1[2], f1[3]};
    *(u32x4*)(d + 3072) = (u32x4){f1[4], f1[5], f1[6], f1[7]};
  }
  lds_barrier();
}
#define SB0 __builtin_amdgcn_sched_barrier(0)
#define QK_BODY \
  "s_cmp_eq_u32 %[wv], 1\n\t" \
  "s_cbranch_scc1 91f\n\t" \
  "s_cmp_eq_u32 %[wv], 2\n\t" \
  "s_cbranch_scc1 92f\n\t" \
  "s_cmp_eq_u32 %[wv], 3\n\t" \
  "s_cbranch_scc1 93f\n\t" \
  "90:\n\t" \
  "s_mov_b32 s58, s42\n\t" \
  ".p2align 6\n\t" \
  "20:\n\t" \
  "s_and_b32 s76, s44, 7\n\t" \
  "s_lshl_b32 s76, s76, 15\n\t" \
  "s_add_i32 s40, s54, s76\n\t" \
  "s_and_b32 s76, s44, 3\n\t" \
  "s_lshl_b32 s45, s76, 16\n\t" \
  "s_mov_b32 s47, s45\n\t" \
  "s_add_i32 s49, s44, -1\n\t" \
  "s_and_b32 s48, s49, 3\n\t" \
  "s_lshl_b32 s48, s48, 16\n\t" \
  "s_lshl_b32 s50, s53, 8\n\t" \
  "s_mov_b32 s59, 0\n\t" \
  "s_mov_b32 s92, s51\n\t" \
  "s_mov_b32 s93, s53\n\t" \
  "s_xor_b32 s76, s40, 0x1000\n\t" \
  "s_xor_b32 s77, s40, 0x4000\n\t" \
  "s_xor_b32 s78, s40, 0x5000\n\t" \
  "s_cmp_lg_u32 s58, 0\n\t" \
  "s_cbranch_scc1 11f\n\t" \
  "10:\n\t" \
  "ds_read_b128 v[48:51], v102 offset:0\n\t" \
  "ds_read_b128 v[52:55], v102 offset:1024\n\t" \
  "ds_read_b128 v[56:59], v102 offset:2048\n\t" \
  "ds_read_b128 v[60:63], v102 offset:3072\n\t" \
  "ds_read_b128 v[16:19], v102 offset:16384\n\t" \
  "ds_read_b128 v[20:23], v102 offset:17408\n\t" \
  "ds_read_b128 v[24:27], v102 offset:18432\n\t" \
  "ds_read_b128 v[28:31], v102 offset:19456\n\t" \
  "ds_read_b128 v[84:87], v102 offset:49152\n\t" \
  "ds_read_b128 v[88:91], v102 offset:50176\n\t" \
  "ds_read_b128 v[92:95], v102 offset:51200\n\t" \
  "ds_read_b128 v[96:99], v102 offset:52224\n\t" \
  "s_waitcnt lgkmcnt(4)\n\t" \
  "ds_read_b128 v[0:3], v103 offset:16384\n\t" \
  "ds_read_b128 v[4:7], v103 offset:17408\n\t" \
  "ds_read_b128 v[8:11], v103 offset:18432\n\t" \
  "ds_read_b128 v[12:15], v103 offset:19456\n\t" \
  "s_bitcmp1_b32 s58, 1\n\t" \
  "s_cselect_b64 s[80:81], -1, 0\n\t" \
  "s_cmp_lg_u32 s58, 0\n\t" \
  "s_cbranch_scc1 17f\n\t" \
  "s_mov_b32 exec_hi, 0\n\t" \
  "16:\n\t" \
  "s_memtime vcc\n\t" \
  "s_waitcnt lgkmcnt(0)\n\t" \
  "s_sub_i32 s79, s41, vcc_lo\n\t" \
  "s_cmp_gt_i32 s79, 64\n\t" \
  "s_cbranch_scc1 16b\n\t" \
  "13:\n\t" \
  "buffer_load_dword v116, v110, %[rh], s48 offen sc1\n\t" \
  "s_waitcnt vmcnt(0)\n\t" \
  "15:\n\t" \
  "v_mov_b32 v117, 20000\n\t" \
  "v_cmp_ne_u32_sdwa vcc, v116, s49 src0_sel:BYTE_0 src1_sel:DWORD\n\t" \
  "v_subrev_u32 v116, s50, v116\n\t" \
  "v_ashrrev_i32 v116, 8, v116\n\t" \
  "s_nop 1\n\t" \
  "v_max_i32_dpp v116, v116, v116 quad_perm:[1,0,3,2] row_mask:0xf bank_mask:0xf\n\t" \
  "s_cbranch_vccnz 14f\n\t" \
  "s_nop 0\n\t" \
  "v_max_i32_dpp v116, v116, v116 quad_perm:[2,3,0,1] row_mask:0xf bank_mask:0xf\n\t" \
  "s_nop 1\n\t" \
  "v_max_i32_dpp v116, v116, v116 row_half_mirror row_mask:0xf bank_mask:0xf\n\t" \
  "s_nop 1\n\t" \
  "v_max_i32_dpp v116, v116, v116 row_mirror row_mask:0xf bank_mask:0xf\n\t" \
  "v_med3_i32 v116, v116, 0, v117\n\t" \
  "v_mul_u32_u24 v116, s52, v116\n\t" \
  "v_lshrrev_b32 v116, 16, v116\n\t" \
  "s_mov_b64 exec, -1\n\t" \
  "v_readlane_b32 s84, v116, 0\n\t" \
  "v_readlane_b32 s85, v116, 16\n\t" \
  "s_max_u32 s84, s84, s85\n\t" \
  "s_add_i32 s84, s84, s51\n\t" \
  "v_mov_b64 v[68:69], 0\n\t" \
  "v_mov_b64 v[70:71], 0\n\t" \
  "v_mov_b64 v[72:73], 0\n\t" \
  "v_mov_b64 v[74:75], 0\n\t" \
  "v_mov_b64 v[76:77], 0\n\t" \
  "v_mov_b64 v[78:79], 0\n\t" \
  "v_mov_b64 v[80:81], 0\n\t" \
  "v_mov_b64 v[82:83], 0\n\t" \
  "1:\n\t" \
  "s_memtime vcc\n\t" \
  "s_waitcnt lgkmcnt(0)\n\t" \
  "s_sub_i32 s79, s84, vcc_lo\n\t" \
  "s_cmp_gt_i32 s79, 128\n\t" \
  "s_cbranch_scc1 1b\n\t" \
  "2:\n\t" \
  "s_sub_i32 s79, s79, 33\n\t" \
  "s_cmp_gt_i32 s79, 56\n\t" \
  "s_cbranch_scc1 2b\n\t" \
  "buffer_load_dwordx4 v[32:35], v100, %[rh], s40 offen sc1\n\t" \
  "s_nop 12\n\t" \
  "buffer_load_dwordx4 v[36:39], v100, %[rh], s76 offen sc1\n\t" \
  "s_nop 12\n\t" \
  "buffer_load_dwordx4 v[40:43], v100, %[rh], s77 offen sc1\n\t" \
  "s_nop 12\n\t" \
  "buffer_load_dwordx4 v[44:47], v100, %[rh], s78 offen sc1\n\t" \
  "s_memtime vcc\n\t" \
  "s_memrealtime s[82:83]\n\t" \
  "s_nop 4\n\t" \
  "s_memrealtime s[84:85]\n\t" \
  "s_nop 4\n\t" \
  "s_memrealtime s[94:95]\n\t" \
  "s_nop 4\n\t" \
  "s_memrealtime s[86:87]\n\t" \
  "s_nop 4\n\t" \
  "s_memrealtime s[88:89]\n\t" \
  "s_nop 4\n\t" \
  "s_memrealtime s[90:91]\n\t" \
  "s_mov_b32 s92, 0\n\t" \
  "s_waitcnt lgkmcnt(0)\n\t" \
  "s_cmp_eq_u32 s82, s84\n\t" \
  "s_addc_u32 s92, s92, 0\n\t" \
  "s_cmp_eq_u32 s82, s94\n\t" \
  "s_addc_u32 s92, s92, 0\n\t" \
  "s_cmp_eq_u32 s82, s86\n\t" \
  "s_addc_u32 s92, s92, 0\n\t" \
  "s_cmp_eq_u32 s82, s88\n\t" \
  "s_addc_u32 s92, s92, 0\n\t" \
  "s_cmp_eq_u32 s82, s90\n\t" \
  "s_addc_u32 s92, s92, 0\n\t" \
  "s_mul_i32 s92, s92, 24\n\t" \
  "s_add_i32 s92, s92, vcc_lo\n\t" \
  "s_lshl4_add_u32 s93, s82, 64\n\t" \
  "3:\n\t" \
  "s_waitcnt lgkmcnt(0)\n\t" \
  "s_waitcnt vmcnt(3)\n\t" \
  "v_smfmac_f32_16x16x64_bf16 v[68:71], v[32:35], a[0:7], v104\n\t" \
  "ds_write_b128 v101, v[32:35] offset:0\n\t" \
  "v_smfmac_f32_16x16x64_bf16 v[72:75], v[32:35], a[8:15], v104\n\t" \
  "v_smfmac_f32_16x16x64_bf16 v[76:79], v[32:35], v[48:55], v104\n\t" \
  "v_smfmac_f32_16x16x64_bf16 v[80:83], v[32:35], v[56:63], v104\n\t" \
  "s_waitcnt vmcnt(2)\n\t" \
  "v_smfmac_f32_16x16x64_bf16 v[68:71], v[36:39], a[16:23], v104\n\t" \
  "ds_write_b128 v101, v[36:39] offset:1024\n\t" \
  "v_smfmac_f32_16x16x64_bf16 v[72:75], v[36:39], a[24:31], v104\n\t" \
  "v_smfmac_f32_16x16x64_bf16 v[76:79], v[36:39], a[32:39], v104\n\t" \
  "v_cmp_u_f32 vcc, v68, v69\n\t" \
  "v_smfmac_f32_16x16x64_bf16 v[80:83], v[36:39], a[40:47], v104\n\t" \
  "v_cmp_u_f32 s[82:83], v70, v71\n\t" \
  "s_waitcnt vmcnt(1)\n\t" \
  "v_smfmac_f32_16x16x64_bf16 v[68:71], v[40:43], v[16:23], v104\n\t" \
  "ds_write_b128 v101, v[40:43] offset:2048\n\t" \
  "v_smfmac_f32_16x16x64_bf16 v[72:75], v[40:43], v[24:31], v104\n\t" \
  "s_or_b64 vcc, vcc, s[82:83]\n\t" \
  "s_or_b64 vcc, vcc, s[80:81]\n\t" \
  "s_waitcnt lgkmcnt(1)\n\t" \
  "v_smfmac_f32_16x16x64_bf16 v[76:79], v[40:43], a[48:55], v104\n\t" \
  "s_cbranch_scc1 8f\n\t" \
  "5:\n\t" \
  "s_barrier\n\t" \
  "4:\n\t" \
  "s_memtime s[56:57]\n\t" \
  "ds_read_b128 v[48:51], v101 offset:4096\n\t" \
  "v_smfmac_f32_16x16x64_bf16 v[80:83], v[40:43], a[56:63], v104\n\t" \
  "ds_read_b128 v[52:55], v101 offset:5120\n\t" \
  "ds_read_b128 v[56:59], v101 offset:8192\n\t" \
  "s_waitcnt vmcnt(0)\n\t" \
  "v_smfmac_f32_16x16x64_bf16 v[68:71], v[44:47], a[64:71], v104\n\t" \
  "ds_write_b128 v101, v[44:47] offset:3072\n\t" \
  "v_smfmac_f32_16x16x64_bf16 v[72:75], v[44:47], a[72:79], v104\n\t" \
  "v_smfmac_f32_16x16x64_bf16 v[76:79], v[44:47], a[80:87], v104\n\t" \
  "v_cmp_u_f32 vcc, v68, v69\n\t" \
  "v_smfmac_f32_16x16x64_bf16 v[80:83], v[44:47], a[88:95], v104\n\t" \
  "v_cmp_u_f32 s[82:83], v70, v71\n\t" \
  "s_waitcnt lgkmcnt(3)\n\t" \
  "v_smfmac_f32_16x16x64_bf16 v[68:71], v[48:51], a[96:103], v104\n\t" \
  "ds_read_b128 v[60:63], v101 offset:9216\n\t" \
  "ds_read_b128 v[64:67], v101 offset:12288\n\t" \
  "v_smfmac_f32_16x16x64_bf16 v[72:75], v[48:51], a[104:111], v104\n\t" \
  "s_or_b64 vcc, vcc, s[82:83]\n\t" \
  "s_waitcnt lgkmcnt(2)\n\t" \
  "v_smfmac_f32_16x16x64_bf16 v[68:71], v[52:55], a[112:119], v104\n\t" \
  "s_cbranch_scc1 9f\n\t" \
  "6:\n\t" \
  "s_barrier\n\t" \
  "v_smfmac_f32_16x16x64_bf16 v[72:75], v[52:55], a[120:127], v104\n\t" \
  "ds_read_b128 v[48:51], v101 offset:13312\n\t" \
  "v_smfmac_f32_16x16x64_bf16 v[68:71], v[56:59], a[192:199], v104\n\t" \
  "ds_read_b128 v[52:55], v101 offset:6144\n\t" \
  "v_smfmac_f32_16x16x64_bf16 v[72:75], v[56:59], a[200:207], v104\n\t" \
  "s_waitcnt lgkmcnt(3)\n\t" \
  "v_smfmac_f32_16x16x64_bf16 v[68:71], v[60:63], a[208:215], v104\n\t" \
  "ds_read_b128 v[56:59], v101 offset:7168\n\t" \
  "v_smfmac_f32_16x16x64_bf16 v[72:75], v[60:63], a[216:223], v104\n\t" \
  "s_waitcnt lgkmcnt(3)\n\t" \
  "v_smfmac_f32_16x16x64_bf16 v[68:71], v[64:67], v[152:159], v104\n\t" \
  "ds_read_b128 v[60:63], v101 offset:10240\n\t" \
  "ds_read_b128 v[32:35], v119 offset:0\n\t" \
  "v_smfmac_f32_16x16x64_bf16 v[72:75], v[64:67], v[160:167], v104\n\t" \
  "ds_read_b128 v[16:19], v103 offset:49152\n\t" \
  "s_waitcnt lgkmcnt(5)\n\t" \
  "v_smfmac_f32_16x16x64_bf16 v[68:71], v[48:51], v[168:175], v104\n\t" \
  "ds_read_b128 v[64:67], v101 offset:11264\n\t" \
  "ds_read_b128 v[36:39], v119 offset:1024\n\t" \
  "v_smfmac_f32_16x16x64_bf16 v[72:75], v[48:51], v[176:183], v104\n\t" \
  "ds_read_b128 v[20:23], v103 offset:50176\n\t" \
  "s_waitcnt lgkmcnt(7)\n\t" \
  "v_smfmac_f32_16x16x64_bf16 v[68:71], v[52:55], v[84:91], v104\n\t" \
  "ds_read_b128 v[48:51], v101 offset:15360\n\t" \
  "ds_read_b128 v[40:43], v119 offset:16\n\t" \
  "v_smfmac_f32_16x16x64_bf16 v[72:75], v[52:55], v[92:99], v104\n\t" \
  "ds_read_b128 v[24:27], v103 offset:51200\n\t" \
  "s_waitcnt lgkmcnt(9)\n\t" \
  "v_smfmac_f32_16x16x64_bf16 v[68:71], v[56:59], a[160:167], v104\n\t" \
  "ds_read_b128 v[52:55], v101 offset:14336\n\t" \
  "ds_read_b128 v[44:47], v119 offset:1040\n\t" \
  "v_smfmac_f32_16x16x64_bf16 v[72:75], v[56:59], a[168:175], v104\n\t" \
  "ds_read_b128 v[28:31], v103 offset:52224\n\t" \
  "s_waitcnt lgkmcnt(11)\n\t" \
  "v_smfmac_f32_16x16x64_bf16 v[68:71], v[60:63], v[0:7], v104\n\t" \
  "ds_read_b128 v[92:95], v119 offset:2048\n\t" \
  "v_smfmac_f32_16x16x64_bf16 v[72:75], v[60:63], v[8:15], v104\n\t" \
  "ds_read_b128 v[96:99], v119 offset:3072\n\t" \
  "s_waitcnt lgkmcnt(10)\n\t" \
  "v_smfmac_f32_16x16x64_bf16 v[68:71], v[64:67], v[120:127], v104\n\t" \
  "ds_read_b128 v[0:3], v102 offset:32768\n\t" \
  "v_smfmac_f32_16x16x64_bf16 v[72:75], v[64:67], v[128:135], v104\n\t" \
  "ds_read_b128 v[4:7], v102 offset:33792\n\t" \
  "s_waitcnt lgkmcnt(9)\n\t" \
  "v_smfmac_f32_16x16x64_bf16 v[68:71], v[48:51], v[216:223], v104\n\t" \
  "ds_read_b128 v[8:11], v102 offset:34816\n\t" \
  "s_waitcnt lgkmcnt(7)\n\t" \
  "v_smfmac_f32_16x16x64_bf16 v[68:71], v[52:55], v[16:23], v104\n\t" \
  "ds_read_b128 v[12:15], v102 offset:35840\n\t" \
  "v_smfmac_f32_16x16x64_bf16 v[72:75], v[48:51], v[224:231], v104\n\t" \
  "ds_read_b128 v[16:19], v119 offset:2064\n\t" \
  "ds_read_b128 v[20:23], v119 offset:3088\n\t" \
  "s_waitcnt lgkmcnt(8)\n\t" \
  "v_smfmac_f32_16x16x64_bf16 v[72:75], v[52:55], v[24:31], v104\n\t" \
  "v_add_f32 v116, v68, v70\n\t" \
  "v_add_f32 v117, v69, v71\n\t" \
  "v_smfmac_f32_16x16x64_bf16 v[76:79], v[56:59], a[176:183], v104\n\t" \
  "v_cvt_pk_bf16_f32 v116, v116, v117\n\t" \
  "ds_read_b128 v[24:27], v103 offset:2048\n\t" \
  "v_smfmac_f32_16x16x64_bf16 v[80:83], v[56:59], a[184:191], v104\n\t" \
  "v_add_f32 v117, v72, v74\n\t" \
  "v_add_f32 v118, v73, v75\n\t" \
  "v_cvt_pk_bf16_f32 v117, v117, v118\n\t" \
  "ds_write2st64_b32 v105, v116, v117 offset1:1\n\t" \
  "v_smfmac_f32_16x16x64_bf16 v[76:79], v[60:63], a[240:247], v104\n\t" \
  "v_mov_b64 v[68:69], 0\n\t" \
  "v_smfmac_f32_16x16x64_bf16 v[80:83], v[60:63], a[248:255], v104\n\t" \
  "v_mov_b64 v[70:71], 0\n\t" \
  "v_smfmac_f32_16x16x64_bf16 v[76:79], v[64:67], v[136:143], v104\n\t" \
  "v_mov_b64 v[72:73], 0\n\t" \
  "v_smfmac_f32_16x16x64_bf16 v[80:83], v[64:67], v[144:151], v104\n\t" \
  "s_waitcnt lgkmcnt(0)\n\t" \
  "s_barrier\n\t" \
  "ds_read_b64_tr_b16 v[84:85], v107 offset:0\n\t" \
  "ds_read_b64_tr_b16 v[86:87], v107 offset:128\n\t" \
  "v_smfmac_f32_16x16x64_bf16 v[76:79], v[48:51], v[232:239], v104\n\t" \
  "ds_read_b64_tr_b16 v[88:89], v107 offset:1024\n\t" \
  "ds_read_b64_tr_b16 v[90:91], v107 offset:1152\n\t" \
  "v_smfmac_f32_16x16x64_bf16 v[80:83], v[48:51], v[240:247], v104\n\t" \
  "ds_read_b128 v[56:59], v101 offset:4096\n\t" \
  "v_mov_b64 v[74:75], 0\n\t" \
  "v_smfmac_f32_16x16x64_bf16 v[76:79], v[52:55], v[200:207], v104\n\t" \
  "ds_read_b128 v[60:63], v101 offset:13312\n\t" \
  "ds_read_b128 v[28:31], v103 offset:3072\n\t" \
  "v_smfmac_f32_16x16x64_bf16 v[80:83], v[52:55], v[208:215], v104\n\t" \
  "s_waitcnt lgkmcnt(5)\n\t" \
  "v_smfmac_f32_16x16x64_bf16 v[68:71], v[84:87], v[32:39], v104\n\t" \
  "v_smfmac_f32_16x16x64_bf16 v[72:75], v[84:87], v[40:47], v104\n\t" \
  "ds_read_b128 v[64:67], v101 offset:6144\n\t" \
  "s_waitcnt lgkmcnt(4)\n\t" \
  "v_smfmac_f32_16x16x64_bf16 v[68:71], v[88:91], v[92:99], v104\n\t" \
  "v_smfmac_f32_16x16x64_bf16 v[72:75], v[88:91], v[16:23], v104\n\t" \
  "ds_read_b128 v[48:51], v101 offset:8192\n\t" \
  "s_waitcnt lgkmcnt(4)\n\t" \
  "v_smfmac_f32_16x16x64_bf16 v[76:79], v[56:59], v[0:7], v104\n\t" \
  "ds_read_b128 v[16:19], v103 offset:0\n\t" \
  "v_add_f32 v40, v68, v70\n\t" \
  "v_smfmac_f32_16x16x64_bf16 v[80:83], v[56:59], v[8:15], v104\n\t" \
  "v_add_f32 v41, v72, v74\n\t" \
  "v_max_f32 v42, v40, v41\n\t" \
  "s_waitcnt lgkmcnt(4)\n\t" \
  "v_smfmac_f32_16x16x64_bf16 v[76:79], v[60:63], v[184:191], v104\n\t" \
  "ds_read_b128 v[20:23], v103 offset:1024\n\t" \
  "v_max_f32_dpp v42, v42, v42 quad_perm:[1,0,3,2] row_mask:0xf bank_mask:0xf\n\t" \
  "v_smfmac_f32_16x16x64_bf16 v[80:83], v[60:63], v[192:199], v104\n\t" \
  "ds_read_b128 v[52:55], v101 offset:5120\n\t" \
  "v_max_f32_dpp v42, v42, v42 quad_perm:[2,3,0,1] row_mask:0xf bank_mask:0xf\n\t" \
  "s_waitcnt lgkmcnt(4)\n\t" \
  "v_smfmac_f32_16x16x64_bf16 v[76:79], v[64:67], a[144:151], v104\n\t" \
  "ds_read_b128 v[56:59], v101 offset:9216\n\t" \
  "v_max_f32_dpp v42, v42, v42 row_half_mirror row_mask:0xf bank_mask:0xf\n\t" \
  "v_smfmac_f32_16x16x64_bf16 v[80:83], v[64:67], a[152:159], v104\n\t" \
  "ds_read_b128 v[60:63], v101 offset:12288\n\t" \
  "v_max_f32_dpp v42, v42, v42 row_mirror row_mask:0xf bank_mask:0xf\n\t" \
  "v_mul_f32 v42, %[cs], v42\n\t" \
  "s_waitcnt lgkmcnt(3)\n\t" \
  "v_smfmac_f32_16x16x64_bf16 v[76:79], v[48:51], v[16:23], v104\n\t" \
  "ds_read_b128 v[0:3], v103 offset:32768\n\t" \
  "v_fma_f32 v40, v40, %[cs], -v42\n\t" \
  "v_fma_f32 v41, v41, %[cs], -v42\n\t" \
  "v_smfmac_f32_16x16x64_bf16 v[80:83], v[48:51], v[24:31], v104\n\t" \
  "ds_read_b128 v[4:7], v103 offset:33792\n\t" \
  "v_exp_f32 v43, v40\n\t" \
  "v_exp_f32 v44, v41\n\t" \
  "s_waitcnt lgkmcnt(4)\n\t" \
  "v_smfmac_f32_16x16x64_bf16 v[76:79], v[52:55], a[128:135], v104\n\t" \
  "ds_read_b128 v[8:11], v103 offset:34816\n\t" \
  "v_cvt_pk_bf16_f32 v45, v43, v44\n\t" \
  "buffer_store_dword v45, v112, %[rh], s47 offen sc1\n\t" \
  "v_smfmac_f32_16x16x64_bf16 v[80:83], v[52:55], a[136:143], v104\n\t" \
  "ds_read_b128 v[12:15], v103 offset:35840\n\t" \
  "s_waitcnt lgkmcnt(5)\n\t" \
  "v_smfmac_f32_16x16x64_bf16 v[76:79], v[56:59], a[224:231], v104\n\t" \
  "s_waitcnt lgkmcnt(2)\n\t" \
  "v_smfmac_f32_16x16x64_bf16 v[76:79], v[60:63], v[0:7], v104\n\t" \
  "v_smfmac_f32_16x16x64_bf16 v[80:83], v[56:59], a[232:239], v104\n\t" \
  "s_waitcnt lgkmcnt(0)\n\t" \
  "v_smfmac_f32_16x16x64_bf16 v[80:83], v[60:63], v[8:15], v104\n\t" \
  "v_add_f32 v20, v76, v78\n\t" \
  "v_add_f32 v21, v77, v79\n\t" \
  "v_cvt_pk_bf16_f32 v20, v20, v21\n\t" \
  "ds_write_b32 v106, v20\n\t" \
  "v_add_f32 v92, v43, v44\n\t" \
  "v_mov_b32 v25, v115\n\t" \
  "s_nop 1\n\t" \
  "v_add_f32 v23, v80, v82\n\t" \
  "v_add_f32 v21, v81, v83\n\t" \
  "v_cvt_pk_bf16_f32 v21, v23, v21\n\t" \
  "ds_write_b32 v106, v21 offset:256\n\t" \
  "v_add_f32_dpp v92, v92, v92 row_ror:8 row_mask:0xf bank_mask:0xf\n\t" \
  "v_mov_b64 v[0:1], 0\n\t" \
  "v_mov_b64 v[2:3], 0\n\t" \
  "v_mov_b32 v27, v114\n\t" \
  "s_waitcnt lgkmcnt(0)\n\t" \
  "s_barrier\n\t" \
  "ds_read_b64_tr_b16 v[4:5], v108 offset:0\n\t" \
  "ds_read_b64_tr_b16 v[6:7], v108 offset:64\n\t" \
  "ds_read_b64_tr_b16 v[8:9], v108 offset:512\n\t" \
  "ds_read_b64_tr_b16 v[10:11], v108 offset:576\n\t" \
  "ds_read_b64_tr_b16 v[12:13], v108 offset:1024\n\t" \
  "ds_read_b64_tr_b16 v[14:15], v108 offset:1088\n\t" \
  "ds_read_b64_tr_b16 v[16:17], v108 offset:1536\n\t" \
  "ds_read_b64_tr_b16 v[18:19], v108 offset:1600\n\t" \
  "s_waitcnt lgkmcnt(4)\n\t" \
  "v_smfmac_f32_16x16x64_bf16 v[0:3], v[84:87], v[4:11], v104\n\t" \
  "s_waitcnt lgkmcnt(0)\n\t" \
  "v_smfmac_f32_16x16x64_bf16 v[0:3], v[88:91], v[12:19], v104\n\t" \
  "s_nop 7\n\t" \
  "v_add_f32 v96, v0, v2\n\t" \
  "v_fma_f32 v96, v96, %[cs], -v42\n\t" \
  "v_exp_f32 v24, v96\n\t" \
  "s_nop 0\n\t" \
  "v_add_f32 v29, v92, v24\n\t" \
  "s_nop 0\n\t" \
  "v_mov_b32_dpp v25, v24 quad_perm:[1,0,3,2] row_mask:0xf bank_mask:0x3\n\t" \
  "v_add_f32_dpp v29, v29, v29 quad_perm:[1,0,3,2] row_mask:0xf bank_mask:0xf\n\t" \
  "v_mov_b32_dpp v27, v24 quad_perm:[0,1,2,3] row_mask:0xf bank_mask:0x3\n\t" \
  "s_nop 0\n\t" \
  "v_add_f32_dpp v29, v29, v29 quad_perm:[2,3,0,1] row_mask:0xf bank_mask:0xf\n\t" \
  "s_nop 1\n\t" \
  "v_add_f32_dpp v29, v29, v29 row_half_mirror row_mask:0xf bank_mask:0xf\n\t" \
  "v_rcp_f32 v26, v29\n\t" \
  "v_cmpx_gt_f32 vcc, 0x5d5e0b6b, v29\n\t" \
  "v_mul_f32 v24, v27, v26\n\t" \
  "v_mul_f32 v25, v25, v26\n\t" \
  "v_cvt_pk_bf16_f32 v24, v24, v25\n\t" \
  "buffer_store_dword v24, v113, %[rh], s47 offen sc1\n\t" \
  "s_memtime s[76:77]\n\t" \
  "s_mov_b64 exec, -1\n\t" \
  "v_mov_b32 v97, v29\n\t" \
  "s_cmp_eq_u64 vcc, -1\n\t" \
  "s_cselect_b32 s42, 0, 8\n\t" \
  "s_waitcnt lgkmcnt(0)\n\t" \
  "s_mov_b32 s57, s76\n\t" \
  "s_add_i32 s41, s56, 1950\n\t" \
  "s_sub_i32 s76, s92, s51\n\t" \
  "s_sub_i32 s77, s93, s53\n\t" \
  "s_cmp_eq_u32 s76, 0\n\t" \
  "s_cbranch_scc1 21f\n\t" \
  "v_cvt_f32_u32 v117, s76\n\t" \
  "v_cvt_f32_u32 v116, s77\n\t" \
  "v_rcp_f32 v117, v117\n\t" \
  "s_mov_b32 s51, s92\n\t" \
  "v_mul_f32 v117, v117, v116\n\t" \
  "v_max_f32 v117, 0x3f028f5c, v117\n\t" \
  "v_min_f32 v117, 0x3ffeb852, v117\n\t" \
  "v_rcp_f32 v116, v117\n\t" \
  "v_mul_f32 v117, 0x4f000000, v117\n\t" \
  "v_mul_f32 v116, 0x47800000, v116\n\t" \
  "v_cvt_u32_f32 v117, v117\n\t" \
  "v_cvt_u32_f32 v116, v116\n\t" \
  "s_mov_b32 s53, s93\n\t" \
  "v_readfirstlane_b32 s46, v117\n\t" \
  "v_readfirstlane_b32 s52, v116\n\t" \
  "21:\n\t" \
  "s_add_i32 s44, s44, 1\n\t" \
  "s_and_b32 s58, s58, 4\n\t" \
  "s_and_b32 s76, s44, 64\n\t" \
  "s_or_b32 s42, s42, s76\n\t" \
  "s_cbranch_scc0 20b\n\t" \
  "s_branch 99f\n\t" \
  "14:\n\t" \
  "s_add_u32 s59, s59, 0x10000\n\t" \
  "s_cmp_lt_u32 s59, 0x100000\n\t" \
  "s_cbranch_scc1 13b\n\t" \
  "s_mov_b64 exec, -1\n\t" \
  "s_mov_b32 s42, 16\n\t" \
  "s_branch 99f\n\t" \
  "8:\n\t" \
  "s_bitcmp1_b32 s58, 1\n\t" \
  "s_cbranch_scc1 4b\n\t" \
  "s_bitcmp1_b32 s58, 2\n\t" \
  "s_cbranch_scc1 5b\n\t" \
  "v_max3_u32 v116, v32, v33, v34\n\t" \
  "v_max3_u32 v117, v35, v36, v37\n\t" \
  "v_max3_u32 v116, v116, v38, v39\n\t" \
  "v_max_u32 v116, v116, v117\n\t" \
  "v_cmp_eq_u32 vcc, -1, v116\n\t" \
  "s_cbranch_vccz 5b\n\t" \
  "s_branch 0f\n\t" \
  "9:\n\t" \
  "s_bitcmp1_b32 s58, 2\n\t" \
  "s_cbranch_scc1 6b\n\t" \
  "v_max3_u32 v116, v40, v41, v42\n\t" \
  "v_max3_u32 v117, v43, v44, v45\n\t" \
  "v_max3_u32 v116, v116, v46, v47\n\t" \
  "v_max_u32 v116, v116, v117\n\t" \
  "v_cmp_eq_u32 vcc, -1, v116\n\t" \
  "s_cbranch_vccz 6b\n\t" \
  "s_or_b32 s58, s58, 2\n\t" \
  "0:\n\t" \
  "s_or_b32 s58, s58, 1\n\t" \
  "11:\n\t" \
  "buffer_load_dwordx4 v[32:35], v100, %[rh], s40 offen sc1\n\t" \
  "buffer_load_dwordx4 v[36:39], v100, %[rh], s76 offen sc1\n\t" \
  "buffer_load_dwordx4 v[40:43], v100, %[rh], s77 offen sc1\n\t" \
  "buffer_load_dwordx4 v[44:47], v100, %[rh], s78 offen sc1\n\t" \
  "s_waitcnt lgkmcnt(0)\n\t" \
  "s_branch 10b\n\t" \
  "17:\n\t" \
  "v_mov_b64 v[68:69], 0\n\t" \
  "v_mov_b64 v[70:71], 0\n\t" \
  "v_mov_b64 v[72:73], 0\n\t" \
  "v_mov_b64 v[74:75], 0\n\t" \
  "v_mov_b64 v[76:77], 0\n\t" \
  "v_mov_b64 v[78:79], 0\n\t" \
  "v_mov_b64 v[80:81], 0\n\t" \
  "v_mov_b64 v[82:83], 0\n\t" \
  "s_add_u32 s59, s59, 1\n\t" \
  "s_bitcmp1_b32 s58, 2\n\t" \
  "s_cbranch_scc1 3b\n\t" \
  "s_waitcnt vmcnt(0)\n\t" \
  "v_max_u32 v116, v32, v33\n\t" \
  "v_max3_u32 v116, v116, v34, v35\n\t" \
  "v_max3_u32 v116, v116, v36, v37\n\t" \
  "v_max3_u32 v116, v116, v38, v39\n\t" \
  "v_max3_u32 v116, v116, v40, v41\n\t" \
  "v_max3_u32 v116, v116, v42, v43\n\t" \
  "v_max3_u32 v116, v116, v44, v45\n\t" \
  "v_max3_u32 v116, v116, v46, v47\n\t" \
  "v_cmp_eq_u32 vcc, -1, v116\n\t" \
  "s_cbranch_vccz 3b\n\t" \
  "s_bitcmp1_b32 s59, 6\n\t" \
  "s_cbranch_scc1 12f\n\t" \
  "buffer_load_dwordx4 v[32:35], v100, %[rh], s40 offen sc1\n\t" \
  "buffer_load_dwordx4 v[36:39], v100, %[rh], s76 offen sc1\n\t" \
  "buffer_load_dwordx4 v[40:43], v100, %[rh], s77 offen sc1\n\t" \
  "buffer_load_dwordx4 v[44:47], v100, %[rh], s78 offen sc1\n\t" \
  "s_branch 17b\n\t" \
  "12:\n\t" \
  "s_mov_b32 s42, s58\n\t" \
  "s_branch 99f\n\t" \
  "91:\n\t" \
  "s_mov_b32 s58, s42\n\t" \
  ".p2align 6\n\t" \
  "20:\n\t" \
  "s_and_b32 s76, s44, 7\n\t" \
  "s_lshl_b32 s76, s76, 15\n\t" \
  "s_add_i32 s40, s54, s76\n\t" \
  "s_and_b32 s76, s44, 3\n\t" \
  "s_lshl_b32 s45, s76, 16\n\t" \
  "s_mov_b32 s47, s45\n\t" \
  "s_add_i32 s49, s44, -1\n\t" \
  "s_and_b32 s48, s49, 3\n\t" \
  "s_lshl_b32 s48, s48, 16\n\t" \
  "s_lshl_b32 s50, s53, 8\n\t" \
  "s_mov_b32 s59, 0\n\t" \
  "s_mov_b32 s92, s51\n\t" \
  "s_mov_b32 s93, s53\n\t" \
  "s_xor_b32 s76, s40, 0x1000\n\t" \
  "s_xor_b32 s77, s40, 0x4000\n\t" \
  "s_xor_b32 s78, s40, 0x5000\n\t" \
  "s_cmp_lg_u32 s58, 0\n\t" \
  "s_cbranch_scc1 11f\n\t" \
  "10:\n\t" \
  "ds_read_b128 v[48:51], v102 offset:32768\n\t" \
  "ds_read_b128 v[52:55], v102 offset:33792\n\t" \
  "ds_read_b128 v[56:59], v102 offset:34816\n\t" \
  "ds_read_b128 v[60:63], v102 offset:35840\n\t" \
  "ds_read_b128 v[16:19], v102 offset:49152\n\t" \
  "ds_read_b128 v[20:23], v102 offset:50176\n\t" \
  "ds_read_b128 v[24:27], v102 offset:51200\n\t" \
  "ds_read_b128 v[28:31], v102 offset:52224\n\t" \
  "ds_read_b128 v[84:87], v103 offset:16384\n\t" \
  "ds_read_b128 v[88:91], v103 offset:17408\n\t" \
  "ds_read_b128 v[92:95], v103 offset:18432\n\t" \
  "ds_read_b128 v[96:99], v103 offset:19456\n\t" \
  "s_waitcnt lgkmcnt(4)\n\t" \
  "ds_read_b128 v[0:3], v103 offset:49152\n\t" \
  "ds_read_b128 v[4:7], v103 offset:50176\n\t" \
  "ds_read_b128 v[8:11], v103 offset:51200\n\t" \
  "ds_read_b128 v[12:15], v103 offset:52224\n\t" \
  "s_bitcmp1_b32 s58, 1\n\t" \
  "s_cselect_b64 s[80:81], -1, 0\n\t" \
  "s_cmp_lg_u32 s58, 0\n\t" \
  "s_cbranch_scc1 17f\n\t" \
  "s_mov_b32 exec_hi, 0\n\t" \
  "16:\n\t" \
  "s_memtime vcc\n\t" \
  "s_waitcnt lgkmcnt(0)\n\t" \
  "s_sub_i32 s79, s41, vcc_lo\n\t" \
  "s_cmp_gt_i32 s79, 64\n\t" \
  "s_cbranch_scc1 16b\n\t" \
  "13:\n\t" \
  "buffer_load_dword v116, v110, %[rh], s48 offen sc1\n\t" \
  "s_waitcnt vmcnt(0)\n\t" \
  "15:\n\t" \
  "v_mov_b32 v117, 20000\n\t" \
  "v_cmp_ne_u32_sdwa vcc, v116, s49 src0_sel:BYTE_0 src1_sel:DWORD\n\t" \
  "v_subrev_u32 v116, s50, v116\n\t" \
  "v_ashrrev_i32 v116, 8, v116\n\t" \
  "s_nop 1\n\t" \
  "v_max_i32_dpp v116, v116, v116 quad_perm:[1,0,3,2] row_mask:0xf bank_mask:0xf\n\t" \
  "s_cbranch_vccnz 14f\n\t" \
  "s_nop 0\n\t" \
  "v_max_i32_dpp v116, v116, v116 quad_perm:[2,3,0,1] row_mask:0xf bank_mask:0xf\n\t" \
  "s_nop 1\n\t" \
  "v_max_i32_dpp v116, v116, v116 row_half_mirror row_mask:0xf bank_mask:0xf\n\t" \
  "s_nop 1\n\t" \
  "v_max_i32_dpp v116, v116, v116 row_mirror row_mask:0xf bank_mask:0xf\n\t" \
  "v_med3_i32 v116, v116, 0, v117\n\t" \
  "v_mul_u32_u24 v116, s52, v116\n\t" \
  "v_lshrrev_b32 v116, 16, v116\n\t" \
  "s_mov_b64 exec, -1\n\t" \
  "v_readlane_b32 s84, v116, 0\n\t" \
  "v_readlane_b32 s85, v116, 16\n\t" \
  "s_max_u32 s84, s84, s85\n\t" \
  "s_add_i32 s84, s84, s51\n\t" \
  "v_mov_b64 v[68:69], 0\n\t" \
  "v_mov_b64 v[70:71], 0\n\t" \
  "v_mov_b64 v[72:73], 0\n\t" \
  "v_mov_b64 v[74:75], 0\n\t" \
  "v_mov_b64 v[76:77], 0\n\t" \
  "v_mov_b64 v[78:79], 0\n\t" \
  "v_mov_b64 v[80:81], 0\n\t" \
  "v_mov_b64 v[82:83], 0\n\t" \
  "1:\n\t" \
  "s_memtime vcc\n\t" \
  "s_waitcnt lgkmcnt(0)\n\t" \
  "s_sub_i32 s79, s84, vcc_lo\n\t" \
  "s_cmp_gt_i32 s79, 128\n\t" \
  "s_cbranch_scc1 1b\n\t" \
  "2:\n\t" \
  "s_sub_i32 s79, s79, 33\n\t" \
  "s_cmp_gt_i32 s79, 56\n\t" \
  "s_cbranch_scc1 2b\n\t" \
  "buffer_load_dwordx4 v[32:35], v100, %[rh], s40 offen sc1\n\t" \
  "s_nop 12\n\t" \
  "buffer_load_dwordx4 v[36:39], v100, %[rh], s76 offen sc1\n\t" \
  "s_nop 12\n\t" \
  "buffer_load_dwordx4 v[40:43], v100, %[rh], s77 offen sc1\n\t" \
  "s_nop 12\n\t" \
  "buffer_load_dwordx4 v[44:47], v100, %[rh], s78 offen sc1\n\t" \
  "s_memtime vcc\n\t" \
  "s_memrealtime s[82:83]\n\t" \
  "s_nop 4\n\t" \
  "s_memrealtime s[84:85]\n\t" \
  "s_nop 4\n\t" \
  "s_memrealtime s[94:95]\n\t" \
  "s_nop 4\n\t" \
  "s_memrealtime s[86:87]\n\t" \
  "s_nop 4\n\t" \
  "s_memrealtime s[88:89]\n\t" \
  "s_nop 4\n\t" \
  "s_memrealtime s[90:91]\n\t" \
  "s_mov_b32 s92, 0\n\t" \
  "s_waitcnt lgkmcnt(0)\n\t" \
  "s_cmp_eq_u32 s82, s84\n\t" \
  "s_addc_u32 s92, s92, 0\n\t" \
  "s_cmp_eq_u32 s82, s94\n\t" \
  "s_addc_u32 s92, s92, 0\n\t" \
  "s_cmp_eq_u32 s82, s86\n\t" \
  "s_addc_u32 s92, s92, 0\n\t" \
  "s_cmp_eq_u32 s82, s88\n\t" \
  "s_addc_u32 s92, s92, 0\n\t" \
  "s_cmp_eq_u32 s82, s90\n\t" \
  "s_addc_u32 s92, s92, 0\n\t" \
  "s_mul_i32 s92, s92, 24\n\t" \
  "s_add_i32 s92, s92, vcc_lo\n\t" \
  "s_lshl4_add_u32 s93, s82, 64\n\t" \
  "3:\n\t" \
  "s_waitcnt lgkmcnt(0)\n\t" \
  "s_waitcnt vmcnt(3)\n\t" \
  "v_smfmac_f32_16x16x64_bf16 v[68:71], v[32:35], a[96:103], v104\n\t" \
  "ds_write_b128 v101, v[32:35] offset:4096\n\t" \
  "v_smfmac_f32_16x16x64_bf16 v[72:75], v[32:35], a[104:111], v104\n\t" \
  "v_smfmac_f32_16x16x64_bf16 v[76:79], v[32:35], v[48:55], v104\n\t" \
  "v_smfmac_f32_16x16x64_bf16 v[80:83], v[32:35], v[56:63], v104\n\t" \
  "s_waitcnt vmcnt(2)\n\t" \
  "v_smfmac_f32_16x16x64_bf16 v[68:71], v[36:39], a[112:119], v104\n\t" \
  "ds_write_b128 v101, v[36:39] offset:5120\n\t" \
  "v_smfmac_f32_16x16x64_bf16 v[72:75], v[36:39], a[120:127], v104\n\t" \
  "v_smfmac_f32_16x16x64_bf16 v[76:79], v[36:39], a[128:135], v104\n\t" \
  "v_cmp_u_f32 vcc, v68, v69\n\t" \
  "v_smfmac_f32_16x16x64_bf16 v[80:83], v[36:39], a[136:143], v104\n\t" \
  "v_cmp_u_f32 s[82:83], v70, v71\n\t" \
  "s_waitcnt vmcnt(1)\n\t" \
  "v_smfmac_f32_16x16x64_bf16 v[68:71], v[40:43], v[16:23], v104\n\t" \
  "ds_write_b128 v101, v[40:43] offset:6144\n\t" \
  "v_smfmac_f32_16x16x64_bf16 v[72:75], v[40:43], v[24:31], v104\n\t" \
  "s_or_b64 vcc, vcc, s[82:83]\n\t" \
  "s_or_b64 vcc, vcc, s[80:81]\n\t" \
  "s_waitcnt lgkmcnt(1)\n\t" \
  "v_smfmac_f32_16x16x64_bf16 v[76:79], v[40:43], a[144:151], v104\n\t" \
  "s_cbranch_scc1 8f\n\t" \
  "5:\n\t" \
  "s_barrier\n\t" \
  "4:\n\t" \
  "s_memtime s[56:57]\n\t" \
  "ds_read_b128 v[48:51], v101 offset:8192\n\t" \
  "v_smfmac_f32_16x16x64_bf16 v[80:83], v[40:43], a[152:159], v104\n\t" \
  "ds_read_b128 v[52:55], v101 offset:9216\n\t" \
  "ds_read_b128 v[56:59], v101 offset:12288\n\t" \
  "s_waitcnt vmcnt(0)\n\t" \
  "v_smfmac_f32_16x16x64_bf16 v[68:71], v[44:47], a[160:167], v104\n\t" \
  "ds_write_b128 v101, v[44:47] offset:7168\n\t" \
  "v_smfmac_f32_16x16x64_bf16 v[72:75], v[44:47], a[168:175], v104\n\t" \
  "v_smfmac_f32_16x16x64_bf16 v[76:79], v[44:47], a[176:183], v104\n\t" \
  "v_cmp_u_f32 vcc, v68, v69\n\t" \
  "v_smfmac_f32_16x16x64_bf16 v[80:83], v[44:47], a[184:191], v104\n\t" \
  "v_cmp_u_f32 s[82:83], v70, v71\n\t" \
  "s_waitcnt lgkmcnt(3)\n\t" \
  "v_smfmac_f32_16x16x64_bf16 v[68:71], v[48:51], a[192:199], v104\n\t" \
  "ds_read_b128 v[60:63], v101 offset:13312\n\t" \
  "ds_read_b128 v[64:67], v101 offset:0\n\t" \
  "v_smfmac_f32_16x16x64_bf16 v[72:75], v[48:51], a[200:207], v104\n\t" \
  "s_or_b64 vcc, vcc, s[82:83]\n\t" \
  "s_waitcnt lgkmcnt(2)\n\t" \
  "v_smfmac_f32_16x16x64_bf16 v[68:71], v[52:55], a[208:215], v104\n\t" \
  "s_cbranch_scc1 9f\n\t" \
  "6:\n\t" \
  "s_barrier\n\t" \
  "v_smfmac_f32_16x16x64_bf16 v[72:75], v[52:55], a[216:223], v104\n\t" \
  "ds_read_b128 v[48:51], v101 offset:1024\n\t" \
  "v_smfmac_f32_16x16x64_bf16 v[68:71], v[56:59], v[152:159], v104\n\t" \
  "ds_read_b128 v[52:55], v101 offset:10240\n\t" \
  "v_smfmac_f32_16x16x64_bf16 v[72:75], v[56:59], v[160:167], v104\n\t" \
  "s_waitcnt lgkmcnt(3)\n\t" \
  "v_smfmac_f32_16x16x64_bf16 v[68:71], v[60:63], v[168:175], v104\n\t" \
  "ds_read_b128 v[56:59], v101 offset:11264\n\t" \
  "v_smfmac_f32_16x16x64_bf16 v[72:75], v[60:63], v[176:183], v104\n\t" \
  "s_waitcnt lgkmcnt(3)\n\t" \
  "v_smfmac_f32_16x16x64_bf16 v[68:71], v[64:67], a[0:7], v104\n\t" \
  "ds_read_b128 v[60:63], v101 offset:14336\n\t" \
  "ds_read_b128 v[32:35], v119 offset:0\n\t" \
  "v_smfmac_f32_16x16x64_bf16 v[72:75], v[64:67], a[8:15], v104\n\t" \
  "ds_read_b128 v[16:19], v102 offset:16384\n\t" \
  "s_waitcnt lgkmcnt(5)\n\t" \
  "v_smfmac_f32_16x16x64_bf16 v[68:71], v[48:51], a[16:23], v104\n\t" \
  "ds_read_b128 v[64:67], v101 offset:15360\n\t" \
  "ds_read_b128 v[36:39], v119 offset:1024\n\t" \
  "v_smfmac_f32_16x16x64_bf16 v[72:75], v[48:51], a[24:31], v104\n\t" \
  "ds_read_b128 v[20:23], v102 offset:17408\n\t" \
  "s_waitcnt lgkmcnt(7)\n\t" \
  "v_smfmac_f32_16x16x64_bf16 v[68:71], v[52:55], v[84:91], v104\n\t" \
  "ds_read_b128 v[48:51], v101 offset:3072\n\t" \
  "ds_read_b128 v[40:43], v119 offset:16\n\t" \
  "v_smfmac_f32_16x16x64_bf16 v[72:75], v[52:55], v[92:99], v104\n\t" \
  "ds_read_b128 v[24:27], v102 offset:18432\n\t" \
  "s_waitcnt lgkmcnt(9)\n\t" \
  "v_smfmac_f32_16x16x64_bf16 v[68:71], v[56:59], v[120:127], v104\n\t" \
  "ds_read_b128 v[52:55], v101 offset:2048\n\t" \
  "ds_read_b128 v[44:47], v119 offset:1040\n\t" \
  "v_smfmac_f32_16x16x64_bf16 v[72:75], v[56:59], v[128:135], v104\n\t" \
  "ds_read_b128 v[28:31], v102 offset:19456\n\t" \
  "s_waitcnt lgkmcnt(11)\n\t" \
  "v_smfmac_f32_16x16x64_bf16 v[68:71], v[60:63], v[0:7], v104\n\t" \
  "ds_read_b128 v[92:95], v119 offset:2048\n\t" \
  "v_smfmac_f32_16x16x64_bf16 v[72:75], v[60:63], v[8:15], v104\n\t" \
  "ds_read_b128 v[96:99], v119 offset:3072\n\t" \
  "s_waitcnt lgkmcnt(10)\n\t" \
  "v_smfmac_f32_16x16x64_bf16 v[68:71], v[64:67], v[216:223], v104\n\t" \
  "ds_read_b128 v[0:3], v103 offset:0\n\t" \
  "v_smfmac_f32_16x16x64_bf16 v[72:75], v[64:67], v[224:231], v104\n\t" \
  "ds_read_b128 v[4:7], v103 offset:1024\n\t" \
  "s_waitcnt lgkmcnt(9)\n\t" \
  "v_smfmac_f32_16x16x64_bf16 v[68:71], v[48:51], a[64:71], v104\n\t" \
  "ds_read_b128 v[8:11], v103 offset:2048\n\t" \
  "s_waitcnt lgkmcnt(7)\n\t" \
  "v_smfmac_f32_16x16x64_bf16 v[68:71], v[52:55], v[16:23], v104\n\t" \
  "ds_read_b128 v[12:15], v103 offset:3072\n\t" \
  "v_smfmac_f32_16x16x64_bf16 v[72:75], v[48:51], a[72:79], v104\n\t" \
  "ds_read_b128 v[16:19], v119 offset:2064\n\t" \
  "ds_read_b128 v[20:23], v119 offset:3088\n\t" \
  "s_waitcnt lgkmcnt(8)\n\t" \
  "v_smfmac_f32_16x16x64_bf16 v[72:75], v[52:55], v[24:31], v104\n\t" \
  "v_add_f32 v116, v68, v70\n\t" \
  "v_add_f32 v117, v69, v71\n\t" \
  "v_smfmac_f32_16x16x64_bf16 v[76:79], v[56:59], v[136:143], v104\n\t" \
  "v_cvt_pk_bf16_f32 v116, v116, v117\n\t" \
  "ds_read_b128 v[24:27], v103 offset:34816\n\t" \
  "v_smfmac_f32_16x16x64_bf16 v[80:83], v[56:59], v[144:151], v104\n\t" \
  "v_add_f32 v117, v72, v74\n\t" \
  "v_add_f32 v118, v73, v75\n\t" \
  "v_cvt_pk_bf16_f32 v117, v117, v118\n\t" \
  "ds_write2st64_b32 v105, v116, v117 offset1:1\n\t" \
  "v_smfmac_f32_16x16x64_bf16 v[76:79], v[60:63], v[200:207], v104\n\t" \
  "v_mov_b64 v[68:69], 0\n\t" \
  "v_smfmac_f32_16x16x64_bf16 v[80:83], v[60:63], v[208:215], v104\n\t" \
  "v_mov_b64 v[70:71], 0\n\t" \
  "v_smfmac_f32_16x16x64_bf16 v[76:79], v[64:67], v[232:239], v104\n\t" \
  "v_mov_b64 v[72:73], 0\n\t" \
  "v_smfmac_f32_16x16x64_bf16 v[80:83], v[64:67], v[240:247], v104\n\t" \
  "s_waitcnt lgkmcnt(0)\n\t" \
  "s_barrier\n\t" \
  "ds_read_b64_tr_b16 v[84:85], v107 offset:0\n\t" \
  "ds_read_b64_tr_b16 v[86:87], v107 offset:128\n\t" \
  "v_smfmac_f32_16x16x64_bf16 v[76:79], v[48:51], a[80:87], v104\n\t" \
  "ds_read_b64_tr_b16 v[88:89], v107 offset:1024\n\t" \
  "ds_read_b64_tr_b16 v[90:91], v107 offset:1152\n\t" \
  "v_smfmac_f32_16x16x64_bf16 v[80:83], v[48:51], a[88:95], v104\n\t" \
  "ds_read_b128 v[56:59], v101 offset:8192\n\t" \
  "v_mov_b64 v[74:75], 0\n\t" \
  "v_smfmac_f32_16x16x64_bf16 v[76:79], v[52:55], a[48:55], v104\n\t" \
  "ds_read_b128 v[60:63], v101 offset:1024\n\t" \
  "ds_read_b128 v[28:31], v103 offset:35840\n\t" \
  "v_smfmac_f32_16x16x64_bf16 v[80:83], v[52:55], a[56:63], v104\n\t" \
  "s_waitcnt lgkmcnt(5)\n\t" \
  "v_smfmac_f32_16x16x64_bf16 v[68:71], v[84:87], v[32:39], v104\n\t" \
  "v_smfmac_f32_16x16x64_bf16 v[72:75], v[84:87], v[40:47], v104\n\t" \
  "ds_read_b128 v[64:67], v101 offset:10240\n\t" \
  "s_waitcnt lgkmcnt(4)\n\t" \
  "v_smfmac_f32_16x16x64_bf16 v[68:71], v[88:91], v[92:99], v104\n\t" \
  "v_smfmac_f32_16x16x64_bf16 v[72:75], v[88:91], v[16:23], v104\n\t" \
  "ds_read_b128 v[48:51], v101 offset:12288\n\t" \
  "s_waitcnt lgkmcnt(4)\n\t" \
  "v_smfmac_f32_16x16x64_bf16 v[76:79], v[56:59], v[0:7], v104\n\t" \
  "ds_read_b128 v[16:19], v103 offset:32768\n\t" \
  "v_add_f32 v40, v69, v71\n\t" \
  "v_smfmac_f32_16x16x64_bf16 v[80:83], v[56:59], v[8:15], v104\n\t" \
  "v_add_f32 v41, v73, v75\n\t" \
  "v_max_f32 v42, v40, v41\n\t" \
  "s_waitcnt lgkmcnt(4)\n\t" \
  "v_smfmac_f32_16x16x64_bf16 v[76:79], v[60:63], a[32:39], v104\n\t" \
  "ds_read_b128 v[20:23], v103 offset:33792\n\t" \
  "v_max_f32_dpp v42, v42, v42 quad_perm:[1,0,3,2] row_mask:0xf bank_mask:0xf\n\t" \
  "v_smfmac_f32_16x16x64_bf16 v[80:83], v[60:63], a[40:47], v104\n\t" \
  "ds_read_b128 v[52:55], v101 offset:9216\n\t" \
  "v_max_f32_dpp v42, v42, v42 quad_perm:[2,3,0,1] row_mask:0xf bank_mask:0xf\n\t" \
  "s_waitcnt lgkmcnt(4)\n\t" \
  "v_smfmac_f32_16x16x64_bf16 v[76:79], v[64:67], a[240:247], v104\n\t" \
  "ds_read_b128 v[56:59], v101 offset:13312\n\t" \
  "v_max_f32_dpp v42, v42, v42 row_half_mirror row_mask:0xf bank_mask:0xf\n\t" \
  "v_smfmac_f32_16x16x64_bf16 v[80:83], v[64:67], a[248:255], v104\n\t" \
  "ds_read_b128 v[60:63], v101 offset:0\n\t" \
  "v_max_f32_dpp v42, v42, v42 row_mirror row_mask:0xf bank_mask:0xf\n\t" \
  "v_mul_f32 v42, %[cs], v42\n\t" \
  "s_waitcnt lgkmcnt(3)\n\t" \
  "v_smfmac_f32_16x16x64_bf16 v[76:79], v[48:51], v[16:23], v104\n\t" \
  "ds_read_b128 v[0:3], v102 offset:0\n\t" \
  "v_fma_f32 v40, v40, %[cs], -v42\n\t" \
  "v_fma_f32 v41, v41, %[cs], -v42\n\t" \
  "v_smfmac_f32_16x16x64_bf16 v[80:83], v[48:51], v[24:31], v104\n\t" \
  "ds_read_b128 v[4:7], v102 offset:1024\n\t" \
  "v_exp_f32 v43, v40\n\t" \
  "v_exp_f32 v44, v41\n\t" \
  "s_waitcnt lgkmcnt(4)\n\t" \
  "v_smfmac_f32_16x16x64_bf16 v[76:79], v[52:55], a[224:231], v104\n\t" \
  "ds_read_b128 v[8:11], v102 offset:2048\n\t" \
  "v_cvt_pk_bf16_f32 v45, v43, v44\n\t" \
  "buffer_store_dword v45, v112, %[rh], s47 offen sc1\n\t" \
  "v_smfmac_f32_16x16x64_bf16 v[80:83], v[52:55], a[232:239], v104\n\t" \
  "ds_read_b128 v[12:15], v102 offset:3072\n\t" \
  "s_waitcnt lgkmcnt(5)\n\t" \
  "v_smfmac_f32_16x16x64_bf16 v[76:79], v[56:59], v[184:191], v104\n\t" \
  "s_waitcnt lgkmcnt(2)\n\t" \
  "v_smfmac_f32_16x16x64_bf16 v[76:79], v[60:63], v[0:7], v104\n\t" \
  "v_smfmac_f32_16x16x64_bf16 v[80:83], v[56:59], v[192:199], v104\n\t" \
  "s_waitcnt lgkmcnt(0)\n\t" \
  "v_smfmac_f32_16x16x64_bf16 v[80:83], v[60:63], v[8:15], v104\n\t" \
  "v_add_f32 v20, v76, v78\n\t" \
  "v_add_f32 v21, v77, v79\n\t" \
  "v_cvt_pk_bf16_f32 v20, v20, v21\n\t" \
  "ds_write_b32 v106, v20\n\t" \
  "v_add_f32 v92, v43, v44\n\t" \
  "v_mov_b32 v25, v115\n\t" \
  "s_nop 1\n\t" \
  "v_add_f32 v23, v80, v82\n\t" \
  "v_add_f32 v21, v81, v83\n\t" \
  "v_cvt_pk_bf16_f32 v21, v23, v21\n\t" \
  "ds_write_b32 v106, v21 offset:256\n\t" \
  "v_add_f32_dpp v92, v92, v92 row_ror:8 row_mask:0xf bank_mask:0xf\n\t" \
  "v_mov_b64 v[0:1], 0\n\t" \
  "v_mov_b64 v[2:3], 0\n\t" \
  "v_mov_b32 v27, v114\n\t" \
  "s_waitcnt lgkmcnt(0)\n\t" \
  "s_barrier\n\t" \
  "ds_read_b64_tr_b16 v[4:5], v108 offset:0\n\t" \
  "ds_read_b64_tr_b16 v[6:7], v108 offset:64\n\t" \
  "ds_read_b64_tr_b16 v[8:9], v108 offset:512\n\t" \
  "ds_read_b64_tr_b16 v[10:11], v108 offset:576\n\t" \
  "ds_read_b64_tr_b16 v[12:13], v108 offset:1024\n\t" \
  "ds_read_b64_tr_b16 v[14:15], v108 offset:1088\n\t" \
  "ds_read_b64_tr_b16 v[16:17], v108 offset:1536\n\t" \
  "ds_read_b64_tr_b16 v[18:19], v108 offset:1600\n\t" \
  "s_waitcnt lgkmcnt(4)\n\t" \
  "v_smfmac_f32_16x16x64_bf16 v[0:3], v[84:87], v[4:11], v104\n\t" \
  "s_waitcnt lgkmcnt(0)\n\t" \
  "v_smfmac_f32_16x16x64_bf16 v[0:3], v[88:91], v[12:19], v104\n\t" \
  "s_nop 7\n\t" \
  "v_add_f32 v96, v1, v3\n\t" \
  "v_fma_f32 v96, v96, %[cs], -v42\n\t" \
  "v_exp_f32 v24, v96\n\t" \
  "s_nop 0\n\t" \
  "v_add_f32 v29, v92, v24\n\t" \
  "s_nop 0\n\t" \
  "v_mov_b32_dpp v25, v24 quad_perm:[1,0,3,2] row_mask:0xf bank_mask:0x3\n\t" \
  "v_add_f32_dpp v29, v29, v29 quad_perm:[1,0,3,2] row_mask:0xf bank_mask:0xf\n\t" \
  "v_mov_b32_dpp v27, v24 quad_perm:[0,1,2,3] row_mask:0xf bank_mask:0x3\n\t" \
  "s_nop 0\n\t" \
  "v_add_f32_dpp v29, v29, v29 quad_perm:[2,3,0,1] row_mask:0xf bank_mask:0xf\n\t" \
  "s_nop 1\n\t" \
  "v_add_f32_dpp v29, v29, v29 row_half_mirror row_mask:0xf bank_mask:0xf\n\t" \
  "v_rcp_f32 v26, v29\n\t" \
  "v_cmpx_gt_f32 vcc, 0x5d5e0b6b, v29\n\t" \
  "v_mul_f32 v24, v27, v26\n\t" \
  "v_mul_f32 v25, v25, v26\n\t" \
  "v_cvt_pk_bf16_f32 v24, v24, v25\n\t" \
  "buffer_store_dword v24, v113, %[rh], s47 offen sc1\n\t" \
  "s_memtime s[76:77]\n\t" \
  "s_mov_b64 exec, -1\n\t" \
  "v_mov_b32 v97, v29\n\t" \
  "s_cmp_eq_u64 vcc, -1\n\t" \
  "s_cselect_b32 s42, 0, 8\n\t" \
  "s_waitcnt lgkmcnt(0)\n\t" \
  "s_mov_b32 s57, s76\n\t" \
  "s_add_i32 s41, s56, 1950\n\t" \
  "s_sub_i32 s76, s92, s51\n\t" \
  "s_sub_i32 s77, s93, s53\n\t" \
  "s_cmp_eq_u32 s76, 0\n\t" \
  "s_cbranch_scc1 21f\n\t" \
  "v_cvt_f32_u32 v117, s76\n\t" \
  "v_cvt_f32_u32 v116, s77\n\t" \
  "v_rcp_f32 v117, v117\n\t" \
  "s_mov_b32 s51, s92\n\t" \
  "v_mul_f32 v117, v117, v116\n\t" \
  "v_max_f32 v117, 0x3f028f5c, v117\n\t" \
  "v_min_f32 v117, 0x3ffeb852, v117\n\t" \
  "v_rcp_f32 v116, v117\n\t" \
  "v_mul_f32 v117, 0x4f000000, v117\n\t" \
  "v_mul_f32 v116, 0x47800000, v116\n\t" \
  "v_cvt_u32_f32 v117, v117\n\t" \
  "v_cvt_u32_f32 v116, v116\n\t" \
  "s_mov_b32 s53, s93\n\t" \
  "v_readfirstlane_b32 s46, v117\n\t" \
  "v_readfirstlane_b32 s52, v116\n\t" \
  "21:\n\t" \
  "s_add_i32 s44, s44, 1\n\t" \
  "s_and_b32 s58, s58, 4\n\t" \
  "s_and_b32 s76, s44, 64\n\t" \
  "s_or_b32 s42, s42, s76\n\t" \
  "s_cbranch_scc0 20b\n\t" \
  "s_branch 99f\n\t" \
  "14:\n\t" \
  "s_add_u32 s59, s59, 0x10000\n\t" \
  "s_cmp_lt_u32 s59, 0x100000\n\t" \
  "s_cbranch_scc1 13b\n\t" \
  "s_mov_b64 exec, -1\n\t" \
  "s_mov_b32 s42, 16\n\t" \
  "s_branch 99f\n\t" \
  "8:\n\t" \
  "s_bitcmp1_b32 s58, 1\n\t" \
  "s_cbranch_scc1 4b\n\t" \
  "s_bitcmp1_b32 s58, 2\n\t" \
  "s_cbranch_scc1 5b\n\t" \
  "v_max3_u32 v116, v32, v33, v34\n\t" \
  "v_max3_u32 v117, v35, v36, v37\n\t" \
  "v_max3_u32 v116, v116, v38, v39\n\t" \
  "v_max_u32 v116, v116, v117\n\t" \
  "v_cmp_eq_u32 vcc, -1, v116\n\t" \
  "s_cbranch_vccz 5b\n\t" \
  "s_branch 0f\n\t" \
  "9:\n\t" \
  "s_bitcmp1_b32 s58, 2\n\t" \
  "s_cbranch_scc1 6b\n\t" \
  "v_max3_u32 v116, v40, v41, v42\n\t" \
  "v_max3_u32 v117, v43, v44, v45\n\t" \
  "v_max3_u32 v116, v116, v46, v47\n\t" \
  "v_max_u32 v116, v116, v117\n\t" \
  "v_cmp_eq_u32 vcc, -1, v116\n\t" \
  "s_cbranch_vccz 6b\n\t" \
  "s_or_b32 s58, s58, 2\n\t" \
  "0:\n\t" \
  "s_or_b32 s58, s58, 1\n\t" \
  "11:\n\t" \
  "buffer_load_dwordx4 v[32:35], v100, %[rh], s40 offen sc1\n\t" \
  "buffer_load_dwordx4 v[36:39], v100, %[rh], s76 offen sc1\n\t" \
  "buffer_load_dwordx4 v[40:43], v100, %[rh], s77 offen sc1\n\t" \
  "buffer_load_dwordx4 v[44:47], v100, %[rh], s78 offen sc1\n\t" \
  "s_waitcnt lgkmcnt(0)\n\t" \
  "s_branch 10b\n\t" \
  "17:\n\t" \
  "v_mov_b64 v[68:69], 0\n\t" \
  "v_mov_b64 v[70:71], 0\n\t" \
  "v_mov_b64 v[72:73], 0\n\t" \
  "v_mov_b64 v[74:75], 0\n\t" \
  "v_mov_b64 v[76:77], 0\n\t" \
  "v_mov_b64 v[78:79], 0\n\t" \
  "v_mov_b64 v[80:81], 0\n\t" \
  "v_mov_b64 v[82:83], 0\n\t" \
  "s_add_u32 s59, s59, 1\n\t" \
  "s_bitcmp1_b32 s58, 2\n\t" \
  "s_cbranch_scc1 3b\n\t" \
  "s_waitcnt vmcnt(0)\n\t" \
  "v_max_u32 v116, v32, v33\n\t" \
  "v_max3_u32 v116, v116, v34, v35\n\t" \
  "v_max3_u32 v116, v116, v36, v37\n\t" \
  "v_max3_u32 v116, v116, v38, v39\n\t" \
  "v_max3_u32 v116, v116, v40, v41\n\t" \
  "v_max3_u32 v116, v116, v42, v43\n\t" \
  "v_max3_u32 v116, v116, v44, v45\n\t" \
  "v_max3_u32 v116, v116, v46, v47\n\t" \
  "v_cmp_eq_u32 vcc, -1, v116\n\t" \
  "s_cbranch_vccz 3b\n\t" \
  "s_bitcmp1_b32 s59, 6\n\t" \
  "s_cbranch_scc1 12f\n\t" \
  "buffer_load_dwordx4 v[32:35], v100, %[rh], s40 offen sc1\n\t" \
  "buffer_load_dwordx4 v[36:39], v100, %[rh], s76 offen sc1\n\t" \
  "buffer_load_dwordx4 v[40:43], v100, %[rh], s77 offen sc1\n\t" \
  "buffer_load_dwordx4 v[44:47], v100, %[rh], s78 offen sc1\n\t" \
  "s_branch 17b\n\t" \
  "12:\n\t" \
  "s_mov_b32 s42, s58\n\t" \
  "s_branch 99f\n\t" \
  "92:\n\t" \
  "s_mov_b32 s58, s42\n\t" \
  ".p2align 6\n\t" \
  "20:\n\t" \
  "s_and_b32 s76, s44, 7\n\t" \
  "s_lshl_b32 s76, s76, 15\n\t" \
  "s_add_i32 s40, s54, s76\n\t" \
  "s_and_b32 s76, s44, 3\n\t" \
  "s_lshl_b32 s45, s76, 16\n\t" \
  "s_add_i32 s47, s44, 0\n\t" \
  "s_min_u32 s47, s47, 63\n\t" \
  "s_and_b32 s76, s47, 4\n\t" \
  "s_lshl_b32 s76, s76, 11\n\t" \
  "s_lshl_b32 s47, s47, 19\n\t" \
  "s_add_i32 s47, s47, s55\n\t" \
  "s_xor_b32 s47, s47, s76\n\t" \
  "s_add_i32 s97, s47, 0x1000\n\t" \
  "s_add_i32 s49, s44, -1\n\t" \
  "s_and_b32 s48, s49, 3\n\t" \
  "s_lshl_b32 s48, s48, 16\n\t" \
  "s_lshl_b32 s50, s53, 8\n\t" \
  "s_mov_b32 s59, 0\n\t" \
  "s_mov_b32 s92, s51\n\t" \
  "s_mov_b32 s93, s53\n\t" \
  "s_xor_b32 s76, s40, 0x1000\n\t" \
  "s_xor_b32 s77, s40, 0x4000\n\t" \
  "s_xor_b32 s78, s40, 0x5000\n\t" \
  "s_cmp_lg_u32 s58, 0\n\t" \
  "s_cbranch_scc1 11f\n\t" \
  "10:\n\t" \
  "ds_read_b128 v[48:51], v103 offset:0\n\t" \
  "ds_read_b128 v[52:55], v103 offset:1024\n\t" \
  "ds_read_b128 v[56:59], v103 offset:2048\n\t" \
  "ds_read_b128 v[60:63], v103 offset:3072\n\t" \
  "ds_read_b128 v[16:19], v103 offset:16384\n\t" \
  "ds_read_b128 v[20:23], v103 offset:17408\n\t" \
  "ds_read_b128 v[24:27], v103 offset:18432\n\t" \
  "ds_read_b128 v[28:31], v103 offset:19456\n\t" \
  "ds_read_b128 v[84:87], v103 offset:49152\n\t" \
  "ds_read_b128 v[88:91], v103 offset:50176\n\t" \
  "ds_read_b128 v[92:95], v103 offset:51200\n\t" \
  "ds_read_b128 v[96:99], v103 offset:52224\n\t" \
  "s_waitcnt lgkmcnt(4)\n\t" \
  "ds_read_b128 v[0:3], v102 offset:16384\n\t" \
  "ds_read_b128 v[4:7], v102 offset:17408\n\t" \
  "ds_read_b128 v[8:11], v102 offset:18432\n\t" \
  "ds_read_b128 v[12:15], v102 offset:19456\n\t" \
  "v_mov_b32 v116, s49\n\t" \
  "s_waitcnt vmcnt(0)\n\t" \
  "buffer_store_dword v116, v109, %[rh], s48 offen sc1\n\t" \
  "s_bitcmp1_b32 s58, 1\n\t" \
  "s_cselect_b64 s[80:81], -1, 0\n\t" \
  "s_cmp_lg_u32 s58, 0\n\t" \
  "s_cbranch_scc1 17f\n\t" \
  "s_mov_b32 exec_hi, 0\n\t" \
  "v_mov_b32 v116, v118\n\t" \
  "s_branch 15f\n\t" \
  "13:\n\t" \
  "buffer_load_dword v116, v110, %[rh], s48 offen sc1\n\t" \
  "s_waitcnt vmcnt(0)\n\t" \
  "15:\n\t" \
  "v_mov_b32 v117, 20000\n\t" \
  "v_cmp_ne_u32_sdwa vcc, v116, s49 src0_sel:BYTE_0 src1_sel:DWORD\n\t" \
  "v_subrev_u32 v116, s50, v116\n\t" \
  "v_ashrrev_i32 v116, 8, v116\n\t" \
  "s_nop 1\n\t" \
  "v_max_i32_dpp v116, v116, v116 quad_perm:[1,0,3,2] row_mask:0xf bank_mask:0xf\n\t" \
  "s_cbranch_vccnz 14f\n\t" \
  "s_nop 0\n\t" \
  "v_max_i32_dpp v116, v116, v116 quad_perm:[2,3,0,1] row_mask:0xf bank_mask:0xf\n\t" \
  "s_nop 1\n\t" \
  "v_max_i32_dpp v116, v116, v116 row_half_mirror row_mask:0xf bank_mask:0xf\n\t" \
  "s_nop 1\n\t" \
  "v_max_i32_dpp v116, v116, v116 row_mirror row_mask:0xf bank_mask:0xf\n\t" \
  "v_med3_i32 v116, v116, 0, v117\n\t" \
  "v_mul_u32_u24 v116, s52, v116\n\t" \
  "v_lshrrev_b32 v116, 16, v116\n\t" \
  "s_mov_b64 exec, -1\n\t" \
  "v_readlane_b32 s84, v116, 0\n\t" \
  "v_readlane_b32 s85, v116, 16\n\t" \
  "s_max_u32 s84, s84, s85\n\t" \
  "s_add_i32 s84, s84, s51\n\t" \
  "v_mov_b64 v[68:69], 0\n\t" \
  "v_mov_b64 v[70:71], 0\n\t" \
  "v_mov_b64 v[72:73], 0\n\t" \
  "v_mov_b64 v[74:75], 0\n\t" \
  "v_mov_b64 v[76:77], 0\n\t" \
  "v_mov_b64 v[78:79], 0\n\t" \
  "v_mov_b64 v[80:81], 0\n\t" \
  "v_mov_b64 v[82:83], 0\n\t" \
  "1:\n\t" \
  "s_memtime vcc\n\t" \
  "s_waitcnt lgkmcnt(0)\n\t" \
  "s_sub_i32 s79, s84, vcc_lo\n\t" \
  "s_cmp_gt_i32 s79, 128\n\t" \
  "s_cbranch_scc1 1b\n\t" \
  "2:\n\t" \
  "s_sub_i32 s79, s79, 33\n\t" \
  "s_cmp_gt_i32 s79, 56\n\t" \
  "s_cbranch_scc1 2b\n\t" \
  "buffer_load_dwordx4 v[32:35], v100, %[rh], s40 offen sc1\n\t" \
  "s_nop 12\n\t" \
  "buffer_load_dwordx4 v[36:39], v100, %[rh], s76 offen sc1\n\t" \
  "s_nop 12\n\t" \
  "buffer_load_dwordx4 v[40:43], v100, %[rh], s77 offen sc1\n\t" \
  "s_nop 12\n\t" \
  "buffer_load_dwordx4 v[44:47], v100, %[rh], s78 offen sc1\n\t" \
  "s_memtime vcc\n\t" \
  "s_memrealtime s[82:83]\n\t" \
  "s_nop 4\n\t" \
  "s_memrealtime s[84:85]\n\t" \
  "s_nop 4\n\t" \
  "s_memrealtime s[94:95]\n\t" \
  "s_nop 4\n\t" \
  "s_memrealtime s[86:87]\n\t" \
  "s_nop 4\n\t" \
  "s_memrealtime s[88:89]\n\t" \
  "s_nop 4\n\t" \
  "s_memrealtime s[90:91]\n\t" \
  "s_mov_b32 s92, 0\n\t" \
  "s_waitcnt lgkmcnt(0)\n\t" \
  "s_cmp_eq_u32 s82, s84\n\t" \
  "s_addc_u32 s92, s92, 0\n\t" \
  "s_cmp_eq_u32 s82, s94\n\t" \
  "s_addc_u32 s92, s92, 0\n\t" \
  "s_cmp_eq_u32 s82, s86\n\t" \
  "s_addc_u32 s92, s92, 0\n\t" \
  "s_cmp_eq_u32 s82, s88\n\t" \
  "s_addc_u32 s92, s92, 0\n\t" \
  "s_cmp_eq_u32 s82, s90\n\t" \
  "s_addc_u32 s92, s92, 0\n\t" \
  "s_mul_i32 s92, s92, 24\n\t" \
  "s_add_i32 s92, s92, vcc_lo\n\t" \
  "s_lshl4_add_u32 s93, s82, 64\n\t" \
  "3:\n\t" \
  "s_waitcnt lgkmcnt(0)\n\t" \
  "s_waitcnt vmcnt(3)\n\t" \
  "v_smfmac_f32_16x16x64_bf16 v[68:71], v[32:35], a[192:199], v104\n\t" \
  "ds_write_b128 v101, v[32:35] offset:8192\n\t" \
  "v_smfmac_f32_16x16x64_bf16 v[72:75], v[32:35], a[200:207], v104\n\t" \
  "v_smfmac_f32_16x16x64_bf16 v[76:79], v[32:35], v[48:55], v104\n\t" \
  "v_smfmac_f32_16x16x64_bf16 v[80:83], v[32:35], v[56:63], v104\n\t" \
  "s_waitcnt vmcnt(2)\n\t" \
  "v_smfmac_f32_16x16x64_bf16 v[68:71], v[36:39], a[208:215], v104\n\t" \
  "ds_write_b128 v101, v[36:39] offset:9216\n\t" \
  "v_smfmac_f32_16x16x64_bf16 v[72:75], v[36:39], a[216:223], v104\n\t" \
  "v_smfmac_f32_16x16x64_bf16 v[76:79], v[36:39], a[224:231], v104\n\t" \
  "v_cmp_u_f32 vcc, v68, v69\n\t" \
  "v_smfmac_f32_16x16x64_bf16 v[80:83], v[36:39], a[232:239], v104\n\t" \
  "v_cmp_u_f32 s[82:83], v70, v71\n\t" \
  "s_waitcnt vmcnt(1)\n\t" \
  "v_smfmac_f32_16x16x64_bf16 v[68:71], v[40:43], v[16:23], v104\n\t" \
  "ds_write_b128 v101, v[40:43] offset:10240\n\t" \
  "v_smfmac_f32_16x16x64_bf16 v[72:75], v[40:43], v[24:31], v104\n\t" \
  "s_or_b64 vcc, vcc, s[82:83]\n\t" \
  "s_or_b64 vcc, vcc, s[80:81]\n\t" \
  "s_waitcnt lgkmcnt(1)\n\t" \
  "v_smfmac_f32_16x16x64_bf16 v[76:79], v[40:43], a[240:247], v104\n\t" \
  "s_cbranch_scc1 8f\n\t" \
  "5:\n\t" \
  "s_barrier\n\t" \
  "4:\n\t" \
  "s_memtime s[56:57]\n\t" \
  "ds_read_b128 v[48:51], v101 offset:12288\n\t" \
  "v_smfmac_f32_16x16x64_bf16 v[80:83], v[40:43], a[248:255], v104\n\t" \
  "ds_read_b128 v[52:55], v101 offset:13312\n\t" \
  "ds_read_b128 v[56:59], v101 offset:0\n\t" \
  "s_waitcnt vmcnt(0)\n\t" \
  "v_smfmac_f32_16x16x64_bf16 v[68:71], v[44:47], v[120:127], v104\n\t" \
  "v_smfmac_f32_16x16x64_bf16 v[72:75], v[44:47], v[128:135], v104\n\t" \
  "ds_write_b128 v101, v[44:47] offset:11264\n\t" \
  "v_smfmac_f32_16x16x64_bf16 v[76:79], v[44:47], v[136:143], v104\n\t" \
  "v_cmp_u_f32 vcc, v68, v69\n\t" \
  "v_smfmac_f32_16x16x64_bf16 v[80:83], v[44:47], v[144:151], v104\n\t" \
  "v_cmp_u_f32 s[82:83], v70, v71\n\t" \
  "s_waitcnt lgkmcnt(3)\n\t" \
  "v_smfmac_f32_16x16x64_bf16 v[68:71], v[48:51], v[152:159], v104\n\t" \
  "ds_read_b128 v[60:63], v101 offset:1024\n\t" \
  "ds_read_b128 v[64:67], v101 offset:4096\n\t" \
  "v_smfmac_f32_16x16x64_bf16 v[72:75], v[48:51], v[160:167], v104\n\t" \
  "s_or_b64 vcc, vcc, s[82:83]\n\t" \
  "s_waitcnt lgkmcnt(2)\n\t" \
  "v_smfmac_f32_16x16x64_bf16 v[68:71], v[52:55], v[168:175], v104\n\t" \
  "s_cbranch_scc1 9f\n\t" \
  "6:\n\t" \
  "s_barrier\n\t" \
  "v_smfmac_f32_16x16x64_bf16 v[72:75], v[52:55], v[176:183], v104\n\t" \
  "ds_read_b128 v[48:51], v101 offset:5120\n\t" \
  "v_smfmac_f32_16x16x64_bf16 v[68:71], v[56:59], a[0:7], v104\n\t" \
  "ds_read_b128 v[52:55], v101 offset:14336\n\t" \
  "v_smfmac_f32_16x16x64_bf16 v[72:75], v[56:59], a[8:15], v104\n\t" \
  "buffer_load_dwordx4 v[32:35], v100, %[ru], s97 offen offset:0 sc1\n\t" \
  "s_waitcnt lgkmcnt(3)\n\t" \
  "v_smfmac_f32_16x16x64_bf16 v[68:71], v[60:63], a[16:23], v104\n\t" \
  "ds_read_b128 v[56:59], v101 offset:15360\n\t" \
  "v_smfmac_f32_16x16x64_bf16 v[72:75], v[60:63], a[24:31], v104\n\t" \
  "buffer_load_dwordx4 v[36:39], v100, %[ru], s97 offen offset:1024 sc1\n\t" \
  "s_waitcnt lgkmcnt(3)\n\t" \
  "v_smfmac_f32_16x16x64_bf16 v[68:71], v[64:67], a[96:103], v104\n\t" \
  "ds_read_b128 v[60:63], v101 offset:2048\n\t" \
  "buffer_load_dwordx4 v[40:43], v100, %[ru], s97 offen offset:2048 sc1\n\t" \
  "v_smfmac_f32_16x16x64_bf16 v[72:75], v[64:67], a[104:111], v104\n\t" \
  "ds_read_b128 v[16:19], v102 offset:49152\n\t" \
  "s_waitcnt lgkmcnt(4)\n\t" \
  "v_smfmac_f32_16x16x64_bf16 v[68:71], v[48:51], a[112:119], v104\n\t" \
  "ds_read_b128 v[64:67], v101 offset:3072\n\t" \
  "buffer_load_dwordx4 v[44:47], v100, %[ru], s97 offen offset:3072 sc1\n\t" \
  "v_smfmac_f32_16x16x64_bf16 v[72:75], v[48:51], a[120:127], v104\n\t" \
  "ds_read_b128 v[20:23], v102 offset:50176\n\t" \
  "s_waitcnt lgkmcnt(5)\n\t" \
  "v_smfmac_f32_16x16x64_bf16 v[68:71], v[52:55], v[84:91], v104\n\t" \
  "ds_read_b128 v[48:51], v101 offset:7168\n\t" \
  "v_smfmac_f32_16x16x64_bf16 v[72:75], v[52:55], v[92:99], v104\n\t" \
  "ds_read_b128 v[24:27], v102 offset:51200\n\t" \
  "s_waitcnt lgkmcnt(6)\n\t" \
  "v_smfmac_f32_16x16x64_bf16 v[68:71], v[56:59], v[216:223], v104\n\t" \
  "ds_read_b128 v[52:55], v101 offset:6144\n\t" \
  "v_smfmac_f32_16x16x64_bf16 v[72:75], v[56:59], v[224:231], v104\n\t" \
  "ds_read_b128 v[28:31], v102 offset:52224\n\t" \
  "s_waitcnt lgkmcnt(7)\n\t" \
  "v_smfmac_f32_16x16x64_bf16 v[68:71], v[60:63], v[0:7], v104\n\t" \
  "buffer_load_dwordx4 v[84:87], v100, %[ru], s47 offen offset:0 sc1\n\t" \
  "v_smfmac_f32_16x16x64_bf16 v[72:75], v[60:63], v[8:15], v104\n\t" \
  "buffer_load_dwordx4 v[88:91], v100, %[ru], s47 offen offset:1024 sc1\n\t" \
  "s_waitcnt lgkmcnt(5)\n\t" \
  "v_smfmac_f32_16x16x64_bf16 v[68:71], v[64:67], a[64:71], v104\n\t" \
  "ds_read_b128 v[0:3], v103 offset:32768\n\t" \
  "v_smfmac_f32_16x16x64_bf16 v[72:75], v[64:67], a[72:79], v104\n\t" \
  "ds_read_b128 v[4:7], v103 offset:33792\n\t" \
  "s_waitcnt lgkmcnt(5)\n\t" \
  "v_smfmac_f32_16x16x64_bf16 v[68:71], v[48:51], a[160:167], v104\n\t" \
  "ds_read_b128 v[8:11], v103 offset:34816\n\t" \
  "s_waitcnt lgkmcnt(4)\n\t" \
  "v_smfmac_f32_16x16x64_bf16 v[68:71], v[52:55], v[16:23], v104\n\t" \
  "ds_read_b128 v[12:15], v103 offset:35840\n\t" \
  "v_smfmac_f32_16x16x64_bf16 v[72:75], v[48:51], a[168:175], v104\n\t" \
  "buffer_load_dwordx4 v[92:95], v100, %[ru], s47 offen offset:2048 sc1\n\t" \
  "s_waitcnt lgkmcnt(4)\n\t" \
  "v_smfmac_f32_16x16x64_bf16 v[72:75], v[52:55], v[24:31], v104\n\t" \
  "v_add_f32 v116, v68, v70\n\t" \
  "v_add_f32 v117, v69, v71\n\t" \
  "v_smfmac_f32_16x16x64_bf16 v[76:79], v[56:59], v[232:239], v104\n\t" \
  "v_cvt_pk_bf16_f32 v116, v116, v117\n\t" \
  "ds_read_b128 v[24:27], v102 offset:2048\n\t" \
  "v_smfmac_f32_16x16x64_bf16 v[80:83], v[56:59], v[240:247], v104\n\t" \
  "v_add_f32 v117, v72, v74\n\t" \
  "v_add_f32 v118, v73, v75\n\t" \
  "v_cvt_pk_bf16_f32 v117, v117, v118\n\t" \
  "ds_write2st64_b32 v105, v116, v117 offset1:1\n\t" \
  "v_smfmac_f32_16x16x64_bf16 v[76:79], v[60:63], a[48:55], v104\n\t" \
  "buffer_load_dwordx4 v[96:99], v100, %[ru], s47 offen offset:3072 sc1\n\t" \
  "v_smfmac_f32_16x16x64_bf16 v[80:83], v[60:63], a[56:63], v104\n\t" \
  "v_smfmac_f32_16x16x64_bf16 v[76:79], v[64:67], a[80:87], v104\n\t" \
  "buffer_load_dword v117, v107, %[ru], 0 offen sc1\n\t" \
  "v_smfmac_f32_16x16x64_bf16 v[80:83], v[64:67], a[88:95], v104\n\t" \
  "s_waitcnt lgkmcnt(0)\n\t" \
  "s_barrier\n\t" \
  "ds_read_b128 v[56:59], v101 offset:12288\n\t" \
  "v_smfmac_f32_16x16x64_bf16 v[76:79], v[48:51], a[176:183], v104\n\t" \
  "v_smfmac_f32_16x16x64_bf16 v[80:83], v[48:51], a[184:191], v104\n\t" \
  "ds_read_b128 v[60:63], v101 offset:5120\n\t" \
  "v_smfmac_f32_16x16x64_bf16 v[76:79], v[52:55], a[144:151], v104\n\t" \
  "ds_read_b128 v[16:19], v102 offset:0\n\t" \
  "v_smfmac_f32_16x16x64_bf16 v[80:83], v[52:55], a[152:159], v104\n\t" \
  "ds_read_b128 v[64:67], v101 offset:14336\n\t" \
  "s_waitcnt lgkmcnt(3)\n\t" \
  "v_smfmac_f32_16x16x64_bf16 v[76:79], v[56:59], v[0:7], v104\n\t" \
  "ds_read_b128 v[20:23], v102 offset:1024\n\t" \
  "v_smfmac_f32_16x16x64_bf16 v[80:83], v[56:59], v[8:15], v104\n\t" \
  "ds_read_b128 v[48:51], v101 offset:0\n\t" \
  "s_waitcnt lgkmcnt(4)\n\t" \
  "v_smfmac_f32_16x16x64_bf16 v[76:79], v[60:63], a[128:135], v104\n\t" \
  "ds_read_b128 v[28:31], v102 offset:3072\n\t" \
  "v_smfmac_f32_16x16x64_bf16 v[80:83], v[60:63], a[136:143], v104\n\t" \
  "ds_read_b128 v[52:55], v101 offset:13312\n\t" \
  "s_waitcnt lgkmcnt(4)\n\t" \
  "v_smfmac_f32_16x16x64_bf16 v[76:79], v[64:67], v[200:207], v104\n\t" \
  "ds_read_b128 v[0:3], v102 offset:32768\n\t" \
  "v_smfmac_f32_16x16x64_bf16 v[80:83], v[64:67], v[208:215], v104\n\t" \
  "ds_read_b128 v[56:59], v101 offset:1024\n\t" \
  "ds_read_b128 v[4:7], v102 offset:33792\n\t" \
  "s_waitcnt lgkmcnt(5)\n\t" \
  "v_smfmac_f32_16x16x64_bf16 v[76:79], v[48:51], v[16:23], v104\n\t" \
  "ds_read_b128 v[60:63], v101 offset:4096\n\t" \
  "s_waitcnt lgkmcnt(5)\n\t" \
  "v_smfmac_f32_16x16x64_bf16 v[80:83], v[48:51], v[24:31], v104\n\t" \
  "ds_read_b128 v[8:11], v102 offset:34816\n\t" \
  "s_waitcnt lgkmcnt(5)\n\t" \
  "v_smfmac_f32_16x16x64_bf16 v[76:79], v[52:55], v[184:191], v104\n\t" \
  "ds_read_b128 v[12:15], v102 offset:35840\n\t" \
  "v_smfmac_f32_16x16x64_bf16 v[80:83], v[52:55], v[192:199], v104\n\t" \
  "s_waitcnt lgkmcnt(4)\n\t" \
  "v_smfmac_f32_16x16x64_bf16 v[76:79], v[56:59], a[32:39], v104\n\t" \
  "s_waitcnt lgkmcnt(2)\n\t" \
  "v_smfmac_f32_16x16x64_bf16 v[76:79], v[60:63], v[0:7], v104\n\t" \
  "v_smfmac_f32_16x16x64_bf16 v[80:83], v[56:59], a[40:47], v104\n\t" \
  "s_waitcnt lgkmcnt(0)\n\t" \
  "v_smfmac_f32_16x16x64_bf16 v[80:83], v[60:63], v[8:15], v104\n\t" \
  "v_add_f32 v68, v76, v78\n\t" \
  "v_add_f32 v69, v77, v79\n\t" \
  "v_cvt_pk_bf16_f32 v68, v68, v69\n\t" \
  "ds_write_b32 v106, v68\n\t" \
  "ds_read_b128 v[0:3], v101 offset:20480\n\t" \
  "ds_read_b128 v[4:7], v101 offset:21504\n\t" \
  "ds_read_b128 v[8:11], v101 offset:22528\n\t" \
  "ds_read_b128 v[12:15], v101 offset:23552\n\t" \
  "v_add_f32 v70, v80, v82\n\t" \
  "v_add_f32 v71, v81, v83\n\t" \
  "v_cvt_pk_bf16_f32 v70, v70, v71\n\t" \
  "ds_write_b32 v106, v70 offset:256\n\t" \
  "ds_read_b128 v[16:19], v101 offset:24576\n\t" \
  "ds_read_b128 v[20:23], v101 offset:25600\n\t" \
  "ds_read_b128 v[24:27], v101 offset:26624\n\t" \
  "ds_read_b128 v[28:31], v101 offset:27648\n\t" \
  "s_waitcnt lgkmcnt(0)\n\t" \
  "s_barrier\n\t" \
  "s_waitcnt vmcnt(1)\n\t" \
  "v_mfma_f32_16x16x32_bf16 v[64:67], v[0:3], v[32:35], 0\n\t" \
  "v_mfma_f32_16x16x32_bf16 v[68:71], v[16:19], v[32:35], v[112:115]\n\t" \
  "v_mfma_f32_16x16x32_bf16 v[72:75], v[0:3], v[84:87], v[112:115]\n\t" \
  "v_mfma_f32_16x16x32_bf16 v[64:67], v[4:7], v[36:39], v[64:67]\n\t" \
  "v_mfma_f32_16x16x32_bf16 v[68:71], v[20:23], v[36:39], v[68:71]\n\t" \
  "v_mfma_f32_16x16x32_bf16 v[72:75], v[4:7], v[88:91], v[72:75]\n\t" \
  "ds_read_b64_tr_b16 v[48:49], v108 offset:0\n\t" \
  "ds_read_b64_tr_b16 v[50:51], v108 offset:64\n\t" \
  "ds_read_b64_tr_b16 v[52:53], v108 offset:512\n\t" \
  "ds_read_b64_tr_b16 v[54:55], v108 offset:576\n\t" \
  "ds_read_b64_tr_b16 v[56:57], v108 offset:1024\n\t" \
  "ds_read_b64_tr_b16 v[58:59], v108 offset:1088\n\t" \
  "ds_read_b64_tr_b16 v[60:61], v108 offset:1536\n\t" \
  "ds_read_b64_tr_b16 v[62:63], v108 offset:1600\n\t" \
  "v_mfma_f32_16x16x32_bf16 v[64:67], v[8:11], v[40:43], v[64:67]\n\t" \
  "v_mfma_f32_16x16x32_bf16 v[68:71], v[24:27], v[40:43], v[68:71]\n\t" \
  "v_mfma_f32_16x16x32_bf16 v[72:75], v[8:11], v[92:95], v[72:75]\n\t" \
  "v_mfma_f32_16x16x32_bf16 v[64:67], v[12:15], v[44:47], v[64:67]\n\t" \
  "v_mfma_f32_16x16x32_bf16 v[68:71], v[28:31], v[44:47], v[68:71]\n\t" \
  "v_mfma_f32_16x16x32_bf16 v[72:75], v[12:15], v[96:99], v[72:75]\n\t" \
  "s_waitcnt vmcnt(0)\n\t" \
  "v_readfirstlane_b32 s76, v117\n\t" \
  "s_add_i32 s77, s44, 1\n\t" \
  "s_min_i32 s77, s77, 63\n\t" \
  "s_cmp_lt_i32 s76, s77\n\t" \
  "s_cselect_b32 s42, 32, 0\n\t" \
  "s_waitcnt lgkmcnt(0)\n\t" \
  "v_mfma_f32_16x16x32_bf16 v[76:79], v[48:51], v[32:35], 0\n\t" \
  "v_mfma_f32_16x16x32_bf16 v[80:83], v[48:51], v[84:87], 0\n\t" \
  "v_mfma_f32_16x16x32_bf16 v[76:79], v[52:55], v[36:39], v[76:79]\n\t" \
  "v_max3_f32 v1, v64, v65, v66\n\t" \
  "v_mfma_f32_16x16x32_bf16 v[80:83], v[52:55], v[88:91], v[80:83]\n\t" \
  "v_max3_f32 v1, v1, v67, v68\n\t" \
  "v_mfma_f32_16x16x32_bf16 v[76:79], v[56:59], v[40:43], v[76:79]\n\t" \
  "v_max3_f32 v0, v72, v73, v74\n\t" \
  "v_mfma_f32_16x16x32_bf16 v[80:83], v[56:59], v[92:95], v[80:83]\n\t" \
  "v_max3_f32 v1, v1, v69, v70\n\t" \
  "v_mfma_f32_16x16x32_bf16 v[76:79], v[60:63], v[44:47], v[76:79]\n\t" \
  "v_max_f32 v0, v0, v75\n\t" \
  "v_mfma_f32_16x16x32_bf16 v[80:83], v[60:63], v[96:99], v[80:83]\n\t" \
  "v_max_f32 v1, v1, v71\n\t" \
  "s_nop 1\n\t" \
  "v_permlane32_swap_b32 v0, v1\n\t" \
  "s_lshl_b32 s76, s45, 1\n\t" \
  "v_max_f32 v0, v0, v1\n\t" \
  "s_nop 2\n\t" \
  "v_mov_b32_dpp v80, v76 quad_perm:[0,1,2,3] row_mask:0xc bank_mask:0xf\n\t" \
  "v_mov_b32_dpp v81, v77 quad_perm:[0,1,2,3] row_mask:0xc bank_mask:0xf\n\t" \
  "v_mov_b32_dpp v82, v78 quad_perm:[0,1,2,3] row_mask:0xc bank_mask:0xf\n\t" \
  "v_mov_b32_dpp v83, v79 quad_perm:[0,1,2,3] row_mask:0xc bank_mask:0xf\n\t" \
  "v_max3_f32 v0, v0, v80, v81\n\t" \
  "v_max3_f32 v0, v0, v82, v83\n\t" \
  "v_mov_b32 v1, v0\n\t" \
  "s_nop 1\n\t" \
  "v_permlane16_swap_b32 v0, v1\n\t" \
  "s_nop 0\n\t" \
  "v_max_f32 v0, v0, v1\n\t" \
  "v_mul_f32 v0, %[cs], v0\n\t" \
  "v_mov_b32 v1, v0\n\t" \
  "v_mov_b32 v2, v0\n\t" \
  "s_nop 1\n\t" \
  "v_permlane32_swap_b32 v1, v2\n\t" \
  "s_nop 0\n\t" \
  "v_fma_f32 v80, v80, %[cs], -v0\n\t" \
  "v_fma_f32 v81, v81, %[cs], -v0\n\t" \
  "v_fma_f32 v82, v82, %[cs], -v0\n\t" \
  "v_fma_f32 v83, v83, %[cs], -v0\n\t" \
  "v_fma_f32 v72, v72, %[cs], -v1\n\t" \
  "v_fma_f32 v73, v73, %[cs], -v1\n\t" \
  "v_fma_f32 v74, v74, %[cs], -v1\n\t" \
  "v_fma_f32 v75, v75, %[cs], -v1\n\t" \
  "v_fma_f32 v64, v64, %[cs], -v2\n\t" \
  "v_fma_f32 v65, v65, %[cs], -v2\n\t" \
  "v_fma_f32 v66, v66, %[cs], -v2\n\t" \
  "v_fma_f32 v67, v67, %[cs], -v2\n\t" \
  "v_fma_f32 v68, v68, %[cs], -v2\n\t" \
  "v_fma_f32 v69, v69, %[cs], -v2\n\t" \
  "v_fma_f32 v70, v70, %[cs], -v2\n\t" \
  "v_fma_f32 v71, v71, %[cs], -v2\n\t" \
  "v_exp_f32 v80, v80\n\t" \
  "v_exp_f32 v81, v81\n\t" \
  "v_exp_f32 v82, v82\n\t" \
  "v_exp_f32 v83, v83\n\t" \
  "v_exp_f32 v72, v72\n\t" \
  "v_exp_f32 v73, v73\n\t" \
  "v_exp_f32 v74, v74\n\t" \
  "v_exp_f32 v75, v75\n\t" \
  "v_exp_f32 v64, v64\n\t" \
  "v_exp_f32 v65, v65\n\t" \
  "v_exp_f32 v66, v66\n\t" \
  "v_exp_f32 v67, v67\n\t" \
  "v_exp_f32 v68, v68\n\t" \
  "v_exp_f32 v69, v69\n\t" \
  "v_exp_f32 v70, v70\n\t" \
  "v_exp_f32 v71, v71\n\t" \
  "v_add_f32 v3, v80, v81\n\t" \
  "v_add_f32 v0, v72, v73\n\t" \
  "v_add_f32 v1, v64, v65\n\t" \
  "v_add_f32 v2, v68, v69\n\t" \
  "v_add_f32 v3, v3, v82\n\t" \
  "v_add_f32 v0, v0, v74\n\t" \
  "v_add_f32 v1, v1, v66\n\t" \
  "v_add_f32 v2, v2, v70\n\t" \
  "v_add_f32 v0, v0, v75\n\t" \
  "v_add_f32 v1, v1, v67\n\t" \
  "v_add_f32 v2, v2, v71\n\t" \
  "v_add_f32 v1, v1, v2\n\t" \
  "v_add_f32 v3, v3, v83\n\t" \
  "s_nop 0\n\t" \
  "v_permlane32_swap_b32 v0, v1\n\t" \
  "s_nop 0\n\t" \
  "v_add_f32 v0, v0, v1\n\t" \
  "v_add_f32 v0, v0, v3\n\t" \
  "v_mov_b32 v1, v0\n\t" \
  "s_nop 1\n\t" \
  "v_permlane16_swap_b32 v0, v1\n\t" \
  "s_nop 0\n\t" \
  "v_add_f32 v0, v0, v1\n\t" \
  "v_rcp_f32 v0, v0\n\t" \
  "buffer_load_dword v118, v110, %[rh], s45 offen sc1\n\t" \
  "v_mov_b32 v1, v0\n\t" \
  "v_mov_b32 v2, v0\n\t" \
  "s_nop 1\n\t" \
  "v_permlane32_swap_b32 v1, v2\n\t" \
  "s_nop 0\n\t" \
  "v_mul_f32 v80, v80, v0\n\t" \
  "v_mul_f32 v81, v81, v0\n\t" \
  "v_mul_f32 v82, v82, v0\n\t" \
  "v_mul_f32 v83, v83, v0\n\t" \
  "v_mul_f32 v72, v72, v1\n\t" \
  "v_mul_f32 v73, v73, v1\n\t" \
  "v_mul_f32 v74, v74, v1\n\t" \
  "v_mul_f32 v75, v75, v1\n\t" \
  "v_mul_f32 v64, v64, v2\n\t" \
  "v_mul_f32 v65, v65, v2\n\t" \
  "v_mul_f32 v66, v66, v2\n\t" \
  "v_mul_f32 v67, v67, v2\n\t" \
  "v_mul_f32 v68, v68, v2\n\t" \
  "v_mul_f32 v69, v69, v2\n\t" \
  "v_mul_f32 v70, v70, v2\n\t" \
  "v_mul_f32 v71, v71, v2\n\t" \
  "v_cvt_pk_bf16_f32 v0, v72, v73\n\t" \
  "v_cvt_pk_bf16_f32 v1, v64, v65\n\t" \
  "v_cvt_pk_bf16_f32 v2, v74, v75\n\t" \
  "v_cvt_pk_bf16_f32 v3, v66, v67\n\t" \
  "v_cvt_pk_bf16_f32 v4, v68, v69\n\t" \
  "v_cvt_pk_bf16_f32 v5, v80, v81\n\t" \
  "v_cvt_pk_bf16_f32 v6, v70, v71\n\t" \
  "v_cvt_pk_bf16_f32 v7, v82, v83\n\t" \
  "buffer_store_dwordx4 v[0:3], v119, %[rh], s76 offen sc1\n\t" \
  "buffer_store_dwordx4 v[4:7], v119, %[rh], s76 offen offset:1024 sc1\n\t" \
  "s_add_i32 s41, s56, 1950\n\t" \
  "s_sub_i32 s76, s92, s51\n\t" \
  "s_sub_i32 s77, s93, s53\n\t" \
  "s_cmp_eq_u32 s76, 0\n\t" \
  "s_cbranch_scc1 21f\n\t" \
  "v_cvt_f32_u32 v117, s76\n\t" \
  "v_cvt_f32_u32 v116, s77\n\t" \
  "v_rcp_f32 v117, v117\n\t" \
  "s_mov_b32 s51, s92\n\t" \
  "v_mul_f32 v117, v117, v116\n\t" \
  "v_max_f32 v117, 0x3f028f5c, v117\n\t" \
  "v_min_f32 v117, 0x3ffeb852, v117\n\t" \
  "v_rcp_f32 v116, v117\n\t" \
  "v_mul_f32 v117, 0x4f000000, v117\n\t" \
  "v_mul_f32 v116, 0x47800000, v116\n\t" \
  "v_cvt_u32_f32 v117, v117\n\t" \
  "v_cvt_u32_f32 v116, v116\n\t" \
  "s_mov_b32 s53, s93\n\t" \
  "v_readfirstlane_b32 s46, v117\n\t" \
  "v_readfirstlane_b32 s52, v116\n\t" \
  "21:\n\t" \
  "s_add_i32 s44, s44, 1\n\t" \
  "s_and_b32 s58, s58, 4\n\t" \
  "s_and_b32 s76, s44, 64\n\t" \
  "s_or_b32 s42, s42, s76\n\t" \
  "s_cbranch_scc0 20b\n\t" \
  "s_branch 99f\n\t" \
  "14:\n\t" \
  "s_add_u32 s59, s59, 0x10000\n\t" \
  "s_cmp_lt_u32 s59, 0x100000\n\t" \
  "s_cbranch_scc1 13b\n\t" \
  "s_mov_b64 exec, -1\n\t" \
  "s_mov_b32 s42, 16\n\t" \
  "s_branch 99f\n\t" \
  "8:\n\t" \
  "s_bitcmp1_b32 s58, 1\n\t" \
  "s_cbranch_scc1 4b\n\t" \
  "s_bitcmp1_b32 s58, 2\n\t" \
  "s_cbranch_scc1 5b\n\t" \
  "v_max3_u32 v116, v32, v33, v34\n\t" \
  "v_max3_u32 v117, v35, v36, v37\n\t" \
  "v_max3_u32 v116, v116, v38, v39\n\t" \
  "v_max_u32 v116, v116, v117\n\t" \
  "v_cmp_eq_u32 vcc, -1, v116\n\t" \
  "s_cbranch_vccz 5b\n\t" \
  "s_branch 0f\n\t" \
  "9:\n\t" \
  "s_bitcmp1_b32 s58, 2\n\t" \
  "s_cbranch_scc1 6b\n\t" \
  "v_max3_u32 v116, v40, v41, v42\n\t" \
  "v_max3_u32 v117, v43, v44, v45\n\t" \
  "v_max3_u32 v116, v116, v46, v47\n\t" \
  "v_max_u32 v116, v116, v117\n\t" \
  "v_cmp_eq_u32 vcc, -1, v116\n\t" \
  "s_cbranch_vccz 6b\n\t" \
  "s_or_b32 s58, s58, 2\n\t" \
  "0:\n\t" \
  "s_or_b32 s58, s58, 1\n\t" \
  "11:\n\t" \
  "buffer_load_dwordx4 v[32:35], v100, %[rh], s40 offen sc1\n\t" \
  "buffer_load_dwordx4 v[36:39], v100, %[rh], s76 offen sc1\n\t" \
  "buffer_load_dwordx4 v[40:43], v100, %[rh], s77 offen sc1\n\t" \
  "buffer_load_dwordx4 v[44:47], v100, %[rh], s78 offen sc1\n\t" \
  "s_waitcnt lgkmcnt(0)\n\t" \
  "s_branch 10b\n\t" \
  "17:\n\t" \
  "v_mov_b64 v[68:69], 0\n\t" \
  "v_mov_b64 v[70:71], 0\n\t" \
  "v_mov_b64 v[72:73], 0\n\t" \
  "v_mov_b64 v[74:75], 0\n\t" \
  "v_mov_b64 v[76:77], 0\n\t" \
  "v_mov_b64 v[78:79], 0\n\t" \
  "v_mov_b64 v[80:81], 0\n\t" \
  "v_mov_b64 v[82:83], 0\n\t" \
  "s_add_u32 s59, s59, 1\n\t" \
  "s_bitcmp1_b32 s58, 2\n\t" \
  "s_cbranch_scc1 3b\n\t" \
  "s_waitcnt vmcnt(0)\n\t" \
  "v_max_u32 v116, v32, v33\n\t" \
  "v_max3_u32 v116, v116, v34, v35\n\t" \
  "v_max3_u32 v116, v116, v36, v37\n\t" \
  "v_max3_u32 v116, v116, v38, v39\n\t" \
  "v_max3_u32 v116, v116, v40, v41\n\t" \
  "v_max3_u32 v116, v116, v42, v43\n\t" \
  "v_max3_u32 v116, v116, v44, v45\n\t" \
  "v_max3_u32 v116, v116, v46, v47\n\t" \
  "v_cmp_eq_u32 vcc, -1, v116\n\t" \
  "s_cbranch_vccz 3b\n\t" \
  "s_bitcmp1_b32 s59, 6\n\t" \
  "s_cbranch_scc1 12f\n\t" \
  "buffer_load_dwordx4 v[32:35], v100, %[rh], s40 offen sc1\n\t" \
  "buffer_load_dwordx4 v[36:39], v100, %[rh], s76 offen sc1\n\t" \
  "buffer_load_dwordx4 v[40:43], v100, %[rh], s77 offen sc1\n\t" \
  "buffer_load_dwordx4 v[44:47], v100, %[rh], s78 offen sc1\n\t" \
  "s_branch 17b\n\t" \
  "12:\n\t" \
  "s_mov_b32 s42, s58\n\t" \
  "s_branch 99f\n\t" \
  "93:\n\t" \
  "s_mov_b32 s58, s42\n\t" \
  ".p2align 6\n\t" \
  "20:\n\t" \
  "s_and_b32 s76, s44, 7\n\t" \
  "s_lshl_b32 s76, s76, 15\n\t" \
  "s_add_i32 s40, s54, s76\n\t" \
  "s_and_b32 s76, s44, 3\n\t" \
  "s_lshl_b32 s45, s76, 16\n\t" \
  "s_add_i32 s47, s44, 1\n\t" \
  "s_min_u32 s47, s47, 63\n\t" \
  "s_and_b32 s76, s47, 4\n\t" \
  "s_lshl_b32 s76, s76, 11\n\t" \
  "s_lshl_b32 s47, s47, 19\n\t" \
  "s_add_i32 s47, s47, s55\n\t" \
  "s_xor_b32 s47, s47, s76\n\t" \
  "s_add_i32 s97, s47, 0x1000\n\t" \
  "s_add_i32 s49, s44, -1\n\t" \
  "s_and_b32 s48, s49, 3\n\t" \
  "s_lshl_b32 s48, s48, 16\n\t" \
  "s_lshl_b32 s50, s53, 8\n\t" \
  "s_mov_b32 s59, 0\n\t" \
  "s_mov_b32 s92, s51\n\t" \
  "s_mov_b32 s93, s53\n\t" \
  "s_xor_b32 s76, s40, 0x1000\n\t" \
  "s_xor_b32 s77, s40, 0x4000\n\t" \
  "s_xor_b32 s78, s40, 0x5000\n\t" \
  "s_cmp_lg_u32 s58, 0\n\t" \
  "s_cbranch_scc1 11f\n\t" \
  "10:\n\t" \
  "ds_read_b128 v[48:51], v103 offset:32768\n\t" \
  "ds_read_b128 v[52:55], v103 offset:33792\n\t" \
  "ds_read_b128 v[56:59], v103 offset:34816\n\t" \
  "ds_read_b128 v[60:63], v103 offset:35840\n\t" \
  "ds_read_b128 v[16:19], v103 offset:49152\n\t" \
  "ds_read_b128 v[20:23], v103 offset:50176\n\t" \
  "ds_read_b128 v[24:27], v103 offset:51200\n\t" \
  "ds_read_b128 v[28:31], v103 offset:52224\n\t" \
  "ds_read_b128 v[84:87], v102 offset:16384\n\t" \
  "ds_read_b128 v[88:91], v102 offset:17408\n\t" \
  "ds_read_b128 v[92:95], v102 offset:18432\n\t" \
  "ds_read_b128 v[96:99], v102 offset:19456\n\t" \
  "s_waitcnt lgkmcnt(4)\n\t" \
  "ds_read_b128 v[0:3], v102 offset:49152\n\t" \
  "ds_read_b128 v[4:7], v102 offset:50176\n\t" \
  "ds_read_b128 v[8:11], v102 offset:51200\n\t" \
  "ds_read_b128 v[12:15], v102 offset:52224\n\t" \
  "s_bitcmp1_b32 s58, 1\n\t" \
  "s_cselect_b64 s[80:81], -1, 0\n\t" \
  "s_cmp_lg_u32 s58, 0\n\t" \
  "s_cbranch_scc1 17f\n\t" \
  "s_mov_b32 exec_hi, 0\n\t" \
  "16:\n\t" \
  "s_memtime vcc\n\t" \
  "s_waitcnt lgkmcnt(0)\n\t" \
  "s_sub_i32 s79, s41, vcc_lo\n\t" \
  "s_cmp_gt_i32 s79, 64\n\t" \
  "s_cbranch_scc1 16b\n\t" \
  "13:\n\t" \
  "buffer_load_dword v116, v110, %[rh], s48 offen sc1\n\t" \
  "s_waitcnt vmcnt(0)\n\t" \
  "15:\n\t" \
  "v_mov_b32 v117, 20000\n\t" \
  "v_cmp_ne_u32_sdwa vcc, v116, s49 src0_sel:BYTE_0 src1_sel:DWORD\n\t" \
  "v_subrev_u32 v116, s50, v116\n\t" \
  "v_ashrrev_i32 v116, 8, v116\n\t" \
  "s_nop 1\n\t" \
  "v_max_i32_dpp v116, v116, v116 quad_perm:[1,0,3,2] row_mask:0xf bank_mask:0xf\n\t" \
  "s_cbranch_vccnz 14f\n\t" \
  "s_nop 0\n\t" \
  "v_max_i32_dpp v116, v116, v116 quad_perm:[2,3,0,1] row_mask:0xf bank_mask:0xf\n\t" \
  "s_nop 1\n\t" \
  "v_max_i32_dpp v116, v116, v116 row_half_mirror row_mask:0xf bank_mask:0xf\n\t" \
  "s_nop 1\n\t" \
  "v_max_i32_dpp v116, v116, v116 row_mirror row_mask:0xf bank_mask:0xf\n\t" \
  "v_med3_i32 v116, v116, 0, v117\n\t" \
  "v_mul_u32_u24 v116, s52, v116\n\t" \
  "v_lshrrev_b32 v116, 16, v116\n\t" \
  "s_mov_b64 exec, -1\n\t" \
  "v_readlane_b32 s84, v116, 0\n\t" \
  "v_readlane_b32 s85, v116, 16\n\t" \
  "s_max_u32 s84, s84, s85\n\t" \
  "s_add_i32 s84, s84, s51\n\t" \
  "v_mov_b64 v[68:69], 0\n\t" \
  "v_mov_b64 v[70:71], 0\n\t" \
  "v_mov_b64 v[72:73], 0\n\t" \
  "v_mov_b64 v[74:75], 0\n\t" \
  "v_mov_b64 v[76:77], 0\n\t" \
  "v_mov_b64 v[78:79], 0\n\t" \
  "v_mov_b64 v[80:81], 0\n\t" \
  "v_mov_b64 v[82:83], 0\n\t" \
  "1:\n\t" \
  "s_memtime vcc\n\t" \
  "s_waitcnt lgkmcnt(0)\n\t" \
  "s_sub_i32 s79, s84, vcc_lo\n\t" \
  "s_cmp_gt_i32 s79, 128\n\t" \
  "s_cbranch_scc1 1b\n\t" \
  "2:\n\t" \
  "s_sub_i32 s79, s79, 33\n\t" \
  "s_cmp_gt_i32 s79, 56\n\t" \
  "s_cbranch_scc1 2b\n\t" \
  "buffer_load_dwordx4 v[32:35], v100, %[rh], s40 offen sc1\n\t" \
  "s_nop 12\n\t" \
  "buffer_load_dwordx4 v[36:39], v100, %[rh], s76 offen sc1\n\t" \
  "s_nop 12\n\t" \
  "buffer_load_dwordx4 v[40:43], v100, %[rh], s77 offen sc1\n\t" \
  "s_nop 12\n\t" \
  "buffer_load_dwordx4 v[44:47], v100, %[rh], s78 offen sc1\n\t" \
  "s_memtime vcc\n\t" \
  "s_memrealtime s[82:83]\n\t" \
  "s_nop 4\n\t" \
  "s_memrealtime s[84:85]\n\t" \
  "s_nop 4\n\t" \
  "s_memrealtime s[94:95]\n\t" \
  "s_nop 4\n\t" \
  "s_memrealtime s[86:87]\n\t" \
  "s_nop 4\n\t" \
  "s_memrealtime s[88:89]\n\t" \
  "s_nop 4\n\t" \
  "s_memrealtime s[90:91]\n\t" \
  "s_mov_b32 s92, 0\n\t" \
  "s_waitcnt lgkmcnt(0)\n\t" \
  "s_cmp_eq_u32 s82, s84\n\t" \
  "s_addc_u32 s92, s92, 0\n\t" \
  "s_cmp_eq_u32 s82, s94\n\t" \
  "s_addc_u32 s92, s92, 0\n\t" \
  "s_cmp_eq_u32 s82, s86\n\t" \
  "s_addc_u32 s92, s92, 0\n\t" \
  "s_cmp_eq_u32 s82, s88\n\t" \
  "s_addc_u32 s92, s92, 0\n\t" \
  "s_cmp_eq_u32 s82, s90\n\t" \
  "s_addc_u32 s92, s92, 0\n\t" \
  "s_mul_i32 s92, s92, 24\n\t" \
  "s_add_i32 s92, s92, vcc_lo\n\t" \
  "s_lshl4_add_u32 s93, s82, 64\n\t" \
  "3:\n\t" \
  "s_sub_i32 s84, s92, s43\n\t" \
  "s_lshl_b32 s85, s93, 8\n\t" \
  "s_or_b32 s85, s85, s44\n\t" \
  "v_mov_b32 v118, s85\n\t" \
  "s_waitcnt lgkmcnt(0)\n\t" \
  "s_waitcnt vmcnt(3)\n\t" \
  "v_smfmac_f32_16x16x64_bf16 v[68:71], v[32:35], v[152:159], v104\n\t" \
  "ds_write_b128 v101, v[32:35] offset:12288\n\t" \
  "v_smfmac_f32_16x16x64_bf16 v[72:75], v[32:35], v[160:167], v104\n\t" \
  "v_smfmac_f32_16x16x64_bf16 v[76:79], v[32:35], v[48:55], v104\n\t" \
  "v_smfmac_f32_16x16x64_bf16 v[80:83], v[32:35], v[56:63], v104\n\t" \
  "s_waitcnt vmcnt(2)\n\t" \
  "v_smfmac_f32_16x16x64_bf16 v[68:71], v[36:39], v[168:175], v104\n\t" \
  "ds_write_b128 v101, v[36:39] offset:13312\n\t" \
  "v_smfmac_f32_16x16x64_bf16 v[72:75], v[36:39], v[176:183], v104\n\t" \
  "v_smfmac_f32_16x16x64_bf16 v[76:79], v[36:39], v[184:191], v104\n\t" \
  "v_cmp_u_f32 vcc, v68, v69\n\t" \
  "v_smfmac_f32_16x16x64_bf16 v[80:83], v[36:39], v[192:199], v104\n\t" \
  "v_cmp_u_f32 s[82:83], v70, v71\n\t" \
  "s_waitcnt vmcnt(1)\n\t" \
  "v_smfmac_f32_16x16x64_bf16 v[68:71], v[40:43], v[16:23], v104\n\t" \
  "ds_write_b128 v101, v[40:43] offset:14336\n\t" \
  "v_smfmac_f32_16x16x64_bf16 v[72:75], v[40:43], v[24:31], v104\n\t" \
  "s_or_b64 vcc, vcc, s[82:83]\n\t" \
  "s_or_b64 vcc, vcc, s[80:81]\n\t" \
  "s_waitcnt lgkmcnt(1)\n\t" \
  "v_smfmac_f32_16x16x64_bf16 v[76:79], v[40:43], v[200:207], v104\n\t" \
  "s_cbranch_scc1 8f\n\t" \
  "5:\n\t" \
  "s_barrier\n\t" \
  "4:\n\t" \
  "s_memtime s[56:57]\n\t" \
  "ds_read_b128 v[48:51], v101 offset:0\n\t" \
  "v_smfmac_f32_16x16x64_bf16 v[80:83], v[40:43], v[208:215], v104\n\t" \
  "ds_read_b128 v[52:55], v101 offset:1024\n\t" \
  "ds_read_b128 v[56:59], v101 offset:4096\n\t" \
  "s_waitcnt vmcnt(0)\n\t" \
  "v_smfmac_f32_16x16x64_bf16 v[68:71], v[44:47], v[216:223], v104\n\t" \
  "v_smfmac_f32_16x16x64_bf16 v[72:75], v[44:47], v[224:231], v104\n\t" \
  "ds_write_b128 v101, v[44:47] offset:15360\n\t" \
  "v_smfmac_f32_16x16x64_bf16 v[76:79], v[44:47], v[232:239], v104\n\t" \
  "v_cmp_u_f32 vcc, v68, v69\n\t" \
  "v_smfmac_f32_16x16x64_bf16 v[80:83], v[44:47], v[240:247], v104\n\t" \
  "v_cmp_u_f32 s[82:83], v70, v71\n\t" \
  "s_waitcnt lgkmcnt(3)\n\t" \
  "v_smfmac_f32_16x16x64_bf16 v[68:71], v[48:51], a[0:7], v104\n\t" \
  "ds_read_b128 v[60:63], v101 offset:5120\n\t" \
  "ds_read_b128 v[64:67], v101 offset:8192\n\t" \
  "v_smfmac_f32_16x16x64_bf16 v[72:75], v[48:51], a[8:15], v104\n\t" \
  "s_or_b64 vcc, vcc, s[82:83]\n\t" \
  "s_waitcnt lgkmcnt(2)\n\t" \
  "v_smfmac_f32_16x16x64_bf16 v[68:71], v[52:55], a[16:23], v104\n\t" \
  "s_cbranch_scc1 9f\n\t" \
  "6:\n\t" \
  "s_barrier\n\t" \
  "v_smfmac_f32_16x16x64_bf16 v[72:75], v[52:55], a[24:31], v104\n\t" \
  "s_waitcnt lgkmcnt(0)\n\t" \
  "ds_read_b128 v[48:51], v101 offset:9216\n\t" \
  "v_smfmac_f32_16x16x64_bf16 v[68:71], v[56:59], a[96:103], v104\n\t" \
  "ds_read_b128 v[52:55], v101 offset:2048\n\t" \
  "s_sub_i32 s79, s56, s84\n\t" \
  "s_mul_hi_u32 s79, s79, s46\n\t" \
  "v_smfmac_f32_16x16x64_bf16 v[72:75], v[56:59], a[104:111], v104\n\t" \
  "v_lshl_add_u32 v116, s79, 9, v118\n\t" \
  "buffer_store_dword v116, v112, %[rh], s45 offen sc1\n\t" \
  "v_smfmac_f32_16x16x64_bf16 v[68:71], v[60:63], a[112:119], v104\n\t" \
  "ds_read_b128 v[56:59], v101 offset:3072\n\t" \
  "v_smfmac_f32_16x16x64_bf16 v[72:75], v[60:63], a[120:127], v104\n\t" \
  "v_smfmac_f32_16x16x64_bf16 v[68:71], v[64:67], a[192:199], v104\n\t" \
  "ds_read_b128 v[60:63], v101 offset:6144\n\t" \
  "v_smfmac_f32_16x16x64_bf16 v[72:75], v[64:67], a[200:207], v104\n\t" \
  "ds_read_b128 v[16:19], v103 offset:16384\n\t" \
  "s_waitcnt lgkmcnt(4)\n\t" \
  "v_smfmac_f32_16x16x64_bf16 v[68:71], v[48:51], a[208:215], v104\n\t" \
  "ds_read_b128 v[64:67], v101 offset:7168\n\t" \
  "v_smfmac_f32_16x16x64_bf16 v[72:75], v[48:51], a[216:223], v104\n\t" \
  "ds_read_b128 v[20:23], v103 offset:17408\n\t" \
  "s_waitcnt lgkmcnt(5)\n\t" \
  "v_smfmac_f32_16x16x64_bf16 v[68:71], v[52:55], v[84:91], v104\n\t" \
  "ds_read_b128 v[48:51], v101 offset:11264\n\t" \
  "v_smfmac_f32_16x16x64_bf16 v[72:75], v[52:55], v[92:99], v104\n\t" \
  "ds_read_b128 v[24:27], v103 offset:18432\n\t" \
  "s_waitcnt lgkmcnt(6)\n\t" \
  "v_smfmac_f32_16x16x64_bf16 v[68:71], v[56:59], a[64:71], v104\n\t" \
  "ds_read_b128 v[52:55], v101 offset:10240\n\t" \
  "v_smfmac_f32_16x16x64_bf16 v[72:75], v[56:59], a[72:79], v104\n\t" \
  "ds_read_b128 v[28:31], v103 offset:19456\n\t" \
  "s_waitcnt lgkmcnt(7)\n\t" \
  "v_smfmac_f32_16x16x64_bf16 v[68:71], v[60:63], v[0:7], v104\n\t" \
  "v_smfmac_f32_16x16x64_bf16 v[72:75], v[60:63], v[8:15], v104\n\t" \
  "s_waitcnt lgkmcnt(5)\n\t" \
  "v_smfmac_f32_16x16x64_bf16 v[68:71], v[64:67], a[160:167], v104\n\t" \
  "ds_read_b128 v[0:3], v102 offset:0\n\t" \
  "v_smfmac_f32_16x16x64_bf16 v[72:75], v[64:67], a[168:175], v104\n\t" \
  "ds_read_b128 v[4:7], v102 offset:1024\n\t" \
  "s_waitcnt lgkmcnt(5)\n\t" \
  "v_smfmac_f32_16x16x64_bf16 v[68:71], v[48:51], v[120:127], v104\n\t" \
  "ds_read_b128 v[8:11], v102 offset:2048\n\t" \
  "s_waitcnt lgkmcnt(4)\n\t" \
  "v_smfmac_f32_16x16x64_bf16 v[68:71], v[52:55], v[16:23], v104\n\t" \
  "ds_read_b128 v[12:15], v102 offset:3072\n\t" \
  "v_smfmac_f32_16x16x64_bf16 v[72:75], v[48:51], v[128:135], v104\n\t" \
  "s_waitcnt lgkmcnt(4)\n\t" \
  "v_smfmac_f32_16x16x64_bf16 v[72:75], v[52:55], v[24:31], v104\n\t" \
  "v_add_f32 v116, v68, v70\n\t" \
  "v_add_f32 v117, v69, v71\n\t" \
  "v_smfmac_f32_16x16x64_bf16 v[76:79], v[56:59], a[80:87], v104\n\t" \
  "v_cvt_pk_bf16_f32 v116, v116, v117\n\t" \
  "ds_read_b128 v[24:27], v102 offset:34816\n\t" \
  "v_smfmac_f32_16x16x64_bf16 v[80:83], v[56:59], a[88:95], v104\n\t" \
  "v_add_f32 v117, v72, v74\n\t" \
  "v_add_f32 v118, v73, v75\n\t" \
  "v_cvt_pk_bf16_f32 v117, v117, v118\n\t" \
  "ds_write2st64_b32 v105, v116, v117 offset1:1\n\t" \
  "v_smfmac_f32_16x16x64_bf16 v[76:79], v[60:63], a[144:151], v104\n\t" \
  "v_smfmac_f32_16x16x64_bf16 v[80:83], v[60:63], a[152:159], v104\n\t" \
  "v_smfmac_f32_16x16x64_bf16 v[76:79], v[64:67], a[176:183], v104\n\t" \
  "buffer_load_dword v117, v107, %[ru], 0 offen sc1\n\t" \
  "v_smfmac_f32_16x16x64_bf16 v[80:83], v[64:67], a[184:191], v104\n\t" \
  "s_waitcnt lgkmcnt(0)\n\t" \
  "s_barrier\n\t" \
  "ds_read_b128 v[56:59], v101 offset:0\n\t" \
  "v_smfmac_f32_16x16x64_bf16 v[76:79], v[48:51], v[136:143], v104\n\t" \
  "v_smfmac_f32_16x16x64_bf16 v[80:83], v[48:51], v[144:151], v104\n\t" \
  "ds_read_b128 v[60:63], v101 offset:9216\n\t" \
  "buffer_load_dwordx4 v[84:87], v100, %[ru], s47 offen offset:0 sc1\n\t" \
  "v_smfmac_f32_16x16x64_bf16 v[76:79], v[52:55], a[240:247], v104\n\t" \
  "ds_read_b128 v[16:19], v102 offset:32768\n\t" \
  "v_smfmac_f32_16x16x64_bf16 v[80:83], v[52:55], a[248:255], v104\n\t" \
  "ds_read_b128 v[64:67], v101 offset:2048\n\t" \
  "buffer_load_dwordx4 v[88:91], v100, %[ru], s47 offen offset:1024 sc1\n\t" \
  "s_waitcnt lgkmcnt(3)\n\t" \
  "v_smfmac_f32_16x16x64_bf16 v[76:79], v[56:59], v[0:7], v104\n\t" \
  "ds_read_b128 v[20:23], v102 offset:33792\n\t" \
  "v_smfmac_f32_16x16x64_bf16 v[80:83], v[56:59], v[8:15], v104\n\t" \
  "ds_read_b128 v[48:51], v101 offset:4096\n\t" \
  "buffer_load_dwordx4 v[92:95], v100, %[ru], s47 offen offset:2048 sc1\n\t" \
  "s_waitcnt lgkmcnt(4)\n\t" \
  "v_smfmac_f32_16x16x64_bf16 v[76:79], v[60:63], a[224:231], v104\n\t" \
  "ds_read_b128 v[28:31], v102 offset:35840\n\t" \
  "v_smfmac_f32_16x16x64_bf16 v[80:83], v[60:63], a[232:239], v104\n\t" \
  "ds_read_b128 v[52:55], v101 offset:1024\n\t" \
  "buffer_load_dwordx4 v[96:99], v100, %[ru], s47 offen offset:3072 sc1\n\t" \
  "s_waitcnt lgkmcnt(4)\n\t" \
  "v_smfmac_f32_16x16x64_bf16 v[76:79], v[64:67], a[48:55], v104\n\t" \
  "ds_read_b128 v[0:3], v103 offset:0\n\t" \
  "v_smfmac_f32_16x16x64_bf16 v[80:83], v[64:67], a[56:63], v104\n\t" \
  "ds_read_b128 v[56:59], v101 offset:5120\n\t" \
  "ds_read_b128 v[4:7], v103 offset:1024\n\t" \
  "buffer_load_dwordx4 v[32:35], v100, %[ru], s97 offen offset:0 sc1\n\t" \
  "s_waitcnt lgkmcnt(5)\n\t" \
  "v_smfmac_f32_16x16x64_bf16 v[76:79], v[48:51], v[16:23], v104\n\t" \
  "ds_read_b128 v[60:63], v101 offset:8192\n\t" \
  "s_waitcnt lgkmcnt(5)\n\t" \
  "v_smfmac_f32_16x16x64_bf16 v[80:83], v[48:51], v[24:31], v104\n\t" \
  "ds_read_b128 v[8:11], v103 offset:2048\n\t" \
  "buffer_load_dwordx4 v[36:39], v100, %[ru], s97 offen offset:1024 sc1\n\t" \
  "s_waitcnt lgkmcnt(5)\n\t" \
  "v_smfmac_f32_16x16x64_bf16 v[76:79], v[52:55], a[32:39], v104\n\t" \
  "ds_read_b128 v[12:15], v103 offset:3072\n\t" \
  "v_smfmac_f32_16x16x64_bf16 v[80:83], v[52:55], a[40:47], v104\n\t" \
  "buffer_load_dwordx4 v[40:43], v100, %[ru], s97 offen offset:2048 sc1\n\t" \
  "s_waitcnt lgkmcnt(4)\n\t" \
  "v_smfmac_f32_16x16x64_bf16 v[76:79], v[56:59], a[128:135], v104\n\t" \
  "s_waitcnt lgkmcnt(2)\n\t" \
  "v_smfmac_f32_16x16x64_bf16 v[76:79], v[60:63], v[0:7], v104\n\t" \
  "buffer_load_dwordx4 v[44:47], v100, %[ru], s97 offen offset:3072 sc1\n\t" \
  "v_smfmac_f32_16x16x64_bf16 v[80:83], v[56:59], a[136:143], v104\n\t" \
  "s_waitcnt lgkmcnt(0)\n\t" \
  "v_smfmac_f32_16x16x64_bf16 v[80:83], v[60:63], v[8:15], v104\n\t" \
  "v_add_f32 v68, v76, v78\n\t" \
  "v_add_f32 v69, v77, v79\n\t" \
  "v_cvt_pk_bf16_f32 v68, v68, v69\n\t" \
  "ds_write_b32 v106, v68\n\t" \
  "s_nop 3\n\t" \
  "v_add_f32 v70, v80, v82\n\t" \
  "v_add_f32 v71, v81, v83\n\t" \
  "v_cvt_pk_bf16_f32 v70, v70, v71\n\t" \
  "ds_write_b32 v106, v70 offset:256\n\t" \
  "s_waitcnt lgkmcnt(0)\n\t" \
  "s_barrier\n\t" \
  "s_memtime s[76:77]\n\t" \
  "s_waitcnt lgkmcnt(0)\n\t" \
  "s_add_i32 s57, s76, 240\n\t" \
  "s_sleep 5\n\t" \
  "s_waitcnt vmcnt(8)\n\t" \
  "v_readfirstlane_b32 s76, v117\n\t" \
  "s_add_i32 s77, s44, 2\n\t" \
  "s_min_i32 s77, s77, 63\n\t" \
  "s_cmp_lt_i32 s76, s77\n\t" \
  "s_cselect_b32 s42, 32, 0\n\t" \
  "s_add_i32 s41, s56, 1950\n\t" \
  "s_sub_i32 s76, s57, s56\n\t" \
  "s_add_i32 s77, s43, 8\n\t" \
  "s_min_u32 s76, s76, s77\n\t" \
  "s_sub_i32 s43, s43, 4\n\t" \
  "s_max_u32 s43, s43, s76\n\t" \
  "s_add_i32 s76, s45, 0x10000\n\t" \
  "s_and_b32 s76, s76, 0x30000\n\t" \
  "v_mov_b32 v116, -1\n\t" \
  "buffer_store_dword v116, v113, %[rh], s76 offen sc1\n\t" \
  "s_sub_i32 s76, s92, s51\n\t" \
  "s_sub_i32 s77, s93, s53\n\t" \
  "s_cmp_eq_u32 s76, 0\n\t" \
  "s_cbranch_scc1 21f\n\t" \
  "v_cvt_f32_u32 v117, s76\n\t" \
  "v_cvt_f32_u32 v116, s77\n\t" \
  "v_rcp_f32 v117, v117\n\t" \
  "s_mov_b32 s51, s92\n\t" \
  "v_mul_f32 v117, v117, v116\n\t" \
  "v_max_f32 v117, 0x3f028f5c, v117\n\t" \
  "v_min_f32 v117, 0x3ffeb852, v117\n\t" \
  "v_rcp_f32 v116, v117\n\t" \
  "v_mul_f32 v117, 0x4f000000, v117\n\t" \
  "v_mul_f32 v116, 0x47800000, v116\n\t" \
  "v_cvt_u32_f32 v117, v117\n\t" \
  "v_cvt_u32_f32 v116, v116\n\t" \
  "s_mov_b32 s53, s93\n\t" \
  "v_readfirstlane_b32 s46, v117\n\t" \
  "v_readfirstlane_b32 s52, v116\n\t" \
  "21:\n\t" \
  "s_waitcnt vmcnt(0)\n\t" \
  "ds_write_b128 v101, v[84:87] offset:20480\n\t" \
  "ds_write_b128 v101, v[88:91] offset:21504\n\t" \
  "ds_write_b128 v101, v[92:95] offset:22528\n\t" \
  "ds_write_b128 v101, v[96:99] offset:23552\n\t" \
  "ds_write_b128 v101, v[32:35] offset:24576\n\t" \
  "ds_write_b128 v101, v[36:39] offset:25600\n\t" \
  "ds_write_b128 v101, v[40:43] offset:26624\n\t" \
  "ds_write_b128 v101, v[44:47] offset:27648\n\t" \
  "v_mov_b32 v0, -1\n\t" \
  "v_mov_b32 v1, -1\n\t" \
  "v_mov_b32 v2, -1\n\t" \
  "v_mov_b32 v3, -1\n\t" \
  "s_and_b32 s76, s49, 3\n\t" \
  "s_lshl_b32 s76, s76, 16\n\t" \
  "buffer_store_dwordx4 v[0:3], v109, %[rh], s76 offen sc1\n\t" \
  "s_add_i32 s44, s44, 1\n\t" \
  "s_and_b32 s58, s58, 4\n\t" \
  "s_and_b32 s76, s44, 64\n\t" \
  "s_or_b32 s42, s42, s76\n\t" \
  "s_cbranch_scc0 20b\n\t" \
  "s_branch 99f\n\t" \
  "14:\n\t" \
  "s_add_u32 s59, s59, 0x10000\n\t" \
  "s_cmp_lt_u32 s59, 0x100000\n\t" \
  "s_cbranch_scc1 13b\n\t" \
  "s_mov_b64 exec, -1\n\t" \
  "s_mov_b32 s42, 16\n\t" \
  "s_branch 99f\n\t" \
  "8:\n\t" \
  "s_bitcmp1_b32 s58, 1\n\t" \
  "s_cbranch_scc1 4b\n\t" \
  "s_bitcmp1_b32 s58, 2\n\t" \
  "s_cbranch_scc1 5b\n\t" \
  "v_max3_u32 v116, v32, v33, v34\n\t" \
  "v_max3_u32 v117, v35, v36, v37\n\t" \
  "v_max3_u32 v116, v116, v38, v39\n\t" \
  "v_max_u32 v116, v116, v117\n\t" \
  "v_cmp_eq_u32 vcc, -1, v116\n\t" \
  "s_cbranch_vccz 5b\n\t" \
  "s_branch 0f\n\t" \
  "9:\n\t" \
  "s_bitcmp1_b32 s58, 2\n\t" \
  "s_cbranch_scc1 6b\n\t" \
  "v_max3_u32 v116, v40, v41, v42\n\t" \
  "v_max3_u32 v117, v43, v44, v45\n\t" \
  "v_max3_u32 v116, v116, v46, v47\n\t" \
  "v_max_u32 v116, v116, v117\n\t" \
  "v_cmp_eq_u32 vcc, -1, v116\n\t" \
  "s_cbranch_vccz 6b\n\t" \
  "s_or_b32 s58, s58, 2\n\t" \
  "0:\n\t" \
  "s_or_b32 s58, s58, 1\n\t" \
  "11:\n\t" \
  "buffer_load_dwordx4 v[32:35], v100, %[rh], s40 offen sc1\n\t" \
  "buffer_load_dwordx4 v[36:39], v100, %[rh], s76 offen sc1\n\t" \
  "buffer_load_dwordx4 v[40:43], v100, %[rh], s77 offen sc1\n\t" \
  "buffer_load_dwordx4 v[44:47], v100, %[rh], s78 offen sc1\n\t" \
  "s_waitcnt lgkmcnt(0)\n\t" \
  "s_branch 10b\n\t" \
  "17:\n\t" \
  "v_mov_b64 v[68:69], 0\n\t" \
  "v_mov_b64 v[70:71], 0\n\t" \
  "v_mov_b64 v[72:73], 0\n\t" \
  "v_mov_b64 v[74:75], 0\n\t" \
  "v_mov_b64 v[76:77], 0\n\t" \
  "v_mov_b64 v[78:79], 0\n\t" \
  "v_mov_b64 v[80:81], 0\n\t" \
  "v_mov_b64 v[82:83], 0\n\t" \
  "s_add_u32 s59, s59, 1\n\t" \
  "s_bitcmp1_b32 s58, 2\n\t" \
  "s_cbranch_scc1 3b\n\t" \
  "s_waitcnt vmcnt(0)\n\t" \
  "v_max_u32 v116, v32, v33\n\t" \
  "v_max3_u32 v116, v116, v34, v35\n\t" \
  "v_max3_u32 v116, v116, v36, v37\n\t" \
  "v_max3_u32 v116, v116, v38, v39\n\t" \
  "v_max3_u32 v116, v116, v40, v41\n\t" \
  "v_max3_u32 v116, v116, v42, v43\n\t" \
  "v_max3_u32 v116, v116, v44, v45\n\t" \
  "v_max3_u32 v116, v116, v46, v47\n\t" \
  "v_cmp_eq_u32 vcc, -1, v116\n\t" \
  "s_cbranch_vccz 3b\n\t" \
  "s_bitcmp1_b32 s59, 6\n\t" \
  "s_cbranch_scc1 12f\n\t" \
  "buffer_load_dwordx4 v[32:35], v100, %[rh], s40 offen sc1\n\t" \
  "buffer_load_dwordx4 v[36:39], v100, %[rh], s76 offen sc1\n\t" \
  "buffer_load_dwordx4 v[40:43], v100, %[rh], s77 offen sc1\n\t" \
  "buffer_load_dwordx4 v[44:47], v100, %[rh], s78 offen sc1\n\t" \
  "s_branch 17b\n\t" \
  "12:\n\t" \
  "s_mov_b32 s42, s58\n\t" \
  "s_branch 99f\n\t" \
  "99:\n\t" \
  "s_waitcnt vmcnt(0) lgkmcnt(0)\n\t" \
  ""
#define VO_BODY \
  "s_cmp_eq_u32 %[wv], 1\n\t" \
  "s_cbranch_scc1 91f\n\t" \
  "s_cmp_eq_u32 %[wv], 2\n\t" \
  "s_cbranch_scc1 92f\n\t" \
  "s_cmp_eq_u32 %[wv], 3\n\t" \
  "s_cbranch_scc1 93f\n\t" \
  "90:\n\t" \
  "s_mov_b32 s58, s42\n\t" \
  ".p2align 6\n\t" \
  "20:\n\t" \
  "s_and_b32 s76, s44, 7\n\t" \
  "s_lshl_b32 s76, s76, 15\n\t" \
  "s_add_i32 s40, s54, s76\n\t" \
  "s_and_b32 s76, s44, 3\n\t" \
  "s_lshl_b32 s45, s76, 16\n\t" \
  "s_and_b32 s47, s44, 3\n\t" \
  "s_lshl_b32 s76, s47, 2\n\t" \
  "s_lshl_b32 s47, s47, 20\n\t" \
  "s_add_i32 s47, s47, s55\n\t" \
  "s_lshr_b32 s76, %[um], s76\n\t" \
  "s_and_b32 s76, s76, 7\n\t" \
  "s_lshl_b32 s76, s76, 12\n\t" \
  "s_xor_b32 s47, s47, s76\n\t" \
  "s_add_i32 s49, s44, -1\n\t" \
  "s_and_b32 s48, s49, 3\n\t" \
  "s_lshl_b32 s48, s48, 16\n\t" \
  "s_lshl_b32 s50, s53, 8\n\t" \
  "s_mov_b32 s59, 0\n\t" \
  "s_mov_b32 s92, s51\n\t" \
  "s_mov_b32 s93, s53\n\t" \
  "s_xor_b32 s76, s40, 0x1000\n\t" \
  "s_xor_b32 s77, s40, 0x4000\n\t" \
  "s_xor_b32 s78, s40, 0x5000\n\t" \
  "s_cmp_lg_u32 s58, 0\n\t" \
  "s_cbranch_scc1 11f\n\t" \
  "10:\n\t" \
  "ds_read_b128 v[48:51], v102 offset:0\n\t" \
  "ds_read_b128 v[52:55], v102 offset:1024\n\t" \
  "ds_read_b128 v[56:59], v102 offset:2048\n\t" \
  "ds_read_b128 v[60:63], v102 offset:3072\n\t" \
  "ds_read_b128 v[0:3], v102 offset:16384\n\t" \
  "ds_read_b128 v[4:7], v102 offset:17408\n\t" \
  "ds_read_b128 v[8:11], v102 offset:18432\n\t" \
  "ds_read_b128 v[12:15], v102 offset:19456\n\t" \
  "ds_read_b128 v[16:19], v102 offset:32768\n\t" \
  "ds_read_b128 v[20:23], v102 offset:33792\n\t" \
  "ds_read_b128 v[24:27], v102 offset:34816\n\t" \
  "ds_read_b128 v[28:31], v102 offset:35840\n\t" \
  "s_waitcnt lgkmcnt(4)\n\t" \
  "ds_read_b128 v[80:83], v102 offset:49152\n\t" \
  "ds_read_b128 v[84:87], v102 offset:50176\n\t" \
  "ds_read_b128 v[88:91], v102 offset:51200\n\t" \
  "ds_read_b128 v[92:95], v102 offset:52224\n\t" \
  "s_bitcmp1_b32 s58, 1\n\t" \
  "s_cselect_b64 s[80:81], -1, 0\n\t" \
  "s_cmp_lg_u32 s58, 0\n\t" \
  "s_cbranch_scc1 17f\n\t" \
  "s_mov_b32 exec_hi, 0\n\t" \
  "16:\n\t" \
  "s_memtime vcc\n\t" \
  "s_waitcnt lgkmcnt(0)\n\t" \
  "s_sub_i32 s79, s41, vcc_lo\n\t" \
  "s_cmp_gt_i32 s79, 64\n\t" \
  "s_cbranch_scc1 16b\n\t" \
  "13:\n\t" \
  "buffer_load_dword v116, v110, %[rh], s48 offen sc1\n\t" \
  "s_waitcnt vmcnt(0)\n\t" \
  "15:\n\t" \
  "v_mov_b32 v117, 20000\n\t" \
  "v_cmp_ne_u32_sdwa vcc, v116, s49 src0_sel:BYTE_0 src1_sel:DWORD\n\t" \
  "v_subrev_u32 v116, s50, v116\n\t" \
  "v_ashrrev_i32 v116, 8, v116\n\t" \
  "s_nop 1\n\t" \
  "v_max_i32_dpp v116, v116, v116 quad_perm:[1,0,3,2] row_mask:0xf bank_mask:0xf\n\t" \
  "s_cbranch_vccnz 14f\n\t" \
  "s_nop 0\n\t" \
  "v_max_i32_dpp v116, v116, v116 quad_perm:[2,3,0,1] row_mask:0xf bank_mask:0xf\n\t" \
  "s_nop 1\n\t" \
  "v_max_i32_dpp v116, v116, v116 row_half_mirror row_mask:0xf bank_mask:0xf\n\t" \
  "s_nop 1\n\t" \
  "v_max_i32_dpp v116, v116, v116 row_mirror row_mask:0xf bank_mask:0xf\n\t" \
  "v_med3_i32 v116, v116, 0, v117\n\t" \
  "v_mul_u32_u24 v116, s52, v116\n\t" \
  "v_lshrrev_b32 v116, 16, v116\n\t" \
  "s_mov_b64 exec, -1\n\t" \
  "v_readlane_b32 s84, v116, 0\n\t" \
  "v_readlane_b32 s85, v116, 16\n\t" \
  "s_max_u32 s84, s84, s85\n\t" \
  "s_add_i32 s84, s84, s51\n\t" \
  "v_mov_b64 v[72:73], 0\n\t" \
  "v_mov_b64 v[74:75], 0\n\t" \
  "v_mov_b64 v[76:77], 0\n\t" \
  "v_mov_b64 v[78:79], 0\n\t" \
  "1:\n\t" \
  "s_memtime vcc\n\t" \
  "s_waitcnt lgkmcnt(0)\n\t" \
  "s_sub_i32 s79, s84, vcc_lo\n\t" \
  "s_cmp_gt_i32 s79, 128\n\t" \
  "s_cbranch_scc1 1b\n\t" \
  "2:\n\t" \
  "s_sub_i32 s79, s79, 33\n\t" \
  "s_cmp_gt_i32 s79, 56\n\t" \
  "s_cbranch_scc1 2b\n\t" \
  "buffer_load_dwordx4 v[32:35], v100, %[rh], s40 offen sc1\n\t" \
  "s_nop 12\n\t" \
  "buffer_load_dwordx4 v[36:39], v100, %[rh], s76 offen sc1\n\t" \
  "s_nop 12\n\t" \
  "buffer_load_dwordx4 v[40:43], v100, %[rh], s77 offen sc1\n\t" \
  "s_nop 12\n\t" \
  "buffer_load_dwordx4 v[44:47], v100, %[rh], s78 offen sc1\n\t" \
  "s_memtime vcc\n\t" \
  "s_memrealtime s[82:83]\n\t" \
  "s_nop 4\n\t" \
  "s_memrealtime s[84:85]\n\t" \
  "s_nop 4\n\t" \
  "s_memrealtime s[94:95]\n\t" \
  "s_nop 4\n\t" \
  "s_memrealtime s[86:87]\n\t" \
  "s_nop 4\n\t" \
  "s_memrealtime s[88:89]\n\t" \
  "s_nop 4\n\t" \
  "s_memrealtime s[90:91]\n\t" \
  "s_mov_b32 s92, 0\n\t" \
  "s_waitcnt lgkmcnt(0)\n\t" \
  "s_cmp_eq_u32 s82, s84\n\t" \
  "s_addc_u32 s92, s92, 0\n\t" \
  "s_cmp_eq_u32 s82, s94\n\t" \
  "s_addc_u32 s92, s92, 0\n\t" \
  "s_cmp_eq_u32 s82, s86\n\t" \
  "s_addc_u32 s92, s92, 0\n\t" \
  "s_cmp_eq_u32 s82, s88\n\t" \
  "s_addc_u32 s92, s92, 0\n\t" \
  "s_cmp_eq_u32 s82, s90\n\t" \
  "s_addc_u32 s92, s92, 0\n\t" \
  "s_mul_i32 s92, s92, 24\n\t" \
  "s_add_i32 s92, s92, vcc_lo\n\t" \
  "s_lshl4_add_u32 s93, s82, 64\n\t" \
  "3:\n\t" \
  "s_waitcnt lgkmcnt(0)\n\t" \
  "s_waitcnt vmcnt(3)\n\t" \
  "v_smfmac_f32_16x16x64_bf16 v[72:75], v[32:35], v[48:55], v104\n\t" \
  "ds_write_b128 v101, v[32:35] offset:0\n\t" \
  "v_smfmac_f32_16x16x64_bf16 v[76:79], v[32:35], v[56:63], v104\n\t" \
  "s_waitcnt vmcnt(2)\n\t" \
  "ds_write_b128 v101, v[36:39] offset:1024\n\t" \
  "v_smfmac_f32_16x16x64_bf16 v[72:75], v[36:39], a[0:7], v104\n\t" \
  "v_smfmac_f32_16x16x64_bf16 v[76:79], v[36:39], a[8:15], v104\n\t" \
  "s_nop 3\n\t" \
  "v_cmp_u_f32 vcc, v72, v73\n\t" \
  "v_cmp_u_f32 s[82:83], v74, v75\n\t" \
  "s_waitcnt vmcnt(1)\n\t" \
  "v_smfmac_f32_16x16x64_bf16 v[72:75], v[40:43], a[16:23], v104\n\t" \
  "s_or_b64 vcc, vcc, s[82:83]\n\t" \
  "s_or_b64 vcc, vcc, s[80:81]\n\t" \
  "v_smfmac_f32_16x16x64_bf16 v[76:79], v[40:43], a[24:31], v104\n\t" \
  "s_waitcnt lgkmcnt(0)\n\t" \
  "s_cbranch_scc1 8f\n\t" \
  "5:\n\t" \
  "s_barrier\n\t" \
  "4:\n\t" \
  "s_memtime s[56:57]\n\t" \
  "ds_read_b128 v[48:51], v101 offset:4096\n\t" \
  "ds_read_b128 v[52:55], v101 offset:5120\n\t" \
  "ds_read_b128 v[56:59], v101 offset:9216\n\t" \
  "s_waitcnt vmcnt(0)\n\t" \
  "v_smfmac_f32_16x16x64_bf16 v[72:75], v[44:47], a[32:39], v104\n\t" \
  "ds_read_b128 v[60:63], v101 offset:8192\n\t" \
  "ds_read_b128 v[64:67], v101 offset:13312\n\t" \
  "v_smfmac_f32_16x16x64_bf16 v[76:79], v[44:47], a[40:47], v104\n\t" \
  "ds_read_b128 v[68:71], v101 offset:12288\n\t" \
  "ds_write_b128 v101, v[40:43] offset:2048\n\t" \
  "s_nop 1\n\t" \
  "v_cmp_u_f32 vcc, v72, v73\n\t" \
  "v_cmp_u_f32 s[82:83], v74, v75\n\t" \
  "s_waitcnt lgkmcnt(6)\n\t" \
  "v_smfmac_f32_16x16x64_bf16 v[72:75], v[48:51], v[0:7], v104\n\t" \
  "s_or_b64 vcc, vcc, s[82:83]\n\t" \
  "v_smfmac_f32_16x16x64_bf16 v[76:79], v[48:51], v[8:15], v104\n\t" \
  "s_cbranch_scc1 9f\n\t" \
  "6:\n\t" \
  "s_waitcnt lgkmcnt(5)\n\t" \
  "v_smfmac_f32_16x16x64_bf16 v[72:75], v[52:55], a[48:55], v104\n\t" \
  "ds_write_b128 v101, v[44:47] offset:3072\n\t" \
  "v_smfmac_f32_16x16x64_bf16 v[76:79], v[52:55], a[56:63], v104\n\t" \
  "v_mov_b64 v[40:41], 0\n\t" \
  "s_waitcnt lgkmcnt(5)\n\t" \
  "v_smfmac_f32_16x16x64_bf16 v[72:75], v[56:59], a[96:103], v104\n\t" \
  "v_mov_b64 v[42:43], 0\n\t" \
  "v_smfmac_f32_16x16x64_bf16 v[76:79], v[56:59], a[104:111], v104\n\t" \
  "s_waitcnt lgkmcnt(4)\n\t" \
  "v_smfmac_f32_16x16x64_bf16 v[72:75], v[60:63], v[16:23], v104\n\t" \
  "s_waitcnt lgkmcnt(0)\n\t" \
  "s_barrier\n\t" \
  "v_smfmac_f32_16x16x64_bf16 v[76:79], v[60:63], v[24:31], v104\n\t" \
  "ds_read_b128 v[48:51], v101 offset:6144\n\t" \
  "ds_read_b128 v[52:55], v101 offset:10240\n\t" \
  "v_smfmac_f32_16x16x64_bf16 v[72:75], v[64:67], a[144:151], v104\n\t" \
  "ds_read_b128 v[56:59], v101 offset:14336\n\t" \
  "ds_read_b128 v[60:63], v101 offset:7168\n\t" \
  "v_smfmac_f32_16x16x64_bf16 v[76:79], v[64:67], a[152:159], v104\n\t" \
  "ds_read_b128 v[32:35], v101 offset:11264\n\t" \
  "ds_read_b128 v[36:39], v101 offset:15360\n\t" \
  "v_smfmac_f32_16x16x64_bf16 v[72:75], v[68:71], v[80:87], v104\n\t" \
  "ds_read_b128 v[0:3], v103 offset:0\n\t" \
  "v_mov_b64 v[44:45], 0\n\t" \
  "v_smfmac_f32_16x16x64_bf16 v[76:79], v[68:71], v[88:95], v104\n\t" \
  "v_mov_b64 v[46:47], 0\n\t" \
  "s_waitcnt lgkmcnt(5)\n\t" \
  "v_smfmac_f32_16x16x64_bf16 v[72:75], v[48:51], a[64:71], v104\n\t" \
  "ds_read_b128 v[4:7], v103 offset:1024\n\t" \
  "v_mov_b64 v[80:81], 0\n\t" \
  "v_smfmac_f32_16x16x64_bf16 v[72:75], v[52:55], a[112:119], v104\n\t" \
  "v_mov_b64 v[82:83], 0\n\t" \
  "s_waitcnt lgkmcnt(2)\n\t" \
  "v_smfmac_f32_16x16x64_bf16 v[72:75], v[56:59], a[160:167], v104\n\t" \
  "ds_read_b128 v[8:11], v103 offset:2048\n\t" \
  "v_mov_b64 v[84:85], 0\n\t" \
  "v_smfmac_f32_16x16x64_bf16 v[72:75], v[60:63], a[80:87], v104\n\t" \
  "ds_read_b128 v[12:15], v103 offset:3072\n\t" \
  "v_mov_b64 v[86:87], 0\n\t" \
  "v_smfmac_f32_16x16x64_bf16 v[72:75], v[32:35], a[128:135], v104\n\t" \
  "ds_read_b128 v[16:19], v103 offset:16384\n\t" \
  "v_mov_b64 v[88:89], 0\n\t" \
  "v_smfmac_f32_16x16x64_bf16 v[72:75], v[36:39], a[176:183], v104\n\t" \
  "ds_read_b128 v[20:23], v103 offset:17408\n\t" \
  "v_mov_b64 v[90:91], 0\n\t" \
  "v_smfmac_f32_16x16x64_bf16 v[76:79], v[48:51], a[72:79], v104\n\t" \
  "ds_read_b128 v[24:27], v103 offset:18432\n\t" \
  "v_mov_b64 v[92:93], 0\n\t" \
  "v_smfmac_f32_16x16x64_bf16 v[76:79], v[52:55], a[120:127], v104\n\t" \
  "v_add_f32 v96, v72, v74\n\t" \
  "v_add_f32 v97, v73, v75\n\t" \
  "v_cvt_pk_bf16_f32 v96, v96, v97\n\t" \
  "ds_write_b32 v105, v96\n\t" \
  "v_smfmac_f32_16x16x64_bf16 v[76:79], v[56:59], a[168:175], v104\n\t" \
  "ds_read_b128 v[28:31], v103 offset:19456\n\t" \
  "v_mov_b64 v[94:95], 0\n\t" \
  "v_smfmac_f32_16x16x64_bf16 v[76:79], v[60:63], a[88:95], v104\n\t" \
  "v_smfmac_f32_16x16x64_bf16 v[76:79], v[32:35], a[136:143], v104\n\t" \
  "v_smfmac_f32_16x16x64_bf16 v[76:79], v[36:39], a[184:191], v104\n\t" \
  "s_waitcnt lgkmcnt(1)\n\t" \
  "s_barrier\n\t" \
  "ds_read_b64_tr_b16 v[96:97], v107 offset:0\n\t" \
  "ds_read_b64_tr_b16 v[98:99], v107 offset:64\n\t" \
  "ds_read_b64_tr_b16 v[56:57], v107 offset:1024\n\t" \
  "ds_read_b64_tr_b16 v[58:59], v107 offset:1088\n\t" \
  "s_nop 1\n\t" \
  "v_add_f32 v116, v76, v78\n\t" \
  "v_add_f32 v117, v77, v79\n\t" \
  "v_cvt_pk_bf16_f32 v116, v116, v117\n\t" \
  "ds_write_b32 v105, v116 offset:256\n\t" \
  "s_waitcnt lgkmcnt(3)\n\t" \
  "v_smfmac_f32_16x16x64_bf16 v[40:43], v[96:99], a[192:199], v106\n\t" \
  "v_smfmac_f32_16x16x64_bf16 v[44:47], v[96:99], a[200:207], v106\n\t" \
  "s_waitcnt lgkmcnt(0)\n\t" \
  "s_barrier\n\t" \
  "ds_read_b64_tr_b16 v[52:53], v107 offset:256\n\t" \
  "ds_read_b64_tr_b16 v[54:55], v107 offset:320\n\t" \
  "v_smfmac_f32_16x16x64_bf16 v[40:43], v[56:59], a[224:231], v106\n\t" \
  "ds_read_b64_tr_b16 v[60:61], v107 offset:1280\n\t" \
  "ds_read_b64_tr_b16 v[62:63], v107 offset:1344\n\t" \
  "v_smfmac_f32_16x16x64_bf16 v[44:47], v[56:59], a[232:239], v106\n\t" \
  "v_smfmac_f32_16x16x64_bf16 v[80:83], v[96:99], a[240:247], v106\n\t" \
  "v_mov_b64 v[32:33], 0\n\t" \
  "v_smfmac_f32_16x16x64_bf16 v[84:87], v[96:99], a[248:255], v106\n\t" \
  "v_mov_b64 v[34:35], 0\n\t" \
  "v_smfmac_f32_16x16x64_bf16 v[80:83], v[56:59], v[136:143], v106\n\t" \
  "v_mov_b64 v[36:37], 0\n\t" \
  "v_smfmac_f32_16x16x64_bf16 v[84:87], v[56:59], v[144:151], v106\n\t" \
  "s_waitcnt lgkmcnt(2)\n\t" \
  "v_smfmac_f32_16x16x64_bf16 v[40:43], v[52:55], a[208:215], v106\n\t" \
  "v_smfmac_f32_16x16x64_bf16 v[44:47], v[52:55], a[216:223], v106\n\t" \
  "s_xor_b32 s76, s47, 0x40000\n\t" \
  "s_waitcnt lgkmcnt(0)\n\t" \
  "v_smfmac_f32_16x16x64_bf16 v[40:43], v[60:63], v[0:7], v106\n\t" \
  "v_mov_b64 v[38:39], 0\n\t" \
  "s_xor_b32 s77, s47, 0x84000\n\t" \
  "v_smfmac_f32_16x16x64_bf16 v[44:47], v[60:63], v[8:15], v106\n\t" \
  "s_xor_b32 s78, s47, 0xc4000\n\t" \
  "v_smfmac_f32_16x16x64_bf16 v[80:83], v[52:55], v[120:127], v106\n\t" \
  "ds_read_b128 v[0:3], v103 offset:32768\n\t" \
  "v_cvt_pk_bf16_f32 v64, v40, v41\n\t" \
  "v_smfmac_f32_16x16x64_bf16 v[84:87], v[52:55], v[128:135], v106\n\t" \
  "ds_read_b128 v[4:7], v103 offset:33792\n\t" \
  "v_cvt_pk_bf16_f32 v66, v42, v43\n\t" \
  "v_smfmac_f32_16x16x64_bf16 v[80:83], v[60:63], v[16:23], v106\n\t" \
  "ds_read_b128 v[8:11], v103 offset:34816\n\t" \
  "v_cvt_pk_bf16_f32 v65, v44, v45\n\t" \
  "v_smfmac_f32_16x16x64_bf16 v[84:87], v[60:63], v[24:31], v106\n\t" \
  "ds_read_b128 v[12:15], v103 offset:35840\n\t" \
  "v_cvt_pk_bf16_f32 v67, v46, v47\n\t" \
  "v_smfmac_f32_16x16x64_bf16 v[88:91], v[96:99], v[152:159], v106\n\t" \
  "buffer_store_dwordx4 v[64:67], v108, %[ru], s47 offen sc1\n\t" \
  "v_cvt_pk_bf16_f32 v68, v80, v81\n\t" \
  "v_smfmac_f32_16x16x64_bf16 v[92:95], v[96:99], v[160:167], v106\n\t" \
  "v_cvt_pk_bf16_f32 v70, v82, v83\n\t" \
  "v_smfmac_f32_16x16x64_bf16 v[88:91], v[52:55], v[168:175], v106\n\t" \
  "v_cvt_pk_bf16_f32 v69, v84, v85\n\t" \
  "v_smfmac_f32_16x16x64_bf16 v[92:95], v[52:55], v[176:183], v106\n\t" \
  "v_cvt_pk_bf16_f32 v71, v86, v87\n\t" \
  "v_smfmac_f32_16x16x64_bf16 v[88:91], v[56:59], v[184:191], v106\n\t" \
  "ds_read_b128 v[16:19], v103 offset:49152\n\t" \
  "buffer_store_dwordx4 v[68:71], v108, %[ru], s76 offen sc1\n\t" \
  "v_smfmac_f32_16x16x64_bf16 v[92:95], v[56:59], v[192:199], v106\n\t" \
  "ds_read_b128 v[20:23], v103 offset:50176\n\t" \
  "s_waitcnt lgkmcnt(4)\n\t" \
  "v_smfmac_f32_16x16x64_bf16 v[88:91], v[60:63], v[0:7], v106\n\t" \
  "ds_read_b128 v[24:27], v103 offset:51200\n\t" \
  "s_waitcnt lgkmcnt(3)\n\t" \
  "v_smfmac_f32_16x16x64_bf16 v[92:95], v[60:63], v[8:15], v106\n\t" \
  "ds_read_b128 v[28:31], v103 offset:52224\n\t" \
  "v_smfmac_f32_16x16x64_bf16 v[32:35], v[96:99], v[200:207], v106\n\t" \
  "v_cvt_pk_bf16_f32 v72, v88, v89\n\t" \
  "v_smfmac_f32_16x16x64_bf16 v[36:39], v[96:99], v[208:215], v106\n\t" \
  "v_cvt_pk_bf16_f32 v74, v90, v91\n\t" \
  "v_smfmac_f32_16x16x64_bf16 v[32:35], v[52:55], v[216:223], v106\n\t" \
  "v_cvt_pk_bf16_f32 v73, v92, v93\n\t" \
  "v_smfmac_f32_16x16x64_bf16 v[36:39], v[52:55], v[224:231], v106\n\t" \
  "v_cvt_pk_bf16_f32 v75, v94, v95\n\t" \
  "v_smfmac_f32_16x16x64_bf16 v[32:35], v[56:59], v[232:239], v106\n\t" \
  "buffer_store_dwordx4 v[72:75], v108, %[ru], s77 offen sc1\n\t" \
  "s_waitcnt lgkmcnt(2)\n\t" \
  "v_smfmac_f32_16x16x64_bf16 v[32:35], v[60:63], v[16:23], v106\n\t" \
  "v_smfmac_f32_16x16x64_bf16 v[36:39], v[56:59], v[240:247], v106\n\t" \
  "s_waitcnt lgkmcnt(0)\n\t" \
  "v_smfmac_f32_16x16x64_bf16 v[36:39], v[60:63], v[24:31], v106\n\t" \
  "v_cvt_pk_bf16_f32 v76, v32, v33\n\t" \
  "v_cvt_pk_bf16_f32 v78, v34, v35\n\t" \
  "s_nop 5\n\t" \
  "v_cvt_pk_bf16_f32 v77, v36, v37\n\t" \
  "v_cvt_pk_bf16_f32 v79, v38, v39\n\t" \
  "buffer_store_dwordx4 v[76:79], v108, %[ru], s78 offen sc1\n\t" \
  "s_memtime s[76:77]\n\t" \
  "s_mov_b32 s42, 0\n\t" \
  "s_waitcnt lgkmcnt(0)\n\t" \
  "s_mov_b32 s57, s76\n\t" \
  "s_add_i32 s41, s56, 1950\n\t" \
  "s_sub_i32 s76, s92, s51\n\t" \
  "s_sub_i32 s77, s93, s53\n\t" \
  "s_cmp_eq_u32 s76, 0\n\t" \
  "s_cbranch_scc1 21f\n\t" \
  "v_cvt_f32_u32 v117, s76\n\t" \
  "v_cvt_f32_u32 v116, s77\n\t" \
  "v_rcp_f32 v117, v117\n\t" \
  "s_mov_b32 s51, s92\n\t" \
  "v_mul_f32 v117, v117, v116\n\t" \
  "v_max_f32 v117, 0x3f028f5c, v117\n\t" \
  "v_min_f32 v117, 0x3ffeb852, v117\n\t" \
  "v_rcp_f32 v116, v117\n\t" \
  "v_mul_f32 v117, 0x4f000000, v117\n\t" \
  "v_mul_f32 v116, 0x47800000, v116\n\t" \
  "v_cvt_u32_f32 v117, v117\n\t" \
  "v_cvt_u32_f32 v116, v116\n\t" \
  "s_mov_b32 s53, s93\n\t" \
  "v_readfirstlane_b32 s46, v117\n\t" \
  "v_readfirstlane_b32 s52, v116\n\t" \
  "21:\n\t" \
  "s_add_i32 s44, s44, 1\n\t" \
  "s_and_b32 s58, s58, 4\n\t" \
  "s_and_b32 s76, s44, 64\n\t" \
  "s_or_b32 s42, s42, s76\n\t" \
  "s_cbranch_scc0 20b\n\t" \
  "s_branch 99f\n\t" \
  "14:\n\t" \
  "s_add_u32 s59, s59, 0x10000\n\t" \
  "s_cmp_lt_u32 s59, 0x100000\n\t" \
  "s_cbranch_scc1 13b\n\t" \
  "s_mov_b64 exec, -1\n\t" \
  "s_mov_b32 s42, 16\n\t" \
  "s_branch 99f\n\t" \
  "8:\n\t" \
  "s_bitcmp1_b32 s58, 1\n\t" \
  "s_cbranch_scc1 4b\n\t" \
  "s_bitcmp1_b32 s58, 2\n\t" \
  "s_cbranch_scc1 5b\n\t" \
  "v_max3_u32 v116, v32, v33, v34\n\t" \
  "v_max3_u32 v117, v35, v36, v37\n\t" \
  "v_max3_u32 v116, v116, v38, v39\n\t" \
  "v_max_u32 v116, v116, v117\n\t" \
  "v_cmp_eq_u32 vcc, -1, v116\n\t" \
  "s_cbranch_vccz 5b\n\t" \
  "s_branch 0f\n\t" \
  "9:\n\t" \
  "s_bitcmp1_b32 s58, 2\n\t" \
  "s_cbranch_scc1 6b\n\t" \
  "v_max3_u32 v116, v40, v41, v42\n\t" \
  "v_max3_u32 v117, v43, v44, v45\n\t" \
  "v_max3_u32 v116, v116, v46, v47\n\t" \
  "v_max_u32 v116, v116, v117\n\t" \
  "v_cmp_eq_u32 vcc, -1, v116\n\t" \
  "s_cbranch_vccz 6b\n\t" \
  "s_or_b32 s58, s58, 2\n\t" \
  "0:\n\t" \
  "s_or_b32 s58, s58, 1\n\t" \
  "11:\n\t" \
  "buffer_load_dwordx4 v[32:35], v100, %[rh], s40 offen sc1\n\t" \
  "buffer_load_dwordx4 v[36:39], v100, %[rh], s76 offen sc1\n\t" \
  "buffer_load_dwordx4 v[40:43], v100, %[rh], s77 offen sc1\n\t" \
  "buffer_load_dwordx4 v[44:47], v100, %[rh], s78 offen sc1\n\t" \
  "s_waitcnt lgkmcnt(0)\n\t" \
  "s_branch 10b\n\t" \
  "17:\n\t" \
  "v_mov_b64 v[72:73], 0\n\t" \
  "v_mov_b64 v[74:75], 0\n\t" \
  "v_mov_b64 v[76:77], 0\n\t" \
  "v_mov_b64 v[78:79], 0\n\t" \
  "s_add_u32 s59, s59, 1\n\t" \
  "s_bitcmp1_b32 s58, 2\n\t" \
  "s_cbranch_scc1 3b\n\t" \
  "s_waitcnt vmcnt(0)\n\t" \
  "v_max_u32 v116, v32, v33\n\t" \
  "v_max3_u32 v116, v116, v34, v35\n\t" \
  "v_max3_u32 v116, v116, v36, v37\n\t" \
  "v_max3_u32 v116, v116, v38, v39\n\t" \
  "v_max3_u32 v116, v116, v40, v41\n\t" \
  "v_max3_u32 v116, v116, v42, v43\n\t" \
  "v_max3_u32 v116, v116, v44, v45\n\t" \
  "v_max3_u32 v116, v116, v46, v47\n\t" \
  "v_cmp_eq_u32 vcc, -1, v116\n\t" \
  "s_cbranch_vccz 3b\n\t" \
  "s_bitcmp1_b32 s59, 6\n\t" \
  "s_cbranch_scc1 12f\n\t" \
  "buffer_load_dwordx4 v[32:35], v100, %[rh], s40 offen sc1\n\t" \
  "buffer_load_dwordx4 v[36:39], v100, %[rh], s76 offen sc1\n\t" \
  "buffer_load_dwordx4 v[40:43], v100, %[rh], s77 offen sc1\n\t" \
  "buffer_load_dwordx4 v[44:47], v100, %[rh], s78 offen sc1\n\t" \
  "s_branch 17b\n\t" \
  "12:\n\t" \
  "s_mov_b32 s42, s58\n\t" \
  "s_branch 99f\n\t" \
  "91:\n\t" \
  "s_mov_b32 s58, s42\n\t" \
  ".p2align 6\n\t" \
  "20:\n\t" \
  "s_and_b32 s76, s44, 7\n\t" \
  "s_lshl_b32 s76, s76, 15\n\t" \
  "s_add_i32 s40, s54, s76\n\t" \
  "s_and_b32 s76, s44, 3\n\t" \
  "s_lshl_b32 s45, s76, 16\n\t" \
  "s_and_b32 s47, s44, 3\n\t" \
  "s_lshl_b32 s76, s47, 2\n\t" \
  "s_lshl_b32 s47, s47, 20\n\t" \
  "s_add_i32 s47, s47, s55\n\t" \
  "s_lshr_b32 s76, %[um], s76\n\t" \
  "s_and_b32 s76, s76, 7\n\t" \
  "s_lshl_b32 s76, s76, 12\n\t" \
  "s_xor_b32 s47, s47, s76\n\t" \
  "s_add_i32 s49, s44, -1\n\t" \
  "s_and_b32 s48, s49, 3\n\t" \
  "s_lshl_b32 s48, s48, 16\n\t" \
  "s_lshl_b32 s50, s53, 8\n\t" \
  "s_mov_b32 s59, 0\n\t" \
  "s_mov_b32 s92, s51\n\t" \
  "s_mov_b32 s93, s53\n\t" \
  "s_xor_b32 s76, s40, 0x1000\n\t" \
  "s_xor_b32 s77, s40, 0x4000\n\t" \
  "s_xor_b32 s78, s40, 0x5000\n\t" \
  "s_cmp_lg_u32 s58, 0\n\t" \
  "s_cbranch_scc1 11f\n\t" \
  "10:\n\t" \
  "ds_read_b128 v[48:51], v102 offset:16384\n\t" \
  "ds_read_b128 v[52:55], v102 offset:17408\n\t" \
  "ds_read_b128 v[56:59], v102 offset:18432\n\t" \
  "ds_read_b128 v[60:63], v102 offset:19456\n\t" \
  "ds_read_b128 v[0:3], v102 offset:32768\n\t" \
  "ds_read_b128 v[4:7], v102 offset:33792\n\t" \
  "ds_read_b128 v[8:11], v102 offset:34816\n\t" \
  "ds_read_b128 v[12:15], v102 offset:35840\n\t" \
  "ds_read_b128 v[16:19], v102 offset:49152\n\t" \
  "ds_read_b128 v[20:23], v102 offset:50176\n\t" \
  "ds_read_b128 v[24:27], v102 offset:51200\n\t" \
  "ds_read_b128 v[28:31], v102 offset:52224\n\t" \
  "s_waitcnt lgkmcnt(4)\n\t" \
  "ds_read_b128 v[80:83], v102 offset:0\n\t" \
  "ds_read_b128 v[84:87], v102 offset:1024\n\t" \
  "ds_read_b128 v[88:91], v102 offset:2048\n\t" \
  "ds_read_b128 v[92:95], v102 offset:3072\n\t" \
  "s_bitcmp1_b32 s58, 1\n\t" \
  "s_cselect_b64 s[80:81], -1, 0\n\t" \
  "s_cmp_lg_u32 s58, 0\n\t" \
  "s_cbranch_scc1 17f\n\t" \
  "s_mov_b32 exec_hi, 0\n\t" \
  "16:\n\t" \
  "s_memtime vcc\n\t" \
  "s_waitcnt lgkmcnt(0)\n\t" \
  "s_sub_i32 s79, s41, vcc_lo\n\t" \
  "s_cmp_gt_i32 s79, 64\n\t" \
  "s_cbranch_scc1 16b\n\t" \
  "13:\n\t" \
  "buffer_load_dword v116, v110, %[rh], s48 offen sc1\n\t" \
  "s_waitcnt vmcnt(0)\n\t" \
  "15:\n\t" \
  "v_mov_b32 v117, 20000\n\t" \
  "v_cmp_ne_u32_sdwa vcc, v116, s49 src0_sel:BYTE_0 src1_sel:DWORD\n\t" \
  "v_subrev_u32 v116, s50, v116\n\t" \
  "v_ashrrev_i32 v116, 8, v116\n\t" \
  "s_nop 1\n\t" \
  "v_max_i32_dpp v116, v116, v116 quad_perm:[1,0,3,2] row_mask:0xf bank_mask:0xf\n\t" \
  "s_cbranch_vccnz 14f\n\t" \
  "s_nop 0\n\t" \
  "v_max_i32_dpp v116, v116, v116 quad_perm:[2,3,0,1] row_mask:0xf bank_mask:0xf\n\t" \
  "s_nop 1\n\t" \
  "v_max_i32_dpp v116, v116, v116 row_half_mirror row_mask:0xf bank_mask:0xf\n\t" \
  "s_nop 1\n\t" \
  "v_max_i32_dpp v116, v116, v116 row_mirror row_mask:0xf bank_mask:0xf\n\t" \
  "v_med3_i32 v116, v116, 0, v117\n\t" \
  "v_mul_u32_u24 v116, s52, v116\n\t" \
  "v_lshrrev_b32 v116, 16, v116\n\t" \
  "s_mov_b64 exec, -1\n\t" \
  "v_readlane_b32 s84, v116, 0\n\t" \
  "v_readlane_b32 s85, v116, 16\n\t" \
  "s_max_u32 s84, s84, s85\n\t" \
  "s_add_i32 s84, s84, s51\n\t" \
  "v_mov_b64 v[72:73], 0\n\t" \
  "v_mov_b64 v[74:75], 0\n\t" \
  "v_mov_b64 v[76:77], 0\n\t" \
  "v_mov_b64 v[78:79], 0\n\t" \
  "1:\n\t" \
  "s_memtime vcc\n\t" \
  "s_waitcnt lgkmcnt(0)\n\t" \
  "s_sub_i32 s79, s84, vcc_lo\n\t" \
  "s_cmp_gt_i32 s79, 128\n\t" \
  "s_cbranch_scc1 1b\n\t" \
  "2:\n\t" \
  "s_sub_i32 s79, s79, 33\n\t" \
  "s_cmp_gt_i32 s79, 56\n\t" \
  "s_cbranch_scc1 2b\n\t" \
  "buffer_load_dwordx4 v[32:35], v100, %[rh], s40 offen sc1\n\t" \
  "s_nop 12\n\t" \
  "buffer_load_dwordx4 v[36:39], v100, %[rh], s76 offen sc1\n\t" \
  "s_nop 12\n\t" \
  "buffer_load_dwordx4 v[40:43], v100, %[rh], s77 offen sc1\n\t" \
  "s_nop 12\n\t" \
  "buffer_load_dwordx4 v[44:47], v100, %[rh], s78 offen sc1\n\t" \
  "s_memtime vcc\n\t" \
  "s_memrealtime s[82:83]\n\t" \
  "s_nop 4\n\t" \
  "s_memrealtime s[84:85]\n\t" \
  "s_nop 4\n\t" \
  "s_memrealtime s[94:95]\n\t" \
  "s_nop 4\n\t" \
  "s_memrealtime s[86:87]\n\t" \
  "s_nop 4\n\t" \
  "s_memrealtime s[88:89]\n\t" \
  "s_nop 4\n\t" \
  "s_memrealtime s[90:91]\n\t" \
  "s_mov_b32 s92, 0\n\t" \
  "s_waitcnt lgkmcnt(0)\n\t" \
  "s_cmp_eq_u32 s82, s84\n\t" \
  "s_addc_u32 s92, s92, 0\n\t" \
  "s_cmp_eq_u32 s82, s94\n\t" \
  "s_addc_u32 s92, s92, 0\n\t" \
  "s_cmp_eq_u32 s82, s86\n\t" \
  "s_addc_u32 s92, s92, 0\n\t" \
  "s_cmp_eq_u32 s82, s88\n\t" \
  "s_addc_u32 s92, s92, 0\n\t" \
  "s_cmp_eq_u32 s82, s90\n\t" \
  "s_addc_u32 s92, s92, 0\n\t" \
  "s_mul_i32 s92, s92, 24\n\t" \
  "s_add_i32 s92, s92, vcc_lo\n\t" \
  "s_lshl4_add_u32 s93, s82, 64\n\t" \
  "3:\n\t" \
  "s_waitcnt lgkmcnt(0)\n\t" \
  "s_waitcnt vmcnt(3)\n\t" \
  "v_smfmac_f32_16x16x64_bf16 v[72:75], v[32:35], v[48:55], v104\n\t" \
  "ds_write_b128 v101, v[32:35] offset:4096\n\t" \
  "v_smfmac_f32_16x16x64_bf16 v[76:79], v[32:35], v[56:63], v104\n\t" \
  "s_waitcnt vmcnt(2)\n\t" \
  "ds_write_b128 v101, v[36:39] offset:5120\n\t" \
  "v_smfmac_f32_16x16x64_bf16 v[72:75], v[36:39], a[48:55], v104\n\t" \
  "v_smfmac_f32_16x16x64_bf16 v[76:79], v[36:39], a[56:63], v104\n\t" \
  "s_nop 3\n\t" \
  "v_cmp_u_f32 vcc, v72, v73\n\t" \
  "v_cmp_u_f32 s[82:83], v74, v75\n\t" \
  "s_waitcnt vmcnt(1)\n\t" \
  "v_smfmac_f32_16x16x64_bf16 v[72:75], v[40:43], a[64:71], v104\n\t" \
  "s_or_b64 vcc, vcc, s[82:83]\n\t" \
  "s_or_b64 vcc, vcc, s[80:81]\n\t" \
  "v_smfmac_f32_16x16x64_bf16 v[76:79], v[40:43], a[72:79], v104\n\t" \
  "s_waitcnt lgkmcnt(0)\n\t" \
  "s_cbranch_scc1 8f\n\t" \
  "5:\n\t" \
  "s_barrier\n\t" \
  "4:\n\t" \
  "s_memtime s[56:57]\n\t" \
  "ds_read_b128 v[48:51], v101 offset:8192\n\t" \
  "ds_read_b128 v[52:55], v101 offset:9216\n\t" \
  "ds_read_b128 v[56:59], v101 offset:13312\n\t" \
  "s_waitcnt vmcnt(0)\n\t" \
  "v_smfmac_f32_16x16x64_bf16 v[72:75], v[44:47], a[80:87], v104\n\t" \
  "ds_read_b128 v[60:63], v101 offset:12288\n\t" \
  "ds_read_b128 v[64:67], v101 offset:1024\n\t" \
  "v_smfmac_f32_16x16x64_bf16 v[76:79], v[44:47], a[88:95], v104\n\t" \
  "ds_read_b128 v[68:71], v101 offset:0\n\t" \
  "ds_write_b128 v101, v[40:43] offset:6144\n\t" \
  "s_nop 1\n\t" \
  "v_cmp_u_f32 vcc, v72, v73\n\t" \
  "v_cmp_u_f32 s[82:83], v74, v75\n\t" \
  "s_waitcnt lgkmcnt(6)\n\t" \
  "v_smfmac_f32_16x16x64_bf16 v[72:75], v[48:51], v[0:7], v104\n\t" \
  "s_or_b64 vcc, vcc, s[82:83]\n\t" \
  "v_smfmac_f32_16x16x64_bf16 v[76:79], v[48:51], v[8:15], v104\n\t" \
  "s_cbranch_scc1 9f\n\t" \
  "6:\n\t" \
  "s_waitcnt lgkmcnt(5)\n\t" \
  "v_smfmac_f32_16x16x64_bf16 v[72:75], v[52:55], a[96:103], v104\n\t" \
  "ds_write_b128 v101, v[44:47] offset:7168\n\t" \
  "v_smfmac_f32_16x16x64_bf16 v[76:79], v[52:55], a[104:111], v104\n\t" \
  "v_mov_b64 v[40:41], 0\n\t" \
  "s_waitcnt lgkmcnt(5)\n\t" \
  "v_smfmac_f32_16x16x64_bf16 v[72:75], v[56:59], a[144:151], v104\n\t" \
  "v_mov_b64 v[42:43], 0\n\t" \
  "v_smfmac_f32_16x16x64_bf16 v[76:79], v[56:59], a[152:159], v104\n\t" \
  "s_waitcnt lgkmcnt(4)\n\t" \
  "v_smfmac_f32_16x16x64_bf16 v[72:75], v[60:63], v[16:23], v104\n\t" \
  "s_waitcnt lgkmcnt(0)\n\t" \
  "s_barrier\n\t" \
  "v_smfmac_f32_16x16x64_bf16 v[76:79], v[60:63], v[24:31], v104\n\t" \
  "ds_read_b128 v[48:51], v101 offset:10240\n\t" \
  "ds_read_b128 v[52:55], v101 offset:14336\n\t" \
  "v_smfmac_f32_16x16x64_bf16 v[72:75], v[64:67], a[0:7], v104\n\t" \
  "ds_read_b128 v[56:59], v101 offset:2048\n\t" \
  "ds_read_b128 v[60:63], v101 offset:11264\n\t" \
  "v_smfmac_f32_16x16x64_bf16 v[76:79], v[64:67], a[8:15], v104\n\t" \
  "ds_read_b128 v[32:35], v101 offset:15360\n\t" \
  "ds_read_b128 v[36:39], v101 offset:3072\n\t" \
  "v_smfmac_f32_16x16x64_bf16 v[72:75], v[68:71], v[80:87], v104\n\t" \
  "ds_read_b128 v[0:3], v103 offset:0\n\t" \
  "v_mov_b64 v[44:45], 0\n\t" \
  "v_smfmac_f32_16x16x64_bf16 v[76:79], v[68:71], v[88:95], v104\n\t" \
  "v_mov_b64 v[46:47], 0\n\t" \
  "s_waitcnt lgkmcnt(5)\n\t" \
  "v_smfmac_f32_16x16x64_bf16 v[72:75], v[48:51], a[112:119], v104\n\t" \
  "ds_read_b128 v[4:7], v103 offset:1024\n\t" \
  "v_mov_b64 v[80:81], 0\n\t" \
  "v_smfmac_f32_16x16x64_bf16 v[72:75], v[52:55], a[160:167], v104\n\t" \
  "v_mov_b64 v[82:83], 0\n\t" \
  "s_waitcnt lgkmcnt(2)\n\t" \
  "v_smfmac_f32_16x16x64_bf16 v[72:75], v[56:59], a[16:23], v104\n\t" \
  "ds_read_b128 v[8:11], v103 offset:2048\n\t" \
  "v_mov_b64 v[84:85], 0\n\t" \
  "v_smfmac_f32_16x16x64_bf16 v[72:75], v[60:63], a[128:135], v104\n\t" \
  "ds_read_b128 v[12:15], v103 offset:3072\n\t" \
  "v_mov_b64 v[86:87], 0\n\t" \
  "v_smfmac_f32_16x16x64_bf16 v[72:75], v[32:35], a[176:183], v104\n\t" \
  "ds_read_b128 v[16:19], v103 offset:16384\n\t" \
  "v_mov_b64 v[88:89], 0\n\t" \
  "v_smfmac_f32_16x16x64_bf16 v[72:75], v[36:39], a[32:39], v104\n\t" \
  "ds_read_b128 v[20:23], v103 offset:17408\n\t" \
  "v_mov_b64 v[90:91], 0\n\t" \
  "v_smfmac_f32_16x16x64_bf16 v[76:79], v[48:51], a[120:127], v104\n\t" \
  "ds_read_b128 v[24:27], v103 offset:18432\n\t" \
  "v_mov_b64 v[92:93], 0\n\t" \
  "v_smfmac_f32_16x16x64_bf16 v[76:79], v[52:55], a[168:175], v104\n\t" \
  "v_add_f32 v96, v72, v74\n\t" \
  "v_add_f32 v97, v73, v75\n\t" \
  "v_cvt_pk_bf16_f32 v96, v96, v97\n\t" \
  "ds_write_b32 v105, v96\n\t" \
  "v_smfmac_f32_16x16x64_bf16 v[76:79], v[56:59], a[24:31], v104\n\t" \
  "ds_read_b128 v[28:31], v103 offset:19456\n\t" \
  "v_mov_b64 v[94:95], 0\n\t" \
  "v_smfmac_f32_16x16x64_bf16 v[76:79], v[60:63], a[136:143], v104\n\t" \
  "v_smfmac_f32_16x16x64_bf16 v[76:79], v[32:35], a[184:191], v104\n\t" \
  "v_smfmac_f32_16x16x64_bf16 v[76:79], v[36:39], a[40:47], v104\n\t" \
  "s_waitcnt lgkmcnt(1)\n\t" \
  "s_barrier\n\t" \
  "ds_read_b64_tr_b16 v[96:97], v107 offset:0\n\t" \
  "ds_read_b64_tr_b16 v[98:99], v107 offset:64\n\t" \
  "ds_read_b64_tr_b16 v[56:57], v107 offset:1024\n\t" \
  "ds_read_b64_tr_b16 v[58:59], v107 offset:1088\n\t" \
  "s_nop 1\n\t" \
  "v_add_f32 v116, v76, v78\n\t" \
  "v_add_f32 v117, v77, v79\n\t" \
  "v_cvt_pk_bf16_f32 v116, v116, v117\n\t" \
  "ds_write_b32 v105, v116 offset:256\n\t" \
  "s_waitcnt lgkmcnt(3)\n\t" \
  "v_smfmac_f32_16x16x64_bf16 v[40:43], v[96:99], a[192:199], v106\n\t" \
  "v_smfmac_f32_16x16x64_bf16 v[44:47], v[96:99], a[200:207], v106\n\t" \
  "s_waitcnt lgkmcnt(0)\n\t" \
  "s_barrier\n\t" \
  "ds_read_b64_tr_b16 v[52:53], v107 offset:256\n\t" \
  "ds_read_b64_tr_b16 v[54:55], v107 offset:320\n\t" \
  "v_smfmac_f32_16x16x64_bf16 v[40:43], v[56:59], a[224:231], v106\n\t" \
  "ds_read_b64_tr_b16 v[60:61], v107 offset:1280\n\t" \
  "ds_read_b64_tr_b16 v[62:63], v107 offset:1344\n\t" \
  "v_smfmac_f32_16x16x64_bf16 v[44:47], v[56:59], a[232:239], v106\n\t" \
  "v_smfmac_f32_16x16x64_bf16 v[80:83], v[96:99], a[240:247], v106\n\t" \
  "v_mov_b64 v[32:33], 0\n\t" \
  "v_smfmac_f32_16x16x64_bf16 v[84:87], v[96:99], a[248:255], v106\n\t" \
  "v_mov_b64 v[34:35], 0\n\t" \
  "v_smfmac_f32_16x16x64_bf16 v[80:83], v[56:59], v[136:143], v106\n\t" \
  "v_mov_b64 v[36:37], 0\n\t" \
  "v_smfmac_f32_16x16x64_bf16 v[84:87], v[56:59], v[144:151], v106\n\t" \
  "s_waitcnt lgkmcnt(2)\n\t" \
  "v_smfmac_f32_16x16x64_bf16 v[40:43], v[52:55], a[208:215], v106\n\t" \
  "v_smfmac_f32_16x16x64_bf16 v[44:47], v[52:55], a[216:223], v106\n\t" \
  "s_xor_b32 s76, s47, 0x40000\n\t" \
  "s_waitcnt lgkmcnt(0)\n\t" \
  "v_smfmac_f32_16x16x64_bf16 v[40:43], v[60:63], v[0:7], v106\n\t" \
  "v_mov_b64 v[38:39], 0\n\t" \
  "s_xor_b32 s77, s47, 0x84000\n\t" \
  "v_smfmac_f32_16x16x64_bf16 v[44:47], v[60:63], v[8:15], v106\n\t" \
  "s_xor_b32 s78, s47, 0xc4000\n\t" \
  "v_smfmac_f32_16x16x64_bf16 v[80:83], v[52:55], v[120:127], v106\n\t" \
  "ds_read_b128 v[0:3], v103 offset:32768\n\t" \
  "v_cvt_pk_bf16_f32 v64, v40, v41\n\t" \
  "v_smfmac_f32_16x16x64_bf16 v[84:87], v[52:55], v[128:135], v106\n\t" \
  "ds_read_b128 v[4:7], v103 offset:33792\n\t" \
  "v_cvt_pk_bf16_f32 v66, v42, v43\n\t" \
  "v_smfmac_f32_16x16x64_bf16 v[80:83], v[60:63], v[16:23], v106\n\t" \
  "ds_read_b128 v[8:11], v103 offset:34816\n\t" \
  "v_cvt_pk_bf16_f32 v65, v44, v45\n\t" \
  "v_smfmac_f32_16x16x64_bf16 v[84:87], v[60:63], v[24:31], v106\n\t" \
  "ds_read_b128 v[12:15], v103 offset:35840\n\t" \
  "v_cvt_pk_bf16_f32 v67, v46, v47\n\t" \
  "v_smfmac_f32_16x16x64_bf16 v[88:91], v[96:99], v[152:159], v106\n\t" \
  "v_cvt_pk_bf16_f32 v68, v80, v81\n\t" \
  "v_smfmac_f32_16x16x64_bf16 v[92:95], v[96:99], v[160:167], v106\n\t" \
  "buffer_store_dwordx4 v[64:67], v108, %[ru], s47 offen sc1\n\t" \
  "v_cvt_pk_bf16_f32 v70, v82, v83\n\t" \
  "v_smfmac_f32_16x16x64_bf16 v[88:91], v[52:55], v[168:175], v106\n\t" \
  "v_cvt_pk_bf16_f32 v69, v84, v85\n\t" \
  "v_smfmac_f32_16x16x64_bf16 v[92:95], v[52:55], v[176:183], v106\n\t" \
  "v_cvt_pk_bf16_f32 v71, v86, v87\n\t" \
  "v_smfmac_f32_16x16x64_bf16 v[88:91], v[56:59], v[184:191], v106\n\t" \
  "ds_read_b128 v[16:19], v103 offset:49152\n\t" \
  "v_smfmac_f32_16x16x64_bf16 v[92:95], v[56:59], v[192:199], v106\n\t" \
  "ds_read_b128 v[20:23], v103 offset:50176\n\t" \
  "buffer_store_dwordx4 v[68:71], v108, %[ru], s76 offen sc1\n\t" \
  "s_waitcnt lgkmcnt(4)\n\t" \
  "v_smfmac_f32_16x16x64_bf16 v[88:91], v[60:63], v[0:7], v106\n\t" \
  "ds_read_b128 v[24:27], v103 offset:51200\n\t" \
  "s_waitcnt lgkmcnt(3)\n\t" \
  "v_smfmac_f32_16x16x64_bf16 v[92:95], v[60:63], v[8:15], v106\n\t" \
  "ds_read_b128 v[28:31], v103 offset:52224\n\t" \
  "v_smfmac_f32_16x16x64_bf16 v[32:35], v[96:99], v[200:207], v106\n\t" \
  "v_cvt_pk_bf16_f32 v72, v88, v89\n\t" \
  "v_smfmac_f32_16x16x64_bf16 v[36:39], v[96:99], v[208:215], v106\n\t" \
  "v_cvt_pk_bf16_f32 v74, v90, v91\n\t" \
  "v_smfmac_f32_16x16x64_bf16 v[32:35], v[52:55], v[216:223], v106\n\t" \
  "v_cvt_pk_bf16_f32 v73, v92, v93\n\t" \
  "v_smfmac_f32_16x16x64_bf16 v[36:39], v[52:55], v[224:231], v106\n\t" \
  "v_cvt_pk_bf16_f32 v75, v94, v95\n\t" \
  "v_smfmac_f32_16x16x64_bf16 v[32:35], v[56:59], v[232:239], v106\n\t" \
  "s_waitcnt lgkmcnt(2)\n\t" \
  "v_smfmac_f32_16x16x64_bf16 v[32:35], v[60:63], v[16:23], v106\n\t" \
  "buffer_store_dwordx4 v[72:75], v108, %[ru], s77 offen sc1\n\t" \
  "v_smfmac_f32_16x16x64_bf16 v[36:39], v[56:59], v[240:247], v106\n\t" \
  "s_waitcnt lgkmcnt(0)\n\t" \
  "v_smfmac_f32_16x16x64_bf16 v[36:39], v[60:63], v[24:31], v106\n\t" \
  "v_cvt_pk_bf16_f32 v76, v32, v33\n\t" \
  "v_cvt_pk_bf16_f32 v78, v34, v35\n\t" \
  "s_nop 5\n\t" \
  "v_cvt_pk_bf16_f32 v77, v36, v37\n\t" \
  "v_cvt_pk_bf16_f32 v79, v38, v39\n\t" \
  "buffer_store_dwordx4 v[76:79], v108, %[ru], s78 offen sc1\n\t" \
  "s_memtime s[76:77]\n\t" \
  "s_mov_b32 s42, 0\n\t" \
  "s_waitcnt lgkmcnt(0)\n\t" \
  "s_mov_b32 s57, s76\n\t" \
  "s_add_i32 s41, s56, 1950\n\t" \
  "s_sub_i32 s76, s92, s51\n\t" \
  "s_sub_i32 s77, s93, s53\n\t" \
  "s_cmp_eq_u32 s76, 0\n\t" \
  "s_cbranch_scc1 21f\n\t" \
  "v_cvt_f32_u32 v117, s76\n\t" \
  "v_cvt_f32_u32 v116, s77\n\t" \
  "v_rcp_f32 v117, v117\n\t" \
  "s_mov_b32 s51, s92\n\t" \
  "v_mul_f32 v117, v117, v116\n\t" \
  "v_max_f32 v117, 0x3f028f5c, v117\n\t" \
  "v_min_f32 v117, 0x3ffeb852, v117\n\t" \
  "v_rcp_f32 v116, v117\n\t" \
  "v_mul_f32 v117, 0x4f000000, v117\n\t" \
  "v_mul_f32 v116, 0x47800000, v116\n\t" \
  "v_cvt_u32_f32 v117, v117\n\t" \
  "v_cvt_u32_f32 v116, v116\n\t" \
  "s_mov_b32 s53, s93\n\t" \
  "v_readfirstlane_b32 s46, v117\n\t" \
  "v_readfirstlane_b32 s52, v116\n\t" \
  "21:\n\t" \
  "s_add_i32 s44, s44, 1\n\t" \
  "s_and_b32 s58, s58, 4\n\t" \
  "s_and_b32 s76, s44, 64\n\t" \
  "s_or_b32 s42, s42, s76\n\t" \
  "s_cbranch_scc0 20b\n\t" \
  "s_branch 99f\n\t" \
  "14:\n\t" \
  "s_add_u32 s59, s59, 0x10000\n\t" \
  "s_cmp_lt_u32 s59, 0x100000\n\t" \
  "s_cbranch_scc1 13b\n\t" \
  "s_mov_b64 exec, -1\n\t" \
  "s_mov_b32 s42, 16\n\t" \
  "s_branch 99f\n\t" \
  "8:\n\t" \
  "s_bitcmp1_b32 s58, 1\n\t" \
  "s_cbranch_scc1 4b\n\t" \
  "s_bitcmp1_b32 s58, 2\n\t" \
  "s_cbranch_scc1 5b\n\t" \
  "v_max3_u32 v116, v32, v33, v34\n\t" \
  "v_max3_u32 v117, v35, v36, v37\n\t" \
  "v_max3_u32 v116, v116, v38, v39\n\t" \
  "v_max_u32 v116, v116, v117\n\t" \
  "v_cmp_eq_u32 vcc, -1, v116\n\t" \
  "s_cbranch_vccz 5b\n\t" \
  "s_branch 0f\n\t" \
  "9:\n\t" \
  "s_bitcmp1_b32 s58, 2\n\t" \
  "s_cbranch_scc1 6b\n\t" \
  "v_max3_u32 v116, v40, v41, v42\n\t" \
  "v_max3_u32 v117, v43, v44, v45\n\t" \
  "v_max3_u32 v116, v116, v46, v47\n\t" \
  "v_max_u32 v116, v116, v117\n\t" \
  "v_cmp_eq_u32 vcc, -1, v116\n\t" \
  "s_cbranch_vccz 6b\n\t" \
  "s_or_b32 s58, s58, 2\n\t" \
  "0:\n\t" \
  "s_or_b32 s58, s58, 1\n\t" \
  "11:\n\t" \
  "buffer_load_dwordx4 v[32:35], v100, %[rh], s40 offen sc1\n\t" \
  "buffer_load_dwordx4 v[36:39], v100, %[rh], s76 offen sc1\n\t" \
  "buffer_load_dwordx4 v[40:43], v100, %[rh], s77 offen sc1\n\t" \
  "buffer_load_dwordx4 v[44:47], v100, %[rh], s78 offen sc1\n\t" \
  "s_waitcnt lgkmcnt(0)\n\t" \
  "s_branch 10b\n\t" \
  "17:\n\t" \
  "v_mov_b64 v[72:73], 0\n\t" \
  "v_mov_b64 v[74:75], 0\n\t" \
  "v_mov_b64 v[76:77], 0\n\t" \
  "v_mov_b64 v[78:79], 0\n\t" \
  "s_add_u32 s59, s59, 1\n\t" \
  "s_bitcmp1_b32 s58, 2\n\t" \
  "s_cbranch_scc1 3b\n\t" \
  "s_waitcnt vmcnt(0)\n\t" \
  "v_max_u32 v116, v32, v33\n\t" \
  "v_max3_u32 v116, v116, v34, v35\n\t" \
  "v_max3_u32 v116, v116, v36, v37\n\t" \
  "v_max3_u32 v116, v116, v38, v39\n\t" \
  "v_max3_u32 v116, v116, v40, v41\n\t" \
  "v_max3_u32 v116, v116, v42, v43\n\t" \
  "v_max3_u32 v116, v116, v44, v45\n\t" \
  "v_max3_u32 v116, v116, v46, v47\n\t" \
  "v_cmp_eq_u32 vcc, -1, v116\n\t" \
  "s_cbranch_vccz 3b\n\t" \
  "s_bitcmp1_b32 s59, 6\n\t" \
  "s_cbranch_scc1 12f\n\t" \
  "buffer_load_dwordx4 v[32:35], v100, %[rh], s40 offen sc1\n\t" \
  "buffer_load_dwordx4 v[36:39], v100, %[rh], s76 offen sc1\n\t" \
  "buffer_load_dwordx4 v[40:43], v100, %[rh], s77 offen sc1\n\t" \
  "buffer_load_dwordx4 v[44:47], v100, %[rh], s78 offen sc1\n\t" \
  "s_branch 17b\n\t" \
  "12:\n\t" \
  "s_mov_b32 s42, s58\n\t" \
  "s_branch 99f\n\t" \
  "92:\n\t" \
  "s_mov_b32 s58, s42\n\t" \
  ".p2align 6\n\t" \
  "20:\n\t" \
  "s_and_b32 s76, s44, 7\n\t" \
  "s_lshl_b32 s76, s76, 15\n\t" \
  "s_add_i32 s40, s54, s76\n\t" \
  "s_and_b32 s76, s44, 3\n\t" \
  "s_lshl_b32 s45, s76, 16\n\t" \
  "s_and_b32 s47, s44, 3\n\t" \
  "s_lshl_b32 s76, s47, 2\n\t" \
  "s_lshl_b32 s47, s47, 20\n\t" \
  "s_add_i32 s47, s47, s55\n\t" \
  "s_lshr_b32 s76, %[um], s76\n\t" \
  "s_and_b32 s76, s76, 7\n\t" \
  "s_lshl_b32 s76, s76, 12\n\t" \
  "s_xor_b32 s47, s47, s76\n\t" \
  "s_add_i32 s49, s44, -1\n\t" \
  "s_and_b32 s48, s49, 3\n\t" \
  "s_lshl_b32 s48, s48, 16\n\t" \
  "s_lshl_b32 s50, s53, 8\n\t" \
  "s_mov_b32 s59, 0\n\t" \
  "s_mov_b32 s92, s51\n\t" \
  "s_mov_b32 s93, s53\n\t" \
  "s_xor_b32 s76, s40, 0x1000\n\t" \
  "s_xor_b32 s77, s40, 0x4000\n\t" \
  "s_xor_b32 s78, s40, 0x5000\n\t" \
  "s_cmp_lg_u32 s58, 0\n\t" \
  "s_cbranch_scc1 11f\n\t" \
  "10:\n\t" \
  "ds_read_b128 v[48:51], v102 offset:32768\n\t" \
  "ds_read_b128 v[52:55], v102 offset:33792\n\t" \
  "ds_read_b128 v[56:59], v102 offset:34816\n\t" \
  "ds_read_b128 v[60:63], v102 offset:35840\n\t" \
  "ds_read_b128 v[0:3], v102 offset:49152\n\t" \
  "ds_read_b128 v[4:7], v102 offset:50176\n\t" \
  "ds_read_b128 v[8:11], v102 offset:51200\n\t" \
  "ds_read_b128 v[12:15], v102 offset:52224\n\t" \
  "ds_read_b128 v[16:19], v102 offset:0\n\t" \
  "ds_read_b128 v[20:23], v102 offset:1024\n\t" \
  "ds_read_b128 v[24:27], v102 offset:2048\n\t" \
  "ds_read_b128 v[28:31], v102 offset:3072\n\t" \
  "s_waitcnt lgkmcnt(4)\n\t" \
  "ds_read_b128 v[80:83], v102 offset:16384\n\t" \
  "ds_read_b128 v[84:87], v102 offset:17408\n\t" \
  "ds_read_b128 v[88:91], v102 offset:18432\n\t" \
  "ds_read_b128 v[92:95], v102 offset:19456\n\t" \
  "s_bitcmp1_b32 s58, 1\n\t" \
  "s_cselect_b64 s[80:81], -1, 0\n\t" \
  "s_cmp_lg_u32 s58, 0\n\t" \
  "s_cbranch_scc1 17f\n\t" \
  "s_mov_b32 exec_hi, 0\n\t" \
  "16:\n\t" \
  "s_memtime vcc\n\t" \
  "s_waitcnt lgkmcnt(0)\n\t" \
  "s_sub_i32 s79, s41, vcc_lo\n\t" \
  "s_cmp_gt_i32 s79, 64\n\t" \
  "s_cbranch_scc1 16b\n\t" \
  "13:\n\t" \
  "buffer_load_dword v116, v110, %[rh], s48 offen sc1\n\t" \
  "s_waitcnt vmcnt(0)\n\t" \
  "15:\n\t" \
  "v_mov_b32 v117, 20000\n\t" \
  "v_cmp_ne_u32_sdwa vcc, v116, s49 src0_sel:BYTE_0 src1_sel:DWORD\n\t" \
  "v_subrev_u32 v116, s50, v116\n\t" \
  "v_ashrrev_i32 v116, 8, v116\n\t" \
  "s_nop 1\n\t" \
  "v_max_i32_dpp v116, v116, v116 quad_perm:[1,0,3,2] row_mask:0xf bank_mask:0xf\n\t" \
  "s_cbranch_vccnz 14f\n\t" \
  "s_nop 0\n\t" \
  "v_max_i32_dpp v116, v116, v116 quad_perm:[2,3,0,1] row_mask:0xf bank_mask:0xf\n\t" \
  "s_nop 1\n\t" \
  "v_max_i32_dpp v116, v116, v116 row_half_mirror row_mask:0xf bank_mask:0xf\n\t" \
  "s_nop 1\n\t" \
  "v_max_i32_dpp v116, v116, v116 row_mirror row_mask:0xf bank_mask:0xf\n\t" \
  "v_med3_i32 v116, v116, 0, v117\n\t" \
  "v_mul_u32_u24 v116, s52, v116\n\t" \
  "v_lshrrev_b32 v116, 16, v116\n\t" \
  "s_mov_b64 exec, -1\n\t" \
  "v_readlane_b32 s84, v116, 0\n\t" \
  "v_readlane_b32 s85, v116, 16\n\t" \
  "s_max_u32 s84, s84, s85\n\t" \
  "s_add_i32 s84, s84, s51\n\t" \
  "v_mov_b64 v[72:73], 0\n\t" \
  "v_mov_b64 v[74:75], 0\n\t" \
  "v_mov_b64 v[76:77], 0\n\t" \
  "v_mov_b64 v[78:79], 0\n\t" \
  "1:\n\t" \
  "s_memtime vcc\n\t" \
  "s_waitcnt lgkmcnt(0)\n\t" \
  "s_sub_i32 s79, s84, vcc_lo\n\t" \
  "s_cmp_gt_i32 s79, 128\n\t" \
  "s_cbranch_scc1 1b\n\t" \
  "2:\n\t" \
  "s_sub_i32 s79, s79, 33\n\t" \
  "s_cmp_gt_i32 s79, 56\n\t" \
  "s_cbranch_scc1 2b\n\t" \
  "buffer_load_dwordx4 v[32:35], v100, %[rh], s40 offen sc1\n\t" \
  "s_nop 12\n\t" \
  "buffer_load_dwordx4 v[36:39], v100, %[rh], s76 offen sc1\n\t" \
  "s_nop 12\n\t" \
  "buffer_load_dwordx4 v[40:43], v100, %[rh], s77 offen sc1\n\t" \
  "s_nop 12\n\t" \
  "buffer_load_dwordx4 v[44:47], v100, %[rh], s78 offen sc1\n\t" \
  "s_memtime vcc\n\t" \
  "s_memrealtime s[82:83]\n\t" \
  "s_nop 4\n\t" \
  "s_memrealtime s[84:85]\n\t" \
  "s_nop 4\n\t" \
  "s_memrealtime s[94:95]\n\t" \
  "s_nop 4\n\t" \
  "s_memrealtime s[86:87]\n\t" \
  "s_nop 4\n\t" \
  "s_memrealtime s[88:89]\n\t" \
  "s_nop 4\n\t" \
  "s_memrealtime s[90:91]\n\t" \
  "s_mov_b32 s92, 0\n\t" \
  "s_waitcnt lgkmcnt(0)\n\t" \
  "s_cmp_eq_u32 s82, s84\n\t" \
  "s_addc_u32 s92, s92, 0\n\t" \
  "s_cmp_eq_u32 s82, s94\n\t" \
  "s_addc_u32 s92, s92, 0\n\t" \
  "s_cmp_eq_u32 s82, s86\n\t" \
  "s_addc_u32 s92, s92, 0\n\t" \
  "s_cmp_eq_u32 s82, s88\n\t" \
  "s_addc_u32 s92, s92, 0\n\t" \
  "s_cmp_eq_u32 s82, s90\n\t" \
  "s_addc_u32 s92, s92, 0\n\t" \
  "s_mul_i32 s92, s92, 24\n\t" \
  "s_add_i32 s92, s92, vcc_lo\n\t" \
  "s_lshl4_add_u32 s93, s82, 64\n\t" \
  "3:\n\t" \
  "s_waitcnt lgkmcnt(0)\n\t" \
  "s_waitcnt vmcnt(3)\n\t" \
  "v_smfmac_f32_16x16x64_bf16 v[72:75], v[32:35], v[48:55], v104\n\t" \
  "ds_write_b128 v101, v[32:35] offset:8192\n\t" \
  "v_smfmac_f32_16x16x64_bf16 v[76:79], v[32:35], v[56:63], v104\n\t" \
  "s_waitcnt vmcnt(2)\n\t" \
  "ds_write_b128 v101, v[36:39] offset:9216\n\t" \
  "v_smfmac_f32_16x16x64_bf16 v[72:75], v[36:39], a[96:103], v104\n\t" \
  "v_smfmac_f32_16x16x64_bf16 v[76:79], v[36:39], a[104:111], v104\n\t" \
  "s_nop 3\n\t" \
  "v_cmp_u_f32 vcc, v72, v73\n\t" \
  "v_cmp_u_f32 s[82:83], v74, v75\n\t" \
  "s_waitcnt vmcnt(1)\n\t" \
  "v_smfmac_f32_16x16x64_bf16 v[72:75], v[40:43], a[112:119], v104\n\t" \
  "s_or_b64 vcc, vcc, s[82:83]\n\t" \
  "s_or_b64 vcc, vcc, s[80:81]\n\t" \
  "v_smfmac_f32_16x16x64_bf16 v[76:79], v[40:43], a[120:127], v104\n\t" \
  "s_waitcnt lgkmcnt(0)\n\t" \
  "s_cbranch_scc1 8f\n\t" \
  "5:\n\t" \
  "s_barrier\n\t" \
  "4:\n\t" \
  "s_memtime s[56:57]\n\t" \
  "ds_read_b128 v[48:51], v101 offset:12288\n\t" \
  "ds_read_b128 v[52:55], v101 offset:13312\n\t" \
  "ds_read_b128 v[56:59], v101 offset:1024\n\t" \
  "s_waitcnt vmcnt(0)\n\t" \
  "v_smfmac_f32_16x16x64_bf16 v[72:75], v[44:47], a[128:135], v104\n\t" \
  "ds_read_b128 v[60:63], v101 offset:0\n\t" \
  "ds_read_b128 v[64:67], v101 offset:5120\n\t" \
  "v_smfmac_f32_16x16x64_bf16 v[76:79], v[44:47], a[136:143], v104\n\t" \
  "ds_read_b128 v[68:71], v101 offset:4096\n\t" \
  "ds_write_b128 v101, v[40:43] offset:10240\n\t" \
  "s_nop 1\n\t" \
  "v_cmp_u_f32 vcc, v72, v73\n\t" \
  "v_cmp_u_f32 s[82:83], v74, v75\n\t" \
  "s_waitcnt lgkmcnt(6)\n\t" \
  "v_smfmac_f32_16x16x64_bf16 v[72:75], v[48:51], v[0:7], v104\n\t" \
  "s_or_b64 vcc, vcc, s[82:83]\n\t" \
  "v_smfmac_f32_16x16x64_bf16 v[76:79], v[48:51], v[8:15], v104\n\t" \
  "s_cbranch_scc1 9f\n\t" \
  "6:\n\t" \
  "s_waitcnt lgkmcnt(5)\n\t" \
  "v_smfmac_f32_16x16x64_bf16 v[72:75], v[52:55], a[144:151], v104\n\t" \
  "ds_write_b128 v101, v[44:47] offset:11264\n\t" \
  "v_smfmac_f32_16x16x64_bf16 v[76:79], v[52:55], a[152:159], v104\n\t" \
  "v_mov_b64 v[40:41], 0\n\t" \
  "s_waitcnt lgkmcnt(5)\n\t" \
  "v_smfmac_f32_16x16x64_bf16 v[72:75], v[56:59], a[0:7], v104\n\t" \
  "v_mov_b64 v[42:43], 0\n\t" \
  "v_smfmac_f32_16x16x64_bf16 v[76:79], v[56:59], a[8:15], v104\n\t" \
  "s_waitcnt lgkmcnt(4)\n\t" \
  "v_smfmac_f32_16x16x64_bf16 v[72:75], v[60:63], v[16:23], v104\n\t" \
  "s_waitcnt lgkmcnt(0)\n\t" \
  "s_barrier\n\t" \
  "v_smfmac_f32_16x16x64_bf16 v[76:79], v[60:63], v[24:31], v104\n\t" \
  "ds_read_b128 v[48:51], v101 offset:14336\n\t" \
  "ds_read_b128 v[52:55], v101 offset:2048\n\t" \
  "v_smfmac_f32_16x16x64_bf16 v[72:75], v[64:67], a[48:55], v104\n\t" \
  "ds_read_b128 v[56:59], v101 offset:6144\n\t" \
  "ds_read_b128 v[60:63], v101 offset:15360\n\t" \
  "v_smfmac_f32_16x16x64_bf16 v[76:79], v[64:67], a[56:63], v104\n\t" \
  "ds_read_b128 v[32:35], v101 offset:3072\n\t" \
  "ds_read_b128 v[36:39], v101 offset:7168\n\t" \
  "v_smfmac_f32_16x16x64_bf16 v[72:75], v[68:71], v[80:87], v104\n\t" \
  "ds_read_b128 v[0:3], v103 offset:0\n\t" \
  "v_mov_b64 v[44:45], 0\n\t" \
  "v_smfmac_f32_16x16x64_bf16 v[76:79], v[68:71], v[88:95], v104\n\t" \
  "v_mov_b64 v[46:47], 0\n\t" \
  "s_waitcnt lgkmcnt(5)\n\t" \
  "v_smfmac_f32_16x16x64_bf16 v[72:75], v[48:51], a[160:167], v104\n\t" \
  "ds_read_b128 v[4:7], v103 offset:1024\n\t" \
  "v_mov_b64 v[80:81], 0\n\t" \
  "v_smfmac_f32_16x16x64_bf16 v[72:75], v[52:55], a[16:23], v104\n\t" \
  "v_mov_b64 v[82:83], 0\n\t" \
  "s_waitcnt lgkmcnt(2)\n\t" \
  "v_smfmac_f32_16x16x64_bf16 v[72:75], v[56:59], a[64:71], v104\n\t" \
  "ds_read_b128 v[8:11], v103 offset:2048\n\t" \
  "v_mov_b64 v[84:85], 0\n\t" \
  "v_smfmac_f32_16x16x64_bf16 v[72:75], v[60:63], a[176:183], v104\n\t" \
  "ds_read_b128 v[12:15], v103 offset:3072\n\t" \
  "v_mov_b64 v[86:87], 0\n\t" \
  "v_smfmac_f32_16x16x64_bf16 v[72:75], v[32:35], a[32:39], v104\n\t" \
  "ds_read_b128 v[16:19], v103 offset:16384\n\t" \
  "v_mov_b64 v[88:89], 0\n\t" \
  "v_smfmac_f32_16x16x64_bf16 v[72:75], v[36:39], a[80:87], v104\n\t" \
  "ds_read_b128 v[20:23], v103 offset:17408\n\t" \
  "v_mov_b64 v[90:91], 0\n\t" \
  "v_smfmac_f32_16x16x64_bf16 v[76:79], v[48:51], a[168:175], v104\n\t" \
  "ds_read_b128 v[24:27], v103 offset:18432\n\t" \
  "v_mov_b64 v[92:93], 0\n\t" \
  "v_smfmac_f32_16x16x64_bf16 v[76:79], v[52:55], a[24:31], v104\n\t" \
  "v_add_f32 v96, v72, v74\n\t" \
  "v_add_f32 v97, v73, v75\n\t" \
  "v_cvt_pk_bf16_f32 v96, v96, v97\n\t" \
  "ds_write_b32 v105, v96\n\t" \
  "v_smfmac_f32_16x16x64_bf16 v[76:79], v[56:59], a[72:79], v104\n\t" \
  "ds_read_b128 v[28:31], v103 offset:19456\n\t" \
  "v_mov_b64 v[94:95], 0\n\t" \
  "v_smfmac_f32_16x16x64_bf16 v[76:79], v[60:63], a[184:191], v104\n\t" \
  "v_smfmac_f32_16x16x64_bf16 v[76:79], v[32:35], a[40:47], v104\n\t" \
  "v_smfmac_f32_16x16x64_bf16 v[76:79], v[36:39], a[88:95], v104\n\t" \
  "s_waitcnt lgkmcnt(1)\n\t" \
  "s_barrier\n\t" \
  "ds_read_b64_tr_b16 v[96:97], v107 offset:0\n\t" \
  "ds_read_b64_tr_b16 v[98:99], v107 offset:64\n\t" \
  "ds_read_b64_tr_b16 v[56:57], v107 offset:1024\n\t" \
  "ds_read_b64_tr_b16 v[58:59], v107 offset:1088\n\t" \
  "s_nop 1\n\t" \
  "v_add_f32 v116, v76, v78\n\t" \
  "v_add_f32 v117, v77, v79\n\t" \
  "v_cvt_pk_bf16_f32 v116, v116, v117\n\t" \
  "ds_write_b32 v105, v116 offset:256\n\t" \
  "s_waitcnt lgkmcnt(3)\n\t" \
  "v_smfmac_f32_16x16x64_bf16 v[40:43], v[96:99], a[192:199], v106\n\t" \
  "v_smfmac_f32_16x16x64_bf16 v[44:47], v[96:99], a[200:207], v106\n\t" \
  "s_waitcnt lgkmcnt(0)\n\t" \
  "s_barrier\n\t" \
  "ds_read_b64_tr_b16 v[52:53], v107 offset:256\n\t" \
  "ds_read_b64_tr_b16 v[54:55], v107 offset:320\n\t" \
  "v_smfmac_f32_16x16x64_bf16 v[40:43], v[56:59], a[224:231], v106\n\t" \
  "ds_read_b64_tr_b16 v[60:61], v107 offset:1280\n\t" \
  "ds_read_b64_tr_b16 v[62:63], v107 offset:1344\n\t" \
  "v_smfmac_f32_16x16x64_bf16 v[44:47], v[56:59], a[232:239], v106\n\t" \
  "v_smfmac_f32_16x16x64_bf16 v[80:83], v[96:99], a[240:247], v106\n\t" \
  "v_mov_b64 v[32:33], 0\n\t" \
  "v_smfmac_f32_16x16x64_bf16 v[84:87], v[96:99], a[248:255], v106\n\t" \
  "v_mov_b64 v[34:35], 0\n\t" \
  "v_smfmac_f32_16x16x64_bf16 v[80:83], v[56:59], v[136:143], v106\n\t" \
  "v_mov_b64 v[36:37], 0\n\t" \
  "v_smfmac_f32_16x16x64_bf16 v[84:87], v[56:59], v[144:151], v106\n\t" \
  "s_waitcnt lgkmcnt(2)\n\t" \
  "v_smfmac_f32_16x16x64_bf16 v[40:43], v[52:55], a[208:215], v106\n\t" \
  "v_smfmac_f32_16x16x64_bf16 v[44:47], v[52:55], a[216:223], v106\n\t" \
  "s_xor_b32 s76, s47, 0x40000\n\t" \
  "s_waitcnt lgkmcnt(0)\n\t" \
  "v_smfmac_f32_16x16x64_bf16 v[40:43], v[60:63], v[0:7], v106\n\t" \
  "v_mov_b64 v[38:39], 0\n\t" \
  "s_xor_b32 s77, s47, 0x84000\n\t" \
  "v_smfmac_f32_16x16x64_bf16 v[44:47], v[60:63], v[8:15], v106\n\t" \
  "s_xor_b32 s78, s47, 0xc4000\n\t" \
  "v_smfmac_f32_16x16x64_bf16 v[80:83], v[52:55], v[120:127], v106\n\t" \
  "ds_read_b128 v[0:3], v103 offset:32768\n\t" \
  "v_cvt_pk_bf16_f32 v64, v40, v41\n\t" \
  "v_smfmac_f32_16x16x64_bf16 v[84:87], v[52:55], v[128:135], v106\n\t" \
  "ds_read_b128 v[4:7], v103 offset:33792\n\t" \
  "v_cvt_pk_bf16_f32 v66, v42, v43\n\t" \
  "v_smfmac_f32_16x16x64_bf16 v[80:83], v[60:63], v[16:23], v106\n\t" \
  "ds_read_b128 v[8:11], v103 offset:34816\n\t" \
  "v_cvt_pk_bf16_f32 v65, v44, v45\n\t" \
  "v_smfmac_f32_16x16x64_bf16 v[84:87], v[60:63], v[24:31], v106\n\t" \
  "ds_read_b128 v[12:15], v103 offset:35840\n\t" \
  "v_cvt_pk_bf16_f32 v67, v46, v47\n\t" \
  "v_smfmac_f32_16x16x64_bf16 v[88:91], v[96:99], v[152:159], v106\n\t" \
  "v_cvt_pk_bf16_f32 v68, v80, v81\n\t" \
  "v_smfmac_f32_16x16x64_bf16 v[92:95], v[96:99], v[160:167], v106\n\t" \
  "v_cvt_pk_bf16_f32 v70, v82, v83\n\t" \
  "v_smfmac_f32_16x16x64_bf16 v[88:91], v[52:55], v[168:175], v106\n\t" \
  "buffer_store_dwordx4 v[64:67], v108, %[ru], s47 offen sc1\n\t" \
  "v_cvt_pk_bf16_f32 v69, v84, v85\n\t" \
  "v_smfmac_f32_16x16x64_bf16 v[92:95], v[52:55], v[176:183], v106\n\t" \
  "v_cvt_pk_bf16_f32 v71, v86, v87\n\t" \
  "v_smfmac_f32_16x16x64_bf16 v[88:91], v[56:59], v[184:191], v106\n\t" \
  "ds_read_b128 v[16:19], v103 offset:49152\n\t" \
  "v_smfmac_f32_16x16x64_bf16 v[92:95], v[56:59], v[192:199], v106\n\t" \
  "ds_read_b128 v[20:23], v103 offset:50176\n\t" \
  "s_waitcnt lgkmcnt(4)\n\t" \
  "v_smfmac_f32_16x16x64_bf16 v[88:91], v[60:63], v[0:7], v106\n\t" \
  "ds_read_b128 v[24:27], v103 offset:51200\n\t" \
  "buffer_store_dwordx4 v[68:71], v108, %[ru], s76 offen sc1\n\t" \
  "s_waitcnt lgkmcnt(3)\n\t" \
  "v_smfmac_f32_16x16x64_bf16 v[92:95], v[60:63], v[8:15], v106\n\t" \
  "ds_read_b128 v[28:31], v103 offset:52224\n\t" \
  "v_smfmac_f32_16x16x64_bf16 v[32:35], v[96:99], v[200:207], v106\n\t" \
  "v_cvt_pk_bf16_f32 v72, v88, v89\n\t" \
  "v_smfmac_f32_16x16x64_bf16 v[36:39], v[96:99], v[208:215], v106\n\t" \
  "v_cvt_pk_bf16_f32 v74, v90, v91\n\t" \
  "v_smfmac_f32_16x16x64_bf16 v[32:35], v[52:55], v[216:223], v106\n\t" \
  "v_cvt_pk_bf16_f32 v73, v92, v93\n\t" \
  "v_smfmac_f32_16x16x64_bf16 v[36:39], v[52:55], v[224:231], v106\n\t" \
  "v_cvt_pk_bf16_f32 v75, v94, v95\n\t" \
  "v_smfmac_f32_16x16x64_bf16 v[32:35], v[56:59], v[232:239], v106\n\t" \
  "s_waitcnt lgkmcnt(2)\n\t" \
  "v_smfmac_f32_16x16x64_bf16 v[32:35], v[60:63], v[16:23], v106\n\t" \
  "v_smfmac_f32_16x16x64_bf16 v[36:39], v[56:59], v[240:247], v106\n\t" \
  "buffer_store_dwordx4 v[72:75], v108, %[ru], s77 offen sc1\n\t" \
  "s_waitcnt lgkmcnt(0)\n\t" \
  "v_smfmac_f32_16x16x64_bf16 v[36:39], v[60:63], v[24:31], v106\n\t" \
  "v_cvt_pk_bf16_f32 v76, v32, v33\n\t" \
  "v_cvt_pk_bf16_f32 v78, v34, v35\n\t" \
  "s_nop 5\n\t" \
  "v_cvt_pk_bf16_f32 v77, v36, v37\n\t" \
  "v_cvt_pk_bf16_f32 v79, v38, v39\n\t" \
  "buffer_store_dwordx4 v[76:79], v108, %[ru], s78 offen sc1\n\t" \
  "s_memtime s[76:77]\n\t" \
  "s_mov_b32 s42, 0\n\t" \
  "s_waitcnt lgkmcnt(0)\n\t" \
  "s_mov_b32 s57, s76\n\t" \
  "s_add_i32 s41, s56, 1950\n\t" \
  "s_sub_i32 s76, s92, s51\n\t" \
  "s_sub_i32 s77, s93, s53\n\t" \
  "s_cmp_eq_u32 s76, 0\n\t" \
  "s_cbranch_scc1 21f\n\t" \
  "v_cvt_f32_u32 v117, s76\n\t" \
  "v_cvt_f32_u32 v116, s77\n\t" \
  "v_rcp_f32 v117, v117\n\t" \
  "s_mov_b32 s51, s92\n\t" \
  "v_mul_f32 v117, v117, v116\n\t" \
  "v_max_f32 v117, 0x3f028f5c, v117\n\t" \
  "v_min_f32 v117, 0x3ffeb852, v117\n\t" \
  "v_rcp_f32 v116, v117\n\t" \
  "v_mul_f32 v117, 0x4f000000, v117\n\t" \
  "v_mul_f32 v116, 0x47800000, v116\n\t" \
  "v_cvt_u32_f32 v117, v117\n\t" \
  "v_cvt_u32_f32 v116, v116\n\t" \
  "s_mov_b32 s53, s93\n\t" \
  "v_readfirstlane_b32 s46, v117\n\t" \
  "v_readfirstlane_b32 s52, v116\n\t" \
  "21:\n\t" \
  "s_add_i32 s44, s44, 1\n\t" \
  "s_and_b32 s58, s58, 4\n\t" \
  "s_and_b32 s76, s44, 64\n\t" \
  "s_or_b32 s42, s42, s76\n\t" \
  "s_cbranch_scc0 20b\n\t" \
  "s_branch 99f\n\t" \
  "14:\n\t" \
  "s_add_u32 s59, s59, 0x10000\n\t" \
  "s_cmp_lt_u32 s59, 0x100000\n\t" \
  "s_cbranch_scc1 13b\n\t" \
  "s_mov_b64 exec, -1\n\t" \
  "s_mov_b32 s42, 16\n\t" \
  "s_branch 99f\n\t" \
  "8:\n\t" \
  "s_bitcmp1_b32 s58, 1\n\t" \
  "s_cbranch_scc1 4b\n\t" \
  "s_bitcmp1_b32 s58, 2\n\t" \
  "s_cbranch_scc1 5b\n\t" \
  "v_max3_u32 v116, v32, v33, v34\n\t" \
  "v_max3_u32 v117, v35, v36, v37\n\t" \
  "v_max3_u32 v116, v116, v38, v39\n\t" \
  "v_max_u32 v116, v116, v117\n\t" \
  "v_cmp_eq_u32 vcc, -1, v116\n\t" \
  "s_cbranch_vccz 5b\n\t" \
  "s_branch 0f\n\t" \
  "9:\n\t" \
  "s_bitcmp1_b32 s58, 2\n\t" \
  "s_cbranch_scc1 6b\n\t" \
  "v_max3_u32 v116, v40, v41, v42\n\t" \
  "v_max3_u32 v117, v43, v44, v45\n\t" \
  "v_max3_u32 v116, v116, v46, v47\n\t" \
  "v_max_u32 v116, v116, v117\n\t" \
  "v_cmp_eq_u32 vcc, -1, v116\n\t" \
  "s_cbranch_vccz 6b\n\t" \
  "s_or_b32 s58, s58, 2\n\t" \
  "0:\n\t" \
  "s_or_b32 s58, s58, 1\n\t" \
  "11:\n\t" \
  "buffer_load_dwordx4 v[32:35], v100, %[rh], s40 offen sc1\n\t" \
  "buffer_load_dwordx4 v[36:39], v100, %[rh], s76 offen sc1\n\t" \
  "buffer_load_dwordx4 v[40:43], v100, %[rh], s77 offen sc1\n\t" \
  "buffer_load_dwordx4 v[44:47], v100, %[rh], s78 offen sc1\n\t" \
  "s_waitcnt lgkmcnt(0)\n\t" \
  "s_branch 10b\n\t" \
  "17:\n\t" \
  "v_mov_b64 v[72:73], 0\n\t" \
  "v_mov_b64 v[74:75], 0\n\t" \
  "v_mov_b64 v[76:77], 0\n\t" \
  "v_mov_b64 v[78:79], 0\n\t" \
  "s_add_u32 s59, s59, 1\n\t" \
  "s_bitcmp1_b32 s58, 2\n\t" \
  "s_cbranch_scc1 3b\n\t" \
  "s_waitcnt vmcnt(0)\n\t" \
  "v_max_u32 v116, v32, v33\n\t" \
  "v_max3_u32 v116, v116, v34, v35\n\t" \
  "v_max3_u32 v116, v116, v36, v37\n\t" \
  "v_max3_u32 v116, v116, v38, v39\n\t" \
  "v_max3_u32 v116, v116, v40, v41\n\t" \
  "v_max3_u32 v116, v116, v42, v43\n\t" \
  "v_max3_u32 v116, v116, v44, v45\n\t" \
  "v_max3_u32 v116, v116, v46, v47\n\t" \
  "v_cmp_eq_u32 vcc, -1, v116\n\t" \
  "s_cbranch_vccz 3b\n\t" \
  "s_bitcmp1_b32 s59, 6\n\t" \
  "s_cbranch_scc1 12f\n\t" \
  "buffer_load_dwordx4 v[32:35], v100, %[rh], s40 offen sc1\n\t" \
  "buffer_load_dwordx4 v[36:39], v100, %[rh], s76 offen sc1\n\t" \
  "buffer_load_dwordx4 v[40:43], v100, %[rh], s77 offen sc1\n\t" \
  "buffer_load_dwordx4 v[44:47], v100, %[rh], s78 offen sc1\n\t" \
  "s_branch 17b\n\t" \
  "12:\n\t" \
  "s_mov_b32 s42, s58\n\t" \
  "s_branch 99f\n\t" \
  "93:\n\t" \
  "s_mov_b32 s58, s42\n\t" \
  ".p2align 6\n\t" \
  "20:\n\t" \
  "s_and_b32 s76, s44, 7\n\t" \
  "s_lshl_b32 s76, s76, 15\n\t" \
  "s_add_i32 s40, s54, s76\n\t" \
  "s_and_b32 s76, s44, 3\n\t" \
  "s_lshl_b32 s45, s76, 16\n\t" \
  "s_and_b32 s47, s44, 3\n\t" \
  "s_lshl_b32 s76, s47, 2\n\t" \
  "s_lshl_b32 s47, s47, 20\n\t" \
  "s_add_i32 s47, s47, s55\n\t" \
  "s_lshr_b32 s76, %[um], s76\n\t" \
  "s_and_b32 s76, s76, 7\n\t" \
  "s_lshl_b32 s76, s76, 12\n\t" \
  "s_xor_b32 s47, s47, s76\n\t" \
  "s_add_i32 s49, s44, -1\n\t" \
  "s_and_b32 s48, s49, 3\n\t" \
  "s_lshl_b32 s48, s48, 16\n\t" \
  "s_lshl_b32 s50, s53, 8\n\t" \
  "s_mov_b32 s59, 0\n\t" \
  "s_mov_b32 s92, s51\n\t" \
  "s_mov_b32 s93, s53\n\t" \
  "s_xor_b32 s76, s40, 0x1000\n\t" \
  "s_xor_b32 s77, s40, 0x4000\n\t" \
  "s_xor_b32 s78, s40, 0x5000\n\t" \
  "s_cmp_lg_u32 s58, 0\n\t" \
  "s_cbranch_scc1 11f\n\t" \
  "10:\n\t" \
  "ds_read_b128 v[48:51], v102 offset:49152\n\t" \
  "ds_read_b128 v[52:55], v102 offset:50176\n\t" \
  "ds_read_b128 v[56:59], v102 offset:51200\n\t" \
  "ds_read_b128 v[60:63], v102 offset:52224\n\t" \
  "ds_read_b128 v[0:3], v102 offset:0\n\t" \
  "ds_read_b128 v[4:7], v102 offset:1024\n\t" \
  "ds_read_b128 v[8:11], v102 offset:2048\n\t" \
  "ds_read_b128 v[12:15], v102 offset:3072\n\t" \
  "ds_read_b128 v[16:19], v102 offset:16384\n\t" \
  "ds_read_b128 v[20:23], v102 offset:17408\n\t" \
  "ds_read_b128 v[24:27], v102 offset:18432\n\t" \
  "ds_read_b128 v[28:31], v102 offset:19456\n\t" \
  "s_waitcnt lgkmcnt(4)\n\t" \
  "ds_read_b128 v[80:83], v102 offset:32768\n\t" \
  "ds_read_b128 v[84:87], v102 offset:33792\n\t" \
  "ds_read_b128 v[88:91], v102 offset:34816\n\t" \
  "ds_read_b128 v[92:95], v102 offset:35840\n\t" \
  "s_bitcmp1_b32 s58, 1\n\t" \
  "s_cselect_b64 s[80:81], -1, 0\n\t" \
  "s_cmp_lg_u32 s58, 0\n\t" \
  "s_cbranch_scc1 17f\n\t" \
  "s_mov_b32 exec_hi, 0\n\t" \
  "16:\n\t" \
  "s_memtime vcc\n\t" \
  "s_waitcnt lgkmcnt(0)\n\t" \
  "s_sub_i32 s79, s41, vcc_lo\n\t" \
  "s_cmp_gt_i32 s79, 64\n\t" \
  "s_cbranch_scc1 16b\n\t" \
  "13:\n\t" \
  "buffer_load_dword v116, v110, %[rh], s48 offen sc1\n\t" \
  "s_waitcnt vmcnt(0)\n\t" \
  "15:\n\t" \
  "v_mov_b32 v117, 20000\n\t" \
  "v_cmp_ne_u32_sdwa vcc, v116, s49 src0_sel:BYTE_0 src1_sel:DWORD\n\t" \
  "v_subrev_u32 v116, s50, v116\n\t" \
  "v_ashrrev_i32 v116, 8, v116\n\t" \
  "s_nop 1\n\t" \
  "v_max_i32_dpp v116, v116, v116 quad_perm:[1,0,3,2] row_mask:0xf bank_mask:0xf\n\t" \
  "s_cbranch_vccnz 14f\n\t" \
  "s_nop 0\n\t" \
  "v_max_i32_dpp v116, v116, v116 quad_perm:[2,3,0,1] row_mask:0xf bank_mask:0xf\n\t" \
  "s_nop 1\n\t" \
  "v_max_i32_dpp v116, v116, v116 row_half_mirror row_mask:0xf bank_mask:0xf\n\t" \
  "s_nop 1\n\t" \
  "v_max_i32_dpp v116, v116, v116 row_mirror row_mask:0xf bank_mask:0xf\n\t" \
  "v_med3_i32 v116, v116, 0, v117\n\t" \
  "v_mul_u32_u24 v116, s52, v116\n\t" \
  "v_lshrrev_b32 v116, 16, v116\n\t" \
  "s_mov_b64 exec, -1\n\t" \
  "v_readlane_b32 s84, v116, 0\n\t" \
  "v_readlane_b32 s85, v116, 16\n\t" \
  "s_max_u32 s84, s84, s85\n\t" \
  "s_add_i32 s84, s84, s51\n\t" \
  "v_mov_b64 v[72:73], 0\n\t" \
  "v_mov_b64 v[74:75], 0\n\t" \
  "v_mov_b64 v[76:77], 0\n\t" \
  "v_mov_b64 v[78:79], 0\n\t" \
  "1:\n\t" \
  "s_memtime vcc\n\t" \
  "s_waitcnt lgkmcnt(0)\n\t" \
  "s_sub_i32 s79, s84, vcc_lo\n\t" \
  "s_cmp_gt_i32 s79, 128\n\t" \
  "s_cbranch_scc1 1b\n\t" \
  "2:\n\t" \
  "s_sub_i32 s79, s79, 33\n\t" \
  "s_cmp_gt_i32 s79, 56\n\t" \
  "s_cbranch_scc1 2b\n\t" \
  "buffer_load_dwordx4 v[32:35], v100, %[rh], s40 offen sc1\n\t" \
  "s_nop 12\n\t" \
  "buffer_load_dwordx4 v[36:39], v100, %[rh], s76 offen sc1\n\t" \
  "s_nop 12\n\t" \
  "buffer_load_dwordx4 v[40:43], v100, %[rh], s77 offen sc1\n\t" \
  "s_nop 12\n\t" \
  "buffer_load_dwordx4 v[44:47], v100, %[rh], s78 offen sc1\n\t" \
  "s_memtime vcc\n\t" \
  "s_memrealtime s[82:83]\n\t" \
  "s_nop 4\n\t" \
  "s_memrealtime s[84:85]\n\t" \
  "s_nop 4\n\t" \
  "s_memrealtime s[94:95]\n\t" \
  "s_nop 4\n\t" \
  "s_memrealtime s[86:87]\n\t" \
  "s_nop 4\n\t" \
  "s_memrealtime s[88:89]\n\t" \
  "s_nop 4\n\t" \
  "s_memrealtime s[90:91]\n\t" \
  "s_mov_b32 s92, 0\n\t" \
  "s_waitcnt lgkmcnt(0)\n\t" \
  "s_cmp_eq_u32 s82, s84\n\t" \
  "s_addc_u32 s92, s92, 0\n\t" \
  "s_cmp_eq_u32 s82, s94\n\t" \
  "s_addc_u32 s92, s92, 0\n\t" \
  "s_cmp_eq_u32 s82, s86\n\t" \
  "s_addc_u32 s92, s92, 0\n\t" \
  "s_cmp_eq_u32 s82, s88\n\t" \
  "s_addc_u32 s92, s92, 0\n\t" \
  "s_cmp_eq_u32 s82, s90\n\t" \
  "s_addc_u32 s92, s92, 0\n\t" \
  "s_mul_i32 s92, s92, 24\n\t" \
  "s_add_i32 s92, s92, vcc_lo\n\t" \
  "s_lshl4_add_u32 s93, s82, 64\n\t" \
  "3:\n\t" \
  "s_waitcnt lgkmcnt(0)\n\t" \
  "s_waitcnt vmcnt(3)\n\t" \
  "v_smfmac_f32_16x16x64_bf16 v[72:75], v[32:35], v[48:55], v104\n\t" \
  "ds_write_b128 v101, v[32:35] offset:12288\n\t" \
  "v_smfmac_f32_16x16x64_bf16 v[76:79], v[32:35], v[56:63], v104\n\t" \
  "s_waitcnt vmcnt(2)\n\t" \
  "ds_write_b128 v101, v[36:39] offset:13312\n\t" \
  "v_smfmac_f32_16x16x64_bf16 v[72:75], v[36:39], a[144:151], v104\n\t" \
  "v_smfmac_f32_16x16x64_bf16 v[76:79], v[36:39], a[152:159], v104\n\t" \
  "s_nop 3\n\t" \
  "v_cmp_u_f32 vcc, v72, v73\n\t" \
  "v_cmp_u_f32 s[82:83], v74, v75\n\t" \
  "s_waitcnt vmcnt(1)\n\t" \
  "v_smfmac_f32_16x16x64_bf16 v[72:75], v[40:43], a[160:167], v104\n\t" \
  "s_or_b64 vcc, vcc, s[82:83]\n\t" \
  "s_or_b64 vcc, vcc, s[80:81]\n\t" \
  "v_smfmac_f32_16x16x64_bf16 v[76:79], v[40:43], a[168:175], v104\n\t" \
  "s_waitcnt lgkmcnt(0)\n\t" \
  "s_cbranch_scc1 8f\n\t" \
  "5:\n\t" \
  "s_barrier\n\t" \
  "4:\n\t" \
  "s_memtime s[56:57]\n\t" \
  "ds_read_b128 v[48:51], v101 offset:0\n\t" \
  "ds_read_b128 v[52:55], v101 offset:1024\n\t" \
  "ds_read_b128 v[56:59], v101 offset:5120\n\t" \
  "s_waitcnt vmcnt(0)\n\t" \
  "v_smfmac_f32_16x16x64_bf16 v[72:75], v[44:47], a[176:183], v104\n\t" \
  "ds_read_b128 v[60:63], v101 offset:4096\n\t" \
  "ds_read_b128 v[64:67], v101 offset:9216\n\t" \
  "v_smfmac_f32_16x16x64_bf16 v[76:79], v[44:47], a[184:191], v104\n\t" \
  "ds_read_b128 v[68:71], v101 offset:8192\n\t" \
  "ds_write_b128 v101, v[40:43] offset:14336\n\t" \
  "s_nop 1\n\t" \
  "v_cmp_u_f32 vcc, v72, v73\n\t" \
  "v_cmp_u_f32 s[82:83], v74, v75\n\t" \
  "s_waitcnt lgkmcnt(6)\n\t" \
  "v_smfmac_f32_16x16x64_bf16 v[72:75], v[48:51], v[0:7], v104\n\t" \
  "s_or_b64 vcc, vcc, s[82:83]\n\t" \
  "v_smfmac_f32_16x16x64_bf16 v[76:79], v[48:51], v[8:15], v104\n\t" \
  "s_cbranch_scc1 9f\n\t" \
  "6:\n\t" \
  "s_waitcnt lgkmcnt(5)\n\t" \
  "v_smfmac_f32_16x16x64_bf16 v[72:75], v[52:55], a[0:7], v104\n\t" \
  "ds_write_b128 v101, v[44:47] offset:15360\n\t" \
  "v_smfmac_f32_16x16x64_bf16 v[76:79], v[52:55], a[8:15], v104\n\t" \
  "v_mov_b64 v[40:41], 0\n\t" \
  "s_waitcnt lgkmcnt(5)\n\t" \
  "v_smfmac_f32_16x16x64_bf16 v[72:75], v[56:59], a[48:55], v104\n\t" \
  "v_mov_b64 v[42:43], 0\n\t" \
  "v_smfmac_f32_16x16x64_bf16 v[76:79], v[56:59], a[56:63], v104\n\t" \
  "s_waitcnt lgkmcnt(4)\n\t" \
  "v_smfmac_f32_16x16x64_bf16 v[72:75], v[60:63], v[16:23], v104\n\t" \
  "s_waitcnt lgkmcnt(0)\n\t" \
  "s_barrier\n\t" \
  "v_smfmac_f32_16x16x64_bf16 v[76:79], v[60:63], v[24:31], v104\n\t" \
  "ds_read_b128 v[48:51], v101 offset:2048\n\t" \
  "ds_read_b128 v[52:55], v101 offset:6144\n\t" \
  "v_smfmac_f32_16x16x64_bf16 v[72:75], v[64:67], a[96:103], v104\n\t" \
  "ds_read_b128 v[56:59], v101 offset:10240\n\t" \
  "ds_read_b128 v[60:63], v101 offset:3072\n\t" \
  "v_smfmac_f32_16x16x64_bf16 v[76:79], v[64:67], a[104:111], v104\n\t" \
  "ds_read_b128 v[32:35], v101 offset:7168\n\t" \
  "ds_read_b128 v[36:39], v101 offset:11264\n\t" \
  "v_smfmac_f32_16x16x64_bf16 v[72:75], v[68:71], v[80:87], v104\n\t" \
  "ds_read_b128 v[0:3], v103 offset:0\n\t" \
  "v_mov_b64 v[44:45], 0\n\t" \
  "v_smfmac_f32_16x16x64_bf16 v[76:79], v[68:71], v[88:95], v104\n\t" \
  "v_mov_b64 v[46:47], 0\n\t" \
  "s_waitcnt lgkmcnt(5)\n\t" \
  "v_smfmac_f32_16x16x64_bf16 v[72:75], v[48:51], a[16:23], v104\n\t" \
  "ds_read_b128 v[4:7], v103 offset:1024\n\t" \
  "v_mov_b64 v[80:81], 0\n\t" \
  "v_smfmac_f32_16x16x64_bf16 v[72:75], v[52:55], a[64:71], v104\n\t" \
  "v_mov_b64 v[82:83], 0\n\t" \
  "s_waitcnt lgkmcnt(2)\n\t" \
  "v_smfmac_f32_16x16x64_bf16 v[72:75], v[56:59], a[112:119], v104\n\t" \
  "ds_read_b128 v[8:11], v103 offset:2048\n\t" \
  "v_mov_b64 v[84:85], 0\n\t" \
  "v_smfmac_f32_16x16x64_bf16 v[72:75], v[60:63], a[32:39], v104\n\t" \
  "ds_read_b128 v[12:15], v103 offset:3072\n\t" \
  "v_mov_b64 v[86:87], 0\n\t" \
  "v_smfmac_f32_16x16x64_bf16 v[72:75], v[32:35], a[80:87], v104\n\t" \
  "ds_read_b128 v[16:19], v103 offset:16384\n\t" \
  "v_mov_b64 v[88:89], 0\n\t" \
  "v_smfmac_f32_16x16x64_bf16 v[72:75], v[36:39], a[128:135], v104\n\t" \
  "ds_read_b128 v[20:23], v103 offset:17408\n\t" \
  "v_mov_b64 v[90:91], 0\n\t" \
  "v_smfmac_f32_16x16x64_bf16 v[76:79], v[48:51], a[24:31], v104\n\t" \
  "ds_read_b128 v[24:27], v103 offset:18432\n\t" \
  "v_mov_b64 v[92:93], 0\n\t" \
  "v_smfmac_f32_16x16x64_bf16 v[76:79], v[52:55], a[72:79], v104\n\t" \
  "v_add_f32 v96, v72, v74\n\t" \
  "v_add_f32 v97, v73, v75\n\t" \
  "v_cvt_pk_bf16_f32 v96, v96, v97\n\t" \
  "ds_write_b32 v105, v96\n\t" \
  "v_smfmac_f32_16x16x64_bf16 v[76:79], v[56:59], a[120:127], v104\n\t" \
  "ds_read_b128 v[28:31], v103 offset:19456\n\t" \
  "v_mov_b64 v[94:95], 0\n\t" \
  "v_smfmac_f32_16x16x64_bf16 v[76:79], v[60:63], a[40:47], v104\n\t" \
  "v_smfmac_f32_16x16x64_bf16 v[76:79], v[32:35], a[88:95], v104\n\t" \
  "v_smfmac_f32_16x16x64_bf16 v[76:79], v[36:39], a[136:143], v104\n\t" \
  "s_waitcnt lgkmcnt(1)\n\t" \
  "s_barrier\n\t" \
  "ds_read_b64_tr_b16 v[96:97], v107 offset:0\n\t" \
  "ds_read_b64_tr_b16 v[98:99], v107 offset:64\n\t" \
  "ds_read_b64_tr_b16 v[56:57], v107 offset:1024\n\t" \
  "ds_read_b64_tr_b16 v[58:59], v107 offset:1088\n\t" \
  "s_nop 1\n\t" \
  "v_add_f32 v116, v76, v78\n\t" \
  "v_add_f32 v117, v77, v79\n\t" \
  "v_cvt_pk_bf16_f32 v116, v116, v117\n\t" \
  "ds_write_b32 v105, v116 offset:256\n\t" \
  "s_waitcnt lgkmcnt(3)\n\t" \
  "v_smfmac_f32_16x16x64_bf16 v[40:43], v[96:99], a[192:199], v106\n\t" \
  "v_smfmac_f32_16x16x64_bf16 v[44:47], v[96:99], a[200:207], v106\n\t" \
  "s_waitcnt lgkmcnt(0)\n\t" \
  "s_barrier\n\t" \
  "ds_read_b64_tr_b16 v[52:53], v107 offset:256\n\t" \
  "ds_read_b64_tr_b16 v[54:55], v107 offset:320\n\t" \
  "v_smfmac_f32_16x16x64_bf16 v[40:43], v[56:59], a[224:231], v106\n\t" \
  "ds_read_b64_tr_b16 v[60:61], v107 offset:1280\n\t" \
  "ds_read_b64_tr_b16 v[62:63], v107 offset:1344\n\t" \
  "v_smfmac_f32_16x16x64_bf16 v[44:47], v[56:59], a[232:239], v106\n\t" \
  "v_smfmac_f32_16x16x64_bf16 v[80:83], v[96:99], a[240:247], v106\n\t" \
  "v_mov_b64 v[32:33], 0\n\t" \
  "v_smfmac_f32_16x16x64_bf16 v[84:87], v[96:99], a[248:255], v106\n\t" \
  "v_mov_b64 v[34:35], 0\n\t" \
  "v_smfmac_f32_16x16x64_bf16 v[80:83], v[56:59], v[136:143], v106\n\t" \
  "v_mov_b64 v[36:37], 0\n\t" \
  "v_smfmac_f32_16x16x64_bf16 v[84:87], v[56:59], v[144:151], v106\n\t" \
  "s_waitcnt lgkmcnt(2)\n\t" \
  "v_smfmac_f32_16x16x64_bf16 v[40:43], v[52:55], a[208:215], v106\n\t" \
  "v_smfmac_f32_16x16x64_bf16 v[44:47], v[52:55], a[216:223], v106\n\t" \
  "s_xor_b32 s76, s47, 0x40000\n\t" \
  "s_waitcnt lgkmcnt(0)\n\t" \
  "v_smfmac_f32_16x16x64_bf16 v[40:43], v[60:63], v[0:7], v106\n\t" \
  "v_mov_b64 v[38:39], 0\n\t" \
  "s_xor_b32 s77, s47, 0x84000\n\t" \
  "v_smfmac_f32_16x16x64_bf16 v[44:47], v[60:63], v[8:15], v106\n\t" \
  "s_xor_b32 s78, s47, 0xc4000\n\t" \
  "v_smfmac_f32_16x16x64_bf16 v[80:83], v[52:55], v[120:127], v106\n\t" \
  "ds_read_b128 v[0:3], v103 offset:32768\n\t" \
  "v_cvt_pk_bf16_f32 v64, v40, v41\n\t" \
  "v_smfmac_f32_16x16x64_bf16 v[84:87], v[52:55], v[128:135], v106\n\t" \
  "ds_read_b128 v[4:7], v103 offset:33792\n\t" \
  "v_cvt_pk_bf16_f32 v66, v42, v43\n\t" \
  "v_smfmac_f32_16x16x64_bf16 v[80:83], v[60:63], v[16:23], v106\n\t" \
  "ds_read_b128 v[8:11], v103 offset:34816\n\t" \
  "v_cvt_pk_bf16_f32 v65, v44, v45\n\t" \
  "v_smfmac_f32_16x16x64_bf16 v[84:87], v[60:63], v[24:31], v106\n\t" \
  "ds_read_b128 v[12:15], v103 offset:35840\n\t" \
  "v_cvt_pk_bf16_f32 v67, v46, v47\n\t" \
  "v_smfmac_f32_16x16x64_bf16 v[88:91], v[96:99], v[152:159], v106\n\t" \
  "v_cvt_pk_bf16_f32 v68, v80, v81\n\t" \
  "v_smfmac_f32_16x16x64_bf16 v[92:95], v[96:99], v[160:167], v106\n\t" \
  "v_cvt_pk_bf16_f32 v70, v82, v83\n\t" \
  "v_smfmac_f32_16x16x64_bf16 v[88:91], v[52:55], v[168:175], v106\n\t" \
  "v_cvt_pk_bf16_f32 v69, v84, v85\n\t" \
  "v_smfmac_f32_16x16x64_bf16 v[92:95], v[52:55], v[176:183], v106\n\t" \
  "buffer_store_dwordx4 v[64:67], v108, %[ru], s47 offen sc1\n\t" \
  "v_cvt_pk_bf16_f32 v71, v86, v87\n\t" \
  "v_smfmac_f32_16x16x64_bf16 v[88:91], v[56:59], v[184:191], v106\n\t" \
  "ds_read_b128 v[16:19], v103 offset:49152\n\t" \
  "v_smfmac_f32_16x16x64_bf16 v[92:95], v[56:59], v[192:199], v106\n\t" \
  "ds_read_b128 v[20:23], v103 offset:50176\n\t" \
  "s_waitcnt lgkmcnt(4)\n\t" \
  "v_smfmac_f32_16x16x64_bf16 v[88:91], v[60:63], v[0:7], v106\n\t" \
  "ds_read_b128 v[24:27], v103 offset:51200\n\t" \
  "s_waitcnt lgkmcnt(3)\n\t" \
  "v_smfmac_f32_16x16x64_bf16 v[92:95], v[60:63], v[8:15], v106\n\t" \
  "ds_read_b128 v[28:31], v103 offset:52224\n\t" \
  "buffer_store_dwordx4 v[68:71], v108, %[ru], s76 offen sc1\n\t" \
  "v_smfmac_f32_16x16x64_bf16 v[32:35], v[96:99], v[200:207], v106\n\t" \
  "v_cvt_pk_bf16_f32 v72, v88, v89\n\t" \
  "v_smfmac_f32_16x16x64_bf16 v[36:39], v[96:99], v[208:215], v106\n\t" \
  "v_cvt_pk_bf16_f32 v74, v90, v91\n\t" \
  "v_smfmac_f32_16x16x64_bf16 v[32:35], v[52:55], v[216:223], v106\n\t" \
  "v_cvt_pk_bf16_f32 v73, v92, v93\n\t" \
  "v_smfmac_f32_16x16x64_bf16 v[36:39], v[52:55], v[224:231], v106\n\t" \
  "v_cvt_pk_bf16_f32 v75, v94, v95\n\t" \
  "v_smfmac_f32_16x16x64_bf16 v[32:35], v[56:59], v[232:239], v106\n\t" \
  "s_waitcnt lgkmcnt(2)\n\t" \
  "v_smfmac_f32_16x16x64_bf16 v[32:35], v[60:63], v[16:23], v106\n\t" \
  "v_smfmac_f32_16x16x64_bf16 v[36:39], v[56:59], v[240:247], v106\n\t" \
  "s_waitcnt lgkmcnt(0)\n\t" \
  "v_smfmac_f32_16x16x64_bf16 v[36:39], v[60:63], v[24:31], v106\n\t" \
  "buffer_store_dwordx4 v[72:75], v108, %[ru], s77 offen sc1\n\t" \
  "v_cvt_pk_bf16_f32 v76, v32, v33\n\t" \
  "v_cvt_pk_bf16_f32 v78, v34, v35\n\t" \
  "s_nop 5\n\t" \
  "v_cvt_pk_bf16_f32 v77, v36, v37\n\t" \
  "v_cvt_pk_bf16_f32 v79, v38, v39\n\t" \
  "buffer_store_dwordx4 v[76:79], v108, %[ru], s78 offen sc1\n\t" \
  "s_memtime s[76:77]\n\t" \
  "s_mov_b32 s42, 0\n\t" \
  "s_waitcnt lgkmcnt(0)\n\t" \
  "s_mov_b32 s57, s76\n\t" \
  "s_add_i32 s41, s56, 1950\n\t" \
  "s_sub_i32 s76, s92, s51\n\t" \
  "s_sub_i32 s77, s93, s53\n\t" \
  "s_cmp_eq_u32 s76, 0\n\t" \
  "s_cbranch_scc1 21f\n\t" \
  "v_cvt_f32_u32 v117, s76\n\t" \
  "v_cvt_f32_u32 v116, s77\n\t" \
  "v_rcp_f32 v117, v117\n\t" \
  "s_mov_b32 s51, s92\n\t" \
  "v_mul_f32 v117, v117, v116\n\t" \
  "v_max_f32 v117, 0x3f028f5c, v117\n\t" \
  "v_min_f32 v117, 0x3ffeb852, v117\n\t" \
  "v_rcp_f32 v116, v117\n\t" \
  "v_mul_f32 v117, 0x4f000000, v117\n\t" \
  "v_mul_f32 v116, 0x47800000, v116\n\t" \
  "v_cvt_u32_f32 v117, v117\n\t" \
  "v_cvt_u32_f32 v116, v116\n\t" \
  "s_mov_b32 s53, s93\n\t" \
  "v_readfirstlane_b32 s46, v117\n\t" \
  "v_readfirstlane_b32 s52, v116\n\t" \
  "21:\n\t" \
  "s_add_i32 s44, s44, 1\n\t" \
  "s_and_b32 s58, s58, 4\n\t" \
  "s_and_b32 s76, s44, 64\n\t" \
  "s_or_b32 s42, s42, s76\n\t" \
  "s_cbranch_scc0 20b\n\t" \
  "s_branch 99f\n\t" \
  "14:\n\t" \
  "s_add_u32 s59, s59, 0x10000\n\t" \
  "s_cmp_lt_u32 s59, 0x100000\n\t" \
  "s_cbranch_scc1 13b\n\t" \
  "s_mov_b64 exec, -1\n\t" \
  "s_mov_b32 s42, 16\n\t" \
  "s_branch 99f\n\t" \
  "8:\n\t" \
  "s_bitcmp1_b32 s58, 1\n\t" \
  "s_cbranch_scc1 4b\n\t" \
  "s_bitcmp1_b32 s58, 2\n\t" \
  "s_cbranch_scc1 5b\n\t" \
  "v_max3_u32 v116, v32, v33, v34\n\t" \
  "v_max3_u32 v117, v35, v36, v37\n\t" \
  "v_max3_u32 v116, v116, v38, v39\n\t" \
  "v_max_u32 v116, v116, v117\n\t" \
  "v_cmp_eq_u32 vcc, -1, v116\n\t" \
  "s_cbranch_vccz 5b\n\t" \
  "s_branch 0f\n\t" \
  "9:\n\t" \
  "s_bitcmp1_b32 s58, 2\n\t" \
  "s_cbranch_scc1 6b\n\t" \
  "v_max3_u32 v116, v40, v41, v42\n\t" \
  "v_max3_u32 v117, v43, v44, v45\n\t" \
  "v_max3_u32 v116, v116, v46, v47\n\t" \
  "v_max_u32 v116, v116, v117\n\t" \
  "v_cmp_eq_u32 vcc, -1, v116\n\t" \
  "s_cbranch_vccz 6b\n\t" \
  "s_or_b32 s58, s58, 2\n\t" \
  "0:\n\t" \
  "s_or_b32 s58, s58, 1\n\t" \
  "11:\n\t" \
  "buffer_load_dwordx4 v[32:35], v100, %[rh], s40 offen sc1\n\t" \
  "buffer_load_dwordx4 v[36:39], v100, %[rh], s76 offen sc1\n\t" \
  "buffer_load_dwordx4 v[40:43], v100, %[rh], s77 offen sc1\n\t" \
  "buffer_load_dwordx4 v[44:47], v100, %[rh], s78 offen sc1\n\t" \
  "s_waitcnt lgkmcnt(0)\n\t" \
  "s_branch 10b\n\t" \
  "17:\n\t" \
  "v_mov_b64 v[72:73], 0\n\t" \
  "v_mov_b64 v[74:75], 0\n\t" \
  "v_mov_b64 v[76:77], 0\n\t" \
  "v_mov_b64 v[78:79], 0\n\t" \
  "s_add_u32 s59, s59, 1\n\t" \
  "s_bitcmp1_b32 s58, 2\n\t" \
  "s_cbranch_scc1 3b\n\t" \
  "s_waitcnt vmcnt(0)\n\t" \
  "v_max_u32 v116, v32, v33\n\t" \
  "v_max3_u32 v116, v116, v34, v35\n\t" \
  "v_max3_u32 v116, v116, v36, v37\n\t" \
  "v_max3_u32 v116, v116, v38, v39\n\t" \
  "v_max3_u32 v116, v116, v40, v41\n\t" \
  "v_max3_u32 v116, v116, v42, v43\n\t" \
  "v_max3_u32 v116, v116, v44, v45\n\t" \
  "v_max3_u32 v116, v116, v46, v47\n\t" \
  "v_cmp_eq_u32 vcc, -1, v116\n\t" \
  "s_cbranch_vccz 3b\n\t" \
  "s_bitcmp1_b32 s59, 6\n\t" \
  "s_cbranch_scc1 12f\n\t" \
  "buffer_load_dwordx4 v[32:35], v100, %[rh], s40 offen sc1\n\t" \
  "buffer_load_dwordx4 v[36:39], v100, %[rh], s76 offen sc1\n\t" \
  "buffer_load_dwordx4 v[40:43], v100, %[rh], s77 offen sc1\n\t" \
  "buffer_load_dwordx4 v[44:47], v100, %[rh], s78 offen sc1\n\t" \
  "s_branch 17b\n\t" \
  "12:\n\t" \
  "s_mov_b32 s42, s58\n\t" \
  "s_branch 99f\n\t" \
  "99:\n\t" \
  "s_waitcnt vmcnt(0) lgkmcnt(0)\n\t" \
  ".Lchain_last:\n\t" \
  ""
#define CB_BODY \
  "s_mov_b32 s58, 8\n\t" \
  "s_mov_b32 s59, 0\n\t" \
  "s_sub_i32 s56, s51, 8000\n\t" \
  "s_lshl_b32 s50, s53, 8\n\t" \
  "s_branch 40f\n\t" \
  ".p2align 6\n\t" \
  "20:\n\t" \
  "s_mov_b64 exec, 0xff\n\t" \
  "s_mov_b32 s88, s51\n\t" \
  "s_add_i32 s85, s44, -1\n\t" \
  "s_add_i32 s79, s44, 2\n\t" \
  "v_cmp_gt_i32 vcc, s79, v210\n\t" \
  "s_and_b32 s96, s79, 15\n\t" \
  "s_lshl_b32 s96, s96, 22\n\t" \
  "s_add_i32 s96, s96, s42\n\t" \
  "s_lshr_b32 s82, s96, 21\n\t" \
  "s_bitcmp0_b32 %[sel], s82\n\t" \
  "s_cselect_b32 s82, 0x2000, 0\n\t" \
  "s_add_i32 s96, s96, s82\n\t" \
  "s_cmp_lt_u32 s79, 64\n\t" \
  "s_cselect_b32 s79, vcc_lo, -1\n\t" \
  "s_cmp_eq_u32 s79, 0\n\t" \
  "s_cselect_b32 s96, s96, 0x7f000000\n\t" \
  "s_cmp_eq_u32 s44, 0\n\t" \
  "s_cbranch_scc1 23f\n\t" \
  "21:\n\t" \
  "v_cmp_ne_u32_sdwa vcc, v208, s85 src0_sel:BYTE_0 src1_sel:DWORD\n\t" \
  "v_subrev_u32 v208, s50, v208\n\t" \
  "v_ashrrev_i32 v208, 8, v208\n\t" \
  "s_nop 1\n\t" \
  "v_max_i32_dpp v208, v208, v208 quad_perm:[1,0,3,2] row_mask:0xf bank_mask:0xf\n\t" \
  "s_cbranch_vccnz 22f\n\t" \
  "24:\n\t" \
  "s_nop 0\n\t" \
  "v_max_i32_dpp v208, v208, v208 quad_perm:[2,3,0,1] row_mask:0xf bank_mask:0xf\n\t" \
  "s_nop 1\n\t" \
  "v_max_i32_dpp v208, v208, v208 row_half_mirror row_mask:0xf bank_mask:0xf\n\t" \
  "v_add_u32 v208, 976, v208\n\t" \
  "v_med3_i32 v208, v208, 0, v246\n\t" \
  "v_mul_u32_u24 v208, s52, v208\n\t" \
  "v_lshrrev_b32 v208, 16, v208\n\t" \
  "s_nop 0\n\t" \
  "v_readlane_b32 s88, v208, 0\n\t" \
  "s_add_i32 s88, s88, s51\n\t" \
  "23:\n\t" \
  "s_mov_b32 exec_lo, -1\n\t" \
  "v_min_u32 v209, 1520, v209\n\t" \
  "s_nop 1\n\t" \
  "v_max_u32_dpp v209, v209, v209 quad_perm:[1,0,3,2] row_mask:0xf bank_mask:0xf\n\t" \
  "s_nop 1\n\t" \
  "v_max_u32_dpp v209, v209, v209 quad_perm:[2,3,0,1] row_mask:0xf bank_mask:0xf\n\t" \
  "s_nop 1\n\t" \
  "v_max_u32_dpp v209, v209, v209 row_half_mirror row_mask:0xf bank_mask:0xf\n\t" \
  "s_nop 1\n\t" \
  "v_max_u32_dpp v209, v209, v209 row_mirror row_mask:0xf bank_mask:0xf\n\t" \
  "s_nop 1\n\t" \
  "v_readlane_b32 s89, v209, 0\n\t" \
  "v_readlane_b32 s90, v209, 16\n\t" \
  "s_max_u32 s57, s89, s90\n\t" \
  "s_add_i32 s57, s57, 330\n\t" \
  "s_and_b32 s81, s44, 3\n\t" \
  "s_lshl_b32 s81, s81, 16\n\t" \
  "s_mov_b64 exec, 0xff\n\t" \
  "1:\n\t" \
  "s_memtime vcc\n\t" \
  "s_waitcnt lgkmcnt(0)\n\t" \
  "s_sub_i32 s79, s88, vcc_lo\n\t" \
  "s_cmp_gt_i32 s79, 128\n\t" \
  "s_cbranch_scc1 1b\n\t" \
  "2:\n\t" \
  "s_sub_i32 s79, s79, 33\n\t" \
  "s_cmp_gt_i32 s79, 56\n\t" \
  "s_cbranch_scc1 2b\n\t" \
  "buffer_load_dword v208, v234, %[rh], s81 offen sc1\n\t" \
  "s_mov_b64 exec, -1\n\t" \
  "v_accvgpr_read_b32 v0, a0\n\t" \
  "v_accvgpr_read_b32 v1, a8\n\t" \
  "v_accvgpr_read_b32 v2, a2\n\t" \
  "v_accvgpr_read_b32 v3, a10\n\t" \
  "v_accvgpr_read_b32 v4, a4\n\t" \
  "v_accvgpr_read_b32 v5, a12\n\t" \
  "v_accvgpr_read_b32 v6, a6\n\t" \
  "v_accvgpr_read_b32 v7, a14\n\t" \
  "v_accvgpr_read_b32 v8, a1\n\t" \
  "v_accvgpr_read_b32 v9, a9\n\t" \
  "v_accvgpr_read_b32 v10, a3\n\t" \
  "v_accvgpr_read_b32 v11, a11\n\t" \
  "v_accvgpr_read_b32 v12, a5\n\t" \
  "v_accvgpr_read_b32 v13, a13\n\t" \
  "v_accvgpr_read_b32 v14, a7\n\t" \
  "v_accvgpr_read_b32 v15, a15\n\t" \
  "v_accvgpr_read_b32 v16, a32\n\t" \
  "v_accvgpr_read_b32 v17, a40\n\t" \
  "v_accvgpr_read_b32 v18, a34\n\t" \
  "v_accvgpr_read_b32 v19, a42\n\t" \
  "v_accvgpr_read_b32 v20, a36\n\t" \
  "v_accvgpr_read_b32 v21, a44\n\t" \
  "v_accvgpr_read_b32 v22, a38\n\t" \
  "v_accvgpr_read_b32 v23, a46\n\t" \
  "v_accvgpr_read_b32 v24, a33\n\t" \
  "v_accvgpr_read_b32 v25, a41\n\t" \
  "v_accvgpr_read_b32 v26, a35\n\t" \
  "v_accvgpr_read_b32 v27, a43\n\t" \
  "v_accvgpr_read_b32 v28, a37\n\t" \
  "v_accvgpr_read_b32 v29, a45\n\t" \
  "v_accvgpr_read_b32 v30, a39\n\t" \
  "v_accvgpr_read_b32 v31, a47\n\t" \
  "v_accvgpr_read_b32 v32, a16\n\t" \
  "v_accvgpr_read_b32 v33, a24\n\t" \
  "v_accvgpr_read_b32 v34, a18\n\t" \
  "v_accvgpr_read_b32 v35, a26\n\t" \
  "v_accvgpr_read_b32 v36, a20\n\t" \
  "v_accvgpr_read_b32 v37, a28\n\t" \
  "v_accvgpr_read_b32 v38, a22\n\t" \
  "v_accvgpr_read_b32 v39, a30\n\t" \
  "v_accvgpr_read_b32 v40, a17\n\t" \
  "v_accvgpr_read_b32 v41, a25\n\t" \
  "v_accvgpr_read_b32 v42, a19\n\t" \
  "v_accvgpr_read_b32 v43, a27\n\t" \
  "v_accvgpr_read_b32 v44, a21\n\t" \
  "v_accvgpr_read_b32 v45, a29\n\t" \
  "v_accvgpr_read_b32 v46, a23\n\t" \
  "v_accvgpr_read_b32 v47, a31\n\t" \
  "v_accvgpr_read_b32 v48, a48\n\t" \
  "v_accvgpr_read_b32 v49, a56\n\t" \
  "v_accvgpr_read_b32 v50, a50\n\t" \
  "v_accvgpr_read_b32 v51, a58\n\t" \
  "v_accvgpr_read_b32 v52, a52\n\t" \
  "v_accvgpr_read_b32 v53, a60\n\t" \
  "v_accvgpr_read_b32 v54, a54\n\t" \
  "v_accvgpr_read_b32 v55, a62\n\t" \
  "v_accvgpr_read_b32 v56, a49\n\t" \
  "v_accvgpr_read_b32 v57, a57\n\t" \
  "v_accvgpr_read_b32 v58, a51\n\t" \
  "v_accvgpr_read_b32 v59, a59\n\t" \
  "v_accvgpr_read_b32 v60, a53\n\t" \
  "v_accvgpr_read_b32 v61, a61\n\t" \
  "v_accvgpr_read_b32 v62, a55\n\t" \
  "v_accvgpr_read_b32 v63, a63\n\t" \
  "s_mov_b64 exec, 0xff\n\t" \
  "s_add_i32 s89, s57, s53\n\t" \
  "s_mov_b32 s84, 600\n\t" \
  "13:\n\t" \
  "s_waitcnt vmcnt(0)\n\t" \
  "s_memtime s[86:87]\n\t" \
  "v_cmp_ne_u32_sdwa vcc, v208, s44 src0_sel:BYTE_0 src1_sel:DWORD\n\t" \
  "v_subrev_u32 v208, s50, v208\n\t" \
  "v_ashrrev_i32 v208, 8, v208\n\t" \
  "s_nop 1\n\t" \
  "v_max_i32_dpp v208, v208, v208 quad_perm:[1,0,3,2] row_mask:0xf bank_mask:0xf\n\t" \
  "s_nop 1\n\t" \
  "v_max_i32_dpp v208, v208, v208 quad_perm:[2,3,0,1] row_mask:0xf bank_mask:0xf\n\t" \
  "s_cbranch_vccnz 14f\n\t" \
  "15:\n\t" \
  "s_nop 0\n\t" \
  "v_max_i32_dpp v208, v208, v208 row_half_mirror row_mask:0xf bank_mask:0xf\n\t" \
  "v_add_u32 v209, 255, v208\n\t" \
  "v_med3_i32 v209, v209, 0, v246\n\t" \
  "v_mul_u32_u24 v209, s52, v209\n\t" \
  "v_readlane_b32 s82, v208, 0\n\t" \
  "v_lshrrev_b32 v209, 16, v209\n\t" \
  "s_waitcnt lgkmcnt(0)\n\t" \
  "s_sub_i32 s79, s51, s86\n\t" \
  "v_readlane_b32 s83, v209, 0\n\t" \
  "s_mov_b64 exec, -1\n\t" \
  "s_add_i32 s79, s79, s83\n\t" \
  "s_sub_i32 s85, s84, s79\n\t" \
  "s_max_i32 s85, s85, 0\n\t" \
  "s_add_i32 s79, s79, -634\n\t" \
  "s_sub_i32 s79, s79, %[far]\n\t" \
  "s_mul_hi_u32 s85, s85, s46\n\t" \
  "s_lshl1_add_u32 s85, s85, s82\n\t" \
  "s_add_i32 s85, s85, s89\n\t" \
  "s_lshl_b32 s85, s85, 8\n\t" \
  "s_or_b32 s85, s85, s44\n\t" \
  "v_mov_b32 v209, s85\n\t" \
  "buffer_store_dword v209, v235, %[rh], s81 offen sc1\n\t" \
  "s_add_i32 s85, s53, s82\n\t" \
  "v_mov_b32 v208, s85\n\t" \
  "ds_write_b32 v245, v208\n\t" \
  "3:\n\t" \
  "s_sub_i32 s79, s79, 33\n\t" \
  "s_cmp_gt_i32 s79, 56\n\t" \
  "s_cbranch_scc1 3b\n\t" \
  "buffer_load_dwordx4 v[192:195], v226, %[rh], s49 offen offset:0 sc1\n\t" \
  "s_nop 6\n\t" \
  "buffer_load_dwordx4 v[196:199], v226, %[rh], s49 offen offset:2048 sc1\n\t" \
  "s_nop 6\n\t" \
  "buffer_load_dwordx4 v[200:203], v226, %[rh], s80 offen offset:0 sc1\n\t" \
  "s_nop 6\n\t" \
  "buffer_load_dwordx4 v[204:207], v226, %[rh], s80 offen offset:2048 sc1\n\t" \
  "s_nop 6\n\t" \
  "buffer_load_dwordx4 v[64:67], v225, %[ru], s48 offen offset:0 sc1\n\t" \
  "s_nop 6\n\t" \
  "buffer_load_dwordx4 v[72:75], v225, %[ru], s76 offen offset:0 sc1\n\t" \
  "s_add_i32 s79, s79, 178\n\t" \
  "4:\n\t" \
  "s_sub_i32 s79, s79, 33\n\t" \
  "s_cmp_gt_i32 s79, 56\n\t" \
  "s_cbranch_scc1 4b\n\t" \
  "buffer_load_dwordx4 v[68:71], v225, %[ru], s77 offen offset:0 sc1\n\t" \
  "s_nop 6\n\t" \
  "buffer_load_dwordx4 v[76:79], v225, %[ru], s78 offen offset:0 sc1\n\t" \
  "s_nop 6\n\t" \
  "buffer_load_dwordx4 v[212:215], v228, %[rh], s54 offen offset:0 sc1\n\t" \
  "s_nop 6\n\t" \
  "buffer_load_dwordx4 v[208:211], v227, %[rh], s54 offen offset:0 sc1\n\t" \
  "10:\n\t" \
  "s_waitcnt vmcnt(9)\n\t" \
  "v_smfmac_f32_16x16x64_bf16 v[144:147], v[192:195], v[0:7], v229\n\t" \
  "v_smfmac_f32_16x16x64_bf16 v[148:151], v[192:195], v[8:15], v229\n\t" \
  "s_waitcnt vmcnt(8)\n\t" \
  "v_smfmac_f32_16x16x64_bf16 v[152:155], v[196:199], v[16:23], v229\n\t" \
  "v_cvt_pk_bf16_f32 v176, v144, v145\n\t" \
  "v_cvt_pk_bf16_f32 v177, v146, v147\n\t" \
  "v_smfmac_f32_16x16x64_bf16 v[156:159], v[196:199], v[24:31], v229\n\t" \
  "v_cvt_pk_bf16_f32 v184, v148, v149\n\t" \
  "v_cvt_pk_bf16_f32 v185, v150, v151\n\t" \
  "s_waitcnt vmcnt(7)\n\t" \
  "v_smfmac_f32_16x16x64_bf16 v[160:163], v[200:203], v[32:39], v229\n\t" \
  "v_cvt_pk_bf16_f32 v178, v152, v153\n\t" \
  "v_cvt_pk_bf16_f32 v179, v154, v155\n\t" \
  "v_smfmac_f32_16x16x64_bf16 v[164:167], v[200:203], v[40:47], v229\n\t" \
  "v_cvt_pk_bf16_f32 v186, v156, v157\n\t" \
  "v_cvt_pk_bf16_f32 v187, v158, v159\n\t" \
  "s_waitcnt vmcnt(6)\n\t" \
  "v_smfmac_f32_16x16x64_bf16 v[168:171], v[204:207], v[48:55], v229\n\t" \
  "v_cvt_pk_bf16_f32 v180, v160, v161\n\t" \
  "v_cvt_pk_bf16_f32 v181, v162, v163\n\t" \
  "v_smfmac_f32_16x16x64_bf16 v[172:175], v[204:207], v[56:63], v229\n\t" \
  "v_cvt_pk_bf16_f32 v188, v164, v165\n\t" \
  "v_cvt_pk_bf16_f32 v189, v166, v167\n\t" \
  "s_waitcnt vmcnt(5)\n\t" \
  "s_waitcnt vmcnt(4)\n\t" \
  "v_swap_b32 v65, v72\n\t" \
  "v_swap_b32 v67, v74\n\t" \
  "s_nop 1\n\t" \
  "v_cvt_pk_bf16_f32 v182, v168, v169\n\t" \
  "v_cvt_pk_bf16_f32 v183, v170, v171\n\t" \
  "v_cvt_pk_bf16_f32 v190, v172, v173\n\t" \
  "v_cvt_pk_bf16_f32 v191, v174, v175\n\t" \
  "s_waitcnt vmcnt(1)\n\t" \
  "v_smfmac_f32_16x16x64_bf16 v[216:219], v[212:215], v[176:183], v230\n\t" \
  "v_smfmac_f32_16x16x64_bf16 v[220:223], v[212:215], v[184:191], v230\n\t" \
  "v_swap_b32 v69, v76\n\t" \
  "v_swap_b32 v71, v78\n\t" \
  "s_waitcnt vmcnt(0)\n\t" \
  "s_nop 0\n\t" \
  "v_smfmac_f32_16x16x64_bf16 v[216:219], v[208:211], v[64:71], v229\n\t" \
  "v_smfmac_f32_16x16x64_bf16 v[220:223], v[208:211], v[72:79], v229\n\t" \
  "s_nop 3\n\t" \
  "ds_write2st64_b32 v231, v216, v217 offset0:0 offset1:1\n\t" \
  "ds_write2st64_b32 v231, v218, v219 offset0:2 offset1:3\n\t" \
  "v_cmp_u_f32 vcc, v216, v217\n\t" \
  "v_cmp_u_f32 s[82:83], v218, v219\n\t" \
  "ds_write2st64_b32 v231, v220, v221 offset0:16 offset1:17\n\t" \
  "ds_write2st64_b32 v231, v222, v223 offset0:18 offset1:19\n\t" \
  "v_cmp_u_f32 s[84:85], v220, v222\n\t" \
  "s_or_b64 vcc, vcc, s[82:83]\n\t" \
  "s_or_b64 vcc, vcc, s[84:85]\n\t" \
  "s_cbranch_scc1 8f\n\t" \
  "6:\n\t" \
  "s_waitcnt lgkmcnt(0)\n\t" \
  "s_barrier\n\t" \
  "ds_read_b64 v[136:137], v232 offset:0\n\t" \
  "ds_read_b64 v[138:139], v232 offset:1024\n\t" \
  "ds_read_b64 v[140:141], v232 offset:2048\n\t" \
  "ds_read_b64 v[142:143], v232 offset:3072\n\t" \
  "s_add_i32 s79, s44, 1\n\t" \
  "s_and_b32 s79, s79, 7\n\t" \
  "s_lshl_b32 s79, s79, 15\n\t" \
  "s_waitcnt lgkmcnt(2)\n\t" \
  "v_add_f32 v136, v136, v138\n\t" \
  "v_add_f32 v137, v137, v139\n\t" \
  "s_waitcnt lgkmcnt(0)\n\t" \
  "v_add_f32 v140, v140, v142\n\t" \
  "v_add_f32 v141, v141, v143\n\t" \
  "v_add_f32 v136, v136, v140\n\t" \
  "v_add_f32 v137, v137, v141\n\t" \
  "s_nop 0\n\t" \
  "v_add_f32_dpp v136, v136, v136 quad_perm:[1,0,3,2] row_mask:0xf bank_mask:0xf\n\t" \
  "v_add_f32_dpp v137, v137, v137 quad_perm:[1,0,3,2] row_mask:0xf bank_mask:0xf\n\t" \
  "v_cvt_pk_bf16_f32 v136, v136, v137\n\t" \
  "buffer_store_dword v136, v233, %[rh], s79 offen sc1\n\t" \
  "s_memtime s[56:57]\n\t" \
  "s_cmp_eq_u32 s96, 0x7f000000\n\t" \
  "s_cbranch_scc1 40f\n\t" \
  "s_nop 8\n\t" \
  "39:\n\t" \
  "s_xor_b32 s81, s96, 0x0\n\t" \
  "buffer_load_dwordx4 a[0:3], v224, %[ru], s81 offen offset:0 sc1\n\t" \
  "ds_read_b128 v[208:211], v247\n\t" \
  "v_mov_b64 v[212:213], -1\n\t" \
  "v_mov_b64 v[214:215], -1\n\t" \
  "s_add_i32 s79, s44, -1\n\t" \
  "s_and_b32 s79, s79, 7\n\t" \
  "s_lshl_b32 s79, s79, 15\n\t" \
  "buffer_store_dwordx4 v[212:215], v243, %[rh], s79 offen sc1\n\t" \
  "s_waitcnt lgkmcnt(0)\n\t" \
  "s_sub_i32 s76, s56, s51\n\t" \
  "s_mul_hi_u32 s76, s76, s46\n\t" \
  "s_lshl1_add_u32 s76, s76, s53\n\t" \
  "v_sub_u32 v208, s76, v208\n\t" \
  "v_sub_u32 v209, s76, v209\n\t" \
  "v_sub_u32 v210, s76, v210\n\t" \
  "v_sub_u32 v211, s76, v211\n\t" \
  "v_min3_u32 v208, v208, v209, v210\n\t" \
  "buffer_load_dwordx4 a[4:7], v224, %[ru], s81 offen offset:1024 sc1\n\t" \
  "v_min_u32 v208, v208, v211\n\t" \
  "s_nop 0\n\t" \
  "v_readfirstlane_b32 s76, v208\n\t" \
  "s_bitcmp1_b32 s58, 3\n\t" \
  "s_cselect_b32 s76, s45, s76\n\t" \
  "s_min_u32 s77, s45, s47\n\t" \
  "s_min_u32 s77, s77, s76\n\t" \
  "s_mov_b32 s47, s45\n\t" \
  "s_mov_b32 s45, s76\n\t" \
  "v_mov_b32 v208, s77\n\t" \
  "s_and_b32 s58, s58, 4\n\t" \
  "buffer_store_dword v208, v244, %[rh], s55 offen sc1\n\t" \
  "v_mov_b32 v209, s44\n\t" \
  "buffer_store_dword v209, v238, %[ru], 0 offen sc1\n\t" \
  "s_and_b32 s97, s44, 3\n\t" \
  "s_lshl_b32 s97, s97, 17\n\t" \
  "s_add_i32 s97, s97, s43\n\t" \
  "s_add_i32 s82, s56, 260\n\t" \
  "60:\n\t" \
  "s_memtime vcc\n\t" \
  "s_waitcnt lgkmcnt(0)\n\t" \
  "s_sub_i32 s83, s82, vcc_lo\n\t" \
  "s_cmp_gt_i32 s83, 30\n\t" \
  "s_cbranch_scc1 60b\n\t" \
  "s_xor_b32 s81, s96, 0x20000\n\t" \
  "buffer_load_dwordx4 a[8:11], v224, %[ru], s81 offen offset:0 sc1\n\t" \
  "s_andn2_b32 s84, s48, 0xfff\n\t" \
  "s_cmp_lt_i32 s44, 60\n\t" \
  "s_cselect_b32 s84, s84, 0x7f000000\n\t" \
  "s_add_i32 s79, s44, 2\n\t" \
  "s_and_b32 s48, s79, 3\n\t" \
  "s_lshl_b32 s76, s48, 2\n\t" \
  "s_lshl_b32 s48, s48, 20\n\t" \
  "s_add_i32 s48, s48, s40\n\t" \
  "s_lshr_b32 s76, %[um], s76\n\t" \
  "s_and_b32 s76, s76, 7\n\t" \
  "s_lshl_b32 s76, s76, 12\n\t" \
  "s_xor_b32 s48, s48, s76\n\t" \
  "s_xor_b32 s76, s48, 0x200\n\t" \
  "s_xor_b32 s77, s48, 0x400\n\t" \
  "s_xor_b32 s78, s48, 0x600\n\t" \
  "s_and_b32 s79, s79, 3\n\t" \
  "s_lshl_b32 s54, s79, 16\n\t" \
  "s_add_i32 s82, s56, 390\n\t" \
  "61:\n\t" \
  "s_memtime vcc\n\t" \
  "s_waitcnt lgkmcnt(0)\n\t" \
  "s_sub_i32 s83, s82, vcc_lo\n\t" \
  "s_cmp_gt_i32 s83, 30\n\t" \
  "s_cbranch_scc1 61b\n\t" \
  "buffer_load_dwordx4 a[12:15], v224, %[ru], s81 offen offset:1024 sc1\n\t" \
  "s_add_i32 s49, s54, s41\n\t" \
  "s_xor_b32 s80, s49, 0x400\n\t" \
  "buffer_store_dword v212, v235, %[rh], s54 offen sc1\n\t" \
  "s_add_i32 s82, s56, 520\n\t" \
  "62:\n\t" \
  "s_memtime vcc\n\t" \
  "s_waitcnt lgkmcnt(0)\n\t" \
  "s_sub_i32 s83, s82, vcc_lo\n\t" \
  "s_cmp_gt_i32 s83, 30\n\t" \
  "s_cbranch_scc1 62b\n\t" \
  "s_xor_b32 s81, s96, 0x40000\n\t" \
  "buffer_load_dwordx4 a[16:19], v224, %[ru], s81 offen offset:0 sc1\n\t" \
  "s_add_i32 s82, s56, 650\n\t" \
  "63:\n\t" \
  "s_memtime vcc\n\t" \
  "s_waitcnt lgkmcnt(0)\n\t" \
  "s_sub_i32 s83, s82, vcc_lo\n\t" \
  "s_cmp_gt_i32 s83, 30\n\t" \
  "s_cbranch_scc1 63b\n\t" \
  "buffer_load_dwordx4 a[20:23], v224, %[ru], s81 offen offset:1024 sc1\n\t" \
  "s_add_i32 s82, s56, 780\n\t" \
  "64:\n\t" \
  "s_memtime vcc\n\t" \
  "s_waitcnt lgkmcnt(0)\n\t" \
  "s_sub_i32 s83, s82, vcc_lo\n\t" \
  "s_cmp_gt_i32 s83, 30\n\t" \
  "s_cbranch_scc1 64b\n\t" \
  "s_xor_b32 s81, s96, 0x60000\n\t" \
  "buffer_load_dwordx4 a[24:27], v224, %[ru], s81 offen offset:0 sc1\n\t" \
  "s_add_i32 s82, s56, 910\n\t" \
  "65:\n\t" \
  "s_memtime vcc\n\t" \
  "s_waitcnt lgkmcnt(0)\n\t" \
  "s_sub_i32 s83, s82, vcc_lo\n\t" \
  "s_cmp_gt_i32 s83, 30\n\t" \
  "s_cbranch_scc1 65b\n\t" \
  "buffer_load_dwordx4 a[28:31], v224, %[ru], s81 offen offset:1024 sc1\n\t" \
  "buffer_store_dwordx4 v[212:215], v224, %[ru], s84 offen offset:0 sc1\n\t" \
  "s_add_i32 s82, s56, 1040\n\t" \
  "66:\n\t" \
  "s_memtime vcc\n\t" \
  "s_waitcnt lgkmcnt(0)\n\t" \
  "s_sub_i32 s83, s82, vcc_lo\n\t" \
  "s_cmp_gt_i32 s83, 30\n\t" \
  "s_cbranch_scc1 66b\n\t" \
  "s_xor_b32 s81, s96, 0x80000\n\t" \
  "buffer_load_dwordx4 a[32:35], v224, %[ru], s81 offen offset:0 sc1\n\t" \
  "s_and_b32 s95, s44, 3\n\t" \
  "s_lshl_b32 s95, s95, 16\n\t" \
  "s_mov_b64 exec, 0xff\n\t" \
  "buffer_load_dword v211, v248, %[rh], s95 offen offset:0 sc1\n\t" \
  "s_mov_b64 exec, -1\n\t" \
  "s_add_i32 s82, s56, 1170\n\t" \
  "67:\n\t" \
  "s_memtime vcc\n\t" \
  "s_waitcnt lgkmcnt(0)\n\t" \
  "s_sub_i32 s83, s82, vcc_lo\n\t" \
  "s_cmp_gt_i32 s83, 30\n\t" \
  "s_cbranch_scc1 67b\n\t" \
  "buffer_load_dwordx4 a[36:39], v224, %[ru], s81 offen offset:1024 sc1\n\t" \
  "buffer_store_dwordx4 v[212:215], v224, %[ru], s84 offen offset:1024 sc1\n\t" \
  "s_add_i32 s82, s56, 1300\n\t" \
  "68:\n\t" \
  "s_memtime vcc\n\t" \
  "s_waitcnt lgkmcnt(0)\n\t" \
  "s_sub_i32 s83, s82, vcc_lo\n\t" \
  "s_cmp_gt_i32 s83, 30\n\t" \
  "s_cbranch_scc1 68b\n\t" \
  "s_xor_b32 s81, s96, 0xa0000\n\t" \
  "buffer_load_dwordx4 a[40:43], v224, %[ru], s81 offen offset:0 sc1\n\t" \
  "s_add_i32 s82, s56, 1430\n\t" \
  "69:\n\t" \
  "s_memtime vcc\n\t" \
  "s_waitcnt lgkmcnt(0)\n\t" \
  "s_sub_i32 s83, s82, vcc_lo\n\t" \
  "s_cmp_gt_i32 s83, 30\n\t" \
  "s_cbranch_scc1 69b\n\t" \
  "buffer_load_dwordx4 a[44:47], v224, %[ru], s81 offen offset:1024 sc1\n\t" \
  "buffer_store_dwordx4 v[212:215], v224, %[ru], s84 offen offset:2048 sc1\n\t" \
  "s_add_i32 s82, s56, 1560\n\t" \
  "70:\n\t" \
  "s_memtime vcc\n\t" \
  "s_waitcnt lgkmcnt(0)\n\t" \
  "s_sub_i32 s83, s82, vcc_lo\n\t" \
  "s_cmp_gt_i32 s83, 30\n\t" \
  "s_cbranch_scc1 70b\n\t" \
  "s_xor_b32 s81, s96, 0xc0000\n\t" \
  "buffer_load_dwordx4 a[48:51], v224, %[ru], s81 offen offset:0 sc1\n\t" \
  "s_add_i32 s82, s56, 1690\n\t" \
  "71:\n\t" \
  "s_memtime vcc\n\t" \
  "s_waitcnt lgkmcnt(0)\n\t" \
  "s_sub_i32 s83, s82, vcc_lo\n\t" \
  "s_cmp_gt_i32 s83, 30\n\t" \
  "s_cbranch_scc1 71b\n\t" \
  "buffer_load_dwordx4 a[52:55], v224, %[ru], s81 offen offset:1024 sc1\n\t" \
  "buffer_store_dwordx4 v[212:215], v224, %[ru], s84 offen offset:3072 sc1\n\t" \
  "s_add_i32 s82, s56, 1820\n\t" \
  "72:\n\t" \
  "s_memtime vcc\n\t" \
  "s_waitcnt lgkmcnt(0)\n\t" \
  "s_sub_i32 s83, s82, vcc_lo\n\t" \
  "s_cmp_gt_i32 s83, 30\n\t" \
  "s_cbranch_scc1 72b\n\t" \
  "s_xor_b32 s81, s96, 0xe0000\n\t" \
  "buffer_load_dwordx4 a[56:59], v224, %[ru], s81 offen offset:0 sc1\n\t" \
  "s_add_i32 s82, s56, 1950\n\t" \
  "73:\n\t" \
  "s_memtime vcc\n\t" \
  "s_waitcnt lgkmcnt(0)\n\t" \
  "s_sub_i32 s83, s82, vcc_lo\n\t" \
  "s_cmp_gt_i32 s83, 30\n\t" \
  "s_cbranch_scc1 73b\n\t" \
  "buffer_load_dwordx4 a[60:63], v224, %[ru], s81 offen offset:1024 sc1\n\t" \
  "s_cmp_lt_i32 s44, 0\n\t" \
  "s_cbranch_scc1 51f\n\t" \
  "s_mov_b32 s79, s56\n\t" \
  "s_memtime vcc\n\t" \
  "s_memrealtime s[82:83]\n\t" \
  "s_nop 4\n\t" \
  "s_memrealtime s[84:85]\n\t" \
  "s_nop 4\n\t" \
  "s_memrealtime s[56:57]\n\t" \
  "s_nop 4\n\t" \
  "s_memrealtime s[86:87]\n\t" \
  "s_nop 4\n\t" \
  "s_memrealtime s[88:89]\n\t" \
  "s_nop 4\n\t" \
  "s_memrealtime s[90:91]\n\t" \
  "s_mov_b32 s92, 0\n\t" \
  "s_waitcnt lgkmcnt(0)\n\t" \
  "s_cmp_eq_u32 s82, s84\n\t" \
  "s_addc_u32 s92, s92, 0\n\t" \
  "s_cmp_eq_u32 s82, s56\n\t" \
  "s_addc_u32 s92, s92, 0\n\t" \
  "s_cmp_eq_u32 s82, s86\n\t" \
  "s_addc_u32 s92, s92, 0\n\t" \
  "s_cmp_eq_u32 s82, s88\n\t" \
  "s_addc_u32 s92, s92, 0\n\t" \
  "s_cmp_eq_u32 s82, s90\n\t" \
  "s_addc_u32 s92, s92, 0\n\t" \
  "s_mul_i32 s92, s92, 24\n\t" \
  "s_add_i32 s92, s92, vcc_lo\n\t" \
  "s_lshl4_add_u32 s93, s82, 64\n\t" \
  "s_mov_b32 s56, s79\n\t" \
  "s_sub_i32 s82, s92, s51\n\t" \
  "s_sub_i32 s83, s93, s53\n\t" \
  "s_cmp_eq_u32 s82, 0\n\t" \
  "s_cbranch_scc1 41f\n\t" \
  "v_cvt_f32_u32 v210, s82\n\t" \
  "v_cvt_f32_u32 v209, s83\n\t" \
  "v_rcp_f32 v210, v210\n\t" \
  "s_mov_b32 s51, s92\n\t" \
  "v_mul_f32 v210, v210, v209\n\t" \
  "v_max_f32 v210, 0x3f028f5c, v210\n\t" \
  "v_min_f32 v210, 0x3ffeb852, v210\n\t" \
  "v_rcp_f32 v209, v210\n\t" \
  "v_mul_f32 v210, 0x4f000000, v210\n\t" \
  "v_mul_f32 v209, 0x47800000, v209\n\t" \
  "v_cvt_u32_f32 v210, v210\n\t" \
  "v_cvt_u32_f32 v209, v209\n\t" \
  "s_mov_b32 s53, s93\n\t" \
  "v_readfirstlane_b32 s46, v210\n\t" \
  "v_readfirstlane_b32 s52, v209\n\t" \
  "41:\n\t" \
  "s_lshl_b32 s50, s53, 8\n\t" \
  "s_add_i32 s85, s44, 1\n\t" \
  "s_and_b32 s94, s85, 3\n\t" \
  "s_lshl_b32 s94, s94, 16\n\t" \
  "s_add_i32 s82, s56, 2300\n\t" \
  "74:\n\t" \
  "s_memtime vcc\n\t" \
  "s_waitcnt lgkmcnt(0)\n\t" \
  "s_sub_i32 s83, s82, vcc_lo\n\t" \
  "s_cmp_gt_i32 s83, 30\n\t" \
  "s_cbranch_scc1 74b\n\t" \
  "s_waitcnt vmcnt(10)\n\t" \
  "s_mov_b64 exec, 0xff\n\t" \
  "47:\n\t" \
  "v_cmp_ne_u32 vcc, s44, v211\n\t" \
  "s_mov_b64 exec, -1\n\t" \
  "s_cbranch_vccnz 45f\n\t" \
  "46:\n\t" \
  "s_xor_b32 s81, s97, 0x0\n\t" \
  "buffer_load_dwordx4 v[80:83], v224, %[rh], s81 offen offset:0 sc1\n\t" \
  "s_xor_b32 s81, s97, 0x800\n\t" \
  "buffer_load_dwordx4 v[84:87], v224, %[rh], s81 offen offset:0 sc1\n\t" \
  "s_xor_b32 s81, s97, 0x0\n\t" \
  "buffer_load_dwordx4 v[88:91], v224, %[rh], s81 offen offset:1024 sc1\n\t" \
  "s_xor_b32 s81, s97, 0x800\n\t" \
  "buffer_load_dwordx4 v[92:95], v224, %[rh], s81 offen offset:1024 sc1\n\t" \
  "s_xor_b32 s81, s97, 0x4000\n\t" \
  "buffer_load_dwordx4 v[96:99], v224, %[rh], s81 offen offset:0 sc1\n\t" \
  "s_xor_b32 s81, s97, 0x4800\n\t" \
  "buffer_load_dwordx4 v[100:103], v224, %[rh], s81 offen offset:0 sc1\n\t" \
  "s_xor_b32 s81, s97, 0x4000\n\t" \
  "buffer_load_dwordx4 v[104:107], v224, %[rh], s81 offen offset:1024 sc1\n\t" \
  "s_xor_b32 s81, s97, 0x4800\n\t" \
  "buffer_load_dwordx4 v[108:111], v224, %[rh], s81 offen offset:1024 sc1\n\t" \
  "s_waitcnt vmcnt(6)\n\t" \
  "v_swap_b32 v81, v84\n\t" \
  "v_swap_b32 v83, v86\n\t" \
  "s_nop 1\n\t" \
  "v_mfma_f32_16x16x32_bf16 v[192:195], v[0:3], v[80:83], 0\n\t" \
  "v_mfma_f32_16x16x32_bf16 v[200:203], v[8:11], v[80:83], 0\n\t" \
  "s_xor_b32 s81, s97, 0x1000\n\t" \
  "buffer_load_dwordx4 v[112:115], v224, %[rh], s81 offen offset:0 sc1\n\t" \
  "v_mfma_f32_16x16x32_bf16 v[196:199], v[0:3], v[84:87], 0\n\t" \
  "v_mfma_f32_16x16x32_bf16 v[204:207], v[8:11], v[84:87], 0\n\t" \
  "s_xor_b32 s81, s97, 0x1800\n\t" \
  "buffer_load_dwordx4 v[116:119], v224, %[rh], s81 offen offset:0 sc1\n\t" \
  "s_waitcnt vmcnt(6)\n\t" \
  "v_swap_b32 v89, v92\n\t" \
  "v_swap_b32 v91, v94\n\t" \
  "s_nop 1\n\t" \
  "v_mfma_f32_16x16x32_bf16 v[196:199], v[4:7], v[88:91], v[196:199]\n\t" \
  "v_mfma_f32_16x16x32_bf16 v[204:207], v[12:15], v[88:91], v[204:207]\n\t" \
  "s_xor_b32 s81, s97, 0x1000\n\t" \
  "buffer_load_dwordx4 v[120:123], v224, %[rh], s81 offen offset:1024 sc1\n\t" \
  "s_xor_b32 s81, s97, 0x1800\n\t" \
  "buffer_load_dwordx4 v[124:127], v224, %[rh], s81 offen offset:1024 sc1\n\t" \
  "s_waitcnt vmcnt(6)\n\t" \
  "v_swap_b32 v97, v100\n\t" \
  "v_swap_b32 v99, v102\n\t" \
  "s_nop 1\n\t" \
  "v_mfma_f32_16x16x32_bf16 v[192:195], v[16:19], v[96:99], v[192:195]\n\t" \
  "v_mfma_f32_16x16x32_bf16 v[200:203], v[24:27], v[96:99], v[200:203]\n\t" \
  "s_xor_b32 s81, s97, 0x5000\n\t" \
  "buffer_load_dwordx4 v[128:131], v224, %[rh], s81 offen offset:0 sc1\n\t" \
  "v_mfma_f32_16x16x32_bf16 v[196:199], v[16:19], v[100:103], v[196:199]\n\t" \
  "v_mfma_f32_16x16x32_bf16 v[204:207], v[24:27], v[100:103], v[204:207]\n\t" \
  "s_xor_b32 s81, s97, 0x5800\n\t" \
  "buffer_load_dwordx4 v[132:135], v224, %[rh], s81 offen offset:0 sc1\n\t" \
  "s_waitcnt vmcnt(6)\n\t" \
  "v_swap_b32 v105, v108\n\t" \
  "v_swap_b32 v107, v110\n\t" \
  "s_nop 1\n\t" \
  "v_mfma_f32_16x16x32_bf16 v[196:199], v[20:23], v[104:107], v[196:199]\n\t" \
  "v_mfma_f32_16x16x32_bf16 v[204:207], v[28:31], v[104:107], v[204:207]\n\t" \
  "s_xor_b32 s81, s97, 0x5000\n\t" \
  "buffer_load_dwordx4 v[136:139], v224, %[rh], s81 offen offset:1024 sc1\n\t" \
  "s_xor_b32 s81, s97, 0x5800\n\t" \
  "buffer_load_dwordx4 v[140:143], v224, %[rh], s81 offen offset:1024 sc1\n\t" \
  "v_permlane32_swap_b32 v92, v108\n\t" \
  "v_permlane32_swap_b32 v93, v109\n\t" \
  "v_permlane32_swap_b32 v94, v110\n\t" \
  "v_permlane32_swap_b32 v95, v111\n\t" \
  "s_nop 1\n\t" \
  "v_mfma_f32_16x16x32_bf16 v[192:195], v[64:67], v[92:95], v[192:195]\n\t" \
  "v_mfma_f32_16x16x32_bf16 v[200:203], v[72:75], v[92:95], v[200:203]\n\t" \
  "v_mfma_f32_16x16x32_bf16 v[196:199], v[64:67], v[108:111], v[196:199]\n\t" \
  "v_mfma_f32_16x16x32_bf16 v[204:207], v[72:75], v[108:111], v[204:207]\n\t" \
  "s_waitcnt vmcnt(6)\n\t" \
  "v_swap_b32 v113, v116\n\t" \
  "v_swap_b32 v115, v118\n\t" \
  "s_nop 1\n\t" \
  "v_mfma_f32_16x16x32_bf16 v[192:195], v[32:35], v[112:115], v[192:195]\n\t" \
  "v_mfma_f32_16x16x32_bf16 v[200:203], v[40:43], v[112:115], v[200:203]\n\t" \
  "v_mfma_f32_16x16x32_bf16 v[196:199], v[32:35], v[116:119], v[196:199]\n\t" \
  "v_mfma_f32_16x16x32_bf16 v[204:207], v[40:43], v[116:119], v[204:207]\n\t" \
  "s_waitcnt vmcnt(4)\n\t" \
  "v_swap_b32 v121, v124\n\t" \
  "v_swap_b32 v123, v126\n\t" \
  "s_nop 1\n\t" \
  "v_mfma_f32_16x16x32_bf16 v[196:199], v[36:39], v[120:123], v[196:199]\n\t" \
  "v_mfma_f32_16x16x32_bf16 v[204:207], v[44:47], v[120:123], v[204:207]\n\t" \
  "s_waitcnt vmcnt(2)\n\t" \
  "v_swap_b32 v129, v132\n\t" \
  "v_swap_b32 v131, v134\n\t" \
  "s_nop 1\n\t" \
  "v_mfma_f32_16x16x32_bf16 v[192:195], v[48:51], v[128:131], v[192:195]\n\t" \
  "v_mfma_f32_16x16x32_bf16 v[200:203], v[56:59], v[128:131], v[200:203]\n\t" \
  "v_mfma_f32_16x16x32_bf16 v[196:199], v[48:51], v[132:135], v[196:199]\n\t" \
  "v_mfma_f32_16x16x32_bf16 v[204:207], v[56:59], v[132:135], v[204:207]\n\t" \
  "s_waitcnt vmcnt(0)\n\t" \
  "v_swap_b32 v137, v140\n\t" \
  "v_swap_b32 v139, v142\n\t" \
  "s_nop 1\n\t" \
  "v_mfma_f32_16x16x32_bf16 v[196:199], v[52:55], v[136:139], v[196:199]\n\t" \
  "v_mfma_f32_16x16x32_bf16 v[204:207], v[60:63], v[136:139], v[204:207]\n\t" \
  "v_permlane32_swap_b32 v124, v140\n\t" \
  "v_permlane32_swap_b32 v125, v141\n\t" \
  "v_permlane32_swap_b32 v126, v142\n\t" \
  "v_permlane32_swap_b32 v127, v143\n\t" \
  "s_nop 1\n\t" \
  "v_mfma_f32_16x16x32_bf16 v[192:195], v[68:71], v[124:127], v[192:195]\n\t" \
  "v_mfma_f32_16x16x32_bf16 v[200:203], v[76:79], v[124:127], v[200:203]\n\t" \
  "v_mfma_f32_16x16x32_bf16 v[196:199], v[68:71], v[140:143], v[196:199]\n\t" \
  "v_mfma_f32_16x16x32_bf16 v[204:207], v[76:79], v[140:143], v[204:207]\n\t" \
  "v_mov_b64 v[144:145], 0\n\t" \
  "v_mov_b64 v[146:147], 0\n\t" \
  "v_mov_b64 v[148:149], 0\n\t" \
  "v_mov_b64 v[150:151], 0\n\t" \
  "v_mov_b64 v[152:153], 0\n\t" \
  "v_mov_b64 v[154:155], 0\n\t" \
  "v_mov_b64 v[156:157], 0\n\t" \
  "v_mov_b64 v[158:159], 0\n\t" \
  "s_add_i32 s82, s56, 3500\n\t" \
  "75:\n\t" \
  "s_memtime vcc\n\t" \
  "s_waitcnt lgkmcnt(0)\n\t" \
  "s_sub_i32 s83, s82, vcc_lo\n\t" \
  "s_cmp_gt_i32 s83, 30\n\t" \
  "s_cbranch_scc1 75b\n\t" \
  "s_mov_b32 exec_hi, 0\n\t" \
  "buffer_load_dword v209, v237, %[rh], s55 offen offset:0 sc1\n\t" \
  "s_mov_b64 exec, 0xff\n\t" \
  "buffer_load_dword v210, v242, %[ru], 0 offen offset:0 sc1\n\t" \
  "buffer_load_dword v208, v236, %[rh], s94 offen offset:0 sc1\n\t" \
  "s_mov_b64 exec, -1\n\t" \
  "ds_write_b128 v239, v[192:195] offset:0\n\t" \
  "ds_write_b128 v239, v[200:203] offset:2048\n\t" \
  "ds_write_b128 v239, v[196:199] offset:1024\n\t" \
  "ds_write_b128 v239, v[204:207] offset:3072\n\t" \
  "s_lshl_b32 s79, s44, 17\n\t" \
  "s_waitcnt lgkmcnt(0)\n\t" \
  "s_barrier\n\t" \
  "ds_read_b128 v[80:83], v240 offset:0\n\t" \
  "ds_read_b128 v[84:87], v240 offset:4096\n\t" \
  "ds_read_b128 v[88:91], v240 offset:8192\n\t" \
  "ds_read_b128 v[92:95], v240 offset:12288\n\t" \
  "v_mov_b64 v[160:161], 0\n\t" \
  "v_mov_b64 v[162:163], 0\n\t" \
  "v_mov_b64 v[164:165], 0\n\t" \
  "v_mov_b64 v[166:167], 0\n\t" \
  "v_mov_b64 v[168:169], 0\n\t" \
  "v_mov_b64 v[170:171], 0\n\t" \
  "v_mov_b64 v[172:173], 0\n\t" \
  "v_mov_b64 v[174:175], 0\n\t" \
  "v_mov_b64 v[216:217], 0\n\t" \
  "v_mov_b64 v[218:219], 0\n\t" \
  "v_mov_b64 v[220:221], 0\n\t" \
  "v_mov_b64 v[222:223], 0\n\t" \
  "s_waitcnt lgkmcnt(2)\n\t" \
  "v_add_f32 v80, v80, v84\n\t" \
  "v_add_f32 v81, v81, v85\n\t" \
  "v_add_f32 v82, v82, v86\n\t" \
  "v_add_f32 v83, v83, v87\n\t" \
  "s_waitcnt lgkmcnt(0)\n\t" \
  "v_add_f32 v88, v88, v92\n\t" \
  "v_add_f32 v89, v89, v93\n\t" \
  "v_add_f32 v90, v90, v94\n\t" \
  "v_add_f32 v91, v91, v95\n\t" \
  "v_add_f32 v80, v80, v88\n\t" \
  "v_add_f32 v81, v81, v89\n\t" \
  "v_add_f32 v82, v82, v90\n\t" \
  "v_add_f32 v83, v83, v91\n\t" \
  "v_cvt_pk_bf16_f32 v80, v80, v81\n\t" \
  "v_cvt_pk_bf16_f32 v81, v82, v83\n\t" \
  "buffer_store_dwordx2 v[80:81], v241, %[ro], s79 offen nt\n\t" \
  "s_cmp_gt_i32 s44, 61\n\t" \
  "s_cbranch_scc1 99f\n\t" \
  "s_waitcnt vmcnt(1)\n\t" \
  "52:\n\t" \
  "s_add_i32 s44, s44, 2\n\t" \
  "s_branch 20b\n\t" \
  "22:\n\t" \
  "s_bitcmp1_b32 s58, 2\n\t" \
  "s_cbranch_scc1 24b\n\t" \
  "s_sleep 2\n\t" \
  "buffer_load_dword v208, v236, %[rh], s94 offen sc1\n\t" \
  "s_waitcnt vmcnt(0)\n\t" \
  "s_add_u32 s59, s59, 1\n\t" \
  "s_bitcmp0_b32 s59, 20\n\t" \
  "s_cbranch_scc1 21b\n\t" \
  "s_or_b32 s58, s58, 4\n\t" \
  "s_branch 21b\n\t" \
  "14:\n\t" \
  "s_bitcmp1_b32 s58, 2\n\t" \
  "s_cbranch_scc1 15b\n\t" \
  "s_mov_b32 s84, 900\n\t" \
  "buffer_load_dword v208, v234, %[rh], s81 offen sc1\n\t" \
  "s_add_u32 s59, s59, 1\n\t" \
  "s_bitcmp0_b32 s59, 20\n\t" \
  "s_cbranch_scc1 13b\n\t" \
  "s_or_b32 s58, s58, 4\n\t" \
  "s_branch 13b\n\t" \
  "8:\n\t" \
  "s_mov_b32 s79, 0\n\t" \
  "17:\n\t" \
  "s_bitcmp1_b32 s58, 2\n\t" \
  "s_cbranch_scc1 18f\n\t" \
  "v_max_u32 v136, v64, v65\n\t" \
  "v_max3_u32 v136, v136, v66, v67\n\t" \
  "v_max3_u32 v136, v136, v68, v69\n\t" \
  "v_max3_u32 v136, v136, v70, v71\n\t" \
  "v_max3_u32 v136, v136, v72, v73\n\t" \
  "v_max3_u32 v136, v136, v74, v75\n\t" \
  "v_max3_u32 v136, v136, v76, v77\n\t" \
  "v_max3_u32 v136, v136, v78, v79\n\t" \
  "v_max3_u32 v136, v136, v192, v193\n\t" \
  "v_max3_u32 v136, v136, v194, v195\n\t" \
  "v_max3_u32 v136, v136, v196, v197\n\t" \
  "v_max3_u32 v136, v136, v198, v199\n\t" \
  "v_max3_u32 v136, v136, v200, v201\n\t" \
  "v_max3_u32 v136, v136, v202, v203\n\t" \
  "v_max3_u32 v136, v136, v204, v205\n\t" \
  "v_max3_u32 v136, v136, v206, v207\n\t" \
  "v_max3_u32 v136, v136, v208, v209\n\t" \
  "v_max3_u32 v136, v136, v210, v211\n\t" \
  "v_max3_u32 v136, v136, v212, v213\n\t" \
  "v_max3_u32 v136, v136, v214, v215\n\t" \
  "v_cmp_eq_u32 vcc, -1, v136\n\t" \
  "s_cbranch_vccz 18f\n\t" \
  "s_mov_b32 s79, 1\n\t" \
  "buffer_load_dwordx4 v[192:195], v226, %[rh], s49 offen offset:0 sc1\n\t" \
  "buffer_load_dwordx4 v[64:67], v225, %[ru], s48 offen offset:0 sc1\n\t" \
  "buffer_load_dwordx4 v[196:199], v226, %[rh], s49 offen offset:2048 sc1\n\t" \
  "buffer_load_dwordx4 v[72:75], v225, %[ru], s76 offen offset:0 sc1\n\t" \
  "buffer_load_dwordx4 v[200:203], v226, %[rh], s80 offen offset:0 sc1\n\t" \
  "buffer_load_dwordx4 v[68:71], v225, %[ru], s77 offen offset:0 sc1\n\t" \
  "buffer_load_dwordx4 v[204:207], v226, %[rh], s80 offen offset:2048 sc1\n\t" \
  "buffer_load_dwordx4 v[76:79], v225, %[ru], s78 offen offset:0 sc1\n\t" \
  "buffer_load_dwordx4 v[208:211], v227, %[rh], s54 offen offset:0 sc1\n\t" \
  "buffer_load_dwordx4 v[212:215], v228, %[rh], s54 offen offset:0 sc1\n\t" \
  "s_waitcnt vmcnt(0)\n\t" \
  "s_add_u32 s59, s59, 1\n\t" \
  "s_bitcmp0_b32 s59, 20\n\t" \
  "s_cbranch_scc1 17b\n\t" \
  "s_or_b32 s58, s58, 4\n\t" \
  "s_branch 17b\n\t" \
  "18:\n\t" \
  "s_cmp_eq_u32 s79, 0\n\t" \
  "s_cbranch_scc1 6b\n\t" \
  "v_mov_b64 v[144:145], 0\n\t" \
  "v_mov_b64 v[146:147], 0\n\t" \
  "v_mov_b64 v[148:149], 0\n\t" \
  "v_mov_b64 v[150:151], 0\n\t" \
  "v_mov_b64 v[152:153], 0\n\t" \
  "v_mov_b64 v[154:155], 0\n\t" \
  "v_mov_b64 v[156:157], 0\n\t" \
  "v_mov_b64 v[158:159], 0\n\t" \
  "v_mov_b64 v[160:161], 0\n\t" \
  "v_mov_b64 v[162:163], 0\n\t" \
  "v_mov_b64 v[164:165], 0\n\t" \
  "v_mov_b64 v[166:167], 0\n\t" \
  "v_mov_b64 v[168:169], 0\n\t" \
  "v_mov_b64 v[170:171], 0\n\t" \
  "v_mov_b64 v[172:173], 0\n\t" \
  "v_mov_b64 v[174:175], 0\n\t" \
  "v_mov_b64 v[216:217], 0\n\t" \
  "v_mov_b64 v[218:219], 0\n\t" \
  "v_mov_b64 v[220:221], 0\n\t" \
  "v_mov_b64 v[222:223], 0\n\t" \
  "s_branch 10b\n\t" \
  "40:\n\t" \
  "s_add_i32 s79, s44, 2\n\t" \
  "s_cmp_lt_i32 s79, 64\n\t" \
  "s_cbranch_scc0 44f\n\t" \
  "42:\n\t" \
  "s_mov_b64 exec, 0xff\n\t" \
  "buffer_load_dword v210, v242, %[ru], 0 offen sc1\n\t" \
  "s_waitcnt vmcnt(0)\n\t" \
  "v_cmp_gt_i32 vcc, s79, v210\n\t" \
  "s_mov_b64 exec, -1\n\t" \
  "s_cbranch_vccz 43f\n\t" \
  "s_sleep 3\n\t" \
  "s_bitcmp1_b32 s58, 2\n\t" \
  "s_cbranch_scc1 43f\n\t" \
  "s_add_u32 s59, s59, 1\n\t" \
  "s_bitcmp0_b32 s59, 20\n\t" \
  "s_cbranch_scc1 42b\n\t" \
  "s_or_b32 s58, s58, 4\n\t" \
  "s_branch 43f\n\t" \
  "43:\n\t" \
  "s_and_b32 s96, s79, 15\n\t" \
  "s_lshl_b32 s96, s96, 22\n\t" \
  "s_add_i32 s96, s96, s42\n\t" \
  "s_lshr_b32 s82, s96, 21\n\t" \
  "s_bitcmp0_b32 %[sel], s82\n\t" \
  "s_cselect_b32 s82, 0x2000, 0\n\t" \
  "s_add_i32 s96, s96, s82\n\t" \
  "s_branch 39b\n\t" \
  "51:\n\t" \
  "s_memtime vcc\n\t" \
  "s_memrealtime s[82:83]\n\t" \
  "s_nop 4\n\t" \
  "s_memrealtime s[84:85]\n\t" \
  "s_nop 4\n\t" \
  "s_memrealtime s[94:95]\n\t" \
  "s_nop 4\n\t" \
  "s_memrealtime s[86:87]\n\t" \
  "s_nop 4\n\t" \
  "s_memrealtime s[88:89]\n\t" \
  "s_nop 4\n\t" \
  "s_memrealtime s[90:91]\n\t" \
  "s_mov_b32 s92, 0\n\t" \
  "s_waitcnt lgkmcnt(0)\n\t" \
  "s_cmp_eq_u32 s82, s84\n\t" \
  "s_addc_u32 s92, s92, 0\n\t" \
  "s_cmp_eq_u32 s82, s94\n\t" \
  "s_addc_u32 s92, s92, 0\n\t" \
  "s_cmp_eq_u32 s82, s86\n\t" \
  "s_addc_u32 s92, s92, 0\n\t" \
  "s_cmp_eq_u32 s82, s88\n\t" \
  "s_addc_u32 s92, s92, 0\n\t" \
  "s_cmp_eq_u32 s82, s90\n\t" \
  "s_addc_u32 s92, s92, 0\n\t" \
  "s_mul_i32 s92, s92, 24\n\t" \
  "s_add_i32 s92, s92, vcc_lo\n\t" \
  "s_lshl4_add_u32 s93, s82, 64\n\t" \
  "s_sub_i32 s82, s92, s51\n\t" \
  "s_sub_i32 s83, s93, s53\n\t" \
  "s_cmp_eq_u32 s82, 0\n\t" \
  "s_cbranch_scc1 53f\n\t" \
  "v_cvt_f32_u32 v210, s82\n\t" \
  "v_cvt_f32_u32 v209, s83\n\t" \
  "v_rcp_f32 v210, v210\n\t" \
  "s_mov_b32 s51, s92\n\t" \
  "v_mul_f32 v210, v210, v209\n\t" \
  "v_max_f32 v210, 0x3f028f5c, v210\n\t" \
  "v_min_f32 v210, 0x3ffeb852, v210\n\t" \
  "v_rcp_f32 v209, v210\n\t" \
  "v_mul_f32 v210, 0x4f000000, v210\n\t" \
  "v_mul_f32 v209, 0x47800000, v209\n\t" \
  "v_cvt_u32_f32 v210, v210\n\t" \
  "v_cvt_u32_f32 v209, v209\n\t" \
  "s_mov_b32 s53, s93\n\t" \
  "v_readfirstlane_b32 s46, v210\n\t" \
  "v_readfirstlane_b32 s52, v209\n\t" \
  "53:\n\t" \
  "s_lshl_b32 s50, s53, 8\n\t" \
  "s_add_i32 s85, s44, 1\n\t" \
  "s_and_b32 s94, s85, 3\n\t" \
  "s_lshl_b32 s94, s94, 16\n\t" \
  "s_mov_b32 exec_hi, 0\n\t" \
  "buffer_load_dword v209, v237, %[rh], s55 offen offset:0 sc1\n\t" \
  "s_mov_b64 exec, 0xff\n\t" \
  "buffer_load_dword v210, v242, %[ru], 0 offen offset:0 sc1\n\t" \
  "buffer_load_dword v208, v236, %[rh], s94 offen offset:0 sc1\n\t" \
  "s_mov_b64 exec, -1\n\t" \
  "v_mov_b64 v[144:145], 0\n\t" \
  "v_mov_b64 v[146:147], 0\n\t" \
  "v_mov_b64 v[148:149], 0\n\t" \
  "v_mov_b64 v[150:151], 0\n\t" \
  "v_mov_b64 v[152:153], 0\n\t" \
  "v_mov_b64 v[154:155], 0\n\t" \
  "v_mov_b64 v[156:157], 0\n\t" \
  "v_mov_b64 v[158:159], 0\n\t" \
  "v_mov_b64 v[160:161], 0\n\t" \
  "v_mov_b64 v[162:163], 0\n\t" \
  "v_mov_b64 v[164:165], 0\n\t" \
  "v_mov_b64 v[166:167], 0\n\t" \
  "v_mov_b64 v[168:169], 0\n\t" \
  "v_mov_b64 v[170:171], 0\n\t" \
  "v_mov_b64 v[172:173], 0\n\t" \
  "v_mov_b64 v[174:175], 0\n\t" \
  "v_mov_b64 v[216:217], 0\n\t" \
  "v_mov_b64 v[218:219], 0\n\t" \
  "v_mov_b64 v[220:221], 0\n\t" \
  "v_mov_b64 v[222:223], 0\n\t" \
  "s_waitcnt vmcnt(0)\n\t" \
  "s_branch 52b\n\t" \
  "44:\n\t" \
  "s_waitcnt lgkmcnt(0)\n\t" \
  "s_and_b32 s97, s44, 3\n\t" \
  "s_lshl_b32 s97, s97, 17\n\t" \
  "s_add_i32 s97, s97, s43\n\t" \
  "s_add_i32 s85, s44, 1\n\t" \
  "s_and_b32 s94, s85, 3\n\t" \
  "s_lshl_b32 s94, s94, 16\n\t" \
  "s_and_b32 s95, s44, 3\n\t" \
  "s_lshl_b32 s95, s95, 16\n\t" \
  "s_add_i32 s82, s56, 500\n\t" \
  "48:\n\t" \
  "s_memtime vcc\n\t" \
  "s_waitcnt lgkmcnt(0)\n\t" \
  "s_sub_i32 s83, s82, vcc_lo\n\t" \
  "s_cmp_gt_i32 s83, 30\n\t" \
  "s_cbranch_scc1 48b\n\t" \
  "45:\n\t" \
  "s_bitcmp1_b32 s58, 2\n\t" \
  "s_cbranch_scc1 46b\n\t" \
  "s_sleep 1\n\t" \
  "s_mov_b64 exec, 0xff\n\t" \
  "buffer_load_dword v211, v248, %[rh], s95 offen sc1\n\t" \
  "s_waitcnt vmcnt(0)\n\t" \
  "s_add_u32 s59, s59, 1\n\t" \
  "s_bitcmp0_b32 s59, 20\n\t" \
  "s_cbranch_scc1 47b\n\t" \
  "s_or_b32 s58, s58, 4\n\t" \
  "s_branch 47b\n\t" \
  "99:\n\t" \
  "s_waitcnt vmcnt(0) lgkmcnt(0)\n\t" \
  ""
#define P_BODY \
  "s_mov_b32 s46, -1\n\t" \
  "s_cmp_eq_u32 %[wv], 1\n\t" \
  "s_cbranch_scc1 91f\n\t" \
  "s_cmp_eq_u32 %[wv], 2\n\t" \
  "s_cbranch_scc1 92f\n\t" \
  "s_cmp_eq_u32 %[wv], 3\n\t" \
  "s_cbranch_scc1 93f\n\t" \
  "90:\n\t" \
  "ds_read_b128 v[0:3], v164 offset:0\n\t" \
  "ds_read_b128 v[4:7], v164 offset:8192\n\t" \
  "ds_read_b128 v[8:11], v165 offset:0\n\t" \
  "ds_read_b128 v[12:15], v165 offset:8192\n\t" \
  "ds_read_b128 v[16:19], v166 offset:0\n\t" \
  "ds_read_b128 v[20:23], v166 offset:8192\n\t" \
  "ds_read_b128 v[24:27], v167 offset:0\n\t" \
  "ds_read_b128 v[28:31], v167 offset:8192\n\t" \
  "ds_read_b128 v[32:35], v164 offset:256\n\t" \
  "ds_read_b128 v[36:39], v164 offset:8448\n\t" \
  "ds_read_b128 v[40:43], v165 offset:256\n\t" \
  "ds_read_b128 v[44:47], v165 offset:8448\n\t" \
  "ds_read_b128 v[48:51], v166 offset:256\n\t" \
  "ds_read_b128 v[52:55], v166 offset:8448\n\t" \
  "ds_read_b128 v[56:59], v167 offset:256\n\t" \
  "ds_read_b128 v[60:63], v167 offset:8448\n\t" \
  "v_mov_b64 v[152:153], -1\n\t" \
  "v_mov_b64 v[154:155], -1\n\t" \
  "s_cmp_le_u32 s44, 6\n\t" \
  ".p2align 6\n\t" \
  "20:\n\t" \
  "s_waitcnt vmcnt(28) lgkmcnt(0)\n\t" \
  "v_mfma_f32_16x16x32_bf16 v[128:131], a[0:3], v[0:3], 0\n\t" \
  "s_cbranch_scc1 30f\n\t" \
  "40:\n\t" \
  "s_barrier\n\t" \
  "v_cvt_pk_bf16_f32 v148, v136, v137\n\t" \
  "v_cvt_pk_bf16_f32 v149, v138, v139\n\t" \
  "v_mfma_f32_16x16x32_bf16 v[132:135], a[4:7], v[0:3], 0\n\t" \
  "v_cvt_pk_bf16_f32 v150, v140, v141\n\t" \
  "v_cvt_pk_bf16_f32 v151, v142, v143\n\t" \
  "v_mfma_f32_16x16x32_bf16 v[136:139], a[0:3], v[4:7], 0\n\t" \
  "s_add_i32 m0, s41, 0\n\t" \
  "v_mfma_f32_16x16x32_bf16 v[140:143], a[4:7], v[4:7], 0\n\t" \
  "buffer_load_dwordx4 v160, %[rx], s40 offen lds\n\t" \
  "v_mfma_f32_16x16x32_bf16 v[128:131], a[8:11], v[8:11], v[128:131]\n\t" \
  "ds_read_b128 v[64:67], v164 offset:16384\n\t" \
  "v_mfma_f32_16x16x32_bf16 v[132:135], a[12:15], v[8:11], v[132:135]\n\t" \
  "ds_read_b128 v[68:71], v164 offset:24576\n\t" \
  "v_mfma_f32_16x16x32_bf16 v[136:139], a[8:11], v[12:15], v[136:139]\n\t" \
  "ds_read_b128 v[72:75], v165 offset:16384\n\t" \
  "v_mfma_f32_16x16x32_bf16 v[140:143], a[12:15], v[12:15], v[140:143]\n\t" \
  "buffer_store_dwordx4 v[144:147], v168, %[ru], s42 offen sc1\n\t" \
  "v_mfma_f32_16x16x32_bf16 v[128:131], a[16:19], v[16:19], v[128:131]\n\t" \
  "ds_read_b128 v[76:79], v165 offset:24576\n\t" \
  "v_mfma_f32_16x16x32_bf16 v[132:135], a[20:23], v[16:19], v[132:135]\n\t" \
  "ds_read_b128 v[80:83], v166 offset:16384\n\t" \
  "v_mfma_f32_16x16x32_bf16 v[136:139], a[16:19], v[20:23], v[136:139]\n\t" \
  "s_add_i32 m0, s41, 1024\n\t" \
  "v_mfma_f32_16x16x32_bf16 v[140:143], a[20:23], v[20:23], v[140:143]\n\t" \
  "buffer_load_dwordx4 v161, %[rx], s40 offen lds\n\t" \
  "v_mfma_f32_16x16x32_bf16 v[128:131], a[24:27], v[24:27], v[128:131]\n\t" \
  "ds_read_b128 v[84:87], v166 offset:24576\n\t" \
  "v_mfma_f32_16x16x32_bf16 v[132:135], a[28:31], v[24:27], v[132:135]\n\t" \
  "ds_read_b128 v[88:91], v167 offset:16384\n\t" \
  "v_mfma_f32_16x16x32_bf16 v[136:139], a[24:27], v[28:31], v[136:139]\n\t" \
  "ds_read_b128 v[92:95], v167 offset:24576\n\t" \
  "v_mfma_f32_16x16x32_bf16 v[140:143], a[28:31], v[28:31], v[140:143]\n\t" \
  "buffer_store_dwordx4 v[148:151], v169, %[ru], s42 offen sc1\n\t" \
  "v_mfma_f32_16x16x32_bf16 v[128:131], a[32:35], v[32:35], v[128:131]\n\t" \
  "ds_read_b128 v[96:99], v164 offset:16640\n\t" \
  "v_mfma_f32_16x16x32_bf16 v[132:135], a[36:39], v[32:35], v[132:135]\n\t" \
  "ds_read_b128 v[100:103], v164 offset:24832\n\t" \
  "v_mfma_f32_16x16x32_bf16 v[136:139], a[32:35], v[36:39], v[136:139]\n\t" \
  "s_add_i32 m0, s41, 2048\n\t" \
  "v_mfma_f32_16x16x32_bf16 v[140:143], a[36:39], v[36:39], v[140:143]\n\t" \
  "buffer_load_dwordx4 v162, %[rx], s40 offen lds\n\t" \
  "v_mfma_f32_16x16x32_bf16 v[128:131], a[40:43], v[40:43], v[128:131]\n\t" \
  "ds_read_b128 v[104:107], v165 offset:16640\n\t" \
  "v_mfma_f32_16x16x32_bf16 v[132:135], a[44:47], v[40:43], v[132:135]\n\t" \
  "ds_read_b128 v[108:111], v165 offset:24832\n\t" \
  "v_mfma_f32_16x16x32_bf16 v[136:139], a[40:43], v[44:47], v[136:139]\n\t" \
  "s_add_i32 m0, s41, 3072\n\t" \
  "v_mfma_f32_16x16x32_bf16 v[140:143], a[44:47], v[44:47], v[140:143]\n\t" \
  "buffer_load_dwordx4 v163, %[rx], s40 offen lds\n\t" \
  "v_mfma_f32_16x16x32_bf16 v[128:131], a[48:51], v[48:51], v[128:131]\n\t" \
  "ds_read_b128 v[112:115], v166 offset:16640\n\t" \
  "v_mfma_f32_16x16x32_bf16 v[132:135], a[52:55], v[48:51], v[132:135]\n\t" \
  "ds_read_b128 v[116:119], v166 offset:24832\n\t" \
  "v_mfma_f32_16x16x32_bf16 v[136:139], a[48:51], v[52:55], v[136:139]\n\t" \
  "ds_read_b128 v[120:123], v167 offset:16640\n\t" \
  "v_mfma_f32_16x16x32_bf16 v[140:143], a[52:55], v[52:55], v[140:143]\n\t" \
  "ds_read_b128 v[124:127], v167 offset:24832\n\t" \
  "v_mfma_f32_16x16x32_bf16 v[128:131], a[56:59], v[56:59], v[128:131]\n\t" \
  "v_mfma_f32_16x16x32_bf16 v[132:135], a[60:63], v[56:59], v[132:135]\n\t" \
  "v_mfma_f32_16x16x32_bf16 v[136:139], a[56:59], v[60:63], v[136:139]\n\t" \
  "v_mfma_f32_16x16x32_bf16 v[140:143], a[60:63], v[60:63], v[140:143]\n\t" \
  "s_cmp_le_u32 s44, 6\n\t" \
  "s_waitcnt vmcnt(30) lgkmcnt(0)\n\t" \
  "v_mfma_f32_16x16x32_bf16 v[128:131], a[64:67], v[64:67], v[128:131]\n\t" \
  "s_cbranch_scc1 31f\n\t" \
  "41:\n\t" \
  "s_barrier\n\t" \
  "v_mfma_f32_16x16x32_bf16 v[132:135], a[68:71], v[64:67], v[132:135]\n\t" \
  "s_add_i32 s76, s40, 512\n\t" \
  "v_mfma_f32_16x16x32_bf16 v[136:139], a[64:67], v[68:71], v[136:139]\n\t" \
  "s_add_i32 m0, s41, 16384\n\t" \
  "v_mfma_f32_16x16x32_bf16 v[140:143], a[68:71], v[68:71], v[140:143]\n\t" \
  "buffer_load_dwordx4 v160, %[rx], s76 offen lds\n\t" \
  "v_mfma_f32_16x16x32_bf16 v[128:131], a[72:75], v[72:75], v[128:131]\n\t" \
  "ds_read_b128 v[0:3], v164 offset:32768\n\t" \
  "v_mfma_f32_16x16x32_bf16 v[132:135], a[76:79], v[72:75], v[132:135]\n\t" \
  "ds_read_b128 v[4:7], v164 offset:40960\n\t" \
  "v_mfma_f32_16x16x32_bf16 v[136:139], a[72:75], v[76:79], v[136:139]\n\t" \
  "ds_read_b128 v[8:11], v165 offset:32768\n\t" \
  "v_mfma_f32_16x16x32_bf16 v[140:143], a[76:79], v[76:79], v[140:143]\n\t" \
  "v_mfma_f32_16x16x32_bf16 v[128:131], a[80:83], v[80:83], v[128:131]\n\t" \
  "ds_read_b128 v[12:15], v165 offset:40960\n\t" \
  "v_mfma_f32_16x16x32_bf16 v[132:135], a[84:87], v[80:83], v[132:135]\n\t" \
  "ds_read_b128 v[16:19], v166 offset:32768\n\t" \
  "v_mfma_f32_16x16x32_bf16 v[136:139], a[80:83], v[84:87], v[136:139]\n\t" \
  "s_add_i32 m0, s41, 17408\n\t" \
  "v_mfma_f32_16x16x32_bf16 v[140:143], a[84:87], v[84:87], v[140:143]\n\t" \
  "buffer_load_dwordx4 v161, %[rx], s76 offen lds\n\t" \
  "v_mfma_f32_16x16x32_bf16 v[128:131], a[88:91], v[88:91], v[128:131]\n\t" \
  "ds_read_b128 v[20:23], v166 offset:40960\n\t" \
  "v_mfma_f32_16x16x32_bf16 v[132:135], a[92:95], v[88:91], v[132:135]\n\t" \
  "ds_read_b128 v[24:27], v167 offset:32768\n\t" \
  "v_mfma_f32_16x16x32_bf16 v[136:139], a[88:91], v[92:95], v[136:139]\n\t" \
  "ds_read_b128 v[28:31], v167 offset:40960\n\t" \
  "v_mfma_f32_16x16x32_bf16 v[140:143], a[92:95], v[92:95], v[140:143]\n\t" \
  "v_mfma_f32_16x16x32_bf16 v[128:131], a[96:99], v[96:99], v[128:131]\n\t" \
  "ds_read_b128 v[32:35], v164 offset:33024\n\t" \
  "v_mfma_f32_16x16x32_bf16 v[132:135], a[100:103], v[96:99], v[132:135]\n\t" \
  "ds_read_b128 v[36:39], v164 offset:41216\n\t" \
  "v_mfma_f32_16x16x32_bf16 v[136:139], a[96:99], v[100:103], v[136:139]\n\t" \
  "s_add_i32 m0, s41, 18432\n\t" \
  "v_mfma_f32_16x16x32_bf16 v[140:143], a[100:103], v[100:103], v[140:143]\n\t" \
  "buffer_load_dwordx4 v162, %[rx], s76 offen lds\n\t" \
  "v_mfma_f32_16x16x32_bf16 v[128:131], a[104:107], v[104:107], v[128:131]\n\t" \
  "ds_read_b128 v[40:43], v165 offset:33024\n\t" \
  "v_mfma_f32_16x16x32_bf16 v[132:135], a[108:111], v[104:107], v[132:135]\n\t" \
  "ds_read_b128 v[44:47], v165 offset:41216\n\t" \
  "v_mfma_f32_16x16x32_bf16 v[136:139], a[104:107], v[108:111], v[136:139]\n\t" \
  "s_add_i32 m0, s41, 19456\n\t" \
  "v_mfma_f32_16x16x32_bf16 v[140:143], a[108:111], v[108:111], v[140:143]\n\t" \
  "buffer_load_dwordx4 v163, %[rx], s76 offen lds\n\t" \
  "v_mfma_f32_16x16x32_bf16 v[128:131], a[112:115], v[112:115], v[128:131]\n\t" \
  "ds_read_b128 v[48:51], v166 offset:33024\n\t" \
  "v_mfma_f32_16x16x32_bf16 v[132:135], a[116:119], v[112:115], v[132:135]\n\t" \
  "ds_read_b128 v[52:55], v166 offset:41216\n\t" \
  "v_mfma_f32_16x16x32_bf16 v[136:139], a[112:115], v[116:119], v[136:139]\n\t" \
  "ds_read_b128 v[56:59], v167 offset:33024\n\t" \
  "v_mfma_f32_16x16x32_bf16 v[140:143], a[116:119], v[116:119], v[140:143]\n\t" \
  "ds_read_b128 v[60:63], v167 offset:41216\n\t" \
  "v_mfma_f32_16x16x32_bf16 v[128:131], a[120:123], v[120:123], v[128:131]\n\t" \
  "s_and_b32 s79, s44, 4\n\t" \
  "v_mfma_f32_16x16x32_bf16 v[132:135], a[124:127], v[120:123], v[132:135]\n\t" \
  "s_lshl_b32 s79, s79, 11\n\t" \
  "v_mfma_f32_16x16x32_bf16 v[136:139], a[120:123], v[124:127], v[136:139]\n\t" \
  "v_mfma_f32_16x16x32_bf16 v[140:143], a[124:127], v[124:127], v[140:143]\n\t" \
  "s_cmp_le_u32 s44, 6\n\t" \
  "s_waitcnt vmcnt(29) lgkmcnt(0)\n\t" \
  "v_mfma_f32_16x16x32_bf16 v[128:131], a[128:131], v[0:3], v[128:131]\n\t" \
  "s_cbranch_scc1 32f\n\t" \
  "42:\n\t" \
  "s_barrier\n\t" \
  "v_mfma_f32_16x16x32_bf16 v[132:135], a[132:135], v[0:3], v[132:135]\n\t" \
  "s_add_i32 s76, s40, 1024\n\t" \
  "v_mfma_f32_16x16x32_bf16 v[136:139], a[128:131], v[4:7], v[136:139]\n\t" \
  "s_add_i32 m0, s41, 32768\n\t" \
  "v_mfma_f32_16x16x32_bf16 v[140:143], a[132:135], v[4:7], v[140:143]\n\t" \
  "buffer_load_dwordx4 v160, %[rx], s76 offen lds\n\t" \
  "v_mfma_f32_16x16x32_bf16 v[128:131], a[136:139], v[8:11], v[128:131]\n\t" \
  "ds_read_b128 v[64:67], v164 offset:49152\n\t" \
  "v_mfma_f32_16x16x32_bf16 v[132:135], a[140:143], v[8:11], v[132:135]\n\t" \
  "ds_read_b128 v[68:71], v164 offset:57344\n\t" \
  "v_mfma_f32_16x16x32_bf16 v[136:139], a[136:139], v[12:15], v[136:139]\n\t" \
  "ds_read_b128 v[72:75], v165 offset:49152\n\t" \
  "v_mfma_f32_16x16x32_bf16 v[140:143], a[140:143], v[12:15], v[140:143]\n\t" \
  "v_mfma_f32_16x16x32_bf16 v[128:131], a[144:147], v[16:19], v[128:131]\n\t" \
  "ds_read_b128 v[76:79], v165 offset:57344\n\t" \
  "v_mfma_f32_16x16x32_bf16 v[132:135], a[148:151], v[16:19], v[132:135]\n\t" \
  "ds_read_b128 v[80:83], v166 offset:49152\n\t" \
  "v_mfma_f32_16x16x32_bf16 v[136:139], a[144:147], v[20:23], v[136:139]\n\t" \
  "s_add_i32 m0, s41, 33792\n\t" \
  "v_mfma_f32_16x16x32_bf16 v[140:143], a[148:151], v[20:23], v[140:143]\n\t" \
  "buffer_load_dwordx4 v161, %[rx], s76 offen lds\n\t" \
  "v_mfma_f32_16x16x32_bf16 v[128:131], a[152:155], v[24:27], v[128:131]\n\t" \
  "ds_read_b128 v[84:87], v166 offset:57344\n\t" \
  "v_mfma_f32_16x16x32_bf16 v[132:135], a[156:159], v[24:27], v[132:135]\n\t" \
  "ds_read_b128 v[88:91], v167 offset:49152\n\t" \
  "v_mfma_f32_16x16x32_bf16 v[136:139], a[152:155], v[28:31], v[136:139]\n\t" \
  "ds_read_b128 v[92:95], v167 offset:57344\n\t" \
  "v_mfma_f32_16x16x32_bf16 v[140:143], a[156:159], v[28:31], v[140:143]\n\t" \
  "v_mfma_f32_16x16x32_bf16 v[128:131], a[160:163], v[32:35], v[128:131]\n\t" \
  "ds_read_b128 v[96:99], v164 offset:49408\n\t" \
  "v_mfma_f32_16x16x32_bf16 v[132:135], a[164:167], v[32:35], v[132:135]\n\t" \
  "ds_read_b128 v[100:103], v164 offset:57600\n\t" \
  "v_mfma_f32_16x16x32_bf16 v[136:139], a[160:163], v[36:39], v[136:139]\n\t" \
  "s_add_i32 m0, s41, 34816\n\t" \
  "v_mfma_f32_16x16x32_bf16 v[140:143], a[164:167], v[36:39], v[140:143]\n\t" \
  "buffer_load_dwordx4 v162, %[rx], s76 offen lds\n\t" \
  "v_mfma_f32_16x16x32_bf16 v[128:131], a[168:171], v[40:43], v[128:131]\n\t" \
  "ds_read_b128 v[104:107], v165 offset:49408\n\t" \
  "v_mfma_f32_16x16x32_bf16 v[132:135], a[172:175], v[40:43], v[132:135]\n\t" \
  "ds_read_b128 v[108:111], v165 offset:57600\n\t" \
  "v_mfma_f32_16x16x32_bf16 v[136:139], a[168:171], v[44:47], v[136:139]\n\t" \
  "s_add_i32 m0, s41, 35840\n\t" \
  "v_mfma_f32_16x16x32_bf16 v[140:143], a[172:175], v[44:47], v[140:143]\n\t" \
  "buffer_load_dwordx4 v163, %[rx], s76 offen lds\n\t" \
  "v_mfma_f32_16x16x32_bf16 v[128:131], a[176:179], v[48:51], v[128:131]\n\t" \
  "ds_read_b128 v[112:115], v166 offset:49408\n\t" \
  "v_mfma_f32_16x16x32_bf16 v[132:135], a[180:183], v[48:51], v[132:135]\n\t" \
  "ds_read_b128 v[116:119], v166 offset:57600\n\t" \
  "v_mfma_f32_16x16x32_bf16 v[136:139], a[176:179], v[52:55], v[136:139]\n\t" \
  "ds_read_b128 v[120:123], v167 offset:49408\n\t" \
  "v_mfma_f32_16x16x32_bf16 v[140:143], a[180:183], v[52:55], v[140:143]\n\t" \
  "ds_read_b128 v[124:127], v167 offset:57600\n\t" \
  "v_mfma_f32_16x16x32_bf16 v[128:131], a[184:187], v[56:59], v[128:131]\n\t" \
  "s_add_i32 s78, s44, -2\n\t" \
  "v_mfma_f32_16x16x32_bf16 v[132:135], a[188:191], v[56:59], v[132:135]\n\t" \
  "s_max_i32 s78, s78, s46\n\t" \
  "v_mfma_f32_16x16x32_bf16 v[136:139], a[184:187], v[60:63], v[136:139]\n\t" \
  "v_mov_b32 v156, s78\n\t" \
  "v_mfma_f32_16x16x32_bf16 v[140:143], a[188:191], v[60:63], v[140:143]\n\t" \
  "s_cmp_le_u32 s44, 6\n\t" \
  "s_waitcnt vmcnt(27) lgkmcnt(0)\n\t" \
  "v_mfma_f32_16x16x32_bf16 v[128:131], a[192:195], v[64:67], v[128:131]\n\t" \
  "s_cbranch_scc1 33f\n\t" \
  "43:\n\t" \
  "s_barrier\n\t" \
  "v_xor_b32 v164, 0x10000, v164\n\t" \
  "v_mfma_f32_16x16x32_bf16 v[132:135], a[196:199], v[64:67], v[132:135]\n\t" \
  "s_add_i32 s76, s40, 1536\n\t" \
  "buffer_store_dword v156, v171, %[ru], 0 offen sc1\n\t" \
  "v_mfma_f32_16x16x32_bf16 v[136:139], a[192:195], v[68:71], v[136:139]\n\t" \
  "s_add_i32 m0, s41, 49152\n\t" \
  "v_xor_b32 v165, 0x10000, v165\n\t" \
  "v_mfma_f32_16x16x32_bf16 v[140:143], a[196:199], v[68:71], v[140:143]\n\t" \
  "buffer_load_dwordx4 v160, %[rx], s76 offen lds\n\t" \
  "v_xor_b32 v166, 0x10000, v166\n\t" \
  "v_mfma_f32_16x16x32_bf16 v[128:131], a[200:203], v[72:75], v[128:131]\n\t" \
  "v_xor_b32 v167, 0x10000, v167\n\t" \
  "ds_read_b128 v[0:3], v164 offset:0\n\t" \
  "v_mfma_f32_16x16x32_bf16 v[132:135], a[204:207], v[72:75], v[132:135]\n\t" \
  "ds_read_b128 v[4:7], v164 offset:8192\n\t" \
  "v_mfma_f32_16x16x32_bf16 v[136:139], a[200:203], v[76:79], v[136:139]\n\t" \
  "ds_read_b128 v[8:11], v165 offset:0\n\t" \
  "v_mfma_f32_16x16x32_bf16 v[140:143], a[204:207], v[76:79], v[140:143]\n\t" \
  "v_mfma_f32_16x16x32_bf16 v[128:131], a[208:211], v[80:83], v[128:131]\n\t" \
  "ds_read_b128 v[12:15], v165 offset:8192\n\t" \
  "v_mfma_f32_16x16x32_bf16 v[132:135], a[212:215], v[80:83], v[132:135]\n\t" \
  "ds_read_b128 v[16:19], v166 offset:0\n\t" \
  "v_mfma_f32_16x16x32_bf16 v[136:139], a[208:211], v[84:87], v[136:139]\n\t" \
  "s_add_i32 m0, s41, 50176\n\t" \
  "ds_read_b128 v[20:23], v166 offset:8192\n\t" \
  "v_mfma_f32_16x16x32_bf16 v[140:143], a[212:215], v[84:87], v[140:143]\n\t" \
  "buffer_load_dwordx4 v161, %[rx], s76 offen lds\n\t" \
  "v_mfma_f32_16x16x32_bf16 v[128:131], a[216:219], v[88:91], v[128:131]\n\t" \
  "ds_read_b128 v[24:27], v167 offset:0\n\t" \
  "v_mfma_f32_16x16x32_bf16 v[132:135], a[220:223], v[88:91], v[132:135]\n\t" \
  "ds_read_b128 v[28:31], v167 offset:8192\n\t" \
  "v_mfma_f32_16x16x32_bf16 v[136:139], a[216:219], v[92:95], v[136:139]\n\t" \
  "ds_read_b128 v[32:35], v164 offset:256\n\t" \
  "v_mfma_f32_16x16x32_bf16 v[140:143], a[220:223], v[92:95], v[140:143]\n\t" \
  "v_mfma_f32_16x16x32_bf16 v[128:131], a[224:227], v[96:99], v[128:131]\n\t" \
  "ds_read_b128 v[36:39], v164 offset:8448\n\t" \
  "v_mfma_f32_16x16x32_bf16 v[132:135], a[228:231], v[96:99], v[132:135]\n\t" \
  "ds_read_b128 v[40:43], v165 offset:256\n\t" \
  "v_mfma_f32_16x16x32_bf16 v[136:139], a[224:227], v[100:103], v[136:139]\n\t" \
  "s_add_i32 m0, s41, 51200\n\t" \
  "v_mfma_f32_16x16x32_bf16 v[140:143], a[228:231], v[100:103], v[140:143]\n\t" \
  "buffer_load_dwordx4 v162, %[rx], s76 offen lds\n\t" \
  "v_mfma_f32_16x16x32_bf16 v[128:131], a[232:235], v[104:107], v[128:131]\n\t" \
  "ds_read_b128 v[44:47], v165 offset:8448\n\t" \
  "v_mfma_f32_16x16x32_bf16 v[132:135], a[236:239], v[104:107], v[132:135]\n\t" \
  "ds_read_b128 v[48:51], v166 offset:256\n\t" \
  "v_mfma_f32_16x16x32_bf16 v[136:139], a[232:235], v[108:111], v[136:139]\n\t" \
  "s_add_i32 m0, s41, 52224\n\t" \
  "v_mfma_f32_16x16x32_bf16 v[140:143], a[236:239], v[108:111], v[140:143]\n\t" \
  "buffer_load_dwordx4 v163, %[rx], s76 offen lds\n\t" \
  "v_mfma_f32_16x16x32_bf16 v[128:131], a[240:243], v[112:115], v[128:131]\n\t" \
  "ds_read_b128 v[52:55], v166 offset:8448\n\t" \
  "v_mfma_f32_16x16x32_bf16 v[132:135], a[244:247], v[112:115], v[132:135]\n\t" \
  "ds_read_b128 v[56:59], v167 offset:256\n\t" \
  "v_mfma_f32_16x16x32_bf16 v[136:139], a[240:243], v[116:119], v[136:139]\n\t" \
  "ds_read_b128 v[60:63], v167 offset:8448\n\t" \
  "v_mfma_f32_16x16x32_bf16 v[140:143], a[244:247], v[116:119], v[140:143]\n\t" \
  "s_add_i32 s44, s44, 1\n\t" \
  "s_xor_b32 s42, s43, s79\n\t" \
  "v_mfma_f32_16x16x32_bf16 v[128:131], a[248:251], v[120:123], v[128:131]\n\t" \
  "s_add_i32 s43, s43, 0x80000\n\t" \
  "s_add_i32 s40, s40, 0x10000\n\t" \
  "v_mfma_f32_16x16x32_bf16 v[132:135], a[252:255], v[120:123], v[132:135]\n\t" \
  "s_xor_b32 s41, s41, 0x10000\n\t" \
  "v_mfma_f32_16x16x32_bf16 v[136:139], a[248:251], v[124:127], v[136:139]\n\t" \
  "v_cvt_pk_bf16_f32 v144, v128, v129\n\t" \
  "v_cvt_pk_bf16_f32 v145, v130, v131\n\t" \
  "v_mfma_f32_16x16x32_bf16 v[140:143], a[252:255], v[124:127], v[140:143]\n\t" \
  "v_cvt_pk_bf16_f32 v146, v132, v133\n\t" \
  "v_cvt_pk_bf16_f32 v147, v134, v135\n\t" \
  "s_cmp_eq_u32 s44, 64\n\t" \
  "s_cbranch_scc1 98f\n\t" \
  "s_cmp_le_u32 s44, 6\n\t" \
  "s_branch 20b\n\t" \
  "30:\n\t" \
  "s_waitcnt vmcnt(24)\n\t" \
  "s_branch 40b\n\t" \
  "31:\n\t" \
  "s_waitcnt vmcnt(24)\n\t" \
  "s_branch 41b\n\t" \
  "32:\n\t" \
  "s_waitcnt vmcnt(6)\n\t" \
  "s_barrier\n\t" \
  "s_add_i32 s46, s44, -1\n\t" \
  "v_mov_b32 v156, s46\n\t" \
  "buffer_store_dword v156, v171, %[ru], 0 offen sc1\n\t" \
  "s_branch 42b\n\t" \
  "33:\n\t" \
  "s_waitcnt vmcnt(24)\n\t" \
  "s_branch 43b\n\t" \
  "91:\n\t" \
  "ds_read_b128 v[0:3], v164 offset:0\n\t" \
  "ds_read_b128 v[4:7], v164 offset:8192\n\t" \
  "ds_read_b128 v[8:11], v165 offset:0\n\t" \
  "ds_read_b128 v[12:15], v165 offset:8192\n\t" \
  "ds_read_b128 v[16:19], v166 offset:0\n\t" \
  "ds_read_b128 v[20:23], v166 offset:8192\n\t" \
  "ds_read_b128 v[24:27], v167 offset:0\n\t" \
  "ds_read_b128 v[28:31], v167 offset:8192\n\t" \
  "ds_read_b128 v[32:35], v164 offset:256\n\t" \
  "ds_read_b128 v[36:39], v164 offset:8448\n\t" \
  "ds_read_b128 v[40:43], v165 offset:256\n\t" \
  "ds_read_b128 v[44:47], v165 offset:8448\n\t" \
  "ds_read_b128 v[48:51], v166 offset:256\n\t" \
  "ds_read_b128 v[52:55], v166 offset:8448\n\t" \
  "ds_read_b128 v[56:59], v167 offset:256\n\t" \
  "ds_read_b128 v[60:63], v167 offset:8448\n\t" \
  "v_mov_b64 v[152:153], -1\n\t" \
  "v_mov_b64 v[154:155], -1\n\t" \
  "s_cmp_le_u32 s44, 6\n\t" \
  ".p2align 6\n\t" \
  "20:\n\t" \
  "s_waitcnt vmcnt(28) lgkmcnt(0)\n\t" \
  "v_mfma_f32_16x16x32_bf16 v[128:131], a[0:3], v[0:3], 0\n\t" \
  "s_cbranch_scc1 30f\n\t" \
  "40:\n\t" \
  "s_barrier\n\t" \
  "v_cvt_pk_bf16_f32 v148, v136, v137\n\t" \
  "v_cvt_pk_bf16_f32 v149, v138, v139\n\t" \
  "v_mfma_f32_16x16x32_bf16 v[132:135], a[4:7], v[0:3], 0\n\t" \
  "v_cvt_pk_bf16_f32 v150, v140, v141\n\t" \
  "v_cvt_pk_bf16_f32 v151, v142, v143\n\t" \
  "v_mfma_f32_16x16x32_bf16 v[136:139], a[0:3], v[4:7], 0\n\t" \
  "ds_read_b128 v[64:67], v164 offset:16384\n\t" \
  "v_mfma_f32_16x16x32_bf16 v[140:143], a[4:7], v[4:7], 0\n\t" \
  "s_add_i32 m0, s41, 0\n\t" \
  "v_mfma_f32_16x16x32_bf16 v[128:131], a[8:11], v[8:11], v[128:131]\n\t" \
  "buffer_load_dwordx4 v160, %[rx], s40 offen lds\n\t" \
  "v_mfma_f32_16x16x32_bf16 v[132:135], a[12:15], v[8:11], v[132:135]\n\t" \
  "ds_read_b128 v[68:71], v164 offset:24576\n\t" \
  "v_mfma_f32_16x16x32_bf16 v[136:139], a[8:11], v[12:15], v[136:139]\n\t" \
  "ds_read_b128 v[72:75], v165 offset:16384\n\t" \
  "v_mfma_f32_16x16x32_bf16 v[140:143], a[12:15], v[12:15], v[140:143]\n\t" \
  "ds_read_b128 v[76:79], v165 offset:24576\n\t" \
  "v_mfma_f32_16x16x32_bf16 v[128:131], a[16:19], v[16:19], v[128:131]\n\t" \
  "buffer_store_dwordx4 v[144:147], v168, %[ru], s42 offen sc1\n\t" \
  "v_mfma_f32_16x16x32_bf16 v[132:135], a[20:23], v[16:19], v[132:135]\n\t" \
  "ds_read_b128 v[80:83], v166 offset:16384\n\t" \
  "v_mfma_f32_16x16x32_bf16 v[136:139], a[16:19], v[20:23], v[136:139]\n\t" \
  "ds_read_b128 v[84:87], v166 offset:24576\n\t" \
  "v_mfma_f32_16x16x32_bf16 v[140:143], a[20:23], v[20:23], v[140:143]\n\t" \
  "s_add_i32 m0, s41, 1024\n\t" \
  "v_mfma_f32_16x16x32_bf16 v[128:131], a[24:27], v[24:27], v[128:131]\n\t" \
  "buffer_load_dwordx4 v161, %[rx], s40 offen lds\n\t" \
  "v_mfma_f32_16x16x32_bf16 v[132:135], a[28:31], v[24:27], v[132:135]\n\t" \
  "ds_read_b128 v[88:91], v167 offset:16384\n\t" \
  "v_mfma_f32_16x16x32_bf16 v[136:139], a[24:27], v[28:31], v[136:139]\n\t" \
  "ds_read_b128 v[92:95], v167 offset:24576\n\t" \
  "v_mfma_f32_16x16x32_bf16 v[140:143], a[28:31], v[28:31], v[140:143]\n\t" \
  "ds_read_b128 v[96:99], v164 offset:16640\n\t" \
  "v_mfma_f32_16x16x32_bf16 v[128:131], a[32:35], v[32:35], v[128:131]\n\t" \
  "buffer_store_dwordx4 v[148:151], v169, %[ru], s42 offen sc1\n\t" \
  "v_mfma_f32_16x16x32_bf16 v[132:135], a[36:39], v[32:35], v[132:135]\n\t" \
  "ds_read_b128 v[100:103], v164 offset:24832\n\t" \
  "v_mfma_f32_16x16x32_bf16 v[136:139], a[32:35], v[36:39], v[136:139]\n\t" \
  "ds_read_b128 v[104:107], v165 offset:16640\n\t" \
  "v_mfma_f32_16x16x32_bf16 v[140:143], a[36:39], v[36:39], v[140:143]\n\t" \
  "s_add_i32 m0, s41, 2048\n\t" \
  "v_mfma_f32_16x16x32_bf16 v[128:131], a[40:43], v[40:43], v[128:131]\n\t" \
  "buffer_load_dwordx4 v162, %[rx], s40 offen lds\n\t" \
  "v_mfma_f32_16x16x32_bf16 v[132:135], a[44:47], v[40:43], v[132:135]\n\t" \
  "ds_read_b128 v[108:111], v165 offset:24832\n\t" \
  "v_mfma_f32_16x16x32_bf16 v[136:139], a[40:43], v[44:47], v[136:139]\n\t" \
  "ds_read_b128 v[112:115], v166 offset:16640\n\t" \
  "v_mfma_f32_16x16x32_bf16 v[140:143], a[44:47], v[44:47], v[140:143]\n\t" \
  "s_add_i32 m0, s41, 3072\n\t" \
  "v_mfma_f32_16x16x32_bf16 v[128:131], a[48:51], v[48:51], v[128:131]\n\t" \
  "buffer_load_dwordx4 v163, %[rx], s40 offen lds\n\t" \
  "v_mfma_f32_16x16x32_bf16 v[132:135], a[52:55], v[48:51], v[132:135]\n\t" \
  "ds_read_b128 v[116:119], v166 offset:24832\n\t" \
  "v_mfma_f32_16x16x32_bf16 v[136:139], a[48:51], v[52:55], v[136:139]\n\t" \
  "ds_read_b128 v[120:123], v167 offset:16640\n\t" \
  "v_mfma_f32_16x16x32_bf16 v[140:143], a[52:55], v[52:55], v[140:143]\n\t" \
  "ds_read_b128 v[124:127], v167 offset:24832\n\t" \
  "v_mfma_f32_16x16x32_bf16 v[128:131], a[56:59], v[56:59], v[128:131]\n\t" \
  "v_mfma_f32_16x16x32_bf16 v[132:135], a[60:63], v[56:59], v[132:135]\n\t" \
  "v_mfma_f32_16x16x32_bf16 v[136:139], a[56:59], v[60:63], v[136:139]\n\t" \
  "v_mfma_f32_16x16x32_bf16 v[140:143], a[60:63], v[60:63], v[140:143]\n\t" \
  "s_cmp_le_u32 s44, 6\n\t" \
  "s_waitcnt vmcnt(30) lgkmcnt(0)\n\t" \
  "v_mfma_f32_16x16x32_bf16 v[128:131], a[64:67], v[64:67], v[128:131]\n\t" \
  "s_cbranch_scc1 31f\n\t" \
  "41:\n\t" \
  "s_barrier\n\t" \
  "v_mfma_f32_16x16x32_bf16 v[132:135], a[68:71], v[64:67], v[132:135]\n\t" \
  "s_add_i32 s76, s40, 512\n\t" \
  "v_mfma_f32_16x16x32_bf16 v[136:139], a[64:67], v[68:71], v[136:139]\n\t" \
  "ds_read_b128 v[0:3], v164 offset:32768\n\t" \
  "v_mfma_f32_16x16x32_bf16 v[140:143], a[68:71], v[68:71], v[140:143]\n\t" \
  "s_add_i32 m0, s41, 16384\n\t" \
  "v_mfma_f32_16x16x32_bf16 v[128:131], a[72:75], v[72:75], v[128:131]\n\t" \
  "buffer_load_dwordx4 v160, %[rx], s76 offen lds\n\t" \
  "v_mfma_f32_16x16x32_bf16 v[132:135], a[76:79], v[72:75], v[132:135]\n\t" \
  "ds_read_b128 v[4:7], v164 offset:40960\n\t" \
  "v_mfma_f32_16x16x32_bf16 v[136:139], a[72:75], v[76:79], v[136:139]\n\t" \
  "ds_read_b128 v[8:11], v165 offset:32768\n\t" \
  "v_mfma_f32_16x16x32_bf16 v[140:143], a[76:79], v[76:79], v[140:143]\n\t" \
  "ds_read_b128 v[12:15], v165 offset:40960\n\t" \
  "v_mfma_f32_16x16x32_bf16 v[128:131], a[80:83], v[80:83], v[128:131]\n\t" \
  "v_mfma_f32_16x16x32_bf16 v[132:135], a[84:87], v[80:83], v[132:135]\n\t" \
  "ds_read_b128 v[16:19], v166 offset:32768\n\t" \
  "v_mfma_f32_16x16x32_bf16 v[136:139], a[80:83], v[84:87], v[136:139]\n\t" \
  "ds_read_b128 v[20:23], v166 offset:40960\n\t" \
  "v_mfma_f32_16x16x32_bf16 v[140:143], a[84:87], v[84:87], v[140:143]\n\t" \
  "s_add_i32 m0, s41, 17408\n\t" \
  "v_mfma_f32_16x16x32_bf16 v[128:131], a[88:91], v[88:91], v[128:131]\n\t" \
  "buffer_load_dwordx4 v161, %[rx], s76 offen lds\n\t" \
  "v_mfma_f32_16x16x32_bf16 v[132:135], a[92:95], v[88:91], v[132:135]\n\t" \
  "ds_read_b128 v[24:27], v167 offset:32768\n\t" \
  "v_mfma_f32_16x16x32_bf16 v[136:139], a[88:91], v[92:95], v[136:139]\n\t" \
  "ds_read_b128 v[28:31], v167 offset:40960\n\t" \
  "v_mfma_f32_16x16x32_bf16 v[140:143], a[92:95], v[92:95], v[140:143]\n\t" \
  "ds_read_b128 v[32:35], v164 offset:33024\n\t" \
  "v_mfma_f32_16x16x32_bf16 v[128:131], a[96:99], v[96:99], v[128:131]\n\t" \
  "v_mfma_f32_16x16x32_bf16 v[132:135], a[100:103], v[96:99], v[132:135]\n\t" \
  "ds_read_b128 v[36:39], v164 offset:41216\n\t" \
  "v_mfma_f32_16x16x32_bf16 v[136:139], a[96:99], v[100:103], v[136:139]\n\t" \
  "ds_read_b128 v[40:43], v165 offset:33024\n\t" \
  "v_mfma_f32_16x16x32_bf16 v[140:143], a[100:103], v[100:103], v[140:143]\n\t" \
  "s_add_i32 m0, s41, 18432\n\t" \
  "v_mfma_f32_16x16x32_bf16 v[128:131], a[104:107], v[104:107], v[128:131]\n\t" \
  "buffer_load_dwordx4 v162, %[rx], s76 offen lds\n\t" \
  "v_mfma_f32_16x16x32_bf16 v[132:135], a[108:111], v[104:107], v[132:135]\n\t" \
  "ds_read_b128 v[44:47], v165 offset:41216\n\t" \
  "v_mfma_f32_16x16x32_bf16 v[136:139], a[104:107], v[108:111], v[136:139]\n\t" \
  "ds_read_b128 v[48:51], v166 offset:33024\n\t" \
  "v_mfma_f32_16x16x32_bf16 v[140:143], a[108:111], v[108:111], v[140:143]\n\t" \
  "s_add_i32 m0, s41, 19456\n\t" \
  "v_mfma_f32_16x16x32_bf16 v[128:131], a[112:115], v[112:115], v[128:131]\n\t" \
  "buffer_load_dwordx4 v163, %[rx], s76 offen lds\n\t" \
  "v_mfma_f32_16x16x32_bf16 v[132:135], a[116:119], v[112:115], v[132:135]\n\t" \
  "ds_read_b128 v[52:55], v166 offset:41216\n\t" \
  "v_mfma_f32_16x16x32_bf16 v[136:139], a[112:115], v[116:119], v[136:139]\n\t" \
  "ds_read_b128 v[56:59], v167 offset:33024\n\t" \
  "v_mfma_f32_16x16x32_bf16 v[140:143], a[116:119], v[116:119], v[140:143]\n\t" \
  "ds_read_b128 v[60:63], v167 offset:41216\n\t" \
  "v_mfma_f32_16x16x32_bf16 v[128:131], a[120:123], v[120:123], v[128:131]\n\t" \
  "s_and_b32 s79, s44, 4\n\t" \
  "v_mfma_f32_16x16x32_bf16 v[132:135], a[124:127], v[120:123], v[132:135]\n\t" \
  "s_lshl_b32 s79, s79, 11\n\t" \
  "v_mfma_f32_16x16x32_bf16 v[136:139], a[120:123], v[124:127], v[136:139]\n\t" \
  "v_mfma_f32_16x16x32_bf16 v[140:143], a[124:127], v[124:127], v[140:143]\n\t" \
  "s_cmp_le_u32 s44, 6\n\t" \
  "s_waitcnt vmcnt(29) lgkmcnt(0)\n\t" \
  "v_mfma_f32_16x16x32_bf16 v[128:131], a[128:131], v[0:3], v[128:131]\n\t" \
  "s_cbranch_scc1 32f\n\t" \
  "42:\n\t" \
  "s_barrier\n\t" \
  "v_mfma_f32_16x16x32_bf16 v[132:135], a[132:135], v[0:3], v[132:135]\n\t" \
  "s_add_i32 s76, s40, 1024\n\t" \
  "v_mfma_f32_16x16x32_bf16 v[136:139], a[128:131], v[4:7], v[136:139]\n\t" \
  "ds_read_b128 v[64:67], v164 offset:49152\n\t" \
  "v_mfma_f32_16x16x32_bf16 v[140:143], a[132:135], v[4:7], v[140:143]\n\t" \
  "s_add_i32 m0, s41, 32768\n\t" \
  "v_mfma_f32_16x16x32_bf16 v[128:131], a[136:139], v[8:11], v[128:131]\n\t" \
  "buffer_load_dwordx4 v160, %[rx], s76 offen lds\n\t" \
  "v_mfma_f32_16x16x32_bf16 v[132:135], a[140:143], v[8:11], v[132:135]\n\t" \
  "ds_read_b128 v[68:71], v164 offset:57344\n\t" \
  "v_mfma_f32_16x16x32_bf16 v[136:139], a[136:139], v[12:15], v[136:139]\n\t" \
  "ds_read_b128 v[72:75], v165 offset:49152\n\t" \
  "v_mfma_f32_16x16x32_bf16 v[140:143], a[140:143], v[12:15], v[140:143]\n\t" \
  "ds_read_b128 v[76:79], v165 offset:57344\n\t" \
  "v_mfma_f32_16x16x32_bf16 v[128:131], a[144:147], v[16:19], v[128:131]\n\t" \
  "v_mfma_f32_16x16x32_bf16 v[132:135], a[148:151], v[16:19], v[132:135]\n\t" \
  "ds_read_b128 v[80:83], v166 offset:49152\n\t" \
  "v_mfma_f32_16x16x32_bf16 v[136:139], a[144:147], v[20:23], v[136:139]\n\t" \
  "ds_read_b128 v[84:87], v166 offset:57344\n\t" \
  "v_mfma_f32_16x16x32_bf16 v[140:143], a[148:151], v[20:23], v[140:143]\n\t" \
  "s_add_i32 m0, s41, 33792\n\t" \
  "v_mfma_f32_16x16x32_bf16 v[128:131], a[152:155], v[24:27], v[128:131]\n\t" \
  "buffer_load_dwordx4 v161, %[rx], s76 offen lds\n\t" \
  "v_mfma_f32_16x16x32_bf16 v[132:135], a[156:159], v[24:27], v[132:135]\n\t" \
  "ds_read_b128 v[88:91], v167 offset:49152\n\t" \
  "v_mfma_f32_16x16x32_bf16 v[136:139], a[152:155], v[28:31], v[136:139]\n\t" \
  "ds_read_b128 v[92:95], v167 offset:57344\n\t" \
  "v_mfma_f32_16x16x32_bf16 v[140:143], a[156:159], v[28:31], v[140:143]\n\t" \
  "ds_read_b128 v[96:99], v164 offset:49408\n\t" \
  "v_mfma_f32_16x16x32_bf16 v[128:131], a[160:163], v[32:35], v[128:131]\n\t" \
  "v_mfma_f32_16x16x32_bf16 v[132:135], a[164:167], v[32:35], v[132:135]\n\t" \
  "ds_read_b128 v[100:103], v164 offset:57600\n\t" \
  "v_mfma_f32_16x16x32_bf16 v[136:139], a[160:163], v[36:39], v[136:139]\n\t" \
  "ds_read_b128 v[104:107], v165 offset:49408\n\t" \
  "v_mfma_f32_16x16x32_bf16 v[140:143], a[164:167], v[36:39], v[140:143]\n\t" \
  "s_add_i32 m0, s41, 34816\n\t" \
  "v_mfma_f32_16x16x32_bf16 v[128:131], a[168:171], v[40:43], v[128:131]\n\t" \
  "buffer_load_dwordx4 v162, %[rx], s76 offen lds\n\t" \
  "v_mfma_f32_16x16x32_bf16 v[132:135], a[172:175], v[40:43], v[132:135]\n\t" \
  "ds_read_b128 v[108:111], v165 offset:57600\n\t" \
  "v_mfma_f32_16x16x32_bf16 v[136:139], a[168:171], v[44:47], v[136:139]\n\t" \
  "ds_read_b128 v[112:115], v166 offset:49408\n\t" \
  "v_mfma_f32_16x16x32_bf16 v[140:143], a[172:175], v[44:47], v[140:143]\n\t" \
  "s_add_i32 m0, s41, 35840\n\t" \
  "v_mfma_f32_16x16x32_bf16 v[128:131], a[176:179], v[48:51], v[128:131]\n\t" \
  "buffer_load_dwordx4 v163, %[rx], s76 offen lds\n\t" \
  "v_mfma_f32_16x16x32_bf16 v[132:135], a[180:183], v[48:51], v[132:135]\n\t" \
  "ds_read_b128 v[116:119], v166 offset:57600\n\t" \
  "v_mfma_f32_16x16x32_bf16 v[136:139], a[176:179], v[52:55], v[136:139]\n\t" \
  "ds_read_b128 v[120:123], v167 offset:49408\n\t" \
  "v_mfma_f32_16x16x32_bf16 v[140:143], a[180:183], v[52:55], v[140:143]\n\t" \
  "ds_read_b128 v[124:127], v167 offset:57600\n\t" \
  "v_mfma_f32_16x16x32_bf16 v[128:131], a[184:187], v[56:59], v[128:131]\n\t" \
  "s_add_i32 s78, s44, -2\n\t" \
  "v_mfma_f32_16x16x32_bf16 v[132:135], a[188:191], v[56:59], v[132:135]\n\t" \
  "s_max_i32 s78, s78, s46\n\t" \
  "v_mfma_f32_16x16x32_bf16 v[136:139], a[184:187], v[60:63], v[136:139]\n\t" \
  "v_mov_b32 v156, s78\n\t" \
  "v_mfma_f32_16x16x32_bf16 v[140:143], a[188:191], v[60:63], v[140:143]\n\t" \
  "s_cmp_le_u32 s44, 6\n\t" \
  "s_waitcnt vmcnt(27) lgkmcnt(0)\n\t" \
  "v_mfma_f32_16x16x32_bf16 v[128:131], a[192:195], v[64:67], v[128:131]\n\t" \
  "s_cbranch_scc1 33f\n\t" \
  "43:\n\t" \
  "s_barrier\n\t" \
  "v_xor_b32 v164, 0x10000, v164\n\t" \
  "v_mfma_f32_16x16x32_bf16 v[132:135], a[196:199], v[64:67], v[132:135]\n\t" \
  "s_add_i32 s76, s40, 1536\n\t" \
  "buffer_store_dword v156, v171, %[ru], 0 offen sc1\n\t" \
  "v_mfma_f32_16x16x32_bf16 v[136:139], a[192:195], v[68:71], v[136:139]\n\t" \
  "v_xor_b32 v165, 0x10000, v165\n\t" \
  "ds_read_b128 v[0:3], v164 offset:0\n\t" \
  "v_mfma_f32_16x16x32_bf16 v[140:143], a[196:199], v[68:71], v[140:143]\n\t" \
  "s_add_i32 m0, s41, 49152\n\t" \
  "v_xor_b32 v166, 0x10000, v166\n\t" \
  "v_mfma_f32_16x16x32_bf16 v[128:131], a[200:203], v[72:75], v[128:131]\n\t" \
  "buffer_load_dwordx4 v160, %[rx], s76 offen lds\n\t" \
  "v_xor_b32 v167, 0x10000, v167\n\t" \
  "v_mfma_f32_16x16x32_bf16 v[132:135], a[204:207], v[72:75], v[132:135]\n\t" \
  "ds_read_b128 v[4:7], v164 offset:8192\n\t" \
  "v_mfma_f32_16x16x32_bf16 v[136:139], a[200:203], v[76:79], v[136:139]\n\t" \
  "ds_read_b128 v[8:11], v165 offset:0\n\t" \
  "v_mfma_f32_16x16x32_bf16 v[140:143], a[204:207], v[76:79], v[140:143]\n\t" \
  "ds_read_b128 v[12:15], v165 offset:8192\n\t" \
  "v_mfma_f32_16x16x32_bf16 v[128:131], a[208:211], v[80:83], v[128:131]\n\t" \
  "v_mfma_f32_16x16x32_bf16 v[132:135], a[212:215], v[80:83], v[132:135]\n\t" \
  "ds_read_b128 v[16:19], v166 offset:0\n\t" \
  "v_mfma_f32_16x16x32_bf16 v[136:139], a[208:211], v[84:87], v[136:139]\n\t" \
  "ds_read_b128 v[20:23], v166 offset:8192\n\t" \
  "v_mfma_f32_16x16x32_bf16 v[140:143], a[212:215], v[84:87], v[140:143]\n\t" \
  "s_add_i32 m0, s41, 50176\n\t" \
  "ds_read_b128 v[24:27], v167 offset:0\n\t" \
  "v_mfma_f32_16x16x32_bf16 v[128:131], a[216:219], v[88:91], v[128:131]\n\t" \
  "buffer_load_dwordx4 v161, %[rx], s76 offen lds\n\t" \
  "v_mfma_f32_16x16x32_bf16 v[132:135], a[220:223], v[88:91], v[132:135]\n\t" \
  "ds_read_b128 v[28:31], v167 offset:8192\n\t" \
  "v_mfma_f32_16x16x32_bf16 v[136:139], a[216:219], v[92:95], v[136:139]\n\t" \
  "ds_read_b128 v[32:35], v164 offset:256\n\t" \
  "v_mfma_f32_16x16x32_bf16 v[140:143], a[220:223], v[92:95], v[140:143]\n\t" \
  "ds_read_b128 v[36:39], v164 offset:8448\n\t" \
  "v_mfma_f32_16x16x32_bf16 v[128:131], a[224:227], v[96:99], v[128:131]\n\t" \
  "v_mfma_f32_16x16x32_bf16 v[132:135], a[228:231], v[96:99], v[132:135]\n\t" \
  "ds_read_b128 v[40:43], v165 offset:256\n\t" \
  "v_mfma_f32_16x16x32_bf16 v[136:139], a[224:227], v[100:103], v[136:139]\n\t" \
  "ds_read_b128 v[44:47], v165 offset:8448\n\t" \
  "v_mfma_f32_16x16x32_bf16 v[140:143], a[228:231], v[100:103], v[140:143]\n\t" \
  "s_add_i32 m0, s41, 51200\n\t" \
  "v_mfma_f32_16x16x32_bf16 v[128:131], a[232:235], v[104:107], v[128:131]\n\t" \
  "buffer_load_dwordx4 v162, %[rx], s76 offen lds\n\t" \
  "v_mfma_f32_16x16x32_bf16 v[132:135], a[236:239], v[104:107], v[132:135]\n\t" \
  "ds_read_b128 v[48:51], v166 offset:256\n\t" \
  "v_mfma_f32_16x16x32_bf16 v[136:139], a[232:235], v[108:111], v[136:139]\n\t" \
  "ds_read_b128 v[52:55], v166 offset:8448\n\t" \
  "v_mfma_f32_16x16x32_bf16 v[140:143], a[236:239], v[108:111], v[140:143]\n\t" \
  "s_add_i32 m0, s41, 52224\n\t" \
  "v_mfma_f32_16x16x32_bf16 v[128:131], a[240:243], v[112:115], v[128:131]\n\t" \
  "buffer_load_dwordx4 v163, %[rx], s76 offen lds\n\t" \
  "v_mfma_f32_16x16x32_bf16 v[132:135], a[244:247], v[112:115], v[132:135]\n\t" \
  "ds_read_b128 v[56:59], v167 offset:256\n\t" \
  "v_mfma_f32_16x16x32_bf16 v[136:139], a[240:243], v[116:119], v[136:139]\n\t" \
  "ds_read_b128 v[60:63], v167 offset:8448\n\t" \
  "v_mfma_f32_16x16x32_bf16 v[140:143], a[244:247], v[116:119], v[140:143]\n\t" \
  "s_add_i32 s44, s44, 1\n\t" \
  "s_xor_b32 s42, s43, s79\n\t" \
  "v_mfma_f32_16x16x32_bf16 v[128:131], a[248:251], v[120:123], v[128:131]\n\t" \
  "s_add_i32 s43, s43, 0x80000\n\t" \
  "s_add_i32 s40, s40, 0x10000\n\t" \
  "v_mfma_f32_16x16x32_bf16 v[132:135], a[252:255], v[120:123], v[132:135]\n\t" \
  "s_xor_b32 s41, s41, 0x10000\n\t" \
  "v_mfma_f32_16x16x32_bf16 v[136:139], a[248:251], v[124:127], v[136:139]\n\t" \
  "v_cvt_pk_bf16_f32 v144, v128, v129\n\t" \
  "v_cvt_pk_bf16_f32 v145, v130, v131\n\t" \
  "v_mfma_f32_16x16x32_bf16 v[140:143], a[252:255], v[124:127], v[140:143]\n\t" \
  "v_cvt_pk_bf16_f32 v146, v132, v133\n\t" \
  "v_cvt_pk_bf16_f32 v147, v134, v135\n\t" \
  "s_cmp_eq_u32 s44, 64\n\t" \
  "s_cbranch_scc1 98f\n\t" \
  "s_cmp_le_u32 s44, 6\n\t" \
  "s_branch 20b\n\t" \
  "30:\n\t" \
  "s_waitcnt vmcnt(24)\n\t" \
  "s_branch 40b\n\t" \
  "31:\n\t" \
  "s_waitcnt vmcnt(24)\n\t" \
  "s_branch 41b\n\t" \
  "32:\n\t" \
  "s_waitcnt vmcnt(6)\n\t" \
  "s_barrier\n\t" \
  "s_add_i32 s46, s44, -1\n\t" \
  "v_mov_b32 v156, s46\n\t" \
  "buffer_store_dword v156, v171, %[ru], 0 offen sc1\n\t" \
  "s_branch 42b\n\t" \
  "33:\n\t" \
  "s_waitcnt vmcnt(24)\n\t" \
  "s_branch 43b\n\t" \
  "92:\n\t" \
  "ds_read_b128 v[0:3], v164 offset:0\n\t" \
  "ds_read_b128 v[4:7], v164 offset:8192\n\t" \
  "ds_read_b128 v[8:11], v165 offset:0\n\t" \
  "ds_read_b128 v[12:15], v165 offset:8192\n\t" \
  "ds_read_b128 v[16:19], v166 offset:0\n\t" \
  "ds_read_b128 v[20:23], v166 offset:8192\n\t" \
  "ds_read_b128 v[24:27], v167 offset:0\n\t" \
  "ds_read_b128 v[28:31], v167 offset:8192\n\t" \
  "ds_read_b128 v[32:35], v164 offset:256\n\t" \
  "ds_read_b128 v[36:39], v164 offset:8448\n\t" \
  "ds_read_b128 v[40:43], v165 offset:256\n\t" \
  "ds_read_b128 v[44:47], v165 offset:8448\n\t" \
  "ds_read_b128 v[48:51], v166 offset:256\n\t" \
  "ds_read_b128 v[52:55], v166 offset:8448\n\t" \
  "ds_read_b128 v[56:59], v167 offset:256\n\t" \
  "ds_read_b128 v[60:63], v167 offset:8448\n\t" \
  "v_mov_b64 v[152:153], -1\n\t" \
  "v_mov_b64 v[154:155], -1\n\t" \
  "s_cmp_le_u32 s44, 6\n\t" \
  ".p2align 6\n\t" \
  "20:\n\t" \
  "s_waitcnt vmcnt(28) lgkmcnt(0)\n\t" \
  "v_mfma_f32_16x16x32_bf16 v[128:131], a[0:3], v[0:3], 0\n\t" \
  "s_cbranch_scc1 30f\n\t" \
  "40:\n\t" \
  "s_barrier\n\t" \
  "v_cvt_pk_bf16_f32 v148, v136, v137\n\t" \
  "v_cvt_pk_bf16_f32 v149, v138, v139\n\t" \
  "v_mfma_f32_16x16x32_bf16 v[132:135], a[4:7], v[0:3], 0\n\t" \
  "v_cvt_pk_bf16_f32 v150, v140, v141\n\t" \
  "v_cvt_pk_bf16_f32 v151, v142, v143\n\t" \
  "v_mfma_f32_16x16x32_bf16 v[136:139], a[0:3], v[4:7], 0\n\t" \
  "ds_read_b128 v[64:67], v164 offset:16384\n\t" \
  "v_mfma_f32_16x16x32_bf16 v[140:143], a[4:7], v[4:7], 0\n\t" \
  "ds_read_b128 v[68:71], v164 offset:24576\n\t" \
  "v_mfma_f32_16x16x32_bf16 v[128:131], a[8:11], v[8:11], v[128:131]\n\t" \
  "s_add_i32 m0, s41, 0\n\t" \
  "v_mfma_f32_16x16x32_bf16 v[132:135], a[12:15], v[8:11], v[132:135]\n\t" \
  "buffer_load_dwordx4 v160, %[rx], s40 offen lds\n\t" \
  "v_mfma_f32_16x16x32_bf16 v[136:139], a[8:11], v[12:15], v[136:139]\n\t" \
  "ds_read_b128 v[72:75], v165 offset:16384\n\t" \
  "v_mfma_f32_16x16x32_bf16 v[140:143], a[12:15], v[12:15], v[140:143]\n\t" \
  "ds_read_b128 v[76:79], v165 offset:24576\n\t" \
  "v_mfma_f32_16x16x32_bf16 v[128:131], a[16:19], v[16:19], v[128:131]\n\t" \
  "ds_read_b128 v[80:83], v166 offset:16384\n\t" \
  "v_mfma_f32_16x16x32_bf16 v[132:135], a[20:23], v[16:19], v[132:135]\n\t" \
  "buffer_store_dwordx4 v[144:147], v168, %[ru], s42 offen sc1\n\t" \
  "v_mfma_f32_16x16x32_bf16 v[136:139], a[16:19], v[20:23], v[136:139]\n\t" \
  "ds_read_b128 v[84:87], v166 offset:24576\n\t" \
  "v_mfma_f32_16x16x32_bf16 v[140:143], a[20:23], v[20:23], v[140:143]\n\t" \
  "ds_read_b128 v[88:91], v167 offset:16384\n\t" \
  "v_mfma_f32_16x16x32_bf16 v[128:131], a[24:27], v[24:27], v[128:131]\n\t" \
  "s_add_i32 m0, s41, 1024\n\t" \
  "v_mfma_f32_16x16x32_bf16 v[132:135], a[28:31], v[24:27], v[132:135]\n\t" \
  "buffer_load_dwordx4 v161, %[rx], s40 offen lds\n\t" \
  "v_mfma_f32_16x16x32_bf16 v[136:139], a[24:27], v[28:31], v[136:139]\n\t" \
  "ds_read_b128 v[92:95], v167 offset:24576\n\t" \
  "v_mfma_f32_16x16x32_bf16 v[140:143], a[28:31], v[28:31], v[140:143]\n\t" \
  "ds_read_b128 v[96:99], v164 offset:16640\n\t" \
  "v_mfma_f32_16x16x32_bf16 v[128:131], a[32:35], v[32:35], v[128:131]\n\t" \
  "ds_read_b128 v[100:103], v164 offset:24832\n\t" \
  "v_mfma_f32_16x16x32_bf16 v[132:135], a[36:39], v[32:35], v[132:135]\n\t" \
  "buffer_store_dwordx4 v[148:151], v169, %[ru], s42 offen sc1\n\t" \
  "v_mfma_f32_16x16x32_bf16 v[136:139], a[32:35], v[36:39], v[136:139]\n\t" \
  "ds_read_b128 v[104:107], v165 offset:16640\n\t" \
  "v_mfma_f32_16x16x32_bf16 v[140:143], a[36:39], v[36:39], v[140:143]\n\t" \
  "ds_read_b128 v[108:111], v165 offset:24832\n\t" \
  "v_mfma_f32_16x16x32_bf16 v[128:131], a[40:43], v[40:43], v[128:131]\n\t" \
  "s_add_i32 m0, s41, 2048\n\t" \
  "v_mfma_f32_16x16x32_bf16 v[132:135], a[44:47], v[40:43], v[132:135]\n\t" \
  "buffer_load_dwordx4 v162, %[rx], s40 offen lds\n\t" \
  "v_mfma_f32_16x16x32_bf16 v[136:139], a[40:43], v[44:47], v[136:139]\n\t" \
  "ds_read_b128 v[112:115], v166 offset:16640\n\t" \
  "v_mfma_f32_16x16x32_bf16 v[140:143], a[44:47], v[44:47], v[140:143]\n\t" \
  "ds_read_b128 v[116:119], v166 offset:24832\n\t" \
  "v_mfma_f32_16x16x32_bf16 v[128:131], a[48:51], v[48:51], v[128:131]\n\t" \
  "s_add_i32 m0, s41, 3072\n\t" \
  "v_mfma_f32_16x16x32_bf16 v[132:135], a[52:55], v[48:51], v[132:135]\n\t" \
  "buffer_load_dwordx4 v163, %[rx], s40 offen lds\n\t" \
  "v_mfma_f32_16x16x32_bf16 v[136:139], a[48:51], v[52:55], v[136:139]\n\t" \
  "ds_read_b128 v[120:123], v167 offset:16640\n\t" \
  "v_mfma_f32_16x16x32_bf16 v[140:143], a[52:55], v[52:55], v[140:143]\n\t" \
  "ds_read_b128 v[124:127], v167 offset:24832\n\t" \
  "v_mfma_f32_16x16x32_bf16 v[128:131], a[56:59], v[56:59], v[128:131]\n\t" \
  "v_mfma_f32_16x16x32_bf16 v[132:135], a[60:63], v[56:59], v[132:135]\n\t" \
  "v_mfma_f32_16x16x32_bf16 v[136:139], a[56:59], v[60:63], v[136:139]\n\t" \
  "v_mfma_f32_16x16x32_bf16 v[140:143], a[60:63], v[60:63], v[140:143]\n\t" \
  "s_cmp_le_u32 s44, 6\n\t" \
  "s_waitcnt vmcnt(30) lgkmcnt(0)\n\t" \
  "v_mfma_f32_16x16x32_bf16 v[128:131], a[64:67], v[64:67], v[128:131]\n\t" \
  "s_cbranch_scc1 31f\n\t" \
  "41:\n\t" \
  "s_barrier\n\t" \
  "v_mfma_f32_16x16x32_bf16 v[132:135], a[68:71], v[64:67], v[132:135]\n\t" \
  "s_add_i32 s76, s40, 512\n\t" \
  "v_mfma_f32_16x16x32_bf16 v[136:139], a[64:67], v[68:71], v[136:139]\n\t" \
  "ds_read_b128 v[0:3], v164 offset:32768\n\t" \
  "v_mfma_f32_16x16x32_bf16 v[140:143], a[68:71], v[68:71], v[140:143]\n\t" \
  "ds_read_b128 v[4:7], v164 offset:40960\n\t" \
  "v_mfma_f32_16x16x32_bf16 v[128:131], a[72:75], v[72:75], v[128:131]\n\t" \
  "s_add_i32 m0, s41, 16384\n\t" \
  "v_mfma_f32_16x16x32_bf16 v[132:135], a[76:79], v[72:75], v[132:135]\n\t" \
  "buffer_load_dwordx4 v160, %[rx], s76 offen lds\n\t" \
  "v_mfma_f32_16x16x32_bf16 v[136:139], a[72:75], v[76:79], v[136:139]\n\t" \
  "ds_read_b128 v[8:11], v165 offset:32768\n\t" \
  "v_mfma_f32_16x16x32_bf16 v[140:143], a[76:79], v[76:79], v[140:143]\n\t" \
  "ds_read_b128 v[12:15], v165 offset:40960\n\t" \
  "v_mfma_f32_16x16x32_bf16 v[128:131], a[80:83], v[80:83], v[128:131]\n\t" \
  "ds_read_b128 v[16:19], v166 offset:32768\n\t" \
  "v_mfma_f32_16x16x32_bf16 v[132:135], a[84:87], v[80:83], v[132:135]\n\t" \
  "v_mfma_f32_16x16x32_bf16 v[136:139], a[80:83], v[84:87], v[136:139]\n\t" \
  "ds_read_b128 v[20:23], v166 offset:40960\n\t" \
  "v_mfma_f32_16x16x32_bf16 v[140:143], a[84:87], v[84:87], v[140:143]\n\t" \
  "ds_read_b128 v[24:27], v167 offset:32768\n\t" \
  "v_mfma_f32_16x16x32_bf16 v[128:131], a[88:91], v[88:91], v[128:131]\n\t" \
  "s_add_i32 m0, s41, 17408\n\t" \
  "v_mfma_f32_16x16x32_bf16 v[132:135], a[92:95], v[88:91], v[132:135]\n\t" \
  "buffer_load_dwordx4 v161, %[rx], s76 offen lds\n\t" \
  "v_mfma_f32_16x16x32_bf16 v[136:139], a[88:91], v[92:95], v[136:139]\n\t" \
  "ds_read_b128 v[28:31], v167 offset:40960\n\t" \
  "v_mfma_f32_16x16x32_bf16 v[140:143], a[92:95], v[92:95], v[140:143]\n\t" \
  "ds_read_b128 v[32:35], v164 offset:33024\n\t" \
  "v_mfma_f32_16x16x32_bf16 v[128:131], a[96:99], v[96:99], v[128:131]\n\t" \
  "ds_read_b128 v[36:39], v164 offset:41216\n\t" \
  "v_mfma_f32_16x16x32_bf16 v[132:135], a[100:103], v[96:99], v[132:135]\n\t" \
  "v_mfma_f32_16x16x32_bf16 v[136:139], a[96:99], v[100:103], v[136:139]\n\t" \
  "ds_read_b128 v[40:43], v165 offset:33024\n\t" \
  "v_mfma_f32_16x16x32_bf16 v[140:143], a[100:103], v[100:103], v[140:143]\n\t" \
  "ds_read_b128 v[44:47], v165 offset:41216\n\t" \
  "v_mfma_f32_16x16x32_bf16 v[128:131], a[104:107], v[104:107], v[128:131]\n\t" \
  "s_add_i32 m0, s41, 18432\n\t" \
  "v_mfma_f32_16x16x32_bf16 v[132:135], a[108:111], v[104:107], v[132:135]\n\t" \
  "buffer_load_dwordx4 v162, %[rx], s76 offen lds\n\t" \
  "v_mfma_f32_16x16x32_bf16 v[136:139], a[104:107], v[108:111], v[136:139]\n\t" \
  "ds_read_b128 v[48:51], v166 offset:33024\n\t" \
  "v_mfma_f32_16x16x32_bf16 v[140:143], a[108:111], v[108:111], v[140:143]\n\t" \
  "ds_read_b128 v[52:55], v166 offset:41216\n\t" \
  "v_mfma_f32_16x16x32_bf16 v[128:131], a[112:115], v[112:115], v[128:131]\n\t" \
  "s_add_i32 m0, s41, 19456\n\t" \
  "v_mfma_f32_16x16x32_bf16 v[132:135], a[116:119], v[112:115], v[132:135]\n\t" \
  "buffer_load_dwordx4 v163, %[rx], s76 offen lds\n\t" \
  "v_mfma_f32_16x16x32_bf16 v[136:139], a[112:115], v[116:119], v[136:139]\n\t" \
  "ds_read_b128 v[56:59], v167 offset:33024\n\t" \
  "v_mfma_f32_16x16x32_bf16 v[140:143], a[116:119], v[116:119], v[140:143]\n\t" \
  "ds_read_b128 v[60:63], v167 offset:41216\n\t" \
  "v_mfma_f32_16x16x32_bf16 v[128:131], a[120:123], v[120:123], v[128:131]\n\t" \
  "s_and_b32 s79, s44, 4\n\t" \
  "v_mfma_f32_16x16x32_bf16 v[132:135], a[124:127], v[120:123], v[132:135]\n\t" \
  "s_lshl_b32 s79, s79, 11\n\t" \
  "v_mfma_f32_16x16x32_bf16 v[136:139], a[120:123], v[124:127], v[136:139]\n\t" \
  "v_mfma_f32_16x16x32_bf16 v[140:143], a[124:127], v[124:127], v[140:143]\n\t" \
  "s_cmp_le_u32 s44, 6\n\t" \
  "s_waitcnt vmcnt(29) lgkmcnt(0)\n\t" \
  "v_mfma_f32_16x16x32_bf16 v[128:131], a[128:131], v[0:3], v[128:131]\n\t" \
  "s_cbranch_scc1 32f\n\t" \
  "42:\n\t" \
  "s_barrier\n\t" \
  "v_mfma_f32_16x16x32_bf16 v[132:135], a[132:135], v[0:3], v[132:135]\n\t" \
  "s_add_i32 s76, s40, 1024\n\t" \
  "v_mfma_f32_16x16x32_bf16 v[136:139], a[128:131], v[4:7], v[136:139]\n\t" \
  "ds_read_b128 v[64:67], v164 offset:49152\n\t" \
  "v_mfma_f32_16x16x32_bf16 v[140:143], a[132:135], v[4:7], v[140:143]\n\t" \
  "ds_read_b128 v[68:71], v164 offset:57344\n\t" \
  "v_mfma_f32_16x16x32_bf16 v[128:131], a[136:139], v[8:11], v[128:131]\n\t" \
  "s_add_i32 m0, s41, 32768\n\t" \
  "v_mfma_f32_16x16x32_bf16 v[132:135], a[140:143], v[8:11], v[132:135]\n\t" \
  "buffer_load_dwordx4 v160, %[rx], s76 offen lds\n\t" \
  "v_mfma_f32_16x16x32_bf16 v[136:139], a[136:139], v[12:15], v[136:139]\n\t" \
  "ds_read_b128 v[72:75], v165 offset:49152\n\t" \
  "v_mfma_f32_16x16x32_bf16 v[140:143], a[140:143], v[12:15], v[140:143]\n\t" \
  "ds_read_b128 v[76:79], v165 offset:57344\n\t" \
  "v_mfma_f32_16x16x32_bf16 v[128:131], a[144:147], v[16:19], v[128:131]\n\t" \
  "ds_read_b128 v[80:83], v166 offset:49152\n\t" \
  "v_mfma_f32_16x16x32_bf16 v[132:135], a[148:151], v[16:19], v[132:135]\n\t" \
  "v_mfma_f32_16x16x32_bf16 v[136:139], a[144:147], v[20:23], v[136:139]\n\t" \
  "ds_read_b128 v[84:87], v166 offset:57344\n\t" \
  "v_mfma_f32_16x16x32_bf16 v[140:143], a[148:151], v[20:23], v[140:143]\n\t" \
  "ds_read_b128 v[88:91], v167 offset:49152\n\t" \
  "v_mfma_f32_16x16x32_bf16 v[128:131], a[152:155], v[24:27], v[128:131]\n\t" \
  "s_add_i32 m0, s41, 33792\n\t" \
  "v_mfma_f32_16x16x32_bf16 v[132:135], a[156:159], v[24:27], v[132:135]\n\t" \
  "buffer_load_dwordx4 v161, %[rx], s76 offen lds\n\t" \
  "v_mfma_f32_16x16x32_bf16 v[136:139], a[152:155], v[28:31], v[136:139]\n\t" \
  "ds_read_b128 v[92:95], v167 offset:57344\n\t" \
  "v_mfma_f32_16x16x32_bf16 v[140:143], a[156:159], v[28:31], v[140:143]\n\t" \
  "ds_read_b128 v[96:99], v164 offset:49408\n\t" \
  "v_mfma_f32_16x16x32_bf16 v[128:131], a[160:163], v[32:35], v[128:131]\n\t" \
  "ds_read_b128 v[100:103], v164 offset:57600\n\t" \
  "v_mfma_f32_16x16x32_bf16 v[132:135], a[164:167], v[32:35], v[132:135]\n\t" \
  "v_mfma_f32_16x16x32_bf16 v[136:139], a[160:163], v[36:39], v[136:139]\n\t" \
  "ds_read_b128 v[104:107], v165 offset:49408\n\t" \
  "v_mfma_f32_16x16x32_bf16 v[140:143], a[164:167], v[36:39], v[140:143]\n\t" \
  "ds_read_b128 v[108:111], v165 offset:57600\n\t" \
  "v_mfma_f32_16x16x32_bf16 v[128:131], a[168:171], v[40:43], v[128:131]\n\t" \
  "s_add_i32 m0, s41, 34816\n\t" \
  "v_mfma_f32_16x16x32_bf16 v[132:135], a[172:175], v[40:43], v[132:135]\n\t" \
  "buffer_load_dwordx4 v162, %[rx], s76 offen lds\n\t" \
  "v_mfma_f32_16x16x32_bf16 v[136:139], a[168:171], v[44:47], v[136:139]\n\t" \
  "ds_read_b128 v[112:115], v166 offset:49408\n\t" \
  "v_mfma_f32_16x16x32_bf16 v[140:143], a[172:175], v[44:47], v[140:143]\n\t" \
  "ds_read_b128 v[116:119], v166 offset:57600\n\t" \
  "v_mfma_f32_16x16x32_bf16 v[128:131], a[176:179], v[48:51], v[128:131]\n\t" \
  "s_add_i32 m0, s41, 35840\n\t" \
  "v_mfma_f32_16x16x32_bf16 v[132:135], a[180:183], v[48:51], v[132:135]\n\t" \
  "buffer_load_dwordx4 v163, %[rx], s76 offen lds\n\t" \
  "v_mfma_f32_16x16x32_bf16 v[136:139], a[176:179], v[52:55], v[136:139]\n\t" \
  "ds_read_b128 v[120:123], v167 offset:49408\n\t" \
  "v_mfma_f32_16x16x32_bf16 v[140:143], a[180:183], v[52:55], v[140:143]\n\t" \
  "ds_read_b128 v[124:127], v167 offset:57600\n\t" \
  "v_mfma_f32_16x16x32_bf16 v[128:131], a[184:187], v[56:59], v[128:131]\n\t" \
  "s_add_i32 s78, s44, -2\n\t" \
  "v_mfma_f32_16x16x32_bf16 v[132:135], a[188:191], v[56:59], v[132:135]\n\t" \
  "s_max_i32 s78, s78, s46\n\t" \
  "v_mfma_f32_16x16x32_bf16 v[136:139], a[184:187], v[60:63], v[136:139]\n\t" \
  "v_mov_b32 v156, s78\n\t" \
  "v_mfma_f32_16x16x32_bf16 v[140:143], a[188:191], v[60:63], v[140:143]\n\t" \
  "s_cmp_le_u32 s44, 6\n\t" \
  "s_waitcnt vmcnt(27) lgkmcnt(0)\n\t" \
  "v_mfma_f32_16x16x32_bf16 v[128:131], a[192:195], v[64:67], v[128:131]\n\t" \
  "s_cbranch_scc1 33f\n\t" \
  "43:\n\t" \
  "s_barrier\n\t" \
  "v_xor_b32 v164, 0x10000, v164\n\t" \
  "v_mfma_f32_16x16x32_bf16 v[132:135], a[196:199], v[64:67], v[132:135]\n\t" \
  "s_add_i32 s76, s40, 1536\n\t" \
  "buffer_store_dword v156, v171, %[ru], 0 offen sc1\n\t" \
  "v_mfma_f32_16x16x32_bf16 v[136:139], a[192:195], v[68:71], v[136:139]\n\t" \
  "v_xor_b32 v165, 0x10000, v165\n\t" \
  "ds_read_b128 v[0:3], v164 offset:0\n\t" \
  "v_mfma_f32_16x16x32_bf16 v[140:143], a[196:199], v[68:71], v[140:143]\n\t" \
  "v_xor_b32 v166, 0x10000, v166\n\t" \
  "ds_read_b128 v[4:7], v164 offset:8192\n\t" \
  "v_mfma_f32_16x16x32_bf16 v[128:131], a[200:203], v[72:75], v[128:131]\n\t" \
  "s_add_i32 m0, s41, 49152\n\t" \
  "v_xor_b32 v167, 0x10000, v167\n\t" \
  "v_mfma_f32_16x16x32_bf16 v[132:135], a[204:207], v[72:75], v[132:135]\n\t" \
  "buffer_load_dwordx4 v160, %[rx], s76 offen lds\n\t" \
  "v_mfma_f32_16x16x32_bf16 v[136:139], a[200:203], v[76:79], v[136:139]\n\t" \
  "ds_read_b128 v[8:11], v165 offset:0\n\t" \
  "v_mfma_f32_16x16x32_bf16 v[140:143], a[204:207], v[76:79], v[140:143]\n\t" \
  "ds_read_b128 v[12:15], v165 offset:8192\n\t" \
  "v_mfma_f32_16x16x32_bf16 v[128:131], a[208:211], v[80:83], v[128:131]\n\t" \
  "ds_read_b128 v[16:19], v166 offset:0\n\t" \
  "v_mfma_f32_16x16x32_bf16 v[132:135], a[212:215], v[80:83], v[132:135]\n\t" \
  "v_mfma_f32_16x16x32_bf16 v[136:139], a[208:211], v[84:87], v[136:139]\n\t" \
  "ds_read_b128 v[20:23], v166 offset:8192\n\t" \
  "v_mfma_f32_16x16x32_bf16 v[140:143], a[212:215], v[84:87], v[140:143]\n\t" \
  "ds_read_b128 v[24:27], v167 offset:0\n\t" \
  "v_mfma_f32_16x16x32_bf16 v[128:131], a[216:219], v[88:91], v[128:131]\n\t" \
  "s_add_i32 m0, s41, 50176\n\t" \
  "ds_read_b128 v[28:31], v167 offset:8192\n\t" \
  "v_mfma_f32_16x16x32_bf16 v[132:135], a[220:223], v[88:91], v[132:135]\n\t" \
  "buffer_load_dwordx4 v161, %[rx], s76 offen lds\n\t" \
  "v_mfma_f32_16x16x32_bf16 v[136:139], a[216:219], v[92:95], v[136:139]\n\t" \
  "ds_read_b128 v[32:35], v164 offset:256\n\t" \
  "v_mfma_f32_16x16x32_bf16 v[140:143], a[220:223], v[92:95], v[140:143]\n\t" \
  "ds_read_b128 v[36:39], v164 offset:8448\n\t" \
  "v_mfma_f32_16x16x32_bf16 v[128:131], a[224:227], v[96:99], v[128:131]\n\t" \
  "ds_read_b128 v[40:43], v165 offset:256\n\t" \
  "v_mfma_f32_16x16x32_bf16 v[132:135], a[228:231], v[96:99], v[132:135]\n\t" \
  "v_mfma_f32_16x16x32_bf16 v[136:139], a[224:227], v[100:103], v[136:139]\n\t" \
  "ds_read_b128 v[44:47], v165 offset:8448\n\t" \
  "v_mfma_f32_16x16x32_bf16 v[140:143], a[228:231], v[100:103], v[140:143]\n\t" \
  "ds_read_b128 v[48:51], v166 offset:256\n\t" \
  "v_mfma_f32_16x16x32_bf16 v[128:131], a[232:235], v[104:107], v[128:131]\n\t" \
  "s_add_i32 m0, s41, 51200\n\t" \
  "v_mfma_f32_16x16x32_bf16 v[132:135], a[236:239], v[104:107], v[132:135]\n\t" \
  "buffer_load_dwordx4 v162, %[rx], s76 offen lds\n\t" \
  "v_mfma_f32_16x16x32_bf16 v[136:139], a[232:235], v[108:111], v[136:139]\n\t" \
  "ds_read_b128 v[52:55], v166 offset:8448\n\t" \
  "v_mfma_f32_16x16x32_bf16 v[140:143], a[236:239], v[108:111], v[140:143]\n\t" \
  "ds_read_b128 v[56:59], v167 offset:256\n\t" \
  "v_mfma_f32_16x16x32_bf16 v[128:131], a[240:243], v[112:115], v[128:131]\n\t" \
  "s_add_i32 m0, s41, 52224\n\t" \
  "v_mfma_f32_16x16x32_bf16 v[132:135], a[244:247], v[112:115], v[132:135]\n\t" \
  "buffer_load_dwordx4 v163, %[rx], s76 offen lds\n\t" \
  "v_mfma_f32_16x16x32_bf16 v[136:139], a[240:243], v[116:119], v[136:139]\n\t" \
  "ds_read_b128 v[60:63], v167 offset:8448\n\t" \
  "v_mfma_f32_16x16x32_bf16 v[140:143], a[244:247], v[116:119], v[140:143]\n\t" \
  "s_add_i32 s44, s44, 1\n\t" \
  "s_xor_b32 s42, s43, s79\n\t" \
  "v_mfma_f32_16x16x32_bf16 v[128:131], a[248:251], v[120:123], v[128:131]\n\t" \
  "s_add_i32 s43, s43, 0x80000\n\t" \
  "s_add_i32 s40, s40, 0x10000\n\t" \
  "v_mfma_f32_16x16x32_bf16 v[132:135], a[252:255], v[120:123], v[132:135]\n\t" \
  "s_xor_b32 s41, s41, 0x10000\n\t" \
  "v_mfma_f32_16x16x32_bf16 v[136:139], a[248:251], v[124:127], v[136:139]\n\t" \
  "v_cvt_pk_bf16_f32 v144, v128, v129\n\t" \
  "v_cvt_pk_bf16_f32 v145, v130, v131\n\t" \
  "v_mfma_f32_16x16x32_bf16 v[140:143], a[252:255], v[124:127], v[140:143]\n\t" \
  "v_cvt_pk_bf16_f32 v146, v132, v133\n\t" \
  "v_cvt_pk_bf16_f32 v147, v134, v135\n\t" \
  "s_cmp_eq_u32 s44, 64\n\t" \
  "s_cbranch_scc1 98f\n\t" \
  "s_cmp_le_u32 s44, 6\n\t" \
  "s_branch 20b\n\t" \
  "30:\n\t" \
  "s_waitcnt vmcnt(24)\n\t" \
  "s_branch 40b\n\t" \
  "31:\n\t" \
  "s_waitcnt vmcnt(24)\n\t" \
  "s_branch 41b\n\t" \
  "32:\n\t" \
  "s_waitcnt vmcnt(6)\n\t" \
  "s_barrier\n\t" \
  "s_add_i32 s46, s44, -1\n\t" \
  "v_mov_b32 v156, s46\n\t" \
  "buffer_store_dword v156, v171, %[ru], 0 offen sc1\n\t" \
  "s_branch 42b\n\t" \
  "33:\n\t" \
  "s_waitcnt vmcnt(24)\n\t" \
  "s_branch 43b\n\t" \
  "93:\n\t" \
  "ds_read_b128 v[0:3], v164 offset:0\n\t" \
  "ds_read_b128 v[4:7], v164 offset:8192\n\t" \
  "ds_read_b128 v[8:11], v165 offset:0\n\t" \
  "ds_read_b128 v[12:15], v165 offset:8192\n\t" \
  "ds_read_b128 v[16:19], v166 offset:0\n\t" \
  "ds_read_b128 v[20:23], v166 offset:8192\n\t" \
  "ds_read_b128 v[24:27], v167 offset:0\n\t" \
  "ds_read_b128 v[28:31], v167 offset:8192\n\t" \
  "ds_read_b128 v[32:35], v164 offset:256\n\t" \
  "ds_read_b128 v[36:39], v164 offset:8448\n\t" \
  "ds_read_b128 v[40:43], v165 offset:256\n\t" \
  "ds_read_b128 v[44:47], v165 offset:8448\n\t" \
  "ds_read_b128 v[48:51], v166 offset:256\n\t" \
  "ds_read_b128 v[52:55], v166 offset:8448\n\t" \
  "ds_read_b128 v[56:59], v167 offset:256\n\t" \
  "ds_read_b128 v[60:63], v167 offset:8448\n\t" \
  "v_mov_b64 v[152:153], -1\n\t" \
  "v_mov_b64 v[154:155], -1\n\t" \
  "s_cmp_le_u32 s44, 6\n\t" \
  ".p2align 6\n\t" \
  "20:\n\t" \
  "s_waitcnt vmcnt(28) lgkmcnt(0)\n\t" \
  "v_mfma_f32_16x16x32_bf16 v[128:131], a[0:3], v[0:3], 0\n\t" \
  "s_cbranch_scc1 30f\n\t" \
  "40:\n\t" \
  "s_barrier\n\t" \
  "v_cvt_pk_bf16_f32 v148, v136, v137\n\t" \
  "v_cvt_pk_bf16_f32 v149, v138, v139\n\t" \
  "v_mfma_f32_16x16x32_bf16 v[132:135], a[4:7], v[0:3], 0\n\t" \
  "v_cvt_pk_bf16_f32 v150, v140, v141\n\t" \
  "v_cvt_pk_bf16_f32 v151, v142, v143\n\t" \
  "v_mfma_f32_16x16x32_bf16 v[136:139], a[0:3], v[4:7], 0\n\t" \
  "ds_read_b128 v[64:67], v164 offset:16384\n\t" \
  "v_mfma_f32_16x16x32_bf16 v[140:143], a[4:7], v[4:7], 0\n\t" \
  "ds_read_b128 v[68:71], v164 offset:24576\n\t" \
  "v_mfma_f32_16x16x32_bf16 v[128:131], a[8:11], v[8:11], v[128:131]\n\t" \
  "ds_read_b128 v[72:75], v165 offset:16384\n\t" \
  "v_mfma_f32_16x16x32_bf16 v[132:135], a[12:15], v[8:11], v[132:135]\n\t" \
  "s_add_i32 m0, s41, 0\n\t" \
  "v_mfma_f32_16x16x32_bf16 v[136:139], a[8:11], v[12:15], v[136:139]\n\t" \
  "buffer_load_dwordx4 v160, %[rx], s40 offen lds\n\t" \
  "v_mfma_f32_16x16x32_bf16 v[140:143], a[12:15], v[12:15], v[140:143]\n\t" \
  "ds_read_b128 v[76:79], v165 offset:24576\n\t" \
  "v_mfma_f32_16x16x32_bf16 v[128:131], a[16:19], v[16:19], v[128:131]\n\t" \
  "ds_read_b128 v[80:83], v166 offset:16384\n\t" \
  "v_mfma_f32_16x16x32_bf16 v[132:135], a[20:23], v[16:19], v[132:135]\n\t" \
  "ds_read_b128 v[84:87], v166 offset:24576\n\t" \
  "v_mfma_f32_16x16x32_bf16 v[136:139], a[16:19], v[20:23], v[136:139]\n\t" \
  "buffer_store_dwordx4 v[144:147], v168, %[ru], s42 offen sc1\n\t" \
  "v_mfma_f32_16x16x32_bf16 v[140:143], a[20:23], v[20:23], v[140:143]\n\t" \
  "ds_read_b128 v[88:91], v167 offset:16384\n\t" \
  "v_mfma_f32_16x16x32_bf16 v[128:131], a[24:27], v[24:27], v[128:131]\n\t" \
  "ds_read_b128 v[92:95], v167 offset:24576\n\t" \
  "v_mfma_f32_16x16x32_bf16 v[132:135], a[28:31], v[24:27], v[132:135]\n\t" \
  "s_add_i32 m0, s41, 1024\n\t" \
  "v_mfma_f32_16x16x32_bf16 v[136:139], a[24:27], v[28:31], v[136:139]\n\t" \
  "buffer_load_dwordx4 v161, %[rx], s40 offen lds\n\t" \
  "v_mfma_f32_16x16x32_bf16 v[140:143], a[28:31], v[28:31], v[140:143]\n\t" \
  "ds_read_b128 v[96:99], v164 offset:16640\n\t" \
  "v_mfma_f32_16x16x32_bf16 v[128:131], a[32:35], v[32:35], v[128:131]\n\t" \
  "ds_read_b128 v[100:103], v164 offset:24832\n\t" \
  "v_mfma_f32_16x16x32_bf16 v[132:135], a[36:39], v[32:35], v[132:135]\n\t" \
  "ds_read_b128 v[104:107], v165 offset:16640\n\t" \
  "v_mfma_f32_16x16x32_bf16 v[136:139], a[32:35], v[36:39], v[136:139]\n\t" \
  "buffer_store_dwordx4 v[148:151], v169, %[ru], s42 offen sc1\n\t" \
  "v_mfma_f32_16x16x32_bf16 v[140:143], a[36:39], v[36:39], v[140:143]\n\t" \
  "ds_read_b128 v[108:111], v165 offset:24832\n\t" \
  "v_mfma_f32_16x16x32_bf16 v[128:131], a[40:43], v[40:43], v[128:131]\n\t" \
  "ds_read_b128 v[112:115], v166 offset:16640\n\t" \
  "v_mfma_f32_16x16x32_bf16 v[132:135], a[44:47], v[40:43], v[132:135]\n\t" \
  "s_add_i32 m0, s41, 2048\n\t" \
  "v_mfma_f32_16x16x32_bf16 v[136:139], a[40:43], v[44:47], v[136:139]\n\t" \
  "buffer_load_dwordx4 v162, %[rx], s40 offen lds\n\t" \
  "v_mfma_f32_16x16x32_bf16 v[140:143], a[44:47], v[44:47], v[140:143]\n\t" \
  "ds_read_b128 v[116:119], v166 offset:24832\n\t" \
  "v_mfma_f32_16x16x32_bf16 v[128:131], a[48:51], v[48:51], v[128:131]\n\t" \
  "ds_read_b128 v[120:123], v167 offset:16640\n\t" \
  "v_mfma_f32_16x16x32_bf16 v[132:135], a[52:55], v[48:51], v[132:135]\n\t" \
  "s_add_i32 m0, s41, 3072\n\t" \
  "v_mfma_f32_16x16x32_bf16 v[136:139], a[48:51], v[52:55], v[136:139]\n\t" \
  "buffer_load_dwordx4 v163, %[rx], s40 offen lds\n\t" \
  "v_mfma_f32_16x16x32_bf16 v[140:143], a[52:55], v[52:55], v[140:143]\n\t" \
  "ds_read_b128 v[124:127], v167 offset:24832\n\t" \
  "v_mfma_f32_16x16x32_bf16 v[128:131], a[56:59], v[56:59], v[128:131]\n\t" \
  "v_mfma_f32_16x16x32_bf16 v[132:135], a[60:63], v[56:59], v[132:135]\n\t" \
  "v_mfma_f32_16x16x32_bf16 v[136:139], a[56:59], v[60:63], v[136:139]\n\t" \
  "v_mfma_f32_16x16x32_bf16 v[140:143], a[60:63], v[60:63], v[140:143]\n\t" \
  "s_cmp_le_u32 s44, 6\n\t" \
  "s_waitcnt vmcnt(30) lgkmcnt(0)\n\t" \
  "v_mfma_f32_16x16x32_bf16 v[128:131], a[64:67], v[64:67], v[128:131]\n\t" \
  "s_cbranch_scc1 31f\n\t" \
  "41:\n\t" \
  "s_barrier\n\t" \
  "v_mfma_f32_16x16x32_bf16 v[132:135], a[68:71], v[64:67], v[132:135]\n\t" \
  "s_add_i32 s76, s40, 512\n\t" \
  "v_mfma_f32_16x16x32_bf16 v[136:139], a[64:67], v[68:71], v[136:139]\n\t" \
  "ds_read_b128 v[0:3], v164 offset:32768\n\t" \
  "v_mfma_f32_16x16x32_bf16 v[140:143], a[68:71], v[68:71], v[140:143]\n\t" \
  "ds_read_b128 v[4:7], v164 offset:40960\n\t" \
  "v_mfma_f32_16x16x32_bf16 v[128:131], a[72:75], v[72:75], v[128:131]\n\t" \
  "ds_read_b128 v[8:11], v165 offset:32768\n\t" \
  "v_mfma_f32_16x16x32_bf16 v[132:135], a[76:79], v[72:75], v[132:135]\n\t" \
  "s_add_i32 m0, s41, 16384\n\t" \
  "v_mfma_f32_16x16x32_bf16 v[136:139], a[72:75], v[76:79], v[136:139]\n\t" \
  "buffer_load_dwordx4 v160, %[rx], s76 offen lds\n\t" \
  "v_mfma_f32_16x16x32_bf16 v[140:143], a[76:79], v[76:79], v[140:143]\n\t" \
  "ds_read_b128 v[12:15], v165 offset:40960\n\t" \
  "v_mfma_f32_16x16x32_bf16 v[128:131], a[80:83], v[80:83], v[128:131]\n\t" \
  "ds_read_b128 v[16:19], v166 offset:32768\n\t" \
  "v_mfma_f32_16x16x32_bf16 v[132:135], a[84:87], v[80:83], v[132:135]\n\t" \
  "ds_read_b128 v[20:23], v166 offset:40960\n\t" \
  "v_mfma_f32_16x16x32_bf16 v[136:139], a[80:83], v[84:87], v[136:139]\n\t" \
  "v_mfma_f32_16x16x32_bf16 v[140:143], a[84:87], v[84:87], v[140:143]\n\t" \
  "ds_read_b128 v[24:27], v167 offset:32768\n\t" \
  "v_mfma_f32_16x16x32_bf16 v[128:131], a[88:91], v[88:91], v[128:131]\n\t" \
  "ds_read_b128 v[28:31], v167 offset:40960\n\t" \
  "v_mfma_f32_16x16x32_bf16 v[132:135], a[92:95], v[88:91], v[132:135]\n\t" \
  "s_add_i32 m0, s41, 17408\n\t" \
  "v_mfma_f32_16x16x32_bf16 v[136:139], a[88:91], v[92:95], v[136:139]\n\t" \
  "buffer_load_dwordx4 v161, %[rx], s76 offen lds\n\t" \
  "v_mfma_f32_16x16x32_bf16 v[140:143], a[92:95], v[92:95], v[140:143]\n\t" \
  "ds_read_b128 v[32:35], v164 offset:33024\n\t" \
  "v_mfma_f32_16x16x32_bf16 v[128:131], a[96:99], v[96:99], v[128:131]\n\t" \
  "ds_read_b128 v[36:39], v164 offset:41216\n\t" \
  "v_mfma_f32_16x16x32_bf16 v[132:135], a[100:103], v[96:99], v[132:135]\n\t" \
  "ds_read_b128 v[40:43], v165 offset:33024\n\t" \
  "v_mfma_f32_16x16x32_bf16 v[136:139], a[96:99], v[100:103], v[136:139]\n\t" \
  "v_mfma_f32_16x16x32_bf16 v[140:143], a[100:103], v[100:103], v[140:143]\n\t" \
  "ds_read_b128 v[44:47], v165 offset:41216\n\t" \
  "v_mfma_f32_16x16x32_bf16 v[128:131], a[104:107], v[104:107], v[128:131]\n\t" \
  "ds_read_b128 v[48:51], v166 offset:33024\n\t" \
  "v_mfma_f32_16x16x32_bf16 v[132:135], a[108:111], v[104:107], v[132:135]\n\t" \
  "s_add_i32 m0, s41, 18432\n\t" \
  "v_mfma_f32_16x16x32_bf16 v[136:139], a[104:107], v[108:111], v[136:139]\n\t" \
  "buffer_load_dwordx4 v162, %[rx], s76 offen lds\n\t" \
  "v_mfma_f32_16x16x32_bf16 v[140:143], a[108:111], v[108:111], v[140:143]\n\t" \
  "ds_read_b128 v[52:55], v166 offset:41216\n\t" \
  "v_mfma_f32_16x16x32_bf16 v[128:131], a[112:115], v[112:115], v[128:131]\n\t" \
  "ds_read_b128 v[56:59], v167 offset:33024\n\t" \
  "v_mfma_f32_16x16x32_bf16 v[132:135], a[116:119], v[112:115], v[132:135]\n\t" \
  "s_add_i32 m0, s41, 19456\n\t" \
  "v_mfma_f32_16x16x32_bf16 v[136:139], a[112:115], v[116:119], v[136:139]\n\t" \
  "buffer_load_dwordx4 v163, %[rx], s76 offen lds\n\t" \
  "v_mfma_f32_16x16x32_bf16 v[140:143], a[116:119], v[116:119], v[140:143]\n\t" \
  "ds_read_b128 v[60:63], v167 offset:41216\n\t" \
  "v_mfma_f32_16x16x32_bf16 v[128:131], a[120:123], v[120:123], v[128:131]\n\t" \
  "s_and_b32 s79, s44, 4\n\t" \
  "v_mfma_f32_16x16x32_bf16 v[132:135], a[124:127], v[120:123], v[132:135]\n\t" \
  "s_lshl_b32 s79, s79, 11\n\t" \
  "v_mfma_f32_16x16x32_bf16 v[136:139], a[120:123], v[124:127], v[136:139]\n\t" \
  "v_mfma_f32_16x16x32_bf16 v[140:143], a[124:127], v[124:127], v[140:143]\n\t" \
  "s_cmp_le_u32 s44, 6\n\t" \
  "s_waitcnt vmcnt(29) lgkmcnt(0)\n\t" \
  "v_mfma_f32_16x16x32_bf16 v[128:131], a[128:131], v[0:3], v[128:131]\n\t" \
  "s_cbranch_scc1 32f\n\t" \
  "42:\n\t" \
  "s_barrier\n\t" \
  "v_mfma_f32_16x16x32_bf16 v[132:135], a[132:135], v[0:3], v[132:135]\n\t" \
  "s_add_i32 s76, s40, 1024\n\t" \
  "v_mfma_f32_16x16x32_bf16 v[136:139], a[128:131], v[4:7], v[136:139]\n\t" \
  "ds_read_b128 v[64:67], v164 offset:49152\n\t" \
  "v_mfma_f32_16x16x32_bf16 v[140:143], a[132:135], v[4:7], v[140:143]\n\t" \
  "ds_read_b128 v[68:71], v164 offset:57344\n\t" \
  "v_mfma_f32_16x16x32_bf16 v[128:131], a[136:139], v[8:11], v[128:131]\n\t" \
  "ds_read_b128 v[72:75], v165 offset:49152\n\t" \
  "v_mfma_f32_16x16x32_bf16 v[132:135], a[140:143], v[8:11], v[132:135]\n\t" \
  "s_add_i32 m0, s41, 32768\n\t" \
  "v_mfma_f32_16x16x32_bf16 v[136:139], a[136:139], v[12:15], v[136:139]\n\t" \
  "buffer_load_dwordx4 v160, %[rx], s76 offen lds\n\t" \
  "v_mfma_f32_16x16x32_bf16 v[140:143], a[140:143], v[12:15], v[140:143]\n\t" \
  "ds_read_b128 v[76:79], v165 offset:57344\n\t" \
  "v_mfma_f32_16x16x32_bf16 v[128:131], a[144:147], v[16:19], v[128:131]\n\t" \
  "ds_read_b128 v[80:83], v166 offset:49152\n\t" \
  "v_mfma_f32_16x16x32_bf16 v[132:135], a[148:151], v[16:19], v[132:135]\n\t" \
  "ds_read_b128 v[84:87], v166 offset:57344\n\t" \
  "v_mfma_f32_16x16x32_bf16 v[136:139], a[144:147], v[20:23], v[136:139]\n\t" \
  "v_mfma_f32_16x16x32_bf16 v[140:143], a[148:151], v[20:23], v[140:143]\n\t" \
  "ds_read_b128 v[88:91], v167 offset:49152\n\t" \
  "v_mfma_f32_16x16x32_bf16 v[128:131], a[152:155], v[24:27], v[128:131]\n\t" \
  "ds_read_b128 v[92:95], v167 offset:57344\n\t" \
  "v_mfma_f32_16x16x32_bf16 v[132:135], a[156:159], v[24:27], v[132:135]\n\t" \
  "s_add_i32 m0, s41, 33792\n\t" \
  "v_mfma_f32_16x16x32_bf16 v[136:139], a[152:155], v[28:31], v[136:139]\n\t" \
  "buffer_load_dwordx4 v161, %[rx], s76 offen lds\n\t" \
  "v_mfma_f32_16x16x32_bf16 v[140:143], a[156:159], v[28:31], v[140:143]\n\t" \
  "ds_read_b128 v[96:99], v164 offset:49408\n\t" \
  "v_mfma_f32_16x16x32_bf16 v[128:131], a[160:163], v[32:35], v[128:131]\n\t" \
  "ds_read_b128 v[100:103], v164 offset:57600\n\t" \
  "v_mfma_f32_16x16x32_bf16 v[132:135], a[164:167], v[32:35], v[132:135]\n\t" \
  "ds_read_b128 v[104:107], v165 offset:49408\n\t" \
  "v_mfma_f32_16x16x32_bf16 v[136:139], a[160:163], v[36:39], v[136:139]\n\t" \
  "v_mfma_f32_16x16x32_bf16 v[140:143], a[164:167], v[36:39], v[140:143]\n\t" \
  "ds_read_b128 v[108:111], v165 offset:57600\n\t" \
  "v_mfma_f32_16x16x32_bf16 v[128:131], a[168:171], v[40:43], v[128:131]\n\t" \
  "ds_read_b128 v[112:115], v166 offset:49408\n\t" \
  "v_mfma_f32_16x16x32_bf16 v[132:135], a[172:175], v[40:43], v[132:135]\n\t" \
  "s_add_i32 m0, s41, 34816\n\t" \
  "v_mfma_f32_16x16x32_bf16 v[136:139], a[168:171], v[44:47], v[136:139]\n\t" \
  "buffer_load_dwordx4 v162, %[rx], s76 offen lds\n\t" \
  "v_mfma_f32_16x16x32_bf16 v[140:143], a[172:175], v[44:47], v[140:143]\n\t" \
  "ds_read_b128 v[116:119], v166 offset:57600\n\t" \
  "v_mfma_f32_16x16x32_bf16 v[128:131], a[176:179], v[48:51], v[128:131]\n\t" \
  "ds_read_b128 v[120:123], v167 offset:49408\n\t" \
  "v_mfma_f32_16x16x32_bf16 v[132:135], a[180:183], v[48:51], v[132:135]\n\t" \
  "s_add_i32 m0, s41, 35840\n\t" \
  "v_mfma_f32_16x16x32_bf16 v[136:139], a[176:179], v[52:55], v[136:139]\n\t" \
  "buffer_load_dwordx4 v163, %[rx], s76 offen lds\n\t" \
  "v_mfma_f32_16x16x32_bf16 v[140:143], a[180:183], v[52:55], v[140:143]\n\t" \
  "ds_read_b128 v[124:127], v167 offset:57600\n\t" \
  "v_mfma_f32_16x16x32_bf16 v[128:131], a[184:187], v[56:59], v[128:131]\n\t" \
  "s_add_i32 s78, s44, -2\n\t" \
  "v_mfma_f32_16x16x32_bf16 v[132:135], a[188:191], v[56:59], v[132:135]\n\t" \
  "s_max_i32 s78, s78, s46\n\t" \
  "v_mfma_f32_16x16x32_bf16 v[136:139], a[184:187], v[60:63], v[136:139]\n\t" \
  "v_mov_b32 v156, s78\n\t" \
  "v_mfma_f32_16x16x32_bf16 v[140:143], a[188:191], v[60:63], v[140:143]\n\t" \
  "s_cmp_le_u32 s44, 6\n\t" \
  "s_waitcnt vmcnt(27) lgkmcnt(0)\n\t" \
  "v_mfma_f32_16x16x32_bf16 v[128:131], a[192:195], v[64:67], v[128:131]\n\t" \
  "s_cbranch_scc1 33f\n\t" \
  "43:\n\t" \
  "s_barrier\n\t" \
  "v_xor_b32 v164, 0x10000, v164\n\t" \
  "v_mfma_f32_16x16x32_bf16 v[132:135], a[196:199], v[64:67], v[132:135]\n\t" \
  "s_add_i32 s76, s40, 1536\n\t" \
  "buffer_store_dword v156, v171, %[ru], 0 offen sc1\n\t" \
  "v_mfma_f32_16x16x32_bf16 v[136:139], a[192:195], v[68:71], v[136:139]\n\t" \
  "v_xor_b32 v165, 0x10000, v165\n\t" \
  "ds_read_b128 v[0:3], v164 offset:0\n\t" \
  "v_mfma_f32_16x16x32_bf16 v[140:143], a[196:199], v[68:71], v[140:143]\n\t" \
  "v_xor_b32 v166, 0x10000, v166\n\t" \
  "ds_read_b128 v[4:7], v164 offset:8192\n\t" \
  "v_mfma_f32_16x16x32_bf16 v[128:131], a[200:203], v[72:75], v[128:131]\n\t" \
  "v_xor_b32 v167, 0x10000, v167\n\t" \
  "ds_read_b128 v[8:11], v165 offset:0\n\t" \
  "v_mfma_f32_16x16x32_bf16 v[132:135], a[204:207], v[72:75], v[132:135]\n\t" \
  "s_add_i32 m0, s41, 49152\n\t" \
  "ds_read_b128 v[12:15], v165 offset:8192\n\t" \
  "v_mfma_f32_16x16x32_bf16 v[136:139], a[200:203], v[76:79], v[136:139]\n\t" \
  "buffer_load_dwordx4 v160, %[rx], s76 offen lds\n\t" \
  "v_mfma_f32_16x16x32_bf16 v[140:143], a[204:207], v[76:79], v[140:143]\n\t" \
  "ds_read_b128 v[16:19], v166 offset:0\n\t" \
  "v_mfma_f32_16x16x32_bf16 v[128:131], a[208:211], v[80:83], v[128:131]\n\t" \
  "ds_read_b128 v[20:23], v166 offset:8192\n\t" \
  "v_mfma_f32_16x16x32_bf16 v[132:135], a[212:215], v[80:83], v[132:135]\n\t" \
  "ds_read_b128 v[24:27], v167 offset:0\n\t" \
  "v_mfma_f32_16x16x32_bf16 v[136:139], a[208:211], v[84:87], v[136:139]\n\t" \
  "v_mfma_f32_16x16x32_bf16 v[140:143], a[212:215], v[84:87], v[140:143]\n\t" \
  "ds_read_b128 v[28:31], v167 offset:8192\n\t" \
  "v_mfma_f32_16x16x32_bf16 v[128:131], a[216:219], v[88:91], v[128:131]\n\t" \
  "ds_read_b128 v[32:35], v164 offset:256\n\t" \
  "v_mfma_f32_16x16x32_bf16 v[132:135], a[220:223], v[88:91], v[132:135]\n\t" \
  "s_add_i32 m0, s41, 50176\n\t" \
  "v_mfma_f32_16x16x32_bf16 v[136:139], a[216:219], v[92:95], v[136:139]\n\t" \
  "buffer_load_dwordx4 v161, %[rx], s76 offen lds\n\t" \
  "v_mfma_f32_16x16x32_bf16 v[140:143], a[220:223], v[92:95], v[140:143]\n\t" \
  "ds_read_b128 v[36:39], v164 offset:8448\n\t" \
  "v_mfma_f32_16x16x32_bf16 v[128:131], a[224:227], v[96:99], v[128:131]\n\t" \
  "ds_read_b128 v[40:43], v165 offset:256\n\t" \
  "v_mfma_f32_16x16x32_bf16 v[132:135], a[228:231], v[96:99], v[132:135]\n\t" \
  "ds_read_b128 v[44:47], v165 offset:8448\n\t" \
  "v_mfma_f32_16x16x32_bf16 v[136:139], a[224:227], v[100:103], v[136:139]\n\t" \
  "v_mfma_f32_16x16x32_bf16 v[140:143], a[228:231], v[100:103], v[140:143]\n\t" \
  "ds_read_b128 v[48:51], v166 offset:256\n\t" \
  "v_mfma_f32_16x16x32_bf16 v[128:131], a[232:235], v[104:107], v[128:131]\n\t" \
  "ds_read_b128 v[52:55], v166 offset:8448\n\t" \
  "v_mfma_f32_16x16x32_bf16 v[132:135], a[236:239], v[104:107], v[132:135]\n\t" \
  "s_add_i32 m0, s41, 51200\n\t" \
  "v_mfma_f32_16x16x32_bf16 v[136:139], a[232:235], v[108:111], v[136:139]\n\t" \
  "buffer_load_dwordx4 v162, %[rx], s76 offen lds\n\t" \
  "v_mfma_f32_16x16x32_bf16 v[140:143], a[236:239], v[108:111], v[140:143]\n\t" \
  "ds_read_b128 v[56:59], v167 offset:256\n\t" \
  "v_mfma_f32_16x16x32_bf16 v[128:131], a[240:243], v[112:115], v[128:131]\n\t" \
  "ds_read_b128 v[60:63], v167 offset:8448\n\t" \
  "v_mfma_f32_16x16x32_bf16 v[132:135], a[244:247], v[112:115], v[132:135]\n\t" \
  "s_add_i32 m0, s41, 52224\n\t" \
  "v_mfma_f32_16x16x32_bf16 v[136:139], a[240:243], v[116:119], v[136:139]\n\t" \
  "buffer_load_dwordx4 v163, %[rx], s76 offen lds\n\t" \
  "v_mfma_f32_16x16x32_bf16 v[140:143], a[244:247], v[116:119], v[140:143]\n\t" \
  "s_add_i32 s44, s44, 1\n\t" \
  "s_xor_b32 s42, s43, s79\n\t" \
  "v_mfma_f32_16x16x32_bf16 v[128:131], a[248:251], v[120:123], v[128:131]\n\t" \
  "s_add_i32 s43, s43, 0x80000\n\t" \
  "s_add_i32 s40, s40, 0x10000\n\t" \
  "v_mfma_f32_16x16x32_bf16 v[132:135], a[252:255], v[120:123], v[132:135]\n\t" \
  "s_xor_b32 s41, s41, 0x10000\n\t" \
  "v_mfma_f32_16x16x32_bf16 v[136:139], a[248:251], v[124:127], v[136:139]\n\t" \
  "v_cvt_pk_bf16_f32 v144, v128, v129\n\t" \
  "v_cvt_pk_bf16_f32 v145, v130, v131\n\t" \
  "v_mfma_f32_16x16x32_bf16 v[140:143], a[252:255], v[124:127], v[140:143]\n\t" \
  "v_cvt_pk_bf16_f32 v146, v132, v133\n\t" \
  "v_cvt_pk_bf16_f32 v147, v134, v135\n\t" \
  "s_cmp_eq_u32 s44, 64\n\t" \
  "s_cbranch_scc1 98f\n\t" \
  "s_cmp_le_u32 s44, 6\n\t" \
  "s_branch 20b\n\t" \
  "30:\n\t" \
  "s_waitcnt vmcnt(24)\n\t" \
  "s_branch 40b\n\t" \
  "31:\n\t" \
  "s_waitcnt vmcnt(24)\n\t" \
  "s_branch 41b\n\t" \
  "32:\n\t" \
  "s_waitcnt vmcnt(6)\n\t" \
  "s_barrier\n\t" \
  "s_add_i32 s46, s44, -1\n\t" \
  "v_mov_b32 v156, s46\n\t" \
  "buffer_store_dword v156, v171, %[ru], 0 offen sc1\n\t" \
  "s_branch 42b\n\t" \
  "33:\n\t" \
  "s_waitcnt vmcnt(24)\n\t" \
  "s_branch 43b\n\t" \
  "98:\n\t" \
  "s_nop 7\n\t" \
  "v_cvt_pk_bf16_f32 v148, v136, v137\n\t" \
  "v_cvt_pk_bf16_f32 v149, v138, v139\n\t" \
  "v_cvt_pk_bf16_f32 v150, v140, v141\n\t" \
  "v_cvt_pk_bf16_f32 v151, v142, v143\n\t" \
  "buffer_store_dwordx4 v[144:147], v168, %[ru], s42 offen sc1\n\t" \
  "buffer_store_dwordx4 v[148:151], v169, %[ru], s42 offen sc1\n\t" \
  "v_mov_b32 v156, 63\n\t" \
  "s_waitcnt vmcnt(0) lgkmcnt(0)\n\t" \
  "s_barrier\n\t" \
  "buffer_store_dword v156, v171, %[ru], 0 offen sc1\n\t" \
  "99:\n\t" \
  ""
#define U_BODY \
  "s_cmp_eq_u32 %[wv], 1\n\t" \
  "s_cbranch_scc1 91f\n\t" \
  "s_cmp_eq_u32 %[wv], 2\n\t" \
  "s_cbranch_scc1 92f\n\t" \
  "s_cmp_eq_u32 %[wv], 3\n\t" \
  "s_cbranch_scc1 93f\n\t" \
  "90:\n\t" \
  "ds_read_b128 v[0:3], v81 offset:0\n\t" \
  "ds_read_b128 v[16:19], v81 offset:4096\n\t" \
  "ds_read_b128 v[4:7], v81 offset:1024\n\t" \
  "ds_read_b128 v[20:23], v81 offset:5120\n\t" \
  "ds_read_b128 v[8:11], v81 offset:2048\n\t" \
  "ds_read_b128 v[24:27], v81 offset:6144\n\t" \
  "ds_read_b128 v[12:15], v81 offset:3072\n\t" \
  "ds_read_b128 v[28:31], v81 offset:7168\n\t" \
  ".p2align 6\n\t" \
  "20:\n\t" \
  "s_waitcnt lgkmcnt(7)\n\t" \
  "v_mfma_f32_16x16x32_bf16 v[32:35], v[0:3], a[0:3], 0\n\t" \
  "buffer_store_dwordx4 v[52:55], v80, %[ru], s44 offen offset:1024 sc1\n\t" \
  "s_waitcnt lgkmcnt(6)\n\t" \
  "v_mfma_f32_16x16x32_bf16 v[36:39], v[16:19], a[0:3], 0\n\t" \
  "s_and_b32 s45, s40, 15\n\t" \
  "s_waitcnt lgkmcnt(5)\n\t" \
  "v_mfma_f32_16x16x32_bf16 v[32:35], v[4:7], a[4:7], v[32:35]\n\t" \
  "v_cvt_pk_bf16_f32 v57, v40, v41\n\t" \
  "s_lshl_b32 s45, s45, 22\n\t" \
  "s_waitcnt lgkmcnt(4)\n\t" \
  "v_mfma_f32_16x16x32_bf16 v[36:39], v[20:23], a[4:7], v[36:39]\n\t" \
  "v_cvt_pk_bf16_f32 v59, v42, v43\n\t" \
  "s_add_i32 s45, s45, s42\n\t" \
  "s_waitcnt lgkmcnt(3)\n\t" \
  "v_mfma_f32_16x16x32_bf16 v[32:35], v[8:11], a[8:11], v[32:35]\n\t" \
  "v_cvt_pk_bf16_f32 v61, v44, v45\n\t" \
  "s_lshr_b32 s76, s45, 21\n\t" \
  "s_waitcnt lgkmcnt(2)\n\t" \
  "v_mfma_f32_16x16x32_bf16 v[36:39], v[24:27], a[8:11], v[36:39]\n\t" \
  "v_cvt_pk_bf16_f32 v63, v46, v47\n\t" \
  "s_bitcmp0_b32 %[sel], s76\n\t" \
  "s_cselect_b32 s76, 0x2000, 0\n\t" \
  "s_waitcnt lgkmcnt(1)\n\t" \
  "v_mfma_f32_16x16x32_bf16 v[32:35], v[12:15], a[12:15], v[32:35]\n\t" \
  "s_add_i32 s45, s45, s76\n\t" \
  "s_waitcnt lgkmcnt(0)\n\t" \
  "v_mfma_f32_16x16x32_bf16 v[36:39], v[28:31], a[12:15], v[36:39]\n\t" \
  "s_add_i32 s77, s40, 1\n\t" \
  "v_mfma_f32_16x16x32_bf16 v[40:43], v[0:3], a[16:19], 0\n\t" \
  "buffer_store_dwordx4 v[56:59], v80, %[ru], s44 offen offset:2048 sc1\n\t" \
  "v_mfma_f32_16x16x32_bf16 v[44:47], v[16:19], a[16:19], 0\n\t" \
  "s_min_u32 s77, s77, 63\n\t" \
  "v_mfma_f32_16x16x32_bf16 v[40:43], v[4:7], a[20:23], v[40:43]\n\t" \
  "v_cvt_pk_bf16_f32 v48, v32, v33\n\t" \
  "s_and_b32 s76, s77, 4\n\t" \
  "v_mfma_f32_16x16x32_bf16 v[44:47], v[20:23], a[20:23], v[44:47]\n\t" \
  "v_cvt_pk_bf16_f32 v50, v34, v35\n\t" \
  "s_lshl_b32 s76, s76, 11\n\t" \
  "v_mfma_f32_16x16x32_bf16 v[40:43], v[8:11], a[24:27], v[40:43]\n\t" \
  "v_cvt_pk_bf16_f32 v52, v36, v37\n\t" \
  "s_lshl_b32 s77, s77, 19\n\t" \
  "v_mfma_f32_16x16x32_bf16 v[44:47], v[24:27], a[24:27], v[44:47]\n\t" \
  "v_cvt_pk_bf16_f32 v54, v38, v39\n\t" \
  "s_add_i32 s77, s77, s43\n\t" \
  "v_mfma_f32_16x16x32_bf16 v[40:43], v[12:15], a[28:31], v[40:43]\n\t" \
  "s_xor_b32 s77, s77, s76\n\t" \
  "v_mfma_f32_16x16x32_bf16 v[44:47], v[28:31], a[28:31], v[44:47]\n\t" \
  "s_add_i32 s78, s40, 6\n\t" \
  "v_mfma_f32_16x16x32_bf16 v[32:35], v[0:3], a[32:35], 0\n\t" \
  "buffer_store_dwordx4 v[60:63], v80, %[ru], s44 offen offset:3072 sc1\n\t" \
  "v_mfma_f32_16x16x32_bf16 v[36:39], v[16:19], a[32:35], 0\n\t" \
  "s_cmp_lt_i32 s41, s78\n\t" \
  "s_cselect_b32 s78, 0, 0x7f000000\n\t" \
  "v_mfma_f32_16x16x32_bf16 v[32:35], v[4:7], a[36:39], v[32:35]\n\t" \
  "v_cvt_pk_bf16_f32 v49, v40, v41\n\t" \
  "s_mov_b32 s46, s41\n\t" \
  "v_mfma_f32_16x16x32_bf16 v[36:39], v[20:23], a[36:39], v[36:39]\n\t" \
  "v_cvt_pk_bf16_f32 v51, v42, v43\n\t" \
  "v_mfma_f32_16x16x32_bf16 v[32:35], v[8:11], a[40:43], v[32:35]\n\t" \
  "v_cvt_pk_bf16_f32 v53, v44, v45\n\t" \
  "v_mfma_f32_16x16x32_bf16 v[36:39], v[24:27], a[40:43], v[36:39]\n\t" \
  "v_cvt_pk_bf16_f32 v55, v46, v47\n\t" \
  "buffer_load_dwordx4 v[64:67], v80, %[ru], s77 offen sc1\n\t" \
  "v_mfma_f32_16x16x32_bf16 v[32:35], v[12:15], a[44:47], v[32:35]\n\t" \
  "buffer_load_dwordx4 v[68:71], v80, %[ru], s77 offen offset:1024 sc1\n\t" \
  "v_mfma_f32_16x16x32_bf16 v[36:39], v[28:31], a[44:47], v[36:39]\n\t" \
  "s_mov_b64 exec, 3\n\t" \
  "buffer_load_dword v72, v83, %[ru], s78 offen sc1\n\t" \
  "s_mov_b64 exec, -1\n\t" \
  "v_mfma_f32_16x16x32_bf16 v[40:43], v[0:3], a[48:51], 0\n\t" \
  "buffer_store_dwordx4 v[48:51], v80, %[ru], s45 offen offset:0 sc1\n\t" \
  "v_mfma_f32_16x16x32_bf16 v[44:47], v[16:19], a[48:51], 0\n\t" \
  "v_mfma_f32_16x16x32_bf16 v[40:43], v[4:7], a[52:55], v[40:43]\n\t" \
  "v_cvt_pk_bf16_f32 v56, v32, v33\n\t" \
  "v_mfma_f32_16x16x32_bf16 v[44:47], v[20:23], a[52:55], v[44:47]\n\t" \
  "v_cvt_pk_bf16_f32 v58, v34, v35\n\t" \
  "v_mfma_f32_16x16x32_bf16 v[40:43], v[8:11], a[56:59], v[40:43]\n\t" \
  "v_cvt_pk_bf16_f32 v60, v36, v37\n\t" \
  "v_mfma_f32_16x16x32_bf16 v[44:47], v[24:27], a[56:59], v[44:47]\n\t" \
  "v_cvt_pk_bf16_f32 v62, v38, v39\n\t" \
  "v_mfma_f32_16x16x32_bf16 v[40:43], v[12:15], a[60:63], v[40:43]\n\t" \
  "v_mfma_f32_16x16x32_bf16 v[44:47], v[28:31], a[60:63], v[44:47]\n\t" \
  "v_mfma_f32_16x16x32_bf16 v[32:35], v[0:3], a[64:67], 0\n\t" \
  "buffer_store_dwordx4 v[52:55], v80, %[ru], s45 offen offset:1024 sc1\n\t" \
  "v_mfma_f32_16x16x32_bf16 v[36:39], v[16:19], a[64:67], 0\n\t" \
  "v_mfma_f32_16x16x32_bf16 v[32:35], v[4:7], a[68:71], v[32:35]\n\t" \
  "v_cvt_pk_bf16_f32 v57, v40, v41\n\t" \
  "v_mfma_f32_16x16x32_bf16 v[36:39], v[20:23], a[68:71], v[36:39]\n\t" \
  "v_cvt_pk_bf16_f32 v59, v42, v43\n\t" \
  "v_mfma_f32_16x16x32_bf16 v[32:35], v[8:11], a[72:75], v[32:35]\n\t" \
  "v_cvt_pk_bf16_f32 v61, v44, v45\n\t" \
  "v_mfma_f32_16x16x32_bf16 v[36:39], v[24:27], a[72:75], v[36:39]\n\t" \
  "v_cvt_pk_bf16_f32 v63, v46, v47\n\t" \
  "v_mfma_f32_16x16x32_bf16 v[32:35], v[12:15], a[76:79], v[32:35]\n\t" \
  "v_mfma_f32_16x16x32_bf16 v[36:39], v[28:31], a[76:79], v[36:39]\n\t" \
  "v_mfma_f32_16x16x32_bf16 v[40:43], v[0:3], a[80:83], 0\n\t" \
  "buffer_store_dwordx4 v[56:59], v80, %[ru], s45 offen offset:2048 sc1\n\t" \
  "v_mfma_f32_16x16x32_bf16 v[44:47], v[16:19], a[80:83], 0\n\t" \
  "v_mfma_f32_16x16x32_bf16 v[40:43], v[4:7], a[84:87], v[40:43]\n\t" \
  "v_cvt_pk_bf16_f32 v48, v32, v33\n\t" \
  "v_mfma_f32_16x16x32_bf16 v[44:47], v[20:23], a[84:87], v[44:47]\n\t" \
  "v_cvt_pk_bf16_f32 v50, v34, v35\n\t" \
  "v_mfma_f32_16x16x32_bf16 v[40:43], v[8:11], a[88:91], v[40:43]\n\t" \
  "v_cvt_pk_bf16_f32 v52, v36, v37\n\t" \
  "v_mfma_f32_16x16x32_bf16 v[44:47], v[24:27], a[88:91], v[44:47]\n\t" \
  "v_cvt_pk_bf16_f32 v54, v38, v39\n\t" \
  "v_mfma_f32_16x16x32_bf16 v[40:43], v[12:15], a[92:95], v[40:43]\n\t" \
  "v_mfma_f32_16x16x32_bf16 v[44:47], v[28:31], a[92:95], v[44:47]\n\t" \
  "v_mfma_f32_16x16x32_bf16 v[32:35], v[0:3], a[96:99], 0\n\t" \
  "buffer_store_dwordx4 v[60:63], v80, %[ru], s45 offen offset:3072 sc1\n\t" \
  "v_mfma_f32_16x16x32_bf16 v[36:39], v[16:19], a[96:99], 0\n\t" \
  "v_mfma_f32_16x16x32_bf16 v[32:35], v[4:7], a[100:103], v[32:35]\n\t" \
  "v_cvt_pk_bf16_f32 v49, v40, v41\n\t" \
  "v_mfma_f32_16x16x32_bf16 v[36:39], v[20:23], a[100:103], v[36:39]\n\t" \
  "v_cvt_pk_bf16_f32 v51, v42, v43\n\t" \
  "v_mfma_f32_16x16x32_bf16 v[32:35], v[8:11], a[104:107], v[32:35]\n\t" \
  "v_cvt_pk_bf16_f32 v53, v44, v45\n\t" \
  "v_mfma_f32_16x16x32_bf16 v[36:39], v[24:27], a[104:107], v[36:39]\n\t" \
  "v_cvt_pk_bf16_f32 v55, v46, v47\n\t" \
  "v_mfma_f32_16x16x32_bf16 v[32:35], v[12:15], a[108:111], v[32:35]\n\t" \
  "v_mfma_f32_16x16x32_bf16 v[36:39], v[28:31], a[108:111], v[36:39]\n\t" \
  "s_add_i32 s45, s45, 0x1000\n\t" \
  "v_mfma_f32_16x16x32_bf16 v[40:43], v[0:3], a[112:115], 0\n\t" \
  "buffer_store_dwordx4 v[48:51], v80, %[ru], s45 offen offset:0 sc1\n\t" \
  "v_mfma_f32_16x16x32_bf16 v[44:47], v[16:19], a[112:115], 0\n\t" \
  "v_mfma_f32_16x16x32_bf16 v[40:43], v[4:7], a[116:119], v[40:43]\n\t" \
  "v_cvt_pk_bf16_f32 v56, v32, v33\n\t" \
  "v_mfma_f32_16x16x32_bf16 v[44:47], v[20:23], a[116:119], v[44:47]\n\t" \
  "v_cvt_pk_bf16_f32 v58, v34, v35\n\t" \
  "v_mfma_f32_16x16x32_bf16 v[40:43], v[8:11], a[120:123], v[40:43]\n\t" \
  "v_cvt_pk_bf16_f32 v60, v36, v37\n\t" \
  "v_mfma_f32_16x16x32_bf16 v[44:47], v[24:27], a[120:123], v[44:47]\n\t" \
  "v_cvt_pk_bf16_f32 v62, v38, v39\n\t" \
  "v_mfma_f32_16x16x32_bf16 v[40:43], v[12:15], a[124:127], v[40:43]\n\t" \
  "v_mfma_f32_16x16x32_bf16 v[44:47], v[28:31], a[124:127], v[44:47]\n\t" \
  "v_mfma_f32_16x16x32_bf16 v[32:35], v[0:3], a[128:131], 0\n\t" \
  "buffer_store_dwordx4 v[52:55], v80, %[ru], s45 offen offset:1024 sc1\n\t" \
  "v_mfma_f32_16x16x32_bf16 v[36:39], v[16:19], a[128:131], 0\n\t" \
  "v_mfma_f32_16x16x32_bf16 v[32:35], v[4:7], a[132:135], v[32:35]\n\t" \
  "v_cvt_pk_bf16_f32 v57, v40, v41\n\t" \
  "v_mfma_f32_16x16x32_bf16 v[36:39], v[20:23], a[132:135], v[36:39]\n\t" \
  "v_cvt_pk_bf16_f32 v59, v42, v43\n\t" \
  "v_mfma_f32_16x16x32_bf16 v[32:35], v[8:11], a[136:139], v[32:35]\n\t" \
  "v_cvt_pk_bf16_f32 v61, v44, v45\n\t" \
  "v_mfma_f32_16x16x32_bf16 v[36:39], v[24:27], a[136:139], v[36:39]\n\t" \
  "v_cvt_pk_bf16_f32 v63, v46, v47\n\t" \
  "v_mfma_f32_16x16x32_bf16 v[32:35], v[12:15], a[140:143], v[32:35]\n\t" \
  "v_mfma_f32_16x16x32_bf16 v[36:39], v[28:31], a[140:143], v[36:39]\n\t" \
  "v_mfma_f32_16x16x32_bf16 v[40:43], v[0:3], a[144:147], 0\n\t" \
  "buffer_store_dwordx4 v[56:59], v80, %[ru], s45 offen offset:2048 sc1\n\t" \
  "v_mfma_f32_16x16x32_bf16 v[44:47], v[16:19], a[144:147], 0\n\t" \
  "v_mfma_f32_16x16x32_bf16 v[40:43], v[4:7], a[148:151], v[40:43]\n\t" \
  "v_cvt_pk_bf16_f32 v48, v32, v33\n\t" \
  "v_mfma_f32_16x16x32_bf16 v[44:47], v[20:23], a[148:151], v[44:47]\n\t" \
  "v_cvt_pk_bf16_f32 v50, v34, v35\n\t" \
  "v_mfma_f32_16x16x32_bf16 v[40:43], v[8:11], a[152:155], v[40:43]\n\t" \
  "v_cvt_pk_bf16_f32 v52, v36, v37\n\t" \
  "v_mfma_f32_16x16x32_bf16 v[44:47], v[24:27], a[152:155], v[44:47]\n\t" \
  "v_cvt_pk_bf16_f32 v54, v38, v39\n\t" \
  "v_mfma_f32_16x16x32_bf16 v[40:43], v[12:15], a[156:159], v[40:43]\n\t" \
  "v_mfma_f32_16x16x32_bf16 v[44:47], v[28:31], a[156:159], v[44:47]\n\t" \
  "v_mfma_f32_16x16x32_bf16 v[32:35], v[0:3], a[160:163], 0\n\t" \
  "buffer_store_dwordx4 v[60:63], v80, %[ru], s45 offen offset:3072 sc1\n\t" \
  "v_mfma_f32_16x16x32_bf16 v[36:39], v[16:19], a[160:163], 0\n\t" \
  "v_mfma_f32_16x16x32_bf16 v[32:35], v[4:7], a[164:167], v[32:35]\n\t" \
  "v_cvt_pk_bf16_f32 v49, v40, v41\n\t" \
  "v_mfma_f32_16x16x32_bf16 v[36:39], v[20:23], a[164:167], v[36:39]\n\t" \
  "v_cvt_pk_bf16_f32 v51, v42, v43\n\t" \
  "v_mfma_f32_16x16x32_bf16 v[32:35], v[8:11], a[168:171], v[32:35]\n\t" \
  "v_cvt_pk_bf16_f32 v53, v44, v45\n\t" \
  "v_mfma_f32_16x16x32_bf16 v[36:39], v[24:27], a[168:171], v[36:39]\n\t" \
  "v_cvt_pk_bf16_f32 v55, v46, v47\n\t" \
  "v_mfma_f32_16x16x32_bf16 v[32:35], v[12:15], a[172:175], v[32:35]\n\t" \
  "v_mfma_f32_16x16x32_bf16 v[36:39], v[28:31], a[172:175], v[36:39]\n\t" \
  "s_add_i32 s45, s45, 0x3000\n\t" \
  "v_mfma_f32_16x16x32_bf16 v[40:43], v[0:3], a[176:179], 0\n\t" \
  "buffer_store_dwordx4 v[48:51], v80, %[ru], s45 offen offset:0 sc1\n\t" \
  "v_mfma_f32_16x16x32_bf16 v[44:47], v[16:19], a[176:179], 0\n\t" \
  "v_mfma_f32_16x16x32_bf16 v[40:43], v[4:7], a[180:183], v[40:43]\n\t" \
  "v_cvt_pk_bf16_f32 v56, v32, v33\n\t" \
  "v_mfma_f32_16x16x32_bf16 v[44:47], v[20:23], a[180:183], v[44:47]\n\t" \
  "v_cvt_pk_bf16_f32 v58, v34, v35\n\t" \
  "v_mfma_f32_16x16x32_bf16 v[40:43], v[8:11], a[184:187], v[40:43]\n\t" \
  "v_cvt_pk_bf16_f32 v60, v36, v37\n\t" \
  "v_mfma_f32_16x16x32_bf16 v[44:47], v[24:27], a[184:187], v[44:47]\n\t" \
  "v_cvt_pk_bf16_f32 v62, v38, v39\n\t" \
  "v_mfma_f32_16x16x32_bf16 v[40:43], v[12:15], a[188:191], v[40:43]\n\t" \
  "v_xor_b32 v81, 0x2000, v81\n\t" \
  "v_mfma_f32_16x16x32_bf16 v[44:47], v[28:31], a[188:191], v[44:47]\n\t" \
  "v_xor_b32 v82, 0x2000, v82\n\t" \
  "v_mfma_f32_16x16x32_bf16 v[32:35], v[0:3], a[192:195], 0\n\t" \
  "buffer_store_dwordx4 v[52:55], v80, %[ru], s45 offen offset:1024 sc1\n\t" \
  "v_mfma_f32_16x16x32_bf16 v[36:39], v[16:19], a[192:195], 0\n\t" \
  "v_mfma_f32_16x16x32_bf16 v[32:35], v[4:7], a[196:199], v[32:35]\n\t" \
  "v_cvt_pk_bf16_f32 v57, v40, v41\n\t" \
  "v_mfma_f32_16x16x32_bf16 v[36:39], v[20:23], a[196:199], v[36:39]\n\t" \
  "v_cvt_pk_bf16_f32 v59, v42, v43\n\t" \
  "v_mfma_f32_16x16x32_bf16 v[32:35], v[8:11], a[200:203], v[32:35]\n\t" \
  "v_cvt_pk_bf16_f32 v61, v44, v45\n\t" \
  "v_mfma_f32_16x16x32_bf16 v[36:39], v[24:27], a[200:203], v[36:39]\n\t" \
  "v_cvt_pk_bf16_f32 v63, v46, v47\n\t" \
  "v_mfma_f32_16x16x32_bf16 v[32:35], v[12:15], a[204:207], v[32:35]\n\t" \
  "s_add_i32 s79, s40, -1\n\t" \
  "v_xor_b32 v85, 8, v85\n\t" \
  "v_mfma_f32_16x16x32_bf16 v[36:39], v[28:31], a[204:207], v[36:39]\n\t" \
  "s_waitcnt vmcnt(10)\n\t" \
  "ds_write_b128 v82, v[64:67]\n\t" \
  "v_mfma_f32_16x16x32_bf16 v[40:43], v[0:3], a[208:211], 0\n\t" \
  "buffer_store_dwordx4 v[56:59], v80, %[ru], s45 offen offset:2048 sc1\n\t" \
  "v_readlane_b32 s76, v72, 0\n\t" \
  "v_readlane_b32 s77, v72, 1\n\t" \
  "v_mfma_f32_16x16x32_bf16 v[44:47], v[16:19], a[208:211], 0\n\t" \
  "ds_write_b128 v82, v[68:71] offset:1024\n\t" \
  "v_mfma_f32_16x16x32_bf16 v[40:43], v[4:7], a[212:215], v[40:43]\n\t" \
  "v_cvt_pk_bf16_f32 v48, v32, v33\n\t" \
  "s_add_i32 s77, s77, 15\n\t" \
  "s_min_i32 s76, s76, s77\n\t" \
  "v_mfma_f32_16x16x32_bf16 v[44:47], v[20:23], a[212:215], v[44:47]\n\t" \
  "v_cvt_pk_bf16_f32 v50, v34, v35\n\t" \
  "v_mov_b32 v73, s76\n\t" \
  "ds_write_b32 v85, v73\n\t" \
  "v_mfma_f32_16x16x32_bf16 v[40:43], v[8:11], a[216:219], v[40:43]\n\t" \
  "v_cvt_pk_bf16_f32 v52, v36, v37\n\t" \
  "v_mfma_f32_16x16x32_bf16 v[44:47], v[24:27], a[216:219], v[44:47]\n\t" \
  "v_cvt_pk_bf16_f32 v54, v38, v39\n\t" \
  "v_mfma_f32_16x16x32_bf16 v[40:43], v[12:15], a[220:223], v[40:43]\n\t" \
  "v_mov_b32 v74, s79\n\t" \
  "v_mfma_f32_16x16x32_bf16 v[44:47], v[28:31], a[220:223], v[44:47]\n\t" \
  "v_mfma_f32_16x16x32_bf16 v[32:35], v[0:3], a[224:227], 0\n\t" \
  "buffer_store_dwordx4 v[60:63], v80, %[ru], s45 offen offset:3072 sc1\n\t" \
  "s_waitcnt lgkmcnt(0)\n\t" \
  "s_barrier\n\t" \
  "ds_read_b32 v73, v85\n\t" \
  "v_mfma_f32_16x16x32_bf16 v[36:39], v[16:19], a[224:227], 0\n\t" \
  "buffer_store_dword v74, v84, %[ru], 0 offen sc1\n\t" \
  "v_mfma_f32_16x16x32_bf16 v[32:35], v[4:7], a[228:231], v[32:35]\n\t" \
  "v_cvt_pk_bf16_f32 v49, v40, v41\n\t" \
  "v_mfma_f32_16x16x32_bf16 v[36:39], v[20:23], a[228:231], v[36:39]\n\t" \
  "v_cvt_pk_bf16_f32 v51, v42, v43\n\t" \
  "v_mfma_f32_16x16x32_bf16 v[32:35], v[8:11], a[232:235], v[32:35]\n\t" \
  "v_cvt_pk_bf16_f32 v53, v44, v45\n\t" \
  "v_mfma_f32_16x16x32_bf16 v[36:39], v[24:27], a[232:235], v[36:39]\n\t" \
  "v_cvt_pk_bf16_f32 v55, v46, v47\n\t" \
  "v_mfma_f32_16x16x32_bf16 v[32:35], v[12:15], a[236:239], v[32:35]\n\t" \
  "s_waitcnt lgkmcnt(0)\n\t" \
  "v_readfirstlane_b32 s76, v73\n\t" \
  "v_mfma_f32_16x16x32_bf16 v[36:39], v[28:31], a[236:239], v[36:39]\n\t" \
  "s_add_i32 s45, s45, 0x1000\n\t" \
  "s_max_i32 s41, s41, s76\n\t" \
  "s_add_i32 s40, s40, 1\n\t" \
  "v_mfma_f32_16x16x32_bf16 v[40:43], v[0:3], a[240:243], 0\n\t" \
  "buffer_store_dwordx4 v[48:51], v80, %[ru], s45 offen offset:0 sc1\n\t" \
  "ds_read_b128 v[0:3], v81 offset:0\n\t" \
  "v_mfma_f32_16x16x32_bf16 v[44:47], v[16:19], a[240:243], 0\n\t" \
  "ds_read_b128 v[16:19], v81 offset:4096\n\t" \
  "v_mfma_f32_16x16x32_bf16 v[40:43], v[4:7], a[244:247], v[40:43]\n\t" \
  "v_cvt_pk_bf16_f32 v56, v32, v33\n\t" \
  "ds_read_b128 v[4:7], v81 offset:1024\n\t" \
  "v_mfma_f32_16x16x32_bf16 v[44:47], v[20:23], a[244:247], v[44:47]\n\t" \
  "v_cvt_pk_bf16_f32 v58, v34, v35\n\t" \
  "ds_read_b128 v[20:23], v81 offset:5120\n\t" \
  "v_mfma_f32_16x16x32_bf16 v[40:43], v[8:11], a[248:251], v[40:43]\n\t" \
  "v_cvt_pk_bf16_f32 v60, v36, v37\n\t" \
  "ds_read_b128 v[8:11], v81 offset:2048\n\t" \
  "v_mfma_f32_16x16x32_bf16 v[44:47], v[24:27], a[248:251], v[44:47]\n\t" \
  "v_cvt_pk_bf16_f32 v62, v38, v39\n\t" \
  "ds_read_b128 v[24:27], v81 offset:6144\n\t" \
  "v_mfma_f32_16x16x32_bf16 v[40:43], v[12:15], a[252:255], v[40:43]\n\t" \
  "ds_read_b128 v[12:15], v81 offset:3072\n\t" \
  "v_mfma_f32_16x16x32_bf16 v[44:47], v[28:31], a[252:255], v[44:47]\n\t" \
  "ds_read_b128 v[28:31], v81 offset:7168\n\t" \
  "s_mov_b32 s44, s45\n\t" \
  "s_min_i32 s46, s46, 63\n\t" \
  "s_cmp_le_i32 s40, s46\n\t" \
  "s_cbranch_scc1 20b\n\t" \
  "buffer_store_dwordx4 v[52:55], v80, %[ru], s44 offen offset:1024 sc1\n\t" \
  "s_nop 3\n\t" \
  "v_cvt_pk_bf16_f32 v57, v40, v41\n\t" \
  "v_cvt_pk_bf16_f32 v59, v42, v43\n\t" \
  "v_cvt_pk_bf16_f32 v61, v44, v45\n\t" \
  "v_cvt_pk_bf16_f32 v63, v46, v47\n\t" \
  "buffer_store_dwordx4 v[56:59], v80, %[ru], s44 offen offset:2048 sc1\n\t" \
  "buffer_store_dwordx4 v[60:63], v80, %[ru], s44 offen offset:3072 sc1\n\t" \
  "s_branch 99f\n\t" \
  "91:\n\t" \
  "ds_read_b128 v[0:3], v81 offset:0\n\t" \
  "ds_read_b128 v[16:19], v81 offset:4096\n\t" \
  "ds_read_b128 v[4:7], v81 offset:1024\n\t" \
  "ds_read_b128 v[20:23], v81 offset:5120\n\t" \
  "ds_read_b128 v[8:11], v81 offset:2048\n\t" \
  "ds_read_b128 v[24:27], v81 offset:6144\n\t" \
  "ds_read_b128 v[12:15], v81 offset:3072\n\t" \
  "ds_read_b128 v[28:31], v81 offset:7168\n\t" \
  ".p2align 6\n\t" \
  "20:\n\t" \
  "s_waitcnt lgkmcnt(7)\n\t" \
  "v_mfma_f32_16x16x32_bf16 v[32:35], v[0:3], a[0:3], 0\n\t" \
  "buffer_store_dwordx4 v[52:55], v80, %[ru], s44 offen offset:1024 sc1\n\t" \
  "s_waitcnt lgkmcnt(6)\n\t" \
  "v_mfma_f32_16x16x32_bf16 v[36:39], v[16:19], a[0:3], 0\n\t" \
  "s_and_b32 s45, s40, 15\n\t" \
  "s_waitcnt lgkmcnt(5)\n\t" \
  "v_mfma_f32_16x16x32_bf16 v[32:35], v[4:7], a[4:7], v[32:35]\n\t" \
  "v_cvt_pk_bf16_f32 v57, v40, v41\n\t" \
  "s_lshl_b32 s45, s45, 22\n\t" \
  "s_waitcnt lgkmcnt(4)\n\t" \
  "v_mfma_f32_16x16x32_bf16 v[36:39], v[20:23], a[4:7], v[36:39]\n\t" \
  "v_cvt_pk_bf16_f32 v59, v42, v43\n\t" \
  "s_add_i32 s45, s45, s42\n\t" \
  "s_waitcnt lgkmcnt(3)\n\t" \
  "v_mfma_f32_16x16x32_bf16 v[32:35], v[8:11], a[8:11], v[32:35]\n\t" \
  "v_cvt_pk_bf16_f32 v61, v44, v45\n\t" \
  "s_lshr_b32 s76, s45, 21\n\t" \
  "s_waitcnt lgkmcnt(2)\n\t" \
  "v_mfma_f32_16x16x32_bf16 v[36:39], v[24:27], a[8:11], v[36:39]\n\t" \
  "v_cvt_pk_bf16_f32 v63, v46, v47\n\t" \
  "s_bitcmp0_b32 %[sel], s76\n\t" \
  "s_cselect_b32 s76, 0x2000, 0\n\t" \
  "s_waitcnt lgkmcnt(1)\n\t" \
  "v_mfma_f32_16x16x32_bf16 v[32:35], v[12:15], a[12:15], v[32:35]\n\t" \
  "s_add_i32 s45, s45, s76\n\t" \
  "s_waitcnt lgkmcnt(0)\n\t" \
  "v_mfma_f32_16x16x32_bf16 v[36:39], v[28:31], a[12:15], v[36:39]\n\t" \
  "s_add_i32 s77, s40, 1\n\t" \
  "v_mfma_f32_16x16x32_bf16 v[40:43], v[0:3], a[16:19], 0\n\t" \
  "buffer_store_dwordx4 v[56:59], v80, %[ru], s44 offen offset:2048 sc1\n\t" \
  "v_mfma_f32_16x16x32_bf16 v[44:47], v[16:19], a[16:19], 0\n\t" \
  "s_min_u32 s77, s77, 63\n\t" \
  "v_mfma_f32_16x16x32_bf16 v[40:43], v[4:7], a[20:23], v[40:43]\n\t" \
  "v_cvt_pk_bf16_f32 v48, v32, v33\n\t" \
  "s_and_b32 s76, s77, 4\n\t" \
  "v_mfma_f32_16x16x32_bf16 v[44:47], v[20:23], a[20:23], v[44:47]\n\t" \
  "v_cvt_pk_bf16_f32 v50, v34, v35\n\t" \
  "s_lshl_b32 s76, s76, 11\n\t" \
  "v_mfma_f32_16x16x32_bf16 v[40:43], v[8:11], a[24:27], v[40:43]\n\t" \
  "v_cvt_pk_bf16_f32 v52, v36, v37\n\t" \
  "s_lshl_b32 s77, s77, 19\n\t" \
  "v_mfma_f32_16x16x32_bf16 v[44:47], v[24:27], a[24:27], v[44:47]\n\t" \
  "v_cvt_pk_bf16_f32 v54, v38, v39\n\t" \
  "s_add_i32 s77, s77, s43\n\t" \
  "v_mfma_f32_16x16x32_bf16 v[40:43], v[12:15], a[28:31], v[40:43]\n\t" \
  "s_xor_b32 s77, s77, s76\n\t" \
  "v_mfma_f32_16x16x32_bf16 v[44:47], v[28:31], a[28:31], v[44:47]\n\t" \
  "s_add_i32 s78, s40, 6\n\t" \
  "v_mfma_f32_16x16x32_bf16 v[32:35], v[0:3], a[32:35], 0\n\t" \
  "buffer_store_dwordx4 v[60:63], v80, %[ru], s44 offen offset:3072 sc1\n\t" \
  "v_mfma_f32_16x16x32_bf16 v[36:39], v[16:19], a[32:35], 0\n\t" \
  "s_cmp_lt_i32 s41, s78\n\t" \
  "s_cselect_b32 s78, 0, 0x7f000000\n\t" \
  "v_mfma_f32_16x16x32_bf16 v[32:35], v[4:7], a[36:39], v[32:35]\n\t" \
  "v_cvt_pk_bf16_f32 v49, v40, v41\n\t" \
  "s_mov_b32 s46, s41\n\t" \
  "v_mfma_f32_16x16x32_bf16 v[36:39], v[20:23], a[36:39], v[36:39]\n\t" \
  "v_cvt_pk_bf16_f32 v51, v42, v43\n\t" \
  "v_mfma_f32_16x16x32_bf16 v[32:35], v[8:11], a[40:43], v[32:35]\n\t" \
  "v_cvt_pk_bf16_f32 v53, v44, v45\n\t" \
  "v_mfma_f32_16x16x32_bf16 v[36:39], v[24:27], a[40:43], v[36:39]\n\t" \
  "v_cvt_pk_bf16_f32 v55, v46, v47\n\t" \
  "buffer_load_dwordx4 v[64:67], v80, %[ru], s77 offen sc1\n\t" \
  "v_mfma_f32_16x16x32_bf16 v[32:35], v[12:15], a[44:47], v[32:35]\n\t" \
  "buffer_load_dwordx4 v[68:71], v80, %[ru], s77 offen offset:1024 sc1\n\t" \
  "v_mfma_f32_16x16x32_bf16 v[36:39], v[28:31], a[44:47], v[36:39]\n\t" \
  "v_mfma_f32_16x16x32_bf16 v[40:43], v[0:3], a[48:51], 0\n\t" \
  "buffer_store_dwordx4 v[48:51], v80, %[ru], s45 offen offset:0 sc1\n\t" \
  "v_mfma_f32_16x16x32_bf16 v[44:47], v[16:19], a[48:51], 0\n\t" \
  "v_mfma_f32_16x16x32_bf16 v[40:43], v[4:7], a[52:55], v[40:43]\n\t" \
  "v_cvt_pk_bf16_f32 v56, v32, v33\n\t" \
  "v_mfma_f32_16x16x32_bf16 v[44:47], v[20:23], a[52:55], v[44:47]\n\t" \
  "v_cvt_pk_bf16_f32 v58, v34, v35\n\t" \
  "v_mfma_f32_16x16x32_bf16 v[40:43], v[8:11], a[56:59], v[40:43]\n\t" \
  "v_cvt_pk_bf16_f32 v60, v36, v37\n\t" \
  "v_mfma_f32_16x16x32_bf16 v[44:47], v[24:27], a[56:59], v[44:47]\n\t" \
  "v_cvt_pk_bf16_f32 v62, v38, v39\n\t" \
  "v_mfma_f32_16x16x32_bf16 v[40:43], v[12:15], a[60:63], v[40:43]\n\t" \
  "v_mfma_f32_16x16x32_bf16 v[44:47], v[28:31], a[60:63], v[44:47]\n\t" \
  "v_mfma_f32_16x16x32_bf16 v[32:35], v[0:3], a[64:67], 0\n\t" \
  "buffer_store_dwordx4 v[52:55], v80, %[ru], s45 offen offset:1024 sc1\n\t" \
  "v_mfma_f32_16x16x32_bf16 v[36:39], v[16:19], a[64:67], 0\n\t" \
  "v_mfma_f32_16x16x32_bf16 v[32:35], v[4:7], a[68:71], v[32:35]\n\t" \
  "v_cvt_pk_bf16_f32 v57, v40, v41\n\t" \
  "v_mfma_f32_16x16x32_bf16 v[36:39], v[20:23], a[68:71], v[36:39]\n\t" \
  "v_cvt_pk_bf16_f32 v59, v42, v43\n\t" \
  "v_mfma_f32_16x16x32_bf16 v[32:35], v[8:11], a[72:75], v[32:35]\n\t" \
  "v_cvt_pk_bf16_f32 v61, v44, v45\n\t" \
  "v_mfma_f32_16x16x32_bf16 v[36:39], v[24:27], a[72:75], v[36:39]\n\t" \
  "v_cvt_pk_bf16_f32 v63, v46, v47\n\t" \
  "v_mfma_f32_16x16x32_bf16 v[32:35], v[12:15], a[76:79], v[32:35]\n\t" \
  "v_mfma_f32_16x16x32_bf16 v[36:39], v[28:31], a[76:79], v[36:39]\n\t" \
  "v_mfma_f32_16x16x32_bf16 v[40:43], v[0:3], a[80:83], 0\n\t" \
  "buffer_store_dwordx4 v[56:59], v80, %[ru], s45 offen offset:2048 sc1\n\t" \
  "v_mfma_f32_16x16x32_bf16 v[44:47], v[16:19], a[80:83], 0\n\t" \
  "v_mfma_f32_16x16x32_bf16 v[40:43], v[4:7], a[84:87], v[40:43]\n\t" \
  "v_cvt_pk_bf16_f32 v48, v32, v33\n\t" \
  "v_mfma_f32_16x16x32_bf16 v[44:47], v[20:23], a[84:87], v[44:47]\n\t" \
  "v_cvt_pk_bf16_f32 v50, v34, v35\n\t" \
  "v_mfma_f32_16x16x32_bf16 v[40:43], v[8:11], a[88:91], v[40:43]\n\t" \
  "v_cvt_pk_bf16_f32 v52, v36, v37\n\t" \
  "v_mfma_f32_16x16x32_bf16 v[44:47], v[24:27], a[88:91], v[44:47]\n\t" \
  "v_cvt_pk_bf16_f32 v54, v38, v39\n\t" \
  "v_mfma_f32_16x16x32_bf16 v[40:43], v[12:15], a[92:95], v[40:43]\n\t" \
  "v_mfma_f32_16x16x32_bf16 v[44:47], v[28:31], a[92:95], v[44:47]\n\t" \
  "v_mfma_f32_16x16x32_bf16 v[32:35], v[0:3], a[96:99], 0\n\t" \
  "buffer_store_dwordx4 v[60:63], v80, %[ru], s45 offen offset:3072 sc1\n\t" \
  "v_mfma_f32_16x16x32_bf16 v[36:39], v[16:19], a[96:99], 0\n\t" \
  "v_mfma_f32_16x16x32_bf16 v[32:35], v[4:7], a[100:103], v[32:35]\n\t" \
  "v_cvt_pk_bf16_f32 v49, v40, v41\n\t" \
  "v_mfma_f32_16x16x32_bf16 v[36:39], v[20:23], a[100:103], v[36:39]\n\t" \
  "v_cvt_pk_bf16_f32 v51, v42, v43\n\t" \
  "v_mfma_f32_16x16x32_bf16 v[32:35], v[8:11], a[104:107], v[32:35]\n\t" \
  "v_cvt_pk_bf16_f32 v53, v44, v45\n\t" \
  "v_mfma_f32_16x16x32_bf16 v[36:39], v[24:27], a[104:107], v[36:39]\n\t" \
  "v_cvt_pk_bf16_f32 v55, v46, v47\n\t" \
  "v_mfma_f32_16x16x32_bf16 v[32:35], v[12:15], a[108:111], v[32:35]\n\t" \
  "v_mfma_f32_16x16x32_bf16 v[36:39], v[28:31], a[108:111], v[36:39]\n\t" \
  "s_add_i32 s45, s45, 0x1000\n\t" \
  "v_mfma_f32_16x16x32_bf16 v[40:43], v[0:3], a[112:115], 0\n\t" \
  "buffer_store_dwordx4 v[48:51], v80, %[ru], s45 offen offset:0 sc1\n\t" \
  "v_mfma_f32_16x16x32_bf16 v[44:47], v[16:19], a[112:115], 0\n\t" \
  "v_mfma_f32_16x16x32_bf16 v[40:43], v[4:7], a[116:119], v[40:43]\n\t" \
  "v_cvt_pk_bf16_f32 v56, v32, v33\n\t" \
  "v_mfma_f32_16x16x32_bf16 v[44:47], v[20:23], a[116:119], v[44:47]\n\t" \
  "v_cvt_pk_bf16_f32 v58, v34, v35\n\t" \
  "v_mfma_f32_16x16x32_bf16 v[40:43], v[8:11], a[120:123], v[40:43]\n\t" \
  "v_cvt_pk_bf16_f32 v60, v36, v37\n\t" \
  "v_mfma_f32_16x16x32_bf16 v[44:47], v[24:27], a[120:123], v[44:47]\n\t" \
  "v_cvt_pk_bf16_f32 v62, v38, v39\n\t" \
  "v_mfma_f32_16x16x32_bf16 v[40:43], v[12:15], a[124:127], v[40:43]\n\t" \
  "v_mfma_f32_16x16x32_bf16 v[44:47], v[28:31], a[124:127], v[44:47]\n\t" \
  "v_mfma_f32_16x16x32_bf16 v[32:35], v[0:3], a[128:131], 0\n\t" \
  "buffer_store_dwordx4 v[52:55], v80, %[ru], s45 offen offset:1024 sc1\n\t" \
  "v_mfma_f32_16x16x32_bf16 v[36:39], v[16:19], a[128:131], 0\n\t" \
  "v_mfma_f32_16x16x32_bf16 v[32:35], v[4:7], a[132:135], v[32:35]\n\t" \
  "v_cvt_pk_bf16_f32 v57, v40, v41\n\t" \
  "v_mfma_f32_16x16x32_bf16 v[36:39], v[20:23], a[132:135], v[36:39]\n\t" \
  "v_cvt_pk_bf16_f32 v59, v42, v43\n\t" \
  "v_mfma_f32_16x16x32_bf16 v[32:35], v[8:11], a[136:139], v[32:35]\n\t" \
  "v_cvt_pk_bf16_f32 v61, v44, v45\n\t" \
  "v_mfma_f32_16x16x32_bf16 v[36:39], v[24:27], a[136:139], v[36:39]\n\t" \
  "v_cvt_pk_bf16_f32 v63, v46, v47\n\t" \
  "v_mfma_f32_16x16x32_bf16 v[32:35], v[12:15], a[140:143], v[32:35]\n\t" \
  "v_mfma_f32_16x16x32_bf16 v[36:39], v[28:31], a[140:143], v[36:39]\n\t" \
  "v_mfma_f32_16x16x32_bf16 v[40:43], v[0:3], a[144:147], 0\n\t" \
  "buffer_store_dwordx4 v[56:59], v80, %[ru], s45 offen offset:2048 sc1\n\t" \
  "v_mfma_f32_16x16x32_bf16 v[44:47], v[16:19], a[144:147], 0\n\t" \
  "v_mfma_f32_16x16x32_bf16 v[40:43], v[4:7], a[148:151], v[40:43]\n\t" \
  "v_cvt_pk_bf16_f32 v48, v32, v33\n\t" \
  "v_mfma_f32_16x16x32_bf16 v[44:47], v[20:23], a[148:151], v[44:47]\n\t" \
  "v_cvt_pk_bf16_f32 v50, v34, v35\n\t" \
  "v_mfma_f32_16x16x32_bf16 v[40:43], v[8:11], a[152:155], v[40:43]\n\t" \
  "v_cvt_pk_bf16_f32 v52, v36, v37\n\t" \
  "v_mfma_f32_16x16x32_bf16 v[44:47], v[24:27], a[152:155], v[44:47]\n\t" \
  "v_cvt_pk_bf16_f32 v54, v38, v39\n\t" \
  "v_mfma_f32_16x16x32_bf16 v[40:43], v[12:15], a[156:159], v[40:43]\n\t" \
  "v_mfma_f32_16x16x32_bf16 v[44:47], v[28:31], a[156:159], v[44:47]\n\t" \
  "v_mfma_f32_16x16x32_bf16 v[32:35], v[0:3], a[160:163], 0\n\t" \
  "buffer_store_dwordx4 v[60:63], v80, %[ru], s45 offen offset:3072 sc1\n\t" \
  "v_mfma_f32_16x16x32_bf16 v[36:39], v[16:19], a[160:163], 0\n\t" \
  "v_mfma_f32_16x16x32_bf16 v[32:35], v[4:7], a[164:167], v[32:35]\n\t" \
  "v_cvt_pk_bf16_f32 v49, v40, v41\n\t" \
  "v_mfma_f32_16x16x32_bf16 v[36:39], v[20:23], a[164:167], v[36:39]\n\t" \
  "v_cvt_pk_bf16_f32 v51, v42, v43\n\t" \
  "v_mfma_f32_16x16x32_bf16 v[32:35], v[8:11], a[168:171], v[32:35]\n\t" \
  "v_cvt_pk_bf16_f32 v53, v44, v45\n\t" \
  "v_mfma_f32_16x16x32_bf16 v[36:39], v[24:27], a[168:171], v[36:39]\n\t" \
  "v_cvt_pk_bf16_f32 v55, v46, v47\n\t" \
  "v_mfma_f32_16x16x32_bf16 v[32:35], v[12:15], a[172:175], v[32:35]\n\t" \
  "v_mfma_f32_16x16x32_bf16 v[36:39], v[28:31], a[172:175], v[36:39]\n\t" \
  "s_add_i32 s45, s45, 0x3000\n\t" \
  "v_mfma_f32_16x16x32_bf16 v[40:43], v[0:3], a[176:179], 0\n\t" \
  "buffer_store_dwordx4 v[48:51], v80, %[ru], s45 offen offset:0 sc1\n\t" \
  "v_mfma_f32_16x16x32_bf16 v[44:47], v[16:19], a[176:179], 0\n\t" \
  "v_mfma_f32_16x16x32_bf16 v[40:43], v[4:7], a[180:183], v[40:43]\n\t" \
  "v_cvt_pk_bf16_f32 v56, v32, v33\n\t" \
  "v_mfma_f32_16x16x32_bf16 v[44:47], v[20:23], a[180:183], v[44:47]\n\t" \
  "v_cvt_pk_bf16_f32 v58, v34, v35\n\t" \
  "v_mfma_f32_16x16x32_bf16 v[40:43], v[8:11], a[184:187], v[40:43]\n\t" \
  "v_cvt_pk_bf16_f32 v60, v36, v37\n\t" \
  "v_mfma_f32_16x16x32_bf16 v[44:47], v[24:27], a[184:187], v[44:47]\n\t" \
  "v_cvt_pk_bf16_f32 v62, v38, v39\n\t" \
  "v_mfma_f32_16x16x32_bf16 v[40:43], v[12:15], a[188:191], v[40:43]\n\t" \
  "v_xor_b32 v81, 0x2000, v81\n\t" \
  "v_mfma_f32_16x16x32_bf16 v[44:47], v[28:31], a[188:191], v[44:47]\n\t" \
  "v_xor_b32 v82, 0x2000, v82\n\t" \
  "v_mfma_f32_16x16x32_bf16 v[32:35], v[0:3], a[192:195], 0\n\t" \
  "buffer_store_dwordx4 v[52:55], v80, %[ru], s45 offen offset:1024 sc1\n\t" \
  "v_mfma_f32_16x16x32_bf16 v[36:39], v[16:19], a[192:195], 0\n\t" \
  "v_mfma_f32_16x16x32_bf16 v[32:35], v[4:7], a[196:199], v[32:35]\n\t" \
  "v_cvt_pk_bf16_f32 v57, v40, v41\n\t" \
  "v_mfma_f32_16x16x32_bf16 v[36:39], v[20:23], a[196:199], v[36:39]\n\t" \
  "v_cvt_pk_bf16_f32 v59, v42, v43\n\t" \
  "v_mfma_f32_16x16x32_bf16 v[32:35], v[8:11], a[200:203], v[32:35]\n\t" \
  "v_cvt_pk_bf16_f32 v61, v44, v45\n\t" \
  "v_mfma_f32_16x16x32_bf16 v[36:39], v[24:27], a[200:203], v[36:39]\n\t" \
  "v_cvt_pk_bf16_f32 v63, v46, v47\n\t" \
  "v_mfma_f32_16x16x32_bf16 v[32:35], v[12:15], a[204:207], v[32:35]\n\t" \
  "s_add_i32 s79, s40, -1\n\t" \
  "v_xor_b32 v85, 8, v85\n\t" \
  "v_mfma_f32_16x16x32_bf16 v[36:39], v[28:31], a[204:207], v[36:39]\n\t" \
  "s_waitcnt vmcnt(10)\n\t" \
  "ds_write_b128 v82, v[64:67]\n\t" \
  "v_mfma_f32_16x16x32_bf16 v[40:43], v[0:3], a[208:211], 0\n\t" \
  "buffer_store_dwordx4 v[56:59], v80, %[ru], s45 offen offset:2048 sc1\n\t" \
  "v_mfma_f32_16x16x32_bf16 v[44:47], v[16:19], a[208:211], 0\n\t" \
  "ds_write_b128 v82, v[68:71] offset:1024\n\t" \
  "v_mfma_f32_16x16x32_bf16 v[40:43], v[4:7], a[212:215], v[40:43]\n\t" \
  "v_cvt_pk_bf16_f32 v48, v32, v33\n\t" \
  "v_mfma_f32_16x16x32_bf16 v[44:47], v[20:23], a[212:215], v[44:47]\n\t" \
  "v_cvt_pk_bf16_f32 v50, v34, v35\n\t" \
  "v_mfma_f32_16x16x32_bf16 v[40:43], v[8:11], a[216:219], v[40:43]\n\t" \
  "v_cvt_pk_bf16_f32 v52, v36, v37\n\t" \
  "v_mfma_f32_16x16x32_bf16 v[44:47], v[24:27], a[216:219], v[44:47]\n\t" \
  "v_cvt_pk_bf16_f32 v54, v38, v39\n\t" \
  "v_mfma_f32_16x16x32_bf16 v[40:43], v[12:15], a[220:223], v[40:43]\n\t" \
  "v_mov_b32 v74, s79\n\t" \
  "v_mfma_f32_16x16x32_bf16 v[44:47], v[28:31], a[220:223], v[44:47]\n\t" \
  "v_mfma_f32_16x16x32_bf16 v[32:35], v[0:3], a[224:227], 0\n\t" \
  "buffer_store_dwordx4 v[60:63], v80, %[ru], s45 offen offset:3072 sc1\n\t" \
  "s_waitcnt lgkmcnt(0)\n\t" \
  "s_barrier\n\t" \
  "ds_read_b32 v73, v85\n\t" \
  "v_mfma_f32_16x16x32_bf16 v[36:39], v[16:19], a[224:227], 0\n\t" \
  "v_mfma_f32_16x16x32_bf16 v[32:35], v[4:7], a[228:231], v[32:35]\n\t" \
  "v_cvt_pk_bf16_f32 v49, v40, v41\n\t" \
  "v_mfma_f32_16x16x32_bf16 v[36:39], v[20:23], a[228:231], v[36:39]\n\t" \
  "v_cvt_pk_bf16_f32 v51, v42, v43\n\t" \
  "v_mfma_f32_16x16x32_bf16 v[32:35], v[8:11], a[232:235], v[32:35]\n\t" \
  "v_cvt_pk_bf16_f32 v53, v44, v45\n\t" \
  "v_mfma_f32_16x16x32_bf16 v[36:39], v[24:27], a[232:235], v[36:39]\n\t" \
  "v_cvt_pk_bf16_f32 v55, v46, v47\n\t" \
  "v_mfma_f32_16x16x32_bf16 v[32:35], v[12:15], a[236:239], v[32:35]\n\t" \
  "s_waitcnt lgkmcnt(0)\n\t" \
  "v_readfirstlane_b32 s76, v73\n\t" \
  "v_mfma_f32_16x16x32_bf16 v[36:39], v[28:31], a[236:239], v[36:39]\n\t" \
  "s_add_i32 s45, s45, 0x1000\n\t" \
  "s_max_i32 s41, s41, s76\n\t" \
  "s_add_i32 s40, s40, 1\n\t" \
  "v_mfma_f32_16x16x32_bf16 v[40:43], v[0:3], a[240:243], 0\n\t" \
  "buffer_store_dwordx4 v[48:51], v80, %[ru], s45 offen offset:0 sc1\n\t" \
  "ds_read_b128 v[0:3], v81 offset:0\n\t" \
  "v_mfma_f32_16x16x32_bf16 v[44:47], v[16:19], a[240:243], 0\n\t" \
  "ds_read_b128 v[16:19], v81 offset:4096\n\t" \
  "v_mfma_f32_16x16x32_bf16 v[40:43], v[4:7], a[244:247], v[40:43]\n\t" \
  "v_cvt_pk_bf16_f32 v56, v32, v33\n\t" \
  "ds_read_b128 v[4:7], v81 offset:1024\n\t" \
  "v_mfma_f32_16x16x32_bf16 v[44:47], v[20:23], a[244:247], v[44:47]\n\t" \
  "v_cvt_pk_bf16_f32 v58, v34, v35\n\t" \
  "ds_read_b128 v[20:23], v81 offset:5120\n\t" \
  "v_mfma_f32_16x16x32_bf16 v[40:43], v[8:11], a[248:251], v[40:43]\n\t" \
  "v_cvt_pk_bf16_f32 v60, v36, v37\n\t" \
  "ds_read_b128 v[8:11], v81 offset:2048\n\t" \
  "v_mfma_f32_16x16x32_bf16 v[44:47], v[24:27], a[248:251], v[44:47]\n\t" \
  "v_cvt_pk_bf16_f32 v62, v38, v39\n\t" \
  "ds_read_b128 v[24:27], v81 offset:6144\n\t" \
  "v_mfma_f32_16x16x32_bf16 v[40:43], v[12:15], a[252:255], v[40:43]\n\t" \
  "ds_read_b128 v[12:15], v81 offset:3072\n\t" \
  "v_mfma_f32_16x16x32_bf16 v[44:47], v[28:31], a[252:255], v[44:47]\n\t" \
  "ds_read_b128 v[28:31], v81 offset:7168\n\t" \
  "s_mov_b32 s44, s45\n\t" \
  "s_min_i32 s46, s46, 63\n\t" \
  "s_cmp_le_i32 s40, s46\n\t" \
  "s_cbranch_scc1 20b\n\t" \
  "buffer_store_dwordx4 v[52:55], v80, %[ru], s44 offen offset:1024 sc1\n\t" \
  "s_nop 3\n\t" \
  "v_cvt_pk_bf16_f32 v57, v40, v41\n\t" \
  "v_cvt_pk_bf16_f32 v59, v42, v43\n\t" \
  "v_cvt_pk_bf16_f32 v61, v44, v45\n\t" \
  "v_cvt_pk_bf16_f32 v63, v46, v47\n\t" \
  "buffer_store_dwordx4 v[56:59], v80, %[ru], s44 offen offset:2048 sc1\n\t" \
  "buffer_store_dwordx4 v[60:63], v80, %[ru], s44 offen offset:3072 sc1\n\t" \
  "s_branch 99f\n\t" \
  "92:\n\t" \
  "ds_read_b128 v[0:3], v81 offset:0\n\t" \
  "ds_read_b128 v[16:19], v81 offset:4096\n\t" \
  "ds_read_b128 v[4:7], v81 offset:1024\n\t" \
  "ds_read_b128 v[20:23], v81 offset:5120\n\t" \
  "ds_read_b128 v[8:11], v81 offset:2048\n\t" \
  "ds_read_b128 v[24:27], v81 offset:6144\n\t" \
  "ds_read_b128 v[12:15], v81 offset:3072\n\t" \
  "ds_read_b128 v[28:31], v81 offset:7168\n\t" \
  ".p2align 6\n\t" \
  "20:\n\t" \
  "s_waitcnt lgkmcnt(7)\n\t" \
  "v_mfma_f32_16x16x32_bf16 v[32:35], v[0:3], a[0:3], 0\n\t" \
  "buffer_store_dwordx4 v[52:55], v80, %[ru], s44 offen offset:1024 sc1\n\t" \
  "s_waitcnt lgkmcnt(6)\n\t" \
  "v_mfma_f32_16x16x32_bf16 v[36:39], v[16:19], a[0:3], 0\n\t" \
  "s_and_b32 s45, s40, 15\n\t" \
  "s_waitcnt lgkmcnt(5)\n\t" \
  "v_mfma_f32_16x16x32_bf16 v[32:35], v[4:7], a[4:7], v[32:35]\n\t" \
  "v_cvt_pk_bf16_f32 v57, v40, v41\n\t" \
  "s_lshl_b32 s45, s45, 22\n\t" \
  "s_waitcnt lgkmcnt(4)\n\t" \
  "v_mfma_f32_16x16x32_bf16 v[36:39], v[20:23], a[4:7], v[36:39]\n\t" \
  "v_cvt_pk_bf16_f32 v59, v42, v43\n\t" \
  "s_add_i32 s45, s45, s42\n\t" \
  "s_waitcnt lgkmcnt(3)\n\t" \
  "v_mfma_f32_16x16x32_bf16 v[32:35], v[8:11], a[8:11], v[32:35]\n\t" \
  "v_cvt_pk_bf16_f32 v61, v44, v45\n\t" \
  "s_lshr_b32 s76, s45, 21\n\t" \
  "s_waitcnt lgkmcnt(2)\n\t" \
  "v_mfma_f32_16x16x32_bf16 v[36:39], v[24:27], a[8:11], v[36:39]\n\t" \
  "v_cvt_pk_bf16_f32 v63, v46, v47\n\t" \
  "s_bitcmp0_b32 %[sel], s76\n\t" \
  "s_cselect_b32 s76, 0x2000, 0\n\t" \
  "s_waitcnt lgkmcnt(1)\n\t" \
  "v_mfma_f32_16x16x32_bf16 v[32:35], v[12:15], a[12:15], v[32:35]\n\t" \
  "s_add_i32 s45, s45, s76\n\t" \
  "s_waitcnt lgkmcnt(0)\n\t" \
  "v_mfma_f32_16x16x32_bf16 v[36:39], v[28:31], a[12:15], v[36:39]\n\t" \
  "s_add_i32 s77, s40, 1\n\t" \
  "v_mfma_f32_16x16x32_bf16 v[40:43], v[0:3], a[16:19], 0\n\t" \
  "buffer_store_dwordx4 v[56:59], v80, %[ru], s44 offen offset:2048 sc1\n\t" \
  "v_mfma_f32_16x16x32_bf16 v[44:47], v[16:19], a[16:19], 0\n\t" \
  "s_min_u32 s77, s77, 63\n\t" \
  "v_mfma_f32_16x16x32_bf16 v[40:43], v[4:7], a[20:23], v[40:43]\n\t" \
  "v_cvt_pk_bf16_f32 v48, v32, v33\n\t" \
  "s_and_b32 s76, s77, 4\n\t" \
  "v_mfma_f32_16x16x32_bf16 v[44:47], v[20:23], a[20:23], v[44:47]\n\t" \
  "v_cvt_pk_bf16_f32 v50, v34, v35\n\t" \
  "s_lshl_b32 s76, s76, 11\n\t" \
  "v_mfma_f32_16x16x32_bf16 v[40:43], v[8:11], a[24:27], v[40:43]\n\t" \
  "v_cvt_pk_bf16_f32 v52, v36, v37\n\t" \
  "s_lshl_b32 s77, s77, 19\n\t" \
  "v_mfma_f32_16x16x32_bf16 v[44:47], v[24:27], a[24:27], v[44:47]\n\t" \
  "v_cvt_pk_bf16_f32 v54, v38, v39\n\t" \
  "s_add_i32 s77, s77, s43\n\t" \
  "v_mfma_f32_16x16x32_bf16 v[40:43], v[12:15], a[28:31], v[40:43]\n\t" \
  "s_xor_b32 s77, s77, s76\n\t" \
  "v_mfma_f32_16x16x32_bf16 v[44:47], v[28:31], a[28:31], v[44:47]\n\t" \
  "s_add_i32 s78, s40, 6\n\t" \
  "v_mfma_f32_16x16x32_bf16 v[32:35], v[0:3], a[32:35], 0\n\t" \
  "buffer_store_dwordx4 v[60:63], v80, %[ru], s44 offen offset:3072 sc1\n\t" \
  "v_mfma_f32_16x16x32_bf16 v[36:39], v[16:19], a[32:35], 0\n\t" \
  "s_cmp_lt_i32 s41, s78\n\t" \
  "s_cselect_b32 s78, 0, 0x7f000000\n\t" \
  "v_mfma_f32_16x16x32_bf16 v[32:35], v[4:7], a[36:39], v[32:35]\n\t" \
  "v_cvt_pk_bf16_f32 v49, v40, v41\n\t" \
  "s_mov_b32 s46, s41\n\t" \
  "v_mfma_f32_16x16x32_bf16 v[36:39], v[20:23], a[36:39], v[36:39]\n\t" \
  "v_cvt_pk_bf16_f32 v51, v42, v43\n\t" \
  "v_mfma_f32_16x16x32_bf16 v[32:35], v[8:11], a[40:43], v[32:35]\n\t" \
  "v_cvt_pk_bf16_f32 v53, v44, v45\n\t" \
  "v_mfma_f32_16x16x32_bf16 v[36:39], v[24:27], a[40:43], v[36:39]\n\t" \
  "v_cvt_pk_bf16_f32 v55, v46, v47\n\t" \
  "buffer_load_dwordx4 v[64:67], v80, %[ru], s77 offen sc1\n\t" \
  "v_mfma_f32_16x16x32_bf16 v[32:35], v[12:15], a[44:47], v[32:35]\n\t" \
  "buffer_load_dwordx4 v[68:71], v80, %[ru], s77 offen offset:1024 sc1\n\t" \
  "v_mfma_f32_16x16x32_bf16 v[36:39], v[28:31], a[44:47], v[36:39]\n\t" \
  "v_mfma_f32_16x16x32_bf16 v[40:43], v[0:3], a[48:51], 0\n\t" \
  "buffer_store_dwordx4 v[48:51], v80, %[ru], s45 offen offset:0 sc1\n\t" \
  "v_mfma_f32_16x16x32_bf16 v[44:47], v[16:19], a[48:51], 0\n\t" \
  "v_mfma_f32_16x16x32_bf16 v[40:43], v[4:7], a[52:55], v[40:43]\n\t" \
  "v_cvt_pk_bf16_f32 v56, v32, v33\n\t" \
  "v_mfma_f32_16x16x32_bf16 v[44:47], v[20:23], a[52:55], v[44:47]\n\t" \
  "v_cvt_pk_bf16_f32 v58, v34, v35\n\t" \
  "v_mfma_f32_16x16x32_bf16 v[40:43], v[8:11], a[56:59], v[40:43]\n\t" \
  "v_cvt_pk_bf16_f32 v60, v36, v37\n\t" \
  "v_mfma_f32_16x16x32_bf16 v[44:47], v[24:27], a[56:59], v[44:47]\n\t" \
  "v_cvt_pk_bf16_f32 v62, v38, v39\n\t" \
  "v_mfma_f32_16x16x32_bf16 v[40:43], v[12:15], a[60:63], v[40:43]\n\t" \
  "v_mfma_f32_16x16x32_bf16 v[44:47], v[28:31], a[60:63], v[44:47]\n\t" \
  "v_mfma_f32_16x16x32_bf16 v[32:35], v[0:3], a[64:67], 0\n\t" \
  "buffer_store_dwordx4 v[52:55], v80, %[ru], s45 offen offset:1024 sc1\n\t" \
  "v_mfma_f32_16x16x32_bf16 v[36:39], v[16:19], a[64:67], 0\n\t" \
  "v_mfma_f32_16x16x32_bf16 v[32:35], v[4:7], a[68:71], v[32:35]\n\t" \
  "v_cvt_pk_bf16_f32 v57, v40, v41\n\t" \
  "v_mfma_f32_16x16x32_bf16 v[36:39], v[20:23], a[68:71], v[36:39]\n\t" \
  "v_cvt_pk_bf16_f32 v59, v42, v43\n\t" \
  "v_mfma_f32_16x16x32_bf16 v[32:35], v[8:11], a[72:75], v[32:35]\n\t" \
  "v_cvt_pk_bf16_f32 v61, v44, v45\n\t" \
  "v_mfma_f32_16x16x32_bf16 v[36:39], v[24:27], a[72:75], v[36:39]\n\t" \
  "v_cvt_pk_bf16_f32 v63, v46, v47\n\t" \
  "v_mfma_f32_16x16x32_bf16 v[32:35], v[12:15], a[76:79], v[32:35]\n\t" \
  "v_mfma_f32_16x16x32_bf16 v[36:39], v[28:31], a[76:79], v[36:39]\n\t" \
  "v_mfma_f32_16x16x32_bf16 v[40:43], v[0:3], a[80:83], 0\n\t" \
  "buffer_store_dwordx4 v[56:59], v80, %[ru], s45 offen offset:2048 sc1\n\t" \
  "v_mfma_f32_16x16x32_bf16 v[44:47], v[16:19], a[80:83], 0\n\t" \
  "v_mfma_f32_16x16x32_bf16 v[40:43], v[4:7], a[84:87], v[40:43]\n\t" \
  "v_cvt_pk_bf16_f32 v48, v32, v33\n\t" \
  "v_mfma_f32_16x16x32_bf16 v[44:47], v[20:23], a[84:87], v[44:47]\n\t" \
  "v_cvt_pk_bf16_f32 v50, v34, v35\n\t" \
  "v_mfma_f32_16x16x32_bf16 v[40:43], v[8:11], a[88:91], v[40:43]\n\t" \
  "v_cvt_pk_bf16_f32 v52, v36, v37\n\t" \
  "v_mfma_f32_16x16x32_bf16 v[44:47], v[24:27], a[88:91], v[44:47]\n\t" \
  "v_cvt_pk_bf16_f32 v54, v38, v39\n\t" \
  "v_mfma_f32_16x16x32_bf16 v[40:43], v[12:15], a[92:95], v[40:43]\n\t" \
  "v_mfma_f32_16x16x32_bf16 v[44:47], v[28:31], a[92:95], v[44:47]\n\t" \
  "v_mfma_f32_16x16x32_bf16 v[32:35], v[0:3], a[96:99], 0\n\t" \
  "buffer_store_dwordx4 v[60:63], v80, %[ru], s45 offen offset:3072 sc1\n\t" \
  "v_mfma_f32_16x16x32_bf16 v[36:39], v[16:19], a[96:99], 0\n\t" \
  "v_mfma_f32_16x16x32_bf16 v[32:35], v[4:7], a[100:103], v[32:35]\n\t" \
  "v_cvt_pk_bf16_f32 v49, v40, v41\n\t" \
  "v_mfma_f32_16x16x32_bf16 v[36:39], v[20:23], a[100:103], v[36:39]\n\t" \
  "v_cvt_pk_bf16_f32 v51, v42, v43\n\t" \
  "v_mfma_f32_16x16x32_bf16 v[32:35], v[8:11], a[104:107], v[32:35]\n\t" \
  "v_cvt_pk_bf16_f32 v53, v44, v45\n\t" \
  "v_mfma_f32_16x16x32_bf16 v[36:39], v[24:27], a[104:107], v[36:39]\n\t" \
  "v_cvt_pk_bf16_f32 v55, v46, v47\n\t" \
  "v_mfma_f32_16x16x32_bf16 v[32:35], v[12:15], a[108:111], v[32:35]\n\t" \
  "v_mfma_f32_16x16x32_bf16 v[36:39], v[28:31], a[108:111], v[36:39]\n\t" \
  "s_add_i32 s45, s45, 0x1000\n\t" \
  "v_mfma_f32_16x16x32_bf16 v[40:43], v[0:3], a[112:115], 0\n\t" \
  "buffer_store_dwordx4 v[48:51], v80, %[ru], s45 offen offset:0 sc1\n\t" \
  "v_mfma_f32_16x16x32_bf16 v[44:47], v[16:19], a[112:115], 0\n\t" \
  "v_mfma_f32_16x16x32_bf16 v[40:43], v[4:7], a[116:119], v[40:43]\n\t" \
  "v_cvt_pk_bf16_f32 v56, v32, v33\n\t" \
  "v_mfma_f32_16x16x32_bf16 v[44:47], v[20:23], a[116:119], v[44:47]\n\t" \
  "v_cvt_pk_bf16_f32 v58, v34, v35\n\t" \
  "v_mfma_f32_16x16x32_bf16 v[40:43], v[8:11], a[120:123], v[40:43]\n\t" \
  "v_cvt_pk_bf16_f32 v60, v36, v37\n\t" \
  "v_mfma_f32_16x16x32_bf16 v[44:47], v[24:27], a[120:123], v[44:47]\n\t" \
  "v_cvt_pk_bf16_f32 v62, v38, v39\n\t" \
  "v_mfma_f32_16x16x32_bf16 v[40:43], v[12:15], a[124:127], v[40:43]\n\t" \
  "v_mfma_f32_16x16x32_bf16 v[44:47], v[28:31], a[124:127], v[44:47]\n\t" \
  "v_mfma_f32_16x16x32_bf16 v[32:35], v[0:3], a[128:131], 0\n\t" \
  "buffer_store_dwordx4 v[52:55], v80, %[ru], s45 offen offset:1024 sc1\n\t" \
  "v_mfma_f32_16x16x32_bf16 v[36:39], v[16:19], a[128:131], 0\n\t" \
  "v_mfma_f32_16x16x32_bf16 v[32:35], v[4:7], a[132:135], v[32:35]\n\t" \
  "v_cvt_pk_bf16_f32 v57, v40, v41\n\t" \
  "v_mfma_f32_16x16x32_bf16 v[36:39], v[20:23], a[132:135], v[36:39]\n\t" \
  "v_cvt_pk_bf16_f32 v59, v42, v43\n\t" \
  "v_mfma_f32_16x16x32_bf16 v[32:35], v[8:11], a[136:139], v[32:35]\n\t" \
  "v_cvt_pk_bf16_f32 v61, v44, v45\n\t" \
  "v_mfma_f32_16x16x32_bf16 v[36:39], v[24:27], a[136:139], v[36:39]\n\t" \
  "v_cvt_pk_bf16_f32 v63, v46, v47\n\t" \
  "v_mfma_f32_16x16x32_bf16 v[32:35], v[12:15], a[140:143], v[32:35]\n\t" \
  "v_mfma_f32_16x16x32_bf16 v[36:39], v[28:31], a[140:143], v[36:39]\n\t" \
  "v_mfma_f32_16x16x32_bf16 v[40:43], v[0:3], a[144:147], 0\n\t" \
  "buffer_store_dwordx4 v[56:59], v80, %[ru], s45 offen offset:2048 sc1\n\t" \
  "v_mfma_f32_16x16x32_bf16 v[44:47], v[16:19], a[144:147], 0\n\t" \
  "v_mfma_f32_16x16x32_bf16 v[40:43], v[4:7], a[148:151], v[40:43]\n\t" \
  "v_cvt_pk_bf16_f32 v48, v32, v33\n\t" \
  "v_mfma_f32_16x16x32_bf16 v[44:47], v[20:23], a[148:151], v[44:47]\n\t" \
  "v_cvt_pk_bf16_f32 v50, v34, v35\n\t" \
  "v_mfma_f32_16x16x32_bf16 v[40:43], v[8:11], a[152:155], v[40:43]\n\t" \
  "v_cvt_pk_bf16_f32 v52, v36, v37\n\t" \
  "v_mfma_f32_16x16x32_bf16 v[44:47], v[24:27], a[152:155], v[44:47]\n\t" \
  "v_cvt_pk_bf16_f32 v54, v38, v39\n\t" \
  "v_mfma_f32_16x16x32_bf16 v[40:43], v[12:15], a[156:159], v[40:43]\n\t" \
  "v_mfma_f32_16x16x32_bf16 v[44:47], v[28:31], a[156:159], v[44:47]\n\t" \
  "v_mfma_f32_16x16x32_bf16 v[32:35], v[0:3], a[160:163], 0\n\t" \
  "buffer_store_dwordx4 v[60:63], v80, %[ru], s45 offen offset:3072 sc1\n\t" \
  "v_mfma_f32_16x16x32_bf16 v[36:39], v[16:19], a[160:163], 0\n\t" \
  "v_mfma_f32_16x16x32_bf16 v[32:35], v[4:7], a[164:167], v[32:35]\n\t" \
  "v_cvt_pk_bf16_f32 v49, v40, v41\n\t" \
  "v_mfma_f32_16x16x32_bf16 v[36:39], v[20:23], a[164:167], v[36:39]\n\t" \
  "v_cvt_pk_bf16_f32 v51, v42, v43\n\t" \
  "v_mfma_f32_16x16x32_bf16 v[32:35], v[8:11], a[168:171], v[32:35]\n\t" \
  "v_cvt_pk_bf16_f32 v53, v44, v45\n\t" \
  "v_mfma_f32_16x16x32_bf16 v[36:39], v[24:27], a[168:171], v[36:39]\n\t" \
  "v_cvt_pk_bf16_f32 v55, v46, v47\n\t" \
  "v_mfma_f32_16x16x32_bf16 v[32:35], v[12:15], a[172:175], v[32:35]\n\t" \
  "v_mfma_f32_16x16x32_bf16 v[36:39], v[28:31], a[172:175], v[36:39]\n\t" \
  "s_add_i32 s45, s45, 0x3000\n\t" \
  "v_mfma_f32_16x16x32_bf16 v[40:43], v[0:3], a[176:179], 0\n\t" \
  "buffer_store_dwordx4 v[48:51], v80, %[ru], s45 offen offset:0 sc1\n\t" \
  "v_mfma_f32_16x16x32_bf16 v[44:47], v[16:19], a[176:179], 0\n\t" \
  "v_mfma_f32_16x16x32_bf16 v[40:43], v[4:7], a[180:183], v[40:43]\n\t" \
  "v_cvt_pk_bf16_f32 v56, v32, v33\n\t" \
  "v_mfma_f32_16x16x32_bf16 v[44:47], v[20:23], a[180:183], v[44:47]\n\t" \
  "v_cvt_pk_bf16_f32 v58, v34, v35\n\t" \
  "v_mfma_f32_16x16x32_bf16 v[40:43], v[8:11], a[184:187], v[40:43]\n\t" \
  "v_cvt_pk_bf16_f32 v60, v36, v37\n\t" \
  "v_mfma_f32_16x16x32_bf16 v[44:47], v[24:27], a[184:187], v[44:47]\n\t" \
  "v_cvt_pk_bf16_f32 v62, v38, v39\n\t" \
  "v_mfma_f32_16x16x32_bf16 v[40:43], v[12:15], a[188:191], v[40:43]\n\t" \
  "v_xor_b32 v81, 0x2000, v81\n\t" \
  "v_mfma_f32_16x16x32_bf16 v[44:47], v[28:31], a[188:191], v[44:47]\n\t" \
  "v_xor_b32 v82, 0x2000, v82\n\t" \
  "v_mfma_f32_16x16x32_bf16 v[32:35], v[0:3], a[192:195], 0\n\t" \
  "buffer_store_dwordx4 v[52:55], v80, %[ru], s45 offen offset:1024 sc1\n\t" \
  "v_mfma_f32_16x16x32_bf16 v[36:39], v[16:19], a[192:195], 0\n\t" \
  "v_mfma_f32_16x16x32_bf16 v[32:35], v[4:7], a[196:199], v[32:35]\n\t" \
  "v_cvt_pk_bf16_f32 v57, v40, v41\n\t" \
  "v_mfma_f32_16x16x32_bf16 v[36:39], v[20:23], a[196:199], v[36:39]\n\t" \
  "v_cvt_pk_bf16_f32 v59, v42, v43\n\t" \
  "v_mfma_f32_16x16x32_bf16 v[32:35], v[8:11], a[200:203], v[32:35]\n\t" \
  "v_cvt_pk_bf16_f32 v61, v44, v45\n\t" \
  "v_mfma_f32_16x16x32_bf16 v[36:39], v[24:27], a[200:203], v[36:39]\n\t" \
  "v_cvt_pk_bf16_f32 v63, v46, v47\n\t" \
  "v_mfma_f32_16x16x32_bf16 v[32:35], v[12:15], a[204:207], v[32:35]\n\t" \
  "s_add_i32 s79, s40, -1\n\t" \
  "v_xor_b32 v85, 8, v85\n\t" \
  "v_mfma_f32_16x16x32_bf16 v[36:39], v[28:31], a[204:207], v[36:39]\n\t" \
  "s_waitcnt vmcnt(10)\n\t" \
  "ds_write_b128 v82, v[64:67]\n\t" \
  "v_mfma_f32_16x16x32_bf16 v[40:43], v[0:3], a[208:211], 0\n\t" \
  "buffer_store_dwordx4 v[56:59], v80, %[ru], s45 offen offset:2048 sc1\n\t" \
  "v_mfma_f32_16x16x32_bf16 v[44:47], v[16:19], a[208:211], 0\n\t" \
  "ds_write_b128 v82, v[68:71] offset:1024\n\t" \
  "v_mfma_f32_16x16x32_bf16 v[40:43], v[4:7], a[212:215], v[40:43]\n\t" \
  "v_cvt_pk_bf16_f32 v48, v32, v33\n\t" \
  "v_mfma_f32_16x16x32_bf16 v[44:47], v[20:23], a[212:215], v[44:47]\n\t" \
  "v_cvt_pk_bf16_f32 v50, v34, v35\n\t" \
  "v_mfma_f32_16x16x32_bf16 v[40:43], v[8:11], a[216:219], v[40:43]\n\t" \
  "v_cvt_pk_bf16_f32 v52, v36, v37\n\t" \
  "v_mfma_f32_16x16x32_bf16 v[44:47], v[24:27], a[216:219], v[44:47]\n\t" \
  "v_cvt_pk_bf16_f32 v54, v38, v39\n\t" \
  "v_mfma_f32_16x16x32_bf16 v[40:43], v[12:15], a[220:223], v[40:43]\n\t" \
  "v_mov_b32 v74, s79\n\t" \
  "v_mfma_f32_16x16x32_bf16 v[44:47], v[28:31], a[220:223], v[44:47]\n\t" \
  "v_mfma_f32_16x16x32_bf16 v[32:35], v[0:3], a[224:227], 0\n\t" \
  "buffer_store_dwordx4 v[60:63], v80, %[ru], s45 offen offset:3072 sc1\n\t" \
  "s_waitcnt lgkmcnt(0)\n\t" \
  "s_barrier\n\t" \
  "ds_read_b32 v73, v85\n\t" \
  "v_mfma_f32_16x16x32_bf16 v[36:39], v[16:19], a[224:227], 0\n\t" \
  "v_mfma_f32_16x16x32_bf16 v[32:35], v[4:7], a[228:231], v[32:35]\n\t" \
  "v_cvt_pk_bf16_f32 v49, v40, v41\n\t" \
  "v_mfma_f32_16x16x32_bf16 v[36:39], v[20:23], a[228:231], v[36:39]\n\t" \
  "v_cvt_pk_bf16_f32 v51, v42, v43\n\t" \
  "v_mfma_f32_16x16x32_bf16 v[32:35], v[8:11], a[232:235], v[32:35]\n\t" \
  "v_cvt_pk_bf16_f32 v53, v44, v45\n\t" \
  "v_mfma_f32_16x16x32_bf16 v[36:39], v[24:27], a[232:235], v[36:39]\n\t" \
  "v_cvt_pk_bf16_f32 v55, v46, v47\n\t" \
  "v_mfma_f32_16x16x32_bf16 v[32:35], v[12:15], a[236:239], v[32:35]\n\t" \
  "s_waitcnt lgkmcnt(0)\n\t" \
  "v_readfirstlane_b32 s76, v73\n\t" \
  "v_mfma_f32_16x16x32_bf16 v[36:39], v[28:31], a[236:239], v[36:39]\n\t" \
  "s_add_i32 s45, s45, 0x1000\n\t" \
  "s_max_i32 s41, s41, s76\n\t" \
  "s_add_i32 s40, s40, 1\n\t" \
  "v_mfma_f32_16x16x32_bf16 v[40:43], v[0:3], a[240:243], 0\n\t" \
  "buffer_store_dwordx4 v[48:51], v80, %[ru], s45 offen offset:0 sc1\n\t" \
  "ds_read_b128 v[0:3], v81 offset:0\n\t" \
  "v_mfma_f32_16x16x32_bf16 v[44:47], v[16:19], a[240:243], 0\n\t" \
  "ds_read_b128 v[16:19], v81 offset:4096\n\t" \
  "v_mfma_f32_16x16x32_bf16 v[40:43], v[4:7], a[244:247], v[40:43]\n\t" \
  "v_cvt_pk_bf16_f32 v56, v32, v33\n\t" \
  "ds_read_b128 v[4:7], v81 offset:1024\n\t" \
  "v_mfma_f32_16x16x32_bf16 v[44:47], v[20:23], a[244:247], v[44:47]\n\t" \
  "v_cvt_pk_bf16_f32 v58, v34, v35\n\t" \
  "ds_read_b128 v[20:23], v81 offset:5120\n\t" \
  "v_mfma_f32_16x16x32_bf16 v[40:43], v[8:11], a[248:251], v[40:43]\n\t" \
  "v_cvt_pk_bf16_f32 v60, v36, v37\n\t" \
  "ds_read_b128 v[8:11], v81 offset:2048\n\t" \
  "v_mfma_f32_16x16x32_bf16 v[44:47], v[24:27], a[248:251], v[44:47]\n\t" \
  "v_cvt_pk_bf16_f32 v62, v38, v39\n\t" \
  "ds_read_b128 v[24:27], v81 offset:6144\n\t" \
  "v_mfma_f32_16x16x32_bf16 v[40:43], v[12:15], a[252:255], v[40:43]\n\t" \
  "ds_read_b128 v[12:15], v81 offset:3072\n\t" \
  "v_mfma_f32_16x16x32_bf16 v[44:47], v[28:31], a[252:255], v[44:47]\n\t" \
  "ds_read_b128 v[28:31], v81 offset:7168\n\t" \
  "s_mov_b32 s44, s45\n\t" \
  "s_min_i32 s46, s46, 63\n\t" \
  "s_cmp_le_i32 s40, s46\n\t" \
  "s_cbranch_scc1 20b\n\t" \
  "buffer_store_dwordx4 v[52:55], v80, %[ru], s44 offen offset:1024 sc1\n\t" \
  "s_nop 3\n\t" \
  "v_cvt_pk_bf16_f32 v57, v40, v41\n\t" \
  "v_cvt_pk_bf16_f32 v59, v42, v43\n\t" \
  "v_cvt_pk_bf16_f32 v61, v44, v45\n\t" \
  "v_cvt_pk_bf16_f32 v63, v46, v47\n\t" \
  "buffer_store_dwordx4 v[56:59], v80, %[ru], s44 offen offset:2048 sc1\n\t" \
  "buffer_store_dwordx4 v[60:63], v80, %[ru], s44 offen offset:3072 sc1\n\t" \
  "s_branch 99f\n\t" \
  "93:\n\t" \
  "ds_read_b128 v[0:3], v81 offset:0\n\t" \
  "ds_read_b128 v[16:19], v81 offset:4096\n\t" \
  "ds_read_b128 v[4:7], v81 offset:1024\n\t" \
  "ds_read_b128 v[20:23], v81 offset:5120\n\t" \
  "ds_read_b128 v[8:11], v81 offset:2048\n\t" \
  "ds_read_b128 v[24:27], v81 offset:6144\n\t" \
  "ds_read_b128 v[12:15], v81 offset:3072\n\t" \
  "ds_read_b128 v[28:31], v81 offset:7168\n\t" \
  ".p2align 6\n\t" \
  "20:\n\t" \
  "s_waitcnt lgkmcnt(7)\n\t" \
  "v_mfma_f32_16x16x32_bf16 v[32:35], v[0:3], a[0:3], 0\n\t" \
  "buffer_store_dwordx4 v[52:55], v80, %[ru], s44 offen offset:1024 sc1\n\t" \
  "s_waitcnt lgkmcnt(6)\n\t" \
  "v_mfma_f32_16x16x32_bf16 v[36:39], v[16:19], a[0:3], 0\n\t" \
  "s_and_b32 s45, s40, 15\n\t" \
  "s_waitcnt lgkmcnt(5)\n\t" \
  "v_mfma_f32_16x16x32_bf16 v[32:35], v[4:7], a[4:7], v[32:35]\n\t" \
  "v_cvt_pk_bf16_f32 v57, v40, v41\n\t" \
  "s_lshl_b32 s45, s45, 22\n\t" \
  "s_waitcnt lgkmcnt(4)\n\t" \
  "v_mfma_f32_16x16x32_bf16 v[36:39], v[20:23], a[4:7], v[36:39]\n\t" \
  "v_cvt_pk_bf16_f32 v59, v42, v43\n\t" \
  "s_add_i32 s45, s45, s42\n\t" \
  "s_waitcnt lgkmcnt(3)\n\t" \
  "v_mfma_f32_16x16x32_bf16 v[32:35], v[8:11], a[8:11], v[32:35]\n\t" \
  "v_cvt_pk_bf16_f32 v61, v44, v45\n\t" \
  "s_lshr_b32 s76, s45, 21\n\t" \
  "s_waitcnt lgkmcnt(2)\n\t" \
  "v_mfma_f32_16x16x32_bf16 v[36:39], v[24:27], a[8:11], v[36:39]\n\t" \
  "v_cvt_pk_bf16_f32 v63, v46, v47\n\t" \
  "s_bitcmp0_b32 %[sel], s76\n\t" \
  "s_cselect_b32 s76, 0x2000, 0\n\t" \
  "s_waitcnt lgkmcnt(1)\n\t" \
  "v_mfma_f32_16x16x32_bf16 v[32:35], v[12:15], a[12:15], v[32:35]\n\t" \
  "s_add_i32 s45, s45, s76\n\t" \
  "s_waitcnt lgkmcnt(0)\n\t" \
  "v_mfma_f32_16x16x32_bf16 v[36:39], v[28:31], a[12:15], v[36:39]\n\t" \
  "s_add_i32 s77, s40, 1\n\t" \
  "v_mfma_f32_16x16x32_bf16 v[40:43], v[0:3], a[16:19], 0\n\t" \
  "buffer_store_dwordx4 v[56:59], v80, %[ru], s44 offen offset:2048 sc1\n\t" \
  "v_mfma_f32_16x16x32_bf16 v[44:47], v[16:19], a[16:19], 0\n\t" \
  "s_min_u32 s77, s77, 63\n\t" \
  "v_mfma_f32_16x16x32_bf16 v[40:43], v[4:7], a[20:23], v[40:43]\n\t" \
  "v_cvt_pk_bf16_f32 v48, v32, v33\n\t" \
  "s_and_b32 s76, s77, 4\n\t" \
  "v_mfma_f32_16x16x32_bf16 v[44:47], v[20:23], a[20:23], v[44:47]\n\t" \
  "v_cvt_pk_bf16_f32 v50, v34, v35\n\t" \
  "s_lshl_b32 s76, s76, 11\n\t" \
  "v_mfma_f32_16x16x32_bf16 v[40:43], v[8:11], a[24:27], v[40:43]\n\t" \
  "v_cvt_pk_bf16_f32 v52, v36, v37\n\t" \
  "s_lshl_b32 s77, s77, 19\n\t" \
  "v_mfma_f32_16x16x32_bf16 v[44:47], v[24:27], a[24:27], v[44:47]\n\t" \
  "v_cvt_pk_bf16_f32 v54, v38, v39\n\t" \
  "s_add_i32 s77, s77, s43\n\t" \
  "v_mfma_f32_16x16x32_bf16 v[40:43], v[12:15], a[28:31], v[40:43]\n\t" \
  "s_xor_b32 s77, s77, s76\n\t" \
  "v_mfma_f32_16x16x32_bf16 v[44:47], v[28:31], a[28:31], v[44:47]\n\t" \
  "s_add_i32 s78, s40, 6\n\t" \
  "v_mfma_f32_16x16x32_bf16 v[32:35], v[0:3], a[32:35], 0\n\t" \
  "buffer_store_dwordx4 v[60:63], v80, %[ru], s44 offen offset:3072 sc1\n\t" \
  "v_mfma_f32_16x16x32_bf16 v[36:39], v[16:19], a[32:35], 0\n\t" \
  "s_cmp_lt_i32 s41, s78\n\t" \
  "s_cselect_b32 s78, 0, 0x7f000000\n\t" \
  "v_mfma_f32_16x16x32_bf16 v[32:35], v[4:7], a[36:39], v[32:35]\n\t" \
  "v_cvt_pk_bf16_f32 v49, v40, v41\n\t" \
  "s_mov_b32 s46, s41\n\t" \
  "v_mfma_f32_16x16x32_bf16 v[36:39], v[20:23], a[36:39], v[36:39]\n\t" \
  "v_cvt_pk_bf16_f32 v51, v42, v43\n\t" \
  "v_mfma_f32_16x16x32_bf16 v[32:35], v[8:11], a[40:43], v[32:35]\n\t" \
  "v_cvt_pk_bf16_f32 v53, v44, v45\n\t" \
  "v_mfma_f32_16x16x32_bf16 v[36:39], v[24:27], a[40:43], v[36:39]\n\t" \
  "v_cvt_pk_bf16_f32 v55, v46, v47\n\t" \
  "buffer_load_dwordx4 v[64:67], v80, %[ru], s77 offen sc1\n\t" \
  "v_mfma_f32_16x16x32_bf16 v[32:35], v[12:15], a[44:47], v[32:35]\n\t" \
  "buffer_load_dwordx4 v[68:71], v80, %[ru], s77 offen offset:1024 sc1\n\t" \
  "v_mfma_f32_16x16x32_bf16 v[36:39], v[28:31], a[44:47], v[36:39]\n\t" \
  "v_mfma_f32_16x16x32_bf16 v[40:43], v[0:3], a[48:51], 0\n\t" \
  "buffer_store_dwordx4 v[48:51], v80, %[ru], s45 offen offset:0 sc1\n\t" \
  "v_mfma_f32_16x16x32_bf16 v[44:47], v[16:19], a[48:51], 0\n\t" \
  "v_mfma_f32_16x16x32_bf16 v[40:43], v[4:7], a[52:55], v[40:43]\n\t" \
  "v_cvt_pk_bf16_f32 v56, v32, v33\n\t" \
  "v_mfma_f32_16x16x32_bf16 v[44:47], v[20:23], a[52:55], v[44:47]\n\t" \
  "v_cvt_pk_bf16_f32 v58, v34, v35\n\t" \
  "v_mfma_f32_16x16x32_bf16 v[40:43], v[8:11], a[56:59], v[40:43]\n\t" \
  "v_cvt_pk_bf16_f32 v60, v36, v37\n\t" \
  "v_mfma_f32_16x16x32_bf16 v[44:47], v[24:27], a[56:59], v[44:47]\n\t" \
  "v_cvt_pk_bf16_f32 v62, v38, v39\n\t" \
  "v_mfma_f32_16x16x32_bf16 v[40:43], v[12:15], a[60:63], v[40:43]\n\t" \
  "v_mfma_f32_16x16x32_bf16 v[44:47], v[28:31], a[60:63], v[44:47]\n\t" \
  "v_mfma_f32_16x16x32_bf16 v[32:35], v[0:3], a[64:67], 0\n\t" \
  "buffer_store_dwordx4 v[52:55], v80, %[ru], s45 offen offset:1024 sc1\n\t" \
  "v_mfma_f32_16x16x32_bf16 v[36:39], v[16:19], a[64:67], 0\n\t" \
  "v_mfma_f32_16x16x32_bf16 v[32:35], v[4:7], a[68:71], v[32:35]\n\t" \
  "v_cvt_pk_bf16_f32 v57, v40, v41\n\t" \
  "v_mfma_f32_16x16x32_bf16 v[36:39], v[20:23], a[68:71], v[36:39]\n\t" \
  "v_cvt_pk_bf16_f32 v59, v42, v43\n\t" \
  "v_mfma_f32_16x16x32_bf16 v[32:35], v[8:11], a[72:75], v[32:35]\n\t" \
  "v_cvt_pk_bf16_f32 v61, v44, v45\n\t" \
  "v_mfma_f32_16x16x32_bf16 v[36:39], v[24:27], a[72:75], v[36:39]\n\t" \
  "v_cvt_pk_bf16_f32 v63, v46, v47\n\t" \
  "v_mfma_f32_16x16x32_bf16 v[32:35], v[12:15], a[76:79], v[32:35]\n\t" \
  "v_mfma_f32_16x16x32_bf16 v[36:39], v[28:31], a[76:79], v[36:39]\n\t" \
  "v_mfma_f32_16x16x32_bf16 v[40:43], v[0:3], a[80:83], 0\n\t" \
  "buffer_store_dwordx4 v[56:59], v80, %[ru], s45 offen offset:2048 sc1\n\t" \
  "v_mfma_f32_16x16x32_bf16 v[44:47], v[16:19], a[80:83], 0\n\t" \
  "v_mfma_f32_16x16x32_bf16 v[40:43], v[4:7], a[84:87], v[40:43]\n\t" \
  "v_cvt_pk_bf16_f32 v48, v32, v33\n\t" \
  "v_mfma_f32_16x16x32_bf16 v[44:47], v[20:23], a[84:87], v[44:47]\n\t" \
  "v_cvt_pk_bf16_f32 v50, v34, v35\n\t" \
  "v_mfma_f32_16x16x32_bf16 v[40:43], v[8:11], a[88:91], v[40:43]\n\t" \
  "v_cvt_pk_bf16_f32 v52, v36, v37\n\t" \
  "v_mfma_f32_16x16x32_bf16 v[44:47], v[24:27], a[88:91], v[44:47]\n\t" \
  "v_cvt_pk_bf16_f32 v54, v38, v39\n\t" \
  "v_mfma_f32_16x16x32_bf16 v[40:43], v[12:15], a[92:95], v[40:43]\n\t" \
  "v_mfma_f32_16x16x32_bf16 v[44:47], v[28:31], a[92:95], v[44:47]\n\t" \
  "v_mfma_f32_16x16x32_bf16 v[32:35], v[0:3], a[96:99], 0\n\t" \
  "buffer_store_dwordx4 v[60:63], v80, %[ru], s45 offen offset:3072 sc1\n\t" \
  "v_mfma_f32_16x16x32_bf16 v[36:39], v[16:19], a[96:99], 0\n\t" \
  "v_mfma_f32_16x16x32_bf16 v[32:35], v[4:7], a[100:103], v[32:35]\n\t" \
  "v_cvt_pk_bf16_f32 v49, v40, v41\n\t" \
  "v_mfma_f32_16x16x32_bf16 v[36:39], v[20:23], a[100:103], v[36:39]\n\t" \
  "v_cvt_pk_bf16_f32 v51, v42, v43\n\t" \
  "v_mfma_f32_16x16x32_bf16 v[32:35], v[8:11], a[104:107], v[32:35]\n\t" \
  "v_cvt_pk_bf16_f32 v53, v44, v45\n\t" \
  "v_mfma_f32_16x16x32_bf16 v[36:39], v[24:27], a[104:107], v[36:39]\n\t" \
  "v_cvt_pk_bf16_f32 v55, v46, v47\n\t" \
  "v_mfma_f32_16x16x32_bf16 v[32:35], v[12:15], a[108:111], v[32:35]\n\t" \
  "v_mfma_f32_16x16x32_bf16 v[36:39], v[28:31], a[108:111], v[36:39]\n\t" \
  "s_add_i32 s45, s45, 0x1000\n\t" \
  "v_mfma_f32_16x16x32_bf16 v[40:43], v[0:3], a[112:115], 0\n\t" \
  "buffer_store_dwordx4 v[48:51], v80, %[ru], s45 offen offset:0 sc1\n\t" \
  "v_mfma_f32_16x16x32_bf16 v[44:47], v[16:19], a[112:115], 0\n\t" \
  "v_mfma_f32_16x16x32_bf16 v[40:43], v[4:7], a[116:119], v[40:43]\n\t" \
  "v_cvt_pk_bf16_f32 v56, v32, v33\n\t" \
  "v_mfma_f32_16x16x32_bf16 v[44:47], v[20:23], a[116:119], v[44:47]\n\t" \
  "v_cvt_pk_bf16_f32 v58, v34, v35\n\t" \
  "v_mfma_f32_16x16x32_bf16 v[40:43], v[8:11], a[120:123], v[40:43]\n\t" \
  "v_cvt_pk_bf16_f32 v60, v36, v37\n\t" \
  "v_mfma_f32_16x16x32_bf16 v[44:47], v[24:27], a[120:123], v[44:47]\n\t" \
  "v_cvt_pk_bf16_f32 v62, v38, v39\n\t" \
  "v_mfma_f32_16x16x32_bf16 v[40:43], v[12:15], a[124:127], v[40:43]\n\t" \
  "v_mfma_f32_16x16x32_bf16 v[44:47], v[28:31], a[124:127], v[44:47]\n\t" \
  "v_mfma_f32_16x16x32_bf16 v[32:35], v[0:3], a[128:131], 0\n\t" \
  "buffer_store_dwordx4 v[52:55], v80, %[ru], s45 offen offset:1024 sc1\n\t" \
  "v_mfma_f32_16x16x32_bf16 v[36:39], v[16:19], a[128:131], 0\n\t" \
  "v_mfma_f32_16x16x32_bf16 v[32:35], v[4:7], a[132:135], v[32:35]\n\t" \
  "v_cvt_pk_bf16_f32 v57, v40, v41\n\t" \
  "v_mfma_f32_16x16x32_bf16 v[36:39], v[20:23], a[132:135], v[36:39]\n\t" \
  "v_cvt_pk_bf16_f32 v59, v42, v43\n\t" \
  "v_mfma_f32_16x16x32_bf16 v[32:35], v[8:11], a[136:139], v[32:35]\n\t" \
  "v_cvt_pk_bf16_f32 v61, v44, v45\n\t" \
  "v_mfma_f32_16x16x32_bf16 v[36:39], v[24:27], a[136:139], v[36:39]\n\t" \
  "v_cvt_pk_bf16_f32 v63, v46, v47\n\t" \
  "v_mfma_f32_16x16x32_bf16 v[32:35], v[12:15], a[140:143], v[32:35]\n\t" \
  "v_mfma_f32_16x16x32_bf16 v[36:39], v[28:31], a[140:143], v[36:39]\n\t" \
  "v_mfma_f32_16x16x32_bf16 v[40:43], v[0:3], a[144:147], 0\n\t" \
  "buffer_store_dwordx4 v[56:59], v80, %[ru], s45 offen offset:2048 sc1\n\t" \
  "v_mfma_f32_16x16x32_bf16 v[44:47], v[16:19], a[144:147], 0\n\t" \
  "v_mfma_f32_16x16x32_bf16 v[40:43], v[4:7], a[148:151], v[40:43]\n\t" \
  "v_cvt_pk_bf16_f32 v48, v32, v33\n\t" \
  "v_mfma_f32_16x16x32_bf16 v[44:47], v[20:23], a[148:151], v[44:47]\n\t" \
  "v_cvt_pk_bf16_f32 v50, v34, v35\n\t" \
  "v_mfma_f32_16x16x32_bf16 v[40:43], v[8:11], a[152:155], v[40:43]\n\t" \
  "v_cvt_pk_bf16_f32 v52, v36, v37\n\t" \
  "v_mfma_f32_16x16x32_bf16 v[44:47], v[24:27], a[152:155], v[44:47]\n\t" \
  "v_cvt_pk_bf16_f32 v54, v38, v39\n\t" \
  "v_mfma_f32_16x16x32_bf16 v[40:43], v[12:15], a[156:159], v[40:43]\n\t" \
  "v_mfma_f32_16x16x32_bf16 v[44:47], v[28:31], a[156:159], v[44:47]\n\t" \
  "v_mfma_f32_16x16x32_bf16 v[32:35], v[0:3], a[160:163], 0\n\t" \
  "buffer_store_dwordx4 v[60:63], v80, %[ru], s45 offen offset:3072 sc1\n\t" \
  "v_mfma_f32_16x16x32_bf16 v[36:39], v[16:19], a[160:163], 0\n\t" \
  "v_mfma_f32_16x16x32_bf16 v[32:35], v[4:7], a[164:167], v[32:35]\n\t" \
  "v_cvt_pk_bf16_f32 v49, v40, v41\n\t" \
  "v_mfma_f32_16x16x32_bf16 v[36:39], v[20:23], a[164:167], v[36:39]\n\t" \
  "v_cvt_pk_bf16_f32 v51, v42, v43\n\t" \
  "v_mfma_f32_16x16x32_bf16 v[32:35], v[8:11], a[168:171], v[32:35]\n\t" \
  "v_cvt_pk_bf16_f32 v53, v44, v45\n\t" \
  "v_mfma_f32_16x16x32_bf16 v[36:39], v[24:27], a[168:171], v[36:39]\n\t" \
  "v_cvt_pk_bf16_f32 v55, v46, v47\n\t" \
  "v_mfma_f32_16x16x32_bf16 v[32:35], v[12:15], a[172:175], v[32:35]\n\t" \
  "v_mfma_f32_16x16x32_bf16 v[36:39], v[28:31], a[172:175], v[36:39]\n\t" \
  "s_add_i32 s45, s45, 0x3000\n\t" \
  "v_mfma_f32_16x16x32_bf16 v[40:43], v[0:3], a[176:179], 0\n\t" \
  "buffer_store_dwordx4 v[48:51], v80, %[ru], s45 offen offset:0 sc1\n\t" \
  "v_mfma_f32_16x16x32_bf16 v[44:47], v[16:19], a[176:179], 0\n\t" \
  "v_mfma_f32_16x16x32_bf16 v[40:43], v[4:7], a[180:183], v[40:43]\n\t" \
  "v_cvt_pk_bf16_f32 v56, v32, v33\n\t" \
  "v_mfma_f32_16x16x32_bf16 v[44:47], v[20:23], a[180:183], v[44:47]\n\t" \
  "v_cvt_pk_bf16_f32 v58, v34, v35\n\t" \
  "v_mfma_f32_16x16x32_bf16 v[40:43], v[8:11], a[184:187], v[40:43]\n\t" \
  "v_cvt_pk_bf16_f32 v60, v36, v37\n\t" \
  "v_mfma_f32_16x16x32_bf16 v[44:47], v[24:27], a[184:187], v[44:47]\n\t" \
  "v_cvt_pk_bf16_f32 v62, v38, v39\n\t" \
  "v_mfma_f32_16x16x32_bf16 v[40:43], v[12:15], a[188:191], v[40:43]\n\t" \
  "v_xor_b32 v81, 0x2000, v81\n\t" \
  "v_mfma_f32_16x16x32_bf16 v[44:47], v[28:31], a[188:191], v[44:47]\n\t" \
  "v_xor_b32 v82, 0x2000, v82\n\t" \
  "v_mfma_f32_16x16x32_bf16 v[32:35], v[0:3], a[192:195], 0\n\t" \
  "buffer_store_dwordx4 v[52:55], v80, %[ru], s45 offen offset:1024 sc1\n\t" \
  "v_mfma_f32_16x16x32_bf16 v[36:39], v[16:19], a[192:195], 0\n\t" \
  "v_mfma_f32_16x16x32_bf16 v[32:35], v[4:7], a[196:199], v[32:35]\n\t" \
  "v_cvt_pk_bf16_f32 v57, v40, v41\n\t" \
  "v_mfma_f32_16x16x32_bf16 v[36:39], v[20:23], a[196:199], v[36:39]\n\t" \
  "v_cvt_pk_bf16_f32 v59, v42, v43\n\t" \
  "v_mfma_f32_16x16x32_bf16 v[32:35], v[8:11], a[200:203], v[32:35]\n\t" \
  "v_cvt_pk_bf16_f32 v61, v44, v45\n\t" \
  "v_mfma_f32_16x16x32_bf16 v[36:39], v[24:27], a[200:203], v[36:39]\n\t" \
  "v_cvt_pk_bf16_f32 v63, v46, v47\n\t" \
  "v_mfma_f32_16x16x32_bf16 v[32:35], v[12:15], a[204:207], v[32:35]\n\t" \
  "s_add_i32 s79, s40, -1\n\t" \
  "v_xor_b32 v85, 8, v85\n\t" \
  "v_mfma_f32_16x16x32_bf16 v[36:39], v[28:31], a[204:207], v[36:39]\n\t" \
  "s_waitcnt vmcnt(10)\n\t" \
  "ds_write_b128 v82, v[64:67]\n\t" \
  "v_mfma_f32_16x16x32_bf16 v[40:43], v[0:3], a[208:211], 0\n\t" \
  "buffer_store_dwordx4 v[56:59], v80, %[ru], s45 offen offset:2048 sc1\n\t" \
  "v_mfma_f32_16x16x32_bf16 v[44:47], v[16:19], a[208:211], 0\n\t" \
  "ds_write_b128 v82, v[68:71] offset:1024\n\t" \
  "v_mfma_f32_16x16x32_bf16 v[40:43], v[4:7], a[212:215], v[40:43]\n\t" \
  "v_cvt_pk_bf16_f32 v48, v32, v33\n\t" \
  "v_mfma_f32_16x16x32_bf16 v[44:47], v[20:23], a[212:215], v[44:47]\n\t" \
  "v_cvt_pk_bf16_f32 v50, v34, v35\n\t" \
  "v_mfma_f32_16x16x32_bf16 v[40:43], v[8:11], a[216:219], v[40:43]\n\t" \
  "v_cvt_pk_bf16_f32 v52, v36, v37\n\t" \
  "v_mfma_f32_16x16x32_bf16 v[44:47], v[24:27], a[216:219], v[44:47]\n\t" \
  "v_cvt_pk_bf16_f32 v54, v38, v39\n\t" \
  "v_mfma_f32_16x16x32_bf16 v[40:43], v[12:15], a[220:223], v[40:43]\n\t" \
  "v_mov_b32 v74, s79\n\t" \
  "v_mfma_f32_16x16x32_bf16 v[44:47], v[28:31], a[220:223], v[44:47]\n\t" \
  "v_mfma_f32_16x16x32_bf16 v[32:35], v[0:3], a[224:227], 0\n\t" \
  "buffer_store_dwordx4 v[60:63], v80, %[ru], s45 offen offset:3072 sc1\n\t" \
  "s_waitcnt lgkmcnt(0)\n\t" \
  "s_barrier\n\t" \
  "ds_read_b32 v73, v85\n\t" \
  "v_mfma_f32_16x16x32_bf16 v[36:39], v[16:19], a[224:227], 0\n\t" \
  "v_mfma_f32_16x16x32_bf16 v[32:35], v[4:7], a[228:231], v[32:35]\n\t" \
  "v_cvt_pk_bf16_f32 v49, v40, v41\n\t" \
  "v_mfma_f32_16x16x32_bf16 v[36:39], v[20:23], a[228:231], v[36:39]\n\t" \
  "v_cvt_pk_bf16_f32 v51, v42, v43\n\t" \
  "v_mfma_f32_16x16x32_bf16 v[32:35], v[8:11], a[232:235], v[32:35]\n\t" \
  "v_cvt_pk_bf16_f32 v53, v44, v45\n\t" \
  "v_mfma_f32_16x16x32_bf16 v[36:39], v[24:27], a[232:235], v[36:39]\n\t" \
  "v_cvt_pk_bf16_f32 v55, v46, v47\n\t" \
  "v_mfma_f32_16x16x32_bf16 v[32:35], v[12:15], a[236:239], v[32:35]\n\t" \
  "s_waitcnt lgkmcnt(0)\n\t" \
  "v_readfirstlane_b32 s76, v73\n\t" \
  "v_mfma_f32_16x16x32_bf16 v[36:39], v[28:31], a[236:239], v[36:39]\n\t" \
  "s_add_i32 s45, s45, 0x1000\n\t" \
  "s_max_i32 s41, s41, s76\n\t" \
  "s_add_i32 s40, s40, 1\n\t" \
  "v_mfma_f32_16x16x32_bf16 v[40:43], v[0:3], a[240:243], 0\n\t" \
  "buffer_store_dwordx4 v[48:51], v80, %[ru], s45 offen offset:0 sc1\n\t" \
  "ds_read_b128 v[0:3], v81 offset:0\n\t" \
  "v_mfma_f32_16x16x32_bf16 v[44:47], v[16:19], a[240:243], 0\n\t" \
  "ds_read_b128 v[16:19], v81 offset:4096\n\t" \
  "v_mfma_f32_16x16x32_bf16 v[40:43], v[4:7], a[244:247], v[40:43]\n\t" \
  "v_cvt_pk_bf16_f32 v56, v32, v33\n\t" \
  "ds_read_b128 v[4:7], v81 offset:1024\n\t" \
  "v_mfma_f32_16x16x32_bf16 v[44:47], v[20:23], a[244:247], v[44:47]\n\t" \
  "v_cvt_pk_bf16_f32 v58, v34, v35\n\t" \
  "ds_read_b128 v[20:23], v81 offset:5120\n\t" \
  "v_mfma_f32_16x16x32_bf16 v[40:43], v[8:11], a[248:251], v[40:43]\n\t" \
  "v_cvt_pk_bf16_f32 v60, v36, v37\n\t" \
  "ds_read_b128 v[8:11], v81 offset:2048\n\t" \
  "v_mfma_f32_16x16x32_bf16 v[44:47], v[24:27], a[248:251], v[44:47]\n\t" \
  "v_cvt_pk_bf16_f32 v62, v38, v39\n\t" \
  "ds_read_b128 v[24:27], v81 offset:6144\n\t" \
  "v_mfma_f32_16x16x32_bf16 v[40:43], v[12:15], a[252:255], v[40:43]\n\t" \
  "ds_read_b128 v[12:15], v81 offset:3072\n\t" \
  "v_mfma_f32_16x16x32_bf16 v[44:47], v[28:31], a[252:255], v[44:47]\n\t" \
  "ds_read_b128 v[28:31], v81 offset:7168\n\t" \
  "s_mov_b32 s44, s45\n\t" \
  "s_min_i32 s46, s46, 63\n\t" \
  "s_cmp_le_i32 s40, s46\n\t" \
  "s_cbranch_scc1 20b\n\t" \
  "buffer_store_dwordx4 v[52:55], v80, %[ru], s44 offen offset:1024 sc1\n\t" \
  "s_nop 3\n\t" \
  "v_cvt_pk_bf16_f32 v57, v40, v41\n\t" \
  "v_cvt_pk_bf16_f32 v59, v42, v43\n\t" \
  "v_cvt_pk_bf16_f32 v61, v44, v45\n\t" \
  "v_cvt_pk_bf16_f32 v63, v46, v47\n\t" \
  "buffer_store_dwordx4 v[56:59], v80, %[ru], s44 offen offset:2048 sc1\n\t" \
  "buffer_store_dwordx4 v[60:63], v80, %[ru], s44 offen offset:3072 sc1\n\t" \
  "s_branch 99f\n\t" \
  "99:\n\t" \
  "s_waitcnt vmcnt(0) lgkmcnt(0)\n\t" \
  ""

#define V10(x) "v" #x "0", "v" #x "1", "v" #x "2", "v" #x "3", "v" #x "4", "v" #x "5", "v" #x "6", "v" #x "7", "v" #x "8", "v" #x "9"
#define A10(x) "a" #x "0", "a" #x "1", "a" #x "2", "a" #x "3", "a" #x "4", "a" #x "5", "a" #x "6", "a" #x "7", "a" #x "8", "a" #x "9"
#define V90 V10(), V10(1), V10(2), V10(3), V10(4), V10(5), V10(6), V10(7), V10(8)
#define S22 "s56", "s57", "s58", "s59", "s76", "s77", "s78", "s79", "s80", "s81", "s82", "s83", "s84", "s85", "s86", "s87", "s88", "s89", \
  "s90", "s91", "s92", "s93", "s94", "s95", "s96", "s97"
#define STEP_PP
#define STEP_CL
#define STEP_PV
#define CB_PV , "{v248}"(C24)
#define STEPS(BODY, ...) asm volatile(BODY : "=&{v[92:99]}"(X), "+&{s[40:55]}"(P) STEP_PV \
  : "{v[120:151]}"(W.v[0]), "{v[152:183]}"(W.v[1]), "{v[184:215]}"(W.v[2]), "{v[216:247]}"(W.v[3]), "{v[100:115]}"(C), [rh] "s"(rh), [wv] "s"(w), [ru] "s"(rum) STEP_PP __VA_ARGS__ \
  : "memory", "vcc", "scc", S22, STEP_CL V90, "v90", "v91", "v116", "v117", "v118")
__device__ __forceinline__ void keep_a(const Wts& W) {
#pragma unroll
  for (int i = 0; i < 32; ++i) asm volatile("" :: "a"(W.a[i]));
}

__device__ __forceinline__ void steps_init(u32x16& P, int h, int w, unsigned place, Sel sel) {
#pragma unroll
  for (int i = 0; i < 16; ++i) P[i] = 0;
  P[2] = 1; P[3] = 1420; P[14] = H_MEM + hot_q(RHO(h), sel) + w * 1024; P[15] = place;
}
__device__ __forceinline__ void steps_enter(u32x16& P, Clk& ck, bool dead) {
  ck.sync();
  P[2] = (P[2] & 3u) | (dead ? 4u : 0u); P[6] = ck.c2t << 15; P[11] = ck.ce; P[12] = ck.t2c; P[13] = ck.te;
#pragma unroll
  for (int i = 0; i < 16; ++i) P[i] = __builtin_amdgcn_readfirstlane(P[i]);
}

__device__ __forceinline__ void role_qk(const ChainArgs& a, int h, char* smem, const int w, Gate& G) {
  int lane = opaque_tid(0);
  const int b = h; (void)b;
  u64* prof = a.prof; (void)prof;
  PDECL(L_PRF, 4);
  const float CS = 0.08838834764831845f * 1.4426950408889634f;
  GAS unsigned* abortw = (GAS unsigned*)(a.sync + SY_ABORT);
  bool dead = false;
  Wts W;
  Clk ck; ck.init();
  load_weights<0>(a, h, w, lane, smem, W);
  if (!G.pass(a.sync)) return;
  PZERO;
  const Sel sel = sel_wait(a.sync, abortw, lane, dead, true, CT_PR + h, 0, CT_PR + 32 + h, 1);
  const __amdgpu_buffer_rsrc_t rh = rsrc_hot(a.sync, sel), rum = rsrc_u(a.sync);
#pragma unroll
  for (int i = 0; i < 2; ++i) *(u32x4*)(smem + L_KX + (2 * w + i) * 1024 + lane * 16) = ld16(rum, OFF_KX + far_blk(h, sel, FARX >> 1) + (2 * w + i) * 1024 + lane * 16);
  const unsigned hq = hot_q(h >> 3, sel);
  auto pz_o = [&](int l) { return (l & 1) ? OOR : H_PZ + hq + (h & 7) * 256 + w * 128 + (l & 8) * 8 + (l >> 4) * 16 + (l & 6) * 2; };
  auto z_at = [&](int l, int half) { return ((l & 6) >> 1) == 2 * w + ((h >> 2) & 1) && ((l >> 4) & 1) == half ? 1.f : 0.f; };
  u32x16 C, P;
  unsigned C16;
  {
    const int r = lane & 15, g = lane >> 4;
    const unsigned l0 = (unsigned)(uintptr_t)(LAS char*)smem;
    C[0] = lane * 16; C[1] = l0 + L_MEM + lane * 16; C[2] = l0 + L_WTS + w * 4096 + lane * 16; C[3] = C[2] + 65536;
    C[4] = (lane & 2) ? 0xEEEE : 0x4444;
    C[5] = l0 + L_XCH + (16 * w + 2 * (r >> 2) + (r & 1)) * 32 + g * 8 + ((r >> 1) & 1) * 4;
    C[6] = l0 + L_XK + (32 * w + r) * 16 + g * 4;
    C[7] = w < 2 ? l0 + L_XCH + (8 * g + (r >> 2)) * 32 + (r & 3) * 8 : SY_CT + (CT_PR + 32 * (w - 2) + h) * 128;
    C[8] = l0 + L_XK + (8 * g + (r >> 2)) * 16 + (r & 1) * 8;
    C[9] = w < 3 ? (lane == 0 ? H_FLG + h * 128 : OOR) :
           lane < 32 ? H_P + hq + (h & 7) * 512 + lane * 16 : (lane < 48 ? H_PZ + hq + (h & 7) * 256 + (lane - 32) * 16 : OOR);
    C[10] = H_TR + quarter((lane >> 3) & 3) + (lane & 7) * 128; C[11] = l0 + L_PRF + w * 1024 + (lane >> 6);
    C[12] = H_P + hq + (h & 7) * 512 + w * 256 + (r >> 2) * 64 + g * 16 + (r & 3) * 4; C[13] = pz_o(lane);
    C[14] = __float_as_uint(z_at(lane, 0)); C[15] = __float_as_uint(z_at(lane, 1));
    C16 = l0 + L_KX + (r >> 3) * 4096 + (g * 16 + 2 * (r & 7)) * 16;
    if (w == 3) { C[12] = lane == 0 ? H_ANN + hq + (h & 7) * 128 : OOR; C[13] = lane == 1 ? H_FLG + h * 128 : C[12]; }
    if (w == 2) {
#pragma unroll
      for (int i = 0; i < 4; ++i) C[12 + i] = 4 * g + i <= r ? 0u : 0xff800000u;
      C16 = H_PX + (h >> 2) * CHK + (h & 3) * 2048 + lane * 16;
    }
    asm volatile("" : "+v"(C), "+v"(C16));
  }
  SSTAMP(96 + h);
  steps_init(P, h, w, (w == 2 ? OFF_QX : OFF_KX) + far_blk(h, sel, FARX >> (w & 1)), sel);
  unsigned spins = 0;
  while (true) {
    u32x8 X; u32x16 pv; (void)pv;
    steps_enter(P, ck, dead);
    STEPS(QK_BODY, , [cs] "s"(CS), "{v119}"(C16));
    keep_a(W);
    const unsigned res = __builtin_amdgcn_readfirstlane(P[2]), s = __builtin_amdgcn_readfirstlane(P[4]);
    lane = opaque_tid(0);
    if (res & 8u) {
      auto sw = [](float v, int k) { return __int_as_float(k == 0 ? __builtin_amdgcn_mov_dpp(__float_as_int(v), 0xB1, 0xf, 0xf, false) :
                                                           k == 1 ? __builtin_amdgcn_mov_dpp(__float_as_int(v), 0x4E, 0xf, 0xf, false) :
                                                                    __builtin_amdgcn_mov_dpp(__float_as_int(v), 0x141, 0xf, 0xf, false)); };
      float d = fmaxf(__uint_as_float(X[4]), 0.f);
      d = fmaxf(d, sw(d, 0)); d = fmaxf(d, sw(d, 1)); d = fmaxf(d, sw(d, 2));
      const float x = __builtin_amdgcn_exp2f(__uint_as_float(X[4]) - d), y = __builtin_amdgcn_exp2f(-d), xn = sw(x, 0);
      float z = fmaf(__uint_as_float(X[0]), y, x);
      z += sw(z, 0); z += sw(z, 1); z += sw(z, 2);
      z = __builtin_amdgcn_rcpf(z);
      const unsigned pm = nosent((lane & 8) ? pk2(z_at(lane, 0) * y * z, z_at(lane, 1) * y * z) : pk2(x * z, xn * z));
      if (!(__uint_as_float(X[5]) < 1.0e18f))
        __builtin_amdgcn_raw_buffer_store_b32(pm, rh, pz_o(lane), ((s - 1) & 3) * 4 * CHK, ST_AUX);
    }
    if (res & 64u) break;
    if (res & 32u) ctr_wait(ctr(a, CT_PR + 32 * (w - 2) + h), min(s + w - 1, (unsigned)NSEG), abortw, lane, dead);
    if (res & 17u) {
      spins |= 63u;
      if (dead || poll_fail(spins, abortw, lane)) dead = true;
    }
  }
  if (w == 2 && lane == 0) st4(rh, H_FLG + 12 * CHK + h * 128, NSEG - 1);
  PDUMP(0);
}

__device__ __forceinline__ void role_vo(const ChainArgs& a, int h, char* smem, const int w, Gate& G) {
  int lane = opaque_tid(0);
  const int b = 32 + h; (void)b;
  u64* prof = a.prof; (void)prof;
  PDECL(L_PRF, 4);
  GAS unsigned* abortw = (GAS unsigned*)(a.sync + SY_ABORT);
  bool dead = false;
  Wts W;
  Clk ck; ck.init();
  load_weights<1>(a, h, w, lane, smem, W);
  if (!G.pass(a.sync)) return;
  PZERO;
  const Sel sel = sel_wait(a.sync, abortw, lane, dead, true);
  const __amdgpu_buffer_rsrc_t rh = rsrc_hot(a.sync, sel), rum = rsrc_u(a.sync);
  u32x16 C, P;
  {
    const int r = lane & 15, g = lane >> 4;
    const unsigned l0 = (unsigned)(uintptr_t)(LAS char*)smem;
#pragma unroll
    for (int i = 0; i < 16; ++i) C[i] = H_TR + quarter((lane >> 3) & 3) + (lane & 7) * 128;
    C[0] = lane * 16; C[1] = l0 + L_MEM + lane * 16; C[2] = l0 + L_WTS + w * 4096 + lane * 16; C[3] = C[2] + 65536;
    C[4] = (lane & 2) ? 0xEEEE : 0x4444;
    C[5] = l0 + L_XCH + (32 * w + r) * 16 + g * 4;
    C[6] = (lane & 8) ? 0xEEEE : 0x4444;
    C[7] = l0 + L_XCH + (32 * (g >> 1) + 8 * (g & 1) + (r >> 2)) * 16 + (r & 1) * 8;
    C[8] = (lane >> 5) * 8 * CHK + (lane & 31) * 16;
    C[11] = l0 + L_PRF + w * 1024 + (lane >> 6);
    asm volatile("" : "+v"(C));
  }
  SSTAMP(128 + h);
  steps_init(P, h, w, um_place(8 * (h & 3) + 2 * w, h >> 3) + (h & 7) * 512, sel);
  unsigned spins = 0;
  while (true) {
    u32x8 X; u32x16 pv; (void)pv;
    steps_enter(P, ck, dead);
    STEPS(VO_BODY, , [um] "s"(sel[1] >> 8));
    keep_a(W);
    lane = opaque_tid(0);
    if (__builtin_amdgcn_readfirstlane(P[2]) & 64u) break;
    spins |= 63u;
    if (dead || poll_fail(spins, abortw, lane)) dead = true;
  }
  PDUMP(0);
}

__device__ __forceinline__ int timed(__amdgpu_buffer_rsrc_t rs, unsigned at) {
  u64 c0, c1;
  asm volatile("s_waitcnt vmcnt(0)\n\ts_memtime %0\n\ts_waitcnt lgkmcnt(0)" : "=s"(c0) :: "memory");
  unsigned v = __builtin_amdgcn_raw_buffer_load_b32(rs, 0, at, LD_AUX);
  asm volatile("s_waitcnt vmcnt(0)\n\ts_memtime %0\n\ts_waitcnt lgkmcnt(0)" : "=s"(c1), "+v"(v) :: "memory");
  const int t = (int)(c1 - c0);
  return t < 320 ? 4000 : t;
}
__device__ __forceinline__ unsigned calibrate(const ChainArgs& a, int cal, u64 t0) {
  const __amdgpu_buffer_rsrc_t rs = mk_rsrc((void*)a.sync, SY_SET);
  int best[2] = {4000, 4000};
  bool sure = false;
  for (int i = 0; i < 32 && !sure && __builtin_amdgcn_s_memrealtime() - t0 < CAL_A; ++i) {
#pragma unroll
    for (int hf = 0; hf < 2; ++hf) best[hf] = min(best[hf], timed(rs, cal * (PAGE + CHK) + 64 * CHK + hf * 8192 + ((5 * cal + i) & 31) * 256));
    sure = min(best[0], best[1]) < 540;
  }
  return ((best[1] < best[0] ? 1u : 0u) ^ die_half()) + (sure ? 2u : 0u);
}
__device__ __forceinline__ unsigned calibrate8(const ChainArgs& a, unsigned at, u64 t0) {
  const __amdgpu_buffer_rsrc_t rs = mk_rsrc((void*)a.sync, SY_SET);
  int hf = 0, t[4] = {4000, 4000, 4000, 4000};
  for (; hf < 32 && timed(rs, at + (hf & 3) * 8192 + 2048 + (((hf >> 2) + (at >> 15)) & 7) * 256) >= 540; ++hf) if (__builtin_amdgcn_s_memrealtime() - t0 >= CAL_T) hf = 32;
  bool ok = false;
  for (int i = 0; i < 8 && hf < 32 && !ok && __builtin_amdgcn_s_memrealtime() - t0 < CAL_T; ++i) {
#pragma unroll
    for (int k = 0; k < 4; ++k) t[k] = min(t[k], timed(rs, at + (hf & 1) * 8192 + quarter(k) + ((i + (at >> 15)) & 7) * 256));
    ok = i && max(max(t[0], t[1]), max(t[2], t[3])) < 540;
  }
  const unsigned xc = (unsigned)__builtin_amdgcn_s_getreg(20 | (3 << 11));
  const int d12 = min(max(t[0] - t[1], -30), 30) + min(max(t[2] - t[3], -30), 30), d14 = min(max(t[0] - t[2], -30), 30) + min(max(t[1] - t[3], -30), 30);
  ok = ok && !(xc & 4);
  return ((hf & 1) ^ die_half()) | (ok ? 2u : 0u) | (64 + (ok ? ((xc & 2) ? -d12 : d12) : 0)) << 8 | (64 + (ok ? ((xc & 1) ? -d14 : d14) : 0)) << 16;
}
__device__ __forceinline__ void bg_drain() {
  asm volatile("s_waitcnt vmcnt(0)" ::: "memory");
  __builtin_amdgcn_s_barrier();
  asm volatile("" ::: "memory");
}

constexpr int LC_SUM = 65536, LC_TQ = LC_SUM + 8192, LC_OUT = 81920, LC_PRF = 98304;
__device__ __forceinline__ void role_cb(const ChainArgs& a, int ci, int par, char* smem, const int w, Gate& G) {
  int lane = opaque_tid(0);
  const int b = 64 + 32 * par + ci, j = ci >> 3; (void)b;
  u64* prof = a.prof; (void)prof;
  PDECL(LC_PRF, 8);
  GAS unsigned* abortw = (GAS unsigned*)(a.sync + SY_ABORT);
  bool dead = false;
  const unsigned l0 = (unsigned)(uintptr_t)(LAS char*)smem;
  const int tq = 2 * ci + (w & 1);
  auto fine = [](int n) { return (n % 3 == 2 ? 32 : n % 3) * PAGE + (n / 3 + 1) * 32768; };
  unsigned m0 = 0, hs = 0;
  if (w == 1 || (w == 2 && !par)) {
    const unsigned x = __builtin_amdgcn_raw_buffer_load_b32(mk_rsrc((void*)a.sync, SY_SET), par ? (ci ^ 2) * (PAGE + CHK) + 64 * CHK + (lane & 1) * 8192 + (lane >> 1) * 256 : fine(ci ^ 2) + (lane & 7) * 4096 + (lane >> 3) * 256 + (w - 1) * 2048, 0, 0);
    asm volatile("" :: "v"(x));
  }
  if (w == 0) hs = par ? calibrate8(a, fine(ci), G.entry) : calibrate(a, ci, G.entry);
  __builtin_amdgcn_s_barrier();
  m0 = nosent(*(const unsigned*)(a.mem0 + (2 * (lane >> 4) + (lane & 1)) * DM + 16 * tq + (lane & 14)));
  Clk ck; ck.init();
  if (!G.pass(a.sync)) return;
  SSTAMP(0 + ci);
  if ((w | lane) == 0) __hip_atomic_store(ctr(a, CT_CAL + b - 64), hs, __ATOMIC_RELAXED, __HIP_MEMORY_SCOPE_AGENT);
  auto init = [&](Sel sl) {
    const unsigned s1v = ~(unsigned)(lane >> 6);
    const u32x4 sv = {s1v, s1v, s1v, s1v};
#pragma unroll
    for (int i = 0; i < 4; ++i) st16(rsrc_hot(a.sync, sl), i < 2 || ci < 2 ? (ci + 16 * (i & 2)) * CHK + w * 2048 + (i & 1) * 1024 + lane * 16 : OOR, sv);
#pragma unroll
    for (int i = 0; i < 8; ++i) st16(rsrc_u(a.sync), (4 * ci + w) * CHK + (sl[0] & 1u) * 8192 + i * 1024 + lane * 16, sv);
    bg_drain();
    if (w == 0 && lane == 0) ctr_add(a, CT_INIT, 1u);
  };
  int told = 0;
  if (b == 65 && w == 3) {
    unsigned spins = 0, v;
    int d;
    auto cnt = [](bool x) { return __builtin_popcountll(__builtin_amdgcn_ballot_w64(x)); };
    while (true) {
      v = __hip_atomic_load(ctr(a, CT_CAL + lane), __ATOMIC_RELAXED, __HIP_MEMORY_SCOPE_AGENT);
      const unsigned u = v == SENT ? 0u : (v & 3u) ^ (lane < 32 ? lane & 1 : lane % 3 == 0), f = lane >= 32 && v != SENT && (v & 2u) ? lane % 3 : 3;
      const int n0 = cnt(u == 2u), n1 = cnt(u == 3u);
      d = max(n0, n1) >= 8 + 4 * min(n0, n1) ? n1 - n0 : 0;
      if (d && !told) {
        told = d;
        const u64 m = __builtin_amdgcn_ballot_w64(((lane ^ (d > 0) ^ die_half()) & 1u) != 0 && lane < 33) | 1ull << 62;
        if (lane == 0) __hip_atomic_store((GAS u64*)(a.sync + SY_SEL), m, __ATOMIC_RELAXED, __HIP_MEMORY_SCOPE_AGENT);
        init((Sel){(unsigned)m, (unsigned)(m >> 32)});
      }
      d = told;
      if ((d && min(min(cnt(f == 0), cnt(f == 1)), cnt(f == 2)) >= CAL_N) || __all(v != SENT) || poll_fail(spins, abortw, lane)) break;
      __builtin_amdgcn_s_sleep(4);
    }
    if (v == SENT) v = 0x404000u;
    unsigned key[3] = {};
    int vote[3][2] = {};
    for (int n = 0; n < 32; ++n) {
      const unsigned x = __builtin_amdgcn_readlane(v, 32 + n);
#pragma unroll
      for (int k = 0; k < 3; ++k) if (n % 3 == k) { vote[k][0] += (int)((x >> 8) & 255u) - 64; vote[k][1] += (int)((x >> 16) & 255u) - 64; }
    }
#pragma unroll
    for (int k = 0; k < 3; ++k) key[k] = ((vote[k][0] > 0) + 2 * (vote[k][1] > 0)) ^ ((KEYX >> (2 * k)) & 3);
    const int h32 = cnt((v & 3) == 3 && lane >= 32 && lane % 3 == 1) - cnt((v & 3) == 2 && lane >= 32 && lane % 3 == 1);
    if (lane == 32) v = h32 > 0;
    if (d) v = lane ^ (d > 0);
    u64 m = __builtin_amdgcn_ballot_w64(((v ^ die_half()) & 1u) != 0 && lane < 33);
    m |= (u64)key[2] << 36;
#pragma unroll
    for (int t = 0; t < 4; ++t) m |= (u64)(((key[t >> 1] ^ t) & 1u) | ((m >> (t >> 1)) & 1u) << 1 | (key[t >> 1] & 2u) << 1) << (40 + 4 * t);
    if (lane == 0) __hip_atomic_store((GAS u64*)(a.sync + SY_SEL), m, __ATOMIC_RELAXED, __HIP_MEMORY_SCOPE_AGENT);
  }
  if (!par && !told) init(sel_wait(a.sync, abortw, lane, dead));
  if (par) {
    while (__builtin_amdgcn_s_memrealtime() - G.entry < XS_T) __builtin_amdgcn_s_sleep(8);
    const __amdgpu_buffer_rsrc_t rx = mk_rsrc((void*)a.X, 4u << 20);
    unsigned x[8];
#pragma unroll
    for (int i = 0; i < 8; ++i) x[i] = __builtin_amdgcn_raw_buffer_load_b32(rx, (ci * 256 + w * 64 + lane) * XS_G, i * 8192 * XS_G, 0);
#pragma unroll
    for (int i = 0; i < 8; ++i) asm volatile("" :: "v"(x[i]));
  }
  if (w == 0) sel_wait(a.sync, abortw, lane, dead, true);
  lds_barrier();
  const Sel sel = sel_wait(a.sync, abortw, lane, dead, true);
  const __amdgpu_buffer_rsrc_t rh = rsrc_hot(a.sync, sel), rum = rsrc_u(a.sync), ro = mk_rsrc(a.out, 8u << 20);
  const unsigned cq = hot_q((ci >> 1) & 3, sel), wq = hot_q(w, sel);
  if (!par && w < 2) st4(rh, H_MEM + cq + j * 1024 + (tq & 3) * 256 + (4 * (lane >> 4) + (lane & 3)) * 16 + ((lane >> 2) & 3) * 4, m0);
  PZERO;
  SSTAMP(32 + ci);
  u32x16 C, P; u32x8 C2;
  {
    const int i = lane & 15, g = lane >> 4;
    const unsigned mo = H_MEM + cq + j * 1024 + (ci & 1) * 512 + w * 128;
    C[0] = lane * 16; C[1] = (lane >> 5) * 2048 + (lane & 31) * 16;
    C[2] = ((i >> 3) ^ (j & 1)) * 512 + (i & 4) * 64 + g * 64 + (i & 3) * 16;
    C[3] = H_PZ + wq + (((g & 2) + (i >> 3) + 4 * (g & 1)) ^ j) * 256 + (i & 4) * 32 + (i & 3) * 16;
    C[4] = H_PZ + wq + ((g + 4 * (i >> 3)) ^ j) * 256 + (i & 4) * 32 + 64 + (i & 3) * 16;
    C[5] = (lane & 8) ? 0xEEEE : 0x4444; C[6] = (lane & 2) ? 0xEEEE : 0x4444;
    C[7] = l0 + LC_SUM + w * 1024 + lane * 4;
    C[8] = l0 + LC_SUM + (w >> 1) * 4096 + (2 * (w & 1) + (lane >> 5)) * 256 + ((2 * (lane & 1) + ((lane >> 3) & 1)) * 16 + 2 * (lane & 6) + 2 * ((lane >> 4) & 1)) * 4;
    C[9] = (lane & 1) ? OOR : mo + lane * 2;
    C[10] = H_ANN + wq + (lane & 7) * 128;
    C[11] = ((ci & 3) == w && lane == 0) ? H_TR + cq + (2 * j + (ci & 1)) * 128 : OOR;
    C[12] = H_TR + quarter((lane >> 1) & 3) + (2 * w + (lane & 1)) * 128; C[13] = (ci & 3) == w ? (lane & 31) * 128 : OOR;
    C[14] = (par | lane) == 0 && w == 1 ? SY_CT + (CT_PR + 128 + ci) * 128 : OOR;
    C[15] = l0 + LC_OUT + w * 4096 + (g * 16 + ((i + 4 * g) & 15)) * 16;
    C2[0] = l0 + LC_OUT + (((lane >> 1) & 2) + (w >> 1)) * 1024 + ((lane & 3) * 16 + ((8 * w + (lane >> 3) + 4 * (lane & 3)) & 15)) * 16;
    C2[1] = (8 * w + (lane >> 3)) * OUTW * 2 + DM * 2 + ci * 64 + (lane & 7) * 8;
    C2[2] = SY_CT + (CT_PR + 96 + 8 * w + (lane & 7)) * 128;
    C2[3] = lane < 8 ? mo + lane * 16 : OOR;
    C2[4] = (w | lane) == 0 ? ci * 128 : OOR;
    C2[5] = l0 + LC_TQ + 4 * w + (lane >> 6); C2[6] = 20000 + (lane >> 6); C2[7] = l0 + LC_TQ + (lane >> 6);
  }
#pragma unroll
  for (int i = 0; i < 16; ++i) P[i] = 0;
  ck.sync();
  P[0] = um_place(ci, w) + j * 512; P[1] = H_P + wq + (j & 2) * 512; P[8] = um_at(par + 2, P[0], sel);
  P[2] = (8 * w + j) * UXH + ux_tile(2 * ci); P[3] = H_PX + 2 * w * CHK + j * 2048; P[4] = par - 2; P[5] = P[7] = K_DEF * TU;
  P[6] = ck.c2t << 15; P[11] = ck.ce; P[12] = ck.t2c; P[13] = ck.te; P[15] = H_DLT + par * 4096;
#pragma unroll
  for (int i = 0; i < 16; ++i) P[i] = __builtin_amdgcn_readfirstlane(P[i]);
  u32x16 pv; (void)pv;
  const unsigned C24 = H_FLG + (8 * w + (lane & 7)) * 128;
  asm volatile(CB_BODY : "+&{s[40:55]}"(P) STEP_PV : "{v[224:239]}"(C), "{v[240:247]}"(C2) CB_PV, [rh] "s"(rh), [ru] "s"(rum), [ro] "s"(ro), [sel] "s"(sel[0]), [um] "s"(sel[1] >> 8), [far] "s"(((w ^ (ci >> 1)) & 1) * FAR1 + ((w ^ (ci >> 1)) & 2) * (FAR2 / 2))
               : "memory", "vcc", "scc", S22, V90, V10(9), V10(10), V10(11), V10(12), V10(13), V10(14), V10(15), V10(16), V10(17), V10(18), V10(19), V10(20), V10(21),
                 "v220", "v221", "v222", "v223", "s98", "s99", "s100", A10(), A10(1), A10(2), A10(3), A10(4), A10(5), "a60", "a61", "a62", "a63");
  lane = opaque_tid(0);
  PDUMP(2);
}

__device__ __forceinline__ void st16g(u16* p, u32x4 v, bool sc1) {
  if (sc1) asm volatile("global_store_dwordx4 %0, %1, off " OUTP "\n\ts_nop 1" :: "v"(p), "v"(v) : "memory");
  else *(u32x4*)p = v;
}
__device__ __forceinline__ u32x4 pk8(f32x4 a, f32x4 b) { return (u32x4){pk2(a[0], a[1]), pk2(a[2], a[3]), pk2(b[0], b[1]), pk2(b[2], b[3])}; }
__device__ __forceinline__ void role_p(const ChainArgs& a, int kind, int h, char* smem, const int w, Gate& G) {
  const int lane = opaque_tid(0), r = lane & 15, g = lane >> 4;
  const u32x4 rw = own_rsrc(kind == 0 ? a.Wq : (kind == 1 ? a.Wk : a.Wv), 8u << 20), rx = own_rsrc(a.X, 4u << 20);
  const unsigned lds0 = (unsigned)(uintptr_t)(LAS char*)smem + w * 4096;
  unsigned vw[4], vx[4];
#pragma unroll
  for (int i = 0; i < 4; ++i) {
    const int row = (w * 4 + i) * 4 + g, xr = 8 * w + 2 * i + (lane >> 5);
    vw[i] = row * HD * 2 + ((r ^ swz_b(row)) * 16);
    vx[i] = xr * DM * 2 + (((lane & 31) ^ (xr & 15)) * 16);
  }
  auto dma = [&](u32x4 rs, unsigned v, unsigned so, unsigned ld, bool nt = false) {
    if (nt) asm volatile("s_mov_b32 m0, %0\n\ts_nop 3\n\tbuffer_load_dwordx4 %1, %2, %3 offen nt lds" :: "s"(ld), "v"(v), "s"(rs), "s"(so) : "m0", "memory");
    else asm volatile("s_mov_b32 m0, %0\n\ts_nop 3\n\tbuffer_load_dwordx4 %1, %2, %3 offen lds" :: "s"(ld), "v"(v), "s"(rs), "s"(so) : "m0", "memory");
  };
  auto item = [&](int t) {
#pragma unroll
    for (int i = 0; i < 4; ++i) {
      if (t < 16) dma(rw, vw[i], (unsigned)((64 * (t ^ PREV) * HD + 128 * h) * 2), lds0 + (t & 7) * 16384 + i * 1024, (NTW >> 2) & 1);
      else dma(rx, vx[i], (unsigned)(((t - 16) >> 2) * 65536 + (t & 3) * 512), lds0 + (t & 7) * 16384 + i * 1024);
    }
  };
  Wts W;
#pragma unroll
  for (int i = 0; i < 32; ++i) asm volatile("" : "=a"(W.a[i]));
  if (kind < 2) while (__builtin_amdgcn_s_memrealtime() - G.entry < (kind ? K_DLY : Q_DLY)) __builtin_amdgcn_s_sleep(8);
#pragma unroll
  for (int t = 0; t < 8; ++t) item(t);
#pragma unroll
  for (int t = 0; t < 16; ++t) {
    if (t) wait_vm<24>(); else wait_vm<28>();
    lds_barrier();
    if (t) item(t + 7);
#pragma unroll
    for (int n = 0; n < 8; ++n) {
      const int row = 32 * (n >> 2) + 8 * g + 4 * (n & 1) + (r >> 2);
      const u32x2 u = __builtin_bit_cast(u32x2, __builtin_amdgcn_ds_read_tr16_b64_v4i16((LAS bf16x4*)(smem + (t & 7) * 16384 + row * 256 + (((4 * w + (r & 3)) ^ swz_b(row)) * 16) + 4 * (n & 2))));
      asm volatile("v_accvgpr_write_b32 a[%2], %0\n\tv_accvgpr_write_b32 a[%3], %1" :: "v"(u[0]), "v"(u[1]), "n"(16 * (t ^ PREV) + 2 * n), "n"(16 * (t ^ PREV) + 2 * n + 1));
    }
  }
  SSTAMP(160 + h);
  if (!G.pass(a.sync)) { wait_vm<0>(); return; }
  bool dead = false;
  const Sel sel = sel_wait(a.sync, (GAS unsigned*)(a.sync + SY_ABORT), lane, dead);
  wait_vm<24>();
  lds_barrier();
  item(23);
  u32x16 C;
#pragma unroll
  for (int i = 0; i < 4; ++i) { C[i] = vx[i]; C[4 + i] = lds0 - w * 4096 + r * 512 + (((4 * i + g) ^ r) * 16); C[12 + i] = 0; }
  C[8] = lane * 16; C[9] = lane * 16 + 4096;
  C[10] = 0;
  C[11] = (w | lane) == 0 ? SY_CT + (CT_PR + 32 * kind + h) * 128 : OOR;
  u32x8 P = {131072u, lds0, 0x7f000000u, (unsigned)(OFF_QX + kind * (32u << 20) + far_blk(h, sel, FARX >> kind) + w * 1024), 0u, 0u, 0u, 0u};
#pragma unroll
  for (int i = 0; i < 8; ++i) P[i] = __builtin_amdgcn_readfirstlane(P[i]);
  asm volatile(P_BODY : "+&{s[40:47]}"(P), "+&{v[160:175]}"(C) : [rx] "s"(rx), [ru] "s"(rsrc_u(a.sync)), [wv] "s"(w), [pp] "s"(a.prof + 296000 + (32 * kind + h) * 16), [pq] "s"(a.prof + 300000 + (32 * kind + h) * 48)
               : "memory", "vcc", "scc", "m0", S22, STEP_CL V90, V10(9), V10(10), V10(11), V10(12), V10(13), V10(14), V10(15), "v176", "v177", "v178", "v179", "v180");
  keep_a(W);
}
#define BG_SLP 64
#define A2N 2
constexpr int A2_NCH = A2N;
__device__ __forceinline__ float blo(unsigned d) { return __uint_as_float(d << 16); }
__device__ __forceinline__ float bhi(unsigned d) { return __uint_as_float(d & 0xffff0000u); }
constexpr int A2_CSTR = 4160;
constexpr int A2_UOFF = 8 * A2_CSTR + 16 * 80;
constexpr int A2_WLDS = A2_UOFF + 64 * 80;
static_assert(4 * A2_WLDS + 256 <= CHAIN_LDS - 16, "lds");
__device__ __forceinline__ unsigned kv_row(int d, Sel sel) { return far_blk(d >> 1, sel) + (d & 1) * 4096; }
__device__ __forceinline__ void a2_wave(const ChainArgs& a, int d0, int d1, char* smem, int w, Sel sel) {
  const int lane = opaque_tid(0);
  char* Wb = smem + w * A2_WLDS;
  char* U = Wb + A2_UOFF;
  const __amdgpu_buffer_rsrc_t rf = mk_rsrc((void*)a.Assm, 4u << 20);
  const __amdgpu_buffer_rsrc_t rkv = mk_rsrc((void*)a.kvT, 8u << 20);
  u32x4 hi[4], lo[4], uv[4];
  auto ld_h = [&](int d) {
#pragma unroll
    for (int i = 0; i < 4; ++i) {
      hi[i] = __builtin_amdgcn_raw_buffer_load_b128(rf, (unsigned)((255 - 64 * i - lane) * 16), (unsigned)(d * 4096), 2);
      lo[i] = __builtin_amdgcn_raw_buffer_load_b128(rf, (unsigned)(max(254 - 64 * i - lane, 0) * 16), (unsigned)(d * 4096), 2);
    }
  };
  auto ld_u = [&](int d) {
#pragma unroll
    for (int i = 0; i < 4; ++i) uv[i] = __builtin_amdgcn_raw_buffer_load_b128(rkv, (unsigned)((i * 64 + lane) * 16), kv_row(d, sel), LD_AUX);
  };
  {
    GAS unsigned* abortw = (GAS unsigned*)(a.sync + SY_ABORT);
    bool dead = false;
    ctr_wait<BG_SLP>(ctr(a, CT_A1 + (min(d0, D_A - 1) >> 7)), d0 < d1 ? 8u : 0u, abortw, lane, dead);
  }
  ld_h(d0); ld_u(d0);
  if (lane < 24) *(f32x4*)(Wb + (lane / 3) * A2_CSTR + (256 + lane % 3) * 16) = vzero();
#pragma unroll
  for (int i = 0; i < 2; ++i) if (64 * i + lane < 80) *(f32x4*)(U - 16 * 80 + (64 * i + lane) * 16) = vzero();
  const int r = lane & 15, g = lane >> 4;
#pragma unroll 1
  for (int d = d0; d < d1; ++d) {
#pragma unroll
    for (int i = 0; i < 4; ++i) {
      u32x4 l = lo[i];
      if (i == 3 && lane == 63) l = (u32x4){0u, 0u, 0u, 0u};
      const unsigned src[8] = {l[0], l[1], l[2], l[3], hi[i][0], hi[i][1], hi[i][2], hi[i][3]};
      unsigned f[15];
#pragma unroll
      for (int x = 0; x < 15; ++x) {
        if ((x & 1) == 0) f[x] = __builtin_amdgcn_alignbit(src[7 - x / 2], src[7 - x / 2], 16);
        else f[x] = __builtin_amdgcn_perm(src[7 - (x + 1) / 2], src[7 - (x - 1) / 2], 0x07060100u);
      }
#pragma unroll
      for (int m = 0; m < 8; ++m) *(u32x4*)(Wb + m * A2_CSTR + (64 * i + lane) * 16) = (u32x4){f[7 - m], f[9 - m], f[11 - m], f[13 - m]};
    }
#pragma unroll
    for (int i = 0; i < 4; ++i) {
      const int ch = i * 64 + lane;
      *(u32x4*)(U + (ch >> 2) * 80 + (ch & 3) * 16) = uv[i];
    }
    const float Dd = bf2f(a.AD[d]);
    asm volatile("" ::: "memory");
    ld_h(min(d + 1, d1 - 1)); ld_u(min(d + 1, d1 - 1));
    f32x4 acc[4][2];
#pragma unroll
    for (int i = 0; i < 8; ++i) acc[i >> 1][i & 1] = vzero();
    const char* pa = Wb + (r & 7) * A2_CSTR + 2 * (2040 - (r & 8) + 8 * g) - 3 * 1024 - 32 - 64;
    const char* pb = U + (r - 1) * 80 + 16 * g;
    bf16x8 fa[2][8], fb[2][4];
    auto rd = [&](int b, int n, int k) {
      if (n < 4) fb[b][n] = *(const bf16x8*)(pb + 80 * (16 * n + 1 - k));
      else fa[b][n - 4] = *(const bf16x8*)(pa + 1024 * (3 - ((n - 4) >> 1)) + 32 * (1 - (n & 1)) + 64 * (1 - k));
    };
#pragma unroll
    for (int n = 0; n < 12; ++n) rd(0, n, 0);
#pragma unroll 1
    for (int k = 0; k < 16; k += 2) {
#pragma unroll
      for (int b = 0; b < 2; ++b) {
        int q = 0;
#pragma unroll
        for (int j = 0; j < 4; ++j)
#pragma unroll
          for (int i = j; i < 4; ++i)
#pragma unroll
            for (int mt = 0; mt < 2; ++mt) {
              asm volatile("v_mfma_f32_16x16x32_bf16 %0, %1, %2, %0" : "+v"(acc[i][mt]) : "v"(fa[b][2 * j + mt]), "v"(fb[b][i - j]));
              SB0;
              if (q < 12) rd(b ^ 1, q, 1);
              if (q == 12) { pa -= 64; pb -= 80; }
              ++q;
              SB0;
            }
      }
    }
    asm volatile("s_nop 7" ::: "memory");
#pragma unroll
    for (int i = 0; i < 8; ++i) asm volatile("" : "+v"(acc[i >> 1][i & 1]));
#pragma unroll
    for (int i = 0; i < 8; ++i) {
      char* pz = U + (16 * (i >> 1) + r) * 80 + 32 * (i & 1) + 8 * g;
      const u32x2 kvv = *(const u32x2*)pz;
      const f32x4 y = acc[i >> 1][i & 1];
      *(u32x2*)pz = (u32x2){pk2(fmaf(blo(kvv[0]), Dd, y[0]), fmaf(bhi(kvv[0]), Dd, y[1])), pk2(fmaf(blo(kvv[1]), Dd, y[2]), fmaf(bhi(kvv[1]), Dd, y[3]))};
    }
#pragma unroll
    for (int i = 0; i < 4; ++i) {
      const int ch = i * 64 + lane;
      st16s(*(const u32x4*)(U + (ch >> 2) * 80 + (ch & 3) * 16), rkv, (unsigned)(ch * 16), kv_row(d, sel));
    }
  }
  wait_vm<0>();
  if (lane == 0 && d0 < d1) ctr_add(a, CT_A2 + (d0 >> 7), d1 - d0);
}

__device__ __forceinline__ void task_a1(const ChainArgs& a, int dp, int tq, char* smem, int wv, Sel sel) {
  const int lane = opaque_tid(0);
  const int dg = 2 * dp + (wv & 1), t0 = tq * 256 + (wv >> 1) * 128;
  const int d = dg * 64 + 8 * (lane & 7);
  const int tb = t0 + 8 * (lane >> 3);
  char* T = smem + wv * 16384;
  u32x4 kk[2][11], vv[2][8], sh[4];
#pragma unroll
  for (int i = 0; i < 4; ++i) sh[i] = *(const u32x4*)(a.Ashift + d * 4 + 8 * i);
  SB0;
#pragma unroll
  for (int gq = 0; gq < 2; ++gq) {
#pragma unroll
    for (int i = 0; i < 11; ++i) {
      const int t = tb + 64 * gq - 3 + i;
      kk[gq][i] = LDNT((const u32x4*)(a.Ak + (long)(t < 0 ? 0 : t) * D_A + d));
    }
#pragma unroll
    for (int i = 0; i < 8; ++i) vv[gq][i] = LDNT((const u32x4*)(a.Av + (long)(tb + 64 * gq + i) * D_A + d));
    SB0;
  }
#pragma unroll
  for (int i = 0; i < 3; ++i) if (tb == 0) kk[0][i] = (u32x4){0u, 0u, 0u, 0u};
#pragma unroll
  for (int gq = 0; gq < 2; ++gq) {
#pragma unroll
    for (int c = 0; c < 8; ++c) {
      const unsigned t01 = sh[c >> 1][2 * (c & 1)], t23 = sh[c >> 1][2 * (c & 1) + 1];
      const float s0 = blo(t01), s1 = bhi(t01), s2 = blo(t23), s3 = bhi(t23);
      float kf[11];
#pragma unroll
      for (int i = 0; i < 11; ++i) kf[i] = (c & 1) ? bhi(kk[gq][i][c >> 1]) : blo(kk[gq][i][c >> 1]);
      u32x4 o;
#pragma unroll
      for (int i = 0; i < 8; i += 2) {
        float r[2];
#pragma unroll
        for (int j = 0; j < 2; ++j)
          r[j] = fmaf(kf[i + j + 3], s0, fmaf(kf[i + j + 2], s1, fmaf(kf[i + j + 1], s2, kf[i + j] * s3))) * ((c & 1) ? bhi(vv[gq][i + j][c >> 1]) : blo(vv[gq][i + j][c >> 1]));
        o[i >> 1] = pk2(r[0], r[1]);
      }
      *(u32x4*)(T + (8 * (lane & 7) + c) * 256 + (((gq * 8 + (lane >> 3)) ^ (lane & 7)) * 16)) = o;
    }
    SB0;
  }
  const __amdgpu_buffer_rsrc_t rkv = mk_rsrc((void*)a.kvT, 8u << 20);
#pragma unroll
  for (int i = 0; i < 16; ++i) {
    const int row = 4 * i + (lane >> 4);
    const u32x4 o = *(const u32x4*)(T + row * 256 + (((lane & 15) ^ (row >> 3)) * 16));
    st16(rkv, kv_row(dg * 64 + row, sel) + (t0 + (lane & 15) * 8) * 2, o);
  }
  bg_drain();
  if (opaque_tid(wv) == 0) ctr_add(a, CT_A1 + dp, 1u);
}

__device__ __forceinline__ void task_a3(const ChainArgs& a, int dp, int tq, char* smem, int wv, Sel sel) {
  const int lane = opaque_tid(0);
  const int dg = 2 * dp + (wv & 1), t0 = tq * 256 + (wv >> 1) * 128;
  const int d = dg * 64 + 8 * (lane & 7);
  const int tb = t0 + 8 * (lane >> 3);
  const __amdgpu_buffer_rsrc_t rz = mk_rsrc((void*)a.kvT, 8u << 20);
  u32x4 qq[16], zz[8][2];
#pragma unroll
  for (int i = 0; i < 16; ++i) qq[i] = LDNT((const u32x4*)(a.Aq + (long)(tb + 64 * (i >> 3) + (i & 7)) * D_A + d));
  char* T = smem + wv * 16384;
  {
    u32x4 zr[16];
    bool dead = false;
    ctr_wait<BG_SLP>(ctr(a, CT_A2 + dp), 128u, (GAS unsigned*)(a.sync + SY_ABORT), lane, dead);
#pragma unroll
    for (int i = 0; i < 16; ++i) zr[i] = ld16(rz, kv_row(dg * 64 + 4 * i + (lane >> 4), sel) + (t0 + (lane & 15) * 8) * 2);
#pragma unroll
    for (int i = 0; i < 16; ++i) {
      const int row = 4 * i + (lane >> 4);
      *(u32x4*)(T + row * 256 + (((lane & 15) ^ (row >> 3)) * 16)) = zr[i];
    }
  }
#pragma unroll
  for (int c = 0; c < 8; ++c)
#pragma unroll
    for (int gq = 0; gq < 2; ++gq) zz[c][gq] = *(const u32x4*)(T + (8 * (lane & 7) + c) * 256 + (((gq * 8 + (lane >> 3)) ^ (lane & 7)) * 16));
#pragma unroll
  for (int i = 0; i < 16; ++i) {
    u32x4 o;
#pragma unroll
    for (int c = 0; c < 8; c += 2) {
      const unsigned z0 = zz[c][i >> 3][(i >> 1) & 3], z1 = zz[c + 1][i >> 3][(i >> 1) & 3];
      o[c >> 1] = pk2(blo(qq[i][c >> 1]) * ((i & 1) ? bhi(z0) : blo(z0)), bhi(qq[i][c >> 1]) * ((i & 1) ? bhi(z1) : blo(z1)));
    }
    st16g(a.out + (long)(tb + 64 * (i >> 3) + (i & 7)) * OUTW + d, o, true);
  }
}

__device__ __forceinline__ void role_a(const ChainArgs& a, char* smem, int wv) {
  constexpr int N2 = 256 / A2_NCH;
  bool dead = false;
  const Sel sel = sel_wait(a.sync, (GAS unsigned*)(a.sync + SY_ABORT), opaque_tid(0), dead);
  while (true) {
    asm volatile("" : "+s"(wv));
    int toff = CHAIN_LDS - 16;
    asm volatile("" : "+v"(toff));
    volatile LAS int* tslot = (volatile LAS int*)(smem + toff);
    if (opaque_tid(wv) == 0) *tslot = (int)__hip_atomic_fetch_add(ctr(a, CT_QUEUE), 1u, __ATOMIC_RELAXED, __HIP_MEMORY_SCOPE_AGENT) + 1;
    __syncthreads();
    const int t = __builtin_amdgcn_readfirstlane(*tslot);
    __syncthreads();
    if (t >= 128 + N2) break;
    if (t < 64) task_a1(a, t >> 3, t & 7, smem, wv, sel);
    else if (t < 64 + N2) a2_wave(a, A2_NCH * (4 * (t - 64) + wv), A2_NCH * (4 * (t - 64) + wv + 1), smem, wv, sel);
    else task_a3(a, (t - 64 - N2) >> 3, t & 7, smem, wv, sel);
  }
}
__device__ __forceinline__ char* lds_base(char* smem, int off) { asm volatile("" : "+v"(off)); return smem + off; }
__device__ __forceinline__ void role_u(const ChainArgs& a, int h, char* smem, int w, Gate& G) {
  int lane = opaque_tid(0);
  GAS unsigned* abortw = (GAS unsigned*)(a.sync + SY_ABORT);
  bool dead = false;
  char* sb = smem + w * 32768;
  const __amdgpu_buffer_rsrc_t rwo = mk_rsrc(a.Wo, 8u << 20);
  const unsigned lds0 = (unsigned)(uintptr_t)(LAS char*)sb;
  unsigned wo4[4];
#pragma unroll
  for (int i = 0; i < 4; ++i) { const int row = i * 4 + (lane >> 4); wo4[i] = row * DM * 2 + (((lane & 15) ^ swz_b(row)) * 16); }
  asm volatile("s_nop 4");
  auto stage = [&](int p) {
#pragma unroll
    for (int i = 0; i < 16; ++i)
      dma16(rwo, wo4[i & 3], (unsigned)(((128 * h + 64 * (p & 1) + 16 * (i >> 2)) * DM + 256 * w + 128 * (p >> 1)) * 2), lds0 + (p & 1) * 16384 + i * 1024, (NTW >> 3) & 1);
  };
  __builtin_amdgcn_s_waitcnt(0x0F70);
  Wts W;
#pragma unroll
  for (int i = 0; i < 32; ++i) asm volatile("" : "=a"(W.a[i]));
  stage(0); stage(1);
#pragma unroll
  for (int p = 0; p < 4; ++p) {
    if (p < 3) wait_vm<16>(); else wait_vm<0>();
#pragma unroll
    for (int t = 0; t < 8; ++t) {
      const u32x8 f = tr_frag(sb + (p & 1) * 16384, t, lane);
#pragma unroll
      for (int i = 0; i < 8; ++i) asm volatile("v_accvgpr_write_b32 a[%1], %0" :: "v"(f[i]), "n"(16 * (8 * (p >> 1) + t) + 8 * (p & 1) + i));
    }
    asm volatile("s_waitcnt lgkmcnt(0)" ::: "memory");
    if (p + 2 < 4) stage(p + 2);
  }
  SSTAMP(192 + h);
  if (!G.pass(a.sync)) return;
  const Sel sel = sel_wait(a.sync, abortw, lane, dead);
  const __amdgpu_buffer_rsrc_t ru = rsrc_u(a.sync);
  volatile LAS int* fl = (volatile LAS int*)(smem + 16384);
  int s = 0;
  while (true) {
    lane = opaque_tid(0);
    const unsigned lo = SY_CT + (CT_PR + 64 + 64 * (lane & 1) + h) * 128, sg = (w | lane) == 0 ? SY_CT + (CT_PR + 96 + h) * 128 : OOR;
    bg_drain();
    st4(ru, sg, s - 1);
    if (s >= NSEG) break;
    if (w == 0) {
      unsigned spins = 0;
      int can;
      while (true) {
        const unsigned cv = ld4(ru, lane < 2 ? lo : OOR);
        can = min(__builtin_amdgcn_readlane(cv, 0), __builtin_amdgcn_readlane(cv, 1) + URING - 1);
        if (can >= min(s + (s < 4 ? U_AH : U_AH2), NSEG - 1)) break;
        if (s < 4) asm volatile("s_sleep 3" ::: "memory"); else asm volatile("s_sleep %0" :: "n"(U_SLP) : "memory");
        if (dead || poll_fail(spins, abortw, lane)) { dead = true; can = NSEG; break; }
      }
      fl[1] = can;
    }
    lds_barrier();
#pragma unroll
    for (int i = 0; i < 2; ++i) *(u32x4*)(smem + w * 2048 + i * 1024 + lane * 16) = ld16(ru, OFF_VX + far_blk(s * NH + h, sel) + w * 2048 + i * 1024 + lane * 16);
    u32x8 P = {(unsigned)s, (unsigned)fl[1], h * UXH + ux_tile(16) * w, (unsigned)(OFF_VX + far_blk(h, sel) + w * 2048), 0x7f000000u, 0u, 0u, 0u};
#pragma unroll
    for (int i = 0; i < 8; ++i) P[i] = __builtin_amdgcn_readfirstlane(P[i]);
    const unsigned l0 = lds0 - w * 32768;
    u32x8 C = {(unsigned)lane * 16, l0 + lane * 16, l0 + w * 2048 + lane * 16, lo, sg, l0 + 16384 + (lane >> 6), 0u, 0u};
    lds_barrier();
    asm volatile(U_BODY : "+&{s[40:47]}"(P), "+&{v[80:87]}"(C) : [ru] "s"(ru), [sel] "s"(sel[0]), [wv] "s"(w), [pp] "s"(a.prof + 297600 + h * 64 + (s ? 0 : 9000))
                 : "memory", "vcc", "scc", "s76", "s77", "s78", "s79", "s80", "s81", V10(), V10(1), V10(2), V10(3), V10(4), V10(5), V10(6), V10(7));
    keep_a(W);
    s = __builtin_amdgcn_readfirstlane(P[0]);
  }
}

__global__ __launch_bounds__(256, 1) void chain_kernel(ChainArgs a) {
  extern __shared__ __attribute__((aligned(16))) char smem[];
  unsigned again = blockIdx.x;
  asm volatile("" : "+s"(again));
  const int b = again & 255;
  again >>= 8;
  {
    u32x4 rc;
    asm volatile("s_getpc_b64 s[60:61]\n\t.Lchain_here:\n\ts_sub_u32 s60, s60, .Lchain_here-_Z12chain_kernel9ChainArgs\n\ts_subb_u32 s61, s61, 0\n\ts_and_b32 s61, s61, 0xffff\n\t"
                 "s_mov_b32 s62, .Lchain_end-_Z12chain_kernel9ChainArgs\n\ts_mov_b32 s63, 0x20000\n\t"
                 "buffer_load_dwordx4 a[252:255], %1, s[60:63], 0 offen\n\tbuffer_load_dwordx4 a[252:255], %1, s[60:63], s63 offen"
                 : "={s[60:63]}"(rc) : "v"((b >> 3) * 4096 + threadIdx.x * 16) : "scc", "a252", "a253", "a254", "a255");
  }
  int w = __builtin_amdgcn_readfirstlane(threadIdx.x >> 6);
  Gate G;
  G.entry = __builtin_amdgcn_s_memrealtime(); G.open = again != 0; G.arr = w == 0 && !again ? b & 7 : -1;
  G.rot = (again ? again : (unsigned)__builtin_amdgcn_s_getreg(20 | (3 << 11)) - b + WRONG) & 7u;
  {
    volatile LAS unsigned* t = (volatile LAS unsigned*)smem;
    if (w == 0) { t[0] = (unsigned)G.entry; t[1] = (unsigned)(G.entry >> 32); }
    __syncthreads();
    G.entry = (u64)(unsigned)__builtin_amdgcn_readfirstlane(t[0]) | ((u64)(unsigned)__builtin_amdgcn_readfirstlane(t[1]) << 32);
    __syncthreads();
  }
  if (b == 0 && !again) {
    const int lane = opaque_tid(0);
    const __amdgpu_buffer_rsrc_t rs = mk_rsrc(a.sync + SY_ABORT, SY_BYTES - SY_ABORT);
    for (int i = w; i * 1024 < SY_BYTES - SY_ABORT; i += 4) st16(rs, i * 1024 + lane * 16, (u32x4){SENT, SENT, SENT, SENT});
    bg_drain();
    if (w == 0 && lane == 0) __hip_atomic_store((GAS u64*)(a.sync + SY_EPOCH), (__builtin_amdgcn_s_memrealtime() & ~7ull) | G.rot, __ATOMIC_RELAXED, __HIP_MEMORY_SCOPE_AGENT);
  }
  {
    const unsigned rot = G.rot;
    const int x = (b + rot) & 7, j = ((b >> 3) + 3) & 31, i = j >> 2, role = (x & 4) + (j & 3), q = 2 * (x & 1) + (x >> 1);
    if ((role == 3 || role == 4) && w >= 2) {
      const int n = opaque_tid(0);
      const unsigned x0 = __builtin_amdgcn_raw_buffer_load_b32(mk_rsrc(a.sync, WS_USED), (n + 64 * (w - 2)) * PAGE + 4096 + j * 128, 0, 2);
      const unsigned x1 = __builtin_amdgcn_raw_buffer_load_b32(mk_rsrc(a.out, 8u << 20), n * PAGE + 4096 + j * 128, 0, 2);
      asm volatile("" :: "v"(x0), "v"(x1));
    }
    if (role == 0) role_qk(a, 8 * q + i, smem, w, G);
    else if (role == 1) role_vo(a, 8 * q + i, smem, w, G);
    else if (role < 4) role_cb(a, 8 * (i >> 1) + 2 * q + (i & 1), role - 2, smem, w, G);
    else if (role < 7) role_p(a, role - 4, 8 * (x - 4) + i, smem, w, G);
    else role_u(a, 8 * (x - 4) + i, smem, w, G);
    if (G.rot != rot) {
      __syncthreads();
      asm volatile("s_getpc_b64 s[60:61]\n\t.Lchain_back:\n\ts_sub_u32 s60, s60, .Lchain_back-_Z12chain_kernel9ChainArgs\n\ts_subb_u32 s61, s61, 0\n\ts_setpc_b64 s[60:61]"
                   :: "{s[0:1]}"(__builtin_amdgcn_kernarg_segment_ptr()), "{s2}"(b | (8u | G.rot) << 8), "{v0}"(opaque_tid(w)) : "s60", "s61", "scc", "memory");
      __builtin_unreachable();
    }
  }
  if ((b + G.rot) & 4) role_a(a, smem, w);
  asm volatile(".Lchain_end:");
}

extern "C" void kernel_launch(void* const* d_in, const int* in_sizes, int n_in,
                              void* d_out, int out_size, void* d_ws, size_t ws_size,
                              hipStream_t stream) {
  char* ws = (char*)(((uintptr_t)d_ws + (2u << 20) - 1) & ~(uintptr_t)((2u << 20) - 1));
  hipFuncSetAttribute((const void*)chain_kernel, hipFuncAttributeMaxDynamicSharedMemorySize, CHAIN_LDS);
  ChainArgs ca;
  ca.prof = (u64*)((char*)d_ws + PROF_AT);
  ca.Aq = (const u16*)d_in[0]; ca.Ak = (const u16*)d_in[1]; ca.Av = (const u16*)d_in[2];
  ca.Ashift = (const u16*)d_in[3]; ca.Assm = (const u16*)d_in[4]; ca.AD = (const u16*)d_in[5];
  ca.X = (const u16*)d_in[6]; ca.mem0 = (const u16*)d_in[7];
  ca.Wq = (const u16*)d_in[8]; ca.Wk = (const u16*)d_in[9]; ca.Wv = (const u16*)d_in[10]; ca.Wo = (const u16*)d_in[11];
  ca.out = (u16*)d_out; ca.sync = ws; ca.kvT = (u16*)(ws + OFF_KVT);
  chain_kernel<<<256, 256, CHAIN_LDS, stream>>>(ca);
}
```

```cpp
#include <hip/hip_runtime.h>
#include <hip/hip_bf16.h>
#include <stdint.h>
#include <type_traits>

constexpr int S_LEN = 2048;
constexpr int D_A   = 1024;
constexpr int DM    = 1024;
constexpr int NH    = 32;
constexpr int DH    = 128;
constexpr int HD    = NH * DH;
constexpr int MEMR  = 8;
constexpr int SEG   = 32;
constexpr int NSEG  = S_LEN / SEG;
constexpr int OUTW  = 2048;

typedef unsigned short u16;
typedef unsigned long long u64;
typedef short bf16x8 __attribute__((ext_vector_type(8)));
typedef short bf16x4 __attribute__((ext_vector_type(4)));
typedef float f32x4 __attribute__((ext_vector_type(4)));
typedef float f32x16 __attribute__((ext_vector_type(16)));
typedef unsigned u32x4 __attribute__((ext_vector_type(4)));
typedef unsigned u32x2 __attribute__((ext_vector_type(2)));
#define LAS __attribute__((address_space(3)))
#define GAS __attribute__((address_space(1)))

constexpr size_t MB = 1u << 20;
constexpr size_t OFF_QX  = 68 * MB;
constexpr size_t OFF_KX  = 100 * MB;
constexpr size_t OFF_VX  = 132 * MB;
constexpr size_t OFF_KVT = 164 * MB;
constexpr unsigned WS_USED = 172u << 20;
constexpr size_t PROF_AT = 0;

__device__ __forceinline__ float bf2f(u16 b) { return __uint_as_float(((unsigned)b) << 16); }
__device__ __forceinline__ u16 f2bf(float f) { __bf16 b = (__bf16)f; return __builtin_bit_cast(u16, b); }
__device__ __forceinline__ float rbf(float f) { return bf2f(f2bf(f)); }
__device__ __forceinline__ unsigned pk2(float a, float b) {
  typedef float f2 __attribute__((ext_vector_type(2)));
  typedef __bf16 b2 __attribute__((ext_vector_type(2)));
  f2 v = {a, b}; b2 r = __builtin_convertvector(v, b2); return __builtin_bit_cast(unsigned, r);
}

__device__ __forceinline__ int opaque_tid(int wv) {
  int l = __builtin_amdgcn_mbcnt_hi(~0u, __builtin_amdgcn_mbcnt_lo(~0u, 0u));
  asm volatile("" : "+v"(l));
  return wv * 64 + l;
}
__device__ __forceinline__ int swz_b(int row) { return ((row & 3) << 2) | ((row >> 2) & 3); }

#define KNOB(name, v) constexpr int name = v
KNOB(K_DEF, 80);
KNOB(W_STEP, 33); KNOB(W_OFF, 56);
#define ORD 0
#define RHO(h) (((h) >> 3) ^ ORD)
#define CAL_A 800
#define CAL_T 1350
#define CAL_N 4
#define WRONG 0
#define OUTP "nt"
#define PREV 0
#define PDMA 1
#define PRD 1
#define XSW 1
#define P_EAG 3
#define FILL0 4
#define NOFL 0
#define NOFILL 0
#define XS_T 1000
#define XS_G 64
#define K_DLY 300
#define Q_DLY 300
#define U_STG 1
#define NTW 0
#define XS_N 32
#define U_AH 0
#define U_POS 11
#define FAR1 0
#define FAR2 0
#define KEYX 0
#define FARX 3
#define U_SLP 40
#define U_AH2 0
#define ETEST 0
#define MS 3
#define F_SLP 0
#define A_DELAY 1500
#define LDNT(p) __builtin_nontemporal_load(p)
constexpr int CHK      = 16384;
constexpr int PAGE     = 2 << 20;
constexpr int SY_UM    = 0;
constexpr int SY_HOT   = 32 * PAGE;
constexpr int H_MEM    = 0;
constexpr int H_P      = 16 * CHK;
constexpr int H_PZ     = H_P + 2 * CHK;
constexpr int H_ANN    = H_PZ + 2048;
constexpr int H_TR     = H_PZ + 3072;
constexpr int H_DLT    = 80 * CHK;
constexpr int H_FLG    = 81 * CHK;
constexpr int H_PX     = 96 * CHK;
constexpr int H_BYTES  = 128 * CHK;
constexpr int SY_SET   = 33 * PAGE;
constexpr int SY_EPOCH = SY_SET + 128;
constexpr int SY_ABORT = SY_SET + 256;
constexpr int SY_SEL   = SY_ABORT + 128;
constexpr int SY_CT    = SY_ABORT + 640;
constexpr int CT_ARR = 0, CT_INIT = 8, CT_QUEUE = 9, CT_A1 = 10, CT_A2 = 18, CT_CAL = 26;
constexpr int CT_PR = CT_CAL + 64;
constexpr int SY_PU = SY_SET + 2 * CHK;
constexpr int SY_BYTES = SY_PU + 12288;
static_assert(SY_CT + (CT_PR + 128) * 128 <= SY_PU, "counters");
constexpr int URING = 16;
constexpr unsigned SENT = 0xFFFFFFFFu;
constexpr unsigned OOR = 0x7fffffffu;
constexpr unsigned SPIN_CAP = 1u << 19;
constexpr int NLF = 16;
constexpr int L_WTS = 0;
constexpr int L_MEM = L_WTS + 4 * NLF * 2048;
constexpr int L_XCH = L_MEM + 16384;
constexpr int L_XK  = L_XCH + 2048;
constexpr int L_KX  = L_XCH + 4096;
constexpr int L_PRF = L_KX + 8192;
constexpr int XRS = 40;
constexpr int CHAIN_LDS = 160 * 1024;
static_assert(L_XCH + 128 * XRS <= L_PRF && L_PRF + 4096 <= CHAIN_LDS, "lds");

typedef __bf16 bfv8 __attribute__((ext_vector_type(8)));
typedef __bf16 bfv16 __attribute__((ext_vector_type(16)));
typedef unsigned u32x8 __attribute__((ext_vector_type(8)));
typedef unsigned u32x12 __attribute__((ext_vector_type(12)));
typedef unsigned u32x16 __attribute__((ext_vector_type(16)));
typedef unsigned u32x32 __attribute__((ext_vector_type(32)));
struct Wts { u32x8 a[32]; u32x32 v[4]; };

#define STAMPL(i) do {} while (0)
#define PVAL(i, v) do {} while (0)
#define PDECL(base, str) do {} while (0)
#define PZERO do {} while (0)
#define PDUMP(mask) do {} while (0)
#define TA(x) do {} while (0)
#define TB(x) TS(x)
#define TS(x) do {} while (0)
#define SSTAMP(i) do {} while (0)
struct ChainArgs {
  u64* prof;
  const u16* mem0; const u16* Wq; const u16* Wk; const u16* Wv; const u16* Wo; const u16* X;
  u16* out; char* sync;
  const u16* Aq; const u16* Ak; const u16* Av; const u16* Ashift; const u16* Assm; const u16* AD; u16* kvT;
};
__device__ __forceinline__ GAS unsigned* ctr(const ChainArgs& a, int i) { return (GAS unsigned*)(a.sync + SY_CT + i * 128); }
__device__ __forceinline__ void ctr_add(const ChainArgs& a, int i, unsigned v) {
  __hip_atomic_fetch_add(ctr(a, i), v, __ATOMIC_RELAXED, __HIP_MEMORY_SCOPE_AGENT);
}

__device__ __forceinline__ __amdgpu_buffer_rsrc_t mk_rsrc(const void* p, unsigned bytes) {
  const u64 v = (u64)p;
  const unsigned lo = (unsigned)__builtin_amdgcn_readfirstlane((unsigned)v), hi = (unsigned)__builtin_amdgcn_readfirstlane((unsigned)(v >> 32));
  const u64 u = (u64)lo | ((u64)hi << 32);
  return __builtin_amdgcn_make_buffer_rsrc((void*)u, 0, bytes, 0x00020000);
}
__device__ __forceinline__ u32x4 own_rsrc(const void* p, unsigned bytes) {
  u32x4 r = {(unsigned)__builtin_amdgcn_readfirstlane((unsigned)(u64)p), (unsigned)__builtin_amdgcn_readfirstlane((unsigned)((u64)p >> 32)) & 0xffffu, bytes, 0x00020000u};
  asm volatile("s_nop 4" : "+s"(r));
  return r;
}
constexpr int LD_AUX = 16, ST_AUX = 16;
__device__ __forceinline__ u32x4 ld16(__amdgpu_buffer_rsrc_t rs, unsigned off) {
  return __builtin_amdgcn_raw_buffer_load_b128(rs, off, 0, LD_AUX);
}
__device__ __forceinline__ void st16(__amdgpu_buffer_rsrc_t rs, unsigned off, u32x4 v) {
  __builtin_amdgcn_raw_buffer_store_b128(v, rs, off, 0, ST_AUX);
}
__device__ __forceinline__ void st16s(u32x4 v, __amdgpu_buffer_rsrc_t rs, unsigned voff, unsigned soff) {
  __builtin_amdgcn_raw_buffer_store_b128(v, rs, voff, soff, ST_AUX);
  asm volatile("s_nop 1" :: "v"(v) : "memory");
}
__device__ __forceinline__ void st8(__amdgpu_buffer_rsrc_t rs, unsigned off, u32x2 v) {
  __builtin_amdgcn_raw_buffer_store_b64(v, rs, off, 0, ST_AUX);
}
__device__ __forceinline__ void st4(__amdgpu_buffer_rsrc_t rs, unsigned off, unsigned v) {
  __builtin_amdgcn_raw_buffer_store_b32(v, rs, off, 0, ST_AUX);
}
__device__ __forceinline__ unsigned ld4(__amdgpu_buffer_rsrc_t rs, unsigned off) {
  return __builtin_amdgcn_raw_buffer_load_b32(rs, off, 0, LD_AUX);
}
constexpr int TU = 16;
__device__ __forceinline__ unsigned cyc32() { return (unsigned)__builtin_amdgcn_s_memtime(); }
__device__ __forceinline__ unsigned to_sgpr(unsigned x) {
  unsigned r;
  asm volatile("s_nop 0\n\tv_readfirstlane_b32 %0, %1\n\ts_nop 4" : "=s"(r) : "v"(x));
  return r;
}
struct Clk {
  unsigned ce, te;
  unsigned c2t, t2c;
  __device__ __forceinline__ void edge(unsigned& c, unsigned& t) {
    u64 c0, r0, r1, r2, r3, r4, r5;
    asm volatile("s_memtime %0\n\ts_memrealtime %1\n\ts_nop 4\n\ts_memrealtime %2\n\ts_nop 4\n\ts_memrealtime %3\n\ts_nop 4\n\t"
                 "s_memrealtime %4\n\ts_nop 4\n\ts_memrealtime %5\n\ts_nop 4\n\ts_memrealtime %6\n\ts_waitcnt lgkmcnt(0)"
                 : "=&s"(c0), "=&s"(r0), "=&s"(r1), "=&s"(r2), "=&s"(r3), "=&s"(r4), "=&s"(r5) :: "memory");
    const unsigned x = (unsigned)r0;
    const unsigned n = ((unsigned)r1 == x) + ((unsigned)r2 == x) + ((unsigned)r3 == x) + ((unsigned)r4 == x) + ((unsigned)r5 == x);
    c = to_sgpr((unsigned)c0 + 24u * n); t = (x + 4u) * TU;
  }
  __device__ __forceinline__ void init() { edge(ce, te); c2t = 44000u; t2c = 97600u; }
  __device__ __forceinline__ void sync() {
    unsigned c, t;
    edge(c, t);
    set(c, t);
  }
  __device__ __forceinline__ void set(unsigned c, unsigned t) {
    if (c == ce) return;
    const float q = (float)(t - te) * __builtin_amdgcn_rcpf((float)(c - ce));
    const float qc = fminf(fmaxf(q, 0.51f), 1.99f);
    c2t = to_sgpr((unsigned)(qc * 65536.f));
    t2c = to_sgpr((unsigned)(__builtin_amdgcn_rcpf(qc) * 65536.f));
    ce = c; te = t;
  }
  __device__ __forceinline__ unsigned time(unsigned c) const { return te + ((min(c - ce, 30000u) * c2t) >> 16); }
  __device__ __forceinline__ unsigned now() const { return time(cyc32()); }
  __device__ __forceinline__ unsigned target(unsigned t) const {
    return to_sgpr(ce + (((unsigned)min(max((int)(t - te), 0), 20000) * t2c) >> 16));
  }
  __device__ __forceinline__ void wait(unsigned t) const { wait_c(target(t)); }
  __device__ __forceinline__ void wait_c(unsigned c) const {
    unsigned y;
    asm volatile("1:\n\ts_memtime vcc\n\ts_waitcnt lgkmcnt(0)\n\ts_sub_i32 %0, %1, vcc_lo\n\ts_cmp_gt_i32 %0, 128\n\ts_cbranch_scc1 1b\n\t"
                 "2:\n\ts_sub_i32 %0, %0, %2\n\ts_cmp_gt_i32 %0, %3\n\ts_cbranch_scc1 2b"
                 : "=&s"(y) : "s"(c), "n"(W_STEP), "n"(W_OFF) : "memory", "scc", "vcc");
  }
};
__device__ __forceinline__ unsigned nosent(unsigned d) { return min(d, SENT - 1u); }
__device__ __forceinline__ int arow(int row, int half) { return 4 * (row >> 1) + 2 * half + (row & 1); }
__device__ __forceinline__ unsigned umax4(u32x4 v) { return max(max(v[0], v[1]), max(v[2], v[3])); }
__device__ __forceinline__ void stamp_again(GAS u64* p) {
  __hip_atomic_store(p, (__builtin_amdgcn_s_memrealtime() & ~7ull) | (__hip_atomic_load(p, __ATOMIC_RELAXED, __HIP_MEMORY_SCOPE_AGENT) & 7ull),
                     __ATOMIC_RELAXED, __HIP_MEMORY_SCOPE_AGENT);
}
__device__ __forceinline__ bool poll_fail(unsigned& spins, GAS unsigned* abortw, int lane) {
  ++spins;
  if (spins > SPIN_CAP) {
    if (lane == 0) __hip_atomic_store(abortw, 1u, __ATOMIC_RELAXED, __HIP_MEMORY_SCOPE_AGENT);
    return true;
  }
  if ((spins & 511u) == 0u && lane == 0) {
    unsigned n = 0;
    for (int i = 0; i < 8; ++i) n += __hip_atomic_load(abortw + (SY_CT - SY_ABORT + (CT_ARR + i) * 128) / 4, __ATOMIC_RELAXED, __HIP_MEMORY_SCOPE_AGENT) + 1u;
    if (n < 256u) stamp_again((GAS u64*)(abortw - 32));
  }
  if ((spins & 63u) == 0u)
    return __builtin_amdgcn_readfirstlane(__hip_atomic_load(abortw, __ATOMIC_RELAXED, __HIP_MEMORY_SCOPE_AGENT)) != SENT;
  return false;
}
struct Gate {
  u64 entry; int arr; unsigned rot; bool open;
  __device__ __forceinline__ bool look(char* sync, u64 v = 0) {
    if (open) return true;
    for (unsigned spins = 0;; ++spins) {
      if (spins) v = __hip_atomic_load((GAS u64*)(sync + SY_EPOCH), __ATOMIC_RELAXED, __HIP_MEMORY_SCOPE_AGENT);
      v = (u64)(unsigned)__builtin_amdgcn_readfirstlane((unsigned)v) | ((u64)(unsigned)__builtin_amdgcn_readfirstlane((unsigned)(v >> 32)) << 32);
      if (v - entry + 2000u < (1ull << 31) || spins > 4 * SPIN_CAP) break;
      if (spins) __builtin_amdgcn_s_sleep(8);
    }
    asm volatile("" ::: "memory");
    const bool same = rot == ((unsigned)v & 7u);
    rot = (unsigned)v & 7u; open = true;
    return same;
  }
  __device__ __forceinline__ void arrive(char* sync) {
    if (arr >= 0 && opaque_tid(0) == 0) __hip_atomic_fetch_add((GAS unsigned*)(sync + SY_CT + (CT_ARR + arr) * 128), 1u, __ATOMIC_RELAXED, __HIP_MEMORY_SCOPE_AGENT);
    arr = -1;
  }
  __device__ __forceinline__ bool pass(char* sync, u64 v = 0) { const bool r = look(sync, v); arrive(sync); return r; }
};
__device__ __forceinline__ unsigned die_half() { return ((unsigned)__builtin_amdgcn_s_getreg(20 | (3 << 11)) >> 2) & 1u; }
typedef u32x2 Sel;
__device__ __forceinline__ Sel sel_wait(char* sync, GAS unsigned* abortw, int lane, bool& dead, bool all = false, int c0 = CT_INIT, int n0 = 0, int c1 = CT_INIT, int n1 = 0) {
  unsigned spins = 0;
  u64 v;
  const bool m = lane == 0 || lane > 3 || !all;
  const int off = m ? SY_SEL : SY_CT + (lane == 1 ? CT_INIT : lane == 2 ? c0 : c1) * 128;
  while (true) {
    v = __hip_atomic_load((GAS u64*)(sync + off), __ATOMIC_RELAXED, __HIP_MEMORY_SCOPE_AGENT);
    if (__all(m ? (v >> (all ? 62 : 63)) == 0 : (int)v >= (lane == 1 ? 31 : lane == 2 ? n0 : n1))) break;
    __builtin_amdgcn_s_sleep(4);
    if (dead || poll_fail(spins, abortw, lane)) { dead = true; v = 0; break; }
  }
  return (Sel){(unsigned)__builtin_amdgcn_readfirstlane((unsigned)v), (unsigned)__builtin_amdgcn_readfirstlane((unsigned)(v >> 32))};
}
__device__ __forceinline__ __amdgpu_buffer_rsrc_t rsrc_u(char* sync) { return mk_rsrc((void*)sync, WS_USED); }
__device__ __forceinline__ unsigned far_blk(int n, Sel sel, int x = 0) { return n * CHK + ((~sel[0] ^ (n >> 7) ^ x) & 1u) * 8192; }
__device__ __forceinline__ constexpr unsigned quarter(int q) { return (q >> 1) * CHK + (q & 1) * 4096; }
__device__ __forceinline__ unsigned hot_q(int q, Sel sel) { return quarter(q ^ ((sel[1] >> 4) & 3u)); }
__device__ __forceinline__ unsigned copy_q(int g, int q, Sel sel) { return g * 16 * CHK + hot_q(q ^ ((g + 1) & 2) ^ ((g + 1) >> 2), sel); }
__device__ __forceinline__ unsigned um_at(int s, unsigned o, Sel sel) { return ((s & 3) * (64 * CHK) + o) ^ (((sel[1] >> (8 + 4 * (s & 3))) & 7u) << 12); }
__device__ __forceinline__ constexpr unsigned um_place(int ci, int g) { return (ci >> 3) * 16 * CHK + (ci & 1) * 8 * CHK + g * 2 * CHK + quarter(((ci >> 1) & 3) ^ ((ci >> 3) & 2)); }
constexpr unsigned UXH = 8 * CHK;
__device__ __forceinline__ unsigned ux_base(int s, int h, Sel sel) { return ((s & 15) * 32 + h) * UXH + ((~sel[0] >> (2 * (s & 15) + (h >> 4))) & 1u) * 8192; }
__device__ __forceinline__ constexpr unsigned ux_tile(int t) { return (t >> 3) * CHK + (t & 7) * 1024; }
__device__ __forceinline__ __amdgpu_buffer_rsrc_t rsrc_hot(char* sync, Sel sel) {
  return mk_rsrc((void*)(sync + SY_HOT + (sel[1] & 1u) * 8192), H_BYTES - 8192);
}
template <int SLP = 4>
__device__ __forceinline__ void ctr_wait(GAS unsigned* c, unsigned need, GAS unsigned* abortw, int lane, bool& dead) {
  unsigned spins = 0;
  while (true) {
    unsigned v = __builtin_amdgcn_readfirstlane(__hip_atomic_load(c, __ATOMIC_RELAXED, __HIP_MEMORY_SCOPE_AGENT)) + 1u;
    if (v >= need) break;
    __builtin_amdgcn_s_sleep(SLP);
    if (dead || poll_fail(spins, abortw, lane)) { dead = true; break; }
  }
}
template <int N> __device__ __forceinline__ void wait_vm() { asm volatile("s_waitcnt vmcnt(%0)" :: "n"(N) : "memory"); }
__device__ __forceinline__ void lds_barrier() {
  asm volatile("s_waitcnt lgkmcnt(0)" ::: "memory");
  __builtin_amdgcn_s_barrier();
  asm volatile("" ::: "memory");
}
__device__ __forceinline__ f32x4 vzero() { f32x4 z = {0.f, 0.f, 0.f, 0.f}; asm volatile("" : "+v"(z)); return z; }
constexpr bool blk_in_lds(int R, int b) {
  return R == 0 ? ((b & 7) == 1 || (b & 7) == 4) : (b < 16 ? (b & 3) == 0 : (b & 3) == 3);
}
constexpr int lds_index(int R, int b) { int n = 0; for (int i = 0; i < b; ++i) if (blk_in_lds(R, i)) ++n; return n; }
constexpr bool frag_in_lds(int R, int f) { return blk_in_lds(R, f >> 1); }
constexpr int frag_reg_slot(int R, int f) { int n = 0; for (int i = 0; i < f; ++i) if (!frag_in_lds(R, i)) ++n; return n; }

struct WSched { int ord[32]; int slot[32]; int issue[32]; int out[32]; int reg[32]; };
constexpr WSched make_wsched(int R) {
  WSched S{};
  int n = 0;
  for (int b = 0; b < 32; ++b) if (!blk_in_lds(R, b)) S.ord[n++] = b;
  for (int i = 0; i < n; ++i) S.reg[i] = frag_reg_slot(R, 2 * S.ord[i]);
  const int npure = n;
  for (int b = 0; b < 32; ++b) if (blk_in_lds(R, b)) S.ord[n++] = b;
  int seq[32] = {};
  int nissued = 0, next = 0;
  for (int i = 0; i < 10; ++i) { S.slot[i] = (i + 10 - npure % 10) % 10; S.issue[i] = -1; seq[i] = nissued++; }
  next = 10;
  S.out[0] = nissued - 1;
  for (int c = 0; c <= npure; ++c) {
    if (c >= 1) {
      const int k = S.slot[c - 1];
      if (next < npure) { S.slot[next] = k; S.issue[next] = c; seq[next] = nissued++; ++next; }
      else if (k < NLF / 2) { S.slot[npure + k] = k; S.issue[npure + k] = c; seq[npure + k] = nissued++; }
    }
    if (c + 1 < npure) S.out[c + 1] = nissued - 1 - seq[c + 1];
  }
  return S;
}
__device__ __forceinline__ u32x8 tr_frag(const char* sb, int tile, int lane) {
  u32x8 f;
#pragma unroll
  for (int q = 0; q < 4; ++q) {
    int row = (q >> 1) * 32 + 8 * (lane >> 4) + 4 * (q & 1) + ((lane & 15) >> 2);
    int p = lane & 3;
    int lc = 2 * tile + (p >> 1);
    int addr = row * 256 + ((lc ^ swz_b(row)) * 16) + 8 * (p & 1);
    bf16x4 v = __builtin_amdgcn_ds_read_tr16_b64_v4i16((LAS bf16x4*)(sb + addr));
    u32x2 u = __builtin_bit_cast(u32x2, v);
    f[2 * q] = u[0]; f[2 * q + 1] = u[1];
  }
  return f;
}
__device__ __forceinline__ void dma16(__amdgpu_buffer_rsrc_t rs, unsigned voff, unsigned soff, unsigned lds, bool nt = false, bool pad = true) {
  if (nt) asm volatile("s_mov_b32 m0, %0\n\ts_nop 3\n\tbuffer_load_dwordx4 %1, %2, %3 offen nt lds" :: "s"(lds), "v"(voff), "s"(rs), "s"(soff) : "m0", "memory");
  else if (pad) asm volatile("s_mov_b32 m0, %0\n\ts_nop 3\n\tbuffer_load_dwordx4 %1, %2, %3 offen lds" :: "s"(lds), "v"(voff), "s"(rs), "s"(soff) : "m0", "memory");
  else asm volatile("s_mov_b32 m0, %0\n\ts_nop 0\n\tbuffer_load_dwordx4 %1, %2, %3 offen lds" :: "s"(lds), "v"(voff), "s"(rs), "s"(soff) : "m0", "memory");
}
__device__ __forceinline__ u32x8 tr_frag2(const char* sb, int w, int f, int lane) {
  u32x8 r;
#pragma unroll
  for (int c = 0; c < 2; ++c)
#pragma unroll
    for (int t = 0; t < 2; ++t) {
      const int row = 32 * (w >> 1) + 16 * c + 4 * (lane >> 4) + ((lane & 15) >> 2);
      const int p = lane & 3, lc = 2 * (4 * (w & 1) + 2 * t + f) + (p >> 1);
      const u32x2 u = __builtin_bit_cast(u32x2, __builtin_amdgcn_ds_read_tr16_b64_v4i16((LAS bf16x4*)(sb + row * 256 + ((lc ^ swz_b(row)) * 16) + 8 * (p & 1))));
      r[4 * c + t] = u[0]; r[4 * c + 2 + t] = u[1];
    }
  return r;
}
template <int ROLE>
__device__ __forceinline__ void load_weights(const ChainArgs& a, int h, int w, int lane, char* smem, Wts& W) {
  u32x8 F[4];
#pragma unroll
  for (int i = 0; i < 32; ++i) asm volatile("" : "=a"(W.a[i]));
  constexpr WSched S = make_wsched(ROLE);
  constexpr int NP = 32 - NLF / 2;
  const __amdgpu_buffer_rsrc_t r0 = mk_rsrc(ROLE == 0 ? a.Wq : a.Wv, 8u << 20), r1 = mk_rsrc(ROLE == 0 ? a.Wk : a.Wo, 8u << 20);
  const unsigned lds0 = (unsigned)(uintptr_t)(LAS char*)smem;
  unsigned vo[2][4];
#pragma unroll
  for (int j = 0; j < 4; ++j) {
    const int row = (w * 4 + j) * 4 + (lane >> 4), lc = (lane & 15) ^ swz_b(row);
    vo[0][j] = row * HD * 2 + lc * 16; vo[1][j] = ((row & 15) + 2 * (row & 16)) * DM * 2 + (row >> 5) * 256 + lc * 16;
  }
  asm volatile("s_nop 4");
  auto stage = [&](int i) {
    const int blk = S.ord[i];
    const bool second = ROLE == 0 ? (blk & 1) : blk >= 16, wo = ROLE == 1 && second;
    const unsigned so = wo ? (unsigned)(((128 * h + 32 * (blk & 2) + 16 * (blk & 1)) * DM + 256 * (((blk - 16) >> 2) ^ (h & 3))) * 2)
                           : (unsigned)((64 * ((ROLE == 0 ? blk >> 1 : blk) ^ RHO(h)) * HD + 128 * h) * 2);
#pragma unroll
    for (int j = 0; j < 4; ++j) dma16(second ? r1 : r0, vo[wo][j], so, lds0 + 16384 * S.slot[i] + (w * 4 + j) * 1024, (NTW >> (wo ? 1 : 0)) & 1);
  };
#pragma unroll
  for (int i = 0; i < 32; ++i) if (S.issue[i] == -1) stage(i);
#pragma unroll
  for (int c = 0; c < NP; ++c) {
    asm volatile("s_waitcnt vmcnt(%0)" :: "n"(4 * S.out[c]) : "memory");
    lds_barrier();
#pragma unroll
    for (int i = 0; i < 32; ++i) if (S.issue[i] == c) stage(i);
    const char* sb = smem + 16384 * S.slot[c];
#pragma unroll
    for (int t = 0; t < 2; ++t) {
      u32x8 f = (ROLE == 1 && S.ord[c] >= 16) ? tr_frag2(sb, w, t, lane) : tr_frag(sb, 2 * w + t, lane);
      if (S.reg[c] < 32) {
#pragma unroll
        for (int i = 0; i < 8; ++i) asm volatile("v_accvgpr_write_b32 a[%1], %0" :: "v"(f[i]), "n"(8 * ((S.reg[c] + t) & 31) + i));
      }
      F[(S.reg[c] + t) & 3] = f;
    }
    if (S.reg[c] >= 32 && (S.reg[c] & 2)) {
      const u32x16 lo = __builtin_shufflevector(F[0], F[1], 0, 1, 2, 3, 4, 5, 6, 7, 8, 9, 10, 11, 12, 13, 14, 15);
      const u32x16 hi = __builtin_shufflevector(F[2], F[3], 0, 1, 2, 3, 4, 5, 6, 7, 8, 9, 10, 11, 12, 13, 14, 15);
      u32x32 x = __builtin_shufflevector(lo, hi, 0, 1, 2, 3, 4, 5, 6, 7, 8, 9, 10, 11, 12, 13, 14, 15, 16, 17, 18, 19, 20, 21, 22, 23, 24, 25, 26, 27, 28, 29, 30, 31);
      asm volatile("" : "+v"(x));
      W.v[(S.reg[c] >> 2) & 3] = x;
    }
  }
  static_assert(S.issue[31] < NP && S.issue[NP] < S.issue[31], "resident blocks are issued in slot order inside the loop");
#pragma unroll
  for (int j = 0; j < NLF / 2; ++j) {
    asm volatile("s_waitcnt vmcnt(%0)" :: "n"(4 * (NLF / 2 - 1 - j)) : "memory");
    lds_barrier();
    const bool two = ROLE == 1 && S.ord[NP + j] >= 16;
    u32x8 f0 = two ? tr_frag2(smem + 16384 * j, w, 0, lane) : tr_frag(smem + 16384 * j, 2 * w, lane);
    u32x8 f1 = two ? tr_frag2(smem + 16384 * j, w, 1, lane) : tr_frag(smem + 16384 * j, 2 * w + 1, lane);
    lds_barrier();
    char* d = smem + 16384 * j + w * 4096 + lane * 16;
    *(u32x4*)d = (u32x4){f0[0], f0[1], f0[2], f0[3]};
    *(u32x4*)(d + 1024) = (u32x4){f0[4], f0[5], f0[6], f0[7]};
    *(u32x4*)(d + 2048) = (u32x4){f1[0], f1[1], f1[2], f1[3]};
    *(u32x4*)(d + 3072) = (u32x4){f1[4], f1[5], f1[6], f1[7]};
  }
  lds_barrier();
}
#define SB0 __builtin_amdgcn_sched_barrier(0)
#define QK_BODY \
  "s_cmp_eq_u32 %[wv], 1\n\t" \
  "s_cbranch_scc1 91f\n\t" \
  "s_cmp_eq_u32 %[wv], 2\n\t" \
  "s_cbranch_scc1 92f\n\t" \
  "s_cmp_eq_u32 %[wv], 3\n\t" \
  "s_cbranch_scc1 93f\n\t" \
  "90:\n\t" \
  "s_mov_b32 s58, s42\n\t" \
  ".p2align 6\n\t" \
  "20:\n\t" \
  "s_and_b32 s76, s44, 7\n\t" \
  "s_lshl_b32 s76, s76, 15\n\t" \
  "s_add_i32 s40, s54, s76\n\t" \
  "s_and_b32 s76, s44, 3\n\t" \
  "s_lshl_b32 s45, s76, 16\n\t" \
  "s_add_i32 s47, s45, s55\n\t" \
  "s_add_i32 s49, s44, -1\n\t" \
  "s_and_b32 s48, s49, 3\n\t" \
  "s_lshl_b32 s48, s48, 16\n\t" \
  "s_lshl_b32 s50, s53, 8\n\t" \
  "s_mov_b32 s59, 0\n\t" \
  "s_mov_b32 s92, s51\n\t" \
  "s_mov_b32 s93, s53\n\t" \
  "s_xor_b32 s76, s40, 0x1000\n\t" \
  "s_xor_b32 s77, s40, 0x4000\n\t" \
  "s_xor_b32 s78, s40, 0x5000\n\t" \
  "s_cmp_lg_u32 s58, 0\n\t" \
  "s_cbranch_scc1 11f\n\t" \
  "10:\n\t" \
  "ds_read_b128 v[48:51], v102 offset:0\n\t" \
  "ds_read_b128 v[52:55], v102 offset:1024\n\t" \
  "ds_read_b128 v[56:59], v102 offset:2048\n\t" \
  "ds_read_b128 v[60:63], v102 offset:3072\n\t" \
  "ds_read_b128 v[16:19], v102 offset:16384\n\t" \
  "ds_read_b128 v[20:23], v102 offset:17408\n\t" \
  "ds_read_b128 v[24:27], v102 offset:18432\n\t" \
  "ds_read_b128 v[28:31], v102 offset:19456\n\t" \
  "ds_read_b128 v[84:87], v102 offset:49152\n\t" \
  "ds_read_b128 v[88:91], v102 offset:50176\n\t" \
  "ds_read_b128 v[92:95], v102 offset:51200\n\t" \
  "ds_read_b128 v[96:99], v102 offset:52224\n\t" \
  "s_waitcnt lgkmcnt(4)\n\t" \
  "ds_read_b128 v[0:3], v103 offset:16384\n\t" \
  "ds_read_b128 v[4:7], v103 offset:17408\n\t" \
  "ds_read_b128 v[8:11], v103 offset:18432\n\t" \
  "ds_read_b128 v[12:15], v103 offset:19456\n\t" \
  "s_bitcmp1_b32 s58, 1\n\t" \
  "s_cselect_b64 s[80:81], -1, 0\n\t" \
  "s_cmp_lg_u32 s58, 0\n\t" \
  "s_cbranch_scc1 17f\n\t" \
  "s_mov_b32 exec_hi, 0\n\t" \
  "16:\n\t" \
  "s_memtime vcc\n\t" \
  "s_waitcnt lgkmcnt(0)\n\t" \
  "s_sub_i32 s79, s41, vcc_lo\n\t" \
  "s_cmp_gt_i32 s79, 64\n\t" \
  "s_cbranch_scc1 16b\n\t" \
  "13:\n\t" \
  "buffer_load_dword v116, v110, %[rh], s48 offen sc1\n\t" \
  "s_waitcnt vmcnt(0)\n\t" \
  "15:\n\t" \
  "v_mov_b32 v117, 20000\n\t" \
  "v_cmp_ne_u32_sdwa vcc, v116, s49 src0_sel:BYTE_0 src1_sel:DWORD\n\t" \
  "v_subrev_u32 v116, s50, v116\n\t" \
  "v_ashrrev_i32 v116, 8, v116\n\t" \
  "s_nop 1\n\t" \
  "v_max_i32_dpp v116, v116, v116 quad_perm:[1,0,3,2] row_mask:0xf bank_mask:0xf\n\t" \
  "s_cbranch_vccnz 14f\n\t" \
  "s_nop 0\n\t" \
  "v_max_i32_dpp v116, v116, v116 quad_perm:[2,3,0,1] row_mask:0xf bank_mask:0xf\n\t" \
  "s_nop 1\n\t" \
  "v_max_i32_dpp v116, v116, v116 row_half_mirror row_mask:0xf bank_mask:0xf\n\t" \
  "s_nop 1\n\t" \
  "v_max_i32_dpp v116, v116, v116 row_mirror row_mask:0xf bank_mask:0xf\n\t" \
  "v_med3_i32 v116, v116, 0, v117\n\t" \
  "v_mul_u32_u24 v116, s52, v116\n\t" \
  "v_lshrrev_b32 v116, 16, v116\n\t" \
  "s_mov_b64 exec, -1\n\t" \
  "v_readlane_b32 s84, v116, 0\n\t" \
  "v_readlane_b32 s85, v116, 16\n\t" \
  "s_max_u32 s84, s84, s85\n\t" \
  "s_add_i32 s84, s84, s51\n\t" \
  "v_mov_b64 v[68:69], 0\n\t" \
  "v_mov_b64 v[70:71], 0\n\t" \
  "v_mov_b64 v[72:73], 0\n\t" \
  "v_mov_b64 v[74:75], 0\n\t" \
  "v_mov_b64 v[76:77], 0\n\t" \
  "v_mov_b64 v[78:79], 0\n\t" \
  "v_mov_b64 v[80:81], 0\n\t" \
  "v_mov_b64 v[82:83], 0\n\t" \
  "1:\n\t" \
  "s_memtime vcc\n\t" \
  "s_waitcnt lgkmcnt(0)\n\t" \
  "s_sub_i32 s79, s84, vcc_lo\n\t" \
  "s_cmp_gt_i32 s79, 128\n\t" \
  "s_cbranch_scc1 1b\n\t" \
  "2:\n\t" \
  "s_sub_i32 s79, s79, 33\n\t" \
  "s_cmp_gt_i32 s79, 56\n\t" \
  "s_cbranch_scc1 2b\n\t" \
  "buffer_load_dwordx4 v[32:35], v100, %[rh], s40 offen sc1\n\t" \
  "s_nop 12\n\t" \
  "buffer_load_dwordx4 v[36:39], v100, %[rh], s76 offen sc1\n\t" \
  "s_nop 12\n\t" \
  "buffer_load_dwordx4 v[40:43], v100, %[rh], s77 offen sc1\n\t" \
  "s_nop 12\n\t" \
  "buffer_load_dwordx4 v[44:47], v100, %[rh], s78 offen sc1\n\t" \
  "s_memtime vcc\n\t" \
  "s_memrealtime s[82:83]\n\t" \
  "s_nop 4\n\t" \
  "s_memrealtime s[84:85]\n\t" \
  "s_nop 4\n\t" \
  "s_memrealtime s[94:95]\n\t" \
  "s_nop 4\n\t" \
  "s_memrealtime s[86:87]\n\t" \
  "s_nop 4\n\t" \
  "s_memrealtime s[88:89]\n\t" \
  "s_nop 4\n\t" \
  "s_memrealtime s[90:91]\n\t" \
  "s_mov_b32 s92, 0\n\t" \
  "s_waitcnt lgkmcnt(0)\n\t" \
  "s_cmp_eq_u32 s82, s84\n\t" \
  "s_addc_u32 s92, s92, 0\n\t" \
  "s_cmp_eq_u32 s82, s94\n\t" \
  "s_addc_u32 s92, s92, 0\n\t" \
  "s_cmp_eq_u32 s82, s86\n\t" \
  "s_addc_u32 s92, s92, 0\n\t" \
  "s_cmp_eq_u32 s82, s88\n\t" \
  "s_addc_u32 s92, s92, 0\n\t" \
  "s_cmp_eq_u32 s82, s90\n\t" \
  "s_addc_u32 s92, s92, 0\n\t" \
  "s_mul_i32 s92, s92, 24\n\t" \
  "s_add_i32 s92, s92, vcc_lo\n\t" \
  "s_lshl4_add_u32 s93, s82, 64\n\t" \
  "3:\n\t" \
  "s_waitcnt lgkmcnt(0)\n\t" \
  "s_waitcnt vmcnt(3)\n\t" \
  "v_smfmac_f32_16x16x64_bf16 v[68:71], v[32:35], a[0:7], v104\n\t" \
  "ds_write_b128 v101, v[32:35] offset:0\n\t" \
  "v_smfmac_f32_16x16x64_bf16 v[72:75], v[32:35], a[8:15], v104\n\t" \
  "v_smfmac_f32_16x16x64_bf16 v[76:79], v[32:35], v[48:55], v104\n\t" \
  "v_smfmac_f32_16x16x64_bf16 v[80:83], v[32:35], v[56:63], v104\n\t" \
  "s_waitcnt vmcnt(2)\n\t" \
  "v_smfmac_f32_16x16x64_bf16 v[68:71], v[36:39], a[16:23], v104\n\t" \
  "ds_write_b128 v101, v[36:39] offset:1024\n\t" \
  "v_smfmac_f32_16x16x64_bf16 v[72:75], v[36:39], a[24:31], v104\n\t" \
  "v_smfmac_f32_16x16x64_bf16 v[76:79], v[36:39], a[32:39], v104\n\t" \
  "v_cmp_u_f32 vcc, v68, v69\n\t" \
  "v_smfmac_f32_16x16x64_bf16 v[80:83], v[36:39], a[40:47], v104\n\t" \
  "v_cmp_u_f32 s[82:83], v70, v71\n\t" \
  "s_waitcnt vmcnt(1)\n\t" \
  "v_smfmac_f32_16x16x64_bf16 v[68:71], v[40:43], v[16:23], v104\n\t" \
  "ds_write_b128 v101, v[40:43] offset:2048\n\t" \
  "v_smfmac_f32_16x16x64_bf16 v[72:75], v[40:43], v[24:31], v104\n\t" \
  "s_or_b64 vcc, vcc, s[82:83]\n\t" \
  "s_or_b64 vcc, vcc, s[80:81]\n\t" \
  "s_waitcnt lgkmcnt(1)\n\t" \
  "v_smfmac_f32_16x16x64_bf16 v[76:79], v[40:43], a[48:55], v104\n\t" \
  "s_cbranch_scc1 8f\n\t" \
  "5:\n\t" \
  "s_barrier\n\t" \
  "4:\n\t" \
  "s_memtime s[56:57]\n\t" \
  "ds_read_b128 v[48:51], v101 offset:4096\n\t" \
  "v_smfmac_f32_16x16x64_bf16 v[80:83], v[40:43], a[56:63], v104\n\t" \
  "ds_read_b128 v[52:55], v101 offset:5120\n\t" \
  "ds_read_b128 v[56:59], v101 offset:8192\n\t" \
  "s_waitcnt vmcnt(0)\n\t" \
  "v_smfmac_f32_16x16x64_bf16 v[68:71], v[44:47], a[64:71], v104\n\t" \
  "ds_write_b128 v101, v[44:47] offset:3072\n\t" \
  "v_smfmac_f32_16x16x64_bf16 v[72:75], v[44:47], a[72:79], v104\n\t" \
  "v_smfmac_f32_16x16x64_bf16 v[76:79], v[44:47], a[80:87], v104\n\t" \
  "v_cmp_u_f32 vcc, v68, v69\n\t" \
  "v_smfmac_f32_16x16x64_bf16 v[80:83], v[44:47], a[88:95], v104\n\t" \
  "v_cmp_u_f32 s[82:83], v70, v71\n\t" \
  "s_waitcnt lgkmcnt(3)\n\t" \
  "v_smfmac_f32_16x16x64_bf16 v[68:71], v[48:51], a[96:103], v104\n\t" \
  "ds_read_b128 v[60:63], v101 offset:9216\n\t" \
  "ds_read_b128 v[64:67], v101 offset:12288\n\t" \
  "v_smfmac_f32_16x16x64_bf16 v[72:75], v[48:51], a[104:111], v104\n\t" \
  "s_or_b64 vcc, vcc, s[82:83]\n\t" \
  "s_waitcnt lgkmcnt(2)\n\t" \
  "v_smfmac_f32_16x16x64_bf16 v[68:71], v[52:55], a[112:119], v104\n\t" \
  "s_cbranch_scc1 9f\n\t" \
  "6:\n\t" \
  "s_barrier\n\t" \
  "v_smfmac_f32_16x16x64_bf16 v[72:75], v[52:55], a[120:127], v104\n\t" \
  "ds_read_b128 v[48:51], v101 offset:13312\n\t" \
  "v_smfmac_f32_16x16x64_bf16 v[68:71], v[56:59], a[192:199], v104\n\t" \
  "ds_read_b128 v[52:55], v101 offset:6144\n\t" \
  "v_smfmac_f32_16x16x64_bf16 v[72:75], v[56:59], a[200:207], v104\n\t" \
  "s_xor_b32 s94, s47, 0x5000\n\t" \
  "s_waitcnt lgkmcnt(3)\n\t" \
  "v_smfmac_f32_16x16x64_bf16 v[68:71], v[60:63], a[208:215], v104\n\t" \
  "ds_read_b128 v[56:59], v101 offset:7168\n\t" \
  "v_smfmac_f32_16x16x64_bf16 v[72:75], v[60:63], a[216:223], v104\n\t" \
  "s_xor_b32 s95, s47, 0x4000\n\t" \
  "s_waitcnt lgkmcnt(3)\n\t" \
  "v_smfmac_f32_16x16x64_bf16 v[68:71], v[64:67], v[152:159], v104\n\t" \
  "ds_read_b128 v[60:63], v101 offset:10240\n\t" \
  "ds_read_b128 v[32:35], v119 offset:0\n\t" \
  "v_smfmac_f32_16x16x64_bf16 v[72:75], v[64:67], v[160:167], v104\n\t" \
  "ds_read_b128 v[16:19], v103 offset:49152\n\t" \
  "s_waitcnt lgkmcnt(5)\n\t" \
  "v_smfmac_f32_16x16x64_bf16 v[68:71], v[48:51], v[168:175], v104\n\t" \
  "ds_read_b128 v[64:67], v101 offset:11264\n\t" \
  "ds_read_b128 v[36:39], v119 offset:1024\n\t" \
  "v_smfmac_f32_16x16x64_bf16 v[72:75], v[48:51], v[176:183], v104\n\t" \
  "ds_read_b128 v[20:23], v103 offset:50176\n\t" \
  "s_waitcnt lgkmcnt(7)\n\t" \
  "v_smfmac_f32_16x16x64_bf16 v[68:71], v[52:55], v[84:91], v104\n\t" \
  "ds_read_b128 v[48:51], v101 offset:15360\n\t" \
  "ds_read_b128 v[40:43], v119 offset:16\n\t" \
  "v_smfmac_f32_16x16x64_bf16 v[72:75], v[52:55], v[92:99], v104\n\t" \
  "ds_read_b128 v[24:27], v103 offset:51200\n\t" \
  "s_waitcnt lgkmcnt(9)\n\t" \
  "v_smfmac_f32_16x16x64_bf16 v[68:71], v[56:59], a[160:167], v104\n\t" \
  "ds_read_b128 v[52:55], v101 offset:14336\n\t" \
  "ds_read_b128 v[44:47], v119 offset:1040\n\t" \
  "v_smfmac_f32_16x16x64_bf16 v[72:75], v[56:59], a[168:175], v104\n\t" \
  "ds_read_b128 v[28:31], v103 offset:52224\n\t" \
  "s_waitcnt lgkmcnt(11)\n\t" \
  "v_smfmac_f32_16x16x64_bf16 v[68:71], v[60:63], v[0:7], v104\n\t" \
  "ds_read_b128 v[92:95], v119 offset:2048\n\t" \
  "v_smfmac_f32_16x16x64_bf16 v[72:75], v[60:63], v[8:15], v104\n\t" \
  "ds_read_b128 v[96:99], v119 offset:3072\n\t" \
  "s_waitcnt lgkmcnt(10)\n\t" \
  "v_smfmac_f32_16x16x64_bf16 v[68:71], v[64:67], v[120:127], v104\n\t" \
  "ds_read_b128 v[0:3], v102 offset:32768\n\t" \
  "v_smfmac_f32_16x16x64_bf16 v[72:75], v[64:67], v[128:135], v104\n\t" \
  "ds_read_b128 v[4:7], v102 offset:33792\n\t" \
  "s_waitcnt lgkmcnt(9)\n\t" \
  "v_smfmac_f32_16x16x64_bf16 v[68:71], v[48:51], v[216:223], v104\n\t" \
  "ds_read_b128 v[8:11], v102 offset:34816\n\t" \
  "s_waitcnt lgkmcnt(7)\n\t" \
  "v_smfmac_f32_16x16x64_bf16 v[68:71], v[52:55], v[16:23], v104\n\t" \
  "ds_read_b128 v[12:15], v102 offset:35840\n\t" \
  "v_smfmac_f32_16x16x64_bf16 v[72:75], v[48:51], v[224:231], v104\n\t" \
  "ds_read_b128 v[16:19], v119 offset:2064\n\t" \
  "ds_read_b128 v[20:23], v119 offset:3088\n\t" \
  "s_waitcnt lgkmcnt(8)\n\t" \
  "v_smfmac_f32_16x16x64_bf16 v[72:75], v[52:55], v[24:31], v104\n\t" \
  "v_add_f32 v116, v68, v70\n\t" \
  "v_add_f32 v117, v69, v71\n\t" \
  "v_smfmac_f32_16x16x64_bf16 v[76:79], v[56:59], a[176:183], v104\n\t" \
  "v_cvt_pk_bf16_f32 v116, v116, v117\n\t" \
  "ds_read_b128 v[24:27], v103 offset:2048\n\t" \
  "v_smfmac_f32_16x16x64_bf16 v[80:83], v[56:59], a[184:191], v104\n\t" \
  "v_add_f32 v117, v72, v74\n\t" \
  "v_add_f32 v118, v73, v75\n\t" \
  "v_cvt_pk_bf16_f32 v117, v117, v118\n\t" \
  "ds_write2st64_b32 v105, v116, v117 offset1:1\n\t" \
  "v_smfmac_f32_16x16x64_bf16 v[76:79], v[60:63], a[240:247], v104\n\t" \
  "v_mov_b64 v[68:69], 0\n\t" \
  "s_xor_b32 s96, s47, 0x1000\n\t" \
  "v_smfmac_f32_16x16x64_bf16 v[80:83], v[60:63], a[248:255], v104\n\t" \
  "v_mov_b64 v[70:71], 0\n\t" \
  "v_smfmac_f32_16x16x64_bf16 v[76:79], v[64:67], v[136:143], v104\n\t" \
  "v_mov_b64 v[72:73], 0\n\t" \
  "v_smfmac_f32_16x16x64_bf16 v[80:83], v[64:67], v[144:151], v104\n\t" \
  "s_waitcnt lgkmcnt(0)\n\t" \
  "s_barrier\n\t" \
  "ds_read_b64_tr_b16 v[84:85], v107 offset:0\n\t" \
  "ds_read_b64_tr_b16 v[86:87], v107 offset:128\n\t" \
  "v_smfmac_f32_16x16x64_bf16 v[76:79], v[48:51], v[232:239], v104\n\t" \
  "ds_read_b64_tr_b16 v[88:89], v107 offset:1024\n\t" \
  "ds_read_b64_tr_b16 v[90:91], v107 offset:1152\n\t" \
  "v_smfmac_f32_16x16x64_bf16 v[80:83], v[48:51], v[240:247], v104\n\t" \
  "ds_read_b128 v[56:59], v101 offset:4096\n\t" \
  "v_mov_b64 v[74:75], 0\n\t" \
  "v_smfmac_f32_16x16x64_bf16 v[76:79], v[52:55], v[200:207], v104\n\t" \
  "ds_read_b128 v[60:63], v101 offset:13312\n\t" \
  "ds_read_b128 v[28:31], v103 offset:3072\n\t" \
  "v_smfmac_f32_16x16x64_bf16 v[80:83], v[52:55], v[208:215], v104\n\t" \
  "s_waitcnt lgkmcnt(5)\n\t" \
  "v_smfmac_f32_16x16x64_bf16 v[68:71], v[84:87], v[32:39], v104\n\t" \
  "v_smfmac_f32_16x16x64_bf16 v[72:75], v[84:87], v[40:47], v104\n\t" \
  "ds_read_b128 v[64:67], v101 offset:6144\n\t" \
  "s_waitcnt lgkmcnt(4)\n\t" \
  "v_smfmac_f32_16x16x64_bf16 v[68:71], v[88:91], v[92:99], v104\n\t" \
  "v_smfmac_f32_16x16x64_bf16 v[72:75], v[88:91], v[16:23], v104\n\t" \
  "ds_read_b128 v[48:51], v101 offset:8192\n\t" \
  "s_waitcnt lgkmcnt(4)\n\t" \
  "v_smfmac_f32_16x16x64_bf16 v[76:79], v[56:59], v[0:7], v104\n\t" \
  "ds_read_b128 v[16:19], v103 offset:0\n\t" \
  "v_add_f32 v40, v68, v70\n\t" \
  "v_smfmac_f32_16x16x64_bf16 v[80:83], v[56:59], v[8:15], v104\n\t" \
  "v_add_f32 v41, v72, v74\n\t" \
  "v_max_f32 v42, v40, v41\n\t" \
  "s_waitcnt lgkmcnt(4)\n\t" \
  "v_smfmac_f32_16x16x64_bf16 v[76:79], v[60:63], v[184:191], v104\n\t" \
  "ds_read_b128 v[20:23], v103 offset:1024\n\t" \
  "v_max_f32_dpp v42, v42, v42 quad_perm:[1,0,3,2] row_mask:0xf bank_mask:0xf\n\t" \
  "v_smfmac_f32_16x16x64_bf16 v[80:83], v[60:63], v[192:199], v104\n\t" \
  "ds_read_b128 v[52:55], v101 offset:5120\n\t" \
  "v_max_f32_dpp v42, v42, v42 quad_perm:[2,3,0,1] row_mask:0xf bank_mask:0xf\n\t" \
  "s_waitcnt lgkmcnt(4)\n\t" \
  "v_smfmac_f32_16x16x64_bf16 v[76:79], v[64:67], a[144:151], v104\n\t" \
  "ds_read_b128 v[56:59], v101 offset:9216\n\t" \
  "v_max_f32_dpp v42, v42, v42 row_half_mirror row_mask:0xf bank_mask:0xf\n\t" \
  "v_smfmac_f32_16x16x64_bf16 v[80:83], v[64:67], a[152:159], v104\n\t" \
  "ds_read_b128 v[60:63], v101 offset:12288\n\t" \
  "v_max_f32_dpp v42, v42, v42 row_mirror row_mask:0xf bank_mask:0xf\n\t" \
  "v_mul_f32 v42, %[cs], v42\n\t" \
  "s_waitcnt lgkmcnt(3)\n\t" \
  "v_smfmac_f32_16x16x64_bf16 v[76:79], v[48:51], v[16:23], v104\n\t" \
  "ds_read_b128 v[0:3], v103 offset:32768\n\t" \
  "v_fma_f32 v40, v40, %[cs], -v42\n\t" \
  "v_fma_f32 v41, v41, %[cs], -v42\n\t" \
  "v_smfmac_f32_16x16x64_bf16 v[80:83], v[48:51], v[24:31], v104\n\t" \
  "ds_read_b128 v[4:7], v103 offset:33792\n\t" \
  "v_exp_f32 v43, v40\n\t" \
  "v_exp_f32 v44, v41\n\t" \
  "s_waitcnt lgkmcnt(4)\n\t" \
  "v_smfmac_f32_16x16x64_bf16 v[76:79], v[52:55], a[128:135], v104\n\t" \
  "ds_read_b128 v[8:11], v103 offset:34816\n\t" \
  "v_cvt_pk_bf16_f32 v45, v43, v44\n\t" \
  "buffer_store_dword v45, v112, %[rh], s94 offen sc1\n\t" \
  "v_smfmac_f32_16x16x64_bf16 v[80:83], v[52:55], a[136:143], v104\n\t" \
  "ds_read_b128 v[12:15], v103 offset:35840\n\t" \
  "buffer_store_dword v45, v112, %[rh], s95 offen sc1\n\t" \
  "s_waitcnt lgkmcnt(5)\n\t" \
  "v_smfmac_f32_16x16x64_bf16 v[76:79], v[56:59], a[224:231], v104\n\t" \
  "buffer_store_dword v45, v112, %[rh], s96 offen sc1\n\t" \
  "s_waitcnt lgkmcnt(2)\n\t" \
  "v_smfmac_f32_16x16x64_bf16 v[76:79], v[60:63], v[0:7], v104\n\t" \
  "buffer_store_dword v45, v112, %[rh], s47 offen sc1\n\t" \
  "v_smfmac_f32_16x16x64_bf16 v[80:83], v[56:59], a[232:239], v104\n\t" \
  "s_waitcnt lgkmcnt(0)\n\t" \
  "v_smfmac_f32_16x16x64_bf16 v[80:83], v[60:63], v[8:15], v104\n\t" \
  "v_add_f32 v20, v76, v78\n\t" \
  "v_add_f32 v21, v77, v79\n\t" \
  "v_cvt_pk_bf16_f32 v20, v20, v21\n\t" \
  "ds_write_b32 v106, v20\n\t" \
  "v_add_f32 v92, v43, v44\n\t" \
  "v_mov_b32 v25, v115\n\t" \
  "s_nop 1\n\t" \
  "v_add_f32 v23, v80, v82\n\t" \
  "v_add_f32 v21, v81, v83\n\t" \
  "v_cvt_pk_bf16_f32 v21, v23, v21\n\t" \
  "ds_write_b32 v106, v21 offset:256\n\t" \
  "v_add_f32_dpp v92, v92, v92 row_ror:8 row_mask:0xf bank_mask:0xf\n\t" \
  "v_mov_b64 v[0:1], 0\n\t" \
  "v_mov_b64 v[2:3], 0\n\t" \
  "v_mov_b32 v27, v114\n\t" \
  "s_waitcnt lgkmcnt(0)\n\t" \
  "s_barrier\n\t" \
  "ds_read_b64_tr_b16 v[4:5], v108 offset:0\n\t" \
  "ds_read_b64_tr_b16 v[6:7], v108 offset:64\n\t" \
  "ds_read_b64_tr_b16 v[8:9], v108 offset:512\n\t" \
  "ds_read_b64_tr_b16 v[10:11], v108 offset:576\n\t" \
  "ds_read_b64_tr_b16 v[12:13], v108 offset:1024\n\t" \
  "ds_read_b64_tr_b16 v[14:15], v108 offset:1088\n\t" \
  "ds_read_b64_tr_b16 v[16:17], v108 offset:1536\n\t" \
  "ds_read_b64_tr_b16 v[18:19], v108 offset:1600\n\t" \
  "s_waitcnt lgkmcnt(4)\n\t" \
  "v_smfmac_f32_16x16x64_bf16 v[0:3], v[84:87], v[4:11], v104\n\t" \
  "s_waitcnt lgkmcnt(0)\n\t" \
  "v_smfmac_f32_16x16x64_bf16 v[0:3], v[88:91], v[12:19], v104\n\t" \
  "s_nop 7\n\t" \
  "v_add_f32 v96, v0, v2\n\t" \
  "v_fma_f32 v96, v96, %[cs], -v42\n\t" \
  "v_exp_f32 v24, v96\n\t" \
  "s_nop 0\n\t" \
  "v_add_f32 v29, v92, v24\n\t" \
  "s_nop 0\n\t" \
  "v_mov_b32_dpp v25, v24 quad_perm:[1,0,3,2] row_mask:0xf bank_mask:0x3\n\t" \
  "v_add_f32_dpp v29, v29, v29 quad_perm:[1,0,3,2] row_mask:0xf bank_mask:0xf\n\t" \
  "v_mov_b32_dpp v27, v24 quad_perm:[0,1,2,3] row_mask:0xf bank_mask:0x3\n\t" \
  "s_nop 0\n\t" \
  "v_add_f32_dpp v29, v29, v29 quad_perm:[2,3,0,1] row_mask:0xf bank_mask:0xf\n\t" \
  "s_nop 1\n\t" \
  "v_add_f32_dpp v29, v29, v29 row_half_mirror row_mask:0xf bank_mask:0xf\n\t" \
  "v_rcp_f32 v26, v29\n\t" \
  "v_cmpx_gt_f32 vcc, 0x5d5e0b6b, v29\n\t" \
  "v_mul_f32 v24, v27, v26\n\t" \
  "v_mul_f32 v25, v25, v26\n\t" \
  "v_cvt_pk_bf16_f32 v24, v24, v25\n\t" \
  "buffer_store_dword v24, v113, %[rh], s94 offen sc1\n\t" \
  "buffer_store_dword v24, v113, %[rh], s95 offen sc1\n\t" \
  "buffer_store_dword v24, v113, %[rh], s96 offen sc1\n\t" \
  "buffer_store_dword v24, v113, %[rh], s47 offen sc1\n\t" \
  "s_memtime s[76:77]\n\t" \
  "s_mov_b64 exec, -1\n\t" \
  "v_mov_b32 v97, v29\n\t" \
  "s_cmp_eq_u64 vcc, -1\n\t" \
  "s_cselect_b32 s42, 0, 8\n\t" \
  "s_waitcnt lgkmcnt(0)\n\t" \
  "s_mov_b32 s57, s76\n\t" \
  "s_add_i32 s41, s56, 1950\n\t" \
  "s_sub_i32 s76, s92, s51\n\t" \
  "s_sub_i32 s77, s93, s53\n\t" \
  "s_cmp_eq_u32 s76, 0\n\t" \
  "s_cbranch_scc1 21f\n\t" \
  "v_cvt_f32_u32 v117, s76\n\t" \
  "v_cvt_f32_u32 v116, s77\n\t" \
  "v_rcp_f32 v117, v117\n\t" \
  "s_mov_b32 s51, s92\n\t" \
  "v_mul_f32 v117, v117, v116\n\t" \
  "v_max_f32 v117, 0x3f028f5c, v117\n\t" \
  "v_min_f32 v117, 0x3ffeb852, v117\n\t" \
  "v_rcp_f32 v116, v117\n\t" \
  "v_mul_f32 v117, 0x4f000000, v117\n\t" \
  "v_mul_f32 v116, 0x47800000, v116\n\t" \
  "v_cvt_u32_f32 v117, v117\n\t" \
  "v_cvt_u32_f32 v116, v116\n\t" \
  "s_mov_b32 s53, s93\n\t" \
  "v_readfirstlane_b32 s46, v117\n\t" \
  "v_readfirstlane_b32 s52, v116\n\t" \
  "21:\n\t" \
  "s_add_i32 s44, s44, 1\n\t" \
  "s_and_b32 s58, s58, 4\n\t" \
  "s_and_b32 s76, s44, 64\n\t" \
  "s_or_b32 s42, s42, s76\n\t" \
  "s_cbranch_scc0 20b\n\t" \
  "s_branch 99f\n\t" \
  "14:\n\t" \
  "s_add_u32 s59, s59, 0x10000\n\t" \
  "s_cmp_lt_u32 s59, 0x100000\n\t" \
  "s_cbranch_scc1 13b\n\t" \
  "s_mov_b64 exec, -1\n\t" \
  "s_mov_b32 s42, 16\n\t" \
  "s_branch 99f\n\t" \
  "8:\n\t" \
  "s_bitcmp1_b32 s58, 1\n\t" \
  "s_cbranch_scc1 4b\n\t" \
  "s_bitcmp1_b32 s58, 2\n\t" \
  "s_cbranch_scc1 5b\n\t" \
  "v_max3_u32 v116, v32, v33, v34\n\t" \
  "v_max3_u32 v117, v35, v36, v37\n\t" \
  "v_max3_u32 v116, v116, v38, v39\n\t" \
  "v_max_u32 v116, v116, v117\n\t" \
  "v_cmp_eq_u32 vcc, -1, v116\n\t" \
  "s_cbranch_vccz 5b\n\t" \
  "s_branch 0f\n\t" \
  "9:\n\t" \
  "s_bitcmp1_b32 s58, 2\n\t" \
  "s_cbranch_scc1 6b\n\t" \
  "v_max3_u32 v116, v40, v41, v42\n\t" \
  "v_max3_u32 v117, v43, v44, v45\n\t" \
  "v_max3_u32 v116, v116, v46, v47\n\t" \
  "v_max_u32 v116, v116, v117\n\t" \
  "v_cmp_eq_u32 vcc, -1, v116\n\t" \
  "s_cbranch_vccz 6b\n\t" \
  "s_or_b32 s58, s58, 2\n\t" \
  "0:\n\t" \
  "s_or_b32 s58, s58, 1\n\t" \
  "11:\n\t" \
  "buffer_load_dwordx4 v[32:35], v100, %[rh], s40 offen sc1\n\t" \
  "buffer_load_dwordx4 v[36:39], v100, %[rh], s76 offen sc1\n\t" \
  "buffer_load_dwordx4 v[40:43], v100, %[rh], s77 offen sc1\n\t" \
  "buffer_load_dwordx4 v[44:47], v100, %[rh], s78 offen sc1\n\t" \
  "s_waitcnt lgkmcnt(0)\n\t" \
  "s_branch 10b\n\t" \
  "17:\n\t" \
  "v_mov_b64 v[68:69], 0\n\t" \
  "v_mov_b64 v[70:71], 0\n\t" \
  "v_mov_b64 v[72:73], 0\n\t" \
  "v_mov_b64 v[74:75], 0\n\t" \
  "v_mov_b64 v[76:77], 0\n\t" \
  "v_mov_b64 v[78:79], 0\n\t" \
  "v_mov_b64 v[80:81], 0\n\t" \
  "v_mov_b64 v[82:83], 0\n\t" \
  "s_add_u32 s59, s59, 1\n\t" \
  "s_bitcmp1_b32 s58, 2\n\t" \
  "s_cbranch_scc1 3b\n\t" \
  "s_waitcnt vmcnt(0)\n\t" \
  "v_max_u32 v116, v32, v33\n\t" \
  "v_max3_u32 v116, v116, v34, v35\n\t" \
  "v_max3_u32 v116, v116, v36, v37\n\t" \
  "v_max3_u32 v116, v116, v38, v39\n\t" \
  "v_max3_u32 v116, v116, v40, v41\n\t" \
  "v_max3_u32 v116, v116, v42, v43\n\t" \
  "v_max3_u32 v116, v116, v44, v45\n\t" \
  "v_max3_u32 v116, v116, v46, v47\n\t" \
  "v_cmp_eq_u32 vcc, -1, v116\n\t" \
  "s_cbranch_vccz 3b\n\t" \
  "s_bitcmp1_b32 s59, 6\n\t" \
  "s_cbranch_scc1 12f\n\t" \
  "buffer_load_dwordx4 v[32:35], v100, %[rh], s40 offen sc1\n\t" \
  "buffer_load_dwordx4 v[36:39], v100, %[rh], s76 offen sc1\n\t" \
  "buffer_load_dwordx4 v[40:43], v100, %[rh], s77 offen sc1\n\t" \
  "buffer_load_dwordx4 v[44:47], v100, %[rh], s78 offen sc1\n\t" \
  "s_branch 17b\n\t" \
  "12:\n\t" \
  "s_mov_b32 s42, s58\n\t" \
  "s_branch 99f\n\t" \
  "91:\n\t" \
  "s_mov_b32 s58, s42\n\t" \
  ".p2align 6\n\t" \
  "20:\n\t" \
  "s_and_b32 s76, s44, 7\n\t" \
  "s_lshl_b32 s76, s76, 15\n\t" \
  "s_add_i32 s40, s54, s76\n\t" \
  "s_and_b32 s76, s44, 3\n\t" \
  "s_lshl_b32 s45, s76, 16\n\t" \
  "s_add_i32 s47, s45, s55\n\t" \
  "s_add_i32 s49, s44, -1\n\t" \
  "s_and_b32 s48, s49, 3\n\t" \
  "s_lshl_b32 s48, s48, 16\n\t" \
  "s_lshl_b32 s50, s53, 8\n\t" \
  "s_mov_b32 s59, 0\n\t" \
  "s_mov_b32 s92, s51\n\t" \
  "s_mov_b32 s93, s53\n\t" \
  "s_xor_b32 s76, s40, 0x1000\n\t" \
  "s_xor_b32 s77, s40, 0x4000\n\t" \
  "s_xor_b32 s78, s40, 0x5000\n\t" \
  "s_cmp_lg_u32 s58, 0\n\t" \
  "s_cbranch_scc1 11f\n\t" \
  "10:\n\t" \
  "ds_read_b128 v[48:51], v102 offset:32768\n\t" \
  "ds_read_b128 v[52:55], v102 offset:33792\n\t" \
  "ds_read_b128 v[56:59], v102 offset:34816\n\t" \
  "ds_read_b128 v[60:63], v102 offset:35840\n\t" \
  "ds_read_b128 v[16:19], v102 offset:49152\n\t" \
  "ds_read_b128 v[20:23], v102 offset:50176\n\t" \
  "ds_read_b128 v[24:27], v102 offset:51200\n\t" \
  "ds_read_b128 v[28:31], v102 offset:52224\n\t" \
  "ds_read_b128 v[84:87], v103 offset:16384\n\t" \
  "ds_read_b128 v[88:91], v103 offset:17408\n\t" \
  "ds_read_b128 v[92:95], v103 offset:18432\n\t" \
  "ds_read_b128 v[96:99], v103 offset:19456\n\t" \
  "s_waitcnt lgkmcnt(4)\n\t" \
  "ds_read_b128 v[0:3], v103 offset:49152\n\t" \
  "ds_read_b128 v[4:7], v103 offset:50176\n\t" \
  "ds_read_b128 v[8:11], v103 offset:51200\n\t" \
  "ds_read_b128 v[12:15], v103 offset:52224\n\t" \
  "s_bitcmp1_b32 s58, 1\n\t" \
  "s_cselect_b64 s[80:81], -1, 0\n\t" \
  "s_cmp_lg_u32 s58, 0\n\t" \
  "s_cbranch_scc1 17f\n\t" \
  "s_mov_b32 exec_hi, 0\n\t" \
  "16:\n\t" \
  "s_memtime vcc\n\t" \
  "s_waitcnt lgkmcnt(0)\n\t" \
  "s_sub_i32 s79, s41, vcc_lo\n\t" \
  "s_cmp_gt_i32 s79, 64\n\t" \
  "s_cbranch_scc1 16b\n\t" \
  "13:\n\t" \
  "buffer_load_dword v116, v110, %[rh], s48 offen sc1\n\t" \
  "s_waitcnt vmcnt(0)\n\t" \
  "15:\n\t" \
  "v_mov_b32 v117, 20000\n\t" \
  "v_cmp_ne_u32_sdwa vcc, v116, s49 src0_sel:BYTE_0 src1_sel:DWORD\n\t" \
  "v_subrev_u32 v116, s50, v116\n\t" \
  "v_ashrrev_i32 v116, 8, v116\n\t" \
  "s_nop 1\n\t" \
  "v_max_i32_dpp v116, v116, v116 quad_perm:[1,0,3,2] row_mask:0xf bank_mask:0xf\n\t" \
  "s_cbranch_vccnz 14f\n\t" \
  "s_nop 0\n\t" \
  "v_max_i32_dpp v116, v116, v116 quad_perm:[2,3,0,1] row_mask:0xf bank_mask:0xf\n\t" \
  "s_nop 1\n\t" \
  "v_max_i32_dpp v116, v116, v116 row_half_mirror row_mask:0xf bank_mask:0xf\n\t" \
  "s_nop 1\n\t" \
  "v_max_i32_dpp v116, v116, v116 row_mirror row_mask:0xf bank_mask:0xf\n\t" \
  "v_med3_i32 v116, v116, 0, v117\n\t" \
  "v_mul_u32_u24 v116, s52, v116\n\t" \
  "v_lshrrev_b32 v116, 16, v116\n\t" \
  "s_mov_b64 exec, -1\n\t" \
  "v_readlane_b32 s84, v116, 0\n\t" \
  "v_readlane_b32 s85, v116, 16\n\t" \
  "s_max_u32 s84, s84, s85\n\t" \
  "s_add_i32 s84, s84, s51\n\t" \
  "v_mov_b64 v[68:69], 0\n\t" \
  "v_mov_b64 v[70:71], 0\n\t" \
  "v_mov_b64 v[72:73], 0\n\t" \
  "v_mov_b64 v[74:75], 0\n\t" \
  "v_mov_b64 v[76:77], 0\n\t" \
  "v_mov_b64 v[78:79], 0\n\t" \
  "v_mov_b64 v[80:81], 0\n\t" \
  "v_mov_b64 v[82:83], 0\n\t" \
  "1:\n\t" \
  "s_memtime vcc\n\t" \
  "s_waitcnt lgkmcnt(0)\n\t" \
  "s_sub_i32 s79, s84, vcc_lo\n\t" \
  "s_cmp_gt_i32 s79, 128\n\t" \
  "s_cbranch_scc1 1b\n\t" \
  "2:\n\t" \
  "s_sub_i32 s79, s79, 33\n\t" \
  "s_cmp_gt_i32 s79, 56\n\t" \
  "s_cbranch_scc1 2b\n\t" \
  "buffer_load_dwordx4 v[32:35], v100, %[rh], s40 offen sc1\n\t" \
  "s_nop 12\n\t" \
  "buffer_load_dwordx4 v[36:39], v100, %[rh], s76 offen sc1\n\t" \
  "s_nop 12\n\t" \
  "buffer_load_dwordx4 v[40:43], v100, %[rh], s77 offen sc1\n\t" \
  "s_nop 12\n\t" \
  "buffer_load_dwordx4 v[44:47], v100, %[rh], s78 offen sc1\n\t" \
  "s_memtime vcc\n\t" \
  "s_memrealtime s[82:83]\n\t" \
  "s_nop 4\n\t" \
  "s_memrealtime s[84:85]\n\t" \
  "s_nop 4\n\t" \
  "s_memrealtime s[94:95]\n\t" \
  "s_nop 4\n\t" \
  "s_memrealtime s[86:87]\n\t" \
  "s_nop 4\n\t" \
  "s_memrealtime s[88:89]\n\t" \
  "s_nop 4\n\t" \
  "s_memrealtime s[90:91]\n\t" \
  "s_mov_b32 s92, 0\n\t" \
  "s_waitcnt lgkmcnt(0)\n\t" \
  "s_cmp_eq_u32 s82, s84\n\t" \
  "s_addc_u32 s92, s92, 0\n\t" \
  "s_cmp_eq_u32 s82, s94\n\t" \
  "s_addc_u32 s92, s92, 0\n\t" \
  "s_cmp_eq_u32 s82, s86\n\t" \
  "s_addc_u32 s92, s92, 0\n\t" \
  "s_cmp_eq_u32 s82, s88\n\t" \
  "s_addc_u32 s92, s92, 0\n\t" \
  "s_cmp_eq_u32 s82, s90\n\t" \
  "s_addc_u32 s92, s92, 0\n\t" \
  "s_mul_i32 s92, s92, 24\n\t" \
  "s_add_i32 s92, s92, vcc_lo\n\t" \
  "s_lshl4_add_u32 s93, s82, 64\n\t" \
  "3:\n\t" \
  "s_waitcnt lgkmcnt(0)\n\t" \
  "s_waitcnt vmcnt(3)\n\t" \
  "v_smfmac_f32_16x16x64_bf16 v[68:71], v[32:35], a[96:103], v104\n\t" \
  "ds_write_b128 v101, v[32:35] offset:4096\n\t" \
  "v_smfmac_f32_16x16x64_bf16 v[72:75], v[32:35], a[104:111], v104\n\t" \
  "v_smfmac_f32_16x16x64_bf16 v[76:79], v[32:35], v[48:55], v104\n\t" \
  "v_smfmac_f32_16x16x64_bf16 v[80:83], v[32:35], v[56:63], v104\n\t" \
  "s_waitcnt vmcnt(2)\n\t" \
  "v_smfmac_f32_16x16x64_bf16 v[68:71], v[36:39], a[112:119], v104\n\t" \
  "ds_write_b128 v101, v[36:39] offset:5120\n\t" \
  "v_smfmac_f32_16x16x64_bf16 v[72:75], v[36:39], a[120:127], v104\n\t" \
  "v_smfmac_f32_16x16x64_bf16 v[76:79], v[36:39], a[128:135], v104\n\t" \
  "v_cmp_u_f32 vcc, v68, v69\n\t" \
  "v_smfmac_f32_16x16x64_bf16 v[80:83], v[36:39], a[136:143], v104\n\t" \
  "v_cmp_u_f32 s[82:83], v70, v71\n\t" \
  "s_waitcnt vmcnt(1)\n\t" \
  "v_smfmac_f32_16x16x64_bf16 v[68:71], v[40:43], v[16:23], v104\n\t" \
  "ds_write_b128 v101, v[40:43] offset:6144\n\t" \
  "v_smfmac_f32_16x16x64_bf16 v[72:75], v[40:43], v[24:31], v104\n\t" \
  "s_or_b64 vcc, vcc, s[82:83]\n\t" \
  "s_or_b64 vcc, vcc, s[80:81]\n\t" \
  "s_waitcnt lgkmcnt(1)\n\t" \
  "v_smfmac_f32_16x16x64_bf16 v[76:79], v[40:43], a[144:151], v104\n\t" \
  "s_cbranch_scc1 8f\n\t" \
  "5:\n\t" \
  "s_barrier\n\t" \
  "4:\n\t" \
  "s_memtime s[56:57]\n\t" \
  "ds_read_b128 v[48:51], v101 offset:8192\n\t" \
  "v_smfmac_f32_16x16x64_bf16 v[80:83], v[40:43], a[152:159], v104\n\t" \
  "ds_read_b128 v[52:55], v101 offset:9216\n\t" \
  "ds_read_b128 v[56:59], v101 offset:12288\n\t" \
  "s_waitcnt vmcnt(0)\n\t" \
  "v_smfmac_f32_16x16x64_bf16 v[68:71], v[44:47], a[160:167], v104\n\t" \
  "ds_write_b128 v101, v[44:47] offset:7168\n\t" \
  "v_smfmac_f32_16x16x64_bf16 v[72:75], v[44:47], a[168:175], v104\n\t" \
  "v_smfmac_f32_16x16x64_bf16 v[76:79], v[44:47], a[176:183], v104\n\t" \
  "v_cmp_u_f32 vcc, v68, v69\n\t" \
  "v_smfmac_f32_16x16x64_bf16 v[80:83], v[44:47], a[184:191], v104\n\t" \
  "v_cmp_u_f32 s[82:83], v70, v71\n\t" \
  "s_waitcnt lgkmcnt(3)\n\t" \
  "v_smfmac_f32_16x16x64_bf16 v[68:71], v[48:51], a[192:199], v104\n\t" \
  "ds_read_b128 v[60:63], v101 offset:13312\n\t" \
  "ds_read_b128 v[64:67], v101 offset:0\n\t" \
  "v_smfmac_f32_16x16x64_bf16 v[72:75], v[48:51], a[200:207], v104\n\t" \
  "s_or_b64 vcc, vcc, s[82:83]\n\t" \
  "s_waitcnt lgkmcnt(2)\n\t" \
  "v_smfmac_f32_16x16x64_bf16 v[68:71], v[52:55], a[208:215], v104\n\t" \
  "s_cbranch_scc1 9f\n\t" \
  "6:\n\t" \
  "s_barrier\n\t" \
  "v_smfmac_f32_16x16x64_bf16 v[72:75], v[52:55], a[216:223], v104\n\t" \
  "ds_read_b128 v[48:51], v101 offset:1024\n\t" \
  "v_smfmac_f32_16x16x64_bf16 v[68:71], v[56:59], v[152:159], v104\n\t" \
  "ds_read_b128 v[52:55], v101 offset:10240\n\t" \
  "v_smfmac_f32_16x16x64_bf16 v[72:75], v[56:59], v[160:167], v104\n\t" \
  "s_xor_b32 s94, s47, 0x5000\n\t" \
  "s_waitcnt lgkmcnt(3)\n\t" \
  "v_smfmac_f32_16x16x64_bf16 v[68:71], v[60:63], v[168:175], v104\n\t" \
  "ds_read_b128 v[56:59], v101 offset:11264\n\t" \
  "v_smfmac_f32_16x16x64_bf16 v[72:75], v[60:63], v[176:183], v104\n\t" \
  "s_xor_b32 s95, s47, 0x4000\n\t" \
  "s_waitcnt lgkmcnt(3)\n\t" \
  "v_smfmac_f32_16x16x64_bf16 v[68:71], v[64:67], a[0:7], v104\n\t" \
  "ds_read_b128 v[60:63], v101 offset:14336\n\t" \
  "ds_read_b128 v[32:35], v119 offset:0\n\t" \
  "v_smfmac_f32_16x16x64_bf16 v[72:75], v[64:67], a[8:15], v104\n\t" \
  "ds_read_b128 v[16:19], v102 offset:16384\n\t" \
  "s_waitcnt lgkmcnt(5)\n\t" \
  "v_smfmac_f32_16x16x64_bf16 v[68:71], v[48:51], a[16:23], v104\n\t" \
  "ds_read_b128 v[64:67], v101 offset:15360\n\t" \
  "ds_read_b128 v[36:39], v119 offset:1024\n\t" \
  "v_smfmac_f32_16x16x64_bf16 v[72:75], v[48:51], a[24:31], v104\n\t" \
  "ds_read_b128 v[20:23], v102 offset:17408\n\t" \
  "s_waitcnt lgkmcnt(7)\n\t" \
  "v_smfmac_f32_16x16x64_bf16 v[68:71], v[52:55], v[84:91], v104\n\t" \
  "ds_read_b128 v[48:51], v101 offset:3072\n\t" \
  "ds_read_b128 v[40:43], v119 offset:16\n\t" \
  "v_smfmac_f32_16x16x64_bf16 v[72:75], v[52:55], v[92:99], v104\n\t" \
  "ds_read_b128 v[24:27], v102 offset:18432\n\t" \
  "s_waitcnt lgkmcnt(9)\n\t" \
  "v_smfmac_f32_16x16x64_bf16 v[68:71], v[56:59], v[120:127], v104\n\t" \
  "ds_read_b128 v[52:55], v101 offset:2048\n\t" \
  "ds_read_b128 v[44:47], v119 offset:1040\n\t" \
  "v_smfmac_f32_16x16x64_bf16 v[72:75], v[56:59], v[128:135], v104\n\t" \
  "ds_read_b128 v[28:31], v102 offset:19456\n\t" \
  "s_waitcnt lgkmcnt(11)\n\t" \
  "v_smfmac_f32_16x16x64_bf16 v[68:71], v[60:63], v[0:7], v104\n\t" \
  "ds_read_b128 v[92:95], v119 offset:2048\n\t" \
  "v_smfmac_f32_16x16x64_bf16 v[72:75], v[60:63], v[8:15], v104\n\t" \
  "ds_read_b128 v[96:99], v119 offset:3072\n\t" \
  "s_waitcnt lgkmcnt(10)\n\t" \
  "v_smfmac_f32_16x16x64_bf16 v[68:71], v[64:67], v[216:223], v104\n\t" \
  "ds_read_b128 v[0:3], v103 offset:0\n\t" \
  "v_smfmac_f32_16x16x64_bf16 v[72:75], v[64:67], v[224:231], v104\n\t" \
  "ds_read_b128 v[4:7], v103 offset:1024\n\t" \
  "s_waitcnt lgkmcnt(9)\n\t" \
  "v_smfmac_f32_16x16x64_bf16 v[68:71], v[48:51], a[64:71], v104\n\t" \
  "ds_read_b128 v[8:11], v103 offset:2048\n\t" \
  "s_waitcnt lgkmcnt(7)\n\t" \
  "v_smfmac_f32_16x16x64_bf16 v[68:71], v[52:55], v[16:23], v104\n\t" \
  "ds_read_b128 v[12:15], v103 offset:3072\n\t" \
  "v_smfmac_f32_16x16x64_bf16 v[72:75], v[48:51], a[72:79], v104\n\t" \
  "ds_read_b128 v[16:19], v119 offset:2064\n\t" \
  "ds_read_b128 v[20:23], v119 offset:3088\n\t" \
  "s_waitcnt lgkmcnt(8)\n\t" \
  "v_smfmac_f32_16x16x64_bf16 v[72:75], v[52:55], v[24:31], v104\n\t" \
  "v_add_f32 v116, v68, v70\n\t" \
  "v_add_f32 v117, v69, v71\n\t" \
  "v_smfmac_f32_16x16x64_bf16 v[76:79], v[56:59], v[136:143], v104\n\t" \
  "v_cvt_pk_bf16_f32 v116, v116, v117\n\t" \
  "ds_read_b128 v[24:27], v103 offset:34816\n\t" \
  "v_smfmac_f32_16x16x64_bf16 v[80:83], v[56:59], v[144:151], v104\n\t" \
  "v_add_f32 v117, v72, v74\n\t" \
  "v_add_f32 v118, v73, v75\n\t" \
  "v_cvt_pk_bf16_f32 v117, v117, v118\n\t" \
  "ds_write2st64_b32 v105, v116, v117 offset1:1\n\t" \
  "v_smfmac_f32_16x16x64_bf16 v[76:79], v[60:63], v[200:207], v104\n\t" \
  "v_mov_b64 v[68:69], 0\n\t" \
  "s_xor_b32 s96, s47, 0x1000\n\t" \
  "v_smfmac_f32_16x16x64_bf16 v[80:83], v[60:63], v[208:215], v104\n\t" \
  "v_mov_b64 v[70:71], 0\n\t" \
  "v_smfmac_f32_16x16x64_bf16 v[76:79], v[64:67], v[232:239], v104\n\t" \
  "v_mov_b64 v[72:73], 0\n\t" \
  "v_smfmac_f32_16x16x64_bf16 v[80:83], v[64:67], v[240:247], v104\n\t" \
  "s_waitcnt lgkmcnt(0)\n\t" \
  "s_barrier\n\t" \
  "ds_read_b64_tr_b16 v[84:85], v107 offset:0\n\t" \
  "ds_read_b64_tr_b16 v[86:87], v107 offset:128\n\t" \
  "v_smfmac_f32_16x16x64_bf16 v[76:79], v[48:51], a[80:87], v104\n\t" \
  "ds_read_b64_tr_b16 v[88:89], v107 offset:1024\n\t" \
  "ds_read_b64_tr_b16 v[90:91], v107 offset:1152\n\t" \
  "v_smfmac_f32_16x16x64_bf16 v[80:83], v[48:51], a[88:95], v104\n\t" \
  "ds_read_b128 v[56:59], v101 offset:8192\n\t" \
  "v_mov_b64 v[74:75], 0\n\t" \
  "v_smfmac_f32_16x16x64_bf16 v[76:79], v[52:55], a[48:55], v104\n\t" \
  "ds_read_b128 v[60:63], v101 offset:1024\n\t" \
  "ds_read_b128 v[28:31], v103 offset:35840\n\t" \
  "v_smfmac_f32_16x16x64_bf16 v[80:83], v[52:55], a[56:63], v104\n\t" \
  "s_waitcnt lgkmcnt(5)\n\t" \
  "v_smfmac_f32_16x16x64_bf16 v[68:71], v[84:87], v[32:39], v104\n\t" \
  "v_smfmac_f32_16x16x64_bf16 v[72:75], v[84:87], v[40:47], v104\n\t" \
  "ds_read_b128 v[64:67], v101 offset:10240\n\t" \
  "s_waitcnt lgkmcnt(4)\n\t" \
  "v_smfmac_f32_16x16x64_bf16 v[68:71], v[88:91], v[92:99], v104\n\t" \
  "v_smfmac_f32_16x16x64_bf16 v[72:75], v[88:91], v[16:23], v104\n\t" \
  "ds_read_b128 v[48:51], v101 offset:12288\n\t" \
  "s_waitcnt lgkmcnt(4)\n\t" \
  "v_smfmac_f32_16x16x64_bf16 v[76:79], v[56:59], v[0:7], v104\n\t" \
  "ds_read_b128 v[16:19], v103 offset:32768\n\t" \
  "v_add_f32 v40, v69, v71\n\t" \
  "v_smfmac_f32_16x16x64_bf16 v[80:83], v[56:59], v[8:15], v104\n\t" \
  "v_add_f32 v41, v73, v75\n\t" \
  "v_max_f32 v42, v40, v41\n\t" \
  "s_waitcnt lgkmcnt(4)\n\t" \
  "v_smfmac_f32_16x16x64_bf16 v[76:79], v[60:63], a[32:39], v104\n\t" \
  "ds_read_b128 v[20:23], v103 offset:33792\n\t" \
  "v_max_f32_dpp v42, v42, v42 quad_perm:[1,0,3,2] row_mask:0xf bank_mask:0xf\n\t" \
  "v_smfmac_f32_16x16x64_bf16 v[80:83], v[60:63], a[40:47], v104\n\t" \
  "ds_read_b128 v[52:55], v101 offset:9216\n\t" \
  "v_max_f32_dpp v42, v42, v42 quad_perm:[2,3,0,1] row_mask:0xf bank_mask:0xf\n\t" \
  "s_waitcnt lgkmcnt(4)\n\t" \
  "v_smfmac_f32_16x16x64_bf16 v[76:79], v[64:67], a[240:247], v104\n\t" \
  "ds_read_b128 v[56:59], v101 offset:13312\n\t" \
  "v_max_f32_dpp v42, v42, v42 row_half_mirror row_mask:0xf bank_mask:0xf\n\t" \
  "v_smfmac_f32_16x16x64_bf16 v[80:83], v[64:67], a[248:255], v104\n\t" \
  "ds_read_b128 v[60:63], v101 offset:0\n\t" \
  "v_max_f32_dpp v42, v42, v42 row_mirror row_mask:0xf bank_mask:0xf\n\t" \
  "v_mul_f32 v42, %[cs], v42\n\t" \
  "s_waitcnt lgkmcnt(3)\n\t" \
  "v_smfmac_f32_16x16x64_bf16 v[76:79], v[48:51], v[16:23], v104\n\t" \
  "ds_read_b128 v[0:3], v102 offset:0\n\t" \
  "v_fma_f32 v40, v40, %[cs], -v42\n\t" \
  "v_fma_f32 v41, v41, %[cs], -v42\n\t" \
  "v_smfmac_f32_16x16x64_bf16 v[80:83], v[48:51], v[24:31], v104\n\t" \
  "ds_read_b128 v[4:7], v102 offset:1024\n\t" \
  "v_exp_f32 v43, v40\n\t" \
  "v_exp_f32 v44, v41\n\t" \
  "s_waitcnt lgkmcnt(4)\n\t" \
  "v_smfmac_f32_16x16x64_bf16 v[76:79], v[52:55], a[224:231], v104\n\t" \
  "ds_read_b128 v[8:11], v102 offset:2048\n\t" \
  "v_cvt_pk_bf16_f32 v45, v43, v44\n\t" \
  "buffer_store_dword v45, v112, %[rh], s94 offen sc1\n\t" \
  "v_smfmac_f32_16x16x64_bf16 v[80:83], v[52:55], a[232:239], v104\n\t" \
  "ds_read_b128 v[12:15], v102 offset:3072\n\t" \
  "buffer_store_dword v45, v112, %[rh], s95 offen sc1\n\t" \
  "s_waitcnt lgkmcnt(5)\n\t" \
  "v_smfmac_f32_16x16x64_bf16 v[76:79], v[56:59], v[184:191], v104\n\t" \
  "buffer_store_dword v45, v112, %[rh], s96 offen sc1\n\t" \
  "s_waitcnt lgkmcnt(2)\n\t" \
  "v_smfmac_f32_16x16x64_bf16 v[76:79], v[60:63], v[0:7], v104\n\t" \
  "buffer_store_dword v45, v112, %[rh], s47 offen sc1\n\t" \
  "v_smfmac_f32_16x16x64_bf16 v[80:83], v[56:59], v[192:199], v104\n\t" \
  "s_waitcnt lgkmcnt(0)\n\t" \
  "v_smfmac_f32_16x16x64_bf16 v[80:83], v[60:63], v[8:15], v104\n\t" \
  "v_add_f32 v20, v76, v78\n\t" \
  "v_add_f32 v21, v77, v79\n\t" \
  "v_cvt_pk_bf16_f32 v20, v20, v21\n\t" \
  "ds_write_b32 v106, v20\n\t" \
  "v_add_f32 v92, v43, v44\n\t" \
  "v_mov_b32 v25, v115\n\t" \
  "s_nop 1\n\t" \
  "v_add_f32 v23, v80, v82\n\t" \
  "v_add_f32 v21, v81, v83\n\t" \
  "v_cvt_pk_bf16_f32 v21, v23, v21\n\t" \
  "ds_write_b32 v106, v21 offset:256\n\t" \
  "v_add_f32_dpp v92, v92, v92 row_ror:8 row_mask:0xf bank_mask:0xf\n\t" \
  "v_mov_b64 v[0:1], 0\n\t" \
  "v_mov_b64 v[2:3], 0\n\t" \
  "v_mov_b32 v27, v114\n\t" \
  "s_waitcnt lgkmcnt(0)\n\t" \
  "s_barrier\n\t" \
  "ds_read_b64_tr_b16 v[4:5], v108 offset:0\n\t" \
  "ds_read_b64_tr_b16 v[6:7], v108 offset:64\n\t" \
  "ds_read_b64_tr_b16 v[8:9], v108 offset:512\n\t" \
  "ds_read_b64_tr_b16 v[10:11], v108 offset:576\n\t" \
  "ds_read_b64_tr_b16 v[12:13], v108 offset:1024\n\t" \
  "ds_read_b64_tr_b16 v[14:15], v108 offset:1088\n\t" \
  "ds_read_b64_tr_b16 v[16:17], v108 offset:1536\n\t" \
  "ds_read_b64_tr_b16 v[18:19], v108 offset:1600\n\t" \
  "s_waitcnt lgkmcnt(4)\n\t" \
  "v_smfmac_f32_16x16x64_bf16 v[0:3], v[84:87], v[4:11], v104\n\t" \
  "s_waitcnt lgkmcnt(0)\n\t" \
  "v_smfmac_f32_16x16x64_bf16 v[0:3], v[88:91], v[12:19], v104\n\t" \
  "s_nop 7\n\t" \
  "v_add_f32 v96, v1, v3\n\t" \
  "v_fma_f32 v96, v96, %[cs], -v42\n\t" \
  "v_exp_f32 v24, v96\n\t" \
  "s_nop 0\n\t" \
  "v_add_f32 v29, v92, v24\n\t" \
  "s_nop 0\n\t" \
  "v_mov_b32_dpp v25, v24 quad_perm:[1,0,3,2] row_mask:0xf bank_mask:0x3\n\t" \
  "v_add_f32_dpp v29, v29, v29 quad_perm:[1,0,3,2] row_mask:0xf bank_mask:0xf\n\t" \
  "v_mov_b32_dpp v27, v24 quad_perm:[0,1,2,3] row_mask:0xf bank_mask:0x3\n\t" \
  "s_nop 0\n\t" \
  "v_add_f32_dpp v29, v29, v29 quad_perm:[2,3,0,1] row_mask:0xf bank_mask:0xf\n\t" \
  "s_nop 1\n\t" \
  "v_add_f32_dpp v29, v29, v29 row_half_mirror row_mask:0xf bank_mask:0xf\n\t" \
  "v_rcp_f32 v26, v29\n\t" \
  "v_cmpx_gt_f32 vcc, 0x5d5e0b6b, v29\n\t" \
  "v_mul_f32 v24, v27, v26\n\t" \
  "v_mul_f32 v25, v25, v26\n\t" \
  "v_cvt_pk_bf16_f32 v24, v24, v25\n\t" \
  "buffer_store_dword v24, v113, %[rh], s94 offen sc1\n\t" \
  "buffer_store_dword v24, v113, %[rh], s95 offen sc1\n\t" \
  "buffer_store_dword v24, v113, %[rh], s96 offen sc1\n\t" \
  "buffer_store_dword v24, v113, %[rh], s47 offen sc1\n\t" \
  "s_memtime s[76:77]\n\t" \
  "s_mov_b64 exec, -1\n\t" \
  "v_mov_b32 v97, v29\n\t" \
  "s_cmp_eq_u64 vcc, -1\n\t" \
  "s_cselect_b32 s42, 0, 8\n\t" \
  "s_waitcnt lgkmcnt(0)\n\t" \
  "s_mov_b32 s57, s76\n\t" \
  "s_add_i32 s41, s56, 1950\n\t" \
  "s_sub_i32 s76, s92, s51\n\t" \
  "s_sub_i32 s77, s93, s53\n\t" \
  "s_cmp_eq_u32 s76, 0\n\t" \
  "s_cbranch_scc1 21f\n\t" \
  "v_cvt_f32_u32 v117, s76\n\t" \
  "v_cvt_f32_u32 v116, s77\n\t" \
  "v_rcp_f32 v117, v117\n\t" \
  "s_mov_b32 s51, s92\n\t" \
  "v_mul_f32 v117, v117, v116\n\t" \
  "v_max_f32 v117, 0x3f028f5c, v117\n\t" \
  "v_min_f32 v117, 0x3ffeb852, v117\n\t" \
  "v_rcp_f32 v116, v117\n\t" \
  "v_mul_f32 v117, 0x4f000000, v117\n\t" \
  "v_mul_f32 v116, 0x47800000, v116\n\t" \
  "v_cvt_u32_f32 v117, v117\n\t" \
  "v_cvt_u32_f32 v116, v116\n\t" \
  "s_mov_b32 s53, s93\n\t" \
  "v_readfirstlane_b32 s46, v117\n\t" \
  "v_readfirstlane_b32 s52, v116\n\t" \
  "21:\n\t" \
  "s_add_i32 s44, s44, 1\n\t" \
  "s_and_b32 s58, s58, 4\n\t" \
  "s_and_b32 s76, s44, 64\n\t" \
  "s_or_b32 s42, s42, s76\n\t" \
  "s_cbranch_scc0 20b\n\t" \
  "s_branch 99f\n\t" \
  "14:\n\t" \
  "s_add_u32 s59, s59, 0x10000\n\t" \
  "s_cmp_lt_u32 s59, 0x100000\n\t" \
  "s_cbranch_scc1 13b\n\t" \
  "s_mov_b64 exec, -1\n\t" \
  "s_mov_b32 s42, 16\n\t" \
  "s_branch 99f\n\t" \
  "8:\n\t" \
  "s_bitcmp1_b32 s58, 1\n\t" \
  "s_cbranch_scc1 4b\n\t" \
  "s_bitcmp1_b32 s58, 2\n\t" \
  "s_cbranch_scc1 5b\n\t" \
  "v_max3_u32 v116, v32, v33, v34\n\t" \
  "v_max3_u32 v117, v35, v36, v37\n\t" \
  "v_max3_u32 v116, v116, v38, v39\n\t" \
  "v_max_u32 v116, v116, v117\n\t" \
  "v_cmp_eq_u32 vcc, -1, v116\n\t" \
  "s_cbranch_vccz 5b\n\t" \
  "s_branch 0f\n\t" \
  "9:\n\t" \
  "s_bitcmp1_b32 s58, 2\n\t" \
  "s_cbranch_scc1 6b\n\t" \
  "v_max3_u32 v116, v40, v41, v42\n\t" \
  "v_max3_u32 v117, v43, v44, v45\n\t" \
  "v_max3_u32 v116, v116, v46, v47\n\t" \
  "v_max_u32 v116, v116, v117\n\t" \
  "v_cmp_eq_u32 vcc, -1, v116\n\t" \
  "s_cbranch_vccz 6b\n\t" \
  "s_or_b32 s58, s58, 2\n\t" \
  "0:\n\t" \
  "s_or_b32 s58, s58, 1\n\t" \
  "11:\n\t" \
  "buffer_load_dwordx4 v[32:35], v100, %[rh], s40 offen sc1\n\t" \
  "buffer_load_dwordx4 v[36:39], v100, %[rh], s76 offen sc1\n\t" \
  "buffer_load_dwordx4 v[40:43], v100, %[rh], s77 offen sc1\n\t" \
  "buffer_load_dwordx4 v[44:47], v100, %[rh], s78 offen sc1\n\t" \
  "s_waitcnt lgkmcnt(0)\n\t" \
  "s_branch 10b\n\t" \
  "17:\n\t" \
  "v_mov_b64 v[68:69], 0\n\t" \
  "v_mov_b64 v[70:71], 0\n\t" \
  "v_mov_b64 v[72:73], 0\n\t" \
  "v_mov_b64 v[74:75], 0\n\t" \
  "v_mov_b64 v[76:77], 0\n\t" \
  "v_mov_b64 v[78:79], 0\n\t" \
  "v_mov_b64 v[80:81], 0\n\t" \
  "v_mov_b64 v[82:83], 0\n\t" \
  "s_add_u32 s59, s59, 1\n\t" \
  "s_bitcmp1_b32 s58, 2\n\t" \
  "s_cbranch_scc1 3b\n\t" \
  "s_waitcnt vmcnt(0)\n\t" \
  "v_max_u32 v116, v32, v33\n\t" \
  "v_max3_u32 v116, v116, v34, v35\n\t" \
  "v_max3_u32 v116, v116, v36, v37\n\t" \
  "v_max3_u32 v116, v116, v38, v39\n\t" \
  "v_max3_u32 v116, v116, v40, v41\n\t" \
  "v_max3_u32 v116, v116, v42, v43\n\t" \
  "v_max3_u32 v116, v116, v44, v45\n\t" \
  "v_max3_u32 v116, v116, v46, v47\n\t" \
  "v_cmp_eq_u32 vcc, -1, v116\n\t" \
  "s_cbranch_vccz 3b\n\t" \
  "s_bitcmp1_b32 s59, 6\n\t" \
  "s_cbranch_scc1 12f\n\t" \
  "buffer_load_dwordx4 v[32:35], v100, %[rh], s40 offen sc1\n\t" \
  "buffer_load_dwordx4 v[36:39], v100, %[rh], s76 offen sc1\n\t" \
  "buffer_load_dwordx4 v[40:43], v100, %[rh], s77 offen sc1\n\t" \
  "buffer_load_dwordx4 v[44:47], v100, %[rh], s78 offen sc1\n\t" \
  "s_branch 17b\n\t" \
  "12:\n\t" \
  "s_mov_b32 s42, s58\n\t" \
  "s_branch 99f\n\t" \
  "92:\n\t" \
  "s_mov_b32 s58, s42\n\t" \
  ".p2align 6\n\t" \
  "20:\n\t" \
  "s_and_b32 s76, s44, 7\n\t" \
  "s_lshl_b32 s76, s76, 15\n\t" \
  "s_add_i32 s40, s54, s76\n\t" \
  "s_and_b32 s76, s44, 3\n\t" \
  "s_lshl_b32 s45, s76, 16\n\t" \
  "s_add_i32 s47, s44, 0\n\t" \
  "s_min_u32 s47, s47, 63\n\t" \
  "s_and_b32 s76, s47, 4\n\t" \
  "s_lshl_b32 s76, s76, 11\n\t" \
  "s_cmp_lt_u32 s47, 4\n\t" \
  "s_cselect_b32 s77, 0x2000, 0\n\t" \
  "s_lshl_b32 s47, s47, 19\n\t" \
  "s_add_i32 s47, s47, s55\n\t" \
  "s_xor_b32 s47, s47, s76\n\t" \
  "s_andn2_b32 s47, s47, s77\n\t" \
  "s_add_i32 s97, s47, 0x1000\n\t" \
  "s_add_i32 s49, s44, -1\n\t" \
  "s_and_b32 s48, s49, 3\n\t" \
  "s_lshl_b32 s48, s48, 16\n\t" \
  "s_lshl_b32 s50, s53, 8\n\t" \
  "s_mov_b32 s59, 0\n\t" \
  "s_mov_b32 s92, s51\n\t" \
  "s_mov_b32 s93, s53\n\t" \
  "s_xor_b32 s76, s40, 0x1000\n\t" \
  "s_xor_b32 s77, s40, 0x4000\n\t" \
  "s_xor_b32 s78, s40, 0x5000\n\t" \
  "s_cmp_lg_u32 s58, 0\n\t" \
  "s_cbranch_scc1 11f\n\t" \
  "10:\n\t" \
  "ds_read_b128 v[48:51], v103 offset:0\n\t" \
  "ds_read_b128 v[52:55], v103 offset:1024\n\t" \
  "ds_read_b128 v[56:59], v103 offset:2048\n\t" \
  "ds_read_b128 v[60:63], v103 offset:3072\n\t" \
  "ds_read_b128 v[16:19], v103 offset:16384\n\t" \
  "ds_read_b128 v[20:23], v103 offset:17408\n\t" \
  "ds_read_b128 v[24:27], v103 offset:18432\n\t" \
  "ds_read_b128 v[28:31], v103 offset:19456\n\t" \
  "ds_read_b128 v[84:87], v103 offset:49152\n\t" \
  "ds_read_b128 v[88:91], v103 offset:50176\n\t" \
  "ds_read_b128 v[92:95], v103 offset:51200\n\t" \
  "ds_read_b128 v[96:99], v103 offset:52224\n\t" \
  "s_waitcnt lgkmcnt(4)\n\t" \
  "ds_read_b128 v[0:3], v102 offset:16384\n\t" \
  "ds_read_b128 v[4:7], v102 offset:17408\n\t" \
  "ds_read_b128 v[8:11], v102 offset:18432\n\t" \
  "ds_read_b128 v[12:15], v102 offset:19456\n\t" \
  "v_mov_b32 v116, s49\n\t" \
  "s_waitcnt vmcnt(0)\n\t" \
  "buffer_store_dword v116, v109, %[rh], s48 offen sc1\n\t" \
  "s_bitcmp1_b32 s58, 1\n\t" \
  "s_cselect_b64 s[80:81], -1, 0\n\t" \
  "s_cmp_lg_u32 s58, 0\n\t" \
  "s_cbranch_scc1 17f\n\t" \
  "s_mov_b32 exec_hi, 0\n\t" \
  "s_waitcnt vmcnt(2)\n\t" \
  "v_mov_b32 v116, v118\n\t" \
  "s_branch 15f\n\t" \
  "13:\n\t" \
  "buffer_load_dword v116, v110, %[rh], s48 offen sc1\n\t" \
  "s_waitcnt vmcnt(0)\n\t" \
  "15:\n\t" \
  "v_mov_b32 v117, 20000\n\t" \
  "v_cmp_ne_u32_sdwa vcc, v116, s49 src0_sel:BYTE_0 src1_sel:DWORD\n\t" \
  "v_subrev_u32 v116, s50, v116\n\t" \
  "v_ashrrev_i32 v116, 8, v116\n\t" \
  "s_nop 1\n\t" \
  "v_max_i32_dpp v116, v116, v116 quad_perm:[1,0,3,2] row_mask:0xf bank_mask:0xf\n\t" \
  "s_cbranch_vccnz 14f\n\t" \
  "s_nop 0\n\t" \
  "v_max_i32_dpp v116, v116, v116 quad_perm:[2,3,0,1] row_mask:0xf bank_mask:0xf\n\t" \
  "s_nop 1\n\t" \
  "v_max_i32_dpp v116, v116, v116 row_half_mirror row_mask:0xf bank_mask:0xf\n\t" \
  "s_nop 1\n\t" \
  "v_max_i32_dpp v116, v116, v116 row_mirror row_mask:0xf bank_mask:0xf\n\t" \
  "v_med3_i32 v116, v116, 0, v117\n\t" \
  "v_mul_u32_u24 v116, s52, v116\n\t" \
  "v_lshrrev_b32 v116, 16, v116\n\t" \
  "s_mov_b64 exec, -1\n\t" \
  "v_readlane_b32 s84, v116, 0\n\t" \
  "v_readlane_b32 s85, v116, 16\n\t" \
  "s_max_u32 s84, s84, s85\n\t" \
  "s_add_i32 s84, s84, s51\n\t" \
  "v_mov_b64 v[68:69], 0\n\t" \
  "v_mov_b64 v[70:71], 0\n\t" \
  "v_mov_b64 v[72:73], 0\n\t" \
  "v_mov_b64 v[74:75], 0\n\t" \
  "v_mov_b64 v[76:77], 0\n\t" \
  "v_mov_b64 v[78:79], 0\n\t" \
  "v_mov_b64 v[80:81], 0\n\t" \
  "v_mov_b64 v[82:83], 0\n\t" \
  "1:\n\t" \
  "s_memtime vcc\n\t" \
  "s_waitcnt lgkmcnt(0)\n\t" \
  "s_sub_i32 s79, s84, vcc_lo\n\t" \
  "s_cmp_gt_i32 s79, 128\n\t" \
  "s_cbranch_scc1 1b\n\t" \
  "2:\n\t" \
  "s_sub_i32 s79, s79, 33\n\t" \
  "s_cmp_gt_i32 s79, 56\n\t" \
  "s_cbranch_scc1 2b\n\t" \
  "buffer_load_dwordx4 v[32:35], v100, %[rh], s40 offen sc1\n\t" \
  "s_nop 12\n\t" \
  "buffer_load_dwordx4 v[36:39], v100, %[rh], s76 offen sc1\n\t" \
  "s_nop 12\n\t" \
  "buffer_load_dwordx4 v[40:43], v100, %[rh], s77 offen sc1\n\t" \
  "s_nop 12\n\t" \
  "buffer_load_dwordx4 v[44:47], v100, %[rh], s78 offen sc1\n\t" \
  "s_memtime vcc\n\t" \
  "s_memrealtime s[82:83]\n\t" \
  "s_nop 4\n\t" \
  "s_memrealtime s[84:85]\n\t" \
  "s_nop 4\n\t" \
  "s_memrealtime s[94:95]\n\t" \
  "s_nop 4\n\t" \
  "s_memrealtime s[86:87]\n\t" \
  "s_nop 4\n\t" \
  "s_memrealtime s[88:89]\n\t" \
  "s_nop 4\n\t" \
  "s_memrealtime s[90:91]\n\t" \
  "s_mov_b32 s92, 0\n\t" \
  "s_waitcnt lgkmcnt(0)\n\t" \
  "s_cmp_eq_u32 s82, s84\n\t" \
  "s_addc_u32 s92, s92, 0\n\t" \
  "s_cmp_eq_u32 s82, s94\n\t" \
  "s_addc_u32 s92, s92, 0\n\t" \
  "s_cmp_eq_u32 s82, s86\n\t" \
  "s_addc_u32 s92, s92, 0\n\t" \
  "s_cmp_eq_u32 s82, s88\n\t" \
  "s_addc_u32 s92, s92, 0\n\t" \
  "s_cmp_eq_u32 s82, s90\n\t" \
  "s_addc_u32 s92, s92, 0\n\t" \
  "s_mul_i32 s92, s92, 24\n\t" \
  "s_add_i32 s92, s92, vcc_lo\n\t" \
  "s_lshl4_add_u32 s93, s82, 64\n\t" \
  "3:\n\t" \
  "s_waitcnt lgkmcnt(0)\n\t" \
  "s_waitcnt vmcnt(3)\n\t" \
  "v_smfmac_f32_16x16x64_bf16 v[68:71], v[32:35], a[192:199], v104\n\t" \
  "ds_write_b128 v101, v[32:35] offset:8192\n\t" \
  "v_smfmac_f32_16x16x64_bf16 v[72:75], v[32:35], a[200:207], v104\n\t" \
  "v_smfmac_f32_16x16x64_bf16 v[76:79], v[32:35], v[48:55], v104\n\t" \
  "v_smfmac_f32_16x16x64_bf16 v[80:83], v[32:35], v[56:63], v104\n\t" \
  "s_waitcnt vmcnt(2)\n\t" \
  "v_smfmac_f32_16x16x64_bf16 v[68:71], v[36:39], a[208:215], v104\n\t" \
  "ds_write_b128 v101, v[36:39] offset:9216\n\t" \
  "v_smfmac_f32_16x16x64_bf16 v[72:75], v[36:39], a[216:223], v104\n\t" \
  "v_smfmac_f32_16x16x64_bf16 v[76:79], v[36:39], a[224:231], v104\n\t" \
  "v_cmp_u_f32 vcc, v68, v69\n\t" \
  "v_smfmac_f32_16x16x64_bf16 v[80:83], v[36:39], a[232:239], v104\n\t" \
  "v_cmp_u_f32 s[82:83], v70, v71\n\t" \
  "s_waitcnt vmcnt(1)\n\t" \
  "v_smfmac_f32_16x16x64_bf16 v[68:71], v[40:43], v[16:23], v104\n\t" \
  "ds_write_b128 v101, v[40:43] offset:10240\n\t" \
  "v_smfmac_f32_16x16x64_bf16 v[72:75], v[40:43], v[24:31], v104\n\t" \
  "s_or_b64 vcc, vcc, s[82:83]\n\t" \
  "s_or_b64 vcc, vcc, s[80:81]\n\t" \
  "s_waitcnt lgkmcnt(1)\n\t" \
  "v_smfmac_f32_16x16x64_bf16 v[76:79], v[40:43], a[240:247], v104\n\t" \
  "s_cbranch_scc1 8f\n\t" \
  "5:\n\t" \
  "s_barrier\n\t" \
  "4:\n\t" \
  "s_memtime s[56:57]\n\t" \
  "ds_read_b128 v[48:51], v101 offset:12288\n\t" \
  "v_smfmac_f32_16x16x64_bf16 v[80:83], v[40:43], a[248:255], v104\n\t" \
  "ds_read_b128 v[52:55], v101 offset:13312\n\t" \
  "ds_read_b128 v[56:59], v101 offset:0\n\t" \
  "s_waitcnt vmcnt(0)\n\t" \
  "v_smfmac_f32_16x16x64_bf16 v[68:71], v[44:47], v[120:127], v104\n\t" \
  "v_smfmac_f32_16x16x64_bf16 v[72:75], v[44:47], v[128:135], v104\n\t" \
  "ds_write_b128 v101, v[44:47] offset:11264\n\t" \
  "v_smfmac_f32_16x16x64_bf16 v[76:79], v[44:47], v[136:143], v104\n\t" \
  "v_cmp_u_f32 vcc, v68, v69\n\t" \
  "v_smfmac_f32_16x16x64_bf16 v[80:83], v[44:47], v[144:151], v104\n\t" \
  "v_cmp_u_f32 s[82:83], v70, v71\n\t" \
  "s_waitcnt lgkmcnt(3)\n\t" \
  "v_smfmac_f32_16x16x64_bf16 v[68:71], v[48:51], v[152:159], v104\n\t" \
  "ds_read_b128 v[60:63], v101 offset:1024\n\t" \
  "ds_read_b128 v[64:67], v101 offset:4096\n\t" \
  "v_smfmac_f32_16x16x64_bf16 v[72:75], v[48:51], v[160:167], v104\n\t" \
  "s_or_b64 vcc, vcc, s[82:83]\n\t" \
  "s_waitcnt lgkmcnt(2)\n\t" \
  "v_smfmac_f32_16x16x64_bf16 v[68:71], v[52:55], v[168:175], v104\n\t" \
  "s_cbranch_scc1 9f\n\t" \
  "6:\n\t" \
  "s_barrier\n\t" \
  "v_smfmac_f32_16x16x64_bf16 v[72:75], v[52:55], v[176:183], v104\n\t" \
  "ds_read_b128 v[48:51], v101 offset:5120\n\t" \
  "v_smfmac_f32_16x16x64_bf16 v[68:71], v[56:59], a[0:7], v104\n\t" \
  "ds_read_b128 v[52:55], v101 offset:14336\n\t" \
  "v_smfmac_f32_16x16x64_bf16 v[72:75], v[56:59], a[8:15], v104\n\t" \
  "buffer_load_dwordx4 v[32:35], v100, %[ru], s97 offen offset:0 sc1\n\t" \
  "s_waitcnt lgkmcnt(3)\n\t" \
  "v_smfmac_f32_16x16x64_bf16 v[68:71], v[60:63], a[16:23], v104\n\t" \
  "ds_read_b128 v[56:59], v101 offset:15360\n\t" \
  "v_smfmac_f32_16x16x64_bf16 v[72:75], v[60:63], a[24:31], v104\n\t" \
  "buffer_load_dwordx4 v[36:39], v100, %[ru], s97 offen offset:1024 sc1\n\t" \
  "s_waitcnt lgkmcnt(3)\n\t" \
  "v_smfmac_f32_16x16x64_bf16 v[68:71], v[64:67], a[96:103], v104\n\t" \
  "ds_read_b128 v[60:63], v101 offset:2048\n\t" \
  "buffer_load_dwordx4 v[40:43], v100, %[ru], s97 offen offset:2048 sc1\n\t" \
  "v_smfmac_f32_16x16x64_bf16 v[72:75], v[64:67], a[104:111], v104\n\t" \
  "ds_read_b128 v[16:19], v102 offset:49152\n\t" \
  "s_waitcnt lgkmcnt(4)\n\t" \
  "v_smfmac_f32_16x16x64_bf16 v[68:71], v[48:51], a[112:119], v104\n\t" \
  "ds_read_b128 v[64:67], v101 offset:3072\n\t" \
  "buffer_load_dwordx4 v[44:47], v100, %[ru], s97 offen offset:3072 sc1\n\t" \
  "v_smfmac_f32_16x16x64_bf16 v[72:75], v[48:51], a[120:127], v104\n\t" \
  "ds_read_b128 v[20:23], v102 offset:50176\n\t" \
  "s_waitcnt lgkmcnt(5)\n\t" \
  "v_smfmac_f32_16x16x64_bf16 v[68:71], v[52:55], v[84:91], v104\n\t" \
  "ds_read_b128 v[48:51], v101 offset:7168\n\t" \
  "v_smfmac_f32_16x16x64_bf16 v[72:75], v[52:55], v[92:99], v104\n\t" \
  "ds_read_b128 v[24:27], v102 offset:51200\n\t" \
  "s_waitcnt lgkmcnt(6)\n\t" \
  "v_smfmac_f32_16x16x64_bf16 v[68:71], v[56:59], v[216:223], v104\n\t" \
  "ds_read_b128 v[52:55], v101 offset:6144\n\t" \
  "v_smfmac_f32_16x16x64_bf16 v[72:75], v[56:59], v[224:231], v104\n\t" \
  "ds_read_b128 v[28:31], v102 offset:52224\n\t" \
  "s_waitcnt lgkmcnt(7)\n\t" \
  "v_smfmac_f32_16x16x64_bf16 v[68:71], v[60:63], v[0:7], v104\n\t" \
  "buffer_load_dwordx4 v[84:87], v100, %[ru], s47 offen offset:0 sc1\n\t" \
  "v_smfmac_f32_16x16x64_bf16 v[72:75], v[60:63], v[8:15], v104\n\t" \
  "buffer_load_dwordx4 v[88:91], v100, %[ru], s47 offen offset:1024 sc1\n\t" \
  "s_waitcnt lgkmcnt(5)\n\t" \
  "v_smfmac_f32_16x16x64_bf16 v[68:71], v[64:67], a[64:71], v104\n\t" \
  "ds_read_b128 v[0:3], v103 offset:32768\n\t" \
  "v_smfmac_f32_16x16x64_bf16 v[72:75], v[64:67], a[72:79], v104\n\t" \
  "ds_read_b128 v[4:7], v103 offset:33792\n\t" \
  "s_waitcnt lgkmcnt(5)\n\t" \
  "v_smfmac_f32_16x16x64_bf16 v[68:71], v[48:51], a[160:167], v104\n\t" \
  "ds_read_b128 v[8:11], v103 offset:34816\n\t" \
  "s_waitcnt lgkmcnt(4)\n\t" \
  "v_smfmac_f32_16x16x64_bf16 v[68:71], v[52:55], v[16:23], v104\n\t" \
  "ds_read_b128 v[12:15], v103 offset:35840\n\t" \
  "v_smfmac_f32_16x16x64_bf16 v[72:75], v[48:51], a[168:175], v104\n\t" \
  "buffer_load_dwordx4 v[92:95], v100, %[ru], s47 offen offset:2048 sc1\n\t" \
  "s_waitcnt lgkmcnt(4)\n\t" \
  "v_smfmac_f32_16x16x64_bf16 v[72:75], v[52:55], v[24:31], v104\n\t" \
  "v_add_f32 v116, v68, v70\n\t" \
  "v_add_f32 v117, v69, v71\n\t" \
  "v_smfmac_f32_16x16x64_bf16 v[76:79], v[56:59], v[232:239], v104\n\t" \
  "v_cvt_pk_bf16_f32 v116, v116, v117\n\t" \
  "ds_read_b128 v[24:27], v102 offset:2048\n\t" \
  "v_smfmac_f32_16x16x64_bf16 v[80:83], v[56:59], v[240:247], v104\n\t" \
  "v_add_f32 v117, v72, v74\n\t" \
  "v_add_f32 v118, v73, v75\n\t" \
  "v_cvt_pk_bf16_f32 v117, v117, v118\n\t" \
  "ds_write2st64_b32 v105, v116, v117 offset1:1\n\t" \
  "v_smfmac_f32_16x16x64_bf16 v[76:79], v[60:63], a[48:55], v104\n\t" \
  "buffer_load_dwordx4 v[96:99], v100, %[ru], s47 offen offset:3072 sc1\n\t" \
  "v_smfmac_f32_16x16x64_bf16 v[80:83], v[60:63], a[56:63], v104\n\t" \
  "v_smfmac_f32_16x16x64_bf16 v[76:79], v[64:67], a[80:87], v104\n\t" \
  "buffer_load_dword v117, v107, %[ru], 0 offen sc1\n\t" \
  "v_smfmac_f32_16x16x64_bf16 v[80:83], v[64:67], a[88:95], v104\n\t" \
  "s_waitcnt lgkmcnt(0)\n\t" \
  "s_barrier\n\t" \
  "ds_read_b128 v[56:59], v101 offset:12288\n\t" \
  "v_smfmac_f32_16x16x64_bf16 v[76:79], v[48:51], a[176:183], v104\n\t" \
  "v_smfmac_f32_16x16x64_bf16 v[80:83], v[48:51], a[184:191], v104\n\t" \
  "ds_read_b128 v[60:63], v101 offset:5120\n\t" \
  "v_smfmac_f32_16x16x64_bf16 v[76:79], v[52:55], a[144:151], v104\n\t" \
  "ds_read_b128 v[16:19], v102 offset:0\n\t" \
  "v_smfmac_f32_16x16x64_bf16 v[80:83], v[52:55], a[152:159], v104\n\t" \
  "ds_read_b128 v[64:67], v101 offset:14336\n\t" \
  "s_waitcnt lgkmcnt(3)\n\t" \
  "v_smfmac_f32_16x16x64_bf16 v[76:79], v[56:59], v[0:7], v104\n\t" \
  "ds_read_b128 v[20:23], v102 offset:1024\n\t" \
  "v_smfmac_f32_16x16x64_bf16 v[80:83], v[56:59], v[8:15], v104\n\t" \
  "ds_read_b128 v[48:51], v101 offset:0\n\t" \
  "s_waitcnt lgkmcnt(4)\n\t" \
  "v_smfmac_f32_16x16x64_bf16 v[76:79], v[60:63], a[128:135], v104\n\t" \
  "ds_read_b128 v[28:31], v102 offset:3072\n\t" \
  "v_smfmac_f32_16x16x64_bf16 v[80:83], v[60:63], a[136:143], v104\n\t" \
  "ds_read_b128 v[52:55], v101 offset:13312\n\t" \
  "s_waitcnt lgkmcnt(4)\n\t" \
  "v_smfmac_f32_16x16x64_bf16 v[76:79], v[64:67], v[200:207], v104\n\t" \
  "ds_read_b128 v[0:3], v102 offset:32768\n\t" \
  "v_smfmac_f32_16x16x64_bf16 v[80:83], v[64:67], v[208:215], v104\n\t" \
  "ds_read_b128 v[56:59], v101 offset:1024\n\t" \
  "ds_read_b128 v[4:7], v102 offset:33792\n\t" \
  "s_waitcnt lgkmcnt(5)\n\t" \
  "v_smfmac_f32_16x16x64_bf16 v[76:79], v[48:51], v[16:23], v104\n\t" \
  "ds_read_b128 v[60:63], v101 offset:4096\n\t" \
  "s_waitcnt lgkmcnt(5)\n\t" \
  "v_smfmac_f32_16x16x64_bf16 v[80:83], v[48:51], v[24:31], v104\n\t" \
  "ds_read_b128 v[8:11], v102 offset:34816\n\t" \
  "s_waitcnt lgkmcnt(5)\n\t" \
  "v_smfmac_f32_16x16x64_bf16 v[76:79], v[52:55], v[184:191], v104\n\t" \
  "ds_read_b128 v[12:15], v102 offset:35840\n\t" \
  "v_smfmac_f32_16x16x64_bf16 v[80:83], v[52:55], v[192:199], v104\n\t" \
  "s_waitcnt lgkmcnt(4)\n\t" \
  "v_smfmac_f32_16x16x64_bf16 v[76:79], v[56:59], a[32:39], v104\n\t" \
  "s_waitcnt lgkmcnt(2)\n\t" \
  "v_smfmac_f32_16x16x64_bf16 v[76:79], v[60:63], v[0:7], v104\n\t" \
  "v_smfmac_f32_16x16x64_bf16 v[80:83], v[56:59], a[40:47], v104\n\t" \
  "s_waitcnt lgkmcnt(0)\n\t" \
  "v_smfmac_f32_16x16x64_bf16 v[80:83], v[60:63], v[8:15], v104\n\t" \
  "v_add_f32 v68, v76, v78\n\t" \
  "v_add_f32 v69, v77, v79\n\t" \
  "v_cvt_pk_bf16_f32 v68, v68, v69\n\t" \
  "ds_write_b32 v106, v68\n\t" \
  "ds_read_b128 v[0:3], v101 offset:20480\n\t" \
  "ds_read_b128 v[4:7], v101 offset:21504\n\t" \
  "ds_read_b128 v[8:11], v101 offset:22528\n\t" \
  "ds_read_b128 v[12:15], v101 offset:23552\n\t" \
  "v_add_f32 v70, v80, v82\n\t" \
  "v_add_f32 v71, v81, v83\n\t" \
  "v_cvt_pk_bf16_f32 v70, v70, v71\n\t" \
  "ds_write_b32 v106, v70 offset:256\n\t" \
  "ds_read_b128 v[16:19], v101 offset:24576\n\t" \
  "ds_read_b128 v[20:23], v101 offset:25600\n\t" \
  "ds_read_b128 v[24:27], v101 offset:26624\n\t" \
  "ds_read_b128 v[28:31], v101 offset:27648\n\t" \
  "s_waitcnt lgkmcnt(0)\n\t" \
  "s_barrier\n\t" \
  "s_waitcnt vmcnt(1)\n\t" \
  "v_mfma_f32_16x16x32_bf16 v[64:67], v[0:3], v[32:35], 0\n\t" \
  "v_mfma_f32_16x16x32_bf16 v[68:71], v[16:19], v[32:35], v[112:115]\n\t" \
  "v_mfma_f32_16x16x32_bf16 v[72:75], v[0:3], v[84:87], v[112:115]\n\t" \
  "v_mfma_f32_16x16x32_bf16 v[64:67], v[4:7], v[36:39], v[64:67]\n\t" \
  "v_mfma_f32_16x16x32_bf16 v[68:71], v[20:23], v[36:39], v[68:71]\n\t" \
  "v_mfma_f32_16x16x32_bf16 v[72:75], v[4:7], v[88:91], v[72:75]\n\t" \
  "ds_read_b64_tr_b16 v[48:49], v108 offset:0\n\t" \
  "ds_read_b64_tr_b16 v[50:51], v108 offset:64\n\t" \
  "ds_read_b64_tr_b16 v[52:53], v108 offset:512\n\t" \
  "ds_read_b64_tr_b16 v[54:55], v108 offset:576\n\t" \
  "ds_read_b64_tr_b16 v[56:57], v108 offset:1024\n\t" \
  "ds_read_b64_tr_b16 v[58:59], v108 offset:1088\n\t" \
  "ds_read_b64_tr_b16 v[60:61], v108 offset:1536\n\t" \
  "ds_read_b64_tr_b16 v[62:63], v108 offset:1600\n\t" \
  "v_mfma_f32_16x16x32_bf16 v[64:67], v[8:11], v[40:43], v[64:67]\n\t" \
  "v_mfma_f32_16x16x32_bf16 v[68:71], v[24:27], v[40:43], v[68:71]\n\t" \
  "v_mfma_f32_16x16x32_bf16 v[72:75], v[8:11], v[92:95], v[72:75]\n\t" \
  "v_mfma_f32_16x16x32_bf16 v[64:67], v[12:15], v[44:47], v[64:67]\n\t" \
  "v_mfma_f32_16x16x32_bf16 v[68:71], v[28:31], v[44:47], v[68:71]\n\t" \
  "v_mfma_f32_16x16x32_bf16 v[72:75], v[12:15], v[96:99], v[72:75]\n\t" \
  "s_waitcnt vmcnt(0)\n\t" \
  "v_readfirstlane_b32 s76, v117\n\t" \
  "s_add_i32 s77, s44, 1\n\t" \
  "s_min_i32 s77, s77, 63\n\t" \
  "s_cmp_lt_i32 s76, s77\n\t" \
  "s_cselect_b32 s42, 32, 0\n\t" \
  "s_waitcnt lgkmcnt(0)\n\t" \
  "v_mfma_f32_16x16x32_bf16 v[76:79], v[48:51], v[32:35], 0\n\t" \
  "v_mfma_f32_16x16x32_bf16 v[80:83], v[48:51], v[84:87], 0\n\t" \
  "v_mfma_f32_16x16x32_bf16 v[76:79], v[52:55], v[36:39], v[76:79]\n\t" \
  "v_max3_f32 v1, v64, v65, v66\n\t" \
  "v_mfma_f32_16x16x32_bf16 v[80:83], v[52:55], v[88:91], v[80:83]\n\t" \
  "v_max3_f32 v1, v1, v67, v68\n\t" \
  "v_mfma_f32_16x16x32_bf16 v[76:79], v[56:59], v[40:43], v[76:79]\n\t" \
  "v_max3_f32 v0, v72, v73, v74\n\t" \
  "v_mfma_f32_16x16x32_bf16 v[80:83], v[56:59], v[92:95], v[80:83]\n\t" \
  "v_max3_f32 v1, v1, v69, v70\n\t" \
  "v_mfma_f32_16x16x32_bf16 v[76:79], v[60:63], v[44:47], v[76:79]\n\t" \
  "v_max_f32 v0, v0, v75\n\t" \
  "v_mfma_f32_16x16x32_bf16 v[80:83], v[60:63], v[96:99], v[80:83]\n\t" \
  "v_max_f32 v1, v1, v71\n\t" \
  "s_nop 1\n\t" \
  "v_permlane32_swap_b32 v0, v1\n\t" \
  "s_and_b32 s76, s44, 3\n\t" \
  "v_max_f32 v0, v0, v1\n\t" \
  "s_lshl_b32 s76, s76, 17\n\t" \
  "s_nop 1\n\t" \
  "v_mov_b32_dpp v80, v76 quad_perm:[0,1,2,3] row_mask:0xc bank_mask:0xf\n\t" \
  "v_mov_b32_dpp v81, v77 quad_perm:[0,1,2,3] row_mask:0xc bank_mask:0xf\n\t" \
  "v_mov_b32_dpp v82, v78 quad_perm:[0,1,2,3] row_mask:0xc bank_mask:0xf\n\t" \
  "v_mov_b32_dpp v83, v79 quad_perm:[0,1,2,3] row_mask:0xc bank_mask:0xf\n\t" \
  "v_max3_f32 v0, v0, v80, v81\n\t" \
  "v_max3_f32 v0, v0, v82, v83\n\t" \
  "v_mov_b32 v1, v0\n\t" \
  "s_nop 1\n\t" \
  "v_permlane16_swap_b32 v0, v1\n\t" \
  "s_nop 0\n\t" \
  "v_max_f32 v0, v0, v1\n\t" \
  "v_mul_f32 v0, %[cs], v0\n\t" \
  "v_mov_b32 v1, v0\n\t" \
  "v_mov_b32 v2, v0\n\t" \
  "s_nop 1\n\t" \
  "v_permlane32_swap_b32 v1, v2\n\t" \
  "s_nop 0\n\t" \
  "v_fma_f32 v80, v80, %[cs], -v0\n\t" \
  "v_fma_f32 v81, v81, %[cs], -v0\n\t" \
  "v_fma_f32 v82, v82, %[cs], -v0\n\t" \
  "v_fma_f32 v83, v83, %[cs], -v0\n\t" \
  "v_fma_f32 v72, v72, %[cs], -v1\n\t" \
  "v_fma_f32 v73, v73, %[cs], -v1\n\t" \
  "v_fma_f32 v74, v74, %[cs], -v1\n\t" \
  "v_fma_f32 v75, v75, %[cs], -v1\n\t" \
  "v_fma_f32 v64, v64, %[cs], -v2\n\t" \
  "v_fma_f32 v65, v65, %[cs], -v2\n\t" \
  "v_fma_f32 v66, v66, %[cs], -v2\n\t" \
  "v_fma_f32 v67, v67, %[cs], -v2\n\t" \
  "v_fma_f32 v68, v68, %[cs], -v2\n\t" \
  "v_fma_f32 v69, v69, %[cs], -v2\n\t" \
  "v_fma_f32 v70, v70, %[cs], -v2\n\t" \
  "v_fma_f32 v71, v71, %[cs], -v2\n\t" \
  "v_exp_f32 v80, v80\n\t" \
  "v_exp_f32 v81, v81\n\t" \
  "v_exp_f32 v82, v82\n\t" \
  "v_exp_f32 v83, v83\n\t" \
  "v_exp_f32 v72, v72\n\t" \
  "v_exp_f32 v73, v73\n\t" \
  "v_exp_f32 v74, v74\n\t" \
  "v_exp_f32 v75, v75\n\t" \
  "v_exp_f32 v64, v64\n\t" \
  "v_exp_f32 v65, v65\n\t" \
  "v_exp_f32 v66, v66\n\t" \
  "v_exp_f32 v67, v67\n\t" \
  "v_exp_f32 v68, v68\n\t" \
  "v_exp_f32 v69, v69\n\t" \
  "v_exp_f32 v70, v70\n\t" \
  "v_exp_f32 v71, v71\n\t" \
  "v_add_f32 v3, v80, v81\n\t" \
  "v_add_f32 v0, v72, v73\n\t" \
  "v_add_f32 v1, v64, v65\n\t" \
  "v_add_f32 v2, v68, v69\n\t" \
  "v_add_f32 v3, v3, v82\n\t" \
  "v_add_f32 v0, v0, v74\n\t" \
  "v_add_f32 v1, v1, v66\n\t" \
  "v_add_f32 v2, v2, v70\n\t" \
  "v_add_f32 v0, v0, v75\n\t" \
  "v_add_f32 v1, v1, v67\n\t" \
  "v_add_f32 v2, v2, v71\n\t" \
  "v_add_f32 v1, v1, v2\n\t" \
  "v_add_f32 v3, v3, v83\n\t" \
  "s_nop 0\n\t" \
  "v_permlane32_swap_b32 v0, v1\n\t" \
  "s_nop 0\n\t" \
  "v_add_f32 v0, v0, v1\n\t" \
  "v_add_f32 v0, v0, v3\n\t" \
  "v_mov_b32 v1, v0\n\t" \
  "s_nop 1\n\t" \
  "v_permlane16_swap_b32 v0, v1\n\t" \
  "s_nop 0\n\t" \
  "v_add_f32 v0, v0, v1\n\t" \
  "v_rcp_f32 v0, v0\n\t" \
  "s_mov_b32 exec_hi, 0\n\t" \
  "buffer_load_dword v118, v110, %[rh], s45 offen sc1\n\t" \
  "s_mov_b64 exec, -1\n\t" \
  "v_mov_b32 v1, v0\n\t" \
  "v_mov_b32 v2, v0\n\t" \
  "s_nop 1\n\t" \
  "v_permlane32_swap_b32 v1, v2\n\t" \
  "s_nop 0\n\t" \
  "v_mul_f32 v80, v80, v0\n\t" \
  "v_mul_f32 v81, v81, v0\n\t" \
  "v_mul_f32 v82, v82, v0\n\t" \
  "v_mul_f32 v83, v83, v0\n\t" \
  "v_mul_f32 v72, v72, v1\n\t" \
  "v_mul_f32 v73, v73, v1\n\t" \
  "v_mul_f32 v74, v74, v1\n\t" \
  "v_mul_f32 v75, v75, v1\n\t" \
  "v_mul_f32 v64, v64, v2\n\t" \
  "v_mul_f32 v65, v65, v2\n\t" \
  "v_mul_f32 v66, v66, v2\n\t" \
  "v_mul_f32 v67, v67, v2\n\t" \
  "v_mul_f32 v68, v68, v2\n\t" \
  "v_mul_f32 v69, v69, v2\n\t" \
  "v_mul_f32 v70, v70, v2\n\t" \
  "v_mul_f32 v71, v71, v2\n\t" \
  "v_cvt_pk_bf16_f32 v0, v72, v73\n\t" \
  "v_cvt_pk_bf16_f32 v1, v64, v65\n\t" \
  "v_cvt_pk_bf16_f32 v2, v74, v75\n\t" \
  "v_cvt_pk_bf16_f32 v3, v66, v67\n\t" \
  "v_cvt_pk_bf16_f32 v4, v68, v69\n\t" \
  "v_cvt_pk_bf16_f32 v5, v80, v81\n\t" \
  "v_cvt_pk_bf16_f32 v6, v70, v71\n\t" \
  "v_cvt_pk_bf16_f32 v7, v82, v83\n\t" \
  "buffer_store_dwordx4 v[0:3], v119, %[rh], s76 offen sc1\n\t" \
  "buffer_store_dwordx4 v[4:7], v119, %[rh], s76 offen offset:1024 sc1\n\t" \
  "s_add_i32 s41, s56, 1950\n\t" \
  "s_sub_i32 s76, s92, s51\n\t" \
  "s_sub_i32 s77, s93, s53\n\t" \
  "s_cmp_eq_u32 s76, 0\n\t" \
  "s_cbranch_scc1 21f\n\t" \
  "v_cvt_f32_u32 v117, s76\n\t" \
  "v_cvt_f32_u32 v116, s77\n\t" \
  "v_rcp_f32 v117, v117\n\t" \
  "s_mov_b32 s51, s92\n\t" \
  "v_mul_f32 v117, v117, v116\n\t" \
  "v_max_f32 v117, 0x3f028f5c, v117\n\t" \
  "v_min_f32 v117, 0x3ffeb852, v117\n\t" \
  "v_rcp_f32 v116, v117\n\t" \
  "v_mul_f32 v117, 0x4f000000, v117\n\t" \
  "v_mul_f32 v116, 0x47800000, v116\n\t" \
  "v_cvt_u32_f32 v117, v117\n\t" \
  "v_cvt_u32_f32 v116, v116\n\t" \
  "s_mov_b32 s53, s93\n\t" \
  "v_readfirstlane_b32 s46, v117\n\t" \
  "v_readfirstlane_b32 s52, v116\n\t" \
  "21:\n\t" \
  "s_add_i32 s44, s44, 1\n\t" \
  "s_and_b32 s58, s58, 4\n\t" \
  "s_and_b32 s76, s44, 64\n\t" \
  "s_or_b32 s42, s42, s76\n\t" \
  "s_cbranch_scc0 20b\n\t" \
  "s_branch 99f\n\t" \
  "14:\n\t" \
  "s_add_u32 s59, s59, 0x10000\n\t" \
  "s_cmp_lt_u32 s59, 0x100000\n\t" \
  "s_cbranch_scc1 13b\n\t" \
  "s_mov_b64 exec, -1\n\t" \
  "s_mov_b32 s42, 16\n\t" \
  "s_branch 99f\n\t" \
  "8:\n\t" \
  "s_bitcmp1_b32 s58, 1\n\t" \
  "s_cbranch_scc1 4b\n\t" \
  "s_bitcmp1_b32 s58, 2\n\t" \
  "s_cbranch_scc1 5b\n\t" \
  "v_max3_u32 v116, v32, v33, v34\n\t" \
  "v_max3_u32 v117, v35, v36, v37\n\t" \
  "v_max3_u32 v116, v116, v38, v39\n\t" \
  "v_max_u32 v116, v116, v117\n\t" \
  "v_cmp_eq_u32 vcc, -1, v116\n\t" \
  "s_cbranch_vccz 5b\n\t" \
  "s_branch 0f\n\t" \
  "9:\n\t" \
  "s_bitcmp1_b32 s58, 2\n\t" \
  "s_cbranch_scc1 6b\n\t" \
  "v_max3_u32 v116, v40, v41, v42\n\t" \
  "v_max3_u32 v117, v43, v44, v45\n\t" \
  "v_max3_u32 v116, v116, v46, v47\n\t" \
  "v_max_u32 v116, v116, v117\n\t" \
  "v_cmp_eq_u32 vcc, -1, v116\n\t" \
  "s_cbranch_vccz 6b\n\t" \
  "s_or_b32 s58, s58, 2\n\t" \
  "0:\n\t" \
  "s_or_b32 s58, s58, 1\n\t" \
  "11:\n\t" \
  "buffer_load_dwordx4 v[32:35], v100, %[rh], s40 offen sc1\n\t" \
  "buffer_load_dwordx4 v[36:39], v100, %[rh], s76 offen sc1\n\t" \
  "buffer_load_dwordx4 v[40:43], v100, %[rh], s77 offen sc1\n\t" \
  "buffer_load_dwordx4 v[44:47], v100, %[rh], s78 offen sc1\n\t" \
  "s_waitcnt lgkmcnt(0)\n\t" \
  "s_branch 10b\n\t" \
  "17:\n\t" \
  "v_mov_b64 v[68:69], 0\n\t" \
  "v_mov_b64 v[70:71], 0\n\t" \
  "v_mov_b64 v[72:73], 0\n\t" \
  "v_mov_b64 v[74:75], 0\n\t" \
  "v_mov_b64 v[76:77], 0\n\t" \
  "v_mov_b64 v[78:79], 0\n\t" \
  "v_mov_b64 v[80:81], 0\n\t" \
  "v_mov_b64 v[82:83], 0\n\t" \
  "s_add_u32 s59, s59, 1\n\t" \
  "s_bitcmp1_b32 s58, 2\n\t" \
  "s_cbranch_scc1 3b\n\t" \
  "s_waitcnt vmcnt(0)\n\t" \
  "v_max_u32 v116, v32, v33\n\t" \
  "v_max3_u32 v116, v116, v34, v35\n\t" \
  "v_max3_u32 v116, v116, v36, v37\n\t" \
  "v_max3_u32 v116, v116, v38, v39\n\t" \
  "v_max3_u32 v116, v116, v40, v41\n\t" \
  "v_max3_u32 v116, v116, v42, v43\n\t" \
  "v_max3_u32 v116, v116, v44, v45\n\t" \
  "v_max3_u32 v116, v116, v46, v47\n\t" \
  "v_cmp_eq_u32 vcc, -1, v116\n\t" \
  "s_cbranch_vccz 3b\n\t" \
  "s_bitcmp1_b32 s59, 6\n\t" \
  "s_cbranch_scc1 12f\n\t" \
  "buffer_load_dwordx4 v[32:35], v100, %[rh], s40 offen sc1\n\t" \
  "buffer_load_dwordx4 v[36:39], v100, %[rh], s76 offen sc1\n\t" \
  "buffer_load_dwordx4 v[40:43], v100, %[rh], s77 offen sc1\n\t" \
  "buffer_load_dwordx4 v[44:47], v100, %[rh], s78 offen sc1\n\t" \
  "s_branch 17b\n\t" \
  "12:\n\t" \
  "s_mov_b32 s42, s58\n\t" \
  "s_branch 99f\n\t" \
  "93:\n\t" \
  "s_mov_b32 s58, s42\n\t" \
  ".p2align 6\n\t" \
  "20:\n\t" \
  "s_and_b32 s76, s44, 7\n\t" \
  "s_lshl_b32 s76, s76, 15\n\t" \
  "s_add_i32 s40, s54, s76\n\t" \
  "s_and_b32 s76, s44, 3\n\t" \
  "s_lshl_b32 s45, s76, 16\n\t" \
  "s_add_i32 s47, s44, 1\n\t" \
  "s_min_u32 s47, s47, 63\n\t" \
  "s_and_b32 s76, s47, 4\n\t" \
  "s_lshl_b32 s76, s76, 11\n\t" \
  "s_cmp_lt_u32 s47, 4\n\t" \
  "s_cselect_b32 s77, 0x2000, 0\n\t" \
  "s_lshl_b32 s47, s47, 19\n\t" \
  "s_add_i32 s47, s47, s55\n\t" \
  "s_xor_b32 s47, s47, s76\n\t" \
  "s_andn2_b32 s47, s47, s77\n\t" \
  "s_add_i32 s97, s47, 0x1000\n\t" \
  "s_add_i32 s49, s44, -1\n\t" \
  "s_and_b32 s48, s49, 3\n\t" \
  "s_lshl_b32 s48, s48, 16\n\t" \
  "s_lshl_b32 s50, s53, 8\n\t" \
  "s_mov_b32 s59, 0\n\t" \
  "s_mov_b32 s92, s51\n\t" \
  "s_mov_b32 s93, s53\n\t" \
  "s_xor_b32 s76, s40, 0x1000\n\t" \
  "s_xor_b32 s77, s40, 0x4000\n\t" \
  "s_xor_b32 s78, s40, 0x5000\n\t" \
  "s_cmp_lg_u32 s58, 0\n\t" \
  "s_cbranch_scc1 11f\n\t" \
  "10:\n\t" \
  "ds_read_b128 v[48:51], v103 offset:32768\n\t" \
  "ds_read_b128 v[52:55], v103 offset:33792\n\t" \
  "ds_read_b128 v[56:59], v103 offset:34816\n\t" \
  "ds_read_b128 v[60:63], v103 offset:35840\n\t" \
  "ds_read_b128 v[16:19], v103 offset:49152\n\t" \
  "ds_read_b128 v[20:23], v103 offset:50176\n\t" \
  "ds_read_b128 v[24:27], v103 offset:51200\n\t" \
  "ds_read_b128 v[28:31], v103 offset:52224\n\t" \
  "ds_read_b128 v[84:87], v102 offset:16384\n\t" \
  "ds_read_b128 v[88:91], v102 offset:17408\n\t" \
  "ds_read_b128 v[92:95], v102 offset:18432\n\t" \
  "ds_read_b128 v[96:99], v102 offset:19456\n\t" \
  "s_waitcnt lgkmcnt(4)\n\t" \
  "ds_read_b128 v[0:3], v102 offset:49152\n\t" \
  "ds_read_b128 v[4:7], v102 offset:50176\n\t" \
  "ds_read_b128 v[8:11], v102 offset:51200\n\t" \
  "ds_read_b128 v[12:15], v102 offset:52224\n\t" \
  "s_bitcmp1_b32 s58, 1\n\t" \
  "s_cselect_b64 s[80:81], -1, 0\n\t" \
  "s_cmp_lg_u32 s58, 0\n\t" \
  "s_cbranch_scc1 17f\n\t" \
  "s_mov_b32 exec_hi, 0\n\t" \
  "s_waitcnt vmcnt(5)\n\t" \
  "v_mov_b32 v116, v118\n\t" \
  "s_branch 15f\n\t" \
  "13:\n\t" \
  "buffer_load_dword v116, v110, %[rh], s48 offen sc1\n\t" \
  "s_waitcnt vmcnt(0)\n\t" \
  "15:\n\t" \
  "v_mov_b32 v117, 20000\n\t" \
  "v_cmp_ne_u32_sdwa vcc, v116, s49 src0_sel:BYTE_0 src1_sel:DWORD\n\t" \
  "v_subrev_u32 v116, s50, v116\n\t" \
  "v_ashrrev_i32 v116, 8, v116\n\t" \
  "s_nop 1\n\t" \
  "v_max_i32_dpp v116, v116, v116 quad_perm:[1,0,3,2] row_mask:0xf bank_mask:0xf\n\t" \
  "s_cbranch_vccnz 14f\n\t" \
  "s_nop 0\n\t" \
  "v_max_i32_dpp v116, v116, v116 quad_perm:[2,3,0,1] row_mask:0xf bank_mask:0xf\n\t" \
  "s_nop 1\n\t" \
  "v_max_i32_dpp v116, v116, v116 row_half_mirror row_mask:0xf bank_mask:0xf\n\t" \
  "s_nop 1\n\t" \
  "v_max_i32_dpp v116, v116, v116 row_mirror row_mask:0xf bank_mask:0xf\n\t" \
  "v_med3_i32 v116, v116, 0, v117\n\t" \
  "v_mul_u32_u24 v116, s52, v116\n\t" \
  "v_lshrrev_b32 v116, 16, v116\n\t" \
  "s_mov_b64 exec, -1\n\t" \
  "v_readlane_b32 s84, v116, 0\n\t" \
  "v_readlane_b32 s85, v116, 16\n\t" \
  "s_max_u32 s84, s84, s85\n\t" \
  "s_add_i32 s84, s84, s51\n\t" \
  "v_mov_b64 v[68:69], 0\n\t" \
  "v_mov_b64 v[70:71], 0\n\t" \
  "v_mov_b64 v[72:73], 0\n\t" \
  "v_mov_b64 v[74:75], 0\n\t" \
  "v_mov_b64 v[76:77], 0\n\t" \
  "v_mov_b64 v[78:79], 0\n\t" \
  "v_mov_b64 v[80:81], 0\n\t" \
  "v_mov_b64 v[82:83], 0\n\t" \
  "1:\n\t" \
  "s_memtime vcc\n\t" \
  "s_waitcnt lgkmcnt(0)\n\t" \
  "s_sub_i32 s79, s84, vcc_lo\n\t" \
  "s_cmp_gt_i32 s79, 128\n\t" \
  "s_cbranch_scc1 1b\n\t" \
  "2:\n\t" \
  "s_sub_i32 s79, s79, 33\n\t" \
  "s_cmp_gt_i32 s79, 56\n\t" \
  "s_cbranch_scc1 2b\n\t" \
  "buffer_load_dwordx4 v[32:35], v100, %[rh], s40 offen sc1\n\t" \
  "s_nop 12\n\t" \
  "buffer_load_dwordx4 v[36:39], v100, %[rh], s76 offen sc1\n\t" \
  "s_nop 12\n\t" \
  "buffer_load_dwordx4 v[40:43], v100, %[rh], s77 offen sc1\n\t" \
  "s_nop 12\n\t" \
  "buffer_load_dwordx4 v[44:47], v100, %[rh], s78 offen sc1\n\t" \
  "s_memtime vcc\n\t" \
  "s_memrealtime s[82:83]\n\t" \
  "s_nop 4\n\t" \
  "s_memrealtime s[84:85]\n\t" \
  "s_nop 4\n\t" \
  "s_memrealtime s[94:95]\n\t" \
  "s_nop 4\n\t" \
  "s_memrealtime s[86:87]\n\t" \
  "s_nop 4\n\t" \
  "s_memrealtime s[88:89]\n\t" \
  "s_nop 4\n\t" \
  "s_memrealtime s[90:91]\n\t" \
  "s_mov_b32 s92, 0\n\t" \
  "s_waitcnt lgkmcnt(0)\n\t" \
  "s_cmp_eq_u32 s82, s84\n\t" \
  "s_addc_u32 s92, s92, 0\n\t" \
  "s_cmp_eq_u32 s82, s94\n\t" \
  "s_addc_u32 s92, s92, 0\n\t" \
  "s_cmp_eq_u32 s82, s86\n\t" \
  "s_addc_u32 s92, s92, 0\n\t" \
  "s_cmp_eq_u32 s82, s88\n\t" \
  "s_addc_u32 s92, s92, 0\n\t" \
  "s_cmp_eq_u32 s82, s90\n\t" \
  "s_addc_u32 s92, s92, 0\n\t" \
  "s_mul_i32 s92, s92, 24\n\t" \
  "s_add_i32 s92, s92, vcc_lo\n\t" \
  "s_lshl4_add_u32 s93, s82, 64\n\t" \
  "3:\n\t" \
  "s_sub_i32 s84, s92, s43\n\t" \
  "s_lshl_b32 s85, s93, 8\n\t" \
  "s_or_b32 s85, s85, s44\n\t" \
  "v_mov_b32 v118, s85\n\t" \
  "s_waitcnt lgkmcnt(0)\n\t" \
  "s_waitcnt vmcnt(3)\n\t" \
  "v_smfmac_f32_16x16x64_bf16 v[68:71], v[32:35], v[152:159], v104\n\t" \
  "ds_write_b128 v101, v[32:35] offset:12288\n\t" \
  "v_smfmac_f32_16x16x64_bf16 v[72:75], v[32:35], v[160:167], v104\n\t" \
  "v_smfmac_f32_16x16x64_bf16 v[76:79], v[32:35], v[48:55], v104\n\t" \
  "v_smfmac_f32_16x16x64_bf16 v[80:83], v[32:35], v[56:63], v104\n\t" \
  "s_waitcnt vmcnt(2)\n\t" \
  "v_smfmac_f32_16x16x64_bf16 v[68:71], v[36:39], v[168:175], v104\n\t" \
  "ds_write_b128 v101, v[36:39] offset:13312\n\t" \
  "v_smfmac_f32_16x16x64_bf16 v[72:75], v[36:39], v[176:183], v104\n\t" \
  "v_smfmac_f32_16x16x64_bf16 v[76:79], v[36:39], v[184:191], v104\n\t" \
  "v_cmp_u_f32 vcc, v68, v69\n\t" \
  "v_smfmac_f32_16x16x64_bf16 v[80:83], v[36:39], v[192:199], v104\n\t" \
  "v_cmp_u_f32 s[82:83], v70, v71\n\t" \
  "s_waitcnt vmcnt(1)\n\t" \
  "v_smfmac_f32_16x16x64_bf16 v[68:71], v[40:43], v[16:23], v104\n\t" \
  "ds_write_b128 v101, v[40:43] offset:14336\n\t" \
  "v_smfmac_f32_16x16x64_bf16 v[72:75], v[40:43], v[24:31], v104\n\t" \
  "s_or_b64 vcc, vcc, s[82:83]\n\t" \
  "s_or_b64 vcc, vcc, s[80:81]\n\t" \
  "s_waitcnt lgkmcnt(1)\n\t" \
  "v_smfmac_f32_16x16x64_bf16 v[76:79], v[40:43], v[200:207], v104\n\t" \
  "s_cbranch_scc1 8f\n\t" \
  "5:\n\t" \
  "s_barrier\n\t" \
  "4:\n\t" \
  "s_memtime s[56:57]\n\t" \
  "ds_read_b128 v[48:51], v101 offset:0\n\t" \
  "v_smfmac_f32_16x16x64_bf16 v[80:83], v[40:43], v[208:215], v104\n\t" \
  "ds_read_b128 v[52:55], v101 offset:1024\n\t" \
  "ds_read_b128 v[56:59], v101 offset:4096\n\t" \
  "s_waitcnt vmcnt(0)\n\t" \
  "v_smfmac_f32_16x16x64_bf16 v[68:71], v[44:47], v[216:223], v104\n\t" \
  "v_smfmac_f32_16x16x64_bf16 v[72:75], v[44:47], v[224:231], v104\n\t" \
  "ds_write_b128 v101, v[44:47] offset:15360\n\t" \
  "v_smfmac_f32_16x16x64_bf16 v[76:79], v[44:47], v[232:239], v104\n\t" \
  "v_cmp_u_f32 vcc, v68, v69\n\t" \
  "v_smfmac_f32_16x16x64_bf16 v[80:83], v[44:47], v[240:247], v104\n\t" \
  "v_cmp_u_f32 s[82:83], v70, v71\n\t" \
  "s_waitcnt lgkmcnt(3)\n\t" \
  "v_smfmac_f32_16x16x64_bf16 v[68:71], v[48:51], a[0:7], v104\n\t" \
  "ds_read_b128 v[60:63], v101 offset:5120\n\t" \
  "ds_read_b128 v[64:67], v101 offset:8192\n\t" \
  "v_smfmac_f32_16x16x64_bf16 v[72:75], v[48:51], a[8:15], v104\n\t" \
  "s_or_b64 vcc, vcc, s[82:83]\n\t" \
  "s_waitcnt lgkmcnt(2)\n\t" \
  "v_smfmac_f32_16x16x64_bf16 v[68:71], v[52:55], a[16:23], v104\n\t" \
  "s_cbranch_scc1 9f\n\t" \
  "6:\n\t" \
  "s_barrier\n\t" \
  "v_smfmac_f32_16x16x64_bf16 v[72:75], v[52:55], a[24:31], v104\n\t" \
  "s_waitcnt lgkmcnt(0)\n\t" \
  "ds_read_b128 v[48:51], v101 offset:9216\n\t" \
  "v_smfmac_f32_16x16x64_bf16 v[68:71], v[56:59], a[96:103], v104\n\t" \
  "ds_read_b128 v[52:55], v101 offset:2048\n\t" \
  "s_sub_i32 s79, s56, s84\n\t" \
  "s_mul_hi_u32 s79, s79, s46\n\t" \
  "v_smfmac_f32_16x16x64_bf16 v[72:75], v[56:59], a[104:111], v104\n\t" \
  "v_lshl_add_u32 v116, s79, 9, v118\n\t" \
  "buffer_store_dword v116, v112, %[rh], s45 offen sc1\n\t" \
  "v_smfmac_f32_16x16x64_bf16 v[68:71], v[60:63], a[112:119], v104\n\t" \
  "ds_read_b128 v[56:59], v101 offset:3072\n\t" \
  "v_smfmac_f32_16x16x64_bf16 v[72:75], v[60:63], a[120:127], v104\n\t" \
  "v_smfmac_f32_16x16x64_bf16 v[68:71], v[64:67], a[192:199], v104\n\t" \
  "ds_read_b128 v[60:63], v101 offset:6144\n\t" \
  "v_smfmac_f32_16x16x64_bf16 v[72:75], v[64:67], a[200:207], v104\n\t" \
  "ds_read_b128 v[16:19], v103 offset:16384\n\t" \
  "s_waitcnt lgkmcnt(4)\n\t" \
  "v_smfmac_f32_16x16x64_bf16 v[68:71], v[48:51], a[208:215], v104\n\t" \
  "ds_read_b128 v[64:67], v101 offset:7168\n\t" \
  "v_smfmac_f32_16x16x64_bf16 v[72:75], v[48:51], a[216:223], v104\n\t" \
  "ds_read_b128 v[20:23], v103 offset:17408\n\t" \
  "s_waitcnt lgkmcnt(5)\n\t" \
  "v_smfmac_f32_16x16x64_bf16 v[68:71], v[52:55], v[84:91], v104\n\t" \
  "ds_read_b128 v[48:51], v101 offset:11264\n\t" \
  "v_smfmac_f32_16x16x64_bf16 v[72:75], v[52:55], v[92:99], v104\n\t" \
  "ds_read_b128 v[24:27], v103 offset:18432\n\t" \
  "s_waitcnt lgkmcnt(6)\n\t" \
  "v_smfmac_f32_16x16x64_bf16 v[68:71], v[56:59], a[64:71], v104\n\t" \
  "ds_read_b128 v[52:55], v101 offset:10240\n\t" \
  "v_smfmac_f32_16x16x64_bf16 v[72:75], v[56:59], a[72:79], v104\n\t" \
  "ds_read_b128 v[28:31], v103 offset:19456\n\t" \
  "s_waitcnt lgkmcnt(7)\n\t" \
  "v_smfmac_f32_16x16x64_bf16 v[68:71], v[60:63], v[0:7], v104\n\t" \
  "v_smfmac_f32_16x16x64_bf16 v[72:75], v[60:63], v[8:15], v104\n\t" \
  "s_waitcnt lgkmcnt(5)\n\t" \
  "v_smfmac_f32_16x16x64_bf16 v[68:71], v[64:67], a[160:167], v104\n\t" \
  "ds_read_b128 v[0:3], v102 offset:0\n\t" \
  "v_smfmac_f32_16x16x64_bf16 v[72:75], v[64:67], a[168:175], v104\n\t" \
  "ds_read_b128 v[4:7], v102 offset:1024\n\t" \
  "s_waitcnt lgkmcnt(5)\n\t" \
  "v_smfmac_f32_16x16x64_bf16 v[68:71], v[48:51], v[120:127], v104\n\t" \
  "ds_read_b128 v[8:11], v102 offset:2048\n\t" \
  "s_waitcnt lgkmcnt(4)\n\t" \
  "v_smfmac_f32_16x16x64_bf16 v[68:71], v[52:55], v[16:23], v104\n\t" \
  "ds_read_b128 v[12:15], v102 offset:3072\n\t" \
  "v_smfmac_f32_16x16x64_bf16 v[72:75], v[48:51], v[128:135], v104\n\t" \
  "s_waitcnt lgkmcnt(4)\n\t" \
  "v_smfmac_f32_16x16x64_bf16 v[72:75], v[52:55], v[24:31], v104\n\t" \
  "v_add_f32 v116, v68, v70\n\t" \
  "v_add_f32 v117, v69, v71\n\t" \
  "v_smfmac_f32_16x16x64_bf16 v[76:79], v[56:59], a[80:87], v104\n\t" \
  "v_cvt_pk_bf16_f32 v116, v116, v117\n\t" \
  "ds_read_b128 v[24:27], v102 offset:34816\n\t" \
  "v_smfmac_f32_16x16x64_bf16 v[80:83], v[56:59], a[88:95], v104\n\t" \
  "v_add_f32 v117, v72, v74\n\t" \
  "v_add_f32 v118, v73, v75\n\t" \
  "v_cvt_pk_bf16_f32 v117, v117, v118\n\t" \
  "ds_write2st64_b32 v105, v116, v117 offset1:1\n\t" \
  "v_smfmac_f32_16x16x64_bf16 v[76:79], v[60:63], a[144:151], v104\n\t" \
  "v_smfmac_f32_16x16x64_bf16 v[80:83], v[60:63], a[152:159], v104\n\t" \
  "v_smfmac_f32_16x16x64_bf16 v[76:79], v[64:67], a[176:183], v104\n\t" \
  "buffer_load_dword v117, v107, %[ru], 0 offen sc1\n\t" \
  "v_smfmac_f32_16x16x64_bf16 v[80:83], v[64:67], a[184:191], v104\n\t" \
  "s_waitcnt lgkmcnt(0)\n\t" \
  "s_barrier\n\t" \
  "ds_read_b128 v[56:59], v101 offset:0\n\t" \
  "v_smfmac_f32_16x16x64_bf16 v[76:79], v[48:51], v[136:143], v104\n\t" \
  "v_smfmac_f32_16x16x64_bf16 v[80:83], v[48:51], v[144:151], v104\n\t" \
  "ds_read_b128 v[60:63], v101 offset:9216\n\t" \
  "buffer_load_dwordx4 v[84:87], v100, %[ru], s47 offen offset:0 sc1\n\t" \
  "v_smfmac_f32_16x16x64_bf16 v[76:79], v[52:55], a[240:247], v104\n\t" \
  "ds_read_b128 v[16:19], v102 offset:32768\n\t" \
  "v_smfmac_f32_16x16x64_bf16 v[80:83], v[52:55], a[248:255], v104\n\t" \
  "ds_read_b128 v[64:67], v101 offset:2048\n\t" \
  "buffer_load_dwordx4 v[88:91], v100, %[ru], s47 offen offset:1024 sc1\n\t" \
  "s_waitcnt lgkmcnt(3)\n\t" \
  "v_smfmac_f32_16x16x64_bf16 v[76:79], v[56:59], v[0:7], v104\n\t" \
  "ds_read_b128 v[20:23], v102 offset:33792\n\t" \
  "v_smfmac_f32_16x16x64_bf16 v[80:83], v[56:59], v[8:15], v104\n\t" \
  "ds_read_b128 v[48:51], v101 offset:4096\n\t" \
  "buffer_load_dwordx4 v[92:95], v100, %[ru], s47 offen offset:2048 sc1\n\t" \
  "s_waitcnt lgkmcnt(4)\n\t" \
  "v_smfmac_f32_16x16x64_bf16 v[76:79], v[60:63], a[224:231], v104\n\t" \
  "ds_read_b128 v[28:31], v102 offset:35840\n\t" \
  "v_smfmac_f32_16x16x64_bf16 v[80:83], v[60:63], a[232:239], v104\n\t" \
  "ds_read_b128 v[52:55], v101 offset:1024\n\t" \
  "buffer_load_dwordx4 v[96:99], v100, %[ru], s47 offen offset:3072 sc1\n\t" \
  "s_waitcnt lgkmcnt(4)\n\t" \
  "v_smfmac_f32_16x16x64_bf16 v[76:79], v[64:67], a[48:55], v104\n\t" \
  "ds_read_b128 v[0:3], v103 offset:0\n\t" \
  "v_smfmac_f32_16x16x64_bf16 v[80:83], v[64:67], a[56:63], v104\n\t" \
  "ds_read_b128 v[56:59], v101 offset:5120\n\t" \
  "ds_read_b128 v[4:7], v103 offset:1024\n\t" \
  "buffer_load_dwordx4 v[32:35], v100, %[ru], s97 offen offset:0 sc1\n\t" \
  "s_waitcnt lgkmcnt(5)\n\t" \
  "v_smfmac_f32_16x16x64_bf16 v[76:79], v[48:51], v[16:23], v104\n\t" \
  "ds_read_b128 v[60:63], v101 offset:8192\n\t" \
  "s_waitcnt lgkmcnt(5)\n\t" \
  "v_smfmac_f32_16x16x64_bf16 v[80:83], v[48:51], v[24:31], v104\n\t" \
  "ds_read_b128 v[8:11], v103 offset:2048\n\t" \
  "buffer_load_dwordx4 v[36:39], v100, %[ru], s97 offen offset:1024 sc1\n\t" \
  "s_waitcnt lgkmcnt(5)\n\t" \
  "v_smfmac_f32_16x16x64_bf16 v[76:79], v[52:55], a[32:39], v104\n\t" \
  "ds_read_b128 v[12:15], v103 offset:3072\n\t" \
  "v_smfmac_f32_16x16x64_bf16 v[80:83], v[52:55], a[40:47], v104\n\t" \
  "buffer_load_dwordx4 v[40:43], v100, %[ru], s97 offen offset:2048 sc1\n\t" \
  "s_waitcnt lgkmcnt(4)\n\t" \
  "v_smfmac_f32_16x16x64_bf16 v[76:79], v[56:59], a[128:135], v104\n\t" \
  "s_waitcnt lgkmcnt(2)\n\t" \
  "v_smfmac_f32_16x16x64_bf16 v[76:79], v[60:63], v[0:7], v104\n\t" \
  "buffer_load_dwordx4 v[44:47], v100, %[ru], s97 offen offset:3072 sc1\n\t" \
  "v_smfmac_f32_16x16x64_bf16 v[80:83], v[56:59], a[136:143], v104\n\t" \
  "s_waitcnt lgkmcnt(0)\n\t" \
  "v_smfmac_f32_16x16x64_bf16 v[80:83], v[60:63], v[8:15], v104\n\t" \
  "v_add_f32 v68, v76, v78\n\t" \
  "v_add_f32 v69, v77, v79\n\t" \
  "v_cvt_pk_bf16_f32 v68, v68, v69\n\t" \
  "ds_write_b32 v106, v68\n\t" \
  "s_nop 3\n\t" \
  "v_add_f32 v70, v80, v82\n\t" \
  "v_add_f32 v71, v81, v83\n\t" \
  "v_cvt_pk_bf16_f32 v70, v70, v71\n\t" \
  "ds_write_b32 v106, v70 offset:256\n\t" \
  "s_waitcnt lgkmcnt(0)\n\t" \
  "s_barrier\n\t" \
  "s_memtime s[76:77]\n\t" \
  "s_waitcnt lgkmcnt(0)\n\t" \
  "s_add_i32 s57, s76, 240\n\t" \
  "s_sleep 5\n\t" \
  "s_waitcnt vmcnt(8)\n\t" \
  "v_readfirstlane_b32 s76, v117\n\t" \
  "s_add_i32 s77, s44, 2\n\t" \
  "s_min_i32 s77, s77, 63\n\t" \
  "s_cmp_lt_i32 s76, s77\n\t" \
  "s_cselect_b32 s42, 32, 0\n\t" \
  "s_add_i32 s41, s56, 1950\n\t" \
  "s_sub_i32 s76, s57, s56\n\t" \
  "s_add_i32 s77, s43, 8\n\t" \
  "s_min_u32 s76, s76, s77\n\t" \
  "s_sub_i32 s43, s43, 4\n\t" \
  "s_max_u32 s43, s43, s76\n\t" \
  "s_sub_i32 s76, s92, s51\n\t" \
  "s_sub_i32 s77, s93, s53\n\t" \
  "s_cmp_eq_u32 s76, 0\n\t" \
  "s_cbranch_scc1 21f\n\t" \
  "v_cvt_f32_u32 v117, s76\n\t" \
  "v_cvt_f32_u32 v116, s77\n\t" \
  "v_rcp_f32 v117, v117\n\t" \
  "s_mov_b32 s51, s92\n\t" \
  "v_mul_f32 v117, v117, v116\n\t" \
  "v_max_f32 v117, 0x3f028f5c, v117\n\t" \
  "v_min_f32 v117, 0x3ffeb852, v117\n\t" \
  "v_rcp_f32 v116, v117\n\t" \
  "v_mul_f32 v117, 0x4f000000, v117\n\t" \
  "v_mul_f32 v116, 0x47800000, v116\n\t" \
  "v_cvt_u32_f32 v117, v117\n\t" \
  "v_cvt_u32_f32 v116, v116\n\t" \
  "s_mov_b32 s53, s93\n\t" \
  "v_readfirstlane_b32 s46, v117\n\t" \
  "v_readfirstlane_b32 s52, v116\n\t" \
  "21:\n\t" \
  "s_waitcnt vmcnt(0)\n\t" \
  "ds_write_b128 v101, v[84:87] offset:20480\n\t" \
  "ds_write_b128 v101, v[88:91] offset:21504\n\t" \
  "ds_write_b128 v101, v[92:95] offset:22528\n\t" \
  "ds_write_b128 v101, v[96:99] offset:23552\n\t" \
  "ds_write_b128 v101, v[32:35] offset:24576\n\t" \
  "ds_write_b128 v101, v[36:39] offset:25600\n\t" \
  "ds_write_b128 v101, v[40:43] offset:26624\n\t" \
  "ds_write_b128 v101, v[44:47] offset:27648\n\t" \
  "v_mov_b32 v0, -1\n\t" \
  "v_mov_b32 v1, -1\n\t" \
  "v_mov_b32 v2, -1\n\t" \
  "v_mov_b32 v3, -1\n\t" \
  "s_add_i32 s76, s45, 0x10000\n\t" \
  "s_and_b32 s76, s76, 0x30000\n\t" \
  "16:\n\t" \
  "s_memtime vcc\n\t" \
  "s_waitcnt lgkmcnt(0)\n\t" \
  "s_sub_i32 s79, s41, vcc_lo\n\t" \
  "s_cmp_gt_i32 s79, 64\n\t" \
  "s_cbranch_scc1 16b\n\t" \
  "s_mov_b32 exec_hi, 0\n\t" \
  "buffer_load_dword v118, v110, %[rh], s45 offen sc1\n\t" \
  "s_mov_b64 exec, -1\n\t" \
  "buffer_store_dword v0, v113, %[rh], s76 offen sc1\n\t" \
  "s_add_i32 s76, s48, 0x0\n\t" \
  "buffer_store_dwordx4 v[0:3], v109, %[rh], s76 offen sc1\n\t" \
  "s_add_i32 s76, s48, 0x1000\n\t" \
  "buffer_store_dwordx4 v[0:3], v109, %[rh], s76 offen sc1\n\t" \
  "s_add_i32 s76, s48, 0x4000\n\t" \
  "buffer_store_dwordx4 v[0:3], v109, %[rh], s76 offen sc1\n\t" \
  "s_add_i32 s76, s48, 0x5000\n\t" \
  "buffer_store_dwordx4 v[0:3], v109, %[rh], s76 offen sc1\n\t" \
  "s_add_i32 s44, s44, 1\n\t" \
  "s_and_b32 s58, s58, 4\n\t" \
  "s_and_b32 s76, s44, 64\n\t" \
  "s_or_b32 s42, s42, s76\n\t" \
  "s_cbranch_scc0 20b\n\t" \
  "s_branch 99f\n\t" \
  "14:\n\t" \
  "s_add_u32 s59, s59, 0x10000\n\t" \
  "s_cmp_lt_u32 s59, 0x100000\n\t" \
  "s_cbranch_scc1 13b\n\t" \
  "s_mov_b64 exec, -1\n\t" \
  "s_mov_b32 s42, 16\n\t" \
  "s_branch 99f\n\t" \
  "8:\n\t" \
  "s_bitcmp1_b32 s58, 1\n\t" \
  "s_cbranch_scc1 4b\n\t" \
  "s_bitcmp1_b32 s58, 2\n\t" \
  "s_cbranch_scc1 5b\n\t" \
  "v_max3_u32 v116, v32, v33, v34\n\t" \
  "v_max3_u32 v117, v35, v36, v37\n\t" \
  "v_max3_u32 v116, v116, v38, v39\n\t" \
  "v_max_u32 v116, v116, v117\n\t" \
  "v_cmp_eq_u32 vcc, -1, v116\n\t" \
  "s_cbranch_vccz 5b\n\t" \
  "s_branch 0f\n\t" \
  "9:\n\t" \
  "s_bitcmp1_b32 s58, 2\n\t" \
  "s_cbranch_scc1 6b\n\t" \
  "v_max3_u32 v116, v40, v41, v42\n\t" \
  "v_max3_u32 v117, v43, v44, v45\n\t" \
  "v_max3_u32 v116, v116, v46, v47\n\t" \
  "v_max_u32 v116, v116, v117\n\t" \
  "v_cmp_eq_u32 vcc, -1, v116\n\t" \
  "s_cbranch_vccz 6b\n\t" \
  "s_or_b32 s58, s58, 2\n\t" \
  "0:\n\t" \
  "s_or_b32 s58, s58, 1\n\t" \
  "11:\n\t" \
  "buffer_load_dwordx4 v[32:35], v100, %[rh], s40 offen sc1\n\t" \
  "buffer_load_dwordx4 v[36:39], v100, %[rh], s76 offen sc1\n\t" \
  "buffer_load_dwordx4 v[40:43], v100, %[rh], s77 offen sc1\n\t" \
  "buffer_load_dwordx4 v[44:47], v100, %[rh], s78 offen sc1\n\t" \
  "s_waitcnt lgkmcnt(0)\n\t" \
  "s_branch 10b\n\t" \
  "17:\n\t" \
  "v_mov_b64 v[68:69], 0\n\t" \
  "v_mov_b64 v[70:71], 0\n\t" \
  "v_mov_b64 v[72:73], 0\n\t" \
  "v_mov_b64 v[74:75], 0\n\t" \
  "v_mov_b64 v[76:77], 0\n\t" \
  "v_mov_b64 v[78:79], 0\n\t" \
  "v_mov_b64 v[80:81], 0\n\t" \
  "v_mov_b64 v[82:83], 0\n\t" \
  "s_add_u32 s59, s59, 1\n\t" \
  "s_bitcmp1_b32 s58, 2\n\t" \
  "s_cbranch_scc1 3b\n\t" \
  "s_waitcnt vmcnt(0)\n\t" \
  "v_max_u32 v116, v32, v33\n\t" \
  "v_max3_u32 v116, v116, v34, v35\n\t" \
  "v_max3_u32 v116, v116, v36, v37\n\t" \
  "v_max3_u32 v116, v116, v38, v39\n\t" \
  "v_max3_u32 v116, v116, v40, v41\n\t" \
  "v_max3_u32 v116, v116, v42, v43\n\t" \
  "v_max3_u32 v116, v116, v44, v45\n\t" \
  "v_max3_u32 v116, v116, v46, v47\n\t" \
  "v_cmp_eq_u32 vcc, -1, v116\n\t" \
  "s_cbranch_vccz 3b\n\t" \
  "s_bitcmp1_b32 s59, 6\n\t" \
  "s_cbranch_scc1 12f\n\t" \
  "buffer_load_dwordx4 v[32:35], v100, %[rh], s40 offen sc1\n\t" \
  "buffer_load_dwordx4 v[36:39], v100, %[rh], s76 offen sc1\n\t" \
  "buffer_load_dwordx4 v[40:43], v100, %[rh], s77 offen sc1\n\t" \
  "buffer_load_dwordx4 v[44:47], v100, %[rh], s78 offen sc1\n\t" \
  "s_branch 17b\n\t" \
  "12:\n\t" \
  "s_mov_b32 s42, s58\n\t" \
  "s_branch 99f\n\t" \
  "99:\n\t" \
  "s_waitcnt vmcnt(0) lgkmcnt(0)\n\t" \
  ""
#define VO_BODY \
  "s_cmp_eq_u32 %[wv], 1\n\t" \
  "s_cbranch_scc1 91f\n\t" \
  "s_cmp_eq_u32 %[wv], 2\n\t" \
  "s_cbranch_scc1 92f\n\t" \
  "s_cmp_eq_u32 %[wv], 3\n\t" \
  "s_cbranch_scc1 93f\n\t" \
  "90:\n\t" \
  "s_mov_b32 s58, s42\n\t" \
  ".p2align 6\n\t" \
  "20:\n\t" \
  "s_and_b32 s76, s44, 7\n\t" \
  "s_lshl_b32 s76, s76, 15\n\t" \
  "s_add_i32 s40, s54, s76\n\t" \
  "s_and_b32 s76, s44, 3\n\t" \
  "s_lshl_b32 s45, s76, 16\n\t" \
  "s_and_b32 s47, s44, 3\n\t" \
  "s_lshl_b32 s76, s47, 2\n\t" \
  "s_lshl_b32 s47, s47, 20\n\t" \
  "s_add_i32 s47, s47, s55\n\t" \
  "s_lshr_b32 s76, %[um], s76\n\t" \
  "s_and_b32 s76, s76, 7\n\t" \
  "s_lshl_b32 s76, s76, 12\n\t" \
  "s_xor_b32 s47, s47, s76\n\t" \
  "s_add_i32 s49, s44, -1\n\t" \
  "s_and_b32 s48, s49, 3\n\t" \
  "s_lshl_b32 s48, s48, 16\n\t" \
  "s_lshl_b32 s50, s53, 8\n\t" \
  "s_mov_b32 s59, 0\n\t" \
  "s_mov_b32 s92, s51\n\t" \
  "s_mov_b32 s93, s53\n\t" \
  "s_xor_b32 s76, s40, 0x1000\n\t" \
  "s_xor_b32 s77, s40, 0x4000\n\t" \
  "s_xor_b32 s78, s40, 0x5000\n\t" \
  "s_cmp_lg_u32 s58, 0\n\t" \
  "s_cbranch_scc1 11f\n\t" \
  "10:\n\t" \
  "ds_read_b128 v[48:51], v102 offset:0\n\t" \
  "ds_read_b128 v[52:55], v102 offset:1024\n\t" \
  "ds_read_b128 v[56:59], v102 offset:2048\n\t" \
  "ds_read_b128 v[60:63], v102 offset:3072\n\t" \
  "ds_read_b128 v[0:3], v102 offset:16384\n\t" \
  "ds_read_b128 v[4:7], v102 offset:17408\n\t" \
  "ds_read_b128 v[8:11], v102 offset:18432\n\t" \
  "ds_read_b128 v[12:15], v102 offset:19456\n\t" \
  "ds_read_b128 v[16:19], v102 offset:32768\n\t" \
  "ds_read_b128 v[20:23], v102 offset:33792\n\t" \
  "ds_read_b128 v[24:27], v102 offset:34816\n\t" \
  "ds_read_b128 v[28:31], v102 offset:35840\n\t" \
  "s_waitcnt lgkmcnt(4)\n\t" \
  "ds_read_b128 v[80:83], v102 offset:49152\n\t" \
  "ds_read_b128 v[84:87], v102 offset:50176\n\t" \
  "ds_read_b128 v[88:91], v102 offset:51200\n\t" \
  "ds_read_b128 v[92:95], v102 offset:52224\n\t" \
  "s_bitcmp1_b32 s58, 1\n\t" \
  "s_cselect_b64 s[80:81], -1, 0\n\t" \
  "s_cmp_lg_u32 s58, 0\n\t" \
  "s_cbranch_scc1 17f\n\t" \
  "s_mov_b32 exec_hi, 0\n\t" \
  "16:\n\t" \
  "s_memtime vcc\n\t" \
  "s_waitcnt lgkmcnt(0)\n\t" \
  "s_sub_i32 s79, s41, vcc_lo\n\t" \
  "s_cmp_gt_i32 s79, 64\n\t" \
  "s_cbranch_scc1 16b\n\t" \
  "13:\n\t" \
  "buffer_load_dword v116, v110, %[rh], s48 offen sc1\n\t" \
  "s_waitcnt vmcnt(0)\n\t" \
  "15:\n\t" \
  "v_mov_b32 v117, 20000\n\t" \
  "v_cmp_ne_u32_sdwa vcc, v116, s49 src0_sel:BYTE_0 src1_sel:DWORD\n\t" \
  "v_subrev_u32 v116, s50, v116\n\t" \
  "v_ashrrev_i32 v116, 8, v116\n\t" \
  "s_nop 1\n\t" \
  "v_max_i32_dpp v116, v116, v116 quad_perm:[1,0,3,2] row_mask:0xf bank_mask:0xf\n\t" \
  "s_cbranch_vccnz 14f\n\t" \
  "s_nop 0\n\t" \
  "v_max_i32_dpp v116, v116, v116 quad_perm:[2,3,0,1] row_mask:0xf bank_mask:0xf\n\t" \
  "s_nop 1\n\t" \
  "v_max_i32_dpp v116, v116, v116 row_half_mirror row_mask:0xf bank_mask:0xf\n\t" \
  "s_nop 1\n\t" \
  "v_max_i32_dpp v116, v116, v116 row_mirror row_mask:0xf bank_mask:0xf\n\t" \
  "v_med3_i32 v116, v116, 0, v117\n\t" \
  "v_mul_u32_u24 v116, s52, v116\n\t" \
  "v_lshrrev_b32 v116, 16, v116\n\t" \
  "s_mov_b64 exec, -1\n\t" \
  "v_readlane_b32 s84, v116, 0\n\t" \
  "v_readlane_b32 s85, v116, 16\n\t" \
  "s_max_u32 s84, s84, s85\n\t" \
  "s_add_i32 s84, s84, s51\n\t" \
  "v_mov_b64 v[72:73], 0\n\t" \
  "v_mov_b64 v[74:75], 0\n\t" \
  "v_mov_b64 v[76:77], 0\n\t" \
  "v_mov_b64 v[78:79], 0\n\t" \
  "1:\n\t" \
  "s_memtime vcc\n\t" \
  "s_waitcnt lgkmcnt(0)\n\t" \
  "s_sub_i32 s79, s84, vcc_lo\n\t" \
  "s_cmp_gt_i32 s79, 128\n\t" \
  "s_cbranch_scc1 1b\n\t" \
  "2:\n\t" \
  "s_sub_i32 s79, s79, 33\n\t" \
  "s_cmp_gt_i32 s79, 56\n\t" \
  "s_cbranch_scc1 2b\n\t" \
  "buffer_load_dwordx4 v[32:35], v100, %[rh], s40 offen sc1\n\t" \
  "s_nop 12\n\t" \
  "buffer_load_dwordx4 v[36:39], v100, %[rh], s76 offen sc1\n\t" \
  "s_nop 12\n\t" \
  "buffer_load_dwordx4 v[40:43], v100, %[rh], s77 offen sc1\n\t" \
  "s_nop 12\n\t" \
  "buffer_load_dwordx4 v[44:47], v100, %[rh], s78 offen sc1\n\t" \
  "s_memtime vcc\n\t" \
  "s_memrealtime s[82:83]\n\t" \
  "s_nop 4\n\t" \
  "s_memrealtime s[84:85]\n\t" \
  "s_nop 4\n\t" \
  "s_memrealtime s[94:95]\n\t" \
  "s_nop 4\n\t" \
  "s_memrealtime s[86:87]\n\t" \
  "s_nop 4\n\t" \
  "s_memrealtime s[88:89]\n\t" \
  "s_nop 4\n\t" \
  "s_memrealtime s[90:91]\n\t" \
  "s_mov_b32 s92, 0\n\t" \
  "s_waitcnt lgkmcnt(0)\n\t" \
  "s_cmp_eq_u32 s82, s84\n\t" \
  "s_addc_u32 s92, s92, 0\n\t" \
  "s_cmp_eq_u32 s82, s94\n\t" \
  "s_addc_u32 s92, s92, 0\n\t" \
  "s_cmp_eq_u32 s82, s86\n\t" \
  "s_addc_u32 s92, s92, 0\n\t" \
  "s_cmp_eq_u32 s82, s88\n\t" \
  "s_addc_u32 s92, s92, 0\n\t" \
  "s_cmp_eq_u32 s82, s90\n\t" \
  "s_addc_u32 s92, s92, 0\n\t" \
  "s_mul_i32 s92, s92, 24\n\t" \
  "s_add_i32 s92, s92, vcc_lo\n\t" \
  "s_lshl4_add_u32 s93, s82, 64\n\t" \
  "3:\n\t" \
  "s_waitcnt lgkmcnt(0)\n\t" \
  "s_waitcnt vmcnt(3)\n\t" \
  "v_smfmac_f32_16x16x64_bf16 v[72:75], v[32:35], v[48:55], v104\n\t" \
  "ds_write_b128 v101, v[32:35] offset:0\n\t" \
  "v_smfmac_f32_16x16x64_bf16 v[76:79], v[32:35], v[56:63], v104\n\t" \
  "s_waitcnt vmcnt(2)\n\t" \
  "ds_write_b128 v101, v[36:39] offset:1024\n\t" \
  "v_smfmac_f32_16x16x64_bf16 v[72:75], v[36:39], a[0:7], v104\n\t" \
  "v_smfmac_f32_16x16x64_bf16 v[76:79], v[36:39], a[8:15], v104\n\t" \
  "s_nop 3\n\t" \
  "v_cmp_u_f32 vcc, v72, v73\n\t" \
  "v_cmp_u_f32 s[82:83], v74, v75\n\t" \
  "s_waitcnt vmcnt(1)\n\t" \
  "v_smfmac_f32_16x16x64_bf16 v[72:75], v[40:43], a[16:23], v104\n\t" \
  "s_or_b64 vcc, vcc, s[82:83]\n\t" \
  "s_or_b64 vcc, vcc, s[80:81]\n\t" \
  "v_smfmac_f32_16x16x64_bf16 v[76:79], v[40:43], a[24:31], v104\n\t" \
  "s_waitcnt lgkmcnt(0)\n\t" \
  "s_cbranch_scc1 8f\n\t" \
  "5:\n\t" \
  "s_barrier\n\t" \
  "4:\n\t" \
  "s_memtime s[56:57]\n\t" \
  "ds_read_b128 v[48:51], v101 offset:4096\n\t" \
  "ds_read_b128 v[52:55], v101 offset:5120\n\t" \
  "ds_read_b128 v[56:59], v101 offset:9216\n\t" \
  "s_waitcnt vmcnt(0)\n\t" \
  "v_smfmac_f32_16x16x64_bf16 v[72:75], v[44:47], a[32:39], v104\n\t" \
  "ds_read_b128 v[60:63], v101 offset:8192\n\t" \
  "ds_read_b128 v[64:67], v101 offset:13312\n\t" \
  "v_smfmac_f32_16x16x64_bf16 v[76:79], v[44:47], a[40:47], v104\n\t" \
  "ds_read_b128 v[68:71], v101 offset:12288\n\t" \
  "ds_write_b128 v101, v[40:43] offset:2048\n\t" \
  "s_nop 1\n\t" \
  "v_cmp_u_f32 vcc, v72, v73\n\t" \
  "v_cmp_u_f32 s[82:83], v74, v75\n\t" \
  "s_waitcnt lgkmcnt(6)\n\t" \
  "v_smfmac_f32_16x16x64_bf16 v[72:75], v[48:51], v[0:7], v104\n\t" \
  "s_or_b64 vcc, vcc, s[82:83]\n\t" \
  "v_smfmac_f32_16x16x64_bf16 v[76:79], v[48:51], v[8:15], v104\n\t" \
  "s_cbranch_scc1 9f\n\t" \
  "6:\n\t" \
  "s_waitcnt lgkmcnt(5)\n\t" \
  "v_smfmac_f32_16x16x64_bf16 v[72:75], v[52:55], a[48:55], v104\n\t" \
  "ds_write_b128 v101, v[44:47] offset:3072\n\t" \
  "v_smfmac_f32_16x16x64_bf16 v[76:79], v[52:55], a[56:63], v104\n\t" \
  "v_mov_b64 v[40:41], 0\n\t" \
  "s_waitcnt lgkmcnt(5)\n\t" \
  "v_smfmac_f32_16x16x64_bf16 v[72:75], v[56:59], a[96:103], v104\n\t" \
  "v_mov_b64 v[42:43], 0\n\t" \
  "v_smfmac_f32_16x16x64_bf16 v[76:79], v[56:59], a[104:111], v104\n\t" \
  "s_waitcnt lgkmcnt(4)\n\t" \
  "v_smfmac_f32_16x16x64_bf16 v[72:75], v[60:63], v[16:23], v104\n\t" \
  "s_waitcnt lgkmcnt(0)\n\t" \
  "s_barrier\n\t" \
  "v_smfmac_f32_16x16x64_bf16 v[76:79], v[60:63], v[24:31], v104\n\t" \
  "ds_read_b128 v[48:51], v101 offset:6144\n\t" \
  "ds_read_b128 v[52:55], v101 offset:10240\n\t" \
  "v_smfmac_f32_16x16x64_bf16 v[72:75], v[64:67], a[144:151], v104\n\t" \
  "ds_read_b128 v[56:59], v101 offset:14336\n\t" \
  "ds_read_b128 v[60:63], v101 offset:7168\n\t" \
  "v_smfmac_f32_16x16x64_bf16 v[76:79], v[64:67], a[152:159], v104\n\t" \
  "ds_read_b128 v[32:35], v101 offset:11264\n\t" \
  "ds_read_b128 v[36:39], v101 offset:15360\n\t" \
  "v_smfmac_f32_16x16x64_bf16 v[72:75], v[68:71], v[80:87], v104\n\t" \
  "ds_read_b128 v[0:3], v103 offset:0\n\t" \
  "v_mov_b64 v[44:45], 0\n\t" \
  "v_smfmac_f32_16x16x64_bf16 v[76:79], v[68:71], v[88:95], v104\n\t" \
  "v_mov_b64 v[46:47], 0\n\t" \
  "s_waitcnt lgkmcnt(5)\n\t" \
  "v_smfmac_f32_16x16x64_bf16 v[72:75], v[48:51], a[64:71], v104\n\t" \
  "ds_read_b128 v[4:7], v103 offset:1024\n\t" \
  "v_mov_b64 v[80:81], 0\n\t" \
  "v_smfmac_f32_16x16x64_bf16 v[72:75], v[52:55], a[112:119], v104\n\t" \
  "v_mov_b64 v[82:83], 0\n\t" \
  "s_waitcnt lgkmcnt(2)\n\t" \
  "v_smfmac_f32_16x16x64_bf16 v[72:75], v[56:59], a[160:167], v104\n\t" \
  "ds_read_b128 v[8:11], v103 offset:2048\n\t" \
  "v_mov_b64 v[84:85], 0\n\t" \
  "v_smfmac_f32_16x16x64_bf16 v[72:75], v[60:63], a[80:87], v104\n\t" \
  "ds_read_b128 v[12:15], v103 offset:3072\n\t" \
  "v_mov_b64 v[86:87], 0\n\t" \
  "v_smfmac_f32_16x16x64_bf16 v[72:75], v[32:35], a[128:135], v104\n\t" \
  "ds_read_b128 v[16:19], v103 offset:16384\n\t" \
  "v_mov_b64 v[88:89], 0\n\t" \
  "v_smfmac_f32_16x16x64_bf16 v[72:75], v[36:39], a[176:183], v104\n\t" \
  "ds_read_b128 v[20:23], v103 offset:17408\n\t" \
  "v_mov_b64 v[90:91], 0\n\t" \
  "v_smfmac_f32_16x16x64_bf16 v[76:79], v[48:51], a[72:79], v104\n\t" \
  "ds_read_b128 v[24:27], v103 offset:18432\n\t" \
  "v_mov_b64 v[92:93], 0\n\t" \
  "v_smfmac_f32_16x16x64_bf16 v[76:79], v[52:55], a[120:127], v104\n\t" \
  "v_add_f32 v96, v72, v74\n\t" \
  "v_add_f32 v97, v73, v75\n\t" \
  "v_cvt_pk_bf16_f32 v96, v96, v97\n\t" \
  "ds_write_b32 v105, v96\n\t" \
  "v_smfmac_f32_16x16x64_bf16 v[76:79], v[56:59], a[168:175], v104\n\t" \
  "ds_read_b128 v[28:31], v103 offset:19456\n\t" \
  "v_mov_b64 v[94:95], 0\n\t" \
  "v_smfmac_f32_16x16x64_bf16 v[76:79], v[60:63], a[88:95], v104\n\t" \
  "v_smfmac_f32_16x16x64_bf16 v[76:79], v[32:35], a[136:143], v104\n\t" \
  "v_smfmac_f32_16x16x64_bf16 v[76:79], v[36:39], a[184:191], v104\n\t" \
  "s_waitcnt lgkmcnt(1)\n\t" \
  "s_barrier\n\t" \
  "ds_read_b64_tr_b16 v[96:97], v107 offset:0\n\t" \
  "ds_read_b64_tr_b16 v[98:99], v107 offset:64\n\t" \
  "ds_read_b64_tr_b16 v[56:57], v107 offset:1024\n\t" \
  "ds_read_b64_tr_b16 v[58:59], v107 offset:1088\n\t" \
  "s_nop 1\n\t" \
  "v_add_f32 v116, v76, v78\n\t" \
  "v_add_f32 v117, v77, v79\n\t" \
  "v_cvt_pk_bf16_f32 v116, v116, v117\n\t" \
  "ds_write_b32 v105, v116 offset:256\n\t" \
  "s_waitcnt lgkmcnt(3)\n\t" \
  "v_smfmac_f32_16x16x64_bf16 v[40:43], v[96:99], a[192:199], v106\n\t" \
  "v_smfmac_f32_16x16x64_bf16 v[44:47], v[96:99], a[200:207], v106\n\t" \
  "s_waitcnt lgkmcnt(0)\n\t" \
  "s_barrier\n\t" \
  "ds_read_b64_tr_b16 v[52:53], v107 offset:256\n\t" \
  "ds_read_b64_tr_b16 v[54:55], v107 offset:320\n\t" \
  "v_smfmac_f32_16x16x64_bf16 v[40:43], v[56:59], a[224:231], v106\n\t" \
  "ds_read_b64_tr_b16 v[60:61], v107 offset:1280\n\t" \
  "ds_read_b64_tr_b16 v[62:63], v107 offset:1344\n\t" \
  "v_smfmac_f32_16x16x64_bf16 v[44:47], v[56:59], a[232:239], v106\n\t" \
  "v_smfmac_f32_16x16x64_bf16 v[80:83], v[96:99], a[240:247], v106\n\t" \
  "v_mov_b64 v[32:33], 0\n\t" \
  "v_smfmac_f32_16x16x64_bf16 v[84:87], v[96:99], a[248:255], v106\n\t" \
  "v_mov_b64 v[34:35], 0\n\t" \
  "v_smfmac_f32_16x16x64_bf16 v[80:83], v[56:59], v[136:143], v106\n\t" \
  "v_mov_b64 v[36:37], 0\n\t" \
  "v_smfmac_f32_16x16x64_bf16 v[84:87], v[56:59], v[144:151], v106\n\t" \
  "s_waitcnt lgkmcnt(2)\n\t" \
  "v_smfmac_f32_16x16x64_bf16 v[40:43], v[52:55], a[208:215], v106\n\t" \
  "v_smfmac_f32_16x16x64_bf16 v[44:47], v[52:55], a[216:223], v106\n\t" \
  "s_xor_b32 s76, s47, 0x40000\n\t" \
  "s_waitcnt lgkmcnt(0)\n\t" \
  "v_smfmac_f32_16x16x64_bf16 v[40:43], v[60:63], v[0:7], v106\n\t" \
  "v_mov_b64 v[38:39], 0\n\t" \
  "s_xor_b32 s77, s47, 0x84000\n\t" \
  "v_smfmac_f32_16x16x64_bf16 v[44:47], v[60:63], v[8:15], v106\n\t" \
  "s_xor_b32 s78, s47, 0xc4000\n\t" \
  "v_smfmac_f32_16x16x64_bf16 v[80:83], v[52:55], v[120:127], v106\n\t" \
  "ds_read_b128 v[0:3], v103 offset:32768\n\t" \
  "v_cvt_pk_bf16_f32 v64, v40, v41\n\t" \
  "v_smfmac_f32_16x16x64_bf16 v[84:87], v[52:55], v[128:135], v106\n\t" \
  "ds_read_b128 v[4:7], v103 offset:33792\n\t" \
  "v_cvt_pk_bf16_f32 v66, v42, v43\n\t" \
  "v_smfmac_f32_16x16x64_bf16 v[80:83], v[60:63], v[16:23], v106\n\t" \
  "ds_read_b128 v[8:11], v103 offset:34816\n\t" \
  "v_cvt_pk_bf16_f32 v65, v44, v45\n\t" \
  "v_smfmac_f32_16x16x64_bf16 v[84:87], v[60:63], v[24:31], v106\n\t" \
  "ds_read_b128 v[12:15], v103 offset:35840\n\t" \
  "v_cvt_pk_bf16_f32 v67, v46, v47\n\t" \
  "v_smfmac_f32_16x16x64_bf16 v[88:91], v[96:99], v[152:159], v106\n\t" \
  "buffer_store_dwordx4 v[64:67], v108, %[ru], s47 offen sc1\n\t" \
  "v_cvt_pk_bf16_f32 v68, v80, v81\n\t" \
  "v_smfmac_f32_16x16x64_bf16 v[92:95], v[96:99], v[160:167], v106\n\t" \
  "v_cvt_pk_bf16_f32 v70, v82, v83\n\t" \
  "v_smfmac_f32_16x16x64_bf16 v[88:91], v[52:55], v[168:175], v106\n\t" \
  "v_cvt_pk_bf16_f32 v69, v84, v85\n\t" \
  "v_smfmac_f32_16x16x64_bf16 v[92:95], v[52:55], v[176:183], v106\n\t" \
  "v_cvt_pk_bf16_f32 v71, v86, v87\n\t" \
  "v_smfmac_f32_16x16x64_bf16 v[88:91], v[56:59], v[184:191], v106\n\t" \
  "ds_read_b128 v[16:19], v103 offset:49152\n\t" \
  "buffer_store_dwordx4 v[68:71], v108, %[ru], s76 offen sc1\n\t" \
  "v_smfmac_f32_16x16x64_bf16 v[92:95], v[56:59], v[192:199], v106\n\t" \
  "ds_read_b128 v[20:23], v103 offset:50176\n\t" \
  "s_waitcnt lgkmcnt(4)\n\t" \
  "v_smfmac_f32_16x16x64_bf16 v[88:91], v[60:63], v[0:7], v106\n\t" \
  "ds_read_b128 v[24:27], v103 offset:51200\n\t" \
  "s_waitcnt lgkmcnt(3)\n\t" \
  "v_smfmac_f32_16x16x64_bf16 v[92:95], v[60:63], v[8:15], v106\n\t" \
  "ds_read_b128 v[28:31], v103 offset:52224\n\t" \
  "v_smfmac_f32_16x16x64_bf16 v[32:35], v[96:99], v[200:207], v106\n\t" \
  "v_cvt_pk_bf16_f32 v72, v88, v89\n\t" \
  "v_smfmac_f32_16x16x64_bf16 v[36:39], v[96:99], v[208:215], v106\n\t" \
  "v_cvt_pk_bf16_f32 v74, v90, v91\n\t" \
  "v_smfmac_f32_16x16x64_bf16 v[32:35], v[52:55], v[216:223], v106\n\t" \
  "v_cvt_pk_bf16_f32 v73, v92, v93\n\t" \
  "v_smfmac_f32_16x16x64_bf16 v[36:39], v[52:55], v[224:231], v106\n\t" \
  "v_cvt_pk_bf16_f32 v75, v94, v95\n\t" \
  "v_smfmac_f32_16x16x64_bf16 v[32:35], v[56:59], v[232:239], v106\n\t" \
  "buffer_store_dwordx4 v[72:75], v108, %[ru], s77 offen sc1\n\t" \
  "s_waitcnt lgkmcnt(2)\n\t" \
  "v_smfmac_f32_16x16x64_bf16 v[32:35], v[60:63], v[16:23], v106\n\t" \
  "v_smfmac_f32_16x16x64_bf16 v[36:39], v[56:59], v[240:247], v106\n\t" \
  "s_waitcnt lgkmcnt(0)\n\t" \
  "v_smfmac_f32_16x16x64_bf16 v[36:39], v[60:63], v[24:31], v106\n\t" \
  "v_cvt_pk_bf16_f32 v76, v32, v33\n\t" \
  "v_cvt_pk_bf16_f32 v78, v34, v35\n\t" \
  "s_nop 5\n\t" \
  "v_cvt_pk_bf16_f32 v77, v36, v37\n\t" \
  "v_cvt_pk_bf16_f32 v79, v38, v39\n\t" \
  "buffer_store_dwordx4 v[76:79], v108, %[ru], s78 offen sc1\n\t" \
  "s_memtime s[76:77]\n\t" \
  "s_mov_b32 s42, 0\n\t" \
  "s_waitcnt lgkmcnt(0)\n\t" \
  "s_mov_b32 s57, s76\n\t" \
  "s_add_i32 s41, s56, 1950\n\t" \
  "s_sub_i32 s76, s92, s51\n\t" \
  "s_sub_i32 s77, s93, s53\n\t" \
  "s_cmp_eq_u32 s76, 0\n\t" \
  "s_cbranch_scc1 21f\n\t" \
  "v_cvt_f32_u32 v117, s76\n\t" \
  "v_cvt_f32_u32 v116, s77\n\t" \
  "v_rcp_f32 v117, v117\n\t" \
  "s_mov_b32 s51, s92\n\t" \
  "v_mul_f32 v117, v117, v116\n\t" \
  "v_max_f32 v117, 0x3f028f5c, v117\n\t" \
  "v_min_f32 v117, 0x3ffeb852, v117\n\t" \
  "v_rcp_f32 v116, v117\n\t" \
  "v_mul_f32 v117, 0x4f000000, v117\n\t" \
  "v_mul_f32 v116, 0x47800000, v116\n\t" \
  "v_cvt_u32_f32 v117, v117\n\t" \
  "v_cvt_u32_f32 v116, v116\n\t" \
  "s_mov_b32 s53, s93\n\t" \
  "v_readfirstlane_b32 s46, v117\n\t" \
  "v_readfirstlane_b32 s52, v116\n\t" \
  "21:\n\t" \
  "s_add_i32 s44, s44, 1\n\t" \
  "s_and_b32 s58, s58, 4\n\t" \
  "s_and_b32 s76, s44, 64\n\t" \
  "s_or_b32 s42, s42, s76\n\t" \
  "s_cbranch_scc0 20b\n\t" \
  "s_branch 99f\n\t" \
  "14:\n\t" \
  "s_add_u32 s59, s59, 0x10000\n\t" \
  "s_cmp_lt_u32 s59, 0x100000\n\t" \
  "s_cbranch_scc1 13b\n\t" \
  "s_mov_b64 exec, -1\n\t" \
  "s_mov_b32 s42, 16\n\t" \
  "s_branch 99f\n\t" \
  "8:\n\t" \
  "s_bitcmp1_b32 s58, 1\n\t" \
  "s_cbranch_scc1 4b\n\t" \
  "s_bitcmp1_b32 s58, 2\n\t" \
  "s_cbranch_scc1 5b\n\t" \
  "v_max3_u32 v116, v32, v33, v34\n\t" \
  "v_max3_u32 v117, v35, v36, v37\n\t" \
  "v_max3_u32 v116, v116, v38, v39\n\t" \
  "v_max_u32 v116, v116, v117\n\t" \
  "v_cmp_eq_u32 vcc, -1, v116\n\t" \
  "s_cbranch_vccz 5b\n\t" \
  "s_branch 0f\n\t" \
  "9:\n\t" \
  "s_bitcmp1_b32 s58, 2\n\t" \
  "s_cbranch_scc1 6b\n\t" \
  "v_max3_u32 v116, v40, v41, v42\n\t" \
  "v_max3_u32 v117, v43, v44, v45\n\t" \
  "v_max3_u32 v116, v116, v46, v47\n\t" \
  "v_max_u32 v116, v116, v117\n\t" \
  "v_cmp_eq_u32 vcc, -1, v116\n\t" \
  "s_cbranch_vccz 6b\n\t" \
  "s_or_b32 s58, s58, 2\n\t" \
  "0:\n\t" \
  "s_or_b32 s58, s58, 1\n\t" \
  "11:\n\t" \
  "buffer_load_dwordx4 v[32:35], v100, %[rh], s40 offen sc1\n\t" \
  "buffer_load_dwordx4 v[36:39], v100, %[rh], s76 offen sc1\n\t" \
  "buffer_load_dwordx4 v[40:43], v100, %[rh], s77 offen sc1\n\t" \
  "buffer_load_dwordx4 v[44:47], v100, %[rh], s78 offen sc1\n\t" \
  "s_waitcnt lgkmcnt(0)\n\t" \
  "s_branch 10b\n\t" \
  "17:\n\t" \
  "v_mov_b64 v[72:73], 0\n\t" \
  "v_mov_b64 v[74:75], 0\n\t" \
  "v_mov_b64 v[76:77], 0\n\t" \
  "v_mov_b64 v[78:79], 0\n\t" \
  "s_add_u32 s59, s59, 1\n\t" \
  "s_bitcmp1_b32 s58, 2\n\t" \
  "s_cbranch_scc1 3b\n\t" \
  "s_waitcnt vmcnt(0)\n\t" \
  "v_max_u32 v116, v32, v33\n\t" \
  "v_max3_u32 v116, v116, v34, v35\n\t" \
  "v_max3_u32 v116, v116, v36, v37\n\t" \
  "v_max3_u32 v116, v116, v38, v39\n\t" \
  "v_max3_u32 v116, v116, v40, v41\n\t" \
  "v_max3_u32 v116, v116, v42, v43\n\t" \
  "v_max3_u32 v116, v116, v44, v45\n\t" \
  "v_max3_u32 v116, v116, v46, v47\n\t" \
  "v_cmp_eq_u32 vcc, -1, v116\n\t" \
  "s_cbranch_vccz 3b\n\t" \
  "s_bitcmp1_b32 s59, 6\n\t" \
  "s_cbranch_scc1 12f\n\t" \
  "buffer_load_dwordx4 v[32:35], v100, %[rh], s40 offen sc1\n\t" \
  "buffer_load_dwordx4 v[36:39], v100, %[rh], s76 offen sc1\n\t" \
  "buffer_load_dwordx4 v[40:43], v100, %[rh], s77 offen sc1\n\t" \
  "buffer_load_dwordx4 v[44:47], v100, %[rh], s78 offen sc1\n\t" \
  "s_branch 17b\n\t" \
  "12:\n\t" \
  "s_mov_b32 s42, s58\n\t" \
  "s_branch 99f\n\t" \
  "91:\n\t" \
  "s_mov_b32 s58, s42\n\t" \
  ".p2align 6\n\t" \
  "20:\n\t" \
  "s_and_b32 s76, s44, 7\n\t" \
  "s_lshl_b32 s76, s76, 15\n\t" \
  "s_add_i32 s40, s54, s76\n\t" \
  "s_and_b32 s76, s44, 3\n\t" \
  "s_lshl_b32 s45, s76, 16\n\t" \
  "s_and_b32 s47, s44, 3\n\t" \
  "s_lshl_b32 s76, s47, 2\n\t" \
  "s_lshl_b32 s47, s47, 20\n\t" \
  "s_add_i32 s47, s47, s55\n\t" \
  "s_lshr_b32 s76, %[um], s76\n\t" \
  "s_and_b32 s76, s76, 7\n\t" \
  "s_lshl_b32 s76, s76, 12\n\t" \
  "s_xor_b32 s47, s47, s76\n\t" \
  "s_add_i32 s49, s44, -1\n\t" \
  "s_and_b32 s48, s49, 3\n\t" \
  "s_lshl_b32 s48, s48, 16\n\t" \
  "s_lshl_b32 s50, s53, 8\n\t" \
  "s_mov_b32 s59, 0\n\t" \
  "s_mov_b32 s92, s51\n\t" \
  "s_mov_b32 s93, s53\n\t" \
  "s_xor_b32 s76, s40, 0x1000\n\t" \
  "s_xor_b32 s77, s40, 0x4000\n\t" \
  "s_xor_b32 s78, s40, 0x5000\n\t" \
  "s_cmp_lg_u32 s58, 0\n\t" \
  "s_cbranch_scc1 11f\n\t" \
  "10:\n\t" \
  "ds_read_b128 v[48:51], v102 offset:16384\n\t" \
  "ds_read_b128 v[52:55], v102 offset:17408\n\t" \
  "ds_read_b128 v[56:59], v102 offset:18432\n\t" \
  "ds_read_b128 v[60:63], v102 offset:19456\n\t" \
  "ds_read_b128 v[0:3], v102 offset:32768\n\t" \
  "ds_read_b128 v[4:7], v102 offset:33792\n\t" \
  "ds_read_b128 v[8:11], v102 offset:34816\n\t" \
  "ds_read_b128 v[12:15], v102 offset:35840\n\t" \
  "ds_read_b128 v[16:19], v102 offset:49152\n\t" \
  "ds_read_b128 v[20:23], v102 offset:50176\n\t" \
  "ds_read_b128 v[24:27], v102 offset:51200\n\t" \
  "ds_read_b128 v[28:31], v102 offset:52224\n\t" \
  "s_waitcnt lgkmcnt(4)\n\t" \
  "ds_read_b128 v[80:83], v102 offset:0\n\t" \
  "ds_read_b128 v[84:87], v102 offset:1024\n\t" \
  "ds_read_b128 v[88:91], v102 offset:2048\n\t" \
  "ds_read_b128 v[92:95], v102 offset:3072\n\t" \
  "s_bitcmp1_b32 s58, 1\n\t" \
  "s_cselect_b64 s[80:81], -1, 0\n\t" \
  "s_cmp_lg_u32 s58, 0\n\t" \
  "s_cbranch_scc1 17f\n\t" \
  "s_mov_b32 exec_hi, 0\n\t" \
  "16:\n\t" \
  "s_memtime vcc\n\t" \
  "s_waitcnt lgkmcnt(0)\n\t" \
  "s_sub_i32 s79, s41, vcc_lo\n\t" \
  "s_cmp_gt_i32 s79, 64\n\t" \
  "s_cbranch_scc1 16b\n\t" \
  "13:\n\t" \
  "buffer_load_dword v116, v110, %[rh], s48 offen sc1\n\t" \
  "s_waitcnt vmcnt(0)\n\t" \
  "15:\n\t" \
  "v_mov_b32 v117, 20000\n\t" \
  "v_cmp_ne_u32_sdwa vcc, v116, s49 src0_sel:BYTE_0 src1_sel:DWORD\n\t" \
  "v_subrev_u32 v116, s50, v116\n\t" \
  "v_ashrrev_i32 v116, 8, v116\n\t" \
  "s_nop 1\n\t" \
  "v_max_i32_dpp v116, v116, v116 quad_perm:[1,0,3,2] row_mask:0xf bank_mask:0xf\n\t" \
  "s_cbranch_vccnz 14f\n\t" \
  "s_nop 0\n\t" \
  "v_max_i32_dpp v116, v116, v116 quad_perm:[2,3,0,1] row_mask:0xf bank_mask:0xf\n\t" \
  "s_nop 1\n\t" \
  "v_max_i32_dpp v116, v116, v116 row_half_mirror row_mask:0xf bank_mask:0xf\n\t" \
  "s_nop 1\n\t" \
  "v_max_i32_dpp v116, v116, v116 row_mirror row_mask:0xf bank_mask:0xf\n\t" \
  "v_med3_i32 v116, v116, 0, v117\n\t" \
  "v_mul_u32_u24 v116, s52, v116\n\t" \
  "v_lshrrev_b32 v116, 16, v116\n\t" \
  "s_mov_b64 exec, -1\n\t" \
  "v_readlane_b32 s84, v116, 0\n\t" \
  "v_readlane_b32 s85, v116, 16\n\t" \
  "s_max_u32 s84, s84, s85\n\t" \
  "s_add_i32 s84, s84, s51\n\t" \
  "v_mov_b64 v[72:73], 0\n\t" \
  "v_mov_b64 v[74:75], 0\n\t" \
  "v_mov_b64 v[76:77], 0\n\t" \
  "v_mov_b64 v[78:79], 0\n\t" \
  "1:\n\t" \
  "s_memtime vcc\n\t" \
  "s_waitcnt lgkmcnt(0)\n\t" \
  "s_sub_i32 s79, s84, vcc_lo\n\t" \
  "s_cmp_gt_i32 s79, 128\n\t" \
  "s_cbranch_scc1 1b\n\t" \
  "2:\n\t" \
  "s_sub_i32 s79, s79, 33\n\t" \
  "s_cmp_gt_i32 s79, 56\n\t" \
  "s_cbranch_scc1 2b\n\t" \
  "buffer_load_dwordx4 v[32:35], v100, %[rh], s40 offen sc1\n\t" \
  "s_nop 12\n\t" \
  "buffer_load_dwordx4 v[36:39], v100, %[rh], s76 offen sc1\n\t" \
  "s_nop 12\n\t" \
  "buffer_load_dwordx4 v[40:43], v100, %[rh], s77 offen sc1\n\t" \
  "s_nop 12\n\t" \
  "buffer_load_dwordx4 v[44:47], v100, %[rh], s78 offen sc1\n\t" \
  "s_memtime vcc\n\t" \
  "s_memrealtime s[82:83]\n\t" \
  "s_nop 4\n\t" \
  "s_memrealtime s[84:85]\n\t" \
  "s_nop 4\n\t" \
  "s_memrealtime s[94:95]\n\t" \
  "s_nop 4\n\t" \
  "s_memrealtime s[86:87]\n\t" \
  "s_nop 4\n\t" \
  "s_memrealtime s[88:89]\n\t" \
  "s_nop 4\n\t" \
  "s_memrealtime s[90:91]\n\t" \
  "s_mov_b32 s92, 0\n\t" \
  "s_waitcnt lgkmcnt(0)\n\t" \
  "s_cmp_eq_u32 s82, s84\n\t" \
  "s_addc_u32 s92, s92, 0\n\t" \
  "s_cmp_eq_u32 s82, s94\n\t" \
  "s_addc_u32 s92, s92, 0\n\t" \
  "s_cmp_eq_u32 s82, s86\n\t" \
  "s_addc_u32 s92, s92, 0\n\t" \
  "s_cmp_eq_u32 s82, s88\n\t" \
  "s_addc_u32 s92, s92, 0\n\t" \
  "s_cmp_eq_u32 s82, s90\n\t" \
  "s_addc_u32 s92, s92, 0\n\t" \
  "s_mul_i32 s92, s92, 24\n\t" \
  "s_add_i32 s92, s92, vcc_lo\n\t" \
  "s_lshl4_add_u32 s93, s82, 64\n\t" \
  "3:\n\t" \
  "s_waitcnt lgkmcnt(0)\n\t" \
  "s_waitcnt vmcnt(3)\n\t" \
  "v_smfmac_f32_16x16x64_bf16 v[72:75], v[32:35], v[48:55], v104\n\t" \
  "ds_write_b128 v101, v[32:35] offset:4096\n\t" \
  "v_smfmac_f32_16x16x64_bf16 v[76:79], v[32:35], v[56:63], v104\n\t" \
  "s_waitcnt vmcnt(2)\n\t" \
  "ds_write_b128 v101, v[36:39] offset:5120\n\t" \
  "v_smfmac_f32_16x16x64_bf16 v[72:75], v[36:39], a[48:55], v104\n\t" \
  "v_smfmac_f32_16x16x64_bf16 v[76:79], v[36:39], a[56:63], v104\n\t" \
  "s_nop 3\n\t" \
  "v_cmp_u_f32 vcc, v72, v73\n\t" \
  "v_cmp_u_f32 s[82:83], v74, v75\n\t" \
  "s_waitcnt vmcnt(1)\n\t" \
  "v_smfmac_f32_16x16x64_bf16 v[72:75], v[40:43], a[64:71], v104\n\t" \
  "s_or_b64 vcc, vcc, s[82:83]\n\t" \
  "s_or_b64 vcc, vcc, s[80:81]\n\t" \
  "v_smfmac_f32_16x16x64_bf16 v[76:79], v[40:43], a[72:79], v104\n\t" \
  "s_waitcnt lgkmcnt(0)\n\t" \
  "s_cbranch_scc1 8f\n\t" \
  "5:\n\t" \
  "s_barrier\n\t" \
  "4:\n\t" \
  "s_memtime s[56:57]\n\t" \
  "ds_read_b128 v[48:51], v101 offset:8192\n\t" \
  "ds_read_b128 v[52:55], v101 offset:9216\n\t" \
  "ds_read_b128 v[56:59], v101 offset:13312\n\t" \
  "s_waitcnt vmcnt(0)\n\t" \
  "v_smfmac_f32_16x16x64_bf16 v[72:75], v[44:47], a[80:87], v104\n\t" \
  "ds_read_b128 v[60:63], v101 offset:12288\n\t" \
  "ds_read_b128 v[64:67], v101 offset:1024\n\t" \
  "v_smfmac_f32_16x16x64_bf16 v[76:79], v[44:47], a[88:95], v104\n\t" \
  "ds_read_b128 v[68:71], v101 offset:0\n\t" \
  "ds_write_b128 v101, v[40:43] offset:6144\n\t" \
  "s_nop 1\n\t" \
  "v_cmp_u_f32 vcc, v72, v73\n\t" \
  "v_cmp_u_f32 s[82:83], v74, v75\n\t" \
  "s_waitcnt lgkmcnt(6)\n\t" \
  "v_smfmac_f32_16x16x64_bf16 v[72:75], v[48:51], v[0:7], v104\n\t" \
  "s_or_b64 vcc, vcc, s[82:83]\n\t" \
  "v_smfmac_f32_16x16x64_bf16 v[76:79], v[48:51], v[8:15], v104\n\t" \
  "s_cbranch_scc1 9f\n\t" \
  "6:\n\t" \
  "s_waitcnt lgkmcnt(5)\n\t" \
  "v_smfmac_f32_16x16x64_bf16 v[72:75], v[52:55], a[96:103], v104\n\t" \
  "ds_write_b128 v101, v[44:47] offset:7168\n\t" \
  "v_smfmac_f32_16x16x64_bf16 v[76:79], v[52:55], a[104:111], v104\n\t" \
  "v_mov_b64 v[40:41], 0\n\t" \
  "s_waitcnt lgkmcnt(5)\n\t" \
  "v_smfmac_f32_16x16x64_bf16 v[72:75], v[56:59], a[144:151], v104\n\t" \
  "v_mov_b64 v[42:43], 0\n\t" \
  "v_smfmac_f32_16x16x64_bf16 v[76:79], v[56:59], a[152:159], v104\n\t" \
  "s_waitcnt lgkmcnt(4)\n\t" \
  "v_smfmac_f32_16x16x64_bf16 v[72:75], v[60:63], v[16:23], v104\n\t" \
  "s_waitcnt lgkmcnt(0)\n\t" \
  "s_barrier\n\t" \
  "v_smfmac_f32_16x16x64_bf16 v[76:79], v[60:63], v[24:31], v104\n\t" \
  "ds_read_b128 v[48:51], v101 offset:10240\n\t" \
  "ds_read_b128 v[52:55], v101 offset:14336\n\t" \
  "v_smfmac_f32_16x16x64_bf16 v[72:75], v[64:67], a[0:7], v104\n\t" \
  "ds_read_b128 v[56:59], v101 offset:2048\n\t" \
  "ds_read_b128 v[60:63], v101 offset:11264\n\t" \
  "v_smfmac_f32_16x16x64_bf16 v[76:79], v[64:67], a[8:15], v104\n\t" \
  "ds_read_b128 v[32:35], v101 offset:15360\n\t" \
  "ds_read_b128 v[36:39], v101 offset:3072\n\t" \
  "v_smfmac_f32_16x16x64_bf16 v[72:75], v[68:71], v[80:87], v104\n\t" \
  "ds_read_b128 v[0:3], v103 offset:0\n\t" \
  "v_mov_b64 v[44:45], 0\n\t" \
  "v_smfmac_f32_16x16x64_bf16 v[76:79], v[68:71], v[88:95], v104\n\t" \
  "v_mov_b64 v[46:47], 0\n\t" \
  "s_waitcnt lgkmcnt(5)\n\t" \
  "v_smfmac_f32_16x16x64_bf16 v[72:75], v[48:51], a[112:119], v104\n\t" \
  "ds_read_b128 v[4:7], v103 offset:1024\n\t" \
  "v_mov_b64 v[80:81], 0\n\t" \
  "v_smfmac_f32_16x16x64_bf16 v[72:75], v[52:55], a[160:167], v104\n\t" \
  "v_mov_b64 v[82:83], 0\n\t" \
  "s_waitcnt lgkmcnt(2)\n\t" \
  "v_smfmac_f32_16x16x64_bf16 v[72:75], v[56:59], a[16:23], v104\n\t" \
  "ds_read_b128 v[8:11], v103 offset:2048\n\t" \
  "v_mov_b64 v[84:85], 0\n\t" \
  "v_smfmac_f32_16x16x64_bf16 v[72:75], v[60:63], a[128:135], v104\n\t" \
  "ds_read_b128 v[12:15], v103 offset:3072\n\t" \
  "v_mov_b64 v[86:87], 0\n\t" \
  "v_smfmac_f32_16x16x64_bf16 v[72:75], v[32:35], a[176:183], v104\n\t" \
  "ds_read_b128 v[16:19], v103 offset:16384\n\t" \
  "v_mov_b64 v[88:89], 0\n\t" \
  "v_smfmac_f32_16x16x64_bf16 v[72:75], v[36:39], a[32:39], v104\n\t" \
  "ds_read_b128 v[20:23], v103 offset:17408\n\t" \
  "v_mov_b64 v[90:91], 0\n\t" \
  "v_smfmac_f32_16x16x64_bf16 v[76:79], v[48:51], a[120:127], v104\n\t" \
  "ds_read_b128 v[24:27], v103 offset:18432\n\t" \
  "v_mov_b64 v[92:93], 0\n\t" \
  "v_smfmac_f32_16x16x64_bf16 v[76:79], v[52:55], a[168:175], v104\n\t" \
  "v_add_f32 v96, v72, v74\n\t" \
  "v_add_f32 v97, v73, v75\n\t" \
  "v_cvt_pk_bf16_f32 v96, v96, v97\n\t" \
  "ds_write_b32 v105, v96\n\t" \
  "v_smfmac_f32_16x16x64_bf16 v[76:79], v[56:59], a[24:31], v104\n\t" \
  "ds_read_b128 v[28:31], v103 offset:19456\n\t" \
  "v_mov_b64 v[94:95], 0\n\t" \
  "v_smfmac_f32_16x16x64_bf16 v[76:79], v[60:63], a[136:143], v104\n\t" \
  "v_smfmac_f32_16x16x64_bf16 v[76:79], v[32:35], a[184:191], v104\n\t" \
  "v_smfmac_f32_16x16x64_bf16 v[76:79], v[36:39], a[40:47], v104\n\t" \
  "s_waitcnt lgkmcnt(1)\n\t" \
  "s_barrier\n\t" \
  "ds_read_b64_tr_b16 v[96:97], v107 offset:0\n\t" \
  "ds_read_b64_tr_b16 v[98:99], v107 offset:64\n\t" \
  "ds_read_b64_tr_b16 v[56:57], v107 offset:1024\n\t" \
  "ds_read_b64_tr_b16 v[58:59], v107 offset:1088\n\t" \
  "s_nop 1\n\t" \
  "v_add_f32 v116, v76, v78\n\t" \
  "v_add_f32 v117, v77, v79\n\t" \
  "v_cvt_pk_bf16_f32 v116, v116, v117\n\t" \
  "ds_write_b32 v105, v116 offset:256\n\t" \
  "s_waitcnt lgkmcnt(3)\n\t" \
  "v_smfmac_f32_16x16x64_bf16 v[40:43], v[96:99], a[192:199], v106\n\t" \
  "v_smfmac_f32_16x16x64_bf16 v[44:47], v[96:99], a[200:207], v106\n\t" \
  "s_waitcnt lgkmcnt(0)\n\t" \
  "s_barrier\n\t" \
  "ds_read_b64_tr_b16 v[52:53], v107 offset:256\n\t" \
  "ds_read_b64_tr_b16 v[54:55], v107 offset:320\n\t" \
  "v_smfmac_f32_16x16x64_bf16 v[40:43], v[56:59], a[224:231], v106\n\t" \
  "ds_read_b64_tr_b16 v[60:61], v107 offset:1280\n\t" \
  "ds_read_b64_tr_b16 v[62:63], v107 offset:1344\n\t" \
  "v_smfmac_f32_16x16x64_bf16 v[44:47], v[56:59], a[232:239], v106\n\t" \
  "v_smfmac_f32_16x16x64_bf16 v[80:83], v[96:99], a[240:247], v106\n\t" \
  "v_mov_b64 v[32:33], 0\n\t" \
  "v_smfmac_f32_16x16x64_bf16 v[84:87], v[96:99], a[248:255], v106\n\t" \
  "v_mov_b64 v[34:35], 0\n\t" \
  "v_smfmac_f32_16x16x64_bf16 v[80:83], v[56:59], v[136:143], v106\n\t" \
  "v_mov_b64 v[36:37], 0\n\t" \
  "v_smfmac_f32_16x16x64_bf16 v[84:87], v[56:59], v[144:151], v106\n\t" \
  "s_waitcnt lgkmcnt(2)\n\t" \
  "v_smfmac_f32_16x16x64_bf16 v[40:43], v[52:55], a[208:215], v106\n\t" \
  "v_smfmac_f32_16x16x64_bf16 v[44:47], v[52:55], a[216:223], v106\n\t" \
  "s_xor_b32 s76, s47, 0x40000\n\t" \
  "s_waitcnt lgkmcnt(0)\n\t" \
  "v_smfmac_f32_16x16x64_bf16 v[40:43], v[60:63], v[0:7], v106\n\t" \
  "v_mov_b64 v[38:39], 0\n\t" \
  "s_xor_b32 s77, s47, 0x84000\n\t" \
  "v_smfmac_f32_16x16x64_bf16 v[44:47], v[60:63], v[8:15], v106\n\t" \
  "s_xor_b32 s78, s47, 0xc4000\n\t" \
  "v_smfmac_f32_16x16x64_bf16 v[80:83], v[52:55], v[120:127], v106\n\t" \
  "ds_read_b128 v[0:3], v103 offset:32768\n\t" \
  "v_cvt_pk_bf16_f32 v64, v40, v41\n\t" \
  "v_smfmac_f32_16x16x64_bf16 v[84:87], v[52:55], v[128:135], v106\n\t" \
  "ds_read_b128 v[4:7], v103 offset:33792\n\t" \
  "v_cvt_pk_bf16_f32 v66, v42, v43\n\t" \
  "v_smfmac_f32_16x16x64_bf16 v[80:83], v[60:63], v[16:23], v106\n\t" \
  "ds_read_b128 v[8:11], v103 offset:34816\n\t" \
  "v_cvt_pk_bf16_f32 v65, v44, v45\n\t" \
  "v_smfmac_f32_16x16x64_bf16 v[84:87], v[60:63], v[24:31], v106\n\t" \
  "ds_read_b128 v[12:15], v103 offset:35840\n\t" \
  "v_cvt_pk_bf16_f32 v67, v46, v47\n\t" \
  "v_smfmac_f32_16x16x64_bf16 v[88:91], v[96:99], v[152:159], v106\n\t" \
  "v_cvt_pk_bf16_f32 v68, v80, v81\n\t" \
  "v_smfmac_f32_16x16x64_bf16 v[92:95], v[96:99], v[160:167], v106\n\t" \
  "buffer_store_dwordx4 v[64:67], v108, %[ru], s47 offen sc1\n\t" \
  "v_cvt_pk_bf16_f32 v70, v82, v83\n\t" \
  "v_smfmac_f32_16x16x64_bf16 v[88:91], v[52:55], v[168:175], v106\n\t" \
  "v_cvt_pk_bf16_f32 v69, v84, v85\n\t" \
  "v_smfmac_f32_16x16x64_bf16 v[92:95], v[52:55], v[176:183], v106\n\t" \
  "v_cvt_pk_bf16_f32 v71, v86, v87\n\t" \
  "v_smfmac_f32_16x16x64_bf16 v[88:91], v[56:59], v[184:191], v106\n\t" \
  "ds_read_b128 v[16:19], v103 offset:49152\n\t" \
  "v_smfmac_f32_16x16x64_bf16 v[92:95], v[56:59], v[192:199], v106\n\t" \
  "ds_read_b128 v[20:23], v103 offset:50176\n\t" \
  "buffer_store_dwordx4 v[68:71], v108, %[ru], s76 offen sc1\n\t" \
  "s_waitcnt lgkmcnt(4)\n\t" \
  "v_smfmac_f32_16x16x64_bf16 v[88:91], v[60:63], v[0:7], v106\n\t" \
  "ds_read_b128 v[24:27], v103 offset:51200\n\t" \
  "s_waitcnt lgkmcnt(3)\n\t" \
  "v_smfmac_f32_16x16x64_bf16 v[92:95], v[60:63], v[8:15], v106\n\t" \
  "ds_read_b128 v[28:31], v103 offset:52224\n\t" \
  "v_smfmac_f32_16x16x64_bf16 v[32:35], v[96:99], v[200:207], v106\n\t" \
  "v_cvt_pk_bf16_f32 v72, v88, v89\n\t" \
  "v_smfmac_f32_16x16x64_bf16 v[36:39], v[96:99], v[208:215], v106\n\t" \
  "v_cvt_pk_bf16_f32 v74, v90, v91\n\t" \
  "v_smfmac_f32_16x16x64_bf16 v[32:35], v[52:55], v[216:223], v106\n\t" \
  "v_cvt_pk_bf16_f32 v73, v92, v93\n\t" \
  "v_smfmac_f32_16x16x64_bf16 v[36:39], v[52:55], v[224:231], v106\n\t" \
  "v_cvt_pk_bf16_f32 v75, v94, v95\n\t" \
  "v_smfmac_f32_16x16x64_bf16 v[32:35], v[56:59], v[232:239], v106\n\t" \
  "s_waitcnt lgkmcnt(2)\n\t" \
  "v_smfmac_f32_16x16x64_bf16 v[32:35], v[60:63], v[16:23], v106\n\t" \
  "buffer_store_dwordx4 v[72:75], v108, %[ru], s77 offen sc1\n\t" \
  "v_smfmac_f32_16x16x64_bf16 v[36:39], v[56:59], v[240:247], v106\n\t" \
  "s_waitcnt lgkmcnt(0)\n\t" \
  "v_smfmac_f32_16x16x64_bf16 v[36:39], v[60:63], v[24:31], v106\n\t" \
  "v_cvt_pk_bf16_f32 v76, v32, v33\n\t" \
  "v_cvt_pk_bf16_f32 v78, v34, v35\n\t" \
  "s_nop 5\n\t" \
  "v_cvt_pk_bf16_f32 v77, v36, v37\n\t" \
  "v_cvt_pk_bf16_f32 v79, v38, v39\n\t" \
  "buffer_store_dwordx4 v[76:79], v108, %[ru], s78 offen sc1\n\t" \
  "s_memtime s[76:77]\n\t" \
  "s_mov_b32 s42, 0\n\t" \
  "s_waitcnt lgkmcnt(0)\n\t" \
  "s_mov_b32 s57, s76\n\t" \
  "s_add_i32 s41, s56, 1950\n\t" \
  "s_sub_i32 s76, s92, s51\n\t" \
  "s_sub_i32 s77, s93, s53\n\t" \
  "s_cmp_eq_u32 s76, 0\n\t" \
  "s_cbranch_scc1 21f\n\t" \
  "v_cvt_f32_u32 v117, s76\n\t" \
  "v_cvt_f32_u32 v116, s77\n\t" \
  "v_rcp_f32 v117, v117\n\t" \
  "s_mov_b32 s51, s92\n\t" \
  "v_mul_f32 v117, v117, v116\n\t" \
  "v_max_f32 v117, 0x3f028f5c, v117\n\t" \
  "v_min_f32 v117, 0x3ffeb852, v117\n\t" \
  "v_rcp_f32 v116, v117\n\t" \
  "v_mul_f32 v117, 0x4f000000, v117\n\t" \
  "v_mul_f32 v116, 0x47800000, v116\n\t" \
  "v_cvt_u32_f32 v117, v117\n\t" \
  "v_cvt_u32_f32 v116, v116\n\t" \
  "s_mov_b32 s53, s93\n\t" \
  "v_readfirstlane_b32 s46, v117\n\t" \
  "v_readfirstlane_b32 s52, v116\n\t" \
  "21:\n\t" \
  "s_add_i32 s44, s44, 1\n\t" \
  "s_and_b32 s58, s58, 4\n\t" \
  "s_and_b32 s76, s44, 64\n\t" \
  "s_or_b32 s42, s42, s76\n\t" \
  "s_cbranch_scc0 20b\n\t" \
  "s_branch 99f\n\t" \
  "14:\n\t" \
  "s_add_u32 s59, s59, 0x10000\n\t" \
  "s_cmp_lt_u32 s59, 0x100000\n\t" \
  "s_cbranch_scc1 13b\n\t" \
  "s_mov_b64 exec, -1\n\t" \
  "s_mov_b32 s42, 16\n\t" \
  "s_branch 99f\n\t" \
  "8:\n\t" \
  "s_bitcmp1_b32 s58, 1\n\t" \
  "s_cbranch_scc1 4b\n\t" \
  "s_bitcmp1_b32 s58, 2\n\t" \
  "s_cbranch_scc1 5b\n\t" \
  "v_max3_u32 v116, v32, v33, v34\n\t" \
  "v_max3_u32 v117, v35, v36, v37\n\t" \
  "v_max3_u32 v116, v116, v38, v39\n\t" \
  "v_max_u32 v116, v116, v117\n\t" \
  "v_cmp_eq_u32 vcc, -1, v116\n\t" \
  "s_cbranch_vccz 5b\n\t" \
  "s_branch 0f\n\t" \
  "9:\n\t" \
  "s_bitcmp1_b32 s58, 2\n\t" \
  "s_cbranch_scc1 6b\n\t" \
  "v_max3_u32 v116, v40, v41, v42\n\t" \
  "v_max3_u32 v117, v43, v44, v45\n\t" \
  "v_max3_u32 v116, v116, v46, v47\n\t" \
  "v_max_u32 v116, v116, v117\n\t" \
  "v_cmp_eq_u32 vcc, -1, v116\n\t" \
  "s_cbranch_vccz 6b\n\t" \
  "s_or_b32 s58, s58, 2\n\t" \
  "0:\n\t" \
  "s_or_b32 s58, s58, 1\n\t" \
  "11:\n\t" \
  "buffer_load_dwordx4 v[32:35], v100, %[rh], s40 offen sc1\n\t" \
  "buffer_load_dwordx4 v[36:39], v100, %[rh], s76 offen sc1\n\t" \
  "buffer_load_dwordx4 v[40:43], v100, %[rh], s77 offen sc1\n\t" \
  "buffer_load_dwordx4 v[44:47], v100, %[rh], s78 offen sc1\n\t" \
  "s_waitcnt lgkmcnt(0)\n\t" \
  "s_branch 10b\n\t" \
  "17:\n\t" \
  "v_mov_b64 v[72:73], 0\n\t" \
  "v_mov_b64 v[74:75], 0\n\t" \
  "v_mov_b64 v[76:77], 0\n\t" \
  "v_mov_b64 v[78:79], 0\n\t" \
  "s_add_u32 s59, s59, 1\n\t" \
  "s_bitcmp1_b32 s58, 2\n\t" \
  "s_cbranch_scc1 3b\n\t" \
  "s_waitcnt vmcnt(0)\n\t" \
  "v_max_u32 v116, v32, v33\n\t" \
  "v_max3_u32 v116, v116, v34, v35\n\t" \
  "v_max3_u32 v116, v116, v36, v37\n\t" \
  "v_max3_u32 v116, v116, v38, v39\n\t" \
  "v_max3_u32 v116, v116, v40, v41\n\t" \
  "v_max3_u32 v116, v116, v42, v43\n\t" \
  "v_max3_u32 v116, v116, v44, v45\n\t" \
  "v_max3_u32 v116, v116, v46, v47\n\t" \
  "v_cmp_eq_u32 vcc, -1, v116\n\t" \
  "s_cbranch_vccz 3b\n\t" \
  "s_bitcmp1_b32 s59, 6\n\t" \
  "s_cbranch_scc1 12f\n\t" \
  "buffer_load_dwordx4 v[32:35], v100, %[rh], s40 offen sc1\n\t" \
  "buffer_load_dwordx4 v[36:39], v100, %[rh], s76 offen sc1\n\t" \
  "buffer_load_dwordx4 v[40:43], v100, %[rh], s77 offen sc1\n\t" \
  "buffer_load_dwordx4 v[44:47], v100, %[rh], s78 offen sc1\n\t" \
  "s_branch 17b\n\t" \
  "12:\n\t" \
  "s_mov_b32 s42, s58\n\t" \
  "s_branch 99f\n\t" \
  "92:\n\t" \
  "s_mov_b32 s58, s42\n\t" \
  ".p2align 6\n\t" \
  "20:\n\t" \
  "s_and_b32 s76, s44, 7\n\t" \
  "s_lshl_b32 s76, s76, 15\n\t" \
  "s_add_i32 s40, s54, s76\n\t" \
  "s_and_b32 s76, s44, 3\n\t" \
  "s_lshl_b32 s45, s76, 16\n\t" \
  "s_and_b32 s47, s44, 3\n\t" \
  "s_lshl_b32 s76, s47, 2\n\t" \
  "s_lshl_b32 s47, s47, 20\n\t" \
  "s_add_i32 s47, s47, s55\n\t" \
  "s_lshr_b32 s76, %[um], s76\n\t" \
  "s_and_b32 s76, s76, 7\n\t" \
  "s_lshl_b32 s76, s76, 12\n\t" \
  "s_xor_b32 s47, s47, s76\n\t" \
  "s_add_i32 s49, s44, -1\n\t" \
  "s_and_b32 s48, s49, 3\n\t" \
  "s_lshl_b32 s48, s48, 16\n\t" \
  "s_lshl_b32 s50, s53, 8\n\t" \
  "s_mov_b32 s59, 0\n\t" \
  "s_mov_b32 s92, s51\n\t" \
  "s_mov_b32 s93, s53\n\t" \
  "s_xor_b32 s76, s40, 0x1000\n\t" \
  "s_xor_b32 s77, s40, 0x4000\n\t" \
  "s_xor_b32 s78, s40, 0x5000\n\t" \
  "s_cmp_lg_u32 s58, 0\n\t" \
  "s_cbranch_scc1 11f\n\t" \
  "10:\n\t" \
  "ds_read_b128 v[48:51], v102 offset:32768\n\t" \
  "ds_read_b128 v[52:55], v102 offset:33792\n\t" \
  "ds_read_b128 v[56:59], v102 offset:34816\n\t" \
  "ds_read_b128 v[60:63], v102 offset:35840\n\t" \
  "ds_read_b128 v[0:3], v102 offset:49152\n\t" \
  "ds_read_b128 v[4:7], v102 offset:50176\n\t" \
  "ds_read_b128 v[8:11], v102 offset:51200\n\t" \
  "ds_read_b128 v[12:15], v102 offset:52224\n\t" \
  "ds_read_b128 v[16:19], v102 offset:0\n\t" \
  "ds_read_b128 v[20:23], v102 offset:1024\n\t" \
  "ds_read_b128 v[24:27], v102 offset:2048\n\t" \
  "ds_read_b128 v[28:31], v102 offset:3072\n\t" \
  "s_waitcnt lgkmcnt(4)\n\t" \
  "ds_read_b128 v[80:83], v102 offset:16384\n\t" \
  "ds_read_b128 v[84:87], v102 offset:17408\n\t" \
  "ds_read_b128 v[88:91], v102 offset:18432\n\t" \
  "ds_read_b128 v[92:95], v102 offset:19456\n\t" \
  "s_bitcmp1_b32 s58, 1\n\t" \
  "s_cselect_b64 s[80:81], -1, 0\n\t" \
  "s_cmp_lg_u32 s58, 0\n\t" \
  "s_cbranch_scc1 17f\n\t" \
  "s_mov_b32 exec_hi, 0\n\t" \
  "16:\n\t" \
  "s_memtime vcc\n\t" \
  "s_waitcnt lgkmcnt(0)\n\t" \
  "s_sub_i32 s79, s41, vcc_lo\n\t" \
  "s_cmp_gt_i32 s79, 64\n\t" \
  "s_cbranch_scc1 16b\n\t" \
  "13:\n\t" \
  "buffer_load_dword v116, v110, %[rh], s48 offen sc1\n\t" \
  "s_waitcnt vmcnt(0)\n\t" \
  "15:\n\t" \
  "v_mov_b32 v117, 20000\n\t" \
  "v_cmp_ne_u32_sdwa vcc, v116, s49 src0_sel:BYTE_0 src1_sel:DWORD\n\t" \
  "v_subrev_u32 v116, s50, v116\n\t" \
  "v_ashrrev_i32 v116, 8, v116\n\t" \
  "s_nop 1\n\t" \
  "v_max_i32_dpp v116, v116, v116 quad_perm:[1,0,3,2] row_mask:0xf bank_mask:0xf\n\t" \
  "s_cbranch_vccnz 14f\n\t" \
  "s_nop 0\n\t" \
  "v_max_i32_dpp v116, v116, v116 quad_perm:[2,3,0,1] row_mask:0xf bank_mask:0xf\n\t" \
  "s_nop 1\n\t" \
  "v_max_i32_dpp v116, v116, v116 row_half_mirror row_mask:0xf bank_mask:0xf\n\t" \
  "s_nop 1\n\t" \
  "v_max_i32_dpp v116, v116, v116 row_mirror row_mask:0xf bank_mask:0xf\n\t" \
  "v_med3_i32 v116, v116, 0, v117\n\t" \
  "v_mul_u32_u24 v116, s52, v116\n\t" \
  "v_lshrrev_b32 v116, 16, v116\n\t" \
  "s_mov_b64 exec, -1\n\t" \
  "v_readlane_b32 s84, v116, 0\n\t" \
  "v_readlane_b32 s85, v116, 16\n\t" \
  "s_max_u32 s84, s84, s85\n\t" \
  "s_add_i32 s84, s84, s51\n\t" \
  "v_mov_b64 v[72:73], 0\n\t" \
  "v_mov_b64 v[74:75], 0\n\t" \
  "v_mov_b64 v[76:77], 0\n\t" \
  "v_mov_b64 v[78:79], 0\n\t" \
  "1:\n\t" \
  "s_memtime vcc\n\t" \
  "s_waitcnt lgkmcnt(0)\n\t" \
  "s_sub_i32 s79, s84, vcc_lo\n\t" \
  "s_cmp_gt_i32 s79, 128\n\t" \
  "s_cbranch_scc1 1b\n\t" \
  "2:\n\t" \
  "s_sub_i32 s79, s79, 33\n\t" \
  "s_cmp_gt_i32 s79, 56\n\t" \
  "s_cbranch_scc1 2b\n\t" \
  "buffer_load_dwordx4 v[32:35], v100, %[rh], s40 offen sc1\n\t" \
  "s_nop 12\n\t" \
  "buffer_load_dwordx4 v[36:39], v100, %[rh], s76 offen sc1\n\t" \
  "s_nop 12\n\t" \
  "buffer_load_dwordx4 v[40:43], v100, %[rh], s77 offen sc1\n\t" \
  "s_nop 12\n\t" \
  "buffer_load_dwordx4 v[44:47], v100, %[rh], s78 offen sc1\n\t" \
  "s_memtime vcc\n\t" \
  "s_memrealtime s[82:83]\n\t" \
  "s_nop 4\n\t" \
  "s_memrealtime s[84:85]\n\t" \
  "s_nop 4\n\t" \
  "s_memrealtime s[94:95]\n\t" \
  "s_nop 4\n\t" \
  "s_memrealtime s[86:87]\n\t" \
  "s_nop 4\n\t" \
  "s_memrealtime s[88:89]\n\t" \
  "s_nop 4\n\t" \
  "s_memrealtime s[90:91]\n\t" \
  "s_mov_b32 s92, 0\n\t" \
  "s_waitcnt lgkmcnt(0)\n\t" \
  "s_cmp_eq_u32 s82, s84\n\t" \
  "s_addc_u32 s92, s92, 0\n\t" \
  "s_cmp_eq_u32 s82, s94\n\t" \
  "s_addc_u32 s92, s92, 0\n\t" \
  "s_cmp_eq_u32 s82, s86\n\t" \
  "s_addc_u32 s92, s92, 0\n\t" \
  "s_cmp_eq_u32 s82, s88\n\t" \
  "s_addc_u32 s92, s92, 0\n\t" \
  "s_cmp_eq_u32 s82, s90\n\t" \
  "s_addc_u32 s92, s92, 0\n\t" \
  "s_mul_i32 s92, s92, 24\n\t" \
  "s_add_i32 s92, s92, vcc_lo\n\t" \
  "s_lshl4_add_u32 s93, s82, 64\n\t" \
  "3:\n\t" \
  "s_waitcnt lgkmcnt(0)\n\t" \
  "s_waitcnt vmcnt(3)\n\t" \
  "v_smfmac_f32_16x16x64_bf16 v[72:75], v[32:35], v[48:55], v104\n\t" \
  "ds_write_b128 v101, v[32:35] offset:8192\n\t" \
  "v_smfmac_f32_16x16x64_bf16 v[76:79], v[32:35], v[56:63], v104\n\t" \
  "s_waitcnt vmcnt(2)\n\t" \
  "ds_write_b128 v101, v[36:39] offset:9216\n\t" \
  "v_smfmac_f32_16x16x64_bf16 v[72:75], v[36:39], a[96:103], v104\n\t" \
  "v_smfmac_f32_16x16x64_bf16 v[76:79], v[36:39], a[104:111], v104\n\t" \
  "s_nop 3\n\t" \
  "v_cmp_u_f32 vcc, v72, v73\n\t" \
  "v_cmp_u_f32 s[82:83], v74, v75\n\t" \
  "s_waitcnt vmcnt(1)\n\t" \
  "v_smfmac_f32_16x16x64_bf16 v[72:75], v[40:43], a[112:119], v104\n\t" \
  "s_or_b64 vcc, vcc, s[82:83]\n\t" \
  "s_or_b64 vcc, vcc, s[80:81]\n\t" \
  "v_smfmac_f32_16x16x64_bf16 v[76:79], v[40:43], a[120:127], v104\n\t" \
  "s_waitcnt lgkmcnt(0)\n\t" \
  "s_cbranch_scc1 8f\n\t" \
  "5:\n\t" \
  "s_barrier\n\t" \
  "4:\n\t" \
  "s_memtime s[56:57]\n\t" \
  "ds_read_b128 v[48:51], v101 offset:12288\n\t" \
  "ds_read_b128 v[52:55], v101 offset:13312\n\t" \
  "ds_read_b128 v[56:59], v101 offset:1024\n\t" \
  "s_waitcnt vmcnt(0)\n\t" \
  "v_smfmac_f32_16x16x64_bf16 v[72:75], v[44:47], a[128:135], v104\n\t" \
  "ds_read_b128 v[60:63], v101 offset:0\n\t" \
  "ds_read_b128 v[64:67], v101 offset:5120\n\t" \
  "v_smfmac_f32_16x16x64_bf16 v[76:79], v[44:47], a[136:143], v104\n\t" \
  "ds_read_b128 v[68:71], v101 offset:4096\n\t" \
  "ds_write_b128 v101, v[40:43] offset:10240\n\t" \
  "s_nop 1\n\t" \
  "v_cmp_u_f32 vcc, v72, v73\n\t" \
  "v_cmp_u_f32 s[82:83], v74, v75\n\t" \
  "s_waitcnt lgkmcnt(6)\n\t" \
  "v_smfmac_f32_16x16x64_bf16 v[72:75], v[48:51], v[0:7], v104\n\t" \
  "s_or_b64 vcc, vcc, s[82:83]\n\t" \
  "v_smfmac_f32_16x16x64_bf16 v[76:79], v[48:51], v[8:15], v104\n\t" \
  "s_cbranch_scc1 9f\n\t" \
  "6:\n\t" \
  "s_waitcnt lgkmcnt(5)\n\t" \
  "v_smfmac_f32_16x16x64_bf16 v[72:75], v[52:55], a[144:151], v104\n\t" \
  "ds_write_b128 v101, v[44:47] offset:11264\n\t" \
  "v_smfmac_f32_16x16x64_bf16 v[76:79], v[52:55], a[152:159], v104\n\t" \
  "v_mov_b64 v[40:41], 0\n\t" \
  "s_waitcnt lgkmcnt(5)\n\t" \
  "v_smfmac_f32_16x16x64_bf16 v[72:75], v[56:59], a[0:7], v104\n\t" \
  "v_mov_b64 v[42:43], 0\n\t" \
  "v_smfmac_f32_16x16x64_bf16 v[76:79], v[56:59], a[8:15], v104\n\t" \
  "s_waitcnt lgkmcnt(4)\n\t" \
  "v_smfmac_f32_16x16x64_bf16 v[72:75], v[60:63], v[16:23], v104\n\t" \
  "s_waitcnt lgkmcnt(0)\n\t" \
  "s_barrier\n\t" \
  "v_smfmac_f32_16x16x64_bf16 v[76:79], v[60:63], v[24:31], v104\n\t" \
  "ds_read_b128 v[48:51], v101 offset:14336\n\t" \
  "ds_read_b128 v[52:55], v101 offset:2048\n\t" \
  "v_smfmac_f32_16x16x64_bf16 v[72:75], v[64:67], a[48:55], v104\n\t" \
  "ds_read_b128 v[56:59], v101 offset:6144\n\t" \
  "ds_read_b128 v[60:63], v101 offset:15360\n\t" \
  "v_smfmac_f32_16x16x64_bf16 v[76:79], v[64:67], a[56:63], v104\n\t" \
  "ds_read_b128 v[32:35], v101 offset:3072\n\t" \
  "ds_read_b128 v[36:39], v101 offset:7168\n\t" \
  "v_smfmac_f32_16x16x64_bf16 v[72:75], v[68:71], v[80:87], v104\n\t" \
  "ds_read_b128 v[0:3], v103 offset:0\n\t" \
  "v_mov_b64 v[44:45], 0\n\t" \
  "v_smfmac_f32_16x16x64_bf16 v[76:79], v[68:71], v[88:95], v104\n\t" \
  "v_mov_b64 v[46:47], 0\n\t" \
  "s_waitcnt lgkmcnt(5)\n\t" \
  "v_smfmac_f32_16x16x64_bf16 v[72:75], v[48:51], a[160:167], v104\n\t" \
  "ds_read_b128 v[4:7], v103 offset:1024\n\t" \
  "v_mov_b64 v[80:81], 0\n\t" \
  "v_smfmac_f32_16x16x64_bf16 v[72:75], v[52:55], a[16:23], v104\n\t" \
  "v_mov_b64 v[82:83], 0\n\t" \
  "s_waitcnt lgkmcnt(2)\n\t" \
  "v_smfmac_f32_16x16x64_bf16 v[72:75], v[56:59], a[64:71], v104\n\t" \
  "ds_read_b128 v[8:11], v103 offset:2048\n\t" \
  "v_mov_b64 v[84:85], 0\n\t" \
  "v_smfmac_f32_16x16x64_bf16 v[72:75], v[60:63], a[176:183], v104\n\t" \
  "ds_read_b128 v[12:15], v103 offset:3072\n\t" \
  "v_mov_b64 v[86:87], 0\n\t" \
  "v_smfmac_f32_16x16x64_bf16 v[72:75], v[32:35], a[32:39], v104\n\t" \
  "ds_read_b128 v[16:19], v103 offset:16384\n\t" \
  "v_mov_b64 v[88:89], 0\n\t" \
  "v_smfmac_f32_16x16x64_bf16 v[72:75], v[36:39], a[80:87], v104\n\t" \
  "ds_read_b128 v[20:23], v103 offset:17408\n\t" \
  "v_mov_b64 v[90:91], 0\n\t" \
  "v_smfmac_f32_16x16x64_bf16 v[76:79], v[48:51], a[168:175], v104\n\t" \
  "ds_read_b128 v[24:27], v103 offset:18432\n\t" \
  "v_mov_b64 v[92:93], 0\n\t" \
  "v_smfmac_f32_16x16x64_bf16 v[76:79], v[52:55], a[24:31], v104\n\t" \
  "v_add_f32 v96, v72, v74\n\t" \
  "v_add_f32 v97, v73, v75\n\t" \
  "v_cvt_pk_bf16_f32 v96, v96, v97\n\t" \
  "ds_write_b32 v105, v96\n\t" \
  "v_smfmac_f32_16x16x64_bf16 v[76:79], v[56:59], a[72:79], v104\n\t" \
  "ds_read_b128 v[28:31], v103 offset:19456\n\t" \
  "v_mov_b64 v[94:95], 0\n\t" \
  "v_smfmac_f32_16x16x64_bf16 v[76:79], v[60:63], a[184:191], v104\n\t" \
  "v_smfmac_f32_16x16x64_bf16 v[76:79], v[32:35], a[40:47], v104\n\t" \
  "v_smfmac_f32_16x16x64_bf16 v[76:79], v[36:39], a[88:95], v104\n\t" \
  "s_waitcnt lgkmcnt(1)\n\t" \
  "s_barrier\n\t" \
  "ds_read_b64_tr_b16 v[96:97], v107 offset:0\n\t" \
  "ds_read_b64_tr_b16 v[98:99], v107 offset:64\n\t" \
  "ds_read_b64_tr_b16 v[56:57], v107 offset:1024\n\t" \
  "ds_read_b64_tr_b16 v[58:59], v107 offset:1088\n\t" \
  "s_nop 1\n\t" \
  "v_add_f32 v116, v76, v78\n\t" \
  "v_add_f32 v117, v77, v79\n\t" \
  "v_cvt_pk_bf16_f32 v116, v116, v117\n\t" \
  "ds_write_b32 v105, v116 offset:256\n\t" \
  "s_waitcnt lgkmcnt(3)\n\t" \
  "v_smfmac_f32_16x16x64_bf16 v[40:43], v[96:99], a[192:199], v106\n\t" \
  "v_smfmac_f32_16x16x64_bf16 v[44:47], v[96:99], a[200:207], v106\n\t" \
  "s_waitcnt lgkmcnt(0)\n\t" \
  "s_barrier\n\t" \
  "ds_read_b64_tr_b16 v[52:53], v107 offset:256\n\t" \
  "ds_read_b64_tr_b16 v[54:55], v107 offset:320\n\t" \
  "v_smfmac_f32_16x16x64_bf16 v[40:43], v[56:59], a[224:231], v106\n\t" \
  "ds_read_b64_tr_b16 v[60:61], v107 offset:1280\n\t" \
  "ds_read_b64_tr_b16 v[62:63], v107 offset:1344\n\t" \
  "v_smfmac_f32_16x16x64_bf16 v[44:47], v[56:59], a[232:239], v106\n\t" \
  "v_smfmac_f32_16x16x64_bf16 v[80:83], v[96:99], a[240:247], v106\n\t" \
  "v_mov_b64 v[32:33], 0\n\t" \
  "v_smfmac_f32_16x16x64_bf16 v[84:87], v[96:99], a[248:255], v106\n\t" \
  "v_mov_b64 v[34:35], 0\n\t" \
  "v_smfmac_f32_16x16x64_bf16 v[80:83], v[56:59], v[136:143], v106\n\t" \
  "v_mov_b64 v[36:37], 0\n\t" \
  "v_smfmac_f32_16x16x64_bf16 v[84:87], v[56:59], v[144:151], v106\n\t" \
  "s_waitcnt lgkmcnt(2)\n\t" \
  "v_smfmac_f32_16x16x64_bf16 v[40:43], v[52:55], a[208:215], v106\n\t" \
  "v_smfmac_f32_16x16x64_bf16 v[44:47], v[52:55], a[216:223], v106\n\t" \
  "s_xor_b32 s76, s47, 0x40000\n\t" \
  "s_waitcnt lgkmcnt(0)\n\t" \
  "v_smfmac_f32_16x16x64_bf16 v[40:43], v[60:63], v[0:7], v106\n\t" \
  "v_mov_b64 v[38:39], 0\n\t" \
  "s_xor_b32 s77, s47, 0x84000\n\t" \
  "v_smfmac_f32_16x16x64_bf16 v[44:47], v[60:63], v[8:15], v106\n\t" \
  "s_xor_b32 s78, s47, 0xc4000\n\t" \
  "v_smfmac_f32_16x16x64_bf16 v[80:83], v[52:55], v[120:127], v106\n\t" \
  "ds_read_b128 v[0:3], v103 offset:32768\n\t" \
  "v_cvt_pk_bf16_f32 v64, v40, v41\n\t" \
  "v_smfmac_f32_16x16x64_bf16 v[84:87], v[52:55], v[128:135], v106\n\t" \
  "ds_read_b128 v[4:7], v103 offset:33792\n\t" \
  "v_cvt_pk_bf16_f32 v66, v42, v43\n\t" \
  "v_smfmac_f32_16x16x64_bf16 v[80:83], v[60:63], v[16:23], v106\n\t" \
  "ds_read_b128 v[8:11], v103 offset:34816\n\t" \
  "v_cvt_pk_bf16_f32 v65, v44, v45\n\t" \
  "v_smfmac_f32_16x16x64_bf16 v[84:87], v[60:63], v[24:31], v106\n\t" \
  "ds_read_b128 v[12:15], v103 offset:35840\n\t" \
  "v_cvt_pk_bf16_f32 v67, v46, v47\n\t" \
  "v_smfmac_f32_16x16x64_bf16 v[88:91], v[96:99], v[152:159], v106\n\t" \
  "v_cvt_pk_bf16_f32 v68, v80, v81\n\t" \
  "v_smfmac_f32_16x16x64_bf16 v[92:95], v[96:99], v[160:167], v106\n\t" \
  "v_cvt_pk_bf16_f32 v70, v82, v83\n\t" \
  "v_smfmac_f32_16x16x64_bf16 v[88:91], v[52:55], v[168:175], v106\n\t" \
  "buffer_store_dwordx4 v[64:67], v108, %[ru], s47 offen sc1\n\t" \
  "v_cvt_pk_bf16_f32 v69, v84, v85\n\t" \
  "v_smfmac_f32_16x16x64_bf16 v[92:95], v[52:55], v[176:183], v106\n\t" \
  "v_cvt_pk_bf16_f32 v71, v86, v87\n\t" \
  "v_smfmac_f32_16x16x64_bf16 v[88:91], v[56:59], v[184:191], v106\n\t" \
  "ds_read_b128 v[16:19], v103 offset:49152\n\t" \
  "v_smfmac_f32_16x16x64_bf16 v[92:95], v[56:59], v[192:199], v106\n\t" \
  "ds_read_b128 v[20:23], v103 offset:50176\n\t" \
  "s_waitcnt lgkmcnt(4)\n\t" \
  "v_smfmac_f32_16x16x64_bf16 v[88:91], v[60:63], v[0:7], v106\n\t" \
  "ds_read_b128 v[24:27], v103 offset:51200\n\t" \
  "buffer_store_dwordx4 v[68:71], v108, %[ru], s76 offen sc1\n\t" \
  "s_waitcnt lgkmcnt(3)\n\t" \
  "v_smfmac_f32_16x16x64_bf16 v[92:95], v[60:63], v[8:15], v106\n\t" \
  "ds_read_b128 v[28:31], v103 offset:52224\n\t" \
  "v_smfmac_f32_16x16x64_bf16 v[32:35], v[96:99], v[200:207], v106\n\t" \
  "v_cvt_pk_bf16_f32 v72, v88, v89\n\t" \
  "v_smfmac_f32_16x16x64_bf16 v[36:39], v[96:99], v[208:215], v106\n\t" \
  "v_cvt_pk_bf16_f32 v74, v90, v91\n\t" \
  "v_smfmac_f32_16x16x64_bf16 v[32:35], v[52:55], v[216:223], v106\n\t" \
  "v_cvt_pk_bf16_f32 v73, v92, v93\n\t" \
  "v_smfmac_f32_16x16x64_bf16 v[36:39], v[52:55], v[224:231], v106\n\t" \
  "v_cvt_pk_bf16_f32 v75, v94, v95\n\t" \
  "v_smfmac_f32_16x16x64_bf16 v[32:35], v[56:59], v[232:239], v106\n\t" \
  "s_waitcnt lgkmcnt(2)\n\t" \
  "v_smfmac_f32_16x16x64_bf16 v[32:35], v[60:63], v[16:23], v106\n\t" \
  "v_smfmac_f32_16x16x64_bf16 v[36:39], v[56:59], v[240:247], v106\n\t" \
  "buffer_store_dwordx4 v[72:75], v108, %[ru], s77 offen sc1\n\t" \
  "s_waitcnt lgkmcnt(0)\n\t" \
  "v_smfmac_f32_16x16x64_bf16 v[36:39], v[60:63], v[24:31], v106\n\t" \
  "v_cvt_pk_bf16_f32 v76, v32, v33\n\t" \
  "v_cvt_pk_bf16_f32 v78, v34, v35\n\t" \
  "s_nop 5\n\t" \
  "v_cvt_pk_bf16_f32 v77, v36, v37\n\t" \
  "v_cvt_pk_bf16_f32 v79, v38, v39\n\t" \
  "buffer_store_dwordx4 v[76:79], v108, %[ru], s78 offen sc1\n\t" \
  "s_memtime s[76:77]\n\t" \
  "s_mov_b32 s42, 0\n\t" \
  "s_waitcnt lgkmcnt(0)\n\t" \
  "s_mov_b32 s57, s76\n\t" \
  "s_add_i32 s41, s56, 1950\n\t" \
  "s_sub_i32 s76, s92, s51\n\t" \
  "s_sub_i32 s77, s93, s53\n\t" \
  "s_cmp_eq_u32 s76, 0\n\t" \
  "s_cbranch_scc1 21f\n\t" \
  "v_cvt_f32_u32 v117, s76\n\t" \
  "v_cvt_f32_u32 v116, s77\n\t" \
  "v_rcp_f32 v117, v117\n\t" \
  "s_mov_b32 s51, s92\n\t" \
  "v_mul_f32 v117, v117, v116\n\t" \
  "v_max_f32 v117, 0x3f028f5c, v117\n\t" \
  "v_min_f32 v117, 0x3ffeb852, v117\n\t" \
  "v_rcp_f32 v116, v117\n\t" \
  "v_mul_f32 v117, 0x4f000000, v117\n\t" \
  "v_mul_f32 v116, 0x47800000, v116\n\t" \
  "v_cvt_u32_f32 v117, v117\n\t" \
  "v_cvt_u32_f32 v116, v116\n\t" \
  "s_mov_b32 s53, s93\n\t" \
  "v_readfirstlane_b32 s46, v117\n\t" \
  "v_readfirstlane_b32 s52, v116\n\t" \
  "21:\n\t" \
  "s_add_i32 s44, s44, 1\n\t" \
  "s_and_b32 s58, s58, 4\n\t" \
  "s_and_b32 s76, s44, 64\n\t" \
  "s_or_b32 s42, s42, s76\n\t" \
  "s_cbranch_scc0 20b\n\t" \
  "s_branch 99f\n\t" \
  "14:\n\t" \
  "s_add_u32 s59, s59, 0x10000\n\t" \
  "s_cmp_lt_u32 s59, 0x100000\n\t" \
  "s_cbranch_scc1 13b\n\t" \
  "s_mov_b64 exec, -1\n\t" \
  "s_mov_b32 s42, 16\n\t" \
  "s_branch 99f\n\t" \
  "8:\n\t" \
  "s_bitcmp1_b32 s58, 1\n\t" \
  "s_cbranch_scc1 4b\n\t" \
  "s_bitcmp1_b32 s58, 2\n\t" \
  "s_cbranch_scc1 5b\n\t" \
  "v_max3_u32 v116, v32, v33, v34\n\t" \
  "v_max3_u32 v117, v35, v36, v37\n\t" \
  "v_max3_u32 v116, v116, v38, v39\n\t" \
  "v_max_u32 v116, v116, v117\n\t" \
  "v_cmp_eq_u32 vcc, -1, v116\n\t" \
  "s_cbranch_vccz 5b\n\t" \
  "s_branch 0f\n\t" \
  "9:\n\t" \
  "s_bitcmp1_b32 s58, 2\n\t" \
  "s_cbranch_scc1 6b\n\t" \
  "v_max3_u32 v116, v40, v41, v42\n\t" \
  "v_max3_u32 v117, v43, v44, v45\n\t" \
  "v_max3_u32 v116, v116, v46, v47\n\t" \
  "v_max_u32 v116, v116, v117\n\t" \
  "v_cmp_eq_u32 vcc, -1, v116\n\t" \
  "s_cbranch_vccz 6b\n\t" \
  "s_or_b32 s58, s58, 2\n\t" \
  "0:\n\t" \
  "s_or_b32 s58, s58, 1\n\t" \
  "11:\n\t" \
  "buffer_load_dwordx4 v[32:35], v100, %[rh], s40 offen sc1\n\t" \
  "buffer_load_dwordx4 v[36:39], v100, %[rh], s76 offen sc1\n\t" \
  "buffer_load_dwordx4 v[40:43], v100, %[rh], s77 offen sc1\n\t" \
  "buffer_load_dwordx4 v[44:47], v100, %[rh], s78 offen sc1\n\t" \
  "s_waitcnt lgkmcnt(0)\n\t" \
  "s_branch 10b\n\t" \
  "17:\n\t" \
  "v_mov_b64 v[72:73], 0\n\t" \
  "v_mov_b64 v[74:75], 0\n\t" \
  "v_mov_b64 v[76:77], 0\n\t" \
  "v_mov_b64 v[78:79], 0\n\t" \
  "s_add_u32 s59, s59, 1\n\t" \
  "s_bitcmp1_b32 s58, 2\n\t" \
  "s_cbranch_scc1 3b\n\t" \
  "s_waitcnt vmcnt(0)\n\t" \
  "v_max_u32 v116, v32, v33\n\t" \
  "v_max3_u32 v116, v116, v34, v35\n\t" \
  "v_max3_u32 v116, v116, v36, v37\n\t" \
  "v_max3_u32 v116, v116, v38, v39\n\t" \
  "v_max3_u32 v116, v116, v40, v41\n\t" \
  "v_max3_u32 v116, v116, v42, v43\n\t" \
  "v_max3_u32 v116, v116, v44, v45\n\t" \
  "v_max3_u32 v116, v116, v46, v47\n\t" \
  "v_cmp_eq_u32 vcc, -1, v116\n\t" \
  "s_cbranch_vccz 3b\n\t" \
  "s_bitcmp1_b32 s59, 6\n\t" \
  "s_cbranch_scc1 12f\n\t" \
  "buffer_load_dwordx4 v[32:35], v100, %[rh], s40 offen sc1\n\t" \
  "buffer_load_dwordx4 v[36:39], v100, %[rh], s76 offen sc1\n\t" \
  "buffer_load_dwordx4 v[40:43], v100, %[rh], s77 offen sc1\n\t" \
  "buffer_load_dwordx4 v[44:47], v100, %[rh], s78 offen sc1\n\t" \
  "s_branch 17b\n\t" \
  "12:\n\t" \
  "s_mov_b32 s42, s58\n\t" \
  "s_branch 99f\n\t" \
  "93:\n\t" \
  "s_mov_b32 s58, s42\n\t" \
  ".p2align 6\n\t" \
  "20:\n\t" \
  "s_and_b32 s76, s44, 7\n\t" \
  "s_lshl_b32 s76, s76, 15\n\t" \
  "s_add_i32 s40, s54, s76\n\t" \
  "s_and_b32 s76, s44, 3\n\t" \
  "s_lshl_b32 s45, s76, 16\n\t" \
  "s_and_b32 s47, s44, 3\n\t" \
  "s_lshl_b32 s76, s47, 2\n\t" \
  "s_lshl_b32 s47, s47, 20\n\t" \
  "s_add_i32 s47, s47, s55\n\t" \
  "s_lshr_b32 s76, %[um], s76\n\t" \
  "s_and_b32 s76, s76, 7\n\t" \
  "s_lshl_b32 s76, s76, 12\n\t" \
  "s_xor_b32 s47, s47, s76\n\t" \
  "s_add_i32 s49, s44, -1\n\t" \
  "s_and_b32 s48, s49, 3\n\t" \
  "s_lshl_b32 s48, s48, 16\n\t" \
  "s_lshl_b32 s50, s53, 8\n\t" \
  "s_mov_b32 s59, 0\n\t" \
  "s_mov_b32 s92, s51\n\t" \
  "s_mov_b32 s93, s53\n\t" \
  "s_xor_b32 s76, s40, 0x1000\n\t" \
  "s_xor_b32 s77, s40, 0x4000\n\t" \
  "s_xor_b32 s78, s40, 0x5000\n\t" \
  "s_cmp_lg_u32 s58, 0\n\t" \
  "s_cbranch_scc1 11f\n\t" \
  "10:\n\t" \
  "ds_read_b128 v[48:51], v102 offset:49152\n\t" \
  "ds_read_b128 v[52:55], v102 offset:50176\n\t" \
  "ds_read_b128 v[56:59], v102 offset:51200\n\t" \
  "ds_read_b128 v[60:63], v102 offset:52224\n\t" \
  "ds_read_b128 v[0:3], v102 offset:0\n\t" \
  "ds_read_b128 v[4:7], v102 offset:1024\n\t" \
  "ds_read_b128 v[8:11], v102 offset:2048\n\t" \
  "ds_read_b128 v[12:15], v102 offset:3072\n\t" \
  "ds_read_b128 v[16:19], v102 offset:16384\n\t" \
  "ds_read_b128 v[20:23], v102 offset:17408\n\t" \
  "ds_read_b128 v[24:27], v102 offset:18432\n\t" \
  "ds_read_b128 v[28:31], v102 offset:19456\n\t" \
  "s_waitcnt lgkmcnt(4)\n\t" \
  "ds_read_b128 v[80:83], v102 offset:32768\n\t" \
  "ds_read_b128 v[84:87], v102 offset:33792\n\t" \
  "ds_read_b128 v[88:91], v102 offset:34816\n\t" \
  "ds_read_b128 v[92:95], v102 offset:35840\n\t" \
  "s_bitcmp1_b32 s58, 1\n\t" \
  "s_cselect_b64 s[80:81], -1, 0\n\t" \
  "s_cmp_lg_u32 s58, 0\n\t" \
  "s_cbranch_scc1 17f\n\t" \
  "s_mov_b32 exec_hi, 0\n\t" \
  "16:\n\t" \
  "s_memtime vcc\n\t" \
  "s_waitcnt lgkmcnt(0)\n\t" \
  "s_sub_i32 s79, s41, vcc_lo\n\t" \
  "s_cmp_gt_i32 s79, 64\n\t" \
  "s_cbranch_scc1 16b\n\t" \
  "13:\n\t" \
  "buffer_load_dword v116, v110, %[rh], s48 offen sc1\n\t" \
  "s_waitcnt vmcnt(0)\n\t" \
  "15:\n\t" \
  "v_mov_b32 v117, 20000\n\t" \
  "v_cmp_ne_u32_sdwa vcc, v116, s49 src0_sel:BYTE_0 src1_sel:DWORD\n\t" \
  "v_subrev_u32 v116, s50, v116\n\t" \
  "v_ashrrev_i32 v116, 8, v116\n\t" \
  "s_nop 1\n\t" \
  "v_max_i32_dpp v116, v116, v116 quad_perm:[1,0,3,2] row_mask:0xf bank_mask:0xf\n\t" \
  "s_cbranch_vccnz 14f\n\t" \
  "s_nop 0\n\t" \
  "v_max_i32_dpp v116, v116, v116 quad_perm:[2,3,0,1] row_mask:0xf bank_mask:0xf\n\t" \
  "s_nop 1\n\t" \
  "v_max_i32_dpp v116, v116, v116 row_half_mirror row_mask:0xf bank_mask:0xf\n\t" \
  "s_nop 1\n\t" \
  "v_max_i32_dpp v116, v116, v116 row_mirror row_mask:0xf bank_mask:0xf\n\t" \
  "v_med3_i32 v116, v116, 0, v117\n\t" \
  "v_mul_u32_u24 v116, s52, v116\n\t" \
  "v_lshrrev_b32 v116, 16, v116\n\t" \
  "s_mov_b64 exec, -1\n\t" \
  "v_readlane_b32 s84, v116, 0\n\t" \
  "v_readlane_b32 s85, v116, 16\n\t" \
  "s_max_u32 s84, s84, s85\n\t" \
  "s_add_i32 s84, s84, s51\n\t" \
  "v_mov_b64 v[72:73], 0\n\t" \
  "v_mov_b64 v[74:75], 0\n\t" \
  "v_mov_b64 v[76:77], 0\n\t" \
  "v_mov_b64 v[78:79], 0\n\t" \
  "1:\n\t" \
  "s_memtime vcc\n\t" \
  "s_waitcnt lgkmcnt(0)\n\t" \
  "s_sub_i32 s79, s84, vcc_lo\n\t" \
  "s_cmp_gt_i32 s79, 128\n\t" \
  "s_cbranch_scc1 1b\n\t" \
  "2:\n\t" \
  "s_sub_i32 s79, s79, 33\n\t" \
  "s_cmp_gt_i32 s79, 56\n\t" \
  "s_cbranch_scc1 2b\n\t" \
  "buffer_load_dwordx4 v[32:35], v100, %[rh], s40 offen sc1\n\t" \
  "s_nop 12\n\t" \
  "buffer_load_dwordx4 v[36:39], v100, %[rh], s76 offen sc1\n\t" \
  "s_nop 12\n\t" \
  "buffer_load_dwordx4 v[40:43], v100, %[rh], s77 offen sc1\n\t" \
  "s_nop 12\n\t" \
  "buffer_load_dwordx4 v[44:47], v100, %[rh], s78 offen sc1\n\t" \
  "s_memtime vcc\n\t" \
  "s_memrealtime s[82:83]\n\t" \
  "s_nop 4\n\t" \
  "s_memrealtime s[84:85]\n\t" \
  "s_nop 4\n\t" \
  "s_memrealtime s[94:95]\n\t" \
  "s_nop 4\n\t" \
  "s_memrealtime s[86:87]\n\t" \
  "s_nop 4\n\t" \
  "s_memrealtime s[88:89]\n\t" \
  "s_nop 4\n\t" \
  "s_memrealtime s[90:91]\n\t" \
  "s_mov_b32 s92, 0\n\t" \
  "s_waitcnt lgkmcnt(0)\n\t" \
  "s_cmp_eq_u32 s82, s84\n\t" \
  "s_addc_u32 s92, s92, 0\n\t" \
  "s_cmp_eq_u32 s82, s94\n\t" \
  "s_addc_u32 s92, s92, 0\n\t" \
  "s_cmp_eq_u32 s82, s86\n\t" \
  "s_addc_u32 s92, s92, 0\n\t" \
  "s_cmp_eq_u32 s82, s88\n\t" \
  "s_addc_u32 s92, s92, 0\n\t" \
  "s_cmp_eq_u32 s82, s90\n\t" \
  "s_addc_u32 s92, s92, 0\n\t" \
  "s_mul_i32 s92, s92, 24\n\t" \
  "s_add_i32 s92, s92, vcc_lo\n\t" \
  "s_lshl4_add_u32 s93, s82, 64\n\t" \
  "3:\n\t" \
  "s_waitcnt lgkmcnt(0)\n\t" \
  "s_waitcnt vmcnt(3)\n\t" \
  "v_smfmac_f32_16x16x64_bf16 v[72:75], v[32:35], v[48:55], v104\n\t" \
  "ds_write_b128 v101, v[32:35] offset:12288\n\t" \
  "v_smfmac_f32_16x16x64_bf16 v[76:79], v[32:35], v[56:63], v104\n\t" \
  "s_waitcnt vmcnt(2)\n\t" \
  "ds_write_b128 v101, v[36:39] offset:13312\n\t" \
  "v_smfmac_f32_16x16x64_bf16 v[72:75], v[36:39], a[144:151], v104\n\t" \
  "v_smfmac_f32_16x16x64_bf16 v[76:79], v[36:39], a[152:159], v104\n\t" \
  "s_nop 3\n\t" \
  "v_cmp_u_f32 vcc, v72, v73\n\t" \
  "v_cmp_u_f32 s[82:83], v74, v75\n\t" \
  "s_waitcnt vmcnt(1)\n\t" \
  "v_smfmac_f32_16x16x64_bf16 v[72:75], v[40:43], a[160:167], v104\n\t" \
  "s_or_b64 vcc, vcc, s[82:83]\n\t" \
  "s_or_b64 vcc, vcc, s[80:81]\n\t" \
  "v_smfmac_f32_16x16x64_bf16 v[76:79], v[40:43], a[168:175], v104\n\t" \
  "s_waitcnt lgkmcnt(0)\n\t" \
  "s_cbranch_scc1 8f\n\t" \
  "5:\n\t" \
  "s_barrier\n\t" \
  "4:\n\t" \
  "s_memtime s[56:57]\n\t" \
  "ds_read_b128 v[48:51], v101 offset:0\n\t" \
  "ds_read_b128 v[52:55], v101 offset:1024\n\t" \
  "ds_read_b128 v[56:59], v101 offset:5120\n\t" \
  "s_waitcnt vmcnt(0)\n\t" \
  "v_smfmac_f32_16x16x64_bf16 v[72:75], v[44:47], a[176:183], v104\n\t" \
  "ds_read_b128 v[60:63], v101 offset:4096\n\t" \
  "ds_read_b128 v[64:67], v101 offset:9216\n\t" \
  "v_smfmac_f32_16x16x64_bf16 v[76:79], v[44:47], a[184:191], v104\n\t" \
  "ds_read_b128 v[68:71], v101 offset:8192\n\t" \
  "ds_write_b128 v101, v[40:43] offset:14336\n\t" \
  "s_nop 1\n\t" \
  "v_cmp_u_f32 vcc, v72, v73\n\t" \
  "v_cmp_u_f32 s[82:83], v74, v75\n\t" \
  "s_waitcnt lgkmcnt(6)\n\t" \
  "v_smfmac_f32_16x16x64_bf16 v[72:75], v[48:51], v[0:7], v104\n\t" \
  "s_or_b64 vcc, vcc, s[82:83]\n\t" \
  "v_smfmac_f32_16x16x64_bf16 v[76:79], v[48:51], v[8:15], v104\n\t" \
  "s_cbranch_scc1 9f\n\t" \
  "6:\n\t" \
  "s_waitcnt lgkmcnt(5)\n\t" \
  "v_smfmac_f32_16x16x64_bf16 v[72:75], v[52:55], a[0:7], v104\n\t" \
  "ds_write_b128 v101, v[44:47] offset:15360\n\t" \
  "v_smfmac_f32_16x16x64_bf16 v[76:79], v[52:55], a[8:15], v104\n\t" \
  "v_mov_b64 v[40:41], 0\n\t" \
  "s_waitcnt lgkmcnt(5)\n\t" \
  "v_smfmac_f32_16x16x64_bf16 v[72:75], v[56:59], a[48:55], v104\n\t" \
  "v_mov_b64 v[42:43], 0\n\t" \
  "v_smfmac_f32_16x16x64_bf16 v[76:79], v[56:59], a[56:63], v104\n\t" \
  "s_waitcnt lgkmcnt(4)\n\t" \
  "v_smfmac_f32_16x16x64_bf16 v[72:75], v[60:63], v[16:23], v104\n\t" \
  "s_waitcnt lgkmcnt(0)\n\t" \
  "s_barrier\n\t" \
  "v_smfmac_f32_16x16x64_bf16 v[76:79], v[60:63], v[24:31], v104\n\t" \
  "ds_read_b128 v[48:51], v101 offset:2048\n\t" \
  "ds_read_b128 v[52:55], v101 offset:6144\n\t" \
  "v_smfmac_f32_16x16x64_bf16 v[72:75], v[64:67], a[96:103], v104\n\t" \
  "ds_read_b128 v[56:59], v101 offset:10240\n\t" \
  "ds_read_b128 v[60:63], v101 offset:3072\n\t" \
  "v_smfmac_f32_16x16x64_bf16 v[76:79], v[64:67], a[104:111], v104\n\t" \
  "ds_read_b128 v[32:35], v101 offset:7168\n\t" \
  "ds_read_b128 v[36:39], v101 offset:11264\n\t" \
  "v_smfmac_f32_16x16x64_bf16 v[72:75], v[68:71], v[80:87], v104\n\t" \
  "ds_read_b128 v[0:3], v103 offset:0\n\t" \
  "v_mov_b64 v[44:45], 0\n\t" \
  "v_smfmac_f32_16x16x64_bf16 v[76:79], v[68:71], v[88:95], v104\n\t" \
  "v_mov_b64 v[46:47], 0\n\t" \
  "s_waitcnt lgkmcnt(5)\n\t" \
  "v_smfmac_f32_16x16x64_bf16 v[72:75], v[48:51], a[16:23], v104\n\t" \
  "ds_read_b128 v[4:7], v103 offset:1024\n\t" \
  "v_mov_b64 v[80:81], 0\n\t" \
  "v_smfmac_f32_16x16x64_bf16 v[72:75], v[52:55], a[64:71], v104\n\t" \
  "v_mov_b64 v[82:83], 0\n\t" \
  "s_waitcnt lgkmcnt(2)\n\t" \
  "v_smfmac_f32_16x16x64_bf16 v[72:75], v[56:59], a[112:119], v104\n\t" \
  "ds_read_b128 v[8:11], v103 offset:2048\n\t" \
  "v_mov_b64 v[84:85], 0\n\t" \
  "v_smfmac_f32_16x16x64_bf16 v[72:75], v[60:63], a[32:39], v104\n\t" \
  "ds_read_b128 v[12:15], v103 offset:3072\n\t" \
  "v_mov_b64 v[86:87], 0\n\t" \
  "v_smfmac_f32_16x16x64_bf16 v[72:75], v[32:35], a[80:87], v104\n\t" \
  "ds_read_b128 v[16:19], v103 offset:16384\n\t" \
  "v_mov_b64 v[88:89], 0\n\t" \
  "v_smfmac_f32_16x16x64_bf16 v[72:75], v[36:39], a[128:135], v104\n\t" \
  "ds_read_b128 v[20:23], v103 offset:17408\n\t" \
  "v_mov_b64 v[90:91], 0\n\t" \
  "v_smfmac_f32_16x16x64_bf16 v[76:79], v[48:51], a[24:31], v104\n\t" \
  "ds_read_b128 v[24:27], v103 offset:18432\n\t" \
  "v_mov_b64 v[92:93], 0\n\t" \
  "v_smfmac_f32_16x16x64_bf16 v[76:79], v[52:55], a[72:79], v104\n\t" \
  "v_add_f32 v96, v72, v74\n\t" \
  "v_add_f32 v97, v73, v75\n\t" \
  "v_cvt_pk_bf16_f32 v96, v96, v97\n\t" \
  "ds_write_b32 v105, v96\n\t" \
  "v_smfmac_f32_16x16x64_bf16 v[76:79], v[56:59], a[120:127], v104\n\t" \
  "ds_read_b128 v[28:31], v103 offset:19456\n\t" \
  "v_mov_b64 v[94:95], 0\n\t" \
  "v_smfmac_f32_16x16x64_bf16 v[76:79], v[60:63], a[40:47], v104\n\t" \
  "v_smfmac_f32_16x16x64_bf16 v[76:79], v[32:35], a[88:95], v104\n\t" \
  "v_smfmac_f32_16x16x64_bf16 v[76:79], v[36:39], a[136:143], v104\n\t" \
  "s_waitcnt lgkmcnt(1)\n\t" \
  "s_barrier\n\t" \
  "ds_read_b64_tr_b16 v[96:97], v107 offset:0\n\t" \
  "ds_read_b64_tr_b16 v[98:99], v107 offset:64\n\t" \
  "ds_read_b64_tr_b16 v[56:57], v107 offset:1024\n\t" \
  "ds_read_b64_tr_b16 v[58:59], v107 offset:1088\n\t" \
  "s_nop 1\n\t" \
  "v_add_f32 v116, v76, v78\n\t" \
  "v_add_f32 v117, v77, v79\n\t" \
  "v_cvt_pk_bf16_f32 v116, v116, v117\n\t" \
  "ds_write_b32 v105, v116 offset:256\n\t" \
  "s_waitcnt lgkmcnt(3)\n\t" \
  "v_smfmac_f32_16x16x64_bf16 v[40:43], v[96:99], a[192:199], v106\n\t" \
  "v_smfmac_f32_16x16x64_bf16 v[44:47], v[96:99], a[200:207], v106\n\t" \
  "s_waitcnt lgkmcnt(0)\n\t" \
  "s_barrier\n\t" \
  "ds_read_b64_tr_b16 v[52:53], v107 offset:256\n\t" \
  "ds_read_b64_tr_b16 v[54:55], v107 offset:320\n\t" \
  "v_smfmac_f32_16x16x64_bf16 v[40:43], v[56:59], a[224:231], v106\n\t" \
  "ds_read_b64_tr_b16 v[60:61], v107 offset:1280\n\t" \
  "ds_read_b64_tr_b16 v[62:63], v107 offset:1344\n\t" \
  "v_smfmac_f32_16x16x64_bf16 v[44:47], v[56:59], a[232:239], v106\n\t" \
  "v_smfmac_f32_16x16x64_bf16 v[80:83], v[96:99], a[240:247], v106\n\t" \
  "v_mov_b64 v[32:33], 0\n\t" \
  "v_smfmac_f32_16x16x64_bf16 v[84:87], v[96:99], a[248:255], v106\n\t" \
  "v_mov_b64 v[34:35], 0\n\t" \
  "v_smfmac_f32_16x16x64_bf16 v[80:83], v[56:59], v[136:143], v106\n\t" \
  "v_mov_b64 v[36:37], 0\n\t" \
  "v_smfmac_f32_16x16x64_bf16 v[84:87], v[56:59], v[144:151], v106\n\t" \
  "s_waitcnt lgkmcnt(2)\n\t" \
  "v_smfmac_f32_16x16x64_bf16 v[40:43], v[52:55], a[208:215], v106\n\t" \
  "v_smfmac_f32_16x16x64_bf16 v[44:47], v[52:55], a[216:223], v106\n\t" \
  "s_xor_b32 s76, s47, 0x40000\n\t" \
  "s_waitcnt lgkmcnt(0)\n\t" \
  "v_smfmac_f32_16x16x64_bf16 v[40:43], v[60:63], v[0:7], v106\n\t" \
  "v_mov_b64 v[38:39], 0\n\t" \
  "s_xor_b32 s77, s47, 0x84000\n\t" \
  "v_smfmac_f32_16x16x64_bf16 v[44:47], v[60:63], v[8:15], v106\n\t" \
  "s_xor_b32 s78, s47, 0xc4000\n\t" \
  "v_smfmac_f32_16x16x64_bf16 v[80:83], v[52:55], v[120:127], v106\n\t" \
  "ds_read_b128 v[0:3], v103 offset:32768\n\t" \
  "v_cvt_pk_bf16_f32 v64, v40, v41\n\t" \
  "v_smfmac_f32_16x16x64_bf16 v[84:87], v[52:55], v[128:135], v106\n\t" \
  "ds_read_b128 v[4:7], v103 offset:33792\n\t" \
  "v_cvt_pk_bf16_f32 v66, v42, v43\n\t" \
  "v_smfmac_f32_16x16x64_bf16 v[80:83], v[60:63], v[16:23], v106\n\t" \
  "ds_read_b128 v[8:11], v103 offset:34816\n\t" \
  "v_cvt_pk_bf16_f32 v65, v44, v45\n\t" \
  "v_smfmac_f32_16x16x64_bf16 v[84:87], v[60:63], v[24:31], v106\n\t" \
  "ds_read_b128 v[12:15], v103 offset:35840\n\t" \
  "v_cvt_pk_bf16_f32 v67, v46, v47\n\t" \
  "v_smfmac_f32_16x16x64_bf16 v[88:91], v[96:99], v[152:159], v106\n\t" \
  "v_cvt_pk_bf16_f32 v68, v80, v81\n\t" \
  "v_smfmac_f32_16x16x64_bf16 v[92:95], v[96:99], v[160:167], v106\n\t" \
  "v_cvt_pk_bf16_f32 v70, v82, v83\n\t" \
  "v_smfmac_f32_16x16x64_bf16 v[88:91], v[52:55], v[168:175], v106\n\t" \
  "v_cvt_pk_bf16_f32 v69, v84, v85\n\t" \
  "v_smfmac_f32_16x16x64_bf16 v[92:95], v[52:55], v[176:183], v106\n\t" \
  "buffer_store_dwordx4 v[64:67], v108, %[ru], s47 offen sc1\n\t" \
  "v_cvt_pk_bf16_f32 v71, v86, v87\n\t" \
  "v_smfmac_f32_16x16x64_bf16 v[88:91], v[56:59], v[184:191], v106\n\t" \
  "ds_read_b128 v[16:19], v103 offset:49152\n\t" \
  "v_smfmac_f32_16x16x64_bf16 v[92:95], v[56:59], v[192:199], v106\n\t" \
  "ds_read_b128 v[20:23], v103 offset:50176\n\t" \
  "s_waitcnt lgkmcnt(4)\n\t" \
  "v_smfmac_f32_16x16x64_bf16 v[88:91], v[60:63], v[0:7], v106\n\t" \
  "ds_read_b128 v[24:27], v103 offset:51200\n\t" \
  "s_waitcnt lgkmcnt(3)\n\t" \
  "v_smfmac_f32_16x16x64_bf16 v[92:95], v[60:63], v[8:15], v106\n\t" \
  "ds_read_b128 v[28:31], v103 offset:52224\n\t" \
  "buffer_store_dwordx4 v[68:71], v108, %[ru], s76 offen sc1\n\t" \
  "v_smfmac_f32_16x16x64_bf16 v[32:35], v[96:99], v[200:207], v106\n\t" \
  "v_cvt_pk_bf16_f32 v72, v88, v89\n\t" \
  "v_smfmac_f32_16x16x64_bf16 v[36:39], v[96:99], v[208:215], v106\n\t" \
  "v_cvt_pk_bf16_f32 v74, v90, v91\n\t" \
  "v_smfmac_f32_16x16x64_bf16 v[32:35], v[52:55], v[216:223], v106\n\t" \
  "v_cvt_pk_bf16_f32 v73, v92, v93\n\t" \
  "v_smfmac_f32_16x16x64_bf16 v[36:39], v[52:55], v[224:231], v106\n\t" \
  "v_cvt_pk_bf16_f32 v75, v94, v95\n\t" \
  "v_smfmac_f32_16x16x64_bf16 v[32:35], v[56:59], v[232:239], v106\n\t" \
  "s_waitcnt lgkmcnt(2)\n\t" \
  "v_smfmac_f32_16x16x64_bf16 v[32:35], v[60:63], v[16:23], v106\n\t" \
  "v_smfmac_f32_16x16x64_bf16 v[36:39], v[56:59], v[240:247], v106\n\t" \
  "s_waitcnt lgkmcnt(0)\n\t" \
  "v_smfmac_f32_16x16x64_bf16 v[36:39], v[60:63], v[24:31], v106\n\t" \
  "buffer_store_dwordx4 v[72:75], v108, %[ru], s77 offen sc1\n\t" \
  "v_cvt_pk_bf16_f32 v76, v32, v33\n\t" \
  "v_cvt_pk_bf16_f32 v78, v34, v35\n\t" \
  "s_nop 5\n\t" \
  "v_cvt_pk_bf16_f32 v77, v36, v37\n\t" \
  "v_cvt_pk_bf16_f32 v79, v38, v39\n\t" \
  "buffer_store_dwordx4 v[76:79], v108, %[ru], s78 offen sc1\n\t" \
  "s_memtime s[76:77]\n\t" \
  "s_mov_b32 s42, 0\n\t" \
  "s_waitcnt lgkmcnt(0)\n\t" \
  "s_mov_b32 s57, s76\n\t" \
  "s_add_i32 s41, s56, 1950\n\t" \
  "s_sub_i32 s76, s92, s51\n\t" \
  "s_sub_i32 s77, s93, s53\n\t" \
  "s_cmp_eq_u32 s76, 0\n\t" \
  "s_cbranch_scc1 21f\n\t" \
  "v_cvt_f32_u32 v117, s76\n\t" \
  "v_cvt_f32_u32 v116, s77\n\t" \
  "v_rcp_f32 v117, v117\n\t" \
  "s_mov_b32 s51, s92\n\t" \
  "v_mul_f32 v117, v117, v116\n\t" \
  "v_max_f32 v117, 0x3f028f5c, v117\n\t" \
  "v_min_f32 v117, 0x3ffeb852, v117\n\t" \
  "v_rcp_f32 v116, v117\n\t" \
  "v_mul_f32 v117, 0x4f000000, v117\n\t" \
  "v_mul_f32 v116, 0x47800000, v116\n\t" \
  "v_cvt_u32_f32 v117, v117\n\t" \
  "v_cvt_u32_f32 v116, v116\n\t" \
  "s_mov_b32 s53, s93\n\t" \
  "v_readfirstlane_b32 s46, v117\n\t" \
  "v_readfirstlane_b32 s52, v116\n\t" \
  "21:\n\t" \
  "s_add_i32 s44, s44, 1\n\t" \
  "s_and_b32 s58, s58, 4\n\t" \
  "s_and_b32 s76, s44, 64\n\t" \
  "s_or_b32 s42, s42, s76\n\t" \
  "s_cbranch_scc0 20b\n\t" \
  "s_branch 99f\n\t" \
  "14:\n\t" \
  "s_add_u32 s59, s59, 0x10000\n\t" \
  "s_cmp_lt_u32 s59, 0x100000\n\t" \
  "s_cbranch_scc1 13b\n\t" \
  "s_mov_b64 exec, -1\n\t" \
  "s_mov_b32 s42, 16\n\t" \
  "s_branch 99f\n\t" \
  "8:\n\t" \
  "s_bitcmp1_b32 s58, 1\n\t" \
  "s_cbranch_scc1 4b\n\t" \
  "s_bitcmp1_b32 s58, 2\n\t" \
  "s_cbranch_scc1 5b\n\t" \
  "v_max3_u32 v116, v32, v33, v34\n\t" \
  "v_max3_u32 v117, v35, v36, v37\n\t" \
  "v_max3_u32 v116, v116, v38, v39\n\t" \
  "v_max_u32 v116, v116, v117\n\t" \
  "v_cmp_eq_u32 vcc, -1, v116\n\t" \
  "s_cbranch_vccz 5b\n\t" \
  "s_branch 0f\n\t" \
  "9:\n\t" \
  "s_bitcmp1_b32 s58, 2\n\t" \
  "s_cbranch_scc1 6b\n\t" \
  "v_max3_u32 v116, v40, v41, v42\n\t" \
  "v_max3_u32 v117, v43, v44, v45\n\t" \
  "v_max3_u32 v116, v116, v46, v47\n\t" \
  "v_max_u32 v116, v116, v117\n\t" \
  "v_cmp_eq_u32 vcc, -1, v116\n\t" \
  "s_cbranch_vccz 6b\n\t" \
  "s_or_b32 s58, s58, 2\n\t" \
  "0:\n\t" \
  "s_or_b32 s58, s58, 1\n\t" \
  "11:\n\t" \
  "buffer_load_dwordx4 v[32:35], v100, %[rh], s40 offen sc1\n\t" \
  "buffer_load_dwordx4 v[36:39], v100, %[rh], s76 offen sc1\n\t" \
  "buffer_load_dwordx4 v[40:43], v100, %[rh], s77 offen sc1\n\t" \
  "buffer_load_dwordx4 v[44:47], v100, %[rh], s78 offen sc1\n\t" \
  "s_waitcnt lgkmcnt(0)\n\t" \
  "s_branch 10b\n\t" \
  "17:\n\t" \
  "v_mov_b64 v[72:73], 0\n\t" \
  "v_mov_b64 v[74:75], 0\n\t" \
  "v_mov_b64 v[76:77], 0\n\t" \
  "v_mov_b64 v[78:79], 0\n\t" \
  "s_add_u32 s59, s59, 1\n\t" \
  "s_bitcmp1_b32 s58, 2\n\t" \
  "s_cbranch_scc1 3b\n\t" \
  "s_waitcnt vmcnt(0)\n\t" \
  "v_max_u32 v116, v32, v33\n\t" \
  "v_max3_u32 v116, v116, v34, v35\n\t" \
  "v_max3_u32 v116, v116, v36, v37\n\t" \
  "v_max3_u32 v116, v116, v38, v39\n\t" \
  "v_max3_u32 v116, v116, v40, v41\n\t" \
  "v_max3_u32 v116, v116, v42, v43\n\t" \
  "v_max3_u32 v116, v116, v44, v45\n\t" \
  "v_max3_u32 v116, v116, v46, v47\n\t" \
  "v_cmp_eq_u32 vcc, -1, v116\n\t" \
  "s_cbranch_vccz 3b\n\t" \
  "s_bitcmp1_b32 s59, 6\n\t" \
  "s_cbranch_scc1 12f\n\t" \
  "buffer_load_dwordx4 v[32:35], v100, %[rh], s40 offen sc1\n\t" \
  "buffer_load_dwordx4 v[36:39], v100, %[rh], s76 offen sc1\n\t" \
  "buffer_load_dwordx4 v[40:43], v100, %[rh], s77 offen sc1\n\t" \
  "buffer_load_dwordx4 v[44:47], v100, %[rh], s78 offen sc1\n\t" \
  "s_branch 17b\n\t" \
  "12:\n\t" \
  "s_mov_b32 s42, s58\n\t" \
  "s_branch 99f\n\t" \
  "99:\n\t" \
  "s_waitcnt vmcnt(0) lgkmcnt(0)\n\t" \
  ".Lchain_last:\n\t" \
  ""
#define CB_BODY \
  "s_mov_b32 s58, 8\n\t" \
  "s_mov_b32 s59, 0\n\t" \
  "s_sub_i32 s56, s51, 8000\n\t" \
  "s_lshl_b32 s50, s53, 8\n\t" \
  "s_branch 40f\n\t" \
  ".p2align 6\n\t" \
  "20:\n\t" \
  "v_readfirstlane_b32 s101, v211\n\t" \
  "s_mov_b64 exec, 0xff\n\t" \
  "s_max_i32 s101, s101, 0\n\t" \
  "s_min_i32 s101, s101, 240\n\t" \
  "s_mov_b32 s88, s51\n\t" \
  "s_add_i32 s85, s44, -1\n\t" \
  "s_add_i32 s79, s44, 2\n\t" \
  "v_cmp_gt_i32 vcc, s79, v210\n\t" \
  "s_and_b32 s96, s79, 15\n\t" \
  "s_lshl_b32 s96, s96, 22\n\t" \
  "s_add_i32 s96, s96, s42\n\t" \
  "s_lshr_b32 s82, s96, 21\n\t" \
  "s_bitcmp0_b32 %[sel], s82\n\t" \
  "s_cselect_b32 s82, 0x2000, 0\n\t" \
  "s_add_i32 s96, s96, s82\n\t" \
  "s_cmp_lt_u32 s79, 64\n\t" \
  "s_cselect_b32 s79, vcc_lo, -1\n\t" \
  "s_cmp_eq_u32 s79, 0\n\t" \
  "s_cselect_b32 s96, s96, 0x7f000000\n\t" \
  "s_cmp_eq_u32 s44, 0\n\t" \
  "s_cbranch_scc1 23f\n\t" \
  "21:\n\t" \
  "v_cmp_ne_u32_sdwa vcc, v208, s85 src0_sel:BYTE_0 src1_sel:DWORD\n\t" \
  "v_subrev_u32 v208, s50, v208\n\t" \
  "v_ashrrev_i32 v208, 8, v208\n\t" \
  "s_nop 1\n\t" \
  "v_max_i32_dpp v208, v208, v208 quad_perm:[1,0,3,2] row_mask:0xf bank_mask:0xf\n\t" \
  "s_cbranch_vccnz 22f\n\t" \
  "24:\n\t" \
  "s_nop 0\n\t" \
  "v_max_i32_dpp v208, v208, v208 quad_perm:[2,3,0,1] row_mask:0xf bank_mask:0xf\n\t" \
  "s_nop 1\n\t" \
  "v_max_i32_dpp v208, v208, v208 row_half_mirror row_mask:0xf bank_mask:0xf\n\t" \
  "v_add_u32 v208, 1056, v208\n\t" \
  "v_med3_i32 v208, v208, 0, v246\n\t" \
  "v_mul_u32_u24 v208, s52, v208\n\t" \
  "v_lshrrev_b32 v208, 16, v208\n\t" \
  "s_nop 0\n\t" \
  "v_readlane_b32 s88, v208, 0\n\t" \
  "s_add_i32 s88, s88, s51\n\t" \
  "s_add_i32 s88, s88, s101\n\t" \
  "23:\n\t" \
  "s_mov_b32 exec_lo, -1\n\t" \
  "v_min_u32 v209, 1520, v209\n\t" \
  "s_nop 1\n\t" \
  "v_max_u32_dpp v209, v209, v209 quad_perm:[1,0,3,2] row_mask:0xf bank_mask:0xf\n\t" \
  "s_nop 1\n\t" \
  "v_max_u32_dpp v209, v209, v209 quad_perm:[2,3,0,1] row_mask:0xf bank_mask:0xf\n\t" \
  "s_nop 1\n\t" \
  "v_max_u32_dpp v209, v209, v209 row_half_mirror row_mask:0xf bank_mask:0xf\n\t" \
  "s_nop 1\n\t" \
  "v_max_u32_dpp v209, v209, v209 row_mirror row_mask:0xf bank_mask:0xf\n\t" \
  "s_nop 1\n\t" \
  "v_readlane_b32 s89, v209, 0\n\t" \
  "v_readlane_b32 s90, v209, 16\n\t" \
  "s_max_u32 s57, s89, s90\n\t" \
  "s_add_i32 s57, s57, 300\n\t" \
  "s_and_b32 s81, s44, 3\n\t" \
  "s_lshl_b32 s81, s81, 16\n\t" \
  "s_mov_b64 exec, 0xff\n\t" \
  "1:\n\t" \
  "s_memtime vcc\n\t" \
  "s_waitcnt lgkmcnt(0)\n\t" \
  "s_sub_i32 s79, s88, vcc_lo\n\t" \
  "s_cmp_gt_i32 s79, 128\n\t" \
  "s_cbranch_scc1 1b\n\t" \
  "2:\n\t" \
  "s_sub_i32 s79, s79, 33\n\t" \
  "s_cmp_gt_i32 s79, 56\n\t" \
  "s_cbranch_scc1 2b\n\t" \
  "buffer_load_dword v208, v234, %[rh], s81 offen sc1\n\t" \
  "s_mov_b64 exec, -1\n\t" \
  "v_accvgpr_read_b32 v0, a0\n\t" \
  "v_accvgpr_read_b32 v1, a8\n\t" \
  "v_accvgpr_read_b32 v2, a2\n\t" \
  "v_accvgpr_read_b32 v3, a10\n\t" \
  "v_accvgpr_read_b32 v4, a4\n\t" \
  "v_accvgpr_read_b32 v5, a12\n\t" \
  "v_accvgpr_read_b32 v6, a6\n\t" \
  "v_accvgpr_read_b32 v7, a14\n\t" \
  "v_accvgpr_read_b32 v8, a1\n\t" \
  "v_accvgpr_read_b32 v9, a9\n\t" \
  "v_accvgpr_read_b32 v10, a3\n\t" \
  "v_accvgpr_read_b32 v11, a11\n\t" \
  "v_accvgpr_read_b32 v12, a5\n\t" \
  "v_accvgpr_read_b32 v13, a13\n\t" \
  "v_accvgpr_read_b32 v14, a7\n\t" \
  "v_accvgpr_read_b32 v15, a15\n\t" \
  "v_accvgpr_read_b32 v16, a32\n\t" \
  "v_accvgpr_read_b32 v17, a40\n\t" \
  "v_accvgpr_read_b32 v18, a34\n\t" \
  "v_accvgpr_read_b32 v19, a42\n\t" \
  "v_accvgpr_read_b32 v20, a36\n\t" \
  "v_accvgpr_read_b32 v21, a44\n\t" \
  "v_accvgpr_read_b32 v22, a38\n\t" \
  "v_accvgpr_read_b32 v23, a46\n\t" \
  "v_accvgpr_read_b32 v24, a33\n\t" \
  "v_accvgpr_read_b32 v25, a41\n\t" \
  "v_accvgpr_read_b32 v26, a35\n\t" \
  "v_accvgpr_read_b32 v27, a43\n\t" \
  "v_accvgpr_read_b32 v28, a37\n\t" \
  "v_accvgpr_read_b32 v29, a45\n\t" \
  "v_accvgpr_read_b32 v30, a39\n\t" \
  "v_accvgpr_read_b32 v31, a47\n\t" \
  "v_accvgpr_read_b32 v32, a16\n\t" \
  "v_accvgpr_read_b32 v33, a24\n\t" \
  "v_accvgpr_read_b32 v34, a18\n\t" \
  "v_accvgpr_read_b32 v35, a26\n\t" \
  "v_accvgpr_read_b32 v36, a20\n\t" \
  "v_accvgpr_read_b32 v37, a28\n\t" \
  "v_accvgpr_read_b32 v38, a22\n\t" \
  "v_accvgpr_read_b32 v39, a30\n\t" \
  "v_accvgpr_read_b32 v40, a17\n\t" \
  "v_accvgpr_read_b32 v41, a25\n\t" \
  "v_accvgpr_read_b32 v42, a19\n\t" \
  "v_accvgpr_read_b32 v43, a27\n\t" \
  "v_accvgpr_read_b32 v44, a21\n\t" \
  "v_accvgpr_read_b32 v45, a29\n\t" \
  "v_accvgpr_read_b32 v46, a23\n\t" \
  "v_accvgpr_read_b32 v47, a31\n\t" \
  "v_accvgpr_read_b32 v48, a48\n\t" \
  "v_accvgpr_read_b32 v49, a56\n\t" \
  "v_accvgpr_read_b32 v50, a50\n\t" \
  "v_accvgpr_read_b32 v51, a58\n\t" \
  "v_accvgpr_read_b32 v52, a52\n\t" \
  "v_accvgpr_read_b32 v53, a60\n\t" \
  "v_accvgpr_read_b32 v54, a54\n\t" \
  "v_accvgpr_read_b32 v55, a62\n\t" \
  "v_accvgpr_read_b32 v56, a49\n\t" \
  "v_accvgpr_read_b32 v57, a57\n\t" \
  "v_accvgpr_read_b32 v58, a51\n\t" \
  "v_accvgpr_read_b32 v59, a59\n\t" \
  "v_accvgpr_read_b32 v60, a53\n\t" \
  "v_accvgpr_read_b32 v61, a61\n\t" \
  "v_accvgpr_read_b32 v62, a55\n\t" \
  "v_accvgpr_read_b32 v63, a63\n\t" \
  "s_mov_b64 exec, 0xff\n\t" \
  "s_add_i32 s89, s57, s53\n\t" \
  "s_mov_b32 s84, 600\n\t" \
  "s_mov_b32 s90, 0\n\t" \
  "13:\n\t" \
  "s_waitcnt vmcnt(0)\n\t" \
  "s_memtime s[86:87]\n\t" \
  "v_cmp_ne_u32_sdwa vcc, v208, s44 src0_sel:BYTE_0 src1_sel:DWORD\n\t" \
  "v_subrev_u32 v208, s50, v208\n\t" \
  "v_ashrrev_i32 v208, 8, v208\n\t" \
  "s_nop 1\n\t" \
  "v_max_i32_dpp v208, v208, v208 quad_perm:[1,0,3,2] row_mask:0xf bank_mask:0xf\n\t" \
  "s_nop 1\n\t" \
  "v_max_i32_dpp v208, v208, v208 quad_perm:[2,3,0,1] row_mask:0xf bank_mask:0xf\n\t" \
  "s_cbranch_vccnz 14f\n\t" \
  "15:\n\t" \
  "s_nop 0\n\t" \
  "v_max_i32_dpp v208, v208, v208 row_half_mirror row_mask:0xf bank_mask:0xf\n\t" \
  "v_add_u32 v209, 255, v208\n\t" \
  "v_med3_i32 v209, v209, 0, v246\n\t" \
  "v_mul_u32_u24 v209, s52, v209\n\t" \
  "v_readlane_b32 s82, v208, 0\n\t" \
  "v_lshrrev_b32 v209, 16, v209\n\t" \
  "s_waitcnt lgkmcnt(0)\n\t" \
  "s_sub_i32 s79, s51, s86\n\t" \
  "v_readlane_b32 s83, v209, 0\n\t" \
  "s_mov_b64 exec, -1\n\t" \
  "s_add_i32 s79, s79, s83\n\t" \
  "s_add_i32 s79, s79, s90\n\t" \
  "s_add_i32 s79, s79, s101\n\t" \
  "s_sub_i32 s85, s84, s79\n\t" \
  "s_max_i32 s85, s85, 0\n\t" \
  "s_add_i32 s85, s85, s90\n\t" \
  "s_add_i32 s79, s79, -582\n\t" \
  "s_mul_hi_u32 s85, s85, s46\n\t" \
  "s_lshl1_add_u32 s85, s85, s82\n\t" \
  "s_add_i32 s85, s85, s89\n\t" \
  "s_lshl_b32 s85, s85, 8\n\t" \
  "s_or_b32 s85, s85, s44\n\t" \
  "v_mov_b32 v209, s85\n\t" \
  "buffer_store_dword v209, v235, %[rh], s81 offen sc1\n\t" \
  "s_add_i32 s85, s53, s82\n\t" \
  "v_mov_b32 v208, s85\n\t" \
  "ds_write_b32 v245, v208\n\t" \
  "3:\n\t" \
  "s_sub_i32 s79, s79, 33\n\t" \
  "s_cmp_gt_i32 s79, 56\n\t" \
  "s_cbranch_scc1 3b\n\t" \
  "buffer_load_dwordx4 v[192:195], v226, %[rh], s49 offen offset:0 sc1\n\t" \
  "s_nop 6\n\t" \
  "buffer_load_dwordx4 v[196:199], v226, %[rh], s49 offen offset:2048 sc1\n\t" \
  "s_nop 6\n\t" \
  "buffer_load_dwordx4 v[200:203], v226, %[rh], s80 offen offset:0 sc1\n\t" \
  "s_nop 6\n\t" \
  "buffer_load_dwordx4 v[204:207], v226, %[rh], s80 offen offset:2048 sc1\n\t" \
  "s_nop 6\n\t" \
  "buffer_load_dwordx4 v[64:67], v225, %[ru], s48 offen offset:0 sc1\n\t" \
  "s_nop 6\n\t" \
  "buffer_load_dwordx4 v[72:75], v225, %[ru], s76 offen offset:0 sc1\n\t" \
  "s_nop 6\n\t" \
  "buffer_load_dwordx4 v[68:71], v225, %[ru], s77 offen offset:0 sc1\n\t" \
  "s_nop 6\n\t" \
  "buffer_load_dwordx4 v[76:79], v225, %[ru], s78 offen offset:0 sc1\n\t" \
  "s_add_i32 s79, s79, 184\n\t" \
  "4:\n\t" \
  "s_sub_i32 s79, s79, 33\n\t" \
  "s_cmp_gt_i32 s79, 56\n\t" \
  "s_cbranch_scc1 4b\n\t" \
  "buffer_load_dwordx4 v[212:215], v228, %[rh], s54 offen offset:0 sc1\n\t" \
  "s_nop 6\n\t" \
  "buffer_load_dwordx4 v[208:211], v227, %[rh], s54 offen offset:0 sc1\n\t" \
  "10:\n\t" \
  "s_waitcnt vmcnt(9)\n\t" \
  "v_smfmac_f32_16x16x64_bf16 v[144:147], v[192:195], v[0:7], v229\n\t" \
  "v_smfmac_f32_16x16x64_bf16 v[148:151], v[192:195], v[8:15], v229\n\t" \
  "s_waitcnt vmcnt(8)\n\t" \
  "v_smfmac_f32_16x16x64_bf16 v[152:155], v[196:199], v[16:23], v229\n\t" \
  "v_cvt_pk_bf16_f32 v176, v144, v145\n\t" \
  "v_cvt_pk_bf16_f32 v177, v146, v147\n\t" \
  "v_smfmac_f32_16x16x64_bf16 v[156:159], v[196:199], v[24:31], v229\n\t" \
  "v_cvt_pk_bf16_f32 v184, v148, v149\n\t" \
  "v_cvt_pk_bf16_f32 v185, v150, v151\n\t" \
  "s_waitcnt vmcnt(7)\n\t" \
  "v_smfmac_f32_16x16x64_bf16 v[160:163], v[200:203], v[32:39], v229\n\t" \
  "v_cvt_pk_bf16_f32 v178, v152, v153\n\t" \
  "v_cvt_pk_bf16_f32 v179, v154, v155\n\t" \
  "v_smfmac_f32_16x16x64_bf16 v[164:167], v[200:203], v[40:47], v229\n\t" \
  "v_cvt_pk_bf16_f32 v186, v156, v157\n\t" \
  "v_cvt_pk_bf16_f32 v187, v158, v159\n\t" \
  "s_waitcnt vmcnt(6)\n\t" \
  "v_smfmac_f32_16x16x64_bf16 v[168:171], v[204:207], v[48:55], v229\n\t" \
  "v_cvt_pk_bf16_f32 v180, v160, v161\n\t" \
  "v_cvt_pk_bf16_f32 v181, v162, v163\n\t" \
  "v_smfmac_f32_16x16x64_bf16 v[172:175], v[204:207], v[56:63], v229\n\t" \
  "v_cvt_pk_bf16_f32 v188, v164, v165\n\t" \
  "v_cvt_pk_bf16_f32 v189, v166, v167\n\t" \
  "s_waitcnt vmcnt(5)\n\t" \
  "s_waitcnt vmcnt(4)\n\t" \
  "v_swap_b32 v65, v72\n\t" \
  "v_swap_b32 v67, v74\n\t" \
  "s_nop 1\n\t" \
  "v_cvt_pk_bf16_f32 v182, v168, v169\n\t" \
  "v_cvt_pk_bf16_f32 v183, v170, v171\n\t" \
  "v_cvt_pk_bf16_f32 v190, v172, v173\n\t" \
  "v_cvt_pk_bf16_f32 v191, v174, v175\n\t" \
  "s_waitcnt vmcnt(1)\n\t" \
  "v_smfmac_f32_16x16x64_bf16 v[216:219], v[212:215], v[176:183], v230\n\t" \
  "v_smfmac_f32_16x16x64_bf16 v[220:223], v[212:215], v[184:191], v230\n\t" \
  "v_swap_b32 v69, v76\n\t" \
  "v_swap_b32 v71, v78\n\t" \
  "s_waitcnt vmcnt(0)\n\t" \
  "s_nop 0\n\t" \
  "v_smfmac_f32_16x16x64_bf16 v[216:219], v[208:211], v[64:71], v229\n\t" \
  "v_smfmac_f32_16x16x64_bf16 v[220:223], v[208:211], v[72:79], v229\n\t" \
  "s_nop 3\n\t" \
  "ds_write2st64_b32 v231, v216, v217 offset0:0 offset1:1\n\t" \
  "ds_write2st64_b32 v231, v218, v219 offset0:2 offset1:3\n\t" \
  "v_cmp_u_f32 vcc, v216, v217\n\t" \
  "v_cmp_u_f32 s[82:83], v218, v219\n\t" \
  "ds_write2st64_b32 v231, v220, v221 offset0:16 offset1:17\n\t" \
  "ds_write2st64_b32 v231, v222, v223 offset0:18 offset1:19\n\t" \
  "v_cmp_u_f32 s[84:85], v220, v222\n\t" \
  "s_or_b64 vcc, vcc, s[82:83]\n\t" \
  "s_or_b64 vcc, vcc, s[84:85]\n\t" \
  "s_cbranch_scc1 8f\n\t" \
  "6:\n\t" \
  "s_waitcnt lgkmcnt(0)\n\t" \
  "s_barrier\n\t" \
  "ds_read_b64 v[136:137], v232 offset:0\n\t" \
  "ds_read_b64 v[138:139], v232 offset:1024\n\t" \
  "ds_read_b64 v[140:141], v232 offset:2048\n\t" \
  "ds_read_b64 v[142:143], v232 offset:3072\n\t" \
  "s_add_i32 s79, s44, 1\n\t" \
  "s_and_b32 s79, s79, 7\n\t" \
  "s_lshl_b32 s79, s79, 15\n\t" \
  "s_waitcnt lgkmcnt(2)\n\t" \
  "v_add_f32 v136, v136, v138\n\t" \
  "v_add_f32 v137, v137, v139\n\t" \
  "s_waitcnt lgkmcnt(0)\n\t" \
  "v_add_f32 v140, v140, v142\n\t" \
  "v_add_f32 v141, v141, v143\n\t" \
  "v_add_f32 v136, v136, v140\n\t" \
  "v_add_f32 v137, v137, v141\n\t" \
  "s_nop 0\n\t" \
  "v_add_f32_dpp v136, v136, v136 quad_perm:[1,0,3,2] row_mask:0xf bank_mask:0xf\n\t" \
  "v_add_f32_dpp v137, v137, v137 quad_perm:[1,0,3,2] row_mask:0xf bank_mask:0xf\n\t" \
  "v_cvt_pk_bf16_f32 v136, v136, v137\n\t" \
  "buffer_store_dword v136, v233, %[rh], s79 offen sc1\n\t" \
  "s_memtime s[56:57]\n\t" \
  "s_cmp_eq_u32 s96, 0x7f000000\n\t" \
  "s_cbranch_scc1 40f\n\t" \
  "s_nop 8\n\t" \
  "39:\n\t" \
  "s_xor_b32 s81, s96, 0x0\n\t" \
  "buffer_load_dwordx4 a[0:3], v224, %[ru], s81 offen offset:0 sc1\n\t" \
  "ds_read_b128 v[208:211], v247\n\t" \
  "v_mov_b64 v[212:213], -1\n\t" \
  "v_mov_b64 v[214:215], -1\n\t" \
  "s_add_i32 s79, s44, -1\n\t" \
  "s_and_b32 s79, s79, 7\n\t" \
  "s_lshl_b32 s79, s79, 15\n\t" \
  "buffer_store_dwordx4 v[212:215], v243, %[rh], s79 offen sc1\n\t" \
  "s_waitcnt lgkmcnt(0)\n\t" \
  "s_sub_i32 s76, s56, s51\n\t" \
  "s_mul_hi_u32 s76, s76, s46\n\t" \
  "s_lshl1_add_u32 s76, s76, s53\n\t" \
  "v_sub_u32 v208, s76, v208\n\t" \
  "v_sub_u32 v209, s76, v209\n\t" \
  "v_sub_u32 v210, s76, v210\n\t" \
  "v_sub_u32 v211, s76, v211\n\t" \
  "v_min3_u32 v208, v208, v209, v210\n\t" \
  "buffer_load_dwordx4 a[4:7], v224, %[ru], s81 offen offset:1024 sc1\n\t" \
  "v_min_u32 v208, v208, v211\n\t" \
  "s_nop 0\n\t" \
  "v_readfirstlane_b32 s76, v208\n\t" \
  "s_bitcmp1_b32 s58, 3\n\t" \
  "s_cselect_b32 s76, s45, s76\n\t" \
  "s_min_u32 s77, s45, s47\n\t" \
  "s_min_u32 s77, s77, s76\n\t" \
  "s_mov_b32 s47, s45\n\t" \
  "s_mov_b32 s45, s76\n\t" \
  "v_mov_b32 v208, s77\n\t" \
  "s_and_b32 s58, s58, 4\n\t" \
  "buffer_store_dword v208, v244, %[rh], s55 offen sc1\n\t" \
  "v_mov_b32 v209, s44\n\t" \
  "buffer_store_dword v209, v238, %[ru], 0 offen sc1\n\t" \
  "s_and_b32 s97, s44, 3\n\t" \
  "s_lshl_b32 s97, s97, 17\n\t" \
  "s_add_i32 s97, s97, s43\n\t" \
  "s_add_i32 s82, s56, 260\n\t" \
  "60:\n\t" \
  "s_memtime vcc\n\t" \
  "s_waitcnt lgkmcnt(0)\n\t" \
  "s_sub_i32 s83, s82, vcc_lo\n\t" \
  "s_cmp_gt_i32 s83, 30\n\t" \
  "s_cbranch_scc1 60b\n\t" \
  "s_xor_b32 s81, s96, 0x20000\n\t" \
  "buffer_load_dwordx4 a[8:11], v224, %[ru], s81 offen offset:0 sc1\n\t" \
  "s_andn2_b32 s84, s48, 0xfff\n\t" \
  "s_cmp_lt_i32 s44, 60\n\t" \
  "s_cselect_b32 s84, s84, 0x7f000000\n\t" \
  "s_add_i32 s79, s44, 2\n\t" \
  "s_and_b32 s48, s79, 3\n\t" \
  "s_lshl_b32 s76, s48, 2\n\t" \
  "s_lshl_b32 s48, s48, 20\n\t" \
  "s_add_i32 s48, s48, s40\n\t" \
  "s_lshr_b32 s76, %[um], s76\n\t" \
  "s_and_b32 s76, s76, 7\n\t" \
  "s_lshl_b32 s76, s76, 12\n\t" \
  "s_xor_b32 s48, s48, s76\n\t" \
  "s_xor_b32 s76, s48, 0x200\n\t" \
  "s_xor_b32 s77, s48, 0x400\n\t" \
  "s_xor_b32 s78, s48, 0x600\n\t" \
  "s_and_b32 s79, s79, 3\n\t" \
  "s_lshl_b32 s54, s79, 16\n\t" \
  "s_add_i32 s82, s56, 390\n\t" \
  "61:\n\t" \
  "s_memtime vcc\n\t" \
  "s_waitcnt lgkmcnt(0)\n\t" \
  "s_sub_i32 s83, s82, vcc_lo\n\t" \
  "s_cmp_gt_i32 s83, 30\n\t" \
  "s_cbranch_scc1 61b\n\t" \
  "buffer_load_dwordx4 a[12:15], v224, %[ru], s81 offen offset:1024 sc1\n\t" \
  "s_add_i32 s49, s54, s41\n\t" \
  "s_xor_b32 s80, s49, 0x400\n\t" \
  "buffer_store_dword v212, v235, %[rh], s54 offen sc1\n\t" \
  "ds_read_b128 v[80:83], v240 offset:0\n\t" \
  "ds_read_b128 v[84:87], v240 offset:4096\n\t" \
  "ds_read_b128 v[88:91], v240 offset:8192\n\t" \
  "ds_read_b128 v[92:95], v240 offset:12288\n\t" \
  "s_add_i32 s85, s44, -2\n\t" \
  "s_lshl_b32 s86, s85, 17\n\t" \
  "s_cmp_lt_i32 s85, 0\n\t" \
  "s_cselect_b32 s86, 0x7f000000, s86\n\t" \
  "s_waitcnt lgkmcnt(0)\n\t" \
  "v_add_f32 v80, v80, v84\n\t" \
  "v_add_f32 v81, v81, v85\n\t" \
  "v_add_f32 v82, v82, v86\n\t" \
  "v_add_f32 v83, v83, v87\n\t" \
  "v_add_f32 v88, v88, v92\n\t" \
  "s_add_i32 s82, s56, 520\n\t" \
  "62:\n\t" \
  "s_memtime vcc\n\t" \
  "s_waitcnt lgkmcnt(0)\n\t" \
  "s_sub_i32 s83, s82, vcc_lo\n\t" \
  "s_cmp_gt_i32 s83, 30\n\t" \
  "s_cbranch_scc1 62b\n\t" \
  "s_xor_b32 s81, s96, 0x40000\n\t" \
  "buffer_load_dwordx4 a[16:19], v224, %[ru], s81 offen offset:0 sc1\n\t" \
  "v_add_f32 v89, v89, v93\n\t" \
  "v_add_f32 v90, v90, v94\n\t" \
  "v_add_f32 v91, v91, v95\n\t" \
  "v_add_f32 v80, v80, v88\n\t" \
  "v_add_f32 v81, v81, v89\n\t" \
  "v_add_f32 v82, v82, v90\n\t" \
  "v_add_f32 v83, v83, v91\n\t" \
  "v_cvt_pk_bf16_f32 v80, v80, v81\n\t" \
  "v_cvt_pk_bf16_f32 v81, v82, v83\n\t" \
  "buffer_store_dwordx2 v[80:81], v241, %[ro], s86 offen nt\n\t" \
  "s_add_i32 s82, s56, 650\n\t" \
  "63:\n\t" \
  "s_memtime vcc\n\t" \
  "s_waitcnt lgkmcnt(0)\n\t" \
  "s_sub_i32 s83, s82, vcc_lo\n\t" \
  "s_cmp_gt_i32 s83, 30\n\t" \
  "s_cbranch_scc1 63b\n\t" \
  "buffer_load_dwordx4 a[20:23], v224, %[ru], s81 offen offset:1024 sc1\n\t" \
  "s_add_i32 s82, s56, 780\n\t" \
  "64:\n\t" \
  "s_memtime vcc\n\t" \
  "s_waitcnt lgkmcnt(0)\n\t" \
  "s_sub_i32 s83, s82, vcc_lo\n\t" \
  "s_cmp_gt_i32 s83, 30\n\t" \
  "s_cbranch_scc1 64b\n\t" \
  "s_xor_b32 s81, s96, 0x60000\n\t" \
  "buffer_load_dwordx4 a[24:27], v224, %[ru], s81 offen offset:0 sc1\n\t" \
  "s_add_i32 s82, s56, 910\n\t" \
  "65:\n\t" \
  "s_memtime vcc\n\t" \
  "s_waitcnt lgkmcnt(0)\n\t" \
  "s_sub_i32 s83, s82, vcc_lo\n\t" \
  "s_cmp_gt_i32 s83, 30\n\t" \
  "s_cbranch_scc1 65b\n\t" \
  "buffer_load_dwordx4 a[28:31], v224, %[ru], s81 offen offset:1024 sc1\n\t" \
  "buffer_store_dwordx4 v[212:215], v224, %[ru], s84 offen offset:0 sc1\n\t" \
  "s_add_i32 s82, s56, 1040\n\t" \
  "66:\n\t" \
  "s_memtime vcc\n\t" \
  "s_waitcnt lgkmcnt(0)\n\t" \
  "s_sub_i32 s83, s82, vcc_lo\n\t" \
  "s_cmp_gt_i32 s83, 30\n\t" \
  "s_cbranch_scc1 66b\n\t" \
  "s_xor_b32 s81, s96, 0x80000\n\t" \
  "buffer_load_dwordx4 a[32:35], v224, %[ru], s81 offen offset:0 sc1\n\t" \
  "s_add_i32 s82, s56, 1170\n\t" \
  "67:\n\t" \
  "s_memtime vcc\n\t" \
  "s_waitcnt lgkmcnt(0)\n\t" \
  "s_sub_i32 s83, s82, vcc_lo\n\t" \
  "s_cmp_gt_i32 s83, 30\n\t" \
  "s_cbranch_scc1 67b\n\t" \
  "buffer_load_dwordx4 a[36:39], v224, %[ru], s81 offen offset:1024 sc1\n\t" \
  "buffer_store_dwordx4 v[212:215], v224, %[ru], s84 offen offset:1024 sc1\n\t" \
  "s_add_i32 s82, s56, 1300\n\t" \
  "68:\n\t" \
  "s_memtime vcc\n\t" \
  "s_waitcnt lgkmcnt(0)\n\t" \
  "s_sub_i32 s83, s82, vcc_lo\n\t" \
  "s_cmp_gt_i32 s83, 30\n\t" \
  "s_cbranch_scc1 68b\n\t" \
  "s_xor_b32 s81, s96, 0xa0000\n\t" \
  "buffer_load_dwordx4 a[40:43], v224, %[ru], s81 offen offset:0 sc1\n\t" \
  "s_add_i32 s82, s56, 1430\n\t" \
  "69:\n\t" \
  "s_memtime vcc\n\t" \
  "s_waitcnt lgkmcnt(0)\n\t" \
  "s_sub_i32 s83, s82, vcc_lo\n\t" \
  "s_cmp_gt_i32 s83, 30\n\t" \
  "s_cbranch_scc1 69b\n\t" \
  "buffer_load_dwordx4 a[44:47], v224, %[ru], s81 offen offset:1024 sc1\n\t" \
  "buffer_store_dwordx4 v[212:215], v224, %[ru], s84 offen offset:2048 sc1\n\t" \
  "s_add_i32 s82, s56, 1560\n\t" \
  "70:\n\t" \
  "s_memtime vcc\n\t" \
  "s_waitcnt lgkmcnt(0)\n\t" \
  "s_sub_i32 s83, s82, vcc_lo\n\t" \
  "s_cmp_gt_i32 s83, 30\n\t" \
  "s_cbranch_scc1 70b\n\t" \
  "s_xor_b32 s81, s96, 0xc0000\n\t" \
  "buffer_load_dwordx4 a[48:51], v224, %[ru], s81 offen offset:0 sc1\n\t" \
  "s_and_b32 s95, s44, 3\n\t" \
  "s_lshl_b32 s95, s95, 16\n\t" \
  "s_mov_b64 exec, 0xff\n\t" \
  "buffer_load_dword v211, v248, %[rh], s95 offen offset:0 sc1\n\t" \
  "s_mov_b64 exec, -1\n\t" \
  "s_add_i32 s82, s56, 1690\n\t" \
  "71:\n\t" \
  "s_memtime vcc\n\t" \
  "s_waitcnt lgkmcnt(0)\n\t" \
  "s_sub_i32 s83, s82, vcc_lo\n\t" \
  "s_cmp_gt_i32 s83, 30\n\t" \
  "s_cbranch_scc1 71b\n\t" \
  "buffer_load_dwordx4 a[52:55], v224, %[ru], s81 offen offset:1024 sc1\n\t" \
  "buffer_store_dwordx4 v[212:215], v224, %[ru], s84 offen offset:3072 sc1\n\t" \
  "s_add_i32 s82, s56, 1820\n\t" \
  "72:\n\t" \
  "s_memtime vcc\n\t" \
  "s_waitcnt lgkmcnt(0)\n\t" \
  "s_sub_i32 s83, s82, vcc_lo\n\t" \
  "s_cmp_gt_i32 s83, 30\n\t" \
  "s_cbranch_scc1 72b\n\t" \
  "s_xor_b32 s81, s96, 0xe0000\n\t" \
  "buffer_load_dwordx4 a[56:59], v224, %[ru], s81 offen offset:0 sc1\n\t" \
  "s_add_i32 s82, s56, 1950\n\t" \
  "73:\n\t" \
  "s_memtime vcc\n\t" \
  "s_waitcnt lgkmcnt(0)\n\t" \
  "s_sub_i32 s83, s82, vcc_lo\n\t" \
  "s_cmp_gt_i32 s83, 30\n\t" \
  "s_cbranch_scc1 73b\n\t" \
  "buffer_load_dwordx4 a[60:63], v224, %[ru], s81 offen offset:1024 sc1\n\t" \
  "s_cmp_lt_i32 s44, 0\n\t" \
  "s_cbranch_scc1 51f\n\t" \
  "s_mov_b32 s79, s56\n\t" \
  "s_memtime vcc\n\t" \
  "s_memrealtime s[82:83]\n\t" \
  "s_nop 4\n\t" \
  "s_memrealtime s[84:85]\n\t" \
  "s_nop 4\n\t" \
  "s_memrealtime s[56:57]\n\t" \
  "s_nop 4\n\t" \
  "s_memrealtime s[86:87]\n\t" \
  "s_nop 4\n\t" \
  "s_memrealtime s[88:89]\n\t" \
  "s_nop 4\n\t" \
  "s_memrealtime s[90:91]\n\t" \
  "s_mov_b32 s92, 0\n\t" \
  "s_waitcnt lgkmcnt(0)\n\t" \
  "s_cmp_eq_u32 s82, s84\n\t" \
  "s_addc_u32 s92, s92, 0\n\t" \
  "s_cmp_eq_u32 s82, s56\n\t" \
  "s_addc_u32 s92, s92, 0\n\t" \
  "s_cmp_eq_u32 s82, s86\n\t" \
  "s_addc_u32 s92, s92, 0\n\t" \
  "s_cmp_eq_u32 s82, s88\n\t" \
  "s_addc_u32 s92, s92, 0\n\t" \
  "s_cmp_eq_u32 s82, s90\n\t" \
  "s_addc_u32 s92, s92, 0\n\t" \
  "s_mul_i32 s92, s92, 24\n\t" \
  "s_add_i32 s92, s92, vcc_lo\n\t" \
  "s_lshl4_add_u32 s93, s82, 64\n\t" \
  "s_mov_b32 s56, s79\n\t" \
  "s_sub_i32 s82, s92, s51\n\t" \
  "s_sub_i32 s83, s93, s53\n\t" \
  "s_cmp_eq_u32 s82, 0\n\t" \
  "s_cbranch_scc1 41f\n\t" \
  "v_cvt_f32_u32 v208, s82\n\t" \
  "v_cvt_f32_u32 v209, s83\n\t" \
  "v_rcp_f32 v208, v208\n\t" \
  "s_mov_b32 s51, s92\n\t" \
  "v_mul_f32 v208, v208, v209\n\t" \
  "v_max_f32 v208, 0x3f028f5c, v208\n\t" \
  "v_min_f32 v208, 0x3ffeb852, v208\n\t" \
  "v_rcp_f32 v209, v208\n\t" \
  "v_mul_f32 v208, 0x4f000000, v208\n\t" \
  "v_mul_f32 v209, 0x47800000, v209\n\t" \
  "v_cvt_u32_f32 v208, v208\n\t" \
  "v_cvt_u32_f32 v209, v209\n\t" \
  "s_mov_b32 s53, s93\n\t" \
  "v_readfirstlane_b32 s46, v208\n\t" \
  "v_readfirstlane_b32 s52, v209\n\t" \
  "41:\n\t" \
  "s_lshl_b32 s50, s53, 8\n\t" \
  "s_add_i32 s85, s44, 1\n\t" \
  "s_and_b32 s94, s85, 3\n\t" \
  "s_lshl_b32 s94, s94, 16\n\t" \
  "s_add_i32 s82, s56, 3100\n\t" \
  "74:\n\t" \
  "s_memtime vcc\n\t" \
  "s_waitcnt lgkmcnt(0)\n\t" \
  "s_sub_i32 s83, s82, vcc_lo\n\t" \
  "s_cmp_gt_i32 s83, 30\n\t" \
  "s_cbranch_scc1 74b\n\t" \
  "s_waitcnt vmcnt(4)\n\t" \
  "s_mov_b64 exec, 0xff\n\t" \
  "47:\n\t" \
  "v_cmp_ne_u32 vcc, s44, v211\n\t" \
  "s_mov_b64 exec, -1\n\t" \
  "s_cbranch_vccnz 45f\n\t" \
  "46:\n\t" \
  "s_barrier\n\t" \
  "s_xor_b32 s81, s97, 0x0\n\t" \
  "buffer_load_dwordx4 v[80:83], v224, %[rh], s81 offen offset:0 sc1\n\t" \
  "s_xor_b32 s81, s97, 0x800\n\t" \
  "buffer_load_dwordx4 v[84:87], v224, %[rh], s81 offen offset:0 sc1\n\t" \
  "s_xor_b32 s81, s97, 0x0\n\t" \
  "buffer_load_dwordx4 v[88:91], v224, %[rh], s81 offen offset:1024 sc1\n\t" \
  "s_xor_b32 s81, s97, 0x800\n\t" \
  "buffer_load_dwordx4 v[92:95], v224, %[rh], s81 offen offset:1024 sc1\n\t" \
  "s_xor_b32 s81, s97, 0x4000\n\t" \
  "buffer_load_dwordx4 v[96:99], v224, %[rh], s81 offen offset:0 sc1\n\t" \
  "s_xor_b32 s81, s97, 0x4800\n\t" \
  "buffer_load_dwordx4 v[100:103], v224, %[rh], s81 offen offset:0 sc1\n\t" \
  "s_xor_b32 s81, s97, 0x4000\n\t" \
  "buffer_load_dwordx4 v[104:107], v224, %[rh], s81 offen offset:1024 sc1\n\t" \
  "s_xor_b32 s81, s97, 0x4800\n\t" \
  "buffer_load_dwordx4 v[108:111], v224, %[rh], s81 offen offset:1024 sc1\n\t" \
  "s_waitcnt vmcnt(6)\n\t" \
  "v_swap_b32 v81, v84\n\t" \
  "v_swap_b32 v83, v86\n\t" \
  "s_nop 1\n\t" \
  "v_mfma_f32_16x16x32_bf16 v[192:195], v[0:3], v[80:83], 0\n\t" \
  "v_mfma_f32_16x16x32_bf16 v[200:203], v[8:11], v[80:83], 0\n\t" \
  "s_xor_b32 s81, s97, 0x1000\n\t" \
  "buffer_load_dwordx4 v[112:115], v224, %[rh], s81 offen offset:0 sc1\n\t" \
  "v_mfma_f32_16x16x32_bf16 v[196:199], v[0:3], v[84:87], 0\n\t" \
  "v_mfma_f32_16x16x32_bf16 v[204:207], v[8:11], v[84:87], 0\n\t" \
  "s_xor_b32 s81, s97, 0x1800\n\t" \
  "buffer_load_dwordx4 v[116:119], v224, %[rh], s81 offen offset:0 sc1\n\t" \
  "s_waitcnt vmcnt(6)\n\t" \
  "v_swap_b32 v89, v92\n\t" \
  "v_swap_b32 v91, v94\n\t" \
  "s_nop 1\n\t" \
  "v_mfma_f32_16x16x32_bf16 v[196:199], v[4:7], v[88:91], v[196:199]\n\t" \
  "v_mfma_f32_16x16x32_bf16 v[204:207], v[12:15], v[88:91], v[204:207]\n\t" \
  "s_xor_b32 s81, s97, 0x1000\n\t" \
  "buffer_load_dwordx4 v[120:123], v224, %[rh], s81 offen offset:1024 sc1\n\t" \
  "s_xor_b32 s81, s97, 0x1800\n\t" \
  "buffer_load_dwordx4 v[124:127], v224, %[rh], s81 offen offset:1024 sc1\n\t" \
  "s_waitcnt vmcnt(6)\n\t" \
  "v_swap_b32 v97, v100\n\t" \
  "v_swap_b32 v99, v102\n\t" \
  "s_nop 1\n\t" \
  "v_mfma_f32_16x16x32_bf16 v[192:195], v[16:19], v[96:99], v[192:195]\n\t" \
  "v_mfma_f32_16x16x32_bf16 v[200:203], v[24:27], v[96:99], v[200:203]\n\t" \
  "s_xor_b32 s81, s97, 0x5000\n\t" \
  "buffer_load_dwordx4 v[128:131], v224, %[rh], s81 offen offset:0 sc1\n\t" \
  "v_mfma_f32_16x16x32_bf16 v[196:199], v[16:19], v[100:103], v[196:199]\n\t" \
  "v_mfma_f32_16x16x32_bf16 v[204:207], v[24:27], v[100:103], v[204:207]\n\t" \
  "s_xor_b32 s81, s97, 0x5800\n\t" \
  "buffer_load_dwordx4 v[132:135], v224, %[rh], s81 offen offset:0 sc1\n\t" \
  "s_waitcnt vmcnt(6)\n\t" \
  "v_swap_b32 v105, v108\n\t" \
  "v_swap_b32 v107, v110\n\t" \
  "s_nop 1\n\t" \
  "v_mfma_f32_16x16x32_bf16 v[196:199], v[20:23], v[104:107], v[196:199]\n\t" \
  "v_mfma_f32_16x16x32_bf16 v[204:207], v[28:31], v[104:107], v[204:207]\n\t" \
  "s_xor_b32 s81, s97, 0x5000\n\t" \
  "buffer_load_dwordx4 v[136:139], v224, %[rh], s81 offen offset:1024 sc1\n\t" \
  "s_xor_b32 s81, s97, 0x5800\n\t" \
  "buffer_load_dwordx4 v[140:143], v224, %[rh], s81 offen offset:1024 sc1\n\t" \
  "s_mov_b32 exec_hi, 0\n\t" \
  "buffer_load_dword v209, v237, %[rh], s55 offen offset:0 sc1\n\t" \
  "s_mov_b64 exec, 0xff\n\t" \
  "buffer_load_dword v210, v242, %[ru], 0 offen offset:0 sc1\n\t" \
  "buffer_load_dword v208, v236, %[rh], s94 offen offset:0 sc1\n\t" \
  "s_mov_b64 exec, 1\n\t" \
  "buffer_load_dword v211, v246, %[rh], %[mg] offen offset:0 sc1\n\t" \
  "s_mov_b64 exec, -1\n\t" \
  "v_permlane32_swap_b32 v92, v108\n\t" \
  "v_permlane32_swap_b32 v93, v109\n\t" \
  "v_permlane32_swap_b32 v94, v110\n\t" \
  "v_permlane32_swap_b32 v95, v111\n\t" \
  "s_nop 1\n\t" \
  "v_mfma_f32_16x16x32_bf16 v[192:195], v[64:67], v[92:95], v[192:195]\n\t" \
  "v_mfma_f32_16x16x32_bf16 v[200:203], v[72:75], v[92:95], v[200:203]\n\t" \
  "v_mfma_f32_16x16x32_bf16 v[196:199], v[64:67], v[108:111], v[196:199]\n\t" \
  "v_mfma_f32_16x16x32_bf16 v[204:207], v[72:75], v[108:111], v[204:207]\n\t" \
  "s_waitcnt vmcnt(10)\n\t" \
  "v_swap_b32 v113, v116\n\t" \
  "v_swap_b32 v115, v118\n\t" \
  "s_nop 1\n\t" \
  "v_mfma_f32_16x16x32_bf16 v[192:195], v[32:35], v[112:115], v[192:195]\n\t" \
  "v_mfma_f32_16x16x32_bf16 v[200:203], v[40:43], v[112:115], v[200:203]\n\t" \
  "v_mfma_f32_16x16x32_bf16 v[196:199], v[32:35], v[116:119], v[196:199]\n\t" \
  "v_mfma_f32_16x16x32_bf16 v[204:207], v[40:43], v[116:119], v[204:207]\n\t" \
  "s_waitcnt vmcnt(8)\n\t" \
  "v_swap_b32 v121, v124\n\t" \
  "v_swap_b32 v123, v126\n\t" \
  "s_nop 1\n\t" \
  "v_mfma_f32_16x16x32_bf16 v[196:199], v[36:39], v[120:123], v[196:199]\n\t" \
  "v_mfma_f32_16x16x32_bf16 v[204:207], v[44:47], v[120:123], v[204:207]\n\t" \
  "s_waitcnt vmcnt(6)\n\t" \
  "v_swap_b32 v129, v132\n\t" \
  "v_swap_b32 v131, v134\n\t" \
  "s_nop 1\n\t" \
  "v_mfma_f32_16x16x32_bf16 v[192:195], v[48:51], v[128:131], v[192:195]\n\t" \
  "v_mfma_f32_16x16x32_bf16 v[200:203], v[56:59], v[128:131], v[200:203]\n\t" \
  "v_mfma_f32_16x16x32_bf16 v[196:199], v[48:51], v[132:135], v[196:199]\n\t" \
  "v_mfma_f32_16x16x32_bf16 v[204:207], v[56:59], v[132:135], v[204:207]\n\t" \
  "s_waitcnt vmcnt(4)\n\t" \
  "v_swap_b32 v137, v140\n\t" \
  "v_swap_b32 v139, v142\n\t" \
  "s_nop 1\n\t" \
  "v_mfma_f32_16x16x32_bf16 v[196:199], v[52:55], v[136:139], v[196:199]\n\t" \
  "v_mfma_f32_16x16x32_bf16 v[204:207], v[60:63], v[136:139], v[204:207]\n\t" \
  "v_permlane32_swap_b32 v124, v140\n\t" \
  "v_permlane32_swap_b32 v125, v141\n\t" \
  "v_permlane32_swap_b32 v126, v142\n\t" \
  "v_permlane32_swap_b32 v127, v143\n\t" \
  "s_nop 1\n\t" \
  "v_mfma_f32_16x16x32_bf16 v[192:195], v[68:71], v[124:127], v[192:195]\n\t" \
  "v_mfma_f32_16x16x32_bf16 v[200:203], v[76:79], v[124:127], v[200:203]\n\t" \
  "v_mfma_f32_16x16x32_bf16 v[196:199], v[68:71], v[140:143], v[196:199]\n\t" \
  "v_mfma_f32_16x16x32_bf16 v[204:207], v[76:79], v[140:143], v[204:207]\n\t" \
  "v_mov_b64 v[144:145], 0\n\t" \
  "v_mov_b64 v[146:147], 0\n\t" \
  "v_mov_b64 v[148:149], 0\n\t" \
  "v_mov_b64 v[150:151], 0\n\t" \
  "v_mov_b64 v[152:153], 0\n\t" \
  "v_mov_b64 v[154:155], 0\n\t" \
  "v_mov_b64 v[156:157], 0\n\t" \
  "v_mov_b64 v[158:159], 0\n\t" \
  "ds_write_b128 v239, v[192:195] offset:0\n\t" \
  "ds_write_b128 v239, v[200:203] offset:2048\n\t" \
  "ds_write_b128 v239, v[196:199] offset:1024\n\t" \
  "ds_write_b128 v239, v[204:207] offset:3072\n\t" \
  "s_cmp_gt_i32 s44, 61\n\t" \
  "s_cbranch_scc1 97f\n\t" \
  "v_mov_b64 v[160:161], 0\n\t" \
  "v_mov_b64 v[162:163], 0\n\t" \
  "v_mov_b64 v[164:165], 0\n\t" \
  "v_mov_b64 v[166:167], 0\n\t" \
  "v_mov_b64 v[168:169], 0\n\t" \
  "v_mov_b64 v[170:171], 0\n\t" \
  "v_mov_b64 v[172:173], 0\n\t" \
  "v_mov_b64 v[174:175], 0\n\t" \
  "v_mov_b64 v[216:217], 0\n\t" \
  "v_mov_b64 v[218:219], 0\n\t" \
  "v_mov_b64 v[220:221], 0\n\t" \
  "v_mov_b64 v[222:223], 0\n\t" \
  "s_waitcnt vmcnt(0)\n\t" \
  "52:\n\t" \
  "s_add_i32 s44, s44, 2\n\t" \
  "s_branch 20b\n\t" \
  "22:\n\t" \
  "s_bitcmp1_b32 s58, 2\n\t" \
  "s_cbranch_scc1 24b\n\t" \
  "s_sleep 2\n\t" \
  "buffer_load_dword v208, v236, %[rh], s94 offen sc1\n\t" \
  "s_waitcnt vmcnt(0)\n\t" \
  "s_add_u32 s59, s59, 1\n\t" \
  "s_bitcmp0_b32 s59, 20\n\t" \
  "s_cbranch_scc1 21b\n\t" \
  "s_or_b32 s58, s58, 4\n\t" \
  "s_branch 21b\n\t" \
  "14:\n\t" \
  "s_bitcmp1_b32 s58, 2\n\t" \
  "s_cbranch_scc1 15b\n\t" \
  "s_mov_b32 s84, 600\n\t" \
  "s_mov_b32 s90, 250\n\t" \
  "s_sleep 1\n\t" \
  "buffer_load_dword v208, v234, %[rh], s81 offen sc1\n\t" \
  "s_add_u32 s59, s59, 1\n\t" \
  "s_bitcmp0_b32 s59, 20\n\t" \
  "s_cbranch_scc1 13b\n\t" \
  "s_or_b32 s58, s58, 4\n\t" \
  "s_branch 13b\n\t" \
  "8:\n\t" \
  "s_mov_b32 s79, 0\n\t" \
  "17:\n\t" \
  "s_bitcmp1_b32 s58, 2\n\t" \
  "s_cbranch_scc1 18f\n\t" \
  "v_max_u32 v136, v64, v65\n\t" \
  "v_max3_u32 v136, v136, v66, v67\n\t" \
  "v_max3_u32 v136, v136, v68, v69\n\t" \
  "v_max3_u32 v136, v136, v70, v71\n\t" \
  "v_max3_u32 v136, v136, v72, v73\n\t" \
  "v_max3_u32 v136, v136, v74, v75\n\t" \
  "v_max3_u32 v136, v136, v76, v77\n\t" \
  "v_max3_u32 v136, v136, v78, v79\n\t" \
  "v_max3_u32 v136, v136, v192, v193\n\t" \
  "v_max3_u32 v136, v136, v194, v195\n\t" \
  "v_max3_u32 v136, v136, v196, v197\n\t" \
  "v_max3_u32 v136, v136, v198, v199\n\t" \
  "v_max3_u32 v136, v136, v200, v201\n\t" \
  "v_max3_u32 v136, v136, v202, v203\n\t" \
  "v_max3_u32 v136, v136, v204, v205\n\t" \
  "v_max3_u32 v136, v136, v206, v207\n\t" \
  "v_max3_u32 v136, v136, v208, v209\n\t" \
  "v_max3_u32 v136, v136, v210, v211\n\t" \
  "v_max3_u32 v136, v136, v212, v213\n\t" \
  "v_max3_u32 v136, v136, v214, v215\n\t" \
  "v_cmp_eq_u32 vcc, -1, v136\n\t" \
  "s_cbranch_vccz 18f\n\t" \
  "s_or_b32 s79, s79, s90\n\t" \
  "s_cbranch_scc1 19f\n\t" \
  "s_cmp_lt_u32 s44, 4\n\t" \
  "s_cbranch_scc1 19f\n\t" \
  "s_add_i32 s79, s101, 40\n\t" \
  "v_mov_b32 v136, s79\n\t" \
  "s_mov_b64 exec, 1\n\t" \
  "buffer_store_dword v136, v246, %[rh], %[mg] offen sc1\n\t" \
  "s_mov_b64 exec, -1\n\t" \
  "19:\n\t" \
  "s_mov_b32 s79, 1\n\t" \
  "buffer_load_dwordx4 v[192:195], v226, %[rh], s49 offen offset:0 sc1\n\t" \
  "buffer_load_dwordx4 v[64:67], v225, %[ru], s48 offen offset:0 sc1\n\t" \
  "buffer_load_dwordx4 v[196:199], v226, %[rh], s49 offen offset:2048 sc1\n\t" \
  "buffer_load_dwordx4 v[72:75], v225, %[ru], s76 offen offset:0 sc1\n\t" \
  "buffer_load_dwordx4 v[200:203], v226, %[rh], s80 offen offset:0 sc1\n\t" \
  "buffer_load_dwordx4 v[68:71], v225, %[ru], s77 offen offset:0 sc1\n\t" \
  "buffer_load_dwordx4 v[204:207], v226, %[rh], s80 offen offset:2048 sc1\n\t" \
  "buffer_load_dwordx4 v[76:79], v225, %[ru], s78 offen offset:0 sc1\n\t" \
  "buffer_load_dwordx4 v[208:211], v227, %[rh], s54 offen offset:0 sc1\n\t" \
  "buffer_load_dwordx4 v[212:215], v228, %[rh], s54 offen offset:0 sc1\n\t" \
  "s_waitcnt vmcnt(0)\n\t" \
  "s_add_u32 s59, s59, 1\n\t" \
  "s_bitcmp0_b32 s59, 20\n\t" \
  "s_cbranch_scc1 17b\n\t" \
  "s_or_b32 s58, s58, 4\n\t" \
  "s_branch 17b\n\t" \
  "18:\n\t" \
  "s_cmp_eq_u32 s79, 0\n\t" \
  "s_cbranch_scc1 6b\n\t" \
  "v_mov_b64 v[144:145], 0\n\t" \
  "v_mov_b64 v[146:147], 0\n\t" \
  "v_mov_b64 v[148:149], 0\n\t" \
  "v_mov_b64 v[150:151], 0\n\t" \
  "v_mov_b64 v[152:153], 0\n\t" \
  "v_mov_b64 v[154:155], 0\n\t" \
  "v_mov_b64 v[156:157], 0\n\t" \
  "v_mov_b64 v[158:159], 0\n\t" \
  "v_mov_b64 v[160:161], 0\n\t" \
  "v_mov_b64 v[162:163], 0\n\t" \
  "v_mov_b64 v[164:165], 0\n\t" \
  "v_mov_b64 v[166:167], 0\n\t" \
  "v_mov_b64 v[168:169], 0\n\t" \
  "v_mov_b64 v[170:171], 0\n\t" \
  "v_mov_b64 v[172:173], 0\n\t" \
  "v_mov_b64 v[174:175], 0\n\t" \
  "v_mov_b64 v[216:217], 0\n\t" \
  "v_mov_b64 v[218:219], 0\n\t" \
  "v_mov_b64 v[220:221], 0\n\t" \
  "v_mov_b64 v[222:223], 0\n\t" \
  "s_branch 10b\n\t" \
  "40:\n\t" \
  "s_add_i32 s79, s44, 2\n\t" \
  "s_cmp_lt_i32 s79, 64\n\t" \
  "s_cbranch_scc0 44f\n\t" \
  "42:\n\t" \
  "s_mov_b64 exec, 0xff\n\t" \
  "buffer_load_dword v210, v242, %[ru], 0 offen sc1\n\t" \
  "s_waitcnt vmcnt(0)\n\t" \
  "v_cmp_gt_i32 vcc, s79, v210\n\t" \
  "s_mov_b64 exec, -1\n\t" \
  "s_cbranch_vccz 43f\n\t" \
  "s_sleep 3\n\t" \
  "s_bitcmp1_b32 s58, 2\n\t" \
  "s_cbranch_scc1 43f\n\t" \
  "s_add_u32 s59, s59, 1\n\t" \
  "s_bitcmp0_b32 s59, 20\n\t" \
  "s_cbranch_scc1 42b\n\t" \
  "s_or_b32 s58, s58, 4\n\t" \
  "s_branch 43f\n\t" \
  "43:\n\t" \
  "s_and_b32 s96, s79, 15\n\t" \
  "s_lshl_b32 s96, s96, 22\n\t" \
  "s_add_i32 s96, s96, s42\n\t" \
  "s_lshr_b32 s82, s96, 21\n\t" \
  "s_bitcmp0_b32 %[sel], s82\n\t" \
  "s_cselect_b32 s82, 0x2000, 0\n\t" \
  "s_add_i32 s96, s96, s82\n\t" \
  "s_branch 39b\n\t" \
  "51:\n\t" \
  "s_memtime vcc\n\t" \
  "s_memrealtime s[82:83]\n\t" \
  "s_nop 4\n\t" \
  "s_memrealtime s[84:85]\n\t" \
  "s_nop 4\n\t" \
  "s_memrealtime s[94:95]\n\t" \
  "s_nop 4\n\t" \
  "s_memrealtime s[86:87]\n\t" \
  "s_nop 4\n\t" \
  "s_memrealtime s[88:89]\n\t" \
  "s_nop 4\n\t" \
  "s_memrealtime s[90:91]\n\t" \
  "s_mov_b32 s92, 0\n\t" \
  "s_waitcnt lgkmcnt(0)\n\t" \
  "s_cmp_eq_u32 s82, s84\n\t" \
  "s_addc_u32 s92, s92, 0\n\t" \
  "s_cmp_eq_u32 s82, s94\n\t" \
  "s_addc_u32 s92, s92, 0\n\t" \
  "s_cmp_eq_u32 s82, s86\n\t" \
  "s_addc_u32 s92, s92, 0\n\t" \
  "s_cmp_eq_u32 s82, s88\n\t" \
  "s_addc_u32 s92, s92, 0\n\t" \
  "s_cmp_eq_u32 s82, s90\n\t" \
  "s_addc_u32 s92, s92, 0\n\t" \
  "s_mul_i32 s92, s92, 24\n\t" \
  "s_add_i32 s92, s92, vcc_lo\n\t" \
  "s_lshl4_add_u32 s93, s82, 64\n\t" \
  "s_sub_i32 s82, s92, s51\n\t" \
  "s_sub_i32 s83, s93, s53\n\t" \
  "s_cmp_eq_u32 s82, 0\n\t" \
  "s_cbranch_scc1 53f\n\t" \
  "v_cvt_f32_u32 v208, s82\n\t" \
  "v_cvt_f32_u32 v209, s83\n\t" \
  "v_rcp_f32 v208, v208\n\t" \
  "s_mov_b32 s51, s92\n\t" \
  "v_mul_f32 v208, v208, v209\n\t" \
  "v_max_f32 v208, 0x3f028f5c, v208\n\t" \
  "v_min_f32 v208, 0x3ffeb852, v208\n\t" \
  "v_rcp_f32 v209, v208\n\t" \
  "v_mul_f32 v208, 0x4f000000, v208\n\t" \
  "v_mul_f32 v209, 0x47800000, v209\n\t" \
  "v_cvt_u32_f32 v208, v208\n\t" \
  "v_cvt_u32_f32 v209, v209\n\t" \
  "s_mov_b32 s53, s93\n\t" \
  "v_readfirstlane_b32 s46, v208\n\t" \
  "v_readfirstlane_b32 s52, v209\n\t" \
  "53:\n\t" \
  "s_lshl_b32 s50, s53, 8\n\t" \
  "s_add_i32 s85, s44, 1\n\t" \
  "s_and_b32 s94, s85, 3\n\t" \
  "s_lshl_b32 s94, s94, 16\n\t" \
  "s_mov_b32 exec_hi, 0\n\t" \
  "buffer_load_dword v209, v237, %[rh], s55 offen offset:0 sc1\n\t" \
  "s_mov_b64 exec, 0xff\n\t" \
  "buffer_load_dword v210, v242, %[ru], 0 offen offset:0 sc1\n\t" \
  "buffer_load_dword v208, v236, %[rh], s94 offen offset:0 sc1\n\t" \
  "s_mov_b64 exec, 1\n\t" \
  "buffer_load_dword v211, v246, %[rh], %[mg] offen offset:0 sc1\n\t" \
  "s_mov_b64 exec, -1\n\t" \
  "v_mov_b64 v[144:145], 0\n\t" \
  "v_mov_b64 v[146:147], 0\n\t" \
  "v_mov_b64 v[148:149], 0\n\t" \
  "v_mov_b64 v[150:151], 0\n\t" \
  "v_mov_b64 v[152:153], 0\n\t" \
  "v_mov_b64 v[154:155], 0\n\t" \
  "v_mov_b64 v[156:157], 0\n\t" \
  "v_mov_b64 v[158:159], 0\n\t" \
  "v_mov_b64 v[160:161], 0\n\t" \
  "v_mov_b64 v[162:163], 0\n\t" \
  "v_mov_b64 v[164:165], 0\n\t" \
  "v_mov_b64 v[166:167], 0\n\t" \
  "v_mov_b64 v[168:169], 0\n\t" \
  "v_mov_b64 v[170:171], 0\n\t" \
  "v_mov_b64 v[172:173], 0\n\t" \
  "v_mov_b64 v[174:175], 0\n\t" \
  "v_mov_b64 v[216:217], 0\n\t" \
  "v_mov_b64 v[218:219], 0\n\t" \
  "v_mov_b64 v[220:221], 0\n\t" \
  "v_mov_b64 v[222:223], 0\n\t" \
  "s_waitcnt vmcnt(0)\n\t" \
  "s_branch 52b\n\t" \
  "97:\n\t" \
  "s_waitcnt lgkmcnt(0)\n\t" \
  "s_barrier\n\t" \
  "ds_read_b128 v[80:83], v240 offset:0\n\t" \
  "ds_read_b128 v[84:87], v240 offset:4096\n\t" \
  "ds_read_b128 v[88:91], v240 offset:8192\n\t" \
  "ds_read_b128 v[92:95], v240 offset:12288\n\t" \
  "s_add_i32 s85, s44, 0\n\t" \
  "s_lshl_b32 s86, s85, 17\n\t" \
  "s_cmp_lt_i32 s85, 0\n\t" \
  "s_cselect_b32 s86, 0x7f000000, s86\n\t" \
  "s_waitcnt lgkmcnt(0)\n\t" \
  "v_add_f32 v80, v80, v84\n\t" \
  "v_add_f32 v81, v81, v85\n\t" \
  "v_add_f32 v82, v82, v86\n\t" \
  "v_add_f32 v83, v83, v87\n\t" \
  "v_add_f32 v88, v88, v92\n\t" \
  "v_add_f32 v89, v89, v93\n\t" \
  "v_add_f32 v90, v90, v94\n\t" \
  "v_add_f32 v91, v91, v95\n\t" \
  "v_add_f32 v80, v80, v88\n\t" \
  "v_add_f32 v81, v81, v89\n\t" \
  "v_add_f32 v82, v82, v90\n\t" \
  "v_add_f32 v83, v83, v91\n\t" \
  "v_cvt_pk_bf16_f32 v80, v80, v81\n\t" \
  "v_cvt_pk_bf16_f32 v81, v82, v83\n\t" \
  "buffer_store_dwordx2 v[80:81], v241, %[ro], s86 offen nt\n\t" \
  "s_branch 99f\n\t" \
  "44:\n\t" \
  "s_waitcnt lgkmcnt(0)\n\t" \
  "ds_read_b128 v[80:83], v240 offset:0\n\t" \
  "ds_read_b128 v[84:87], v240 offset:4096\n\t" \
  "ds_read_b128 v[88:91], v240 offset:8192\n\t" \
  "ds_read_b128 v[92:95], v240 offset:12288\n\t" \
  "s_add_i32 s85, s44, -2\n\t" \
  "s_lshl_b32 s86, s85, 17\n\t" \
  "s_cmp_lt_i32 s85, 0\n\t" \
  "s_cselect_b32 s86, 0x7f000000, s86\n\t" \
  "s_waitcnt lgkmcnt(0)\n\t" \
  "v_add_f32 v80, v80, v84\n\t" \
  "v_add_f32 v81, v81, v85\n\t" \
  "v_add_f32 v82, v82, v86\n\t" \
  "v_add_f32 v83, v83, v87\n\t" \
  "v_add_f32 v88, v88, v92\n\t" \
  "v_add_f32 v89, v89, v93\n\t" \
  "v_add_f32 v90, v90, v94\n\t" \
  "v_add_f32 v91, v91, v95\n\t" \
  "v_add_f32 v80, v80, v88\n\t" \
  "v_add_f32 v81, v81, v89\n\t" \
  "v_add_f32 v82, v82, v90\n\t" \
  "v_add_f32 v83, v83, v91\n\t" \
  "v_cvt_pk_bf16_f32 v80, v80, v81\n\t" \
  "v_cvt_pk_bf16_f32 v81, v82, v83\n\t" \
  "buffer_store_dwordx2 v[80:81], v241, %[ro], s86 offen nt\n\t" \
  "s_and_b32 s97, s44, 3\n\t" \
  "s_lshl_b32 s97, s97, 17\n\t" \
  "s_add_i32 s97, s97, s43\n\t" \
  "s_add_i32 s85, s44, 1\n\t" \
  "s_and_b32 s94, s85, 3\n\t" \
  "s_lshl_b32 s94, s94, 16\n\t" \
  "s_and_b32 s95, s44, 3\n\t" \
  "s_lshl_b32 s95, s95, 16\n\t" \
  "s_add_i32 s82, s56, 500\n\t" \
  "48:\n\t" \
  "s_memtime vcc\n\t" \
  "s_waitcnt lgkmcnt(0)\n\t" \
  "s_sub_i32 s83, s82, vcc_lo\n\t" \
  "s_cmp_gt_i32 s83, 30\n\t" \
  "s_cbranch_scc1 48b\n\t" \
  "45:\n\t" \
  "s_bitcmp1_b32 s58, 2\n\t" \
  "s_cbranch_scc1 46b\n\t" \
  "s_sleep 1\n\t" \
  "s_mov_b64 exec, 0xff\n\t" \
  "buffer_load_dword v211, v248, %[rh], s95 offen sc1\n\t" \
  "s_waitcnt vmcnt(0)\n\t" \
  "s_add_u32 s59, s59, 1\n\t" \
  "s_bitcmp0_b32 s59, 20\n\t" \
  "s_cbranch_scc1 47b\n\t" \
  "s_or_b32 s58, s58, 4\n\t" \
  "s_branch 47b\n\t" \
  "99:\n\t" \
  "s_waitcnt vmcnt(0) lgkmcnt(0)\n\t" \
  ""
#define P_BODY \
  "s_cmp_eq_u32 %[wv], 1\n\t" \
  "s_cbranch_scc1 91f\n\t" \
  "s_cmp_eq_u32 %[wv], 2\n\t" \
  "s_cbranch_scc1 92f\n\t" \
  "s_cmp_eq_u32 %[wv], 3\n\t" \
  "s_cbranch_scc1 93f\n\t" \
  "90:\n\t" \
  "ds_read_b128 v[0:3], v164 offset:0\n\t" \
  "ds_read_b128 v[4:7], v164 offset:8192\n\t" \
  "ds_read_b128 v[8:11], v165 offset:0\n\t" \
  "ds_read_b128 v[12:15], v165 offset:8192\n\t" \
  "ds_read_b128 v[16:19], v166 offset:0\n\t" \
  "ds_read_b128 v[20:23], v166 offset:8192\n\t" \
  "ds_read_b128 v[24:27], v167 offset:0\n\t" \
  "ds_read_b128 v[28:31], v167 offset:8192\n\t" \
  "ds_read_b128 v[32:35], v164 offset:256\n\t" \
  "ds_read_b128 v[36:39], v164 offset:8448\n\t" \
  "ds_read_b128 v[40:43], v165 offset:256\n\t" \
  "ds_read_b128 v[44:47], v165 offset:8448\n\t" \
  "ds_read_b128 v[48:51], v166 offset:256\n\t" \
  "ds_read_b128 v[52:55], v166 offset:8448\n\t" \
  "ds_read_b128 v[56:59], v167 offset:256\n\t" \
  "ds_read_b128 v[60:63], v167 offset:8448\n\t" \
  "v_mov_b64 v[152:153], -1\n\t" \
  "v_mov_b64 v[154:155], -1\n\t" \
  "s_cmp_le_u32 s44, 12\n\t" \
  ".p2align 6\n\t" \
  "20:\n\t" \
  "s_waitcnt vmcnt(28) lgkmcnt(0)\n\t" \
  "v_mfma_f32_16x16x32_bf16 v[128:131], a[0:3], v[0:3], 0\n\t" \
  "s_cbranch_scc1 30f\n\t" \
  "40:\n\t" \
  "s_barrier\n\t" \
  "v_cvt_pk_bf16_f32 v148, v136, v137\n\t" \
  "v_cvt_pk_bf16_f32 v149, v138, v139\n\t" \
  "v_mfma_f32_16x16x32_bf16 v[132:135], a[4:7], v[0:3], 0\n\t" \
  "v_cvt_pk_bf16_f32 v150, v140, v141\n\t" \
  "v_cvt_pk_bf16_f32 v151, v142, v143\n\t" \
  "v_mfma_f32_16x16x32_bf16 v[136:139], a[0:3], v[4:7], 0\n\t" \
  "s_add_i32 m0, s41, 0\n\t" \
  "v_mfma_f32_16x16x32_bf16 v[140:143], a[4:7], v[4:7], 0\n\t" \
  "buffer_load_dwordx4 v160, %[rx], s40 offen lds\n\t" \
  "v_mfma_f32_16x16x32_bf16 v[128:131], a[8:11], v[8:11], v[128:131]\n\t" \
  "ds_read_b128 v[64:67], v164 offset:16384\n\t" \
  "v_mfma_f32_16x16x32_bf16 v[132:135], a[12:15], v[8:11], v[132:135]\n\t" \
  "ds_read_b128 v[68:71], v164 offset:24576\n\t" \
  "v_mfma_f32_16x16x32_bf16 v[136:139], a[8:11], v[12:15], v[136:139]\n\t" \
  "ds_read_b128 v[72:75], v165 offset:16384\n\t" \
  "v_mfma_f32_16x16x32_bf16 v[140:143], a[12:15], v[12:15], v[140:143]\n\t" \
  "buffer_store_dwordx4 v[144:147], v168, %[ru], s42 offen sc1\n\t" \
  "v_mfma_f32_16x16x32_bf16 v[128:131], a[16:19], v[16:19], v[128:131]\n\t" \
  "ds_read_b128 v[76:79], v165 offset:24576\n\t" \
  "v_mfma_f32_16x16x32_bf16 v[132:135], a[20:23], v[16:19], v[132:135]\n\t" \
  "ds_read_b128 v[80:83], v166 offset:16384\n\t" \
  "v_mfma_f32_16x16x32_bf16 v[136:139], a[16:19], v[20:23], v[136:139]\n\t" \
  "s_add_i32 m0, s41, 1024\n\t" \
  "v_mfma_f32_16x16x32_bf16 v[140:143], a[20:23], v[20:23], v[140:143]\n\t" \
  "buffer_load_dwordx4 v161, %[rx], s40 offen lds\n\t" \
  "v_mfma_f32_16x16x32_bf16 v[128:131], a[24:27], v[24:27], v[128:131]\n\t" \
  "ds_read_b128 v[84:87], v166 offset:24576\n\t" \
  "v_mfma_f32_16x16x32_bf16 v[132:135], a[28:31], v[24:27], v[132:135]\n\t" \
  "ds_read_b128 v[88:91], v167 offset:16384\n\t" \
  "v_mfma_f32_16x16x32_bf16 v[136:139], a[24:27], v[28:31], v[136:139]\n\t" \
  "ds_read_b128 v[92:95], v167 offset:24576\n\t" \
  "v_mfma_f32_16x16x32_bf16 v[140:143], a[28:31], v[28:31], v[140:143]\n\t" \
  "buffer_store_dwordx4 v[148:151], v169, %[ru], s42 offen sc1\n\t" \
  "v_mfma_f32_16x16x32_bf16 v[128:131], a[32:35], v[32:35], v[128:131]\n\t" \
  "ds_read_b128 v[96:99], v164 offset:16640\n\t" \
  "v_mfma_f32_16x16x32_bf16 v[132:135], a[36:39], v[32:35], v[132:135]\n\t" \
  "ds_read_b128 v[100:103], v164 offset:24832\n\t" \
  "v_mfma_f32_16x16x32_bf16 v[136:139], a[32:35], v[36:39], v[136:139]\n\t" \
  "s_add_i32 m0, s41, 2048\n\t" \
  "v_mfma_f32_16x16x32_bf16 v[140:143], a[36:39], v[36:39], v[140:143]\n\t" \
  "buffer_load_dwordx4 v162, %[rx], s40 offen lds\n\t" \
  "v_mfma_f32_16x16x32_bf16 v[128:131], a[40:43], v[40:43], v[128:131]\n\t" \
  "ds_read_b128 v[104:107], v165 offset:16640\n\t" \
  "v_mfma_f32_16x16x32_bf16 v[132:135], a[44:47], v[40:43], v[132:135]\n\t" \
  "ds_read_b128 v[108:111], v165 offset:24832\n\t" \
  "v_mfma_f32_16x16x32_bf16 v[136:139], a[40:43], v[44:47], v[136:139]\n\t" \
  "s_add_i32 m0, s41, 3072\n\t" \
  "v_mfma_f32_16x16x32_bf16 v[140:143], a[44:47], v[44:47], v[140:143]\n\t" \
  "buffer_load_dwordx4 v163, %[rx], s40 offen lds\n\t" \
  "v_mfma_f32_16x16x32_bf16 v[128:131], a[48:51], v[48:51], v[128:131]\n\t" \
  "ds_read_b128 v[112:115], v166 offset:16640\n\t" \
  "v_mfma_f32_16x16x32_bf16 v[132:135], a[52:55], v[48:51], v[132:135]\n\t" \
  "ds_read_b128 v[116:119], v166 offset:24832\n\t" \
  "v_mfma_f32_16x16x32_bf16 v[136:139], a[48:51], v[52:55], v[136:139]\n\t" \
  "ds_read_b128 v[120:123], v167 offset:16640\n\t" \
  "v_mfma_f32_16x16x32_bf16 v[140:143], a[52:55], v[52:55], v[140:143]\n\t" \
  "ds_read_b128 v[124:127], v167 offset:24832\n\t" \
  "v_mfma_f32_16x16x32_bf16 v[128:131], a[56:59], v[56:59], v[128:131]\n\t" \
  "v_mfma_f32_16x16x32_bf16 v[132:135], a[60:63], v[56:59], v[132:135]\n\t" \
  "v_mfma_f32_16x16x32_bf16 v[136:139], a[56:59], v[60:63], v[136:139]\n\t" \
  "v_mfma_f32_16x16x32_bf16 v[140:143], a[60:63], v[60:63], v[140:143]\n\t" \
  "s_cmp_le_u32 s44, 12\n\t" \
  "s_waitcnt vmcnt(30) lgkmcnt(0)\n\t" \
  "v_mfma_f32_16x16x32_bf16 v[128:131], a[64:67], v[64:67], v[128:131]\n\t" \
  "s_cbranch_scc1 31f\n\t" \
  "41:\n\t" \
  "s_barrier\n\t" \
  "v_mfma_f32_16x16x32_bf16 v[132:135], a[68:71], v[64:67], v[132:135]\n\t" \
  "s_add_i32 s76, s40, 512\n\t" \
  "v_mfma_f32_16x16x32_bf16 v[136:139], a[64:67], v[68:71], v[136:139]\n\t" \
  "s_add_i32 m0, s41, 16384\n\t" \
  "v_mfma_f32_16x16x32_bf16 v[140:143], a[68:71], v[68:71], v[140:143]\n\t" \
  "buffer_load_dwordx4 v160, %[rx], s76 offen lds\n\t" \
  "v_mfma_f32_16x16x32_bf16 v[128:131], a[72:75], v[72:75], v[128:131]\n\t" \
  "ds_read_b128 v[0:3], v164 offset:32768\n\t" \
  "v_mfma_f32_16x16x32_bf16 v[132:135], a[76:79], v[72:75], v[132:135]\n\t" \
  "ds_read_b128 v[4:7], v164 offset:40960\n\t" \
  "v_mfma_f32_16x16x32_bf16 v[136:139], a[72:75], v[76:79], v[136:139]\n\t" \
  "ds_read_b128 v[8:11], v165 offset:32768\n\t" \
  "v_mfma_f32_16x16x32_bf16 v[140:143], a[76:79], v[76:79], v[140:143]\n\t" \
  "v_mfma_f32_16x16x32_bf16 v[128:131], a[80:83], v[80:83], v[128:131]\n\t" \
  "ds_read_b128 v[12:15], v165 offset:40960\n\t" \
  "v_mfma_f32_16x16x32_bf16 v[132:135], a[84:87], v[80:83], v[132:135]\n\t" \
  "ds_read_b128 v[16:19], v166 offset:32768\n\t" \
  "v_mfma_f32_16x16x32_bf16 v[136:139], a[80:83], v[84:87], v[136:139]\n\t" \
  "s_add_i32 m0, s41, 17408\n\t" \
  "v_mfma_f32_16x16x32_bf16 v[140:143], a[84:87], v[84:87], v[140:143]\n\t" \
  "buffer_load_dwordx4 v161, %[rx], s76 offen lds\n\t" \
  "v_mfma_f32_16x16x32_bf16 v[128:131], a[88:91], v[88:91], v[128:131]\n\t" \
  "ds_read_b128 v[20:23], v166 offset:40960\n\t" \
  "v_mfma_f32_16x16x32_bf16 v[132:135], a[92:95], v[88:91], v[132:135]\n\t" \
  "ds_read_b128 v[24:27], v167 offset:32768\n\t" \
  "v_mfma_f32_16x16x32_bf16 v[136:139], a[88:91], v[92:95], v[136:139]\n\t" \
  "ds_read_b128 v[28:31], v167 offset:40960\n\t" \
  "v_mfma_f32_16x16x32_bf16 v[140:143], a[92:95], v[92:95], v[140:143]\n\t" \
  "v_mfma_f32_16x16x32_bf16 v[128:131], a[96:99], v[96:99], v[128:131]\n\t" \
  "ds_read_b128 v[32:35], v164 offset:33024\n\t" \
  "v_mfma_f32_16x16x32_bf16 v[132:135], a[100:103], v[96:99], v[132:135]\n\t" \
  "ds_read_b128 v[36:39], v164 offset:41216\n\t" \
  "v_mfma_f32_16x16x32_bf16 v[136:139], a[96:99], v[100:103], v[136:139]\n\t" \
  "s_add_i32 m0, s41, 18432\n\t" \
  "v_mfma_f32_16x16x32_bf16 v[140:143], a[100:103], v[100:103], v[140:143]\n\t" \
  "buffer_load_dwordx4 v162, %[rx], s76 offen lds\n\t" \
  "v_mfma_f32_16x16x32_bf16 v[128:131], a[104:107], v[104:107], v[128:131]\n\t" \
  "ds_read_b128 v[40:43], v165 offset:33024\n\t" \
  "v_mfma_f32_16x16x32_bf16 v[132:135], a[108:111], v[104:107], v[132:135]\n\t" \
  "ds_read_b128 v[44:47], v165 offset:41216\n\t" \
  "v_mfma_f32_16x16x32_bf16 v[136:139], a[104:107], v[108:111], v[136:139]\n\t" \
  "s_add_i32 m0, s41, 19456\n\t" \
  "v_mfma_f32_16x16x32_bf16 v[140:143], a[108:111], v[108:111], v[140:143]\n\t" \
  "buffer_load_dwordx4 v163, %[rx], s76 offen lds\n\t" \
  "v_mfma_f32_16x16x32_bf16 v[128:131], a[112:115], v[112:115], v[128:131]\n\t" \
  "ds_read_b128 v[48:51], v166 offset:33024\n\t" \
  "v_mfma_f32_16x16x32_bf16 v[132:135], a[116:119], v[112:115], v[132:135]\n\t" \
  "ds_read_b128 v[52:55], v166 offset:41216\n\t" \
  "v_mfma_f32_16x16x32_bf16 v[136:139], a[112:115], v[116:119], v[136:139]\n\t" \
  "ds_read_b128 v[56:59], v167 offset:33024\n\t" \
  "v_mfma_f32_16x16x32_bf16 v[140:143], a[116:119], v[116:119], v[140:143]\n\t" \
  "ds_read_b128 v[60:63], v167 offset:41216\n\t" \
  "v_mfma_f32_16x16x32_bf16 v[128:131], a[120:123], v[120:123], v[128:131]\n\t" \
  "s_and_b32 s79, s44, 4\n\t" \
  "v_mfma_f32_16x16x32_bf16 v[132:135], a[124:127], v[120:123], v[132:135]\n\t" \
  "s_lshl_b32 s79, s79, 11\n\t" \
  "v_mfma_f32_16x16x32_bf16 v[136:139], a[120:123], v[124:127], v[136:139]\n\t" \
  "s_cmp_lt_u32 s44, 4\n\t" \
  "s_cselect_b32 s80, 0x2000, 0\n\t" \
  "v_mfma_f32_16x16x32_bf16 v[140:143], a[124:127], v[124:127], v[140:143]\n\t" \
  "s_cmp_le_u32 s44, 12\n\t" \
  "s_waitcnt vmcnt(29) lgkmcnt(0)\n\t" \
  "v_mfma_f32_16x16x32_bf16 v[128:131], a[128:131], v[0:3], v[128:131]\n\t" \
  "s_cbranch_scc1 32f\n\t" \
  "42:\n\t" \
  "s_barrier\n\t" \
  "v_mfma_f32_16x16x32_bf16 v[132:135], a[132:135], v[0:3], v[132:135]\n\t" \
  "s_add_i32 s76, s40, 1024\n\t" \
  "v_mfma_f32_16x16x32_bf16 v[136:139], a[128:131], v[4:7], v[136:139]\n\t" \
  "s_add_i32 m0, s41, 32768\n\t" \
  "v_mfma_f32_16x16x32_bf16 v[140:143], a[132:135], v[4:7], v[140:143]\n\t" \
  "buffer_load_dwordx4 v160, %[rx], s76 offen lds\n\t" \
  "v_mfma_f32_16x16x32_bf16 v[128:131], a[136:139], v[8:11], v[128:131]\n\t" \
  "ds_read_b128 v[64:67], v164 offset:49152\n\t" \
  "v_mfma_f32_16x16x32_bf16 v[132:135], a[140:143], v[8:11], v[132:135]\n\t" \
  "ds_read_b128 v[68:71], v164 offset:57344\n\t" \
  "v_mfma_f32_16x16x32_bf16 v[136:139], a[136:139], v[12:15], v[136:139]\n\t" \
  "ds_read_b128 v[72:75], v165 offset:49152\n\t" \
  "v_mfma_f32_16x16x32_bf16 v[140:143], a[140:143], v[12:15], v[140:143]\n\t" \
  "v_mfma_f32_16x16x32_bf16 v[128:131], a[144:147], v[16:19], v[128:131]\n\t" \
  "ds_read_b128 v[76:79], v165 offset:57344\n\t" \
  "v_mfma_f32_16x16x32_bf16 v[132:135], a[148:151], v[16:19], v[132:135]\n\t" \
  "ds_read_b128 v[80:83], v166 offset:49152\n\t" \
  "v_mfma_f32_16x16x32_bf16 v[136:139], a[144:147], v[20:23], v[136:139]\n\t" \
  "s_add_i32 m0, s41, 33792\n\t" \
  "v_mfma_f32_16x16x32_bf16 v[140:143], a[148:151], v[20:23], v[140:143]\n\t" \
  "buffer_load_dwordx4 v161, %[rx], s76 offen lds\n\t" \
  "v_mfma_f32_16x16x32_bf16 v[128:131], a[152:155], v[24:27], v[128:131]\n\t" \
  "ds_read_b128 v[84:87], v166 offset:57344\n\t" \
  "v_mfma_f32_16x16x32_bf16 v[132:135], a[156:159], v[24:27], v[132:135]\n\t" \
  "ds_read_b128 v[88:91], v167 offset:49152\n\t" \
  "v_mfma_f32_16x16x32_bf16 v[136:139], a[152:155], v[28:31], v[136:139]\n\t" \
  "ds_read_b128 v[92:95], v167 offset:57344\n\t" \
  "v_mfma_f32_16x16x32_bf16 v[140:143], a[156:159], v[28:31], v[140:143]\n\t" \
  "v_mfma_f32_16x16x32_bf16 v[128:131], a[160:163], v[32:35], v[128:131]\n\t" \
  "ds_read_b128 v[96:99], v164 offset:49408\n\t" \
  "v_mfma_f32_16x16x32_bf16 v[132:135], a[164:167], v[32:35], v[132:135]\n\t" \
  "ds_read_b128 v[100:103], v164 offset:57600\n\t" \
  "v_mfma_f32_16x16x32_bf16 v[136:139], a[160:163], v[36:39], v[136:139]\n\t" \
  "s_add_i32 m0, s41, 34816\n\t" \
  "v_mfma_f32_16x16x32_bf16 v[140:143], a[164:167], v[36:39], v[140:143]\n\t" \
  "buffer_load_dwordx4 v162, %[rx], s76 offen lds\n\t" \
  "v_mfma_f32_16x16x32_bf16 v[128:131], a[168:171], v[40:43], v[128:131]\n\t" \
  "ds_read_b128 v[104:107], v165 offset:49408\n\t" \
  "v_mfma_f32_16x16x32_bf16 v[132:135], a[172:175], v[40:43], v[132:135]\n\t" \
  "ds_read_b128 v[108:111], v165 offset:57600\n\t" \
  "v_mfma_f32_16x16x32_bf16 v[136:139], a[168:171], v[44:47], v[136:139]\n\t" \
  "s_add_i32 m0, s41, 35840\n\t" \
  "v_mfma_f32_16x16x32_bf16 v[140:143], a[172:175], v[44:47], v[140:143]\n\t" \
  "buffer_load_dwordx4 v163, %[rx], s76 offen lds\n\t" \
  "v_mfma_f32_16x16x32_bf16 v[128:131], a[176:179], v[48:51], v[128:131]\n\t" \
  "ds_read_b128 v[112:115], v166 offset:49408\n\t" \
  "v_mfma_f32_16x16x32_bf16 v[132:135], a[180:183], v[48:51], v[132:135]\n\t" \
  "ds_read_b128 v[116:119], v166 offset:57600\n\t" \
  "v_mfma_f32_16x16x32_bf16 v[136:139], a[176:179], v[52:55], v[136:139]\n\t" \
  "ds_read_b128 v[120:123], v167 offset:49408\n\t" \
  "v_mfma_f32_16x16x32_bf16 v[140:143], a[180:183], v[52:55], v[140:143]\n\t" \
  "ds_read_b128 v[124:127], v167 offset:57600\n\t" \
  "v_mfma_f32_16x16x32_bf16 v[128:131], a[184:187], v[56:59], v[128:131]\n\t" \
  "s_add_i32 s78, s44, -2\n\t" \
  "v_mfma_f32_16x16x32_bf16 v[132:135], a[188:191], v[56:59], v[132:135]\n\t" \
  "s_max_i32 s78, s78, s46\n\t" \
  "v_mfma_f32_16x16x32_bf16 v[136:139], a[184:187], v[60:63], v[136:139]\n\t" \
  "v_mov_b32 v156, s78\n\t" \
  "v_mfma_f32_16x16x32_bf16 v[140:143], a[188:191], v[60:63], v[140:143]\n\t" \
  "s_cmp_le_u32 s44, 12\n\t" \
  "s_waitcnt vmcnt(27) lgkmcnt(0)\n\t" \
  "v_mfma_f32_16x16x32_bf16 v[128:131], a[192:195], v[64:67], v[128:131]\n\t" \
  "s_cbranch_scc1 33f\n\t" \
  "43:\n\t" \
  "s_barrier\n\t" \
  "v_xor_b32 v164, 0x10000, v164\n\t" \
  "v_mfma_f32_16x16x32_bf16 v[132:135], a[196:199], v[64:67], v[132:135]\n\t" \
  "s_add_i32 s76, s40, 1536\n\t" \
  "buffer_store_dword v156, v171, %[ru], 0 offen sc1\n\t" \
  "v_mfma_f32_16x16x32_bf16 v[136:139], a[192:195], v[68:71], v[136:139]\n\t" \
  "s_add_i32 m0, s41, 49152\n\t" \
  "v_xor_b32 v165, 0x10000, v165\n\t" \
  "v_mfma_f32_16x16x32_bf16 v[140:143], a[196:199], v[68:71], v[140:143]\n\t" \
  "buffer_load_dwordx4 v160, %[rx], s76 offen lds\n\t" \
  "v_xor_b32 v166, 0x10000, v166\n\t" \
  "v_mfma_f32_16x16x32_bf16 v[128:131], a[200:203], v[72:75], v[128:131]\n\t" \
  "v_xor_b32 v167, 0x10000, v167\n\t" \
  "ds_read_b128 v[0:3], v164 offset:0\n\t" \
  "v_mfma_f32_16x16x32_bf16 v[132:135], a[204:207], v[72:75], v[132:135]\n\t" \
  "ds_read_b128 v[4:7], v164 offset:8192\n\t" \
  "v_mfma_f32_16x16x32_bf16 v[136:139], a[200:203], v[76:79], v[136:139]\n\t" \
  "ds_read_b128 v[8:11], v165 offset:0\n\t" \
  "v_mfma_f32_16x16x32_bf16 v[140:143], a[204:207], v[76:79], v[140:143]\n\t" \
  "v_mfma_f32_16x16x32_bf16 v[128:131], a[208:211], v[80:83], v[128:131]\n\t" \
  "ds_read_b128 v[12:15], v165 offset:8192\n\t" \
  "v_mfma_f32_16x16x32_bf16 v[132:135], a[212:215], v[80:83], v[132:135]\n\t" \
  "ds_read_b128 v[16:19], v166 offset:0\n\t" \
  "v_mfma_f32_16x16x32_bf16 v[136:139], a[208:211], v[84:87], v[136:139]\n\t" \
  "s_add_i32 m0, s41, 50176\n\t" \
  "ds_read_b128 v[20:23], v166 offset:8192\n\t" \
  "v_mfma_f32_16x16x32_bf16 v[140:143], a[212:215], v[84:87], v[140:143]\n\t" \
  "buffer_load_dwordx4 v161, %[rx], s76 offen lds\n\t" \
  "v_mfma_f32_16x16x32_bf16 v[128:131], a[216:219], v[88:91], v[128:131]\n\t" \
  "ds_read_b128 v[24:27], v167 offset:0\n\t" \
  "v_mfma_f32_16x16x32_bf16 v[132:135], a[220:223], v[88:91], v[132:135]\n\t" \
  "ds_read_b128 v[28:31], v167 offset:8192\n\t" \
  "v_mfma_f32_16x16x32_bf16 v[136:139], a[216:219], v[92:95], v[136:139]\n\t" \
  "ds_read_b128 v[32:35], v164 offset:256\n\t" \
  "v_mfma_f32_16x16x32_bf16 v[140:143], a[220:223], v[92:95], v[140:143]\n\t" \
  "v_mfma_f32_16x16x32_bf16 v[128:131], a[224:227], v[96:99], v[128:131]\n\t" \
  "ds_read_b128 v[36:39], v164 offset:8448\n\t" \
  "v_mfma_f32_16x16x32_bf16 v[132:135], a[228:231], v[96:99], v[132:135]\n\t" \
  "ds_read_b128 v[40:43], v165 offset:256\n\t" \
  "v_mfma_f32_16x16x32_bf16 v[136:139], a[224:227], v[100:103], v[136:139]\n\t" \
  "s_add_i32 m0, s41, 51200\n\t" \
  "v_mfma_f32_16x16x32_bf16 v[140:143], a[228:231], v[100:103], v[140:143]\n\t" \
  "buffer_load_dwordx4 v162, %[rx], s76 offen lds\n\t" \
  "v_mfma_f32_16x16x32_bf16 v[128:131], a[232:235], v[104:107], v[128:131]\n\t" \
  "ds_read_b128 v[44:47], v165 offset:8448\n\t" \
  "v_mfma_f32_16x16x32_bf16 v[132:135], a[236:239], v[104:107], v[132:135]\n\t" \
  "ds_read_b128 v[48:51], v166 offset:256\n\t" \
  "v_mfma_f32_16x16x32_bf16 v[136:139], a[232:235], v[108:111], v[136:139]\n\t" \
  "s_add_i32 m0, s41, 52224\n\t" \
  "v_mfma_f32_16x16x32_bf16 v[140:143], a[236:239], v[108:111], v[140:143]\n\t" \
  "buffer_load_dwordx4 v163, %[rx], s76 offen lds\n\t" \
  "v_mfma_f32_16x16x32_bf16 v[128:131], a[240:243], v[112:115], v[128:131]\n\t" \
  "ds_read_b128 v[52:55], v166 offset:8448\n\t" \
  "v_mfma_f32_16x16x32_bf16 v[132:135], a[244:247], v[112:115], v[132:135]\n\t" \
  "ds_read_b128 v[56:59], v167 offset:256\n\t" \
  "v_mfma_f32_16x16x32_bf16 v[136:139], a[240:243], v[116:119], v[136:139]\n\t" \
  "ds_read_b128 v[60:63], v167 offset:8448\n\t" \
  "v_mfma_f32_16x16x32_bf16 v[140:143], a[244:247], v[116:119], v[140:143]\n\t" \
  "s_add_i32 s44, s44, 1\n\t" \
  "s_xor_b32 s42, s43, s79\n\t" \
  "v_mfma_f32_16x16x32_bf16 v[128:131], a[248:251], v[120:123], v[128:131]\n\t" \
  "s_add_i32 s43, s43, 0x80000\n\t" \
  "s_add_i32 s40, s40, 0x10000\n\t" \
  "v_mfma_f32_16x16x32_bf16 v[132:135], a[252:255], v[120:123], v[132:135]\n\t" \
  "s_xor_b32 s41, s41, 0x10000\n\t" \
  "s_andn2_b32 s42, s42, s80\n\t" \
  "v_mfma_f32_16x16x32_bf16 v[136:139], a[248:251], v[124:127], v[136:139]\n\t" \
  "v_cvt_pk_bf16_f32 v144, v128, v129\n\t" \
  "v_cvt_pk_bf16_f32 v145, v130, v131\n\t" \
  "v_mfma_f32_16x16x32_bf16 v[140:143], a[252:255], v[124:127], v[140:143]\n\t" \
  "v_cvt_pk_bf16_f32 v146, v132, v133\n\t" \
  "v_cvt_pk_bf16_f32 v147, v134, v135\n\t" \
  "s_cmp_eq_u32 s44, 64\n\t" \
  "s_cbranch_scc1 98f\n\t" \
  "s_cmp_le_u32 s44, 12\n\t" \
  "s_branch 20b\n\t" \
  "30:\n\t" \
  "s_waitcnt vmcnt(24)\n\t" \
  "s_branch 40b\n\t" \
  "31:\n\t" \
  "s_waitcnt vmcnt(24)\n\t" \
  "s_branch 41b\n\t" \
  "32:\n\t" \
  "s_waitcnt vmcnt(6)\n\t" \
  "s_barrier\n\t" \
  "s_add_i32 s46, s44, -1\n\t" \
  "v_mov_b32 v156, s46\n\t" \
  "buffer_store_dword v156, v171, %[ru], 0 offen sc1\n\t" \
  "s_branch 42b\n\t" \
  "33:\n\t" \
  "s_waitcnt vmcnt(24)\n\t" \
  "s_branch 43b\n\t" \
  "91:\n\t" \
  "ds_read_b128 v[0:3], v164 offset:0\n\t" \
  "ds_read_b128 v[4:7], v164 offset:8192\n\t" \
  "ds_read_b128 v[8:11], v165 offset:0\n\t" \
  "ds_read_b128 v[12:15], v165 offset:8192\n\t" \
  "ds_read_b128 v[16:19], v166 offset:0\n\t" \
  "ds_read_b128 v[20:23], v166 offset:8192\n\t" \
  "ds_read_b128 v[24:27], v167 offset:0\n\t" \
  "ds_read_b128 v[28:31], v167 offset:8192\n\t" \
  "ds_read_b128 v[32:35], v164 offset:256\n\t" \
  "ds_read_b128 v[36:39], v164 offset:8448\n\t" \
  "ds_read_b128 v[40:43], v165 offset:256\n\t" \
  "ds_read_b128 v[44:47], v165 offset:8448\n\t" \
  "ds_read_b128 v[48:51], v166 offset:256\n\t" \
  "ds_read_b128 v[52:55], v166 offset:8448\n\t" \
  "ds_read_b128 v[56:59], v167 offset:256\n\t" \
  "ds_read_b128 v[60:63], v167 offset:8448\n\t" \
  "v_mov_b64 v[152:153], -1\n\t" \
  "v_mov_b64 v[154:155], -1\n\t" \
  "s_cmp_le_u32 s44, 12\n\t" \
  ".p2align 6\n\t" \
  "20:\n\t" \
  "s_waitcnt vmcnt(28) lgkmcnt(0)\n\t" \
  "v_mfma_f32_16x16x32_bf16 v[128:131], a[0:3], v[0:3], 0\n\t" \
  "s_cbranch_scc1 30f\n\t" \
  "40:\n\t" \
  "s_barrier\n\t" \
  "v_cvt_pk_bf16_f32 v148, v136, v137\n\t" \
  "v_cvt_pk_bf16_f32 v149, v138, v139\n\t" \
  "v_mfma_f32_16x16x32_bf16 v[132:135], a[4:7], v[0:3], 0\n\t" \
  "v_cvt_pk_bf16_f32 v150, v140, v141\n\t" \
  "v_cvt_pk_bf16_f32 v151, v142, v143\n\t" \
  "v_mfma_f32_16x16x32_bf16 v[136:139], a[0:3], v[4:7], 0\n\t" \
  "ds_read_b128 v[64:67], v164 offset:16384\n\t" \
  "v_mfma_f32_16x16x32_bf16 v[140:143], a[4:7], v[4:7], 0\n\t" \
  "s_add_i32 m0, s41, 0\n\t" \
  "v_mfma_f32_16x16x32_bf16 v[128:131], a[8:11], v[8:11], v[128:131]\n\t" \
  "buffer_load_dwordx4 v160, %[rx], s40 offen lds\n\t" \
  "v_mfma_f32_16x16x32_bf16 v[132:135], a[12:15], v[8:11], v[132:135]\n\t" \
  "ds_read_b128 v[68:71], v164 offset:24576\n\t" \
  "v_mfma_f32_16x16x32_bf16 v[136:139], a[8:11], v[12:15], v[136:139]\n\t" \
  "ds_read_b128 v[72:75], v165 offset:16384\n\t" \
  "v_mfma_f32_16x16x32_bf16 v[140:143], a[12:15], v[12:15], v[140:143]\n\t" \
  "ds_read_b128 v[76:79], v165 offset:24576\n\t" \
  "v_mfma_f32_16x16x32_bf16 v[128:131], a[16:19], v[16:19], v[128:131]\n\t" \
  "buffer_store_dwordx4 v[144:147], v168, %[ru], s42 offen sc1\n\t" \
  "v_mfma_f32_16x16x32_bf16 v[132:135], a[20:23], v[16:19], v[132:135]\n\t" \
  "ds_read_b128 v[80:83], v166 offset:16384\n\t" \
  "v_mfma_f32_16x16x32_bf16 v[136:139], a[16:19], v[20:23], v[136:139]\n\t" \
  "ds_read_b128 v[84:87], v166 offset:24576\n\t" \
  "v_mfma_f32_16x16x32_bf16 v[140:143], a[20:23], v[20:23], v[140:143]\n\t" \
  "s_add_i32 m0, s41, 1024\n\t" \
  "v_mfma_f32_16x16x32_bf16 v[128:131], a[24:27], v[24:27], v[128:131]\n\t" \
  "buffer_load_dwordx4 v161, %[rx], s40 offen lds\n\t" \
  "v_mfma_f32_16x16x32_bf16 v[132:135], a[28:31], v[24:27], v[132:135]\n\t" \
  "ds_read_b128 v[88:91], v167 offset:16384\n\t" \
  "v_mfma_f32_16x16x32_bf16 v[136:139], a[24:27], v[28:31], v[136:139]\n\t" \
  "ds_read_b128 v[92:95], v167 offset:24576\n\t" \
  "v_mfma_f32_16x16x32_bf16 v[140:143], a[28:31], v[28:31], v[140:143]\n\t" \
  "ds_read_b128 v[96:99], v164 offset:16640\n\t" \
  "v_mfma_f32_16x16x32_bf16 v[128:131], a[32:35], v[32:35], v[128:131]\n\t" \
  "buffer_store_dwordx4 v[148:151], v169, %[ru], s42 offen sc1\n\t" \
  "v_mfma_f32_16x16x32_bf16 v[132:135], a[36:39], v[32:35], v[132:135]\n\t" \
  "ds_read_b128 v[100:103], v164 offset:24832\n\t" \
  "v_mfma_f32_16x16x32_bf16 v[136:139], a[32:35], v[36:39], v[136:139]\n\t" \
  "ds_read_b128 v[104:107], v165 offset:16640\n\t" \
  "v_mfma_f32_16x16x32_bf16 v[140:143], a[36:39], v[36:39], v[140:143]\n\t" \
  "s_add_i32 m0, s41, 2048\n\t" \
  "v_mfma_f32_16x16x32_bf16 v[128:131], a[40:43], v[40:43], v[128:131]\n\t" \
  "buffer_load_dwordx4 v162, %[rx], s40 offen lds\n\t" \
  "v_mfma_f32_16x16x32_bf16 v[132:135], a[44:47], v[40:43], v[132:135]\n\t" \
  "ds_read_b128 v[108:111], v165 offset:24832\n\t" \
  "v_mfma_f32_16x16x32_bf16 v[136:139], a[40:43], v[44:47], v[136:139]\n\t" \
  "ds_read_b128 v[112:115], v166 offset:16640\n\t" \
  "v_mfma_f32_16x16x32_bf16 v[140:143], a[44:47], v[44:47], v[140:143]\n\t" \
  "s_add_i32 m0, s41, 3072\n\t" \
  "v_mfma_f32_16x16x32_bf16 v[128:131], a[48:51], v[48:51], v[128:131]\n\t" \
  "buffer_load_dwordx4 v163, %[rx], s40 offen lds\n\t" \
  "v_mfma_f32_16x16x32_bf16 v[132:135], a[52:55], v[48:51], v[132:135]\n\t" \
  "ds_read_b128 v[116:119], v166 offset:24832\n\t" \
  "v_mfma_f32_16x16x32_bf16 v[136:139], a[48:51], v[52:55], v[136:139]\n\t" \
  "ds_read_b128 v[120:123], v167 offset:16640\n\t" \
  "v_mfma_f32_16x16x32_bf16 v[140:143], a[52:55], v[52:55], v[140:143]\n\t" \
  "ds_read_b128 v[124:127], v167 offset:24832\n\t" \
  "v_mfma_f32_16x16x32_bf16 v[128:131], a[56:59], v[56:59], v[128:131]\n\t" \
  "v_mfma_f32_16x16x32_bf16 v[132:135], a[60:63], v[56:59], v[132:135]\n\t" \
  "v_mfma_f32_16x16x32_bf16 v[136:139], a[56:59], v[60:63], v[136:139]\n\t" \
  "v_mfma_f32_16x16x32_bf16 v[140:143], a[60:63], v[60:63], v[140:143]\n\t" \
  "s_cmp_le_u32 s44, 12\n\t" \
  "s_waitcnt vmcnt(30) lgkmcnt(0)\n\t" \
  "v_mfma_f32_16x16x32_bf16 v[128:131], a[64:67], v[64:67], v[128:131]\n\t" \
  "s_cbranch_scc1 31f\n\t" \
  "41:\n\t" \
  "s_barrier\n\t" \
  "v_mfma_f32_16x16x32_bf16 v[132:135], a[68:71], v[64:67], v[132:135]\n\t" \
  "s_add_i32 s76, s40, 512\n\t" \
  "v_mfma_f32_16x16x32_bf16 v[136:139], a[64:67], v[68:71], v[136:139]\n\t" \
  "ds_read_b128 v[0:3], v164 offset:32768\n\t" \
  "v_mfma_f32_16x16x32_bf16 v[140:143], a[68:71], v[68:71], v[140:143]\n\t" \
  "s_add_i32 m0, s41, 16384\n\t" \
  "v_mfma_f32_16x16x32_bf16 v[128:131], a[72:75], v[72:75], v[128:131]\n\t" \
  "buffer_load_dwordx4 v160, %[rx], s76 offen lds\n\t" \
  "v_mfma_f32_16x16x32_bf16 v[132:135], a[76:79], v[72:75], v[132:135]\n\t" \
  "ds_read_b128 v[4:7], v164 offset:40960\n\t" \
  "v_mfma_f32_16x16x32_bf16 v[136:139], a[72:75], v[76:79], v[136:139]\n\t" \
  "ds_read_b128 v[8:11], v165 offset:32768\n\t" \
  "v_mfma_f32_16x16x32_bf16 v[140:143], a[76:79], v[76:79], v[140:143]\n\t" \
  "ds_read_b128 v[12:15], v165 offset:40960\n\t" \
  "v_mfma_f32_16x16x32_bf16 v[128:131], a[80:83], v[80:83], v[128:131]\n\t" \
  "v_mfma_f32_16x16x32_bf16 v[132:135], a[84:87], v[80:83], v[132:135]\n\t" \
  "ds_read_b128 v[16:19], v166 offset:32768\n\t" \
  "v_mfma_f32_16x16x32_bf16 v[136:139], a[80:83], v[84:87], v[136:139]\n\t" \
  "ds_read_b128 v[20:23], v166 offset:40960\n\t" \
  "v_mfma_f32_16x16x32_bf16 v[140:143], a[84:87], v[84:87], v[140:143]\n\t" \
  "s_add_i32 m0, s41, 17408\n\t" \
  "v_mfma_f32_16x16x32_bf16 v[128:131], a[88:91], v[88:91], v[128:131]\n\t" \
  "buffer_load_dwordx4 v161, %[rx], s76 offen lds\n\t" \
  "v_mfma_f32_16x16x32_bf16 v[132:135], a[92:95], v[88:91], v[132:135]\n\t" \
  "ds_read_b128 v[24:27], v167 offset:32768\n\t" \
  "v_mfma_f32_16x16x32_bf16 v[136:139], a[88:91], v[92:95], v[136:139]\n\t" \
  "ds_read_b128 v[28:31], v167 offset:40960\n\t" \
  "v_mfma_f32_16x16x32_bf16 v[140:143], a[92:95], v[92:95], v[140:143]\n\t" \
  "ds_read_b128 v[32:35], v164 offset:33024\n\t" \
  "v_mfma_f32_16x16x32_bf16 v[128:131], a[96:99], v[96:99], v[128:131]\n\t" \
  "v_mfma_f32_16x16x32_bf16 v[132:135], a[100:103], v[96:99], v[132:135]\n\t" \
  "ds_read_b128 v[36:39], v164 offset:41216\n\t" \
  "v_mfma_f32_16x16x32_bf16 v[136:139], a[96:99], v[100:103], v[136:139]\n\t" \
  "ds_read_b128 v[40:43], v165 offset:33024\n\t" \
  "v_mfma_f32_16x16x32_bf16 v[140:143], a[100:103], v[100:103], v[140:143]\n\t" \
  "s_add_i32 m0, s41, 18432\n\t" \
  "v_mfma_f32_16x16x32_bf16 v[128:131], a[104:107], v[104:107], v[128:131]\n\t" \
  "buffer_load_dwordx4 v162, %[rx], s76 offen lds\n\t" \
  "v_mfma_f32_16x16x32_bf16 v[132:135], a[108:111], v[104:107], v[132:135]\n\t" \
  "ds_read_b128 v[44:47], v165 offset:41216\n\t" \
  "v_mfma_f32_16x16x32_bf16 v[136:139], a[104:107], v[108:111], v[136:139]\n\t" \
  "ds_read_b128 v[48:51], v166 offset:33024\n\t" \
  "v_mfma_f32_16x16x32_bf16 v[140:143], a[108:111], v[108:111], v[140:143]\n\t" \
  "s_add_i32 m0, s41, 19456\n\t" \
  "v_mfma_f32_16x16x32_bf16 v[128:131], a[112:115], v[112:115], v[128:131]\n\t" \
  "buffer_load_dwordx4 v163, %[rx], s76 offen lds\n\t" \
  "v_mfma_f32_16x16x32_bf16 v[132:135], a[116:119], v[112:115], v[132:135]\n\t" \
  "ds_read_b128 v[52:55], v166 offset:41216\n\t" \
  "v_mfma_f32_16x16x32_bf16 v[136:139], a[112:115], v[116:119], v[136:139]\n\t" \
  "ds_read_b128 v[56:59], v167 offset:33024\n\t" \
  "v_mfma_f32_16x16x32_bf16 v[140:143], a[116:119], v[116:119], v[140:143]\n\t" \
  "ds_read_b128 v[60:63], v167 offset:41216\n\t" \
  "v_mfma_f32_16x16x32_bf16 v[128:131], a[120:123], v[120:123], v[128:131]\n\t" \
  "s_and_b32 s79, s44, 4\n\t" \
  "v_mfma_f32_16x16x32_bf16 v[132:135], a[124:127], v[120:123], v[132:135]\n\t" \
  "s_lshl_b32 s79, s79, 11\n\t" \
  "v_mfma_f32_16x16x32_bf16 v[136:139], a[120:123], v[124:127], v[136:139]\n\t" \
  "s_cmp_lt_u32 s44, 4\n\t" \
  "s_cselect_b32 s80, 0x2000, 0\n\t" \
  "v_mfma_f32_16x16x32_bf16 v[140:143], a[124:127], v[124:127], v[140:143]\n\t" \
  "s_cmp_le_u32 s44, 12\n\t" \
  "s_waitcnt vmcnt(29) lgkmcnt(0)\n\t" \
  "v_mfma_f32_16x16x32_bf16 v[128:131], a[128:131], v[0:3], v[128:131]\n\t" \
  "s_cbranch_scc1 32f\n\t" \
  "42:\n\t" \
  "s_barrier\n\t" \
  "v_mfma_f32_16x16x32_bf16 v[132:135], a[132:135], v[0:3], v[132:135]\n\t" \
  "s_add_i32 s76, s40, 1024\n\t" \
  "v_mfma_f32_16x16x32_bf16 v[136:139], a[128:131], v[4:7], v[136:139]\n\t" \
  "ds_read_b128 v[64:67], v164 offset:49152\n\t" \
  "v_mfma_f32_16x16x32_bf16 v[140:143], a[132:135], v[4:7], v[140:143]\n\t" \
  "s_add_i32 m0, s41, 32768\n\t" \
  "v_mfma_f32_16x16x32_bf16 v[128:131], a[136:139], v[8:11], v[128:131]\n\t" \
  "buffer_load_dwordx4 v160, %[rx], s76 offen lds\n\t" \
  "v_mfma_f32_16x16x32_bf16 v[132:135], a[140:143], v[8:11], v[132:135]\n\t" \
  "ds_read_b128 v[68:71], v164 offset:57344\n\t" \
  "v_mfma_f32_16x16x32_bf16 v[136:139], a[136:139], v[12:15], v[136:139]\n\t" \
  "ds_read_b128 v[72:75], v165 offset:49152\n\t" \
  "v_mfma_f32_16x16x32_bf16 v[140:143], a[140:143], v[12:15], v[140:143]\n\t" \
  "ds_read_b128 v[76:79], v165 offset:57344\n\t" \
  "v_mfma_f32_16x16x32_bf16 v[128:131], a[144:147], v[16:19], v[128:131]\n\t" \
  "v_mfma_f32_16x16x32_bf16 v[132:135], a[148:151], v[16:19], v[132:135]\n\t" \
  "ds_read_b128 v[80:83], v166 offset:49152\n\t" \
  "v_mfma_f32_16x16x32_bf16 v[136:139], a[144:147], v[20:23], v[136:139]\n\t" \
  "ds_read_b128 v[84:87], v166 offset:57344\n\t" \
  "v_mfma_f32_16x16x32_bf16 v[140:143], a[148:151], v[20:23], v[140:143]\n\t" \
  "s_add_i32 m0, s41, 33792\n\t" \
  "v_mfma_f32_16x16x32_bf16 v[128:131], a[152:155], v[24:27], v[128:131]\n\t" \
  "buffer_load_dwordx4 v161, %[rx], s76 offen lds\n\t" \
  "v_mfma_f32_16x16x32_bf16 v[132:135], a[156:159], v[24:27], v[132:135]\n\t" \
  "ds_read_b128 v[88:91], v167 offset:49152\n\t" \
  "v_mfma_f32_16x16x32_bf16 v[136:139], a[152:155], v[28:31], v[136:139]\n\t" \
  "ds_read_b128 v[92:95], v167 offset:57344\n\t" \
  "v_mfma_f32_16x16x32_bf16 v[140:143], a[156:159], v[28:31], v[140:143]\n\t" \
  "ds_read_b128 v[96:99], v164 offset:49408\n\t" \
  "v_mfma_f32_16x16x32_bf16 v[128:131], a[160:163], v[32:35], v[128:131]\n\t" \
  "v_mfma_f32_16x16x32_bf16 v[132:135], a[164:167], v[32:35], v[132:135]\n\t" \
  "ds_read_b128 v[100:103], v164 offset:57600\n\t" \
  "v_mfma_f32_16x16x32_bf16 v[136:139], a[160:163], v[36:39], v[136:139]\n\t" \
  "ds_read_b128 v[104:107], v165 offset:49408\n\t" \
  "v_mfma_f32_16x16x32_bf16 v[140:143], a[164:167], v[36:39], v[140:143]\n\t" \
  "s_add_i32 m0, s41, 34816\n\t" \
  "v_mfma_f32_16x16x32_bf16 v[128:131], a[168:171], v[40:43], v[128:131]\n\t" \
  "buffer_load_dwordx4 v162, %[rx], s76 offen lds\n\t" \
  "v_mfma_f32_16x16x32_bf16 v[132:135], a[172:175], v[40:43], v[132:135]\n\t" \
  "ds_read_b128 v[108:111], v165 offset:57600\n\t" \
  "v_mfma_f32_16x16x32_bf16 v[136:139], a[168:171], v[44:47], v[136:139]\n\t" \
  "ds_read_b128 v[112:115], v166 offset:49408\n\t" \
  "v_mfma_f32_16x16x32_bf16 v[140:143], a[172:175], v[44:47], v[140:143]\n\t" \
  "s_add_i32 m0, s41, 35840\n\t" \
  "v_mfma_f32_16x16x32_bf16 v[128:131], a[176:179], v[48:51], v[128:131]\n\t" \
  "buffer_load_dwordx4 v163, %[rx], s76 offen lds\n\t" \
  "v_mfma_f32_16x16x32_bf16 v[132:135], a[180:183], v[48:51], v[132:135]\n\t" \
  "ds_read_b128 v[116:119], v166 offset:57600\n\t" \
  "v_mfma_f32_16x16x32_bf16 v[136:139], a[176:179], v[52:55], v[136:139]\n\t" \
  "ds_read_b128 v[120:123], v167 offset:49408\n\t" \
  "v_mfma_f32_16x16x32_bf16 v[140:143], a[180:183], v[52:55], v[140:143]\n\t" \
  "ds_read_b128 v[124:127], v167 offset:57600\n\t" \
  "v_mfma_f32_16x16x32_bf16 v[128:131], a[184:187], v[56:59], v[128:131]\n\t" \
  "s_add_i32 s78, s44, -2\n\t" \
  "v_mfma_f32_16x16x32_bf16 v[132:135], a[188:191], v[56:59], v[132:135]\n\t" \
  "s_max_i32 s78, s78, s46\n\t" \
  "v_mfma_f32_16x16x32_bf16 v[136:139], a[184:187], v[60:63], v[136:139]\n\t" \
  "v_mov_b32 v156, s78\n\t" \
  "v_mfma_f32_16x16x32_bf16 v[140:143], a[188:191], v[60:63], v[140:143]\n\t" \
  "s_cmp_le_u32 s44, 12\n\t" \
  "s_waitcnt vmcnt(27) lgkmcnt(0)\n\t" \
  "v_mfma_f32_16x16x32_bf16 v[128:131], a[192:195], v[64:67], v[128:131]\n\t" \
  "s_cbranch_scc1 33f\n\t" \
  "43:\n\t" \
  "s_barrier\n\t" \
  "v_xor_b32 v164, 0x10000, v164\n\t" \
  "v_mfma_f32_16x16x32_bf16 v[132:135], a[196:199], v[64:67], v[132:135]\n\t" \
  "s_add_i32 s76, s40, 1536\n\t" \
  "buffer_store_dword v156, v171, %[ru], 0 offen sc1\n\t" \
  "v_mfma_f32_16x16x32_bf16 v[136:139], a[192:195], v[68:71], v[136:139]\n\t" \
  "v_xor_b32 v165, 0x10000, v165\n\t" \
  "ds_read_b128 v[0:3], v164 offset:0\n\t" \
  "v_mfma_f32_16x16x32_bf16 v[140:143], a[196:199], v[68:71], v[140:143]\n\t" \
  "s_add_i32 m0, s41, 49152\n\t" \
  "v_xor_b32 v166, 0x10000, v166\n\t" \
  "v_mfma_f32_16x16x32_bf16 v[128:131], a[200:203], v[72:75], v[128:131]\n\t" \
  "buffer_load_dwordx4 v160, %[rx], s76 offen lds\n\t" \
  "v_xor_b32 v167, 0x10000, v167\n\t" \
  "v_mfma_f32_16x16x32_bf16 v[132:135], a[204:207], v[72:75], v[132:135]\n\t" \
  "ds_read_b128 v[4:7], v164 offset:8192\n\t" \
  "v_mfma_f32_16x16x32_bf16 v[136:139], a[200:203], v[76:79], v[136:139]\n\t" \
  "ds_read_b128 v[8:11], v165 offset:0\n\t" \
  "v_mfma_f32_16x16x32_bf16 v[140:143], a[204:207], v[76:79], v[140:143]\n\t" \
  "ds_read_b128 v[12:15], v165 offset:8192\n\t" \
  "v_mfma_f32_16x16x32_bf16 v[128:131], a[208:211], v[80:83], v[128:131]\n\t" \
  "v_mfma_f32_16x16x32_bf16 v[132:135], a[212:215], v[80:83], v[132:135]\n\t" \
  "ds_read_b128 v[16:19], v166 offset:0\n\t" \
  "v_mfma_f32_16x16x32_bf16 v[136:139], a[208:211], v[84:87], v[136:139]\n\t" \
  "ds_read_b128 v[20:23], v166 offset:8192\n\t" \
  "v_mfma_f32_16x16x32_bf16 v[140:143], a[212:215], v[84:87], v[140:143]\n\t" \
  "s_add_i32 m0, s41, 50176\n\t" \
  "ds_read_b128 v[24:27], v167 offset:0\n\t" \
  "v_mfma_f32_16x16x32_bf16 v[128:131], a[216:219], v[88:91], v[128:131]\n\t" \
  "buffer_load_dwordx4 v161, %[rx], s76 offen lds\n\t" \
  "v_mfma_f32_16x16x32_bf16 v[132:135], a[220:223], v[88:91], v[132:135]\n\t" \
  "ds_read_b128 v[28:31], v167 offset:8192\n\t" \
  "v_mfma_f32_16x16x32_bf16 v[136:139], a[216:219], v[92:95], v[136:139]\n\t" \
  "ds_read_b128 v[32:35], v164 offset:256\n\t" \
  "v_mfma_f32_16x16x32_bf16 v[140:143], a[220:223], v[92:95], v[140:143]\n\t" \
  "ds_read_b128 v[36:39], v164 offset:8448\n\t" \
  "v_mfma_f32_16x16x32_bf16 v[128:131], a[224:227], v[96:99], v[128:131]\n\t" \
  "v_mfma_f32_16x16x32_bf16 v[132:135], a[228:231], v[96:99], v[132:135]\n\t" \
  "ds_read_b128 v[40:43], v165 offset:256\n\t" \
  "v_mfma_f32_16x16x32_bf16 v[136:139], a[224:227], v[100:103], v[136:139]\n\t" \
  "ds_read_b128 v[44:47], v165 offset:8448\n\t" \
  "v_mfma_f32_16x16x32_bf16 v[140:143], a[228:231], v[100:103], v[140:143]\n\t" \
  "s_add_i32 m0, s41, 51200\n\t" \
  "v_mfma_f32_16x16x32_bf16 v[128:131], a[232:235], v[104:107], v[128:131]\n\t" \
  "buffer_load_dwordx4 v162, %[rx], s76 offen lds\n\t" \
  "v_mfma_f32_16x16x32_bf16 v[132:135], a[236:239], v[104:107], v[132:135]\n\t" \
  "ds_read_b128 v[48:51], v166 offset:256\n\t" \
  "v_mfma_f32_16x16x32_bf16 v[136:139], a[232:235], v[108:111], v[136:139]\n\t" \
  "ds_read_b128 v[52:55], v166 offset:8448\n\t" \
  "v_mfma_f32_16x16x32_bf16 v[140:143], a[236:239], v[108:111], v[140:143]\n\t" \
  "s_add_i32 m0, s41, 52224\n\t" \
  "v_mfma_f32_16x16x32_bf16 v[128:131], a[240:243], v[112:115], v[128:131]\n\t" \
  "buffer_load_dwordx4 v163, %[rx], s76 offen lds\n\t" \
  "v_mfma_f32_16x16x32_bf16 v[132:135], a[244:247], v[112:115], v[132:135]\n\t" \
  "ds_read_b128 v[56:59], v167 offset:256\n\t" \
  "v_mfma_f32_16x16x32_bf16 v[136:139], a[240:243], v[116:119], v[136:139]\n\t" \
  "ds_read_b128 v[60:63], v167 offset:8448\n\t" \
  "v_mfma_f32_16x16x32_bf16 v[140:143], a[244:247], v[116:119], v[140:143]\n\t" \
  "s_add_i32 s44, s44, 1\n\t" \
  "s_xor_b32 s42, s43, s79\n\t" \
  "v_mfma_f32_16x16x32_bf16 v[128:131], a[248:251], v[120:123], v[128:131]\n\t" \
  "s_add_i32 s43, s43, 0x80000\n\t" \
  "s_add_i32 s40, s40, 0x10000\n\t" \
  "v_mfma_f32_16x16x32_bf16 v[132:135], a[252:255], v[120:123], v[132:135]\n\t" \
  "s_xor_b32 s41, s41, 0x10000\n\t" \
  "s_andn2_b32 s42, s42, s80\n\t" \
  "v_mfma_f32_16x16x32_bf16 v[136:139], a[248:251], v[124:127], v[136:139]\n\t" \
  "v_cvt_pk_bf16_f32 v144, v128, v129\n\t" \
  "v_cvt_pk_bf16_f32 v145, v130, v131\n\t" \
  "v_mfma_f32_16x16x32_bf16 v[140:143], a[252:255], v[124:127], v[140:143]\n\t" \
  "v_cvt_pk_bf16_f32 v146, v132, v133\n\t" \
  "v_cvt_pk_bf16_f32 v147, v134, v135\n\t" \
  "s_cmp_eq_u32 s44, 64\n\t" \
  "s_cbranch_scc1 98f\n\t" \
  "s_cmp_le_u32 s44, 12\n\t" \
  "s_branch 20b\n\t" \
  "30:\n\t" \
  "s_waitcnt vmcnt(24)\n\t" \
  "s_branch 40b\n\t" \
  "31:\n\t" \
  "s_waitcnt vmcnt(24)\n\t" \
  "s_branch 41b\n\t" \
  "32:\n\t" \
  "s_waitcnt vmcnt(6)\n\t" \
  "s_barrier\n\t" \
  "s_add_i32 s46, s44, -1\n\t" \
  "v_mov_b32 v156, s46\n\t" \
  "buffer_store_dword v156, v171, %[ru], 0 offen sc1\n\t" \
  "s_branch 42b\n\t" \
  "33:\n\t" \
  "s_waitcnt vmcnt(24)\n\t" \
  "s_branch 43b\n\t" \
  "92:\n\t" \
  "ds_read_b128 v[0:3], v164 offset:0\n\t" \
  "ds_read_b128 v[4:7], v164 offset:8192\n\t" \
  "ds_read_b128 v[8:11], v165 offset:0\n\t" \
  "ds_read_b128 v[12:15], v165 offset:8192\n\t" \
  "ds_read_b128 v[16:19], v166 offset:0\n\t" \
  "ds_read_b128 v[20:23], v166 offset:8192\n\t" \
  "ds_read_b128 v[24:27], v167 offset:0\n\t" \
  "ds_read_b128 v[28:31], v167 offset:8192\n\t" \
  "ds_read_b128 v[32:35], v164 offset:256\n\t" \
  "ds_read_b128 v[36:39], v164 offset:8448\n\t" \
  "ds_read_b128 v[40:43], v165 offset:256\n\t" \
  "ds_read_b128 v[44:47], v165 offset:8448\n\t" \
  "ds_read_b128 v[48:51], v166 offset:256\n\t" \
  "ds_read_b128 v[52:55], v166 offset:8448\n\t" \
  "ds_read_b128 v[56:59], v167 offset:256\n\t" \
  "ds_read_b128 v[60:63], v167 offset:8448\n\t" \
  "v_mov_b64 v[152:153], -1\n\t" \
  "v_mov_b64 v[154:155], -1\n\t" \
  "s_cmp_le_u32 s44, 12\n\t" \
  ".p2align 6\n\t" \
  "20:\n\t" \
  "s_waitcnt vmcnt(28) lgkmcnt(0)\n\t" \
  "v_mfma_f32_16x16x32_bf16 v[128:131], a[0:3], v[0:3], 0\n\t" \
  "s_cbranch_scc1 30f\n\t" \
  "40:\n\t" \
  "s_barrier\n\t" \
  "v_cvt_pk_bf16_f32 v148, v136, v137\n\t" \
  "v_cvt_pk_bf16_f32 v149, v138, v139\n\t" \
  "v_mfma_f32_16x16x32_bf16 v[132:135], a[4:7], v[0:3], 0\n\t" \
  "v_cvt_pk_bf16_f32 v150, v140, v141\n\t" \
  "v_cvt_pk_bf16_f32 v151, v142, v143\n\t" \
  "v_mfma_f32_16x16x32_bf16 v[136:139], a[0:3], v[4:7], 0\n\t" \
  "ds_read_b128 v[64:67], v164 offset:16384\n\t" \
  "v_mfma_f32_16x16x32_bf16 v[140:143], a[4:7], v[4:7], 0\n\t" \
  "ds_read_b128 v[68:71], v164 offset:24576\n\t" \
  "v_mfma_f32_16x16x32_bf16 v[128:131], a[8:11], v[8:11], v[128:131]\n\t" \
  "s_add_i32 m0, s41, 0\n\t" \
  "v_mfma_f32_16x16x32_bf16 v[132:135], a[12:15], v[8:11], v[132:135]\n\t" \
  "buffer_load_dwordx4 v160, %[rx], s40 offen lds\n\t" \
  "v_mfma_f32_16x16x32_bf16 v[136:139], a[8:11], v[12:15], v[136:139]\n\t" \
  "ds_read_b128 v[72:75], v165 offset:16384\n\t" \
  "v_mfma_f32_16x16x32_bf16 v[140:143], a[12:15], v[12:15], v[140:143]\n\t" \
  "ds_read_b128 v[76:79], v165 offset:24576\n\t" \
  "v_mfma_f32_16x16x32_bf16 v[128:131], a[16:19], v[16:19], v[128:131]\n\t" \
  "ds_read_b128 v[80:83], v166 offset:16384\n\t" \
  "v_mfma_f32_16x16x32_bf16 v[132:135], a[20:23], v[16:19], v[132:135]\n\t" \
  "buffer_store_dwordx4 v[144:147], v168, %[ru], s42 offen sc1\n\t" \
  "v_mfma_f32_16x16x32_bf16 v[136:139], a[16:19], v[20:23], v[136:139]\n\t" \
  "ds_read_b128 v[84:87], v166 offset:24576\n\t" \
  "v_mfma_f32_16x16x32_bf16 v[140:143], a[20:23], v[20:23], v[140:143]\n\t" \
  "ds_read_b128 v[88:91], v167 offset:16384\n\t" \
  "v_mfma_f32_16x16x32_bf16 v[128:131], a[24:27], v[24:27], v[128:131]\n\t" \
  "s_add_i32 m0, s41, 1024\n\t" \
  "v_mfma_f32_16x16x32_bf16 v[132:135], a[28:31], v[24:27], v[132:135]\n\t" \
  "buffer_load_dwordx4 v161, %[rx], s40 offen lds\n\t" \
  "v_mfma_f32_16x16x32_bf16 v[136:139], a[24:27], v[28:31], v[136:139]\n\t" \
  "ds_read_b128 v[92:95], v167 offset:24576\n\t" \
  "v_mfma_f32_16x16x32_bf16 v[140:143], a[28:31], v[28:31], v[140:143]\n\t" \
  "ds_read_b128 v[96:99], v164 offset:16640\n\t" \
  "v_mfma_f32_16x16x32_bf16 v[128:131], a[32:35], v[32:35], v[128:131]\n\t" \
  "ds_read_b128 v[100:103], v164 offset:24832\n\t" \
  "v_mfma_f32_16x16x32_bf16 v[132:135], a[36:39], v[32:35], v[132:135]\n\t" \
  "buffer_store_dwordx4 v[148:151], v169, %[ru], s42 offen sc1\n\t" \
  "v_mfma_f32_16x16x32_bf16 v[136:139], a[32:35], v[36:39], v[136:139]\n\t" \
  "ds_read_b128 v[104:107], v165 offset:16640\n\t" \
  "v_mfma_f32_16x16x32_bf16 v[140:143], a[36:39], v[36:39], v[140:143]\n\t" \
  "ds_read_b128 v[108:111], v165 offset:24832\n\t" \
  "v_mfma_f32_16x16x32_bf16 v[128:131], a[40:43], v[40:43], v[128:131]\n\t" \
  "s_add_i32 m0, s41, 2048\n\t" \
  "v_mfma_f32_16x16x32_bf16 v[132:135], a[44:47], v[40:43], v[132:135]\n\t" \
  "buffer_load_dwordx4 v162, %[rx], s40 offen lds\n\t" \
  "v_mfma_f32_16x16x32_bf16 v[136:139], a[40:43], v[44:47], v[136:139]\n\t" \
  "ds_read_b128 v[112:115], v166 offset:16640\n\t" \
  "v_mfma_f32_16x16x32_bf16 v[140:143], a[44:47], v[44:47], v[140:143]\n\t" \
  "ds_read_b128 v[116:119], v166 offset:24832\n\t" \
  "v_mfma_f32_16x16x32_bf16 v[128:131], a[48:51], v[48:51], v[128:131]\n\t" \
  "s_add_i32 m0, s41, 3072\n\t" \
  "v_mfma_f32_16x16x32_bf16 v[132:135], a[52:55], v[48:51], v[132:135]\n\t" \
  "buffer_load_dwordx4 v163, %[rx], s40 offen lds\n\t" \
  "v_mfma_f32_16x16x32_bf16 v[136:139], a[48:51], v[52:55], v[136:139]\n\t" \
  "ds_read_b128 v[120:123], v167 offset:16640\n\t" \
  "v_mfma_f32_16x16x32_bf16 v[140:143], a[52:55], v[52:55], v[140:143]\n\t" \
  "ds_read_b128 v[124:127], v167 offset:24832\n\t" \
  "v_mfma_f32_16x16x32_bf16 v[128:131], a[56:59], v[56:59], v[128:131]\n\t" \
  "v_mfma_f32_16x16x32_bf16 v[132:135], a[60:63], v[56:59], v[132:135]\n\t" \
  "v_mfma_f32_16x16x32_bf16 v[136:139], a[56:59], v[60:63], v[136:139]\n\t" \
  "v_mfma_f32_16x16x32_bf16 v[140:143], a[60:63], v[60:63], v[140:143]\n\t" \
  "s_cmp_le_u32 s44, 12\n\t" \
  "s_waitcnt vmcnt(30) lgkmcnt(0)\n\t" \
  "v_mfma_f32_16x16x32_bf16 v[128:131], a[64:67], v[64:67], v[128:131]\n\t" \
  "s_cbranch_scc1 31f\n\t" \
  "41:\n\t" \
  "s_barrier\n\t" \
  "v_mfma_f32_16x16x32_bf16 v[132:135], a[68:71], v[64:67], v[132:135]\n\t" \
  "s_add_i32 s76, s40, 512\n\t" \
  "v_mfma_f32_16x16x32_bf16 v[136:139], a[64:67], v[68:71], v[136:139]\n\t" \
  "ds_read_b128 v[0:3], v164 offset:32768\n\t" \
  "v_mfma_f32_16x16x32_bf16 v[140:143], a[68:71], v[68:71], v[140:143]\n\t" \
  "ds_read_b128 v[4:7], v164 offset:40960\n\t" \
  "v_mfma_f32_16x16x32_bf16 v[128:131], a[72:75], v[72:75], v[128:131]\n\t" \
  "s_add_i32 m0, s41, 16384\n\t" \
  "v_mfma_f32_16x16x32_bf16 v[132:135], a[76:79], v[72:75], v[132:135]\n\t" \
  "buffer_load_dwordx4 v160, %[rx], s76 offen lds\n\t" \
  "v_mfma_f32_16x16x32_bf16 v[136:139], a[72:75], v[76:79], v[136:139]\n\t" \
  "ds_read_b128 v[8:11], v165 offset:32768\n\t" \
  "v_mfma_f32_16x16x32_bf16 v[140:143], a[76:79], v[76:79], v[140:143]\n\t" \
  "ds_read_b128 v[12:15], v165 offset:40960\n\t" \
  "v_mfma_f32_16x16x32_bf16 v[128:131], a[80:83], v[80:83], v[128:131]\n\t" \
  "ds_read_b128 v[16:19], v166 offset:32768\n\t" \
  "v_mfma_f32_16x16x32_bf16 v[132:135], a[84:87], v[80:83], v[132:135]\n\t" \
  "v_mfma_f32_16x16x32_bf16 v[136:139], a[80:83], v[84:87], v[136:139]\n\t" \
  "ds_read_b128 v[20:23], v166 offset:40960\n\t" \
  "v_mfma_f32_16x16x32_bf16 v[140:143], a[84:87], v[84:87], v[140:143]\n\t" \
  "ds_read_b128 v[24:27], v167 offset:32768\n\t" \
  "v_mfma_f32_16x16x32_bf16 v[128:131], a[88:91], v[88:91], v[128:131]\n\t" \
  "s_add_i32 m0, s41, 17408\n\t" \
  "v_mfma_f32_16x16x32_bf16 v[132:135], a[92:95], v[88:91], v[132:135]\n\t" \
  "buffer_load_dwordx4 v161, %[rx], s76 offen lds\n\t" \
  "v_mfma_f32_16x16x32_bf16 v[136:139], a[88:91], v[92:95], v[136:139]\n\t" \
  "ds_read_b128 v[28:31], v167 offset:40960\n\t" \
  "v_mfma_f32_16x16x32_bf16 v[140:143], a[92:95], v[92:95], v[140:143]\n\t" \
  "ds_read_b128 v[32:35], v164 offset:33024\n\t" \
  "v_mfma_f32_16x16x32_bf16 v[128:131], a[96:99], v[96:99], v[128:131]\n\t" \
  "ds_read_b128 v[36:39], v164 offset:41216\n\t" \
  "v_mfma_f32_16x16x32_bf16 v[132:135], a[100:103], v[96:99], v[132:135]\n\t" \
  "v_mfma_f32_16x16x32_bf16 v[136:139], a[96:99], v[100:103], v[136:139]\n\t" \
  "ds_read_b128 v[40:43], v165 offset:33024\n\t" \
  "v_mfma_f32_16x16x32_bf16 v[140:143], a[100:103], v[100:103], v[140:143]\n\t" \
  "ds_read_b128 v[44:47], v165 offset:41216\n\t" \
  "v_mfma_f32_16x16x32_bf16 v[128:131], a[104:107], v[104:107], v[128:131]\n\t" \
  "s_add_i32 m0, s41, 18432\n\t" \
  "v_mfma_f32_16x16x32_bf16 v[132:135], a[108:111], v[104:107], v[132:135]\n\t" \
  "buffer_load_dwordx4 v162, %[rx], s76 offen lds\n\t" \
  "v_mfma_f32_16x16x32_bf16 v[136:139], a[104:107], v[108:111], v[136:139]\n\t" \
  "ds_read_b128 v[48:51], v166 offset:33024\n\t" \
  "v_mfma_f32_16x16x32_bf16 v[140:143], a[108:111], v[108:111], v[140:143]\n\t" \
  "ds_read_b128 v[52:55], v166 offset:41216\n\t" \
  "v_mfma_f32_16x16x32_bf16 v[128:131], a[112:115], v[112:115], v[128:131]\n\t" \
  "s_add_i32 m0, s41, 19456\n\t" \
  "v_mfma_f32_16x16x32_bf16 v[132:135], a[116:119], v[112:115], v[132:135]\n\t" \
  "buffer_load_dwordx4 v163, %[rx], s76 offen lds\n\t" \
  "v_mfma_f32_16x16x32_bf16 v[136:139], a[112:115], v[116:119], v[136:139]\n\t" \
  "ds_read_b128 v[56:59], v167 offset:33024\n\t" \
  "v_mfma_f32_16x16x32_bf16 v[140:143], a[116:119], v[116:119], v[140:143]\n\t" \
  "ds_read_b128 v[60:63], v167 offset:41216\n\t" \
  "v_mfma_f32_16x16x32_bf16 v[128:131], a[120:123], v[120:123], v[128:131]\n\t" \
  "s_and_b32 s79, s44, 4\n\t" \
  "v_mfma_f32_16x16x32_bf16 v[132:135], a[124:127], v[120:123], v[132:135]\n\t" \
  "s_lshl_b32 s79, s79, 11\n\t" \
  "v_mfma_f32_16x16x32_bf16 v[136:139], a[120:123], v[124:127], v[136:139]\n\t" \
  "s_cmp_lt_u32 s44, 4\n\t" \
  "s_cselect_b32 s80, 0x2000, 0\n\t" \
  "v_mfma_f32_16x16x32_bf16 v[140:143], a[124:127], v[124:127], v[140:143]\n\t" \
  "s_cmp_le_u32 s44, 12\n\t" \
  "s_waitcnt vmcnt(29) lgkmcnt(0)\n\t" \
  "v_mfma_f32_16x16x32_bf16 v[128:131], a[128:131], v[0:3], v[128:131]\n\t" \
  "s_cbranch_scc1 32f\n\t" \
  "42:\n\t" \
  "s_barrier\n\t" \
  "v_mfma_f32_16x16x32_bf16 v[132:135], a[132:135], v[0:3], v[132:135]\n\t" \
  "s_add_i32 s76, s40, 1024\n\t" \
  "v_mfma_f32_16x16x32_bf16 v[136:139], a[128:131], v[4:7], v[136:139]\n\t" \
  "ds_read_b128 v[64:67], v164 offset:49152\n\t" \
  "v_mfma_f32_16x16x32_bf16 v[140:143], a[132:135], v[4:7], v[140:143]\n\t" \
  "ds_read_b128 v[68:71], v164 offset:57344\n\t" \
  "v_mfma_f32_16x16x32_bf16 v[128:131], a[136:139], v[8:11], v[128:131]\n\t" \
  "s_add_i32 m0, s41, 32768\n\t" \
  "v_mfma_f32_16x16x32_bf16 v[132:135], a[140:143], v[8:11], v[132:135]\n\t" \
  "buffer_load_dwordx4 v160, %[rx], s76 offen lds\n\t" \
  "v_mfma_f32_16x16x32_bf16 v[136:139], a[136:139], v[12:15], v[136:139]\n\t" \
  "ds_read_b128 v[72:75], v165 offset:49152\n\t" \
  "v_mfma_f32_16x16x32_bf16 v[140:143], a[140:143], v[12:15], v[140:143]\n\t" \
  "ds_read_b128 v[76:79], v165 offset:57344\n\t" \
  "v_mfma_f32_16x16x32_bf16 v[128:131], a[144:147], v[16:19], v[128:131]\n\t" \
  "ds_read_b128 v[80:83], v166 offset:49152\n\t" \
  "v_mfma_f32_16x16x32_bf16 v[132:135], a[148:151], v[16:19], v[132:135]\n\t" \
  "v_mfma_f32_16x16x32_bf16 v[136:139], a[144:147], v[20:23], v[136:139]\n\t" \
  "ds_read_b128 v[84:87], v166 offset:57344\n\t" \
  "v_mfma_f32_16x16x32_bf16 v[140:143], a[148:151], v[20:23], v[140:143]\n\t" \
  "ds_read_b128 v[88:91], v167 offset:49152\n\t" \
  "v_mfma_f32_16x16x32_bf16 v[128:131], a[152:155], v[24:27], v[128:131]\n\t" \
  "s_add_i32 m0, s41, 33792\n\t" \
  "v_mfma_f32_16x16x32_bf16 v[132:135], a[156:159], v[24:27], v[132:135]\n\t" \
  "buffer_load_dwordx4 v161, %[rx], s76 offen lds\n\t" \
  "v_mfma_f32_16x16x32_bf16 v[136:139], a[152:155], v[28:31], v[136:139]\n\t" \
  "ds_read_b128 v[92:95], v167 offset:57344\n\t" \
  "v_mfma_f32_16x16x32_bf16 v[140:143], a[156:159], v[28:31], v[140:143]\n\t" \
  "ds_read_b128 v[96:99], v164 offset:49408\n\t" \
  "v_mfma_f32_16x16x32_bf16 v[128:131], a[160:163], v[32:35], v[128:131]\n\t" \
  "ds_read_b128 v[100:103], v164 offset:57600\n\t" \
  "v_mfma_f32_16x16x32_bf16 v[132:135], a[164:167], v[32:35], v[132:135]\n\t" \
  "v_mfma_f32_16x16x32_bf16 v[136:139], a[160:163], v[36:39], v[136:139]\n\t" \
  "ds_read_b128 v[104:107], v165 offset:49408\n\t" \
  "v_mfma_f32_16x16x32_bf16 v[140:143], a[164:167], v[36:39], v[140:143]\n\t" \
  "ds_read_b128 v[108:111], v165 offset:57600\n\t" \
  "v_mfma_f32_16x16x32_bf16 v[128:131], a[168:171], v[40:43], v[128:131]\n\t" \
  "s_add_i32 m0, s41, 34816\n\t" \
  "v_mfma_f32_16x16x32_bf16 v[132:135], a[172:175], v[40:43], v[132:135]\n\t" \
  "buffer_load_dwordx4 v162, %[rx], s76 offen lds\n\t" \
  "v_mfma_f32_16x16x32_bf16 v[136:139], a[168:171], v[44:47], v[136:139]\n\t" \
  "ds_read_b128 v[112:115], v166 offset:49408\n\t" \
  "v_mfma_f32_16x16x32_bf16 v[140:143], a[172:175], v[44:47], v[140:143]\n\t" \
  "ds_read_b128 v[116:119], v166 offset:57600\n\t" \
  "v_mfma_f32_16x16x32_bf16 v[128:131], a[176:179], v[48:51], v[128:131]\n\t" \
  "s_add_i32 m0, s41, 35840\n\t" \
  "v_mfma_f32_16x16x32_bf16 v[132:135], a[180:183], v[48:51], v[132:135]\n\t" \
  "buffer_load_dwordx4 v163, %[rx], s76 offen lds\n\t" \
  "v_mfma_f32_16x16x32_bf16 v[136:139], a[176:179], v[52:55], v[136:139]\n\t" \
  "ds_read_b128 v[120:123], v167 offset:49408\n\t" \
  "v_mfma_f32_16x16x32_bf16 v[140:143], a[180:183], v[52:55], v[140:143]\n\t" \
  "ds_read_b128 v[124:127], v167 offset:57600\n\t" \
  "v_mfma_f32_16x16x32_bf16 v[128:131], a[184:187], v[56:59], v[128:131]\n\t" \
  "s_add_i32 s78, s44, -2\n\t" \
  "v_mfma_f32_16x16x32_bf16 v[132:135], a[188:191], v[56:59], v[132:135]\n\t" \
  "s_max_i32 s78, s78, s46\n\t" \
  "v_mfma_f32_16x16x32_bf16 v[136:139], a[184:187], v[60:63], v[136:139]\n\t" \
  "v_mov_b32 v156, s78\n\t" \
  "v_mfma_f32_16x16x32_bf16 v[140:143], a[188:191], v[60:63], v[140:143]\n\t" \
  "s_cmp_le_u32 s44, 12\n\t" \
  "s_waitcnt vmcnt(27) lgkmcnt(0)\n\t" \
  "v_mfma_f32_16x16x32_bf16 v[128:131], a[192:195], v[64:67], v[128:131]\n\t" \
  "s_cbranch_scc1 33f\n\t" \
  "43:\n\t" \
  "s_barrier\n\t" \
  "v_xor_b32 v164, 0x10000, v164\n\t" \
  "v_mfma_f32_16x16x32_bf16 v[132:135], a[196:199], v[64:67], v[132:135]\n\t" \
  "s_add_i32 s76, s40, 1536\n\t" \
  "buffer_store_dword v156, v171, %[ru], 0 offen sc1\n\t" \
  "v_mfma_f32_16x16x32_bf16 v[136:139], a[192:195], v[68:71], v[136:139]\n\t" \
  "v_xor_b32 v165, 0x10000, v165\n\t" \
  "ds_read_b128 v[0:3], v164 offset:0\n\t" \
  "v_mfma_f32_16x16x32_bf16 v[140:143], a[196:199], v[68:71], v[140:143]\n\t" \
  "v_xor_b32 v166, 0x10000, v166\n\t" \
  "ds_read_b128 v[4:7], v164 offset:8192\n\t" \
  "v_mfma_f32_16x16x32_bf16 v[128:131], a[200:203], v[72:75], v[128:131]\n\t" \
  "s_add_i32 m0, s41, 49152\n\t" \
  "v_xor_b32 v167, 0x10000, v167\n\t" \
  "v_mfma_f32_16x16x32_bf16 v[132:135], a[204:207], v[72:75], v[132:135]\n\t" \
  "buffer_load_dwordx4 v160, %[rx], s76 offen lds\n\t" \
  "v_mfma_f32_16x16x32_bf16 v[136:139], a[200:203], v[76:79], v[136:139]\n\t" \
  "ds_read_b128 v[8:11], v165 offset:0\n\t" \
  "v_mfma_f32_16x16x32_bf16 v[140:143], a[204:207], v[76:79], v[140:143]\n\t" \
  "ds_read_b128 v[12:15], v165 offset:8192\n\t" \
  "v_mfma_f32_16x16x32_bf16 v[128:131], a[208:211], v[80:83], v[128:131]\n\t" \
  "ds_read_b128 v[16:19], v166 offset:0\n\t" \
  "v_mfma_f32_16x16x32_bf16 v[132:135], a[212:215], v[80:83], v[132:135]\n\t" \
  "v_mfma_f32_16x16x32_bf16 v[136:139], a[208:211], v[84:87], v[136:139]\n\t" \
  "ds_read_b128 v[20:23], v166 offset:8192\n\t" \
  "v_mfma_f32_16x16x32_bf16 v[140:143], a[212:215], v[84:87], v[140:143]\n\t" \
  "ds_read_b128 v[24:27], v167 offset:0\n\t" \
  "v_mfma_f32_16x16x32_bf16 v[128:131], a[216:219], v[88:91], v[128:131]\n\t" \
  "s_add_i32 m0, s41, 50176\n\t" \
  "ds_read_b128 v[28:31], v167 offset:8192\n\t" \
  "v_mfma_f32_16x16x32_bf16 v[132:135], a[220:223], v[88:91], v[132:135]\n\t" \
  "buffer_load_dwordx4 v161, %[rx], s76 offen lds\n\t" \
  "v_mfma_f32_16x16x32_bf16 v[136:139], a[216:219], v[92:95], v[136:139]\n\t" \
  "ds_read_b128 v[32:35], v164 offset:256\n\t" \
  "v_mfma_f32_16x16x32_bf16 v[140:143], a[220:223], v[92:95], v[140:143]\n\t" \
  "ds_read_b128 v[36:39], v164 offset:8448\n\t" \
  "v_mfma_f32_16x16x32_bf16 v[128:131], a[224:227], v[96:99], v[128:131]\n\t" \
  "ds_read_b128 v[40:43], v165 offset:256\n\t" \
  "v_mfma_f32_16x16x32_bf16 v[132:135], a[228:231], v[96:99], v[132:135]\n\t" \
  "v_mfma_f32_16x16x32_bf16 v[136:139], a[224:227], v[100:103], v[136:139]\n\t" \
  "ds_read_b128 v[44:47], v165 offset:8448\n\t" \
  "v_mfma_f32_16x16x32_bf16 v[140:143], a[228:231], v[100:103], v[140:143]\n\t" \
  "ds_read_b128 v[48:51], v166 offset:256\n\t" \
  "v_mfma_f32_16x16x32_bf16 v[128:131], a[232:235], v[104:107], v[128:131]\n\t" \
  "s_add_i32 m0, s41, 51200\n\t" \
  "v_mfma_f32_16x16x32_bf16 v[132:135], a[236:239], v[104:107], v[132:135]\n\t" \
  "buffer_load_dwordx4 v162, %[rx], s76 offen lds\n\t" \
  "v_mfma_f32_16x16x32_bf16 v[136:139], a[232:235], v[108:111], v[136:139]\n\t" \
  "ds_read_b128 v[52:55], v166 offset:8448\n\t" \
  "v_mfma_f32_16x16x32_bf16 v[140:143], a[236:239], v[108:111], v[140:143]\n\t" \
  "ds_read_b128 v[56:59], v167 offset:256\n\t" \
  "v_mfma_f32_16x16x32_bf16 v[128:131], a[240:243], v[112:115], v[128:131]\n\t" \
  "s_add_i32 m0, s41, 52224\n\t" \
  "v_mfma_f32_16x16x32_bf16 v[132:135], a[244:247], v[112:115], v[132:135]\n\t" \
  "buffer_load_dwordx4 v163, %[rx], s76 offen lds\n\t" \
  "v_mfma_f32_16x16x32_bf16 v[136:139], a[240:243], v[116:119], v[136:139]\n\t" \
  "ds_read_b128 v[60:63], v167 offset:8448\n\t" \
  "v_mfma_f32_16x16x32_bf16 v[140:143], a[244:247], v[116:119], v[140:143]\n\t" \
  "s_add_i32 s44, s44, 1\n\t" \
  "s_xor_b32 s42, s43, s79\n\t" \
  "v_mfma_f32_16x16x32_bf16 v[128:131], a[248:251], v[120:123], v[128:131]\n\t" \
  "s_add_i32 s43, s43, 0x80000\n\t" \
  "s_add_i32 s40, s40, 0x10000\n\t" \
  "v_mfma_f32_16x16x32_bf16 v[132:135], a[252:255], v[120:123], v[132:135]\n\t" \
  "s_xor_b32 s41, s41, 0x10000\n\t" \
  "s_andn2_b32 s42, s42, s80\n\t" \
  "v_mfma_f32_16x16x32_bf16 v[136:139], a[248:251], v[124:127], v[136:139]\n\t" \
  "v_cvt_pk_bf16_f32 v144, v128, v129\n\t" \
  "v_cvt_pk_bf16_f32 v145, v130, v131\n\t" \
  "v_mfma_f32_16x16x32_bf16 v[140:143], a[252:255], v[124:127], v[140:143]\n\t" \
  "v_cvt_pk_bf16_f32 v146, v132, v133\n\t" \
  "v_cvt_pk_bf16_f32 v147, v134, v135\n\t" \
  "s_cmp_eq_u32 s44, 64\n\t" \
  "s_cbranch_scc1 98f\n\t" \
  "s_cmp_le_u32 s44, 12\n\t" \
  "s_branch 20b\n\t" \
  "30:\n\t" \
  "s_waitcnt vmcnt(24)\n\t" \
  "s_branch 40b\n\t" \
  "31:\n\t" \
  "s_waitcnt vmcnt(24)\n\t" \
  "s_branch 41b\n\t" \
  "32:\n\t" \
  "s_waitcnt vmcnt(6)\n\t" \
  "s_barrier\n\t" \
  "s_add_i32 s46, s44, -1\n\t" \
  "v_mov_b32 v156, s46\n\t" \
  "buffer_store_dword v156, v171, %[ru], 0 offen sc1\n\t" \
  "s_branch 42b\n\t" \
  "33:\n\t" \
  "s_waitcnt vmcnt(24)\n\t" \
  "s_branch 43b\n\t" \
  "93:\n\t" \
  "ds_read_b128 v[0:3], v164 offset:0\n\t" \
  "ds_read_b128 v[4:7], v164 offset:8192\n\t" \
  "ds_read_b128 v[8:11], v165 offset:0\n\t" \
  "ds_read_b128 v[12:15], v165 offset:8192\n\t" \
  "ds_read_b128 v[16:19], v166 offset:0\n\t" \
  "ds_read_b128 v[20:23], v166 offset:8192\n\t" \
  "ds_read_b128 v[24:27], v167 offset:0\n\t" \
  "ds_read_b128 v[28:31], v167 offset:8192\n\t" \
  "ds_read_b128 v[32:35], v164 offset:256\n\t" \
  "ds_read_b128 v[36:39], v164 offset:8448\n\t" \
  "ds_read_b128 v[40:43], v165 offset:256\n\t" \
  "ds_read_b128 v[44:47], v165 offset:8448\n\t" \
  "ds_read_b128 v[48:51], v166 offset:256\n\t" \
  "ds_read_b128 v[52:55], v166 offset:8448\n\t" \
  "ds_read_b128 v[56:59], v167 offset:256\n\t" \
  "ds_read_b128 v[60:63], v167 offset:8448\n\t" \
  "v_mov_b64 v[152:153], -1\n\t" \
  "v_mov_b64 v[154:155], -1\n\t" \
  "s_cmp_le_u32 s44, 12\n\t" \
  ".p2align 6\n\t" \
  "20:\n\t" \
  "s_waitcnt vmcnt(28) lgkmcnt(0)\n\t" \
  "v_mfma_f32_16x16x32_bf16 v[128:131], a[0:3], v[0:3], 0\n\t" \
  "s_cbranch_scc1 30f\n\t" \
  "40:\n\t" \
  "s_barrier\n\t" \
  "v_cvt_pk_bf16_f32 v148, v136, v137\n\t" \
  "v_cvt_pk_bf16_f32 v149, v138, v139\n\t" \
  "v_mfma_f32_16x16x32_bf16 v[132:135], a[4:7], v[0:3], 0\n\t" \
  "v_cvt_pk_bf16_f32 v150, v140, v141\n\t" \
  "v_cvt_pk_bf16_f32 v151, v142, v143\n\t" \
  "v_mfma_f32_16x16x32_bf16 v[136:139], a[0:3], v[4:7], 0\n\t" \
  "ds_read_b128 v[64:67], v164 offset:16384\n\t" \
  "v_mfma_f32_16x16x32_bf16 v[140:143], a[4:7], v[4:7], 0\n\t" \
  "ds_read_b128 v[68:71], v164 offset:24576\n\t" \
  "v_mfma_f32_16x16x32_bf16 v[128:131], a[8:11], v[8:11], v[128:131]\n\t" \
  "ds_read_b128 v[72:75], v165 offset:16384\n\t" \
  "v_mfma_f32_16x16x32_bf16 v[132:135], a[12:15], v[8:11], v[132:135]\n\t" \
  "s_add_i32 m0, s41, 0\n\t" \
  "v_mfma_f32_16x16x32_bf16 v[136:139], a[8:11], v[12:15], v[136:139]\n\t" \
  "buffer_load_dwordx4 v160, %[rx], s40 offen lds\n\t" \
  "v_mfma_f32_16x16x32_bf16 v[140:143], a[12:15], v[12:15], v[140:143]\n\t" \
  "ds_read_b128 v[76:79], v165 offset:24576\n\t" \
  "v_mfma_f32_16x16x32_bf16 v[128:131], a[16:19], v[16:19], v[128:131]\n\t" \
  "ds_read_b128 v[80:83], v166 offset:16384\n\t" \
  "v_mfma_f32_16x16x32_bf16 v[132:135], a[20:23], v[16:19], v[132:135]\n\t" \
  "ds_read_b128 v[84:87], v166 offset:24576\n\t" \
  "v_mfma_f32_16x16x32_bf16 v[136:139], a[16:19], v[20:23], v[136:139]\n\t" \
  "buffer_store_dwordx4 v[144:147], v168, %[ru], s42 offen sc1\n\t" \
  "v_mfma_f32_16x16x32_bf16 v[140:143], a[20:23], v[20:23], v[140:143]\n\t" \
  "ds_read_b128 v[88:91], v167 offset:16384\n\t" \
  "v_mfma_f32_16x16x32_bf16 v[128:131], a[24:27], v[24:27], v[128:131]\n\t" \
  "ds_read_b128 v[92:95], v167 offset:24576\n\t" \
  "v_mfma_f32_16x16x32_bf16 v[132:135], a[28:31], v[24:27], v[132:135]\n\t" \
  "s_add_i32 m0, s41, 1024\n\t" \
  "v_mfma_f32_16x16x32_bf16 v[136:139], a[24:27], v[28:31], v[136:139]\n\t" \
  "buffer_load_dwordx4 v161, %[rx], s40 offen lds\n\t" \
  "v_mfma_f32_16x16x32_bf16 v[140:143], a[28:31], v[28:31], v[140:143]\n\t" \
  "ds_read_b128 v[96:99], v164 offset:16640\n\t" \
  "v_mfma_f32_16x16x32_bf16 v[128:131], a[32:35], v[32:35], v[128:131]\n\t" \
  "ds_read_b128 v[100:103], v164 offset:24832\n\t" \
  "v_mfma_f32_16x16x32_bf16 v[132:135], a[36:39], v[32:35], v[132:135]\n\t" \
  "ds_read_b128 v[104:107], v165 offset:16640\n\t" \
  "v_mfma_f32_16x16x32_bf16 v[136:139], a[32:35], v[36:39], v[136:139]\n\t" \
  "buffer_store_dwordx4 v[148:151], v169, %[ru], s42 offen sc1\n\t" \
  "v_mfma_f32_16x16x32_bf16 v[140:143], a[36:39], v[36:39], v[140:143]\n\t" \
  "ds_read_b128 v[108:111], v165 offset:24832\n\t" \
  "v_mfma_f32_16x16x32_bf16 v[128:131], a[40:43], v[40:43], v[128:131]\n\t" \
  "ds_read_b128 v[112:115], v166 offset:16640\n\t" \
  "v_mfma_f32_16x16x32_bf16 v[132:135], a[44:47], v[40:43], v[132:135]\n\t" \
  "s_add_i32 m0, s41, 2048\n\t" \
  "v_mfma_f32_16x16x32_bf16 v[136:139], a[40:43], v[44:47], v[136:139]\n\t" \
  "buffer_load_dwordx4 v162, %[rx], s40 offen lds\n\t" \
  "v_mfma_f32_16x16x32_bf16 v[140:143], a[44:47], v[44:47], v[140:143]\n\t" \
  "ds_read_b128 v[116:119], v166 offset:24832\n\t" \
  "v_mfma_f32_16x16x32_bf16 v[128:131], a[48:51], v[48:51], v[128:131]\n\t" \
  "ds_read_b128 v[120:123], v167 offset:16640\n\t" \
  "v_mfma_f32_16x16x32_bf16 v[132:135], a[52:55], v[48:51], v[132:135]\n\t" \
  "s_add_i32 m0, s41, 3072\n\t" \
  "v_mfma_f32_16x16x32_bf16 v[136:139], a[48:51], v[52:55], v[136:139]\n\t" \
  "buffer_load_dwordx4 v163, %[rx], s40 offen lds\n\t" \
  "v_mfma_f32_16x16x32_bf16 v[140:143], a[52:55], v[52:55], v[140:143]\n\t" \
  "ds_read_b128 v[124:127], v167 offset:24832\n\t" \
  "v_mfma_f32_16x16x32_bf16 v[128:131], a[56:59], v[56:59], v[128:131]\n\t" \
  "v_mfma_f32_16x16x32_bf16 v[132:135], a[60:63], v[56:59], v[132:135]\n\t" \
  "v_mfma_f32_16x16x32_bf16 v[136:139], a[56:59], v[60:63], v[136:139]\n\t" \
  "v_mfma_f32_16x16x32_bf16 v[140:143], a[60:63], v[60:63], v[140:143]\n\t" \
  "s_cmp_le_u32 s44, 12\n\t" \
  "s_waitcnt vmcnt(30) lgkmcnt(0)\n\t" \
  "v_mfma_f32_16x16x32_bf16 v[128:131], a[64:67], v[64:67], v[128:131]\n\t" \
  "s_cbranch_scc1 31f\n\t" \
  "41:\n\t" \
  "s_barrier\n\t" \
  "v_mfma_f32_16x16x32_bf16 v[132:135], a[68:71], v[64:67], v[132:135]\n\t" \
  "s_add_i32 s76, s40, 512\n\t" \
  "v_mfma_f32_16x16x32_bf16 v[136:139], a[64:67], v[68:71], v[136:139]\n\t" \
  "ds_read_b128 v[0:3], v164 offset:32768\n\t" \
  "v_mfma_f32_16x16x32_bf16 v[140:143], a[68:71], v[68:71], v[140:143]\n\t" \
  "ds_read_b128 v[4:7], v164 offset:40960\n\t" \
  "v_mfma_f32_16x16x32_bf16 v[128:131], a[72:75], v[72:75], v[128:131]\n\t" \
  "ds_read_b128 v[8:11], v165 offset:32768\n\t" \
  "v_mfma_f32_16x16x32_bf16 v[132:135], a[76:79], v[72:75], v[132:135]\n\t" \
  "s_add_i32 m0, s41, 16384\n\t" \
  "v_mfma_f32_16x16x32_bf16 v[136:139], a[72:75], v[76:79], v[136:139]\n\t" \
  "buffer_load_dwordx4 v160, %[rx], s76 offen lds\n\t" \
  "v_mfma_f32_16x16x32_bf16 v[140:143], a[76:79], v[76:79], v[140:143]\n\t" \
  "ds_read_b128 v[12:15], v165 offset:40960\n\t" \
  "v_mfma_f32_16x16x32_bf16 v[128:131], a[80:83], v[80:83], v[128:131]\n\t" \
  "ds_read_b128 v[16:19], v166 offset:32768\n\t" \
  "v_mfma_f32_16x16x32_bf16 v[132:135], a[84:87], v[80:83], v[132:135]\n\t" \
  "ds_read_b128 v[20:23], v166 offset:40960\n\t" \
  "v_mfma_f32_16x16x32_bf16 v[136:139], a[80:83], v[84:87], v[136:139]\n\t" \
  "v_mfma_f32_16x16x32_bf16 v[140:143], a[84:87], v[84:87], v[140:143]\n\t" \
  "ds_read_b128 v[24:27], v167 offset:32768\n\t" \
  "v_mfma_f32_16x16x32_bf16 v[128:131], a[88:91], v[88:91], v[128:131]\n\t" \
  "ds_read_b128 v[28:31], v167 offset:40960\n\t" \
  "v_mfma_f32_16x16x32_bf16 v[132:135], a[92:95], v[88:91], v[132:135]\n\t" \
  "s_add_i32 m0, s41, 17408\n\t" \
  "v_mfma_f32_16x16x32_bf16 v[136:139], a[88:91], v[92:95], v[136:139]\n\t" \
  "buffer_load_dwordx4 v161, %[rx], s76 offen lds\n\t" \
  "v_mfma_f32_16x16x32_bf16 v[140:143], a[92:95], v[92:95], v[140:143]\n\t" \
  "ds_read_b128 v[32:35], v164 offset:33024\n\t" \
  "v_mfma_f32_16x16x32_bf16 v[128:131], a[96:99], v[96:99], v[128:131]\n\t" \
  "ds_read_b128 v[36:39], v164 offset:41216\n\t" \
  "v_mfma_f32_16x16x32_bf16 v[132:135], a[100:103], v[96:99], v[132:135]\n\t" \
  "ds_read_b128 v[40:43], v165 offset:33024\n\t" \
  "v_mfma_f32_16x16x32_bf16 v[136:139], a[96:99], v[100:103], v[136:139]\n\t" \
  "v_mfma_f32_16x16x32_bf16 v[140:143], a[100:103], v[100:103], v[140:143]\n\t" \
  "ds_read_b128 v[44:47], v165 offset:41216\n\t" \
  "v_mfma_f32_16x16x32_bf16 v[128:131], a[104:107], v[104:107], v[128:131]\n\t" \
  "ds_read_b128 v[48:51], v166 offset:33024\n\t" \
  "v_mfma_f32_16x16x32_bf16 v[132:135], a[108:111], v[104:107], v[132:135]\n\t" \
  "s_add_i32 m0, s41, 18432\n\t" \
  "v_mfma_f32_16x16x32_bf16 v[136:139], a[104:107], v[108:111], v[136:139]\n\t" \
  "buffer_load_dwordx4 v162, %[rx], s76 offen lds\n\t" \
  "v_mfma_f32_16x16x32_bf16 v[140:143], a[108:111], v[108:111], v[140:143]\n\t" \
  "ds_read_b128 v[52:55], v166 offset:41216\n\t" \
  "v_mfma_f32_16x16x32_bf16 v[128:131], a[112:115], v[112:115], v[128:131]\n\t" \
  "ds_read_b128 v[56:59], v167 offset:33024\n\t" \
  "v_mfma_f32_16x16x32_bf16 v[132:135], a[116:119], v[112:115], v[132:135]\n\t" \
  "s_add_i32 m0, s41, 19456\n\t" \
  "v_mfma_f32_16x16x32_bf16 v[136:139], a[112:115], v[116:119], v[136:139]\n\t" \
  "buffer_load_dwordx4 v163, %[rx], s76 offen lds\n\t" \
  "v_mfma_f32_16x16x32_bf16 v[140:143], a[116:119], v[116:119], v[140:143]\n\t" \
  "ds_read_b128 v[60:63], v167 offset:41216\n\t" \
  "v_mfma_f32_16x16x32_bf16 v[128:131], a[120:123], v[120:123], v[128:131]\n\t" \
  "s_and_b32 s79, s44, 4\n\t" \
  "v_mfma_f32_16x16x32_bf16 v[132:135], a[124:127], v[120:123], v[132:135]\n\t" \
  "s_lshl_b32 s79, s79, 11\n\t" \
  "v_mfma_f32_16x16x32_bf16 v[136:139], a[120:123], v[124:127], v[136:139]\n\t" \
  "s_cmp_lt_u32 s44, 4\n\t" \
  "s_cselect_b32 s80, 0x2000, 0\n\t" \
  "v_mfma_f32_16x16x32_bf16 v[140:143], a[124:127], v[124:127], v[140:143]\n\t" \
  "s_cmp_le_u32 s44, 12\n\t" \
  "s_waitcnt vmcnt(29) lgkmcnt(0)\n\t" \
  "v_mfma_f32_16x16x32_bf16 v[128:131], a[128:131], v[0:3], v[128:131]\n\t" \
  "s_cbranch_scc1 32f\n\t" \
  "42:\n\t" \
  "s_barrier\n\t" \
  "v_mfma_f32_16x16x32_bf16 v[132:135], a[132:135], v[0:3], v[132:135]\n\t" \
  "s_add_i32 s76, s40, 1024\n\t" \
  "v_mfma_f32_16x16x32_bf16 v[136:139], a[128:131], v[4:7], v[136:139]\n\t" \
  "ds_read_b128 v[64:67], v164 offset:49152\n\t" \
  "v_mfma_f32_16x16x32_bf16 v[140:143], a[132:135], v[4:7], v[140:143]\n\t" \
  "ds_read_b128 v[68:71], v164 offset:57344\n\t" \
  "v_mfma_f32_16x16x32_bf16 v[128:131], a[136:139], v[8:11], v[128:131]\n\t" \
  "ds_read_b128 v[72:75], v165 offset:49152\n\t" \
  "v_mfma_f32_16x16x32_bf16 v[132:135], a[140:143], v[8:11], v[132:135]\n\t" \
  "s_add_i32 m0, s41, 32768\n\t" \
  "v_mfma_f32_16x16x32_bf16 v[136:139], a[136:139], v[12:15], v[136:139]\n\t" \
  "buffer_load_dwordx4 v160, %[rx], s76 offen lds\n\t" \
  "v_mfma_f32_16x16x32_bf16 v[140:143], a[140:143], v[12:15], v[140:143]\n\t" \
  "ds_read_b128 v[76:79], v165 offset:57344\n\t" \
  "v_mfma_f32_16x16x32_bf16 v[128:131], a[144:147], v[16:19], v[128:131]\n\t" \
  "ds_read_b128 v[80:83], v166 offset:49152\n\t" \
  "v_mfma_f32_16x16x32_bf16 v[132:135], a[148:151], v[16:19], v[132:135]\n\t" \
  "ds_read_b128 v[84:87], v166 offset:57344\n\t" \
  "v_mfma_f32_16x16x32_bf16 v[136:139], a[144:147], v[20:23], v[136:139]\n\t" \
  "v_mfma_f32_16x16x32_bf16 v[140:143], a[148:151], v[20:23], v[140:143]\n\t" \
  "ds_read_b128 v[88:91], v167 offset:49152\n\t" \
  "v_mfma_f32_16x16x32_bf16 v[128:131], a[152:155], v[24:27], v[128:131]\n\t" \
  "ds_read_b128 v[92:95], v167 offset:57344\n\t" \
  "v_mfma_f32_16x16x32_bf16 v[132:135], a[156:159], v[24:27], v[132:135]\n\t" \
  "s_add_i32 m0, s41, 33792\n\t" \
  "v_mfma_f32_16x16x32_bf16 v[136:139], a[152:155], v[28:31], v[136:139]\n\t" \
  "buffer_load_dwordx4 v161, %[rx], s76 offen lds\n\t" \
  "v_mfma_f32_16x16x32_bf16 v[140:143], a[156:159], v[28:31], v[140:143]\n\t" \
  "ds_read_b128 v[96:99], v164 offset:49408\n\t" \
  "v_mfma_f32_16x16x32_bf16 v[128:131], a[160:163], v[32:35], v[128:131]\n\t" \
  "ds_read_b128 v[100:103], v164 offset:57600\n\t" \
  "v_mfma_f32_16x16x32_bf16 v[132:135], a[164:167], v[32:35], v[132:135]\n\t" \
  "ds_read_b128 v[104:107], v165 offset:49408\n\t" \
  "v_mfma_f32_16x16x32_bf16 v[136:139], a[160:163], v[36:39], v[136:139]\n\t" \
  "v_mfma_f32_16x16x32_bf16 v[140:143], a[164:167], v[36:39], v[140:143]\n\t" \
  "ds_read_b128 v[108:111], v165 offset:57600\n\t" \
  "v_mfma_f32_16x16x32_bf16 v[128:131], a[168:171], v[40:43], v[128:131]\n\t" \
  "ds_read_b128 v[112:115], v166 offset:49408\n\t" \
  "v_mfma_f32_16x16x32_bf16 v[132:135], a[172:175], v[40:43], v[132:135]\n\t" \
  "s_add_i32 m0, s41, 34816\n\t" \
  "v_mfma_f32_16x16x32_bf16 v[136:139], a[168:171], v[44:47], v[136:139]\n\t" \
  "buffer_load_dwordx4 v162, %[rx], s76 offen lds\n\t" \
  "v_mfma_f32_16x16x32_bf16 v[140:143], a[172:175], v[44:47], v[140:143]\n\t" \
  "ds_read_b128 v[116:119], v166 offset:57600\n\t" \
  "v_mfma_f32_16x16x32_bf16 v[128:131], a[176:179], v[48:51], v[128:131]\n\t" \
  "ds_read_b128 v[120:123], v167 offset:49408\n\t" \
  "v_mfma_f32_16x16x32_bf16 v[132:135], a[180:183], v[48:51], v[132:135]\n\t" \
  "s_add_i32 m0, s41, 35840\n\t" \
  "v_mfma_f32_16x16x32_bf16 v[136:139], a[176:179], v[52:55], v[136:139]\n\t" \
  "buffer_load_dwordx4 v163, %[rx], s76 offen lds\n\t" \
  "v_mfma_f32_16x16x32_bf16 v[140:143], a[180:183], v[52:55], v[140:143]\n\t" \
  "ds_read_b128 v[124:127], v167 offset:57600\n\t" \
  "v_mfma_f32_16x16x32_bf16 v[128:131], a[184:187], v[56:59], v[128:131]\n\t" \
  "s_add_i32 s78, s44, -2\n\t" \
  "v_mfma_f32_16x16x32_bf16 v[132:135], a[188:191], v[56:59], v[132:135]\n\t" \
  "s_max_i32 s78, s78, s46\n\t" \
  "v_mfma_f32_16x16x32_bf16 v[136:139], a[184:187], v[60:63], v[136:139]\n\t" \
  "v_mov_b32 v156, s78\n\t" \
  "v_mfma_f32_16x16x32_bf16 v[140:143], a[188:191], v[60:63], v[140:143]\n\t" \
  "s_cmp_le_u32 s44, 12\n\t" \
  "s_waitcnt vmcnt(27) lgkmcnt(0)\n\t" \
  "v_mfma_f32_16x16x32_bf16 v[128:131], a[192:195], v[64:67], v[128:131]\n\t" \
  "s_cbranch_scc1 33f\n\t" \
  "43:\n\t" \
  "s_barrier\n\t" \
  "v_xor_b32 v164, 0x10000, v164\n\t" \
  "v_mfma_f32_16x16x32_bf16 v[132:135], a[196:199], v[64:67], v[132:135]\n\t" \
  "s_add_i32 s76, s40, 1536\n\t" \
  "buffer_store_dword v156, v171, %[ru], 0 offen sc1\n\t" \
  "v_mfma_f32_16x16x32_bf16 v[136:139], a[192:195], v[68:71], v[136:139]\n\t" \
  "v_xor_b32 v165, 0x10000, v165\n\t" \
  "ds_read_b128 v[0:3], v164 offset:0\n\t" \
  "v_mfma_f32_16x16x32_bf16 v[140:143], a[196:199], v[68:71], v[140:143]\n\t" \
  "v_xor_b32 v166, 0x10000, v166\n\t" \
  "ds_read_b128 v[4:7], v164 offset:8192\n\t" \
  "v_mfma_f32_16x16x32_bf16 v[128:131], a[200:203], v[72:75], v[128:131]\n\t" \
  "v_xor_b32 v167, 0x10000, v167\n\t" \
  "ds_read_b128 v[8:11], v165 offset:0\n\t" \
  "v_mfma_f32_16x16x32_bf16 v[132:135], a[204:207], v[72:75], v[132:135]\n\t" \
  "s_add_i32 m0, s41, 49152\n\t" \
  "ds_read_b128 v[12:15], v165 offset:8192\n\t" \
  "v_mfma_f32_16x16x32_bf16 v[136:139], a[200:203], v[76:79], v[136:139]\n\t" \
  "buffer_load_dwordx4 v160, %[rx], s76 offen lds\n\t" \
  "v_mfma_f32_16x16x32_bf16 v[140:143], a[204:207], v[76:79], v[140:143]\n\t" \
  "ds_read_b128 v[16:19], v166 offset:0\n\t" \
  "v_mfma_f32_16x16x32_bf16 v[128:131], a[208:211], v[80:83], v[128:131]\n\t" \
  "ds_read_b128 v[20:23], v166 offset:8192\n\t" \
  "v_mfma_f32_16x16x32_bf16 v[132:135], a[212:215], v[80:83], v[132:135]\n\t" \
  "ds_read_b128 v[24:27], v167 offset:0\n\t" \
  "v_mfma_f32_16x16x32_bf16 v[136:139], a[208:211], v[84:87], v[136:139]\n\t" \
  "v_mfma_f32_16x16x32_bf16 v[140:143], a[212:215], v[84:87], v[140:143]\n\t" \
  "ds_read_b128 v[28:31], v167 offset:8192\n\t" \
  "v_mfma_f32_16x16x32_bf16 v[128:131], a[216:219], v[88:91], v[128:131]\n\t" \
  "ds_read_b128 v[32:35], v164 offset:256\n\t" \
  "v_mfma_f32_16x16x32_bf16 v[132:135], a[220:223], v[88:91], v[132:135]\n\t" \
  "s_add_i32 m0, s41, 50176\n\t" \
  "v_mfma_f32_16x16x32_bf16 v[136:139], a[216:219], v[92:95], v[136:139]\n\t" \
  "buffer_load_dwordx4 v161, %[rx], s76 offen lds\n\t" \
  "v_mfma_f32_16x16x32_bf16 v[140:143], a[220:223], v[92:95], v[140:143]\n\t" \
  "ds_read_b128 v[36:39], v164 offset:8448\n\t" \
  "v_mfma_f32_16x16x32_bf16 v[128:131], a[224:227], v[96:99], v[128:131]\n\t" \
  "ds_read_b128 v[40:43], v165 offset:256\n\t" \
  "v_mfma_f32_16x16x32_bf16 v[132:135], a[228:231], v[96:99], v[132:135]\n\t" \
  "ds_read_b128 v[44:47], v165 offset:8448\n\t" \
  "v_mfma_f32_16x16x32_bf16 v[136:139], a[224:227], v[100:103], v[136:139]\n\t" \
  "v_mfma_f32_16x16x32_bf16 v[140:143], a[228:231], v[100:103], v[140:143]\n\t" \
  "ds_read_b128 v[48:51], v166 offset:256\n\t" \
  "v_mfma_f32_16x16x32_bf16 v[128:131], a[232:235], v[104:107], v[128:131]\n\t" \
  "ds_read_b128 v[52:55], v166 offset:8448\n\t" \
  "v_mfma_f32_16x16x32_bf16 v[132:135], a[236:239], v[104:107], v[132:135]\n\t" \
  "s_add_i32 m0, s41, 51200\n\t" \
  "v_mfma_f32_16x16x32_bf16 v[136:139], a[232:235], v[108:111], v[136:139]\n\t" \
  "buffer_load_dwordx4 v162, %[rx], s76 offen lds\n\t" \
  "v_mfma_f32_16x16x32_bf16 v[140:143], a[236:239], v[108:111], v[140:143]\n\t" \
  "ds_read_b128 v[56:59], v167 offset:256\n\t" \
  "v_mfma_f32_16x16x32_bf16 v[128:131], a[240:243], v[112:115], v[128:131]\n\t" \
  "ds_read_b128 v[60:63], v167 offset:8448\n\t" \
  "v_mfma_f32_16x16x32_bf16 v[132:135], a[244:247], v[112:115], v[132:135]\n\t" \
  "s_add_i32 m0, s41, 52224\n\t" \
  "v_mfma_f32_16x16x32_bf16 v[136:139], a[240:243], v[116:119], v[136:139]\n\t" \
  "buffer_load_dwordx4 v163, %[rx], s76 offen lds\n\t" \
  "v_mfma_f32_16x16x32_bf16 v[140:143], a[244:247], v[116:119], v[140:143]\n\t" \
  "s_add_i32 s44, s44, 1\n\t" \
  "s_xor_b32 s42, s43, s79\n\t" \
  "v_mfma_f32_16x16x32_bf16 v[128:131], a[248:251], v[120:123], v[128:131]\n\t" \
  "s_add_i32 s43, s43, 0x80000\n\t" \
  "s_add_i32 s40, s40, 0x10000\n\t" \
  "v_mfma_f32_16x16x32_bf16 v[132:135], a[252:255], v[120:123], v[132:135]\n\t" \
  "s_xor_b32 s41, s41, 0x10000\n\t" \
  "s_andn2_b32 s42, s42, s80\n\t" \
  "v_mfma_f32_16x16x32_bf16 v[136:139], a[248:251], v[124:127], v[136:139]\n\t" \
  "v_cvt_pk_bf16_f32 v144, v128, v129\n\t" \
  "v_cvt_pk_bf16_f32 v145, v130, v131\n\t" \
  "v_mfma_f32_16x16x32_bf16 v[140:143], a[252:255], v[124:127], v[140:143]\n\t" \
  "v_cvt_pk_bf16_f32 v146, v132, v133\n\t" \
  "v_cvt_pk_bf16_f32 v147, v134, v135\n\t" \
  "s_cmp_eq_u32 s44, 64\n\t" \
  "s_cbranch_scc1 98f\n\t" \
  "s_cmp_le_u32 s44, 12\n\t" \
  "s_branch 20b\n\t" \
  "30:\n\t" \
  "s_waitcnt vmcnt(24)\n\t" \
  "s_branch 40b\n\t" \
  "31:\n\t" \
  "s_waitcnt vmcnt(24)\n\t" \
  "s_branch 41b\n\t" \
  "32:\n\t" \
  "s_waitcnt vmcnt(6)\n\t" \
  "s_barrier\n\t" \
  "s_add_i32 s46, s44, -1\n\t" \
  "v_mov_b32 v156, s46\n\t" \
  "buffer_store_dword v156, v171, %[ru], 0 offen sc1\n\t" \
  "s_branch 42b\n\t" \
  "33:\n\t" \
  "s_waitcnt vmcnt(24)\n\t" \
  "s_branch 43b\n\t" \
  "98:\n\t" \
  "s_nop 7\n\t" \
  "v_cvt_pk_bf16_f32 v148, v136, v137\n\t" \
  "v_cvt_pk_bf16_f32 v149, v138, v139\n\t" \
  "v_cvt_pk_bf16_f32 v150, v140, v141\n\t" \
  "v_cvt_pk_bf16_f32 v151, v142, v143\n\t" \
  "buffer_store_dwordx4 v[144:147], v168, %[ru], s42 offen sc1\n\t" \
  "buffer_store_dwordx4 v[148:151], v169, %[ru], s42 offen sc1\n\t" \
  "v_mov_b32 v156, 63\n\t" \
  "s_waitcnt vmcnt(0) lgkmcnt(0)\n\t" \
  "s_barrier\n\t" \
  "buffer_store_dword v156, v171, %[ru], 0 offen sc1\n\t" \
  "99:\n\t" \
  ""
#define U_BODY \
  "s_cmp_lg_u32 %[wv], 0\n\t" \
  "s_cbranch_scc1 91f\n\t" \
  "90:\n\t" \
  "ds_read_b128 v[0:3], v81 offset:0\n\t" \
  "ds_read_b128 v[16:19], v81 offset:4096\n\t" \
  "ds_read_b128 v[4:7], v81 offset:1024\n\t" \
  "ds_read_b128 v[20:23], v81 offset:5120\n\t" \
  "ds_read_b128 v[8:11], v81 offset:2048\n\t" \
  "ds_read_b128 v[24:27], v81 offset:6144\n\t" \
  "ds_read_b128 v[12:15], v81 offset:3072\n\t" \
  "ds_read_b128 v[28:31], v81 offset:7168\n\t" \
  ".p2align 6\n\t" \
  "20:\n\t" \
  "s_waitcnt lgkmcnt(7)\n\t" \
  "v_mfma_f32_16x16x32_bf16 v[32:35], v[0:3], a[0:3], 0\n\t" \
  "buffer_store_dwordx4 v[52:55], v80, %[ru], s44 offen offset:1024 sc1\n\t" \
  "s_waitcnt lgkmcnt(6)\n\t" \
  "v_mfma_f32_16x16x32_bf16 v[36:39], v[16:19], a[0:3], 0\n\t" \
  "s_and_b32 s45, s40, 15\n\t" \
  "s_waitcnt lgkmcnt(5)\n\t" \
  "v_mfma_f32_16x16x32_bf16 v[32:35], v[4:7], a[4:7], v[32:35]\n\t" \
  "v_cvt_pk_bf16_f32 v57, v40, v41\n\t" \
  "s_lshl_b32 s45, s45, 22\n\t" \
  "s_waitcnt lgkmcnt(4)\n\t" \
  "v_mfma_f32_16x16x32_bf16 v[36:39], v[20:23], a[4:7], v[36:39]\n\t" \
  "v_cvt_pk_bf16_f32 v59, v42, v43\n\t" \
  "s_add_i32 s45, s45, s42\n\t" \
  "s_waitcnt lgkmcnt(3)\n\t" \
  "v_mfma_f32_16x16x32_bf16 v[32:35], v[8:11], a[8:11], v[32:35]\n\t" \
  "v_cvt_pk_bf16_f32 v61, v44, v45\n\t" \
  "s_lshr_b32 s76, s45, 21\n\t" \
  "s_waitcnt lgkmcnt(2)\n\t" \
  "v_mfma_f32_16x16x32_bf16 v[36:39], v[24:27], a[8:11], v[36:39]\n\t" \
  "v_cvt_pk_bf16_f32 v63, v46, v47\n\t" \
  "s_bitcmp0_b32 %[sel], s76\n\t" \
  "s_cselect_b32 s76, 0x2000, 0\n\t" \
  "s_waitcnt lgkmcnt(1)\n\t" \
  "v_mfma_f32_16x16x32_bf16 v[32:35], v[12:15], a[12:15], v[32:35]\n\t" \
  "s_add_i32 s45, s45, s76\n\t" \
  "s_waitcnt lgkmcnt(0)\n\t" \
  "v_mfma_f32_16x16x32_bf16 v[36:39], v[28:31], a[12:15], v[36:39]\n\t" \
  "s_add_i32 s77, s40, 1\n\t" \
  "v_mfma_f32_16x16x32_bf16 v[40:43], v[0:3], a[16:19], 0\n\t" \
  "buffer_store_dwordx4 v[56:59], v80, %[ru], s44 offen offset:2048 sc1\n\t" \
  "v_mfma_f32_16x16x32_bf16 v[44:47], v[16:19], a[16:19], 0\n\t" \
  "s_min_u32 s77, s77, 63\n\t" \
  "v_mfma_f32_16x16x32_bf16 v[40:43], v[4:7], a[20:23], v[40:43]\n\t" \
  "v_cvt_pk_bf16_f32 v48, v32, v33\n\t" \
  "s_and_b32 s76, s77, 4\n\t" \
  "v_mfma_f32_16x16x32_bf16 v[44:47], v[20:23], a[20:23], v[44:47]\n\t" \
  "v_cvt_pk_bf16_f32 v50, v34, v35\n\t" \
  "s_lshl_b32 s76, s76, 11\n\t" \
  "v_mfma_f32_16x16x32_bf16 v[40:43], v[8:11], a[24:27], v[40:43]\n\t" \
  "v_cvt_pk_bf16_f32 v52, v36, v37\n\t" \
  "s_lshl_b32 s77, s77, 19\n\t" \
  "v_mfma_f32_16x16x32_bf16 v[44:47], v[24:27], a[24:27], v[44:47]\n\t" \
  "v_cvt_pk_bf16_f32 v54, v38, v39\n\t" \
  "s_add_i32 s77, s77, s43\n\t" \
  "v_mfma_f32_16x16x32_bf16 v[40:43], v[12:15], a[28:31], v[40:43]\n\t" \
  "s_xor_b32 s77, s77, s76\n\t" \
  "v_mfma_f32_16x16x32_bf16 v[44:47], v[28:31], a[28:31], v[44:47]\n\t" \
  "s_cmp_lt_u32 s40, 3\n\t" \
  "s_cselect_b32 s76, 0x2000, 0\n\t" \
  "v_mfma_f32_16x16x32_bf16 v[32:35], v[0:3], a[32:35], 0\n\t" \
  "buffer_store_dwordx4 v[60:63], v80, %[ru], s44 offen offset:3072 sc1\n\t" \
  "v_mfma_f32_16x16x32_bf16 v[36:39], v[16:19], a[32:35], 0\n\t" \
  "s_andn2_b32 s77, s77, s76\n\t" \
  "v_mfma_f32_16x16x32_bf16 v[32:35], v[4:7], a[36:39], v[32:35]\n\t" \
  "v_cvt_pk_bf16_f32 v49, v40, v41\n\t" \
  "s_add_i32 s78, s40, 3\n\t" \
  "v_mfma_f32_16x16x32_bf16 v[36:39], v[20:23], a[36:39], v[36:39]\n\t" \
  "v_cvt_pk_bf16_f32 v51, v42, v43\n\t" \
  "s_cmp_lt_i32 s41, s78\n\t" \
  "s_cselect_b32 s78, 0, 0x7f000000\n\t" \
  "v_mfma_f32_16x16x32_bf16 v[32:35], v[8:11], a[40:43], v[32:35]\n\t" \
  "v_cvt_pk_bf16_f32 v53, v44, v45\n\t" \
  "v_mfma_f32_16x16x32_bf16 v[36:39], v[24:27], a[40:43], v[36:39]\n\t" \
  "v_cvt_pk_bf16_f32 v55, v46, v47\n\t" \
  "v_mfma_f32_16x16x32_bf16 v[32:35], v[12:15], a[44:47], v[32:35]\n\t" \
  "v_mfma_f32_16x16x32_bf16 v[36:39], v[28:31], a[44:47], v[36:39]\n\t" \
  "s_mov_b64 exec, 3\n\t" \
  "buffer_load_dword v72, v83, %[ru], s78 offen sc1\n\t" \
  "s_mov_b64 exec, -1\n\t" \
  "v_mfma_f32_16x16x32_bf16 v[40:43], v[0:3], a[48:51], 0\n\t" \
  "buffer_store_dwordx4 v[48:51], v80, %[ru], s45 offen offset:0 sc1\n\t" \
  "v_mfma_f32_16x16x32_bf16 v[44:47], v[16:19], a[48:51], 0\n\t" \
  "v_mfma_f32_16x16x32_bf16 v[40:43], v[4:7], a[52:55], v[40:43]\n\t" \
  "v_cvt_pk_bf16_f32 v56, v32, v33\n\t" \
  "v_mfma_f32_16x16x32_bf16 v[44:47], v[20:23], a[52:55], v[44:47]\n\t" \
  "v_cvt_pk_bf16_f32 v58, v34, v35\n\t" \
  "v_mfma_f32_16x16x32_bf16 v[40:43], v[8:11], a[56:59], v[40:43]\n\t" \
  "v_cvt_pk_bf16_f32 v60, v36, v37\n\t" \
  "v_mfma_f32_16x16x32_bf16 v[44:47], v[24:27], a[56:59], v[44:47]\n\t" \
  "v_cvt_pk_bf16_f32 v62, v38, v39\n\t" \
  "v_mfma_f32_16x16x32_bf16 v[40:43], v[12:15], a[60:63], v[40:43]\n\t" \
  "v_mfma_f32_16x16x32_bf16 v[44:47], v[28:31], a[60:63], v[44:47]\n\t" \
  "v_mfma_f32_16x16x32_bf16 v[32:35], v[0:3], a[64:67], 0\n\t" \
  "buffer_store_dwordx4 v[52:55], v80, %[ru], s45 offen offset:1024 sc1\n\t" \
  "v_mfma_f32_16x16x32_bf16 v[36:39], v[16:19], a[64:67], 0\n\t" \
  "v_mfma_f32_16x16x32_bf16 v[32:35], v[4:7], a[68:71], v[32:35]\n\t" \
  "v_cvt_pk_bf16_f32 v57, v40, v41\n\t" \
  "v_mfma_f32_16x16x32_bf16 v[36:39], v[20:23], a[68:71], v[36:39]\n\t" \
  "v_cvt_pk_bf16_f32 v59, v42, v43\n\t" \
  "v_mfma_f32_16x16x32_bf16 v[32:35], v[8:11], a[72:75], v[32:35]\n\t" \
  "v_cvt_pk_bf16_f32 v61, v44, v45\n\t" \
  "v_mfma_f32_16x16x32_bf16 v[36:39], v[24:27], a[72:75], v[36:39]\n\t" \
  "v_cvt_pk_bf16_f32 v63, v46, v47\n\t" \
  "v_mfma_f32_16x16x32_bf16 v[32:35], v[12:15], a[76:79], v[32:35]\n\t" \
  "v_mfma_f32_16x16x32_bf16 v[36:39], v[28:31], a[76:79], v[36:39]\n\t" \
  "v_mfma_f32_16x16x32_bf16 v[40:43], v[0:3], a[80:83], 0\n\t" \
  "buffer_store_dwordx4 v[56:59], v80, %[ru], s45 offen offset:2048 sc1\n\t" \
  "v_mfma_f32_16x16x32_bf16 v[44:47], v[16:19], a[80:83], 0\n\t" \
  "v_mfma_f32_16x16x32_bf16 v[40:43], v[4:7], a[84:87], v[40:43]\n\t" \
  "v_cvt_pk_bf16_f32 v48, v32, v33\n\t" \
  "v_mfma_f32_16x16x32_bf16 v[44:47], v[20:23], a[84:87], v[44:47]\n\t" \
  "v_cvt_pk_bf16_f32 v50, v34, v35\n\t" \
  "v_mfma_f32_16x16x32_bf16 v[40:43], v[8:11], a[88:91], v[40:43]\n\t" \
  "v_cvt_pk_bf16_f32 v52, v36, v37\n\t" \
  "v_mfma_f32_16x16x32_bf16 v[44:47], v[24:27], a[88:91], v[44:47]\n\t" \
  "v_cvt_pk_bf16_f32 v54, v38, v39\n\t" \
  "v_mfma_f32_16x16x32_bf16 v[40:43], v[12:15], a[92:95], v[40:43]\n\t" \
  "v_mfma_f32_16x16x32_bf16 v[44:47], v[28:31], a[92:95], v[44:47]\n\t" \
  "v_mfma_f32_16x16x32_bf16 v[32:35], v[0:3], a[96:99], 0\n\t" \
  "buffer_store_dwordx4 v[60:63], v80, %[ru], s45 offen offset:3072 sc1\n\t" \
  "v_mfma_f32_16x16x32_bf16 v[36:39], v[16:19], a[96:99], 0\n\t" \
  "v_mfma_f32_16x16x32_bf16 v[32:35], v[4:7], a[100:103], v[32:35]\n\t" \
  "v_cvt_pk_bf16_f32 v49, v40, v41\n\t" \
  "v_mfma_f32_16x16x32_bf16 v[36:39], v[20:23], a[100:103], v[36:39]\n\t" \
  "v_cvt_pk_bf16_f32 v51, v42, v43\n\t" \
  "v_mfma_f32_16x16x32_bf16 v[32:35], v[8:11], a[104:107], v[32:35]\n\t" \
  "v_cvt_pk_bf16_f32 v53, v44, v45\n\t" \
  "v_mfma_f32_16x16x32_bf16 v[36:39], v[24:27], a[104:107], v[36:39]\n\t" \
  "v_cvt_pk_bf16_f32 v55, v46, v47\n\t" \
  "v_mfma_f32_16x16x32_bf16 v[32:35], v[12:15], a[108:111], v[32:35]\n\t" \
  "v_mfma_f32_16x16x32_bf16 v[36:39], v[28:31], a[108:111], v[36:39]\n\t" \
  "s_add_i32 s45, s45, 0x1000\n\t" \
  "v_mfma_f32_16x16x32_bf16 v[40:43], v[0:3], a[112:115], 0\n\t" \
  "buffer_store_dwordx4 v[48:51], v80, %[ru], s45 offen offset:0 sc1\n\t" \
  "v_mfma_f32_16x16x32_bf16 v[44:47], v[16:19], a[112:115], 0\n\t" \
  "v_mfma_f32_16x16x32_bf16 v[40:43], v[4:7], a[116:119], v[40:43]\n\t" \
  "v_cvt_pk_bf16_f32 v56, v32, v33\n\t" \
  "v_mfma_f32_16x16x32_bf16 v[44:47], v[20:23], a[116:119], v[44:47]\n\t" \
  "v_cvt_pk_bf16_f32 v58, v34, v35\n\t" \
  "v_mfma_f32_16x16x32_bf16 v[40:43], v[8:11], a[120:123], v[40:43]\n\t" \
  "v_cvt_pk_bf16_f32 v60, v36, v37\n\t" \
  "v_mfma_f32_16x16x32_bf16 v[44:47], v[24:27], a[120:123], v[44:47]\n\t" \
  "v_cvt_pk_bf16_f32 v62, v38, v39\n\t" \
  "v_mfma_f32_16x16x32_bf16 v[40:43], v[12:15], a[124:127], v[40:43]\n\t" \
  "s_waitcnt vmcnt(5)\n\t" \
  "buffer_load_dwordx4 v[64:67], v80, %[ru], s77 offen sc1\n\t" \
  "v_mfma_f32_16x16x32_bf16 v[44:47], v[28:31], a[124:127], v[44:47]\n\t" \
  "buffer_load_dwordx4 v[68:71], v80, %[ru], s77 offen offset:1024 sc1\n\t" \
  "v_mfma_f32_16x16x32_bf16 v[32:35], v[0:3], a[128:131], 0\n\t" \
  "buffer_store_dwordx4 v[52:55], v80, %[ru], s45 offen offset:1024 sc1\n\t" \
  "v_mfma_f32_16x16x32_bf16 v[36:39], v[16:19], a[128:131], 0\n\t" \
  "v_readlane_b32 s76, v72, 0\n\t" \
  "v_readlane_b32 s79, v72, 1\n\t" \
  "v_mfma_f32_16x16x32_bf16 v[32:35], v[4:7], a[132:135], v[32:35]\n\t" \
  "v_cvt_pk_bf16_f32 v57, v40, v41\n\t" \
  "v_mfma_f32_16x16x32_bf16 v[36:39], v[20:23], a[132:135], v[36:39]\n\t" \
  "v_cvt_pk_bf16_f32 v59, v42, v43\n\t" \
  "v_mfma_f32_16x16x32_bf16 v[32:35], v[8:11], a[136:139], v[32:35]\n\t" \
  "v_cvt_pk_bf16_f32 v61, v44, v45\n\t" \
  "v_mfma_f32_16x16x32_bf16 v[36:39], v[24:27], a[136:139], v[36:39]\n\t" \
  "v_cvt_pk_bf16_f32 v63, v46, v47\n\t" \
  "v_mfma_f32_16x16x32_bf16 v[32:35], v[12:15], a[140:143], v[32:35]\n\t" \
  "s_add_i32 s79, s79, 15\n\t" \
  "s_min_i32 s76, s76, s79\n\t" \
  "v_mfma_f32_16x16x32_bf16 v[36:39], v[28:31], a[140:143], v[36:39]\n\t" \
  "s_max_i32 s41, s41, s76\n\t" \
  "v_mov_b32 v73, s41\n\t" \
  "v_mfma_f32_16x16x32_bf16 v[40:43], v[0:3], a[144:147], 0\n\t" \
  "buffer_store_dwordx4 v[56:59], v80, %[ru], s45 offen offset:2048 sc1\n\t" \
  "v_mfma_f32_16x16x32_bf16 v[44:47], v[16:19], a[144:147], 0\n\t" \
  "v_mfma_f32_16x16x32_bf16 v[40:43], v[4:7], a[148:151], v[40:43]\n\t" \
  "v_cvt_pk_bf16_f32 v48, v32, v33\n\t" \
  "v_mfma_f32_16x16x32_bf16 v[44:47], v[20:23], a[148:151], v[44:47]\n\t" \
  "v_cvt_pk_bf16_f32 v50, v34, v35\n\t" \
  "v_mfma_f32_16x16x32_bf16 v[40:43], v[8:11], a[152:155], v[40:43]\n\t" \
  "v_cvt_pk_bf16_f32 v52, v36, v37\n\t" \
  "v_mfma_f32_16x16x32_bf16 v[44:47], v[24:27], a[152:155], v[44:47]\n\t" \
  "v_cvt_pk_bf16_f32 v54, v38, v39\n\t" \
  "v_mfma_f32_16x16x32_bf16 v[40:43], v[12:15], a[156:159], v[40:43]\n\t" \
  "v_mfma_f32_16x16x32_bf16 v[44:47], v[28:31], a[156:159], v[44:47]\n\t" \
  "v_mfma_f32_16x16x32_bf16 v[32:35], v[0:3], a[160:163], 0\n\t" \
  "buffer_store_dwordx4 v[60:63], v80, %[ru], s45 offen offset:3072 sc1\n\t" \
  "v_mfma_f32_16x16x32_bf16 v[36:39], v[16:19], a[160:163], 0\n\t" \
  "v_mfma_f32_16x16x32_bf16 v[32:35], v[4:7], a[164:167], v[32:35]\n\t" \
  "v_cvt_pk_bf16_f32 v49, v40, v41\n\t" \
  "v_mfma_f32_16x16x32_bf16 v[36:39], v[20:23], a[164:167], v[36:39]\n\t" \
  "v_cvt_pk_bf16_f32 v51, v42, v43\n\t" \
  "v_mfma_f32_16x16x32_bf16 v[32:35], v[8:11], a[168:171], v[32:35]\n\t" \
  "v_cvt_pk_bf16_f32 v53, v44, v45\n\t" \
  "v_mfma_f32_16x16x32_bf16 v[36:39], v[24:27], a[168:171], v[36:39]\n\t" \
  "v_cvt_pk_bf16_f32 v55, v46, v47\n\t" \
  "v_mfma_f32_16x16x32_bf16 v[32:35], v[12:15], a[172:175], v[32:35]\n\t" \
  "v_mfma_f32_16x16x32_bf16 v[36:39], v[28:31], a[172:175], v[36:39]\n\t" \
  "s_add_i32 s45, s45, 0x3000\n\t" \
  "v_mfma_f32_16x16x32_bf16 v[40:43], v[0:3], a[176:179], 0\n\t" \
  "buffer_store_dwordx4 v[48:51], v80, %[ru], s45 offen offset:0 sc1\n\t" \
  "v_mfma_f32_16x16x32_bf16 v[44:47], v[16:19], a[176:179], 0\n\t" \
  "v_mfma_f32_16x16x32_bf16 v[40:43], v[4:7], a[180:183], v[40:43]\n\t" \
  "v_cvt_pk_bf16_f32 v56, v32, v33\n\t" \
  "v_mfma_f32_16x16x32_bf16 v[44:47], v[20:23], a[180:183], v[44:47]\n\t" \
  "v_cvt_pk_bf16_f32 v58, v34, v35\n\t" \
  "v_mfma_f32_16x16x32_bf16 v[40:43], v[8:11], a[184:187], v[40:43]\n\t" \
  "v_cvt_pk_bf16_f32 v60, v36, v37\n\t" \
  "v_mfma_f32_16x16x32_bf16 v[44:47], v[24:27], a[184:187], v[44:47]\n\t" \
  "v_cvt_pk_bf16_f32 v62, v38, v39\n\t" \
  "v_mfma_f32_16x16x32_bf16 v[40:43], v[12:15], a[188:191], v[40:43]\n\t" \
  "v_mfma_f32_16x16x32_bf16 v[44:47], v[28:31], a[188:191], v[44:47]\n\t" \
  "v_xor_b32 v81, 0x2000, v81\n\t" \
  "v_mfma_f32_16x16x32_bf16 v[32:35], v[0:3], a[192:195], 0\n\t" \
  "buffer_store_dwordx4 v[52:55], v80, %[ru], s45 offen offset:1024 sc1\n\t" \
  "v_mfma_f32_16x16x32_bf16 v[36:39], v[16:19], a[192:195], 0\n\t" \
  "v_xor_b32 v82, 0x2000, v82\n\t" \
  "v_mfma_f32_16x16x32_bf16 v[32:35], v[4:7], a[196:199], v[32:35]\n\t" \
  "v_cvt_pk_bf16_f32 v57, v40, v41\n\t" \
  "v_mfma_f32_16x16x32_bf16 v[36:39], v[20:23], a[196:199], v[36:39]\n\t" \
  "v_cvt_pk_bf16_f32 v59, v42, v43\n\t" \
  "v_mfma_f32_16x16x32_bf16 v[32:35], v[8:11], a[200:203], v[32:35]\n\t" \
  "v_cvt_pk_bf16_f32 v61, v44, v45\n\t" \
  "v_mfma_f32_16x16x32_bf16 v[36:39], v[24:27], a[200:203], v[36:39]\n\t" \
  "v_cvt_pk_bf16_f32 v63, v46, v47\n\t" \
  "v_mfma_f32_16x16x32_bf16 v[32:35], v[12:15], a[204:207], v[32:35]\n\t" \
  "s_add_i32 s79, s40, -1\n\t" \
  "v_xor_b32 v85, 16, v85\n\t" \
  "v_mfma_f32_16x16x32_bf16 v[36:39], v[28:31], a[204:207], v[36:39]\n\t" \
  "v_xor_b32 v86, 16, v86\n\t" \
  "ds_write_b32 v85, v73\n\t" \
  "v_mfma_f32_16x16x32_bf16 v[40:43], v[0:3], a[208:211], 0\n\t" \
  "buffer_store_dwordx4 v[56:59], v80, %[ru], s45 offen offset:2048 sc1\n\t" \
  "v_mfma_f32_16x16x32_bf16 v[44:47], v[16:19], a[208:211], 0\n\t" \
  "v_mov_b32 v74, s79\n\t" \
  "v_mfma_f32_16x16x32_bf16 v[40:43], v[4:7], a[212:215], v[40:43]\n\t" \
  "v_cvt_pk_bf16_f32 v48, v32, v33\n\t" \
  "v_mfma_f32_16x16x32_bf16 v[44:47], v[20:23], a[212:215], v[44:47]\n\t" \
  "v_cvt_pk_bf16_f32 v50, v34, v35\n\t" \
  "v_mfma_f32_16x16x32_bf16 v[40:43], v[8:11], a[216:219], v[40:43]\n\t" \
  "v_cvt_pk_bf16_f32 v52, v36, v37\n\t" \
  "v_mfma_f32_16x16x32_bf16 v[44:47], v[24:27], a[216:219], v[44:47]\n\t" \
  "v_cvt_pk_bf16_f32 v54, v38, v39\n\t" \
  "v_mfma_f32_16x16x32_bf16 v[40:43], v[12:15], a[220:223], v[40:43]\n\t" \
  "s_waitcnt vmcnt(6)\n\t" \
  "ds_write_b128 v82, v[64:67]\n\t" \
  "v_mfma_f32_16x16x32_bf16 v[44:47], v[28:31], a[220:223], v[44:47]\n\t" \
  "ds_write_b128 v82, v[68:71] offset:1024\n\t" \
  "v_mfma_f32_16x16x32_bf16 v[32:35], v[0:3], a[224:227], 0\n\t" \
  "buffer_store_dwordx4 v[60:63], v80, %[ru], s45 offen offset:3072 sc1\n\t" \
  "v_mfma_f32_16x16x32_bf16 v[36:39], v[16:19], a[224:227], 0\n\t" \
  "v_mfma_f32_16x16x32_bf16 v[32:35], v[4:7], a[228:231], v[32:35]\n\t" \
  "v_cvt_pk_bf16_f32 v49, v40, v41\n\t" \
  "v_mfma_f32_16x16x32_bf16 v[36:39], v[20:23], a[228:231], v[36:39]\n\t" \
  "v_cvt_pk_bf16_f32 v51, v42, v43\n\t" \
  "v_mfma_f32_16x16x32_bf16 v[32:35], v[8:11], a[232:235], v[32:35]\n\t" \
  "v_cvt_pk_bf16_f32 v53, v44, v45\n\t" \
  "v_mfma_f32_16x16x32_bf16 v[36:39], v[24:27], a[232:235], v[36:39]\n\t" \
  "v_cvt_pk_bf16_f32 v55, v46, v47\n\t" \
  "v_mfma_f32_16x16x32_bf16 v[32:35], v[12:15], a[236:239], v[32:35]\n\t" \
  "s_waitcnt lgkmcnt(0)\n\t" \
  "s_barrier\n\t" \
  "ds_read_b128 v[76:79], v86\n\t" \
  "v_mfma_f32_16x16x32_bf16 v[36:39], v[28:31], a[236:239], v[36:39]\n\t" \
  "s_add_i32 s45, s45, 0x1000\n\t" \
  "buffer_store_dword v74, v84, %[ru], 0 offen sc1\n\t" \
  "v_mfma_f32_16x16x32_bf16 v[40:43], v[0:3], a[240:243], 0\n\t" \
  "buffer_store_dwordx4 v[48:51], v80, %[ru], s45 offen offset:0 sc1\n\t" \
  "ds_read_b128 v[0:3], v81 offset:0\n\t" \
  "v_mfma_f32_16x16x32_bf16 v[44:47], v[16:19], a[240:243], 0\n\t" \
  "ds_read_b128 v[16:19], v81 offset:4096\n\t" \
  "v_mfma_f32_16x16x32_bf16 v[40:43], v[4:7], a[244:247], v[40:43]\n\t" \
  "v_cvt_pk_bf16_f32 v56, v32, v33\n\t" \
  "ds_read_b128 v[4:7], v81 offset:1024\n\t" \
  "v_mfma_f32_16x16x32_bf16 v[44:47], v[20:23], a[244:247], v[44:47]\n\t" \
  "v_cvt_pk_bf16_f32 v58, v34, v35\n\t" \
  "ds_read_b128 v[20:23], v81 offset:5120\n\t" \
  "v_mfma_f32_16x16x32_bf16 v[40:43], v[8:11], a[248:251], v[40:43]\n\t" \
  "v_cvt_pk_bf16_f32 v60, v36, v37\n\t" \
  "ds_read_b128 v[8:11], v81 offset:2048\n\t" \
  "v_mfma_f32_16x16x32_bf16 v[44:47], v[24:27], a[248:251], v[44:47]\n\t" \
  "v_cvt_pk_bf16_f32 v62, v38, v39\n\t" \
  "ds_read_b128 v[24:27], v81 offset:6144\n\t" \
  "v_mfma_f32_16x16x32_bf16 v[40:43], v[12:15], a[252:255], v[40:43]\n\t" \
  "s_waitcnt lgkmcnt(6)\n\t" \
  "v_min3_i32 v76, v76, v77, v78\n\t" \
  "ds_read_b128 v[12:15], v81 offset:3072\n\t" \
  "v_mfma_f32_16x16x32_bf16 v[44:47], v[28:31], a[252:255], v[44:47]\n\t" \
  "v_min3_i32 v76, v76, v79, 63\n\t" \
  "s_add_i32 s40, s40, 1\n\t" \
  "ds_read_b128 v[28:31], v81 offset:7168\n\t" \
  "v_readfirstlane_b32 s46, v76\n\t" \
  "s_mov_b32 s44, s45\n\t" \
  "s_cmp_le_i32 s40, s46\n\t" \
  "s_cbranch_scc1 20b\n\t" \
  "buffer_store_dwordx4 v[52:55], v80, %[ru], s44 offen offset:1024 sc1\n\t" \
  "s_nop 3\n\t" \
  "v_cvt_pk_bf16_f32 v57, v40, v41\n\t" \
  "v_cvt_pk_bf16_f32 v59, v42, v43\n\t" \
  "v_cvt_pk_bf16_f32 v61, v44, v45\n\t" \
  "v_cvt_pk_bf16_f32 v63, v46, v47\n\t" \
  "buffer_store_dwordx4 v[56:59], v80, %[ru], s44 offen offset:2048 sc1\n\t" \
  "buffer_store_dwordx4 v[60:63], v80, %[ru], s44 offen offset:3072 sc1\n\t" \
  "s_branch 99f\n\t" \
  "91:\n\t" \
  "ds_read_b128 v[0:3], v81 offset:0\n\t" \
  "ds_read_b128 v[16:19], v81 offset:4096\n\t" \
  "ds_read_b128 v[4:7], v81 offset:1024\n\t" \
  "ds_read_b128 v[20:23], v81 offset:5120\n\t" \
  "ds_read_b128 v[8:11], v81 offset:2048\n\t" \
  "ds_read_b128 v[24:27], v81 offset:6144\n\t" \
  "ds_read_b128 v[12:15], v81 offset:3072\n\t" \
  "ds_read_b128 v[28:31], v81 offset:7168\n\t" \
  ".p2align 6\n\t" \
  "20:\n\t" \
  "s_waitcnt lgkmcnt(7)\n\t" \
  "v_mfma_f32_16x16x32_bf16 v[32:35], v[0:3], a[0:3], 0\n\t" \
  "buffer_store_dwordx4 v[52:55], v80, %[ru], s44 offen offset:1024 sc1\n\t" \
  "s_waitcnt lgkmcnt(6)\n\t" \
  "v_mfma_f32_16x16x32_bf16 v[36:39], v[16:19], a[0:3], 0\n\t" \
  "s_and_b32 s45, s40, 15\n\t" \
  "s_waitcnt lgkmcnt(5)\n\t" \
  "v_mfma_f32_16x16x32_bf16 v[32:35], v[4:7], a[4:7], v[32:35]\n\t" \
  "v_cvt_pk_bf16_f32 v57, v40, v41\n\t" \
  "s_lshl_b32 s45, s45, 22\n\t" \
  "s_waitcnt lgkmcnt(4)\n\t" \
  "v_mfma_f32_16x16x32_bf16 v[36:39], v[20:23], a[4:7], v[36:39]\n\t" \
  "v_cvt_pk_bf16_f32 v59, v42, v43\n\t" \
  "s_add_i32 s45, s45, s42\n\t" \
  "s_waitcnt lgkmcnt(3)\n\t" \
  "v_mfma_f32_16x16x32_bf16 v[32:35], v[8:11], a[8:11], v[32:35]\n\t" \
  "v_cvt_pk_bf16_f32 v61, v44, v45\n\t" \
  "s_lshr_b32 s76, s45, 21\n\t" \
  "s_waitcnt lgkmcnt(2)\n\t" \
  "v_mfma_f32_16x16x32_bf16 v[36:39], v[24:27], a[8:11], v[36:39]\n\t" \
  "v_cvt_pk_bf16_f32 v63, v46, v47\n\t" \
  "s_bitcmp0_b32 %[sel], s76\n\t" \
  "s_cselect_b32 s76, 0x2000, 0\n\t" \
  "s_waitcnt lgkmcnt(1)\n\t" \
  "v_mfma_f32_16x16x32_bf16 v[32:35], v[12:15], a[12:15], v[32:35]\n\t" \
  "s_add_i32 s45, s45, s76\n\t" \
  "s_waitcnt lgkmcnt(0)\n\t" \
  "v_mfma_f32_16x16x32_bf16 v[36:39], v[28:31], a[12:15], v[36:39]\n\t" \
  "s_add_i32 s77, s40, 1\n\t" \
  "v_mfma_f32_16x16x32_bf16 v[40:43], v[0:3], a[16:19], 0\n\t" \
  "buffer_store_dwordx4 v[56:59], v80, %[ru], s44 offen offset:2048 sc1\n\t" \
  "v_mfma_f32_16x16x32_bf16 v[44:47], v[16:19], a[16:19], 0\n\t" \
  "s_min_u32 s77, s77, 63\n\t" \
  "v_mfma_f32_16x16x32_bf16 v[40:43], v[4:7], a[20:23], v[40:43]\n\t" \
  "v_cvt_pk_bf16_f32 v48, v32, v33\n\t" \
  "s_and_b32 s76, s77, 4\n\t" \
  "v_mfma_f32_16x16x32_bf16 v[44:47], v[20:23], a[20:23], v[44:47]\n\t" \
  "v_cvt_pk_bf16_f32 v50, v34, v35\n\t" \
  "s_lshl_b32 s76, s76, 11\n\t" \
  "v_mfma_f32_16x16x32_bf16 v[40:43], v[8:11], a[24:27], v[40:43]\n\t" \
  "v_cvt_pk_bf16_f32 v52, v36, v37\n\t" \
  "s_lshl_b32 s77, s77, 19\n\t" \
  "v_mfma_f32_16x16x32_bf16 v[44:47], v[24:27], a[24:27], v[44:47]\n\t" \
  "v_cvt_pk_bf16_f32 v54, v38, v39\n\t" \
  "s_add_i32 s77, s77, s43\n\t" \
  "v_mfma_f32_16x16x32_bf16 v[40:43], v[12:15], a[28:31], v[40:43]\n\t" \
  "s_xor_b32 s77, s77, s76\n\t" \
  "v_mfma_f32_16x16x32_bf16 v[44:47], v[28:31], a[28:31], v[44:47]\n\t" \
  "s_cmp_lt_u32 s40, 3\n\t" \
  "s_cselect_b32 s76, 0x2000, 0\n\t" \
  "v_mfma_f32_16x16x32_bf16 v[32:35], v[0:3], a[32:35], 0\n\t" \
  "buffer_store_dwordx4 v[60:63], v80, %[ru], s44 offen offset:3072 sc1\n\t" \
  "v_mfma_f32_16x16x32_bf16 v[36:39], v[16:19], a[32:35], 0\n\t" \
  "s_andn2_b32 s77, s77, s76\n\t" \
  "v_mfma_f32_16x16x32_bf16 v[32:35], v[4:7], a[36:39], v[32:35]\n\t" \
  "v_cvt_pk_bf16_f32 v49, v40, v41\n\t" \
  "s_add_i32 s78, s40, 3\n\t" \
  "v_mfma_f32_16x16x32_bf16 v[36:39], v[20:23], a[36:39], v[36:39]\n\t" \
  "v_cvt_pk_bf16_f32 v51, v42, v43\n\t" \
  "s_cmp_lt_i32 s41, s78\n\t" \
  "s_cselect_b32 s78, 0, 0x7f000000\n\t" \
  "v_mfma_f32_16x16x32_bf16 v[32:35], v[8:11], a[40:43], v[32:35]\n\t" \
  "v_cvt_pk_bf16_f32 v53, v44, v45\n\t" \
  "v_mfma_f32_16x16x32_bf16 v[36:39], v[24:27], a[40:43], v[36:39]\n\t" \
  "v_cvt_pk_bf16_f32 v55, v46, v47\n\t" \
  "v_mfma_f32_16x16x32_bf16 v[32:35], v[12:15], a[44:47], v[32:35]\n\t" \
  "v_mfma_f32_16x16x32_bf16 v[36:39], v[28:31], a[44:47], v[36:39]\n\t" \
  "s_mov_b64 exec, 3\n\t" \
  "buffer_load_dword v72, v83, %[ru], s78 offen sc1\n\t" \
  "s_mov_b64 exec, -1\n\t" \
  "v_mfma_f32_16x16x32_bf16 v[40:43], v[0:3], a[48:51], 0\n\t" \
  "buffer_store_dwordx4 v[48:51], v80, %[ru], s45 offen offset:0 sc1\n\t" \
  "v_mfma_f32_16x16x32_bf16 v[44:47], v[16:19], a[48:51], 0\n\t" \
  "v_mfma_f32_16x16x32_bf16 v[40:43], v[4:7], a[52:55], v[40:43]\n\t" \
  "v_cvt_pk_bf16_f32 v56, v32, v33\n\t" \
  "v_mfma_f32_16x16x32_bf16 v[44:47], v[20:23], a[52:55], v[44:47]\n\t" \
  "v_cvt_pk_bf16_f32 v58, v34, v35\n\t" \
  "v_mfma_f32_16x16x32_bf16 v[40:43], v[8:11], a[56:59], v[40:43]\n\t" \
  "v_cvt_pk_bf16_f32 v60, v36, v37\n\t" \
  "v_mfma_f32_16x16x32_bf16 v[44:47], v[24:27], a[56:59], v[44:47]\n\t" \
  "v_cvt_pk_bf16_f32 v62, v38, v39\n\t" \
  "v_mfma_f32_16x16x32_bf16 v[40:43], v[12:15], a[60:63], v[40:43]\n\t" \
  "v_mfma_f32_16x16x32_bf16 v[44:47], v[28:31], a[60:63], v[44:47]\n\t" \
  "v_mfma_f32_16x16x32_bf16 v[32:35], v[0:3], a[64:67], 0\n\t" \
  "buffer_store_dwordx4 v[52:55], v80, %[ru], s45 offen offset:1024 sc1\n\t" \
  "v_mfma_f32_16x16x32_bf16 v[36:39], v[16:19], a[64:67], 0\n\t" \
  "v_mfma_f32_16x16x32_bf16 v[32:35], v[4:7], a[68:71], v[32:35]\n\t" \
  "v_cvt_pk_bf16_f32 v57, v40, v41\n\t" \
  "v_mfma_f32_16x16x32_bf16 v[36:39], v[20:23], a[68:71], v[36:39]\n\t" \
  "v_cvt_pk_bf16_f32 v59, v42, v43\n\t" \
  "v_mfma_f32_16x16x32_bf16 v[32:35], v[8:11], a[72:75], v[32:35]\n\t" \
  "v_cvt_pk_bf16_f32 v61, v44, v45\n\t" \
  "v_mfma_f32_16x16x32_bf16 v[36:39], v[24:27], a[72:75], v[36:39]\n\t" \
  "v_cvt_pk_bf16_f32 v63, v46, v47\n\t" \
  "v_mfma_f32_16x16x32_bf16 v[32:35], v[12:15], a[76:79], v[32:35]\n\t" \
  "v_mfma_f32_16x16x32_bf16 v[36:39], v[28:31], a[76:79], v[36:39]\n\t" \
  "v_mfma_f32_16x16x32_bf16 v[40:43], v[0:3], a[80:83], 0\n\t" \
  "buffer_store_dwordx4 v[56:59], v80, %[ru], s45 offen offset:2048 sc1\n\t" \
  "v_mfma_f32_16x16x32_bf16 v[44:47], v[16:19], a[80:83], 0\n\t" \
  "v_mfma_f32_16x16x32_bf16 v[40:43], v[4:7], a[84:87], v[40:43]\n\t" \
  "v_cvt_pk_bf16_f32 v48, v32, v33\n\t" \
  "v_mfma_f32_16x16x32_bf16 v[44:47], v[20:23], a[84:87], v[44:47]\n\t" \
  "v_cvt_pk_bf16_f32 v50, v34, v35\n\t" \
  "v_mfma_f32_16x16x32_bf16 v[40:43], v[8:11], a[88:91], v[40:43]\n\t" \
  "v_cvt_pk_bf16_f32 v52, v36, v37\n\t" \
  "v_mfma_f32_16x16x32_bf16 v[44:47], v[24:27], a[88:91], v[44:47]\n\t" \
  "v_cvt_pk_bf16_f32 v54, v38, v39\n\t" \
  "v_mfma_f32_16x16x32_bf16 v[40:43], v[12:15], a[92:95], v[40:43]\n\t" \
  "v_mfma_f32_16x16x32_bf16 v[44:47], v[28:31], a[92:95], v[44:47]\n\t" \
  "v_mfma_f32_16x16x32_bf16 v[32:35], v[0:3], a[96:99], 0\n\t" \
  "buffer_store_dwordx4 v[60:63], v80, %[ru], s45 offen offset:3072 sc1\n\t" \
  "v_mfma_f32_16x16x32_bf16 v[36:39], v[16:19], a[96:99], 0\n\t" \
  "v_mfma_f32_16x16x32_bf16 v[32:35], v[4:7], a[100:103], v[32:35]\n\t" \
  "v_cvt_pk_bf16_f32 v49, v40, v41\n\t" \
  "v_mfma_f32_16x16x32_bf16 v[36:39], v[20:23], a[100:103], v[36:39]\n\t" \
  "v_cvt_pk_bf16_f32 v51, v42, v43\n\t" \
  "v_mfma_f32_16x16x32_bf16 v[32:35], v[8:11], a[104:107], v[32:35]\n\t" \
  "v_cvt_pk_bf16_f32 v53, v44, v45\n\t" \
  "v_mfma_f32_16x16x32_bf16 v[36:39], v[24:27], a[104:107], v[36:39]\n\t" \
  "v_cvt_pk_bf16_f32 v55, v46, v47\n\t" \
  "v_mfma_f32_16x16x32_bf16 v[32:35], v[12:15], a[108:111], v[32:35]\n\t" \
  "v_mfma_f32_16x16x32_bf16 v[36:39], v[28:31], a[108:111], v[36:39]\n\t" \
  "s_add_i32 s45, s45, 0x1000\n\t" \
  "v_mfma_f32_16x16x32_bf16 v[40:43], v[0:3], a[112:115], 0\n\t" \
  "buffer_store_dwordx4 v[48:51], v80, %[ru], s45 offen offset:0 sc1\n\t" \
  "v_mfma_f32_16x16x32_bf16 v[44:47], v[16:19], a[112:115], 0\n\t" \
  "v_mfma_f32_16x16x32_bf16 v[40:43], v[4:7], a[116:119], v[40:43]\n\t" \
  "v_cvt_pk_bf16_f32 v56, v32, v33\n\t" \
  "v_mfma_f32_16x16x32_bf16 v[44:47], v[20:23], a[116:119], v[44:47]\n\t" \
  "v_cvt_pk_bf16_f32 v58, v34, v35\n\t" \
  "v_mfma_f32_16x16x32_bf16 v[40:43], v[8:11], a[120:123], v[40:43]\n\t" \
  "v_cvt_pk_bf16_f32 v60, v36, v37\n\t" \
  "v_mfma_f32_16x16x32_bf16 v[44:47], v[24:27], a[120:123], v[44:47]\n\t" \
  "v_cvt_pk_bf16_f32 v62, v38, v39\n\t" \
  "v_mfma_f32_16x16x32_bf16 v[40:43], v[12:15], a[124:127], v[40:43]\n\t" \
  "s_waitcnt vmcnt(5)\n\t" \
  "buffer_load_dwordx4 v[64:67], v80, %[ru], s77 offen sc1\n\t" \
  "v_mfma_f32_16x16x32_bf16 v[44:47], v[28:31], a[124:127], v[44:47]\n\t" \
  "buffer_load_dwordx4 v[68:71], v80, %[ru], s77 offen offset:1024 sc1\n\t" \
  "v_mfma_f32_16x16x32_bf16 v[32:35], v[0:3], a[128:131], 0\n\t" \
  "buffer_store_dwordx4 v[52:55], v80, %[ru], s45 offen offset:1024 sc1\n\t" \
  "v_mfma_f32_16x16x32_bf16 v[36:39], v[16:19], a[128:131], 0\n\t" \
  "v_readlane_b32 s76, v72, 0\n\t" \
  "v_readlane_b32 s79, v72, 1\n\t" \
  "v_mfma_f32_16x16x32_bf16 v[32:35], v[4:7], a[132:135], v[32:35]\n\t" \
  "v_cvt_pk_bf16_f32 v57, v40, v41\n\t" \
  "v_mfma_f32_16x16x32_bf16 v[36:39], v[20:23], a[132:135], v[36:39]\n\t" \
  "v_cvt_pk_bf16_f32 v59, v42, v43\n\t" \
  "v_mfma_f32_16x16x32_bf16 v[32:35], v[8:11], a[136:139], v[32:35]\n\t" \
  "v_cvt_pk_bf16_f32 v61, v44, v45\n\t" \
  "v_mfma_f32_16x16x32_bf16 v[36:39], v[24:27], a[136:139], v[36:39]\n\t" \
  "v_cvt_pk_bf16_f32 v63, v46, v47\n\t" \
  "v_mfma_f32_16x16x32_bf16 v[32:35], v[12:15], a[140:143], v[32:35]\n\t" \
  "s_add_i32 s79, s79, 15\n\t" \
  "s_min_i32 s76, s76, s79\n\t" \
  "v_mfma_f32_16x16x32_bf16 v[36:39], v[28:31], a[140:143], v[36:39]\n\t" \
  "s_max_i32 s41, s41, s76\n\t" \
  "v_mov_b32 v73, s41\n\t" \
  "v_mfma_f32_16x16x32_bf16 v[40:43], v[0:3], a[144:147], 0\n\t" \
  "buffer_store_dwordx4 v[56:59], v80, %[ru], s45 offen offset:2048 sc1\n\t" \
  "v_mfma_f32_16x16x32_bf16 v[44:47], v[16:19], a[144:147], 0\n\t" \
  "v_mfma_f32_16x16x32_bf16 v[40:43], v[4:7], a[148:151], v[40:43]\n\t" \
  "v_cvt_pk_bf16_f32 v48, v32, v33\n\t" \
  "v_mfma_f32_16x16x32_bf16 v[44:47], v[20:23], a[148:151], v[44:47]\n\t" \
  "v_cvt_pk_bf16_f32 v50, v34, v35\n\t" \
  "v_mfma_f32_16x16x32_bf16 v[40:43], v[8:11], a[152:155], v[40:43]\n\t" \
  "v_cvt_pk_bf16_f32 v52, v36, v37\n\t" \
  "v_mfma_f32_16x16x32_bf16 v[44:47], v[24:27], a[152:155], v[44:47]\n\t" \
  "v_cvt_pk_bf16_f32 v54, v38, v39\n\t" \
  "v_mfma_f32_16x16x32_bf16 v[40:43], v[12:15], a[156:159], v[40:43]\n\t" \
  "v_mfma_f32_16x16x32_bf16 v[44:47], v[28:31], a[156:159], v[44:47]\n\t" \
  "v_mfma_f32_16x16x32_bf16 v[32:35], v[0:3], a[160:163], 0\n\t" \
  "buffer_store_dwordx4 v[60:63], v80, %[ru], s45 offen offset:3072 sc1\n\t" \
  "v_mfma_f32_16x16x32_bf16 v[36:39], v[16:19], a[160:163], 0\n\t" \
  "v_mfma_f32_16x16x32_bf16 v[32:35], v[4:7], a[164:167], v[32:35]\n\t" \
  "v_cvt_pk_bf16_f32 v49, v40, v41\n\t" \
  "v_mfma_f32_16x16x32_bf16 v[36:39], v[20:23], a[164:167], v[36:39]\n\t" \
  "v_cvt_pk_bf16_f32 v51, v42, v43\n\t" \
  "v_mfma_f32_16x16x32_bf16 v[32:35], v[8:11], a[168:171], v[32:35]\n\t" \
  "v_cvt_pk_bf16_f32 v53, v44, v45\n\t" \
  "v_mfma_f32_16x16x32_bf16 v[36:39], v[24:27], a[168:171], v[36:39]\n\t" \
  "v_cvt_pk_bf16_f32 v55, v46, v47\n\t" \
  "v_mfma_f32_16x16x32_bf16 v[32:35], v[12:15], a[172:175], v[32:35]\n\t" \
  "v_mfma_f32_16x16x32_bf16 v[36:39], v[28:31], a[172:175], v[36:39]\n\t" \
  "s_add_i32 s45, s45, 0x3000\n\t" \
  "v_mfma_f32_16x16x32_bf16 v[40:43], v[0:3], a[176:179], 0\n\t" \
  "buffer_store_dwordx4 v[48:51], v80, %[ru], s45 offen offset:0 sc1\n\t" \
  "v_mfma_f32_16x16x32_bf16 v[44:47], v[16:19], a[176:179], 0\n\t" \
  "v_mfma_f32_16x16x32_bf16 v[40:43], v[4:7], a[180:183], v[40:43]\n\t" \
  "v_cvt_pk_bf16_f32 v56, v32, v33\n\t" \
  "v_mfma_f32_16x16x32_bf16 v[44:47], v[20:23], a[180:183], v[44:47]\n\t" \
  "v_cvt_pk_bf16_f32 v58, v34, v35\n\t" \
  "v_mfma_f32_16x16x32_bf16 v[40:43], v[8:11], a[184:187], v[40:43]\n\t" \
  "v_cvt_pk_bf16_f32 v60, v36, v37\n\t" \
  "v_mfma_f32_16x16x32_bf16 v[44:47], v[24:27], a[184:187], v[44:47]\n\t" \
  "v_cvt_pk_bf16_f32 v62, v38, v39\n\t" \
  "v_mfma_f32_16x16x32_bf16 v[40:43], v[12:15], a[188:191], v[40:43]\n\t" \
  "v_mfma_f32_16x16x32_bf16 v[44:47], v[28:31], a[188:191], v[44:47]\n\t" \
  "v_xor_b32 v81, 0x2000, v81\n\t" \
  "v_mfma_f32_16x16x32_bf16 v[32:35], v[0:3], a[192:195], 0\n\t" \
  "buffer_store_dwordx4 v[52:55], v80, %[ru], s45 offen offset:1024 sc1\n\t" \
  "v_mfma_f32_16x16x32_bf16 v[36:39], v[16:19], a[192:195], 0\n\t" \
  "v_xor_b32 v82, 0x2000, v82\n\t" \
  "v_mfma_f32_16x16x32_bf16 v[32:35], v[4:7], a[196:199], v[32:35]\n\t" \
  "v_cvt_pk_bf16_f32 v57, v40, v41\n\t" \
  "v_mfma_f32_16x16x32_bf16 v[36:39], v[20:23], a[196:199], v[36:39]\n\t" \
  "v_cvt_pk_bf16_f32 v59, v42, v43\n\t" \
  "v_mfma_f32_16x16x32_bf16 v[32:35], v[8:11], a[200:203], v[32:35]\n\t" \
  "v_cvt_pk_bf16_f32 v61, v44, v45\n\t" \
  "v_mfma_f32_16x16x32_bf16 v[36:39], v[24:27], a[200:203], v[36:39]\n\t" \
  "v_cvt_pk_bf16_f32 v63, v46, v47\n\t" \
  "v_mfma_f32_16x16x32_bf16 v[32:35], v[12:15], a[204:207], v[32:35]\n\t" \
  "s_add_i32 s79, s40, -1\n\t" \
  "v_xor_b32 v85, 16, v85\n\t" \
  "v_mfma_f32_16x16x32_bf16 v[36:39], v[28:31], a[204:207], v[36:39]\n\t" \
  "v_xor_b32 v86, 16, v86\n\t" \
  "ds_write_b32 v85, v73\n\t" \
  "v_mfma_f32_16x16x32_bf16 v[40:43], v[0:3], a[208:211], 0\n\t" \
  "buffer_store_dwordx4 v[56:59], v80, %[ru], s45 offen offset:2048 sc1\n\t" \
  "v_mfma_f32_16x16x32_bf16 v[44:47], v[16:19], a[208:211], 0\n\t" \
  "v_mov_b32 v74, s79\n\t" \
  "v_mfma_f32_16x16x32_bf16 v[40:43], v[4:7], a[212:215], v[40:43]\n\t" \
  "v_cvt_pk_bf16_f32 v48, v32, v33\n\t" \
  "v_mfma_f32_16x16x32_bf16 v[44:47], v[20:23], a[212:215], v[44:47]\n\t" \
  "v_cvt_pk_bf16_f32 v50, v34, v35\n\t" \
  "v_mfma_f32_16x16x32_bf16 v[40:43], v[8:11], a[216:219], v[40:43]\n\t" \
  "v_cvt_pk_bf16_f32 v52, v36, v37\n\t" \
  "v_mfma_f32_16x16x32_bf16 v[44:47], v[24:27], a[216:219], v[44:47]\n\t" \
  "v_cvt_pk_bf16_f32 v54, v38, v39\n\t" \
  "v_mfma_f32_16x16x32_bf16 v[40:43], v[12:15], a[220:223], v[40:43]\n\t" \
  "s_waitcnt vmcnt(6)\n\t" \
  "ds_write_b128 v82, v[64:67]\n\t" \
  "v_mfma_f32_16x16x32_bf16 v[44:47], v[28:31], a[220:223], v[44:47]\n\t" \
  "ds_write_b128 v82, v[68:71] offset:1024\n\t" \
  "v_mfma_f32_16x16x32_bf16 v[32:35], v[0:3], a[224:227], 0\n\t" \
  "buffer_store_dwordx4 v[60:63], v80, %[ru], s45 offen offset:3072 sc1\n\t" \
  "v_mfma_f32_16x16x32_bf16 v[36:39], v[16:19], a[224:227], 0\n\t" \
  "v_mfma_f32_16x16x32_bf16 v[32:35], v[4:7], a[228:231], v[32:35]\n\t" \
  "v_cvt_pk_bf16_f32 v49, v40, v41\n\t" \
  "v_mfma_f32_16x16x32_bf16 v[36:39], v[20:23], a[228:231], v[36:39]\n\t" \
  "v_cvt_pk_bf16_f32 v51, v42, v43\n\t" \
  "v_mfma_f32_16x16x32_bf16 v[32:35], v[8:11], a[232:235], v[32:35]\n\t" \
  "v_cvt_pk_bf16_f32 v53, v44, v45\n\t" \
  "v_mfma_f32_16x16x32_bf16 v[36:39], v[24:27], a[232:235], v[36:39]\n\t" \
  "v_cvt_pk_bf16_f32 v55, v46, v47\n\t" \
  "v_mfma_f32_16x16x32_bf16 v[32:35], v[12:15], a[236:239], v[32:35]\n\t" \
  "s_waitcnt lgkmcnt(0)\n\t" \
  "s_barrier\n\t" \
  "ds_read_b128 v[76:79], v86\n\t" \
  "v_mfma_f32_16x16x32_bf16 v[36:39], v[28:31], a[236:239], v[36:39]\n\t" \
  "s_add_i32 s45, s45, 0x1000\n\t" \
  "v_mfma_f32_16x16x32_bf16 v[40:43], v[0:3], a[240:243], 0\n\t" \
  "buffer_store_dwordx4 v[48:51], v80, %[ru], s45 offen offset:0 sc1\n\t" \
  "ds_read_b128 v[0:3], v81 offset:0\n\t" \
  "v_mfma_f32_16x16x32_bf16 v[44:47], v[16:19], a[240:243], 0\n\t" \
  "ds_read_b128 v[16:19], v81 offset:4096\n\t" \
  "v_mfma_f32_16x16x32_bf16 v[40:43], v[4:7], a[244:247], v[40:43]\n\t" \
  "v_cvt_pk_bf16_f32 v56, v32, v33\n\t" \
  "ds_read_b128 v[4:7], v81 offset:1024\n\t" \
  "v_mfma_f32_16x16x32_bf16 v[44:47], v[20:23], a[244:247], v[44:47]\n\t" \
  "v_cvt_pk_bf16_f32 v58, v34, v35\n\t" \
  "ds_read_b128 v[20:23], v81 offset:5120\n\t" \
  "v_mfma_f32_16x16x32_bf16 v[40:43], v[8:11], a[248:251], v[40:43]\n\t" \
  "v_cvt_pk_bf16_f32 v60, v36, v37\n\t" \
  "ds_read_b128 v[8:11], v81 offset:2048\n\t" \
  "v_mfma_f32_16x16x32_bf16 v[44:47], v[24:27], a[248:251], v[44:47]\n\t" \
  "v_cvt_pk_bf16_f32 v62, v38, v39\n\t" \
  "ds_read_b128 v[24:27], v81 offset:6144\n\t" \
  "v_mfma_f32_16x16x32_bf16 v[40:43], v[12:15], a[252:255], v[40:43]\n\t" \
  "s_waitcnt lgkmcnt(6)\n\t" \
  "v_min3_i32 v76, v76, v77, v78\n\t" \
  "ds_read_b128 v[12:15], v81 offset:3072\n\t" \
  "v_mfma_f32_16x16x32_bf16 v[44:47], v[28:31], a[252:255], v[44:47]\n\t" \
  "v_min3_i32 v76, v76, v79, 63\n\t" \
  "s_add_i32 s40, s40, 1\n\t" \
  "ds_read_b128 v[28:31], v81 offset:7168\n\t" \
  "v_readfirstlane_b32 s46, v76\n\t" \
  "s_mov_b32 s44, s45\n\t" \
  "s_cmp_le_i32 s40, s46\n\t" \
  "s_cbranch_scc1 20b\n\t" \
  "buffer_store_dwordx4 v[52:55], v80, %[ru], s44 offen offset:1024 sc1\n\t" \
  "s_nop 3\n\t" \
  "v_cvt_pk_bf16_f32 v57, v40, v41\n\t" \
  "v_cvt_pk_bf16_f32 v59, v42, v43\n\t" \
  "v_cvt_pk_bf16_f32 v61, v44, v45\n\t" \
  "v_cvt_pk_bf16_f32 v63, v46, v47\n\t" \
  "buffer_store_dwordx4 v[56:59], v80, %[ru], s44 offen offset:2048 sc1\n\t" \
  "buffer_store_dwordx4 v[60:63], v80, %[ru], s44 offen offset:3072 sc1\n\t" \
  "s_branch 99f\n\t" \
  "99:\n\t" \
  "s_waitcnt vmcnt(0) lgkmcnt(0)\n\t" \
  ""

#define V10(x) "v" #x "0", "v" #x "1", "v" #x "2", "v" #x "3", "v" #x "4", "v" #x "5", "v" #x "6", "v" #x "7", "v" #x "8", "v" #x "9"
#define A10(x) "a" #x "0", "a" #x "1", "a" #x "2", "a" #x "3", "a" #x "4", "a" #x "5", "a" #x "6", "a" #x "7", "a" #x "8", "a" #x "9"
#define V90 V10(), V10(1), V10(2), V10(3), V10(4), V10(5), V10(6), V10(7), V10(8)
#define S22 "s56", "s57", "s58", "s59", "s76", "s77", "s78", "s79", "s80", "s81", "s82", "s83", "s84", "s85", "s86", "s87", "s88", "s89", \
  "s90", "s91", "s92", "s93", "s94", "s95", "s96", "s97"
#define STEP_PP
#define STEP_CL
#define STEP_PV
#define CB_PV , "{v248}"(C24)
#define STEPS(BODY, ...) asm volatile(BODY : "=&{v[92:99]}"(X), "+&{s[40:55]}"(P) STEP_PV \
  : "{v[120:151]}"(W.v[0]), "{v[152:183]}"(W.v[1]), "{v[184:215]}"(W.v[2]), "{v[216:247]}"(W.v[3]), "{v[100:115]}"(C), [rh] "s"(rh), [wv] "s"(w), [ru] "s"(rum) STEP_PP __VA_ARGS__ \
  : "memory", "vcc", "scc", S22, STEP_CL V90, "v90", "v91", "v116", "v117", "v118")
__device__ __forceinline__ void keep_a(const Wts& W) {
#pragma unroll
  for (int i = 0; i < 32; ++i) asm volatile("" :: "a"(W.a[i]));
}

__device__ __forceinline__ void steps_init(u32x16& P, int h, int w, unsigned place, Sel sel) {
#pragma unroll
  for (int i = 0; i < 16; ++i) P[i] = 0;
  P[2] = 1; P[3] = 1420; P[14] = H_MEM + hot_q(RHO(h), sel) + w * 1024; P[15] = place;
}
__device__ __forceinline__ void steps_enter(u32x16& P, Clk& ck, bool dead) {
  ck.sync();
  P[2] = (P[2] & 3u) | (dead ? 4u : 0u); P[6] = ck.c2t << 15; P[11] = ck.ce; P[12] = ck.t2c; P[13] = ck.te;
#pragma unroll
  for (int i = 0; i < 16; ++i) P[i] = __builtin_amdgcn_readfirstlane(P[i]);
}

__device__ __forceinline__ void role_qk(const ChainArgs& a, int h, char* smem, const int w, Gate& G) {
  int lane = opaque_tid(0);
  const int b = h; (void)b;
  u64* prof = a.prof; (void)prof;
  PDECL(L_PRF, 4);
  const float CS = 0.08838834764831845f * 1.4426950408889634f;
  GAS unsigned* abortw = (GAS unsigned*)(a.sync + SY_ABORT);
  bool dead = false;
  Wts W;
  Clk ck; ck.init();
  load_weights<0>(a, h, w, lane, smem, W);
  if (!G.pass(a.sync)) return;
  PZERO;
  const Sel sel = sel_wait(a.sync, abortw, lane, dead, true, CT_PR + h, 0, CT_PR + 32 + h, 1);
  const __amdgpu_buffer_rsrc_t rh = rsrc_hot(a.sync, sel), rum = rsrc_u(a.sync);
#pragma unroll
  for (int i = 0; i < 2; ++i) *(u32x4*)(smem + L_KX + (2 * w + i) * 1024 + lane * 16) = ld16(rum, OFF_KX + h * CHK + (2 * w + i) * 1024 + lane * 16);
  const unsigned hq = (h >> 3) * 16 * CHK;
  auto pz_o = [&](int l) { return (l & 1) ? OOR : H_PZ + hq + (h & 7) * 256 + w * 128 + (l & 8) * 8 + (l >> 4) * 16 + (l & 6) * 2; };
  auto z_at = [&](int l, int half) { return ((l & 6) >> 1) == 2 * w + ((h >> 2) & 1) && ((l >> 4) & 1) == half ? 1.f : 0.f; };
  u32x16 C, P;
  unsigned C16;
  {
    const int r = lane & 15, g = lane >> 4;
    const unsigned l0 = (unsigned)(uintptr_t)(LAS char*)smem;
    C[0] = lane * 16; C[1] = l0 + L_MEM + lane * 16; C[2] = l0 + L_WTS + w * 4096 + lane * 16; C[3] = C[2] + 65536;
    C[4] = (lane & 2) ? 0xEEEE : 0x4444;
    C[5] = l0 + L_XCH + (16 * w + 2 * (r >> 2) + (r & 1)) * 32 + g * 8 + ((r >> 1) & 1) * 4;
    C[6] = l0 + L_XK + (32 * w + r) * 16 + g * 4;
    C[7] = w < 2 ? l0 + L_XCH + (8 * g + (r >> 2)) * 32 + (r & 3) * 8 : SY_CT + (CT_PR + 32 * (w - 2) + h) * 128;
    C[8] = l0 + L_XK + (8 * g + (r >> 2)) * 16 + (r & 1) * 8;
    C[9] = w < 3 ? (lane == 0 ? H_FLG + h * 128 : OOR) :
           lane < 32 ? H_P + hq + (h & 7) * 512 + lane * 16 : (lane < 48 ? H_PZ + hq + (h & 7) * 256 + (lane - 32) * 16 : OOR);
    C[10] = H_TR + copy_q((lane >> 3) & 3, h >> 3, sel) + (lane & 7) * 128; C[11] = l0 + L_PRF + w * 1024 + (lane >> 6);
    C[12] = H_P + hq + (h & 7) * 512 + w * 256 + (r >> 2) * 64 + g * 16 + (r & 3) * 4; C[13] = pz_o(lane);
    C[14] = __float_as_uint(z_at(lane, 0)); C[15] = __float_as_uint(z_at(lane, 1));
    C16 = l0 + L_KX + (r >> 3) * 4096 + (g * 16 + 2 * (r & 7)) * 16;
    if (w == 3) { C[12] = lane < 4 ? H_ANN + hq + quarter(lane) + (h & 7) * 128 : OOR; C[13] = lane == 4 ? H_FLG + h * 128 : C[12]; }
    if (w == 2) {
#pragma unroll
      for (int i = 0; i < 4; ++i) C[12 + i] = 4 * g + i <= r ? 0u : 0xff800000u;
      C16 = H_PX + (h >> 2) * CHK + (h & 3) * 2048 + lane * 16;
    }
    asm volatile("" : "+v"(C), "+v"(C16));
  }
  SSTAMP(96 + h);
  steps_init(P, h, w, w < 2 ? copy_q(h >> 3, h >> 3, sel) - hq : (w == 2 ? OFF_QX : OFF_KX) + far_blk(h, sel, FARX >> (w & 1)), sel);
  unsigned spins = 0;
  while (true) {
    u32x8 X; u32x16 pv; (void)pv;
    steps_enter(P, ck, dead);
    STEPS(QK_BODY, , [cs] "s"(CS), "{v119}"(C16));
    keep_a(W);
    const unsigned res = __builtin_amdgcn_readfirstlane(P[2]), s = __builtin_amdgcn_readfirstlane(P[4]);
    lane = opaque_tid(0);
    if (res & 8u) {
      auto sw = [](float v, int k) { return __int_as_float(k == 0 ? __builtin_amdgcn_mov_dpp(__float_as_int(v), 0xB1, 0xf, 0xf, false) :
                                                           k == 1 ? __builtin_amdgcn_mov_dpp(__float_as_int(v), 0x4E, 0xf, 0xf, false) :
                                                                    __builtin_amdgcn_mov_dpp(__float_as_int(v), 0x141, 0xf, 0xf, false)); };
      float d = fmaxf(__uint_as_float(X[4]), 0.f);
      d = fmaxf(d, sw(d, 0)); d = fmaxf(d, sw(d, 1)); d = fmaxf(d, sw(d, 2));
      const float x = __builtin_amdgcn_exp2f(__uint_as_float(X[4]) - d), y = __builtin_amdgcn_exp2f(-d), xn = sw(x, 0);
      float z = fmaf(__uint_as_float(X[0]), y, x);
      z += sw(z, 0); z += sw(z, 1); z += sw(z, 2);
      z = __builtin_amdgcn_rcpf(z);
      const unsigned pm = nosent((lane & 8) ? pk2(z_at(lane, 0) * y * z, z_at(lane, 1) * y * z) : pk2(x * z, xn * z));
      if (!(__uint_as_float(X[5]) < 1.0e18f))
        for (int q = 0; q < 4; ++q) __builtin_amdgcn_raw_buffer_store_b32(pm, rh, pz_o(lane), ((s - 1) & 3) * 4 * CHK + quarter(q), ST_AUX);
    }
    if (res & 64u) break;
    if (res & 32u) ctr_wait(ctr(a, CT_PR + 32 * (w - 2) + h), min(s + w - 1, (unsigned)NSEG), abortw, lane, dead);
    if (res & 17u) {
      spins |= 63u;
      if (dead || poll_fail(spins, abortw, lane)) dead = true;
    }
  }
  if (w == 2 && lane == 0) st4(rh, H_FLG + 12 * CHK + h * 128, NSEG - 1);
  PDUMP(0);
}

__device__ __forceinline__ void role_vo(const ChainArgs& a, int h, char* smem, const int w, Gate& G) {
  int lane = opaque_tid(0);
  const int b = 32 + h; (void)b;
  u64* prof = a.prof; (void)prof;
  PDECL(L_PRF, 4);
  GAS unsigned* abortw = (GAS unsigned*)(a.sync + SY_ABORT);
  bool dead = false;
  Wts W;
  Clk ck; ck.init();
  load_weights<1>(a, h, w, lane, smem, W);
  if (!G.pass(a.sync)) return;
  PZERO;
  const Sel sel = sel_wait(a.sync, abortw, lane, dead, true);
  const __amdgpu_buffer_rsrc_t rh = rsrc_hot(a.sync, sel), rum = rsrc_u(a.sync);
  u32x16 C, P;
  {
    const int r = lane & 15, g = lane >> 4;
    const unsigned l0 = (unsigned)(uintptr_t)(LAS char*)smem;
#pragma unroll
    for (int i = 0; i < 16; ++i) C[i] = H_TR + copy_q((lane >> 3) & 3, h >> 3, sel) + (lane & 7) * 128;
    C[0] = lane * 16; C[1] = l0 + L_MEM + lane * 16; C[2] = l0 + L_WTS + w * 4096 + lane * 16; C[3] = C[2] + 65536;
    C[4] = (lane & 2) ? 0xEEEE : 0x4444;
    C[5] = l0 + L_XCH + (32 * w + r) * 16 + g * 4;
    C[6] = (lane & 8) ? 0xEEEE : 0x4444;
    C[7] = l0 + L_XCH + (32 * (g >> 1) + 8 * (g & 1) + (r >> 2)) * 16 + (r & 1) * 8;
    C[8] = (lane >> 5) * 8 * CHK + (lane & 31) * 16;
    C[11] = l0 + L_PRF + w * 1024 + (lane >> 6);
    asm volatile("" : "+v"(C));
  }
  SSTAMP(128 + h);
  steps_init(P, h, w, um_place(8 * (h & 3) + 2 * w, h >> 3) + (h & 7) * 512, sel);
  unsigned spins = 0;
  while (true) {
    u32x8 X; u32x16 pv; (void)pv;
    steps_enter(P, ck, dead);
    STEPS(VO_BODY, , [um] "s"(sel[1] >> 8));
    keep_a(W);
    lane = opaque_tid(0);
    if (__builtin_amdgcn_readfirstlane(P[2]) & 64u) break;
    spins |= 63u;
    if (dead || poll_fail(spins, abortw, lane)) dead = true;
  }
  PDUMP(0);
}

__device__ __forceinline__ int timed(__amdgpu_buffer_rsrc_t rs, unsigned at) {
  u64 c0, c1;
  asm volatile("s_waitcnt vmcnt(0)\n\ts_memtime %0\n\ts_waitcnt lgkmcnt(0)" : "=s"(c0) :: "memory");
  unsigned v = __builtin_amdgcn_raw_buffer_load_b32(rs, 0, at, LD_AUX);
  asm volatile("s_waitcnt vmcnt(0)\n\ts_memtime %0\n\ts_waitcnt lgkmcnt(0)" : "=s"(c1), "+v"(v) :: "memory");
  const int t = (int)(c1 - c0);
  return t < 320 ? 4000 : t;
}
__device__ __forceinline__ unsigned calibrate(const ChainArgs& a, int cal, u64 t0) {
  const __amdgpu_buffer_rsrc_t rs = mk_rsrc((void*)a.sync, SY_SET);
  int best[2] = {4000, 4000};
  bool sure = false;
  for (int i = 0; i < 32 && !sure && __builtin_amdgcn_s_memrealtime() - t0 < CAL_A; ++i) {
#pragma unroll
    for (int hf = 0; hf < 2; ++hf) best[hf] = min(best[hf], timed(rs, cal * (PAGE + CHK) + 64 * CHK + hf * 8192 + ((5 * cal + i) & 31) * 256));
    sure = min(best[0], best[1]) < 540;
  }
  return ((best[1] < best[0] ? 1u : 0u) ^ die_half()) + (sure ? 2u : 0u);
}
__device__ __forceinline__ unsigned calibrate8(const ChainArgs& a, unsigned at, u64 t0) {
  const __amdgpu_buffer_rsrc_t rs = mk_rsrc((void*)a.sync, SY_SET);
  int hf = 0, t[4] = {4000, 4000, 4000, 4000};
  for (; hf < 32 && timed(rs, at + (hf & 3) * 8192 + 2048 + (((hf >> 2) + (at >> 15)) & 7) * 256) >= 540; ++hf) if (__builtin_amdgcn_s_memrealtime() - t0 >= CAL_T) hf = 32;
  bool ok = false;
  for (int i = 0; i < 8 && hf < 32 && !ok && __builtin_amdgcn_s_memrealtime() - t0 < CAL_T; ++i) {
#pragma unroll
    for (int k = 0; k < 4; ++k) t[k] = min(t[k], timed(rs, at + (hf & 1) * 8192 + quarter(k) + ((i + (at >> 15)) & 7) * 256));
    ok = i && max(max(t[0], t[1]), max(t[2], t[3])) < 540;
  }
  const unsigned xc = (unsigned)__builtin_amdgcn_s_getreg(20 | (3 << 11));
  const int d12 = min(max(t[0] - t[1], -30), 30) + min(max(t[2] - t[3], -30), 30), d14 = min(max(t[0] - t[2], -30), 30) + min(max(t[1] - t[3], -30), 30);
  ok = ok && !(xc & 4);
  return ((hf & 1) ^ die_half()) | (ok ? 2u : 0u) | (64 + (ok ? ((xc & 2) ? -d12 : d12) : 0)) << 8 | (64 + (ok ? ((xc & 1) ? -d14 : d14) : 0)) << 16;
}
__device__ __forceinline__ void bg_drain() {
  asm volatile("s_waitcnt vmcnt(0)" ::: "memory");
  __builtin_amdgcn_s_barrier();
  asm volatile("" ::: "memory");
}

constexpr int LC_SUM = 65536, LC_TQ = LC_SUM + 8192, LC_OUT = 0, LC_PRF = 98304;
__device__ __forceinline__ void role_cb(const ChainArgs& a, int ci, int par, char* smem, const int w, Gate& G) {
  int lane = opaque_tid(0);
  const int b = 64 + 32 * par + ci, j = ci >> 3; (void)b;
  u64* prof = a.prof; (void)prof;
  PDECL(LC_PRF, 8);
  GAS unsigned* abortw = (GAS unsigned*)(a.sync + SY_ABORT);
  bool dead = false;
  const unsigned l0 = (unsigned)(uintptr_t)(LAS char*)smem;
  const int tq = 2 * ci + (w & 1);
  auto fine = [](int n) { return (n % 3 == 2 ? 32 : n % 3) * PAGE + (n / 3 + 1) * 32768; };
  unsigned m0 = 0, hs = 0;
  if (w == 1 || (w == 2 && !par)) {
    const unsigned x = __builtin_amdgcn_raw_buffer_load_b32(mk_rsrc((void*)a.sync, SY_SET), par ? (ci ^ 2) * (PAGE + CHK) + 64 * CHK + (lane & 1) * 8192 + (lane >> 1) * 256 : fine(ci ^ 2) + (lane & 7) * 4096 + (lane >> 3) * 256 + (w - 1) * 2048, 0, 0);
    asm volatile("" :: "v"(x));
  }
  if (w == 3 && !par) {
    const unsigned x = __builtin_amdgcn_raw_buffer_load_b32(mk_rsrc((void*)a.X, 4u << 20), (ci * 64 + lane) * 128, 0, 0);
    asm volatile("" :: "v"(x));
  }
  if (w == 0) hs = par ? calibrate8(a, fine(ci), G.entry) : calibrate(a, ci, G.entry);
  __builtin_amdgcn_s_barrier();
  m0 = nosent(*(const unsigned*)(a.mem0 + (2 * (lane >> 4) + (lane & 1)) * DM + 16 * tq + (lane & 14)));
  Clk ck; ck.init();
  if (!G.pass(a.sync)) return;
  SSTAMP(0 + ci);
  if ((w | lane) == 0) __hip_atomic_store(ctr(a, CT_CAL + b - 64), hs, __ATOMIC_RELAXED, __HIP_MEMORY_SCOPE_AGENT);
  auto init = [&](Sel sl) {
    const unsigned s1v = ~(unsigned)(lane >> 6);
    const u32x4 sv = {s1v, s1v, s1v, s1v};
#pragma unroll
    for (int i = 0; i < 6; ++i) st16(rsrc_hot(a.sync, sl), i < 4 || ci < 18 ? (ci + 16 * (i & 6)) * CHK + w * 2048 + (i & 1) * 1024 + lane * 16 : OOR, sv);
#pragma unroll
    for (int i = 0; i < 8; ++i) st16(rsrc_u(a.sync), (4 * ci + w) * CHK + (sl[0] & 1u) * 8192 + i * 1024 + lane * 16, sv);
    bg_drain();
    if (w == 0 && lane == 0) ctr_add(a, CT_INIT, 1u);
  };
  int told = 0;
  if (b == 65 && w == 3) {
    unsigned spins = 0, v;
    int d;
    auto cnt = [](bool x) { return __builtin_popcountll(__builtin_amdgcn_ballot_w64(x)); };
    while (true) {
      v = __hip_atomic_load(ctr(a, CT_CAL + lane), __ATOMIC_RELAXED, __HIP_MEMORY_SCOPE_AGENT);
      const unsigned u = v == SENT ? 0u : (v & 3u) ^ (lane < 32 ? lane & 1 : lane % 3 == 0), f = lane >= 32 && v != SENT && (v & 2u) ? lane % 3 : 3;
      const int n0 = cnt(u == 2u), n1 = cnt(u == 3u);
      d = max(n0, n1) >= 8 + 4 * min(n0, n1) ? n1 - n0 : 0;
      if (d && !told) {
        told = d;
        const u64 m = __builtin_amdgcn_ballot_w64(((lane ^ (d > 0) ^ die_half()) & 1u) != 0 && lane < 33) | 1ull << 62;
        if (lane == 0) __hip_atomic_store((GAS u64*)(a.sync + SY_SEL), m, __ATOMIC_RELAXED, __HIP_MEMORY_SCOPE_AGENT);
        init((Sel){(unsigned)m, (unsigned)(m >> 32)});
      }
      d = told;
      if ((d && min(min(cnt(f == 0), cnt(f == 1)), cnt(f == 2)) >= CAL_N) || __all(v != SENT) || poll_fail(spins, abortw, lane)) break;
      __builtin_amdgcn_s_sleep(4);
    }
    if (v == SENT) v = 0x404000u;
    unsigned key[3] = {};
    int vote[3][2] = {};
    for (int n = 0; n < 32; ++n) {
      const unsigned x = __builtin_amdgcn_readlane(v, 32 + n);
#pragma unroll
      for (int k = 0; k < 3; ++k) if (n % 3 == k) { vote[k][0] += (int)((x >> 8) & 255u) - 64; vote[k][1] += (int)((x >> 16) & 255u) - 64; }
    }
#pragma unroll
    for (int k = 0; k < 3; ++k) key[k] = ((vote[k][0] > 0) + 2 * (vote[k][1] > 0)) ^ ((KEYX >> (2 * k)) & 3);
    const int h32 = cnt((v & 3) == 3 && lane >= 32 && lane % 3 == 1) - cnt((v & 3) == 2 && lane >= 32 && lane % 3 == 1);
    if (lane == 32) v = h32 > 0;
    if (d) v = lane ^ (d > 0);
    u64 m = __builtin_amdgcn_ballot_w64(((v ^ die_half()) & 1u) != 0 && lane < 33);
    m |= (u64)key[2] << 36;
#pragma unroll
    for (int t = 0; t < 4; ++t) m |= (u64)(((key[t >> 1] ^ t) & 1u) | ((m >> (t >> 1)) & 1u) << 1 | (key[t >> 1] & 2u) << 1) << (40 + 4 * t);
    if (lane == 0) __hip_atomic_store((GAS u64*)(a.sync + SY_SEL), m, __ATOMIC_RELAXED, __HIP_MEMORY_SCOPE_AGENT);
  }
  if (!par && !told) init(sel_wait(a.sync, abortw, lane, dead));
  if (w == 0) sel_wait(a.sync, abortw, lane, dead, true);
  lds_barrier();
  const Sel sel = sel_wait(a.sync, abortw, lane, dead, true);
  const __amdgpu_buffer_rsrc_t rh = rsrc_hot(a.sync, sel), rum = rsrc_u(a.sync), ro = mk_rsrc(a.out, 8u << 20);
  const unsigned cq = hot_q((ci >> 1) & 3, sel), wq = copy_q(w, (ci >> 1) & 3, sel);
  if (!par && w < 2) st4(rh, H_MEM + cq + j * 1024 + (tq & 3) * 256 + (4 * (lane >> 4) + (lane & 3)) * 16 + ((lane >> 2) & 3) * 4, m0);
  PZERO;
  SSTAMP(32 + ci);
  u32x16 C, P; u32x8 C2;
  {
    const int i = lane & 15, g = lane >> 4;
    const unsigned mo = H_MEM + cq + j * 1024 + (ci & 1) * 512 + w * 128;
    C[0] = lane * 16; C[1] = (lane >> 5) * 2048 + (lane & 31) * 16;
    C[2] = ((i >> 3) ^ (j & 1)) * 512 + (i & 4) * 64 + g * 64 + (i & 3) * 16;
    C[3] = H_PZ + wq + (((g & 2) + (i >> 3) + 4 * (g & 1)) ^ j) * 256 + (i & 4) * 32 + (i & 3) * 16;
    C[4] = H_PZ + wq + ((g + 4 * (i >> 3)) ^ j) * 256 + (i & 4) * 32 + 64 + (i & 3) * 16;
    C[5] = (lane & 8) ? 0xEEEE : 0x4444; C[6] = (lane & 2) ? 0xEEEE : 0x4444;
    C[7] = l0 + LC_SUM + w * 1024 + lane * 4;
    C[8] = l0 + LC_SUM + (w >> 1) * 4096 + (2 * (w & 1) + (lane >> 5)) * 256 + ((2 * (lane & 1) + ((lane >> 3) & 1)) * 16 + 2 * (lane & 6) + 2 * ((lane >> 4) & 1)) * 4;
    C[9] = (lane & 1) ? OOR : mo + lane * 2;
    C[10] = H_ANN + wq + (lane & 7) * 128;
    C[11] = ((ci & 3) == w && lane < 4) ? H_TR + ((ci >> 1) & 3) * 16 * CHK + quarter(lane) + (2 * j + (ci & 1)) * 128 : OOR;
    C[12] = H_TR + copy_q((lane >> 1) & 3, (ci >> 1) & 3, sel) + (2 * w + (lane & 1)) * 128; C[13] = (ci & 3) == w ? (lane & 31) * 128 : OOR;
    C[14] = (par | lane) == 0 && w == 1 ? SY_CT + (CT_PR + 96 + ci) * 128 : OOR;
    C[15] = l0 + LC_OUT + w * 4096 + (g * 16 + ((i + 4 * g) & 15)) * 16;
    C2[0] = l0 + LC_OUT + (((lane >> 1) & 2) + (w >> 1)) * 1024 + ((lane & 3) * 16 + ((8 * w + (lane >> 3) + 4 * (lane & 3)) & 15)) * 16;
    C2[1] = (8 * w + (lane >> 3)) * OUTW * 2 + DM * 2 + ci * 64 + (lane & 7) * 8;
    C2[2] = SY_PU + (sel[0] & 2u) * 4096 + (8 * w + (lane & 7)) * 128;
    C2[3] = lane < 8 ? mo + lane * 16 : OOR;
    C2[4] = (w | lane) == 0 ? ci * 128 : OOR;
    C2[5] = l0 + LC_TQ + 4 * w + (lane >> 6); C2[6] = 20000 + (lane >> 6); C2[7] = l0 + LC_TQ + (lane >> 6);
  }
#pragma unroll
  for (int i = 0; i < 16; ++i) P[i] = 0;
  P[0] = um_place(ci, w) + j * 512; P[1] = H_P + wq + (j & 2) * 512; P[8] = um_at(par + 2, P[0], sel);
  P[2] = (8 * w + j) * UXH + ux_tile(2 * ci); P[3] = H_PX + 2 * w * CHK + j * 2048; P[4] = par - 2; P[5] = P[7] = K_DEF * TU;
  P[6] = ck.c2t << 15; P[11] = ck.ce; P[12] = ck.t2c; P[13] = ck.te; P[15] = H_DLT + par * 4096;
#pragma unroll
  for (int i = 0; i < 16; ++i) P[i] = __builtin_amdgcn_readfirstlane(P[i]);
  u32x16 pv; (void)pv;
  const unsigned C24 = H_FLG + (8 * w + (lane & 7)) * 128;
  asm volatile(CB_BODY : "+&{s[40:55]}"(P) STEP_PV, "+&{v[224:239]}"(C), "+&{v[240:247]}"(C2) : "s"(0) CB_PV, [rh] "s"(rh), [ru] "s"(rum), [ro] "s"(ro), [sel] "s"(sel[0]), [um] "s"(sel[1] >> 8), [mg] "s"(H_FLG + 4096 - 20000)
               : "memory", "vcc", "scc", S22, V90, V10(9), V10(10), V10(11), V10(12), V10(13), V10(14), V10(15), V10(16), V10(17), V10(18), V10(19), V10(20), V10(21),
                 "v220", "v221", "v222", "v223", "s98", "s99", "s100", "s101", A10(), A10(1), A10(2), A10(3), A10(4), A10(5), "a60", "a61", "a62", "a63");
  lane = opaque_tid(0);
  PDUMP(2);
}

__device__ __forceinline__ void st16g(u16* p, u32x4 v, bool sc1) {
  if (sc1) asm volatile("global_store_dwordx4 %0, %1, off " OUTP "\n\ts_nop 1" :: "v"(p), "v"(v) : "memory");
  else *(u32x4*)p = v;
}
__device__ __forceinline__ u32x4 pk8(f32x4 a, f32x4 b) { return (u32x4){pk2(a[0], a[1]), pk2(a[2], a[3]), pk2(b[0], b[1]), pk2(b[2], b[3])}; }
__device__ __forceinline__ void role_p(const ChainArgs& a, int kind, int h, char* smem, const int w, Gate& G) {
  const int lane = opaque_tid(0), r = lane & 15, g = lane >> 4;
  const u32x4 rw = own_rsrc(kind == 0 ? a.Wq : (kind == 1 ? a.Wk : a.Wv), 8u << 20), rx = own_rsrc(a.X, 4u << 20);
  const unsigned lds0 = (unsigned)(uintptr_t)(LAS char*)smem + w * 4096;
  unsigned vw[4], vx[4];
#pragma unroll
  for (int i = 0; i < 4; ++i) {
    const int row = (w * 4 + i) * 4 + g, xr = 8 * w + 2 * i + (lane >> 5);
    vw[i] = row * HD * 2 + ((r ^ swz_b(row)) * 16);
    vx[i] = xr * DM * 2 + (((lane & 31) ^ (xr & 15)) * 16);
  }
  auto dma = [&](u32x4 rs, unsigned v, unsigned so, unsigned ld, bool nt = false) {
    if (nt) asm volatile("s_mov_b32 m0, %0\n\ts_nop 3\n\tbuffer_load_dwordx4 %1, %2, %3 offen nt lds" :: "s"(ld), "v"(v), "s"(rs), "s"(so) : "m0", "memory");
    else asm volatile("s_mov_b32 m0, %0\n\ts_nop 3\n\tbuffer_load_dwordx4 %1, %2, %3 offen lds" :: "s"(ld), "v"(v), "s"(rs), "s"(so) : "m0", "memory");
  };
  constexpr int SL = 16384 + MS * 4096, NS = CHAIN_LDS / SL, NI = 4 + MS;
  const unsigned vy = (lane >> 3) * DM * 2 + (((lane & 7) ^ (lane >> 4) ^ ((w & 1) * 4)) * 16);
  auto item = [&](int t) {
#pragma unroll
    for (int i = 0; i < 4; ++i) dma(rw, vw[i], (unsigned)((64 * (t ^ PREV) * HD + 128 * h) * 2), lds0 + (t % NS) * SL + i * 1024, (NTW >> 2) & 1);
#pragma unroll
    for (int i = 0; i < MS; ++i) dma(rx, vy, (unsigned)((32 * i + 8 * w) * DM * 2 + (t ^ PREV) * 128), lds0 - w * 3072 + (t % NS) * SL + 16384 + i * 4096);
  };
  Wts W;
#pragma unroll
  for (int i = 0; i < 32; ++i) asm volatile("" : "=a"(W.a[i]));
  if (kind < 2) while (__builtin_amdgcn_s_memrealtime() - G.entry < (kind ? K_DLY : Q_DLY)) __builtin_amdgcn_s_sleep(8);
#pragma unroll
  for (int t = 0; t < NS; ++t) item(t);
  f32x4 acc[2 * MS][2];
  u64 ep = 0;
#pragma unroll
  for (int t = 0; t < 16; ++t) {
    asm volatile("s_waitcnt vmcnt(%0)" :: "n"(t == 0 ? NI * (NS - 1) : (t < 17 - NS ? NI * (NS - 2) : NI * (15 - t) + 1)) : "memory");
    lds_barrier();
    if (t && t < 17 - NS) item(t + NS - 1);
    if (t == 16 - NS) ep = __hip_atomic_load((GAS u64*)(a.sync + SY_EPOCH), __ATOMIC_RELAXED, __HIP_MEMORY_SCOPE_AGENT);
    u32x4 f[4];
#pragma unroll
    for (int n = 0; n < 8; ++n) {
      const int row = 32 * (n >> 2) + 8 * g + 4 * (n & 1) + (r >> 2);
      const u32x2 u = __builtin_bit_cast(u32x2, __builtin_amdgcn_ds_read_tr16_b64_v4i16((LAS bf16x4*)(smem + (t % NS) * SL + row * 256 + (((4 * w + (r & 3)) ^ swz_b(row)) * 16) + 4 * (n & 2))));
      f[n >> 1][2 * (n & 1)] = u[0]; f[n >> 1][2 * (n & 1) + 1] = u[1];
    }
    u32x4 x[4 * MS];
#pragma unroll
    for (int q = 0; q < 4 * MS; ++q) x[q] = *(const u32x4*)(smem + (t % NS) * SL + 16384 + (16 * (q >> 1) + r) * 128 + (((4 * (q & 1) + g) ^ (r >> 1)) * 16));
    SB0;
#pragma unroll
    for (int q = 0; q < 16; ++q) asm volatile("v_accvgpr_write_b32 a[%1], %0" :: "v"(f[q >> 2][q & 3]), "n"(16 * (t ^ PREV) + q));
#pragma unroll
    for (int q = 0; q < 8 * MS; ++q) {
      if (t == 0 && !(q & 2)) asm volatile("v_mfma_f32_16x16x32_bf16 %0, %1, %2, 0" : "=&v"(acc[q >> 2][q & 1]) : "v"(f[q & 3]), "v"(x[q >> 1]));
      else asm volatile("v_mfma_f32_16x16x32_bf16 %0, %1, %2, %0" : "+v"(acc[q >> 2][q & 1]) : "v"(f[q & 3]), "v"(x[q >> 1]));
    }
  }
  asm volatile("s_nop 7" : "+v"(acc[2 * MS - 1][0]), "+v"(acc[2 * MS - 1][1]));
  SSTAMP(160 + h);
  if (!G.look(a.sync, ep)) { G.arrive(a.sync); return; }
  const __amdgpu_buffer_rsrc_t rs = rsrc_u(a.sync);
#pragma unroll
  for (int q = 0; q < 2 * MS; ++q) st16s(pk8(acc[q][0], acc[q][1]), rs, lane * 16 + (q & 1) * 4096, (unsigned)(OFF_QX + kind * (32u << 20) + ((q >> 1) * NH + h) * CHK + w * 1024));
  lds_barrier();
#pragma unroll
  for (int t = 0; t < 32; ++t) {
    dma(rx, vx[t & 3], (unsigned)((MS + (t >> 4)) * 65536 + ((t >> 2) & 3) * 512), lds0 + (t >> 2) * 16384 + (t & 3) * 1024);
    if (t == 11) {
      wait_vm<12>();
      __builtin_amdgcn_s_barrier();
      st4(rs, (w | lane) == 0 ? SY_CT + (CT_PR + 32 * kind + h) * 128 : OOR, MS - 1);
    }
  }
  G.arrive(a.sync);
  bool dead = false;
  const Sel sel = sel_wait(a.sync, (GAS unsigned*)(a.sync + SY_ABORT), lane, dead);
  wait_vm<0>();
  lds_barrier();
  u32x16 C;
#pragma unroll
  for (int i = 0; i < 4; ++i) { C[i] = vx[i]; C[4 + i] = lds0 - w * 4096 + r * 512 + (((4 * i + g) ^ r) * 16); C[12 + i] = 0; }
  C[8] = lane * 16; C[9] = lane * 16 + 4096;
  C[10] = 0;
  C[11] = (w | lane) == 0 ? SY_CT + (CT_PR + 32 * kind + h) * 128 : OOR;
  u32x8 P = {(MS + 2) * 65536u, lds0, 0x7f000000u, (unsigned)(OFF_QX + kind * (32u << 20) + MS * 32 * CHK + far_blk(h, sel, FARX >> kind) + w * 1024), MS, 0u, MS - 1u, 0u};
#pragma unroll
  for (int i = 0; i < 8; ++i) P[i] = __builtin_amdgcn_readfirstlane(P[i]);
  asm volatile(P_BODY : "+&{s[40:47]}"(P), "+&{v[160:175]}"(C) : [rx] "s"(rx), [ru] "s"(rs), [wv] "s"(w), [pp] "s"(a.prof + 296000 + (32 * kind + h) * 16), [pq] "s"(a.prof + 300000 + (32 * kind + h) * 48)
               : "memory", "vcc", "scc", "m0", S22, STEP_CL V90, V10(9), V10(10), V10(11), V10(12), V10(13), V10(14), V10(15), "v176", "v177", "v178", "v179", "v180");
  keep_a(W);
}
#define BG_SLP 64
#define A2N 2
constexpr int A2_NCH = A2N;
__device__ __forceinline__ float blo(unsigned d) { return __uint_as_float(d << 16); }
__device__ __forceinline__ float bhi(unsigned d) { return __uint_as_float(d & 0xffff0000u); }
constexpr int A2_CSTR = 4160;
constexpr int A2_UOFF = 8 * A2_CSTR + 16 * 80;
constexpr int A2_WLDS = A2_UOFF + 64 * 80;
static_assert(4 * A2_WLDS + 256 <= CHAIN_LDS - 16, "lds");
__device__ __forceinline__ unsigned kv_row(int d, Sel sel) { return far_blk(d >> 1, sel) + (d & 1) * 4096; }
__device__ __forceinline__ void a2_wave(const ChainArgs& a, int d0, int d1, char* smem, int w, Sel sel) {
  const int lane = opaque_tid(0);
  char* Wb = smem + w * A2_WLDS;
  char* U = Wb + A2_UOFF;
  const __amdgpu_buffer_rsrc_t rf = mk_rsrc((void*)a.Assm, 4u << 20);
  const __amdgpu_buffer_rsrc_t rkv = mk_rsrc((void*)a.kvT, 8u << 20);
  u32x4 hi[4], lo[4], uv[4];
  auto ld_h = [&](int d) {
#pragma unroll
    for (int i = 0; i < 4; ++i) {
      hi[i] = __builtin_amdgcn_raw_buffer_load_b128(rf, (unsigned)((255 - 64 * i - lane) * 16), (unsigned)(d * 4096), 2);
      lo[i] = __builtin_amdgcn_raw_buffer_load_b128(rf, (unsigned)(max(254 - 64 * i - lane, 0) * 16), (unsigned)(d * 4096), 2);
    }
  };
  auto ld_u = [&](int d) {
#pragma unroll
    for (int i = 0; i < 4; ++i) uv[i] = __builtin_amdgcn_raw_buffer_load_b128(rkv, (unsigned)((i * 64 + lane) * 16), kv_row(d, sel), LD_AUX);
  };
  {
    GAS unsigned* abortw = (GAS unsigned*)(a.sync + SY_ABORT);
    bool dead = false;
    ctr_wait<BG_SLP>(ctr(a, CT_A1 + (min(d0, D_A - 1) >> 7)), d0 < d1 ? 8u : 0u, abortw, lane, dead);
  }
  ld_h(d0); ld_u(d0);
  if (lane < 24) *(f32x4*)(Wb + (lane / 3) * A2_CSTR + (256 + lane % 3) * 16) = vzero();
#pragma unroll
  for (int i = 0; i < 2; ++i) if (64 * i + lane < 80) *(f32x4*)(U - 16 * 80 + (64 * i + lane) * 16) = vzero();
  const int r = lane & 15, g = lane >> 4;
#pragma unroll 1
  for (int d = d0; d < d1; ++d) {
#pragma unroll
    for (int i = 0; i < 4; ++i) {
      u32x4 l = lo[i];
      if (i == 3 && lane == 63) l = (u32x4){0u, 0u, 0u, 0u};
      const unsigned src[8] = {l[0], l[1], l[2], l[3], hi[i][0], hi[i][1], hi[i][2], hi[i][3]};
      unsigned f[15];
#pragma unroll
      for (int x = 0; x < 15; ++x) {
        if ((x & 1) == 0) f[x] = __builtin_amdgcn_alignbit(src[7 - x / 2], src[7 - x / 2], 16);
        else f[x] = __builtin_amdgcn_perm(src[7 - (x + 1) / 2], src[7 - (x - 1) / 2], 0x07060100u);
      }
#pragma unroll
      for (int m = 0; m < 8; ++m) *(u32x4*)(Wb + m * A2_CSTR + (64 * i + lane) * 16) = (u32x4){f[7 - m], f[9 - m], f[11 - m], f[13 - m]};
    }
#pragma unroll
    for (int i = 0; i < 4; ++i) {
      const int ch = i * 64 + lane;
      *(u32x4*)(U + (ch >> 2) * 80 + (ch & 3) * 16) = uv[i];
    }
    const float Dd = bf2f(a.AD[d]);
    asm volatile("" ::: "memory");
    ld_h(min(d + 1, d1 - 1)); ld_u(min(d + 1, d1 - 1));
    f32x4 acc[4][2];
#pragma unroll
    for (int i = 0; i < 8; ++i) acc[i >> 1][i & 1] = vzero();
    const char* pa = Wb + (r & 7) * A2_CSTR + 2 * (2040 - (r & 8) + 8 * g) - 3 * 1024 - 32 - 64;
    const char* pb = U + (r - 1) * 80 + 16 * g;
    bf16x8 fa[2][8], fb[2][4];
    auto rd = [&](int b, int n, int k) {
      if (n < 4) fb[b][n] = *(const bf16x8*)(pb + 80 * (16 * n + 1 - k));
      else fa[b][n - 4] = *(const bf16x8*)(pa + 1024 * (3 - ((n - 4) >> 1)) + 32 * (1 - (n & 1)) + 64 * (1 - k));
    };
#pragma unroll
    for (int n = 0; n < 12; ++n) rd(0, n, 0);
#pragma unroll 1
    for (int k = 0; k < 16; k += 2) {
#pragma unroll
      for (int b = 0; b < 2; ++b) {
        int q = 0;
#pragma unroll
        for (int j = 0; j < 4; ++j)
#pragma unroll
          for (int i = j; i < 4; ++i)
#pragma unroll
            for (int mt = 0; mt < 2; ++mt) {
              asm volatile("v_mfma_f32_16x16x32_bf16 %0, %1, %2, %0" : "+v"(acc[i][mt]) : "v"(fa[b][2 * j + mt]), "v"(fb[b][i - j]));
              SB0;
              if (q < 12) rd(b ^ 1, q, 1);
              if (q == 12) { pa -= 64; pb -= 80; }
              ++q;
              SB0;
            }
      }
    }
    asm volatile("s_nop 7" ::: "memory");
#pragma unroll
    for (int i = 0; i < 8; ++i) asm volatile("" : "+v"(acc[i >> 1][i & 1]));
#pragma unroll
    for (int i = 0; i < 8; ++i) {
      char* pz = U + (16 * (i >> 1) + r) * 80 + 32 * (i & 1) + 8 * g;
      const u32x2 kvv = *(const u32x2*)pz;
      const f32x4 y = acc[i >> 1][i & 1];
      *(u32x2*)pz = (u32x2){pk2(fmaf(blo(kvv[0]), Dd, y[0]), fmaf(bhi(kvv[0]), Dd, y[1])), pk2(fmaf(blo(kvv[1]), Dd, y[2]), fmaf(bhi(kvv[1]), Dd, y[3]))};
    }
#pragma unroll
    for (int i = 0; i < 4; ++i) {
      const int ch = i * 64 + lane;
      st16s(*(const u32x4*)(U + (ch >> 2) * 80 + (ch & 3) * 16), rkv, (unsigned)(ch * 16), kv_row(d, sel));
    }
  }
  wait_vm<0>();
  if (lane == 0 && d0 < d1) ctr_add(a, CT_A2 + (d0 >> 7), d1 - d0);
}

__device__ __forceinline__ void task_a1(const ChainArgs& a, int dp, int tq, char* smem, int wv, Sel sel) {
  const int lane = opaque_tid(0);
  const int dg = 2 * dp + (wv & 1), t0 = tq * 256 + (wv >> 1) * 128;
  const int d = dg * 64 + 8 * (lane & 7);
  const int tb = t0 + 8 * (lane >> 3);
  char* T = smem + wv * 16384;
  u32x4 kk[2][11], vv[2][8], sh[4];
#pragma unroll
  for (int i = 0; i < 4; ++i) sh[i] = *(const u32x4*)(a.Ashift + d * 4 + 8 * i);
  SB0;
#pragma unroll
  for (int gq = 0; gq < 2; ++gq) {
#pragma unroll
    for (int i = 0; i < 11; ++i) {
      const int t = tb + 64 * gq - 3 + i;
      kk[gq][i] = LDNT((const u32x4*)(a.Ak + (long)(t < 0 ? 0 : t) * D_A + d));
    }
#pragma unroll
    for (int i = 0; i < 8; ++i) vv[gq][i] = LDNT((const u32x4*)(a.Av + (long)(tb + 64 * gq + i) * D_A + d));
    SB0;
  }
#pragma unroll
  for (int i = 0; i < 3; ++i) if (tb == 0) kk[0][i] = (u32x4){0u, 0u, 0u, 0u};
#pragma unroll
  for (int gq = 0; gq < 2; ++gq) {
#pragma unroll
    for (int c = 0; c < 8; ++c) {
      const unsigned t01 = sh[c >> 1][2 * (c & 1)], t23 = sh[c >> 1][2 * (c & 1) + 1];
      const float s0 = blo(t01), s1 = bhi(t01), s2 = blo(t23), s3 = bhi(t23);
      float kf[11];
#pragma unroll
      for (int i = 0; i < 11; ++i) kf[i] = (c & 1) ? bhi(kk[gq][i][c >> 1]) : blo(kk[gq][i][c >> 1]);
      u32x4 o;
#pragma unroll
      for (int i = 0; i < 8; i += 2) {
        float r[2];
#pragma unroll
        for (int j = 0; j < 2; ++j)
          r[j] = fmaf(kf[i + j + 3], s0, fmaf(kf[i + j + 2], s1, fmaf(kf[i + j + 1], s2, kf[i + j] * s3))) * ((c & 1) ? bhi(vv[gq][i + j][c >> 1]) : blo(vv[gq][i + j][c >> 1]));
        o[i >> 1] = pk2(r[0], r[1]);
      }
      *(u32x4*)(T + (8 * (lane & 7) + c) * 256 + (((gq * 8 + (lane >> 3)) ^ (lane & 7)) * 16)) = o;
    }
    SB0;
  }
  const __amdgpu_buffer_rsrc_t rkv = mk_rsrc((void*)a.kvT, 8u << 20);
#pragma unroll
  for (int i = 0; i < 16; ++i) {
    const int row = 4 * i + (lane >> 4);
    const u32x4 o = *(const u32x4*)(T + row * 256 + (((lane & 15) ^ (row >> 3)) * 16));
    st16(rkv, kv_row(dg * 64 + row, sel) + (t0 + (lane & 15) * 8) * 2, o);
  }
  bg_drain();
  if (opaque_tid(wv) == 0) ctr_add(a, CT_A1 + dp, 1u);
}

__device__ __forceinline__ void task_a3(const ChainArgs& a, int dp, int tq, char* smem, int wv, Sel sel) {
  const int lane = opaque_tid(0);
  const int dg = 2 * dp + (wv & 1), t0 = tq * 256 + (wv >> 1) * 128;
  const int d = dg * 64 + 8 * (lane & 7);
  const int tb = t0 + 8 * (lane >> 3);
  const __amdgpu_buffer_rsrc_t rz = mk_rsrc((void*)a.kvT, 8u << 20);
  u32x4 qq[16], zz[8][2];
#pragma unroll
  for (int i = 0; i < 16; ++i) qq[i] = LDNT((const u32x4*)(a.Aq + (long)(tb + 64 * (i >> 3) + (i & 7)) * D_A + d));
  char* T = smem + wv * 16384;
  {
    u32x4 zr[16];
    bool dead = false;
    ctr_wait<BG_SLP>(ctr(a, CT_A2 + dp), 128u, (GAS unsigned*)(a.sync + SY_ABORT), lane, dead);
#pragma unroll
    for (int i = 0; i < 16; ++i) zr[i] = ld16(rz, kv_row(dg * 64 + 4 * i + (lane >> 4), sel) + (t0 + (lane & 15) * 8) * 2);
#pragma unroll
    for (int i = 0; i < 16; ++i) {
      const int row = 4 * i + (lane >> 4);
      *(u32x4*)(T + row * 256 + (((lane & 15) ^ (row >> 3)) * 16)) = zr[i];
    }
  }
#pragma unroll
  for (int c = 0; c < 8; ++c)
#pragma unroll
    for (int gq = 0; gq < 2; ++gq) zz[c][gq] = *(const u32x4*)(T + (8 * (lane & 7) + c) * 256 + (((gq * 8 + (lane >> 3)) ^ (lane & 7)) * 16));
#pragma unroll
  for (int i = 0; i < 16; ++i) {
    u32x4 o;
#pragma unroll
    for (int c = 0; c < 8; c += 2) {
      const unsigned z0 = zz[c][i >> 3][(i >> 1) & 3], z1 = zz[c + 1][i >> 3][(i >> 1) & 3];
      o[c >> 1] = pk2(blo(qq[i][c >> 1]) * ((i & 1) ? bhi(z0) : blo(z0)), bhi(qq[i][c >> 1]) * ((i & 1) ? bhi(z1) : blo(z1)));
    }
    st16g(a.out + (long)(tb + 64 * (i >> 3) + (i & 7)) * OUTW + d, o, true);
  }
}

__device__ __forceinline__ void role_a(const ChainArgs& a, char* smem, int wv) {
  constexpr int N2 = 256 / A2_NCH;
  bool dead = false;
  const Sel sel = sel_wait(a.sync, (GAS unsigned*)(a.sync + SY_ABORT), opaque_tid(0), dead);
  while (true) {
    asm volatile("" : "+s"(wv));
    int toff = CHAIN_LDS - 16;
    asm volatile("" : "+v"(toff));
    volatile LAS int* tslot = (volatile LAS int*)(smem + toff);
    if (opaque_tid(wv) == 0) *tslot = (int)__hip_atomic_fetch_add(ctr(a, CT_QUEUE), 1u, __ATOMIC_RELAXED, __HIP_MEMORY_SCOPE_AGENT) + 1;
    __syncthreads();
    const int t = __builtin_amdgcn_readfirstlane(*tslot);
    __syncthreads();
    if (t >= 128 + N2) break;
    if (t < 64) task_a1(a, t >> 3, t & 7, smem, wv, sel);
    else if (t < 64 + N2) a2_wave(a, A2_NCH * (4 * (t - 64) + wv), A2_NCH * (4 * (t - 64) + wv + 1), smem, wv, sel);
    else task_a3(a, (t - 64 - N2) >> 3, t & 7, smem, wv, sel);
  }
}
__device__ __forceinline__ char* lds_base(char* smem, int off) { asm volatile("" : "+v"(off)); return smem + off; }
__device__ __forceinline__ void role_u(const ChainArgs& a, int h, char* smem, int w, Gate& G) {
  int lane = opaque_tid(0);
  GAS unsigned* abortw = (GAS unsigned*)(a.sync + SY_ABORT);
  bool dead = false;
  char* sb = smem + w * 32768;
  const __amdgpu_buffer_rsrc_t rwo = mk_rsrc(a.Wo, 8u << 20);
  const unsigned lds0 = (unsigned)(uintptr_t)(LAS char*)sb;
  unsigned wo4[4];
#pragma unroll
  for (int i = 0; i < 4; ++i) { const int row = i * 4 + (lane >> 4); wo4[i] = row * DM * 2 + (((lane & 15) ^ swz_b(row)) * 16); }
  asm volatile("s_nop 4");
  auto stage = [&](int p) {
#pragma unroll
    for (int i = 0; i < 16; ++i)
      dma16(rwo, wo4[i & 3], (unsigned)(((128 * h + 64 * (p & 1) + 16 * (i >> 2)) * DM + 256 * w + 128 * (p >> 1)) * 2), lds0 + (p & 1) * 16384 + i * 1024, (NTW >> 3) & 1);
  };
  __builtin_amdgcn_s_waitcnt(0x0F70);
  Wts W;
#pragma unroll
  for (int i = 0; i < 32; ++i) asm volatile("" : "=a"(W.a[i]));
  stage(0); stage(1);
#pragma unroll
  for (int p = 0; p < 4; ++p) {
    if (p < 3) wait_vm<16>(); else wait_vm<0>();
#pragma unroll
    for (int t = 0; t < 8; ++t) {
      const u32x8 f = tr_frag(sb + (p & 1) * 16384, t, lane);
#pragma unroll
      for (int i = 0; i < 8; ++i) asm volatile("v_accvgpr_write_b32 a[%1], %0" :: "v"(f[i]), "n"(16 * (8 * (p >> 1) + t) + 8 * (p & 1) + i));
    }
    asm volatile("s_waitcnt lgkmcnt(0)" ::: "memory");
    if (p + 2 < 4) stage(p + 2);
  }
  SSTAMP(192 + h);
  {
    while (__builtin_amdgcn_s_memrealtime() - G.entry < XS_T) __builtin_amdgcn_s_sleep(8);
    const __amdgpu_buffer_rsrc_t rx = mk_rsrc((void*)a.X, 4u << 20);
    unsigned x[4];
#pragma unroll
    for (int i = 0; i < 4; ++i) x[i] = __builtin_amdgcn_raw_buffer_load_b32(rx, (h * 256 + w * 64 + lane) * 128, i << 20, 0);
#pragma unroll
    for (int i = 0; i < 4; ++i) asm volatile("" :: "v"(x[i]));
  }
  if (!G.pass(a.sync)) return;
  const Sel sel = sel_wait(a.sync, abortw, lane, dead);
  const __amdgpu_buffer_rsrc_t ru = rsrc_u(a.sync);
  volatile LAS int* fl = (volatile LAS int*)(smem + 16384);
  int s = 0;
  while (true) {
    lane = opaque_tid(0);
    const unsigned lo = SY_CT + (CT_PR + 64 + 32 * (lane & 1) + h) * 128, sg = (w | lane) == 0 ? SY_PU + (sel[0] & 2u) * 4096 + h * 128 : OOR;
    bg_drain();
    st4(ru, sg, s - 1);
    if (s >= NSEG) break;
    if (w == 0) {
      unsigned spins = 0;
      int can;
      while (true) {
        const unsigned cv = ld4(ru, lane < 2 ? lo : OOR);
        can = min(__builtin_amdgcn_readlane(cv, 0), __builtin_amdgcn_readlane(cv, 1) + URING - 1);
        if (can >= min(s + (s < 4 ? U_AH : U_AH2), NSEG - 1)) break;
        if (s < 4) asm volatile("s_sleep 3" ::: "memory"); else asm volatile("s_sleep %0" :: "n"(U_SLP) : "memory");
        if (dead || poll_fail(spins, abortw, lane)) { dead = true; can = NSEG; break; }
      }
      fl[1] = can;
    }
    lds_barrier();
#pragma unroll
    for (int i = 0; i < 2; ++i) *(u32x4*)(smem + w * 2048 + i * 1024 + lane * 16) = ld16(ru, OFF_VX + (far_blk(s * NH + h, sel) & (s < 4 ? ~8192u : ~0u)) + w * 2048 + i * 1024 + lane * 16);
    u32x8 P = {(unsigned)s, (unsigned)fl[1], h * UXH + ux_tile(16) * w, (unsigned)(OFF_VX + far_blk(h, sel) + w * 2048), 0x7f000000u, 0u, 0u, 0u};
#pragma unroll
    for (int i = 0; i < 8; ++i) P[i] = __builtin_amdgcn_readfirstlane(P[i]);
    const unsigned l0 = lds0 - w * 32768;
    u32x8 C = {(unsigned)lane * 16, l0 + lane * 16, l0 + w * 2048 + lane * 16, lo, sg, l0 + 16416 + 4 * w + (lane >> 6), l0 + 16416 + (lane >> 6), 0u};
    lds_barrier();
    asm volatile(U_BODY : "+&{s[40:47]}"(P), "+&{v[80:87]}"(C) : [ru] "s"(ru), [sel] "s"(sel[0]), [wv] "s"(w), [pp] "s"(a.prof + 297600 + h * 64 + 0)
                 : "memory", "vcc", "scc", "s76", "s77", "s78", "s79", "s80", "s81", V10(), V10(1), V10(2), V10(3), V10(4), V10(5), V10(6), V10(7));
    keep_a(W);
    s = __builtin_amdgcn_readfirstlane(P[0]);
  }
}

__global__ __launch_bounds__(256, 1) void chain_kernel(ChainArgs a) {
  extern __shared__ __attribute__((aligned(16))) char smem[];
  unsigned again = blockIdx.x;
  asm volatile("" : "+s"(again));
  const int b = again & 255;
  again >>= 8;
  {
    u32x4 rc;
    asm volatile("s_getpc_b64 s[60:61]\n\t.Lchain_here:\n\ts_sub_u32 s60, s60, .Lchain_here-_Z12chain_kernel9ChainArgs\n\ts_subb_u32 s61, s61, 0\n\ts_and_b32 s61, s61, 0xffff\n\t"
                 "s_mov_b32 s62, .Lchain_end-_Z12chain_kernel9ChainArgs\n\ts_mov_b32 s63, 0x20000\n\t"
                 "buffer_load_dwordx4 a[252:255], %1, s[60:63], 0 offen\n\tbuffer_load_dwordx4 a[252:255], %1, s[60:63], s63 offen"
                 : "={s[60:63]}"(rc) : "v"((b >> 3) * 4096 + threadIdx.x * 16) : "scc", "a252", "a253", "a254", "a255");
  }
  int w = __builtin_amdgcn_readfirstlane(threadIdx.x >> 6);
  Gate G;
  G.entry = __builtin_amdgcn_s_memrealtime(); G.open = again != 0; G.arr = w == 0 && !again ? b & 7 : -1;
  G.rot = (again ? again : (unsigned)__builtin_amdgcn_s_getreg(20 | (3 << 11)) - b + WRONG) & 7u;
  {
    volatile LAS unsigned* t = (volatile LAS unsigned*)smem;
    if (w == 0) { t[0] = (unsigned)G.entry; t[1] = (unsigned)(G.entry >> 32); }
    __syncthreads();
    G.entry = (u64)(unsigned)__builtin_amdgcn_readfirstlane(t[0]) | ((u64)(unsigned)__builtin_amdgcn_readfirstlane(t[1]) << 32);
    __syncthreads();
  }
  if (b == 0 && !again) {
    const int lane = opaque_tid(0);
    const __amdgpu_buffer_rsrc_t rs = mk_rsrc(a.sync + SY_ABORT, SY_BYTES - SY_ABORT);
    for (int i = w; i * 1024 < SY_BYTES - SY_ABORT; i += 4) st16(rs, i * 1024 + lane * 16, (u32x4){SENT, SENT, SENT, SENT});
    bg_drain();
    if (w == 0 && lane == 0) __hip_atomic_store((GAS u64*)(a.sync + SY_EPOCH), (__builtin_amdgcn_s_memrealtime() & ~7ull) | G.rot, __ATOMIC_RELAXED, __HIP_MEMORY_SCOPE_AGENT);
  }
  {
    const unsigned rot = G.rot;
    const int x = (b + rot) & 7, j = ((b >> 3) + 3) & 31, i = j >> 2, role = (x & 4) + (j & 3), q = 2 * (x & 1) + (x >> 1);
    if ((role == 3 || role == 4) && w >= 2) {
      const int n = opaque_tid(0);
      const unsigned x0 = __builtin_amdgcn_raw_buffer_load_b32(mk_rsrc(a.sync, WS_USED), (n + 64 * (w - 2)) * PAGE + 4096 + j * 128, 0, 2);
      const unsigned x1 = __builtin_amdgcn_raw_buffer_load_b32(mk_rsrc(a.out, 8u << 20), n * PAGE + 4096 + j * 128, 0, 2);
      asm volatile("" :: "v"(x0), "v"(x1));
    }
    if (role == 0) role_qk(a, 8 * q + i, smem, w, G);
    else if (role == 1) role_vo(a, 8 * q + i, smem, w, G);
    else if (role < 4) role_cb(a, 8 * (i >> 1) + 2 * q + (i & 1), role - 2, smem, w, G);
    else if (role < 7) role_p(a, role - 4, 8 * (x - 4) + i, smem, w, G);
    else role_u(a, 8 * (x - 4) + i, smem, w, G);
    if (G.rot != rot) {
      __syncthreads();
      asm volatile("s_getpc_b64 s[60:61]\n\t.Lchain_back:\n\ts_sub_u32 s60, s60, .Lchain_back-_Z12chain_kernel9ChainArgs\n\ts_subb_u32 s61, s61, 0\n\ts_setpc_b64 s[60:61]"
                   :: "{s[0:1]}"(__builtin_amdgcn_kernarg_segment_ptr()), "{s2}"(b | (8u | G.rot) << 8), "{v0}"(opaque_tid(w)) : "s60", "s61", "scc", "memory");
      __builtin_unreachable();
    }
  }
  if ((b + G.rot) & 4) role_a(a, smem, w);
  asm volatile(".Lchain_end:");
}

extern "C" void kernel_launch(void* const* d_in, const int* in_sizes, int n_in,
                              void* d_out, int out_size, void* d_ws, size_t ws_size,
                              hipStream_t stream) {
  char* ws = (char*)(((uintptr_t)d_ws + (2u << 20) - 1) & ~(uintptr_t)((2u << 20) - 1));
  hipFuncSetAttribute((const void*)chain_kernel, hipFuncAttributeMaxDynamicSharedMemorySize, CHAIN_LDS);
  ChainArgs ca;
  ca.prof = (u64*)((char*)d_ws + PROF_AT);
  ca.Aq = (const u16*)d_in[0]; ca.Ak = (const u16*)d_in[1]; ca.Av = (const u16*)d_in[2];
  ca.Ashift = (const u16*)d_in[3]; ca.Assm = (const u16*)d_in[4]; ca.AD = (const u16*)d_in[5];
  ca.X = (const u16*)d_in[6]; ca.mem0 = (const u16*)d_in[7];
  ca.Wq = (const u16*)d_in[8]; ca.Wk = (const u16*)d_in[9]; ca.Wv = (const u16*)d_in[10]; ca.Wo = (const u16*)d_in[11];
  ca.out = (u16*)d_out; ca.sync = ws; ca.kvT = (u16*)(ws + OFF_KVT);
  chain_kernel<<<256, 256, CHAIN_LDS, stream>>>(ca);
}
```

```cpp
#include <hip/hip_runtime.h>
#include <hip/hip_bf16.h>
#include <stdint.h>
#include <type_traits>

constexpr int S_LEN = 2048;
constexpr int D_A   = 1024;
constexpr int DM    = 1024;
constexpr int NH    = 32;
constexpr int DH    = 128;
constexpr int HD    = NH * DH;
constexpr int MEMR  = 8;
constexpr int SEG   = 32;
constexpr int NSEG  = S_LEN / SEG;
constexpr int OUTW  = 2048;

typedef unsigned short u16;
typedef unsigned long long u64;
typedef short bf16x8 __attribute__((ext_vector_type(8)));
typedef short bf16x4 __attribute__((ext_vector_type(4)));
typedef float f32x4 __attribute__((ext_vector_type(4)));
typedef float f32x16 __attribute__((ext_vector_type(16)));
typedef unsigned u32x4 __attribute__((ext_vector_type(4)));
typedef unsigned u32x2 __attribute__((ext_vector_type(2)));
#define LAS __attribute__((address_space(3)))
#define GAS __attribute__((address_space(1)))

constexpr size_t MB = 1u << 20;
constexpr size_t OFF_KVT = 102 * MB;
constexpr unsigned WS_USED = 110u << 20;
constexpr size_t PROF_AT = 0;

__device__ __forceinline__ float bf2f(u16 b) { return __uint_as_float(((unsigned)b) << 16); }
__device__ __forceinline__ u16 f2bf(float f) { __bf16 b = (__bf16)f; return __builtin_bit_cast(u16, b); }
__device__ __forceinline__ float rbf(float f) { return bf2f(f2bf(f)); }
__device__ __forceinline__ unsigned pk2(float a, float b) {
  typedef float f2 __attribute__((ext_vector_type(2)));
  typedef __bf16 b2 __attribute__((ext_vector_type(2)));
  f2 v = {a, b}; b2 r = __builtin_convertvector(v, b2); return __builtin_bit_cast(unsigned, r);
}

__device__ __forceinline__ int opaque_tid(int wv) {
  int l = __builtin_amdgcn_mbcnt_hi(~0u, __builtin_amdgcn_mbcnt_lo(~0u, 0u));
  asm volatile("" : "+v"(l));
  return wv * 64 + l;
}
__device__ __forceinline__ int swz_b(int row) { return ((row & 3) << 2) | ((row >> 2) & 3); }

#define KNOB(name, v) constexpr int name = v
KNOB(K_DEF0, 80);
KNOB(W_STEP, 33); KNOB(W_OFF, 56);
#define ORD 0
#define RHO(h) (((h) >> 3) ^ ORD)
#define CAL_A 800
#define CAL_T 1350
#define CAL_N 4
#define WRONG 0
#define OUTP "nt"
#define PREV 0
#define PDMA 1
#define PRD 1
#define XSW 1
#define P_EAG 3
#define FILL0 4
#define NOFL 0
#define NOFILL 0
#define XS_T 1000
#define XS_G 64
#define K_DLY 300
#define Q_DLY 300
#define U_STG 1
#define NTW 0
#define XS_N 32
#define U_AH 0
#define U_POS 11
#define FAR1 0
#define FAR2 0
#define KEYX 0
#define U_SLP 40
#define U_AH2 0
#define ETEST 0
#define MS 3
#define CD0 1385
#define KD0 72
#define INI 8
#define F_SLP 0
#define A_DELAY 1500
#define LDNT(p) __builtin_nontemporal_load(p)
constexpr int CHK      = 16384;
constexpr int PAGE     = 2 << 20;
constexpr int SY_UM    = 0;
constexpr int SY_HOT   = 32 * PAGE;
constexpr int H_MEM    = 0;
constexpr int H_P      = 16 * CHK;
constexpr int H_PZ     = H_P + 2 * CHK;
constexpr int H_ANN    = H_PZ + 2048;
constexpr int H_TR     = H_PZ + 3072;
constexpr int H_DLT    = 80 * CHK;
constexpr int H_FLG    = 81 * CHK;
constexpr int H_PX     = 96 * CHK;
constexpr int H_BYTES  = 128 * CHK;
constexpr int SY_SET   = 33 * PAGE;
constexpr int SY_EPOCH = SY_SET + 128;
constexpr int SY_ABORT = SY_SET + 256;
constexpr int SY_SEL   = SY_ABORT + 128;
constexpr int SY_CT    = SY_ABORT + 640;
constexpr int CT_ARR = 0, CT_INIT = 8, CT_QUEUE = 9, CT_A1 = 10, CT_A2 = 18, CT_CAL = 26;
constexpr int CT_PR = CT_CAL + 64;
constexpr int SY_PU = SY_SET + 2 * CHK;
constexpr int SY_BYTES = SY_PU + 12288;
static_assert(SY_CT + (CT_PR + 128) * 128 <= SY_PU, "counters");
constexpr int URING = 16;
constexpr unsigned SENT = 0xFFFFFFFFu;
constexpr unsigned OOR = 0x7fffffffu;
constexpr unsigned SPIN_CAP = 1u << 19;
constexpr int NLF = 16;
constexpr int L_WTS = 0;
constexpr int L_MEM = L_WTS + 4 * NLF * 2048;
constexpr int L_XCH = L_MEM + 16384;
constexpr int L_XK  = L_XCH + 2048;
constexpr int L_KX  = L_XCH + 4096;
constexpr int L_PRF = L_KX + 8192;
constexpr int XRS = 40;
constexpr int CHAIN_LDS = 160 * 1024;
static_assert(L_XCH + 128 * XRS <= L_PRF && L_PRF + 4096 <= CHAIN_LDS, "lds");

typedef __bf16 bfv8 __attribute__((ext_vector_type(8)));
typedef __bf16 bfv16 __attribute__((ext_vector_type(16)));
typedef unsigned u32x8 __attribute__((ext_vector_type(8)));
typedef unsigned u32x12 __attribute__((ext_vector_type(12)));
typedef unsigned u32x16 __attribute__((ext_vector_type(16)));
typedef unsigned u32x32 __attribute__((ext_vector_type(32)));
struct Wts { u32x8 a[32]; u32x32 v[4]; };

#define STAMPL(i) do {} while (0)
#define PVAL(i, v) do {} while (0)
#define PDECL(base, str) do {} while (0)
#define PZERO do {} while (0)
#define PDUMP(mask) do {} while (0)
#define TA(x) do {} while (0)
#define TB(x) TS(x)
#define TS(x) do {} while (0)
#define SSTAMP(i) do {} while (0)
struct ChainArgs {
  u64* prof;
  const u16* mem0; const u16* Wq; const u16* Wk; const u16* Wv; const u16* Wo; const u16* X;
  u16* out; char* sync;
  const u16* Aq; const u16* Ak; const u16* Av; const u16* Ashift; const u16* Assm; const u16* AD; u16* kvT;
};
__device__ __forceinline__ GAS unsigned* ctr(const ChainArgs& a, int i) { return (GAS unsigned*)(a.sync + SY_CT + i * 128); }
__device__ __forceinline__ void ctr_add(const ChainArgs& a, int i, unsigned v) {
  __hip_atomic_fetch_add(ctr(a, i), v, __ATOMIC_RELAXED, __HIP_MEMORY_SCOPE_AGENT);
}

__device__ __forceinline__ __amdgpu_buffer_rsrc_t mk_rsrc(const void* p, unsigned bytes) {
  const u64 v = (u64)p;
  const unsigned lo = (unsigned)__builtin_amdgcn_readfirstlane((unsigned)v), hi = (unsigned)__builtin_amdgcn_readfirstlane((unsigned)(v >> 32));
  const u64 u = (u64)lo | ((u64)hi << 32);
  return __builtin_amdgcn_make_buffer_rsrc((void*)u, 0, bytes, 0x00020000);
}
__device__ __forceinline__ u32x4 own_rsrc(const void* p, unsigned bytes) {
  u32x4 r = {(unsigned)__builtin_amdgcn_readfirstlane((unsigned)(u64)p), (unsigned)__builtin_amdgcn_readfirstlane((unsigned)((u64)p >> 32)) & 0xffffu, bytes, 0x00020000u};
  asm volatile("s_nop 4" : "+s"(r));
  return r;
}
constexpr int LD_AUX = 16, ST_AUX = 16;
__device__ __forceinline__ u32x4 ld16(__amdgpu_buffer_rsrc_t rs, unsigned off) {
  return __builtin_amdgcn_raw_buffer_load_b128(rs, off, 0, LD_AUX);
}
__device__ __forceinline__ void st16(__amdgpu_buffer_rsrc_t rs, unsigned off, u32x4 v) {
  __builtin_amdgcn_raw_buffer_store_b128(v, rs, off, 0, ST_AUX);
}
__device__ __forceinline__ void st16s(u32x4 v, __amdgpu_buffer_rsrc_t rs, unsigned voff, unsigned soff) {
  __builtin_amdgcn_raw_buffer_store_b128(v, rs, voff, soff, ST_AUX);
  asm volatile("s_nop 1" :: "v"(v) : "memory");
}
__device__ __forceinline__ void st8(__amdgpu_buffer_rsrc_t rs, unsigned off, u32x2 v) {
  __builtin_amdgcn_raw_buffer_store_b64(v, rs, off, 0, ST_AUX);
}
__device__ __forceinline__ void st4(__amdgpu_buffer_rsrc_t rs, unsigned off, unsigned v) {
  __builtin_amdgcn_raw_buffer_store_b32(v, rs, off, 0, ST_AUX);
}
__device__ __forceinline__ unsigned ld4(__amdgpu_buffer_rsrc_t rs, unsigned off) {
  return __builtin_amdgcn_raw_buffer_load_b32(rs, off, 0, LD_AUX);
}
constexpr int TU = 16;
__device__ __forceinline__ unsigned cyc32() { return (unsigned)__builtin_amdgcn_s_memtime(); }
__device__ __forceinline__ unsigned to_sgpr(unsigned x) {
  unsigned r;
  asm volatile("s_nop 0\n\tv_readfirstlane_b32 %0, %1\n\ts_nop 4" : "=s"(r) : "v"(x));
  return r;
}
struct Clk {
  unsigned ce, te;
  unsigned c2t, t2c;
  __device__ __forceinline__ void edge(unsigned& c, unsigned& t) {
    u64 c0, r0, r1, r2, r3, r4, r5;
    asm volatile("s_memtime %0\n\ts_memrealtime %1\n\ts_nop 4\n\ts_memrealtime %2\n\ts_nop 4\n\ts_memrealtime %3\n\ts_nop 4\n\t"
                 "s_memrealtime %4\n\ts_nop 4\n\ts_memrealtime %5\n\ts_nop 4\n\ts_memrealtime %6\n\ts_waitcnt lgkmcnt(0)"
                 : "=&s"(c0), "=&s"(r0), "=&s"(r1), "=&s"(r2), "=&s"(r3), "=&s"(r4), "=&s"(r5) :: "memory");
    const unsigned x = (unsigned)r0;
    const unsigned n = ((unsigned)r1 == x) + ((unsigned)r2 == x) + ((unsigned)r3 == x) + ((unsigned)r4 == x) + ((unsigned)r5 == x);
    c = to_sgpr((unsigned)c0 + 24u * n); t = (x + 4u) * TU;
  }
  __device__ __forceinline__ void init() { edge(ce, te); c2t = 44000u; t2c = 97600u; }
  __device__ __forceinline__ void sync() {
    unsigned c, t;
    edge(c, t);
    set(c, t);
  }
  __device__ __forceinline__ void set(unsigned c, unsigned t) {
    if (c == ce) return;
    const float q = (float)(t - te) * __builtin_amdgcn_rcpf((float)(c - ce));
    const float qc = fminf(fmaxf(q, 0.51f), 1.99f);
    c2t = to_sgpr((unsigned)(qc * 65536.f));
    t2c = to_sgpr((unsigned)(__builtin_amdgcn_rcpf(qc) * 65536.f));
    ce = c; te = t;
  }
  __device__ __forceinline__ unsigned time(unsigned c) const { return te + ((min(c - ce, 30000u) * c2t) >> 16); }
  __device__ __forceinline__ unsigned now() const { return time(cyc32()); }
  __device__ __forceinline__ unsigned target(unsigned t) const {
    return to_sgpr(ce + (((unsigned)min(max((int)(t - te), 0), 20000) * t2c) >> 16));
  }
  __device__ __forceinline__ void wait(unsigned t) const { wait_c(target(t)); }
  __device__ __forceinline__ void wait_c(unsigned c) const {
    unsigned y;
    asm volatile("1:\n\ts_memtime vcc\n\ts_waitcnt lgkmcnt(0)\n\ts_sub_i32 %0, %1, vcc_lo\n\ts_cmp_gt_i32 %0, 128\n\ts_cbranch_scc1 1b\n\t"
                 "2:\n\ts_sub_i32 %0, %0, %2\n\ts_cmp_gt_i32 %0, %3\n\ts_cbranch_scc1 2b"
                 : "=&s"(y) : "s"(c), "n"(W_STEP), "n"(W_OFF) : "memory", "scc", "vcc");
  }
};
__device__ __forceinline__ unsigned nosent(unsigned d) { return min(d, SENT - 1u); }
__device__ __forceinline__ int arow(int row, int half) { return 4 * (row >> 1) + 2 * half + (row & 1); }
__device__ __forceinline__ unsigned umax4(u32x4 v) { return max(max(v[0], v[1]), max(v[2], v[3])); }
__device__ __forceinline__ void stamp_again(GAS u64* p) {
  __hip_atomic_store(p, (__builtin_amdgcn_s_memrealtime() & ~7ull) | (__hip_atomic_load(p, __ATOMIC_RELAXED, __HIP_MEMORY_SCOPE_AGENT) & 7ull),
                     __ATOMIC_RELAXED, __HIP_MEMORY_SCOPE_AGENT);
}
__device__ __forceinline__ bool poll_fail(unsigned& spins, GAS unsigned* abortw, int lane) {
  ++spins;
  if (spins > SPIN_CAP) {
    if (lane == 0) __hip_atomic_store(abortw, 1u, __ATOMIC_RELAXED, __HIP_MEMORY_SCOPE_AGENT);
    return true;
  }
  if ((spins & 511u) == 0u && lane == 0) {
    unsigned n = 0;
    for (int i = 0; i < 8; ++i) n += __hip_atomic_load(abortw + (SY_CT - SY_ABORT + (CT_ARR + i) * 128) / 4, __ATOMIC_RELAXED, __HIP_MEMORY_SCOPE_AGENT) + 1u;
    if (n < 256u) stamp_again((GAS u64*)(abortw - 32));
  }
  if ((spins & 63u) == 0u)
    return __builtin_amdgcn_readfirstlane(__hip_atomic_load(abortw, __ATOMIC_RELAXED, __HIP_MEMORY_SCOPE_AGENT)) != SENT;
  return false;
}
struct Gate {
  u64 entry; int arr; unsigned rot; bool open;
  __device__ __forceinline__ bool look(char* sync, u64 v = 0) {
    if (open) return true;
    for (unsigned spins = 0;; ++spins) {
      if (spins) v = __hip_atomic_load((GAS u64*)(sync + SY_EPOCH), __ATOMIC_RELAXED, __HIP_MEMORY_SCOPE_AGENT);
      v = (u64)(unsigned)__builtin_amdgcn_readfirstlane((unsigned)v) | ((u64)(unsigned)__builtin_amdgcn_readfirstlane((unsigned)(v >> 32)) << 32);
      if (v - entry + 2000u < (1ull << 31) || spins > 4 * SPIN_CAP) break;
      if (spins) __builtin_amdgcn_s_sleep(8);
    }
    asm volatile("" ::: "memory");
    const bool same = rot == ((unsigned)v & 7u);
    rot = (unsigned)v & 7u; open = true;
    return same;
  }
  __device__ __forceinline__ void arrive(char* sync) {
    if (arr >= 0 && opaque_tid(0) == 0) __hip_atomic_fetch_add((GAS unsigned*)(sync + SY_CT + (CT_ARR + arr) * 128), 1u, __ATOMIC_RELAXED, __HIP_MEMORY_SCOPE_AGENT);
    arr = -1;
  }
  __device__ __forceinline__ bool pass(char* sync, u64 v = 0) { const bool r = look(sync, v); arrive(sync); return r; }
};
__device__ __forceinline__ unsigned die_half() { return ((unsigned)__builtin_amdgcn_s_getreg(20 | (3 << 11)) >> 2) & 1u; }
typedef u32x2 Sel;
__device__ __forceinline__ Sel sel_wait(char* sync, GAS unsigned* abortw, int lane, bool& dead, bool all = false, int c0 = CT_INIT, int n0 = 0, int c1 = CT_INIT, int n1 = 0) {
  unsigned spins = 0;
  u64 v;
  const bool m = lane == 0 || lane > 3 || !all;
  const int off = m ? SY_SEL : SY_CT + (lane == 1 ? CT_INIT : lane == 2 ? c0 : c1) * 128;
  while (true) {
    v = __hip_atomic_load((GAS u64*)(sync + off), __ATOMIC_RELAXED, __HIP_MEMORY_SCOPE_AGENT);
    if (__all(m ? (v >> (all ? 62 : 63)) == 0 : (int)v >= (lane == 1 ? 31 : lane == 2 ? n0 : n1))) break;
    __builtin_amdgcn_s_sleep(4);
    if (dead || poll_fail(spins, abortw, lane)) { dead = true; v = 0; break; }
  }
  return (Sel){(unsigned)__builtin_amdgcn_readfirstlane((unsigned)v), (unsigned)__builtin_amdgcn_readfirstlane((unsigned)(v >> 32))};
}
__device__ __forceinline__ __amdgpu_buffer_rsrc_t rsrc_u(char* sync) { return mk_rsrc((void*)sync, WS_USED); }
__device__ __forceinline__ unsigned far_blk(int n, Sel sel) { return n * CHK + ((sel[0] ^ (n >> 7)) & 1u) * 8192; }
__device__ __forceinline__ unsigned x_base(int kind) { return (kind == 0 ? 1 : (kind == 1 ? 16 : 35)) * PAGE; }
__device__ __forceinline__ unsigned x_half(int kind, Sel sel) { return kind == 0 ? sel[0] >> 1 : (kind == 1 ? sel[0] >> 16 : 0xaaaau >> (sel[0] & 1u)); }
__device__ __forceinline__ unsigned x_first(int kind) { return kind == 0 ? 33 * PAGE : (kind == 1 ? 18 * PAGE + 8192 : 0); }
__device__ __forceinline__ constexpr unsigned quarter(int q) { return (q >> 1) * CHK + (q & 1) * 4096; }
__device__ __forceinline__ unsigned hot_q(int q, Sel sel) { return quarter(q ^ ((sel[1] >> 4) & 3u)); }
__device__ __forceinline__ unsigned copy_q(int g, int q, Sel sel) { return g * 16 * CHK + hot_q(q ^ ((g + 1) & 2) ^ ((g + 1) >> 2), sel); }
__device__ __forceinline__ unsigned um_at(int s, unsigned o, Sel sel) { return ((s & 3) * (64 * CHK) + o) ^ (((sel[1] >> (8 + 4 * (s & 3))) & 7u) << 12); }
__device__ __forceinline__ constexpr unsigned um_place(int ci, int g) { return (ci >> 3) * 16 * CHK + (ci & 1) * 8 * CHK + g * 2 * CHK + quarter(((ci >> 1) & 3) ^ ((ci >> 3) & 2)); }
constexpr unsigned UXH = 8 * CHK;
__device__ __forceinline__ unsigned ux_base(int s, int h, Sel sel) { return ((s & 15) * 32 + h) * UXH + ((~sel[0] >> (2 * (s & 15) + (h >> 4))) & 1u) * 8192; }
__device__ __forceinline__ constexpr unsigned ux_tile(int t) { return (t >> 3) * CHK + (t & 7) * 1024; }
__device__ __forceinline__ __amdgpu_buffer_rsrc_t rsrc_hot(char* sync, Sel sel) {
  return mk_rsrc((void*)(sync + SY_HOT + (sel[1] & 1u) * 8192), H_BYTES - 8192);
}
template <int SLP = 4>
__device__ __forceinline__ void ctr_wait(GAS unsigned* c, unsigned need, GAS unsigned* abortw, int lane, bool& dead) {
  unsigned spins = 0;
  while (true) {
    unsigned v = __builtin_amdgcn_readfirstlane(__hip_atomic_load(c, __ATOMIC_RELAXED, __HIP_MEMORY_SCOPE_AGENT)) + 1u;
    if (v >= need) break;
    __builtin_amdgcn_s_sleep(SLP);
    if (dead || poll_fail(spins, abortw, lane)) { dead = true; break; }
  }
}
template <int N> __device__ __forceinline__ void wait_vm() { asm volatile("s_waitcnt vmcnt(%0)" :: "n"(N) : "memory"); }
__device__ __forceinline__ void lds_barrier() {
  asm volatile("s_waitcnt lgkmcnt(0)" ::: "memory");
  __builtin_amdgcn_s_barrier();
  asm volatile("" ::: "memory");
}
__device__ __forceinline__ f32x4 vzero() { f32x4 z = {0.f, 0.f, 0.f, 0.f}; asm volatile("" : "+v"(z)); return z; }
constexpr bool blk_in_lds(int R, int b) {
  return R == 0 ? ((b & 7) == 1 || (b & 7) == 4) : (b < 16 ? (b & 3) == 0 : (b & 3) == 3);
}
constexpr int lds_index(int R, int b) { int n = 0; for (int i = 0; i < b; ++i) if (blk_in_lds(R, i)) ++n; return n; }
constexpr bool frag_in_lds(int R, int f) { return blk_in_lds(R, f >> 1); }
constexpr int frag_reg_slot(int R, int f) { int n = 0; for (int i = 0; i < f; ++i) if (!frag_in_lds(R, i)) ++n; return n; }

struct WSched { int ord[32]; int slot[32]; int issue[32]; int out[32]; int reg[32]; };
constexpr WSched make_wsched(int R) {
  WSched S{};
  int n = 0;
  for (int b = 0; b < 32; ++b) if (!blk_in_lds(R, b)) S.ord[n++] = b;
  for (int i = 0; i < n; ++i) S.reg[i] = frag_reg_slot(R, 2 * S.ord[i]);
  const int npure = n;
  for (int b = 0; b < 32; ++b) if (blk_in_lds(R, b)) S.ord[n++] = b;
  int seq[32] = {};
  int nissued = 0, next = 0;
  for (int i = 0; i < 10; ++i) { S.slot[i] = (i + 10 - npure % 10) % 10; S.issue[i] = -1; seq[i] = nissued++; }
  next = 10;
  S.out[0] = nissued - 1;
  for (int c = 0; c <= npure; ++c) {
    if (c >= 1) {
      const int k = S.slot[c - 1];
      if (next < npure) { S.slot[next] = k; S.issue[next] = c; seq[next] = nissued++; ++next; }
      else if (k < NLF / 2) { S.slot[npure + k] = k; S.issue[npure + k] = c; seq[npure + k] = nissued++; }
    }
    if (c + 1 < npure) S.out[c + 1] = nissued - 1 - seq[c + 1];
  }
  return S;
}
__device__ __forceinline__ u32x8 tr_frag(const char* sb, int tile, int lane) {
  u32x8 f;
#pragma unroll
  for (int q = 0; q < 4; ++q) {
    int row = (q >> 1) * 32 + 8 * (lane >> 4) + 4 * (q & 1) + ((lane & 15) >> 2);
    int p = lane & 3;
    int lc = 2 * tile + (p >> 1);
    int addr = row * 256 + ((lc ^ swz_b(row)) * 16) + 8 * (p & 1);
    bf16x4 v = __builtin_amdgcn_ds_read_tr16_b64_v4i16((LAS bf16x4*)(sb + addr));
    u32x2 u = __builtin_bit_cast(u32x2, v);
    f[2 * q] = u[0]; f[2 * q + 1] = u[1];
  }
  return f;
}
__device__ __forceinline__ void dma16(__amdgpu_buffer_rsrc_t rs, unsigned voff, unsigned soff, unsigned lds, bool nt = false, bool pad = true) {
  if (nt) asm volatile("s_mov_b32 m0, %0\n\ts_nop 3\n\tbuffer_load_dwordx4 %1, %2, %3 offen nt lds" :: "s"(lds), "v"(voff), "s"(rs), "s"(soff) : "m0", "memory");
  else if (pad) asm volatile("s_mov_b32 m0, %0\n\ts_nop 3\n\tbuffer_load_dwordx4 %1, %2, %3 offen lds" :: "s"(lds), "v"(voff), "s"(rs), "s"(soff) : "m0", "memory");
  else asm volatile("s_mov_b32 m0, %0\n\ts_nop 0\n\tbuffer_load_dwordx4 %1, %2, %3 offen lds" :: "s"(lds), "v"(voff), "s"(rs), "s"(soff) : "m0", "memory");
}
__device__ __forceinline__ u32x8 tr_frag2(const char* sb, int w, int f, int lane) {
  u32x8 r;
#pragma unroll
  for (int c = 0; c < 2; ++c)
#pragma unroll
    for (int t = 0; t < 2; ++t) {
      const int row = 32 * (w >> 1) + 16 * c + 4 * (lane >> 4) + ((lane & 15) >> 2);
      const int p = lane & 3, lc = 2 * (4 * (w & 1) + 2 * t + f) + (p >> 1);
      const u32x2 u = __builtin_bit_cast(u32x2, __builtin_amdgcn_ds_read_tr16_b64_v4i16((LAS bf16x4*)(sb + row * 256 + ((lc ^ swz_b(row)) * 16) + 8 * (p & 1))));
      r[4 * c + t] = u[0]; r[4 * c + 2 + t] = u[1];
    }
  return r;
}
template <int ROLE>
__device__ __forceinline__ void load_weights(const ChainArgs& a, int h, int w, int lane, char* smem, Wts& W) {
  u32x8 F[4];
#pragma unroll
  for (int i = 0; i < 32; ++i) asm volatile("" : "=a"(W.a[i]));
  constexpr WSched S = make_wsched(ROLE);
  constexpr int NP = 32 - NLF / 2;
  const __amdgpu_buffer_rsrc_t r0 = mk_rsrc(ROLE == 0 ? a.Wq : a.Wv, 8u << 20), r1 = mk_rsrc(ROLE == 0 ? a.Wk : a.Wo, 8u << 20);
  const unsigned lds0 = (unsigned)(uintptr_t)(LAS char*)smem;
  unsigned vo[2][4];
#pragma unroll
  for (int j = 0; j < 4; ++j) {
    const int row = (w * 4 + j) * 4 + (lane >> 4), lc = (lane & 15) ^ swz_b(row);
    vo[0][j] = row * HD * 2 + lc * 16; vo[1][j] = ((row & 15) + 2 * (row & 16)) * DM * 2 + (row >> 5) * 256 + lc * 16;
  }
  asm volatile("s_nop 4");
  auto stage = [&](int i) {
    const int blk = S.ord[i];
    const bool second = ROLE == 0 ? (blk & 1) : blk >= 16, wo = ROLE == 1 && second;
    const unsigned so = wo ? (unsigned)(((128 * h + 32 * (blk & 2) + 16 * (blk & 1)) * DM + 256 * (((blk - 16) >> 2) ^ (h & 3))) * 2)
                           : (unsigned)((64 * ((ROLE == 0 ? blk >> 1 : blk) ^ RHO(h)) * HD + 128 * h) * 2);
#pragma unroll
    for (int j = 0; j < 4; ++j) dma16(second ? r1 : r0, vo[wo][j], so, lds0 + 16384 * S.slot[i] + (w * 4 + j) * 1024, (NTW >> (wo ? 1 : 0)) & 1);
  };
#pragma unroll
  for (int i = 0; i < 32; ++i) if (S.issue[i] == -1) stage(i);
#pragma unroll
  for (int c = 0; c < NP; ++c) {
    asm volatile("s_waitcnt vmcnt(%0)" :: "n"(4 * S.out[c]) : "memory");
    lds_barrier();
#pragma unroll
    for (int i = 0; i < 32; ++i) if (S.issue[i] == c) stage(i);
    const char* sb = smem + 16384 * S.slot[c];
#pragma unroll
    for (int t = 0; t < 2; ++t) {
      u32x8 f = (ROLE == 1 && S.ord[c] >= 16) ? tr_frag2(sb, w, t, lane) : tr_frag(sb, 2 * w + t, lane);
      if (S.reg[c] < 32) {
#pragma unroll
        for (int i = 0; i < 8; ++i) asm volatile("v_accvgpr_write_b32 a[%1], %0" :: "v"(f[i]), "n"(8 * ((S.reg[c] + t) & 31) + i));
      }
      F[(S.reg[c] + t) & 3] = f;
    }
    if (S.reg[c] >= 32 && (S.reg[c] & 2)) {
      const u32x16 lo = __builtin_shufflevector(F[0], F[1], 0, 1, 2, 3, 4, 5, 6, 7, 8, 9, 10, 11, 12, 13, 14, 15);
      const u32x16 hi = __builtin_shufflevector(F[2], F[3], 0, 1, 2, 3, 4, 5, 6, 7, 8, 9, 10, 11, 12, 13, 14, 15);
      u32x32 x = __builtin_shufflevector(lo, hi, 0, 1, 2, 3, 4, 5, 6, 7, 8, 9, 10, 11, 12, 13, 14, 15, 16, 17, 18, 19, 20, 21, 22, 23, 24, 25, 26, 27, 28, 29, 30, 31);
      asm volatile("" : "+v"(x));
      W.v[(S.reg[c] >> 2) & 3] = x;
    }
  }
  static_assert(S.issue[31] < NP && S.issue[NP] < S.issue[31], "resident blocks are issued in slot order inside the loop");
#pragma unroll
  for (int j = 0; j < NLF / 2; ++j) {
    asm volatile("s_waitcnt vmcnt(%0)" :: "n"(4 * (NLF / 2 - 1 - j)) : "memory");
    lds_barrier();
    const bool two = ROLE == 1 && S.ord[NP + j] >= 16;
    u32x8 f0 = two ? tr_frag2(smem + 16384 * j, w, 0, lane) : tr_frag(smem + 16384 * j, 2 * w, lane);
    u32x8 f1 = two ? tr_frag2(smem + 16384 * j, w, 1, lane) : tr_frag(smem + 16384 * j, 2 * w + 1, lane);
    lds_barrier();
    char* d = smem + 16384 * j + w * 4096 + lane * 16;
    *(u32x4*)d = (u32x4){f0[0], f0[1], f0[2], f0[3]};
    *(u32x4*)(d + 1024) = (u32x4){f0[4], f0[5], f0[6], f0[7]};
    *(u32x4*)(d + 2048) = (u32x4){f1[0], f1[1], f1[2], f1[3]};
    *(u32x4*)(d + 3072) = (u32x4){f1[4], f1[5], f1[6], f1[7]};
  }
  lds_barrier();
}
#define SB0 __builtin_amdgcn_sched_barrier(0)
#define QK_BODY \
  "s_cmp_eq_u32 %[wv], 1\n\t" \
  "s_cbranch_scc1 91f\n\t" \
  "s_cmp_eq_u32 %[wv], 2\n\t" \
  "s_cbranch_scc1 92f\n\t" \
  "s_cmp_eq_u32 %[wv], 3\n\t" \
  "s_cbranch_scc1 93f\n\t" \
  "90:\n\t" \
  "s_mov_b32 s58, s42\n\t" \
  ".p2align 6\n\t" \
  "20:\n\t" \
  "s_and_b32 s76, s44, 7\n\t" \
  "s_lshl_b32 s76, s76, 15\n\t" \
  "s_add_i32 s40, s54, s76\n\t" \
  "s_and_b32 s76, s44, 3\n\t" \
  "s_lshl_b32 s45, s76, 16\n\t" \
  "s_add_i32 s47, s45, s55\n\t" \
  "s_add_i32 s49, s44, -1\n\t" \
  "s_and_b32 s48, s49, 3\n\t" \
  "s_lshl_b32 s48, s48, 16\n\t" \
  "s_lshl_b32 s50, s53, 8\n\t" \
  "s_mov_b32 s59, 0\n\t" \
  "s_mov_b32 s92, s51\n\t" \
  "s_mov_b32 s93, s53\n\t" \
  "s_xor_b32 s76, s40, 0x1000\n\t" \
  "s_xor_b32 s77, s40, 0x4000\n\t" \
  "s_xor_b32 s78, s40, 0x5000\n\t" \
  "s_cmp_lg_u32 s58, 0\n\t" \
  "s_cbranch_scc1 11f\n\t" \
  "10:\n\t" \
  "ds_read_b128 v[48:51], v102 offset:0\n\t" \
  "ds_read_b128 v[52:55], v102 offset:1024\n\t" \
  "ds_read_b128 v[56:59], v102 offset:2048\n\t" \
  "ds_read_b128 v[60:63], v102 offset:3072\n\t" \
  "ds_read_b128 v[16:19], v102 offset:16384\n\t" \
  "ds_read_b128 v[20:23], v102 offset:17408\n\t" \
  "ds_read_b128 v[24:27], v102 offset:18432\n\t" \
  "ds_read_b128 v[28:31], v102 offset:19456\n\t" \
  "ds_read_b128 v[84:87], v102 offset:49152\n\t" \
  "ds_read_b128 v[88:91], v102 offset:50176\n\t" \
  "ds_read_b128 v[92:95], v102 offset:51200\n\t" \
  "ds_read_b128 v[96:99], v102 offset:52224\n\t" \
  "s_waitcnt lgkmcnt(4)\n\t" \
  "ds_read_b128 v[0:3], v103 offset:16384\n\t" \
  "ds_read_b128 v[4:7], v103 offset:17408\n\t" \
  "ds_read_b128 v[8:11], v103 offset:18432\n\t" \
  "ds_read_b128 v[12:15], v103 offset:19456\n\t" \
  "s_bitcmp1_b32 s58, 1\n\t" \
  "s_cselect_b64 s[80:81], -1, 0\n\t" \
  "s_cmp_lg_u32 s58, 0\n\t" \
  "s_cbranch_scc1 17f\n\t" \
  "s_mov_b32 exec_hi, 0\n\t" \
  "16:\n\t" \
  "s_memtime vcc\n\t" \
  "s_waitcnt lgkmcnt(0)\n\t" \
  "s_sub_i32 s79, s41, vcc_lo\n\t" \
  "s_cmp_gt_i32 s79, 64\n\t" \
  "s_cbranch_scc1 16b\n\t" \
  "13:\n\t" \
  "buffer_load_dword v116, v110, %[rh], s48 offen sc1\n\t" \
  "s_waitcnt vmcnt(0)\n\t" \
  "15:\n\t" \
  "v_mov_b32 v117, 20000\n\t" \
  "v_cmp_ne_u32_sdwa vcc, v116, s49 src0_sel:BYTE_0 src1_sel:DWORD\n\t" \
  "v_subrev_u32 v116, s50, v116\n\t" \
  "v_ashrrev_i32 v116, 8, v116\n\t" \
  "s_nop 1\n\t" \
  "v_max_i32_dpp v116, v116, v116 quad_perm:[1,0,3,2] row_mask:0xf bank_mask:0xf\n\t" \
  "s_cbranch_vccnz 14f\n\t" \
  "s_nop 0\n\t" \
  "v_max_i32_dpp v116, v116, v116 quad_perm:[2,3,0,1] row_mask:0xf bank_mask:0xf\n\t" \
  "s_nop 1\n\t" \
  "v_max_i32_dpp v116, v116, v116 row_half_mirror row_mask:0xf bank_mask:0xf\n\t" \
  "s_nop 1\n\t" \
  "v_max_i32_dpp v116, v116, v116 row_mirror row_mask:0xf bank_mask:0xf\n\t" \
  "v_med3_i32 v116, v116, 0, v117\n\t" \
  "v_mul_u32_u24 v116, s52, v116\n\t" \
  "v_lshrrev_b32 v116, 16, v116\n\t" \
  "s_mov_b64 exec, -1\n\t" \
  "v_readlane_b32 s84, v116, 0\n\t" \
  "v_readlane_b32 s85, v116, 16\n\t" \
  "s_max_u32 s84, s84, s85\n\t" \
  "s_add_i32 s84, s84, s51\n\t" \
  "v_mov_b64 v[68:69], 0\n\t" \
  "v_mov_b64 v[70:71], 0\n\t" \
  "v_mov_b64 v[72:73], 0\n\t" \
  "v_mov_b64 v[74:75], 0\n\t" \
  "v_mov_b64 v[76:77], 0\n\t" \
  "v_mov_b64 v[78:79], 0\n\t" \
  "v_mov_b64 v[80:81], 0\n\t" \
  "v_mov_b64 v[82:83], 0\n\t" \
  "1:\n\t" \
  "s_memtime vcc\n\t" \
  "s_waitcnt lgkmcnt(0)\n\t" \
  "s_sub_i32 s79, s84, vcc_lo\n\t" \
  "s_cmp_gt_i32 s79, 128\n\t" \
  "s_cbranch_scc1 1b\n\t" \
  "2:\n\t" \
  "s_sub_i32 s79, s79, 33\n\t" \
  "s_cmp_gt_i32 s79, 56\n\t" \
  "s_cbranch_scc1 2b\n\t" \
  "buffer_load_dwordx4 v[32:35], v100, %[rh], s40 offen sc1\n\t" \
  "s_nop 12\n\t" \
  "buffer_load_dwordx4 v[36:39], v100, %[rh], s76 offen sc1\n\t" \
  "s_nop 12\n\t" \
  "buffer_load_dwordx4 v[40:43], v100, %[rh], s77 offen sc1\n\t" \
  "s_nop 12\n\t" \
  "buffer_load_dwordx4 v[44:47], v100, %[rh], s78 offen sc1\n\t" \
  "s_memtime vcc\n\t" \
  "s_memrealtime s[82:83]\n\t" \
  "s_nop 4\n\t" \
  "s_memrealtime s[84:85]\n\t" \
  "s_nop 4\n\t" \
  "s_memrealtime s[94:95]\n\t" \
  "s_nop 4\n\t" \
  "s_memrealtime s[86:87]\n\t" \
  "s_nop 4\n\t" \
  "s_memrealtime s[88:89]\n\t" \
  "s_nop 4\n\t" \
  "s_memrealtime s[90:91]\n\t" \
  "s_mov_b32 s92, 0\n\t" \
  "s_waitcnt lgkmcnt(0)\n\t" \
  "s_cmp_eq_u32 s82, s84\n\t" \
  "s_addc_u32 s92, s92, 0\n\t" \
  "s_cmp_eq_u32 s82, s94\n\t" \
  "s_addc_u32 s92, s92, 0\n\t" \
  "s_cmp_eq_u32 s82, s86\n\t" \
  "s_addc_u32 s92, s92, 0\n\t" \
  "s_cmp_eq_u32 s82, s88\n\t" \
  "s_addc_u32 s92, s92, 0\n\t" \
  "s_cmp_eq_u32 s82, s90\n\t" \
  "s_addc_u32 s92, s92, 0\n\t" \
  "s_mul_i32 s92, s92, 24\n\t" \
  "s_add_i32 s92, s92, vcc_lo\n\t" \
  "s_lshl4_add_u32 s93, s82, 64\n\t" \
  "3:\n\t" \
  "s_waitcnt lgkmcnt(0)\n\t" \
  "s_waitcnt vmcnt(3)\n\t" \
  "v_smfmac_f32_16x16x64_bf16 v[68:71], v[32:35], a[0:7], v104\n\t" \
  "ds_write_b128 v101, v[32:35] offset:0\n\t" \
  "v_smfmac_f32_16x16x64_bf16 v[72:75], v[32:35], a[8:15], v104\n\t" \
  "v_smfmac_f32_16x16x64_bf16 v[76:79], v[32:35], v[48:55], v104\n\t" \
  "v_smfmac_f32_16x16x64_bf16 v[80:83], v[32:35], v[56:63], v104\n\t" \
  "s_waitcnt vmcnt(2)\n\t" \
  "v_smfmac_f32_16x16x64_bf16 v[68:71], v[36:39], a[16:23], v104\n\t" \
  "ds_write_b128 v101, v[36:39] offset:1024\n\t" \
  "v_smfmac_f32_16x16x64_bf16 v[72:75], v[36:39], a[24:31], v104\n\t" \
  "v_smfmac_f32_16x16x64_bf16 v[76:79], v[36:39], a[32:39], v104\n\t" \
  "v_cmp_u_f32 vcc, v68, v69\n\t" \
  "v_smfmac_f32_16x16x64_bf16 v[80:83], v[36:39], a[40:47], v104\n\t" \
  "v_cmp_u_f32 s[82:83], v70, v71\n\t" \
  "s_waitcnt vmcnt(1)\n\t" \
  "v_smfmac_f32_16x16x64_bf16 v[68:71], v[40:43], v[16:23], v104\n\t" \
  "ds_write_b128 v101, v[40:43] offset:2048\n\t" \
  "v_smfmac_f32_16x16x64_bf16 v[72:75], v[40:43], v[24:31], v104\n\t" \
  "s_or_b64 vcc, vcc, s[82:83]\n\t" \
  "s_or_b64 vcc, vcc, s[80:81]\n\t" \
  "s_waitcnt lgkmcnt(1)\n\t" \
  "v_smfmac_f32_16x16x64_bf16 v[76:79], v[40:43], a[48:55], v104\n\t" \
  "s_cbranch_scc1 8f\n\t" \
  "5:\n\t" \
  "s_barrier\n\t" \
  "4:\n\t" \
  "ds_read_b128 v[48:51], v101 offset:4096\n\t" \
  "v_smfmac_f32_16x16x64_bf16 v[80:83], v[40:43], a[56:63], v104\n\t" \
  "ds_read_b128 v[52:55], v101 offset:5120\n\t" \
  "ds_read_b128 v[56:59], v101 offset:8192\n\t" \
  "s_waitcnt vmcnt(0)\n\t" \
  "v_smfmac_f32_16x16x64_bf16 v[68:71], v[44:47], a[64:71], v104\n\t" \
  "ds_write_b128 v101, v[44:47] offset:3072\n\t" \
  "v_smfmac_f32_16x16x64_bf16 v[72:75], v[44:47], a[72:79], v104\n\t" \
  "v_smfmac_f32_16x16x64_bf16 v[76:79], v[44:47], a[80:87], v104\n\t" \
  "v_cmp_u_f32 vcc, v68, v69\n\t" \
  "v_smfmac_f32_16x16x64_bf16 v[80:83], v[44:47], a[88:95], v104\n\t" \
  "v_cmp_u_f32 s[82:83], v70, v71\n\t" \
  "s_waitcnt lgkmcnt(3)\n\t" \
  "v_smfmac_f32_16x16x64_bf16 v[68:71], v[48:51], a[96:103], v104\n\t" \
  "ds_read_b128 v[60:63], v101 offset:9216\n\t" \
  "ds_read_b128 v[64:67], v101 offset:12288\n\t" \
  "v_smfmac_f32_16x16x64_bf16 v[72:75], v[48:51], a[104:111], v104\n\t" \
  "s_or_b64 vcc, vcc, s[82:83]\n\t" \
  "s_waitcnt lgkmcnt(2)\n\t" \
  "v_smfmac_f32_16x16x64_bf16 v[68:71], v[52:55], a[112:119], v104\n\t" \
  "s_cbranch_scc1 9f\n\t" \
  "6:\n\t" \
  "s_barrier\n\t" \
  "v_smfmac_f32_16x16x64_bf16 v[72:75], v[52:55], a[120:127], v104\n\t" \
  "ds_read_b128 v[48:51], v101 offset:13312\n\t" \
  "v_smfmac_f32_16x16x64_bf16 v[68:71], v[56:59], a[192:199], v104\n\t" \
  "ds_read_b128 v[52:55], v101 offset:6144\n\t" \
  "v_smfmac_f32_16x16x64_bf16 v[72:75], v[56:59], a[200:207], v104\n\t" \
  "s_xor_b32 s94, s47, 0x5000\n\t" \
  "s_waitcnt lgkmcnt(3)\n\t" \
  "v_smfmac_f32_16x16x64_bf16 v[68:71], v[60:63], a[208:215], v104\n\t" \
  "ds_read_b128 v[56:59], v101 offset:7168\n\t" \
  "v_smfmac_f32_16x16x64_bf16 v[72:75], v[60:63], a[216:223], v104\n\t" \
  "s_xor_b32 s95, s47, 0x4000\n\t" \
  "s_waitcnt lgkmcnt(3)\n\t" \
  "v_smfmac_f32_16x16x64_bf16 v[68:71], v[64:67], v[152:159], v104\n\t" \
  "ds_read_b128 v[60:63], v101 offset:10240\n\t" \
  "ds_read_b128 v[32:35], v119 offset:0\n\t" \
  "v_smfmac_f32_16x16x64_bf16 v[72:75], v[64:67], v[160:167], v104\n\t" \
  "ds_read_b128 v[16:19], v103 offset:49152\n\t" \
  "s_waitcnt lgkmcnt(5)\n\t" \
  "v_smfmac_f32_16x16x64_bf16 v[68:71], v[48:51], v[168:175], v104\n\t" \
  "ds_read_b128 v[64:67], v101 offset:11264\n\t" \
  "ds_read_b128 v[36:39], v119 offset:1024\n\t" \
  "v_smfmac_f32_16x16x64_bf16 v[72:75], v[48:51], v[176:183], v104\n\t" \
  "ds_read_b128 v[20:23], v103 offset:50176\n\t" \
  "s_waitcnt lgkmcnt(7)\n\t" \
  "v_smfmac_f32_16x16x64_bf16 v[68:71], v[52:55], v[84:91], v104\n\t" \
  "ds_read_b128 v[48:51], v101 offset:15360\n\t" \
  "ds_read_b128 v[40:43], v119 offset:16\n\t" \
  "v_smfmac_f32_16x16x64_bf16 v[72:75], v[52:55], v[92:99], v104\n\t" \
  "ds_read_b128 v[24:27], v103 offset:51200\n\t" \
  "s_waitcnt lgkmcnt(9)\n\t" \
  "v_smfmac_f32_16x16x64_bf16 v[68:71], v[56:59], a[160:167], v104\n\t" \
  "ds_read_b128 v[52:55], v101 offset:14336\n\t" \
  "ds_read_b128 v[44:47], v119 offset:1040\n\t" \
  "v_smfmac_f32_16x16x64_bf16 v[72:75], v[56:59], a[168:175], v104\n\t" \
  "ds_read_b128 v[28:31], v103 offset:52224\n\t" \
  "s_waitcnt lgkmcnt(11)\n\t" \
  "v_smfmac_f32_16x16x64_bf16 v[68:71], v[60:63], v[0:7], v104\n\t" \
  "ds_read_b128 v[92:95], v119 offset:2048\n\t" \
  "v_smfmac_f32_16x16x64_bf16 v[72:75], v[60:63], v[8:15], v104\n\t" \
  "ds_read_b128 v[96:99], v119 offset:3072\n\t" \
  "s_waitcnt lgkmcnt(10)\n\t" \
  "v_smfmac_f32_16x16x64_bf16 v[68:71], v[64:67], v[120:127], v104\n\t" \
  "ds_read_b128 v[0:3], v102 offset:32768\n\t" \
  "v_smfmac_f32_16x16x64_bf16 v[72:75], v[64:67], v[128:135], v104\n\t" \
  "ds_read_b128 v[4:7], v102 offset:33792\n\t" \
  "s_waitcnt lgkmcnt(9)\n\t" \
  "v_smfmac_f32_16x16x64_bf16 v[68:71], v[48:51], v[216:223], v104\n\t" \
  "ds_read_b128 v[8:11], v102 offset:34816\n\t" \
  "s_waitcnt lgkmcnt(7)\n\t" \
  "v_smfmac_f32_16x16x64_bf16 v[68:71], v[52:55], v[16:23], v104\n\t" \
  "ds_read_b128 v[12:15], v102 offset:35840\n\t" \
  "v_smfmac_f32_16x16x64_bf16 v[72:75], v[48:51], v[224:231], v104\n\t" \
  "ds_read_b128 v[16:19], v119 offset:2064\n\t" \
  "ds_read_b128 v[20:23], v119 offset:3088\n\t" \
  "s_waitcnt lgkmcnt(8)\n\t" \
  "v_smfmac_f32_16x16x64_bf16 v[72:75], v[52:55], v[24:31], v104\n\t" \
  "v_add_f32 v116, v68, v70\n\t" \
  "v_add_f32 v117, v69, v71\n\t" \
  "v_smfmac_f32_16x16x64_bf16 v[76:79], v[56:59], a[176:183], v104\n\t" \
  "v_cvt_pk_bf16_f32 v116, v116, v117\n\t" \
  "ds_read_b128 v[24:27], v103 offset:2048\n\t" \
  "v_smfmac_f32_16x16x64_bf16 v[80:83], v[56:59], a[184:191], v104\n\t" \
  "v_add_f32 v117, v72, v74\n\t" \
  "v_add_f32 v118, v73, v75\n\t" \
  "v_cvt_pk_bf16_f32 v117, v117, v118\n\t" \
  "ds_write2st64_b32 v105, v116, v117 offset1:1\n\t" \
  "v_smfmac_f32_16x16x64_bf16 v[76:79], v[60:63], a[240:247], v104\n\t" \
  "v_mov_b64 v[68:69], 0\n\t" \
  "s_xor_b32 s96, s47, 0x1000\n\t" \
  "v_smfmac_f32_16x16x64_bf16 v[80:83], v[60:63], a[248:255], v104\n\t" \
  "v_mov_b64 v[70:71], 0\n\t" \
  "v_smfmac_f32_16x16x64_bf16 v[76:79], v[64:67], v[136:143], v104\n\t" \
  "v_mov_b64 v[72:73], 0\n\t" \
  "v_smfmac_f32_16x16x64_bf16 v[80:83], v[64:67], v[144:151], v104\n\t" \
  "s_waitcnt lgkmcnt(0)\n\t" \
  "s_barrier\n\t" \
  "ds_read_b64_tr_b16 v[84:85], v107 offset:0\n\t" \
  "ds_read_b64_tr_b16 v[86:87], v107 offset:128\n\t" \
  "v_smfmac_f32_16x16x64_bf16 v[76:79], v[48:51], v[232:239], v104\n\t" \
  "ds_read_b64_tr_b16 v[88:89], v107 offset:1024\n\t" \
  "ds_read_b64_tr_b16 v[90:91], v107 offset:1152\n\t" \
  "v_smfmac_f32_16x16x64_bf16 v[80:83], v[48:51], v[240:247], v104\n\t" \
  "ds_read_b128 v[56:59], v101 offset:4096\n\t" \
  "v_mov_b64 v[74:75], 0\n\t" \
  "v_smfmac_f32_16x16x64_bf16 v[76:79], v[52:55], v[200:207], v104\n\t" \
  "ds_read_b128 v[60:63], v101 offset:13312\n\t" \
  "ds_read_b128 v[28:31], v103 offset:3072\n\t" \
  "v_smfmac_f32_16x16x64_bf16 v[80:83], v[52:55], v[208:215], v104\n\t" \
  "s_waitcnt lgkmcnt(5)\n\t" \
  "v_smfmac_f32_16x16x64_bf16 v[68:71], v[84:87], v[32:39], v104\n\t" \
  "v_smfmac_f32_16x16x64_bf16 v[72:75], v[84:87], v[40:47], v104\n\t" \
  "ds_read_b128 v[64:67], v101 offset:6144\n\t" \
  "s_waitcnt lgkmcnt(4)\n\t" \
  "v_smfmac_f32_16x16x64_bf16 v[68:71], v[88:91], v[92:99], v104\n\t" \
  "v_smfmac_f32_16x16x64_bf16 v[72:75], v[88:91], v[16:23], v104\n\t" \
  "ds_read_b128 v[48:51], v101 offset:8192\n\t" \
  "s_waitcnt lgkmcnt(4)\n\t" \
  "v_smfmac_f32_16x16x64_bf16 v[76:79], v[56:59], v[0:7], v104\n\t" \
  "ds_read_b128 v[16:19], v103 offset:0\n\t" \
  "v_add_f32 v40, v68, v70\n\t" \
  "v_smfmac_f32_16x16x64_bf16 v[80:83], v[56:59], v[8:15], v104\n\t" \
  "v_add_f32 v41, v72, v74\n\t" \
  "v_max_f32 v42, v40, v41\n\t" \
  "s_waitcnt lgkmcnt(4)\n\t" \
  "v_smfmac_f32_16x16x64_bf16 v[76:79], v[60:63], v[184:191], v104\n\t" \
  "ds_read_b128 v[20:23], v103 offset:1024\n\t" \
  "v_max_f32_dpp v42, v42, v42 quad_perm:[1,0,3,2] row_mask:0xf bank_mask:0xf\n\t" \
  "v_smfmac_f32_16x16x64_bf16 v[80:83], v[60:63], v[192:199], v104\n\t" \
  "ds_read_b128 v[52:55], v101 offset:5120\n\t" \
  "v_max_f32_dpp v42, v42, v42 quad_perm:[2,3,0,1] row_mask:0xf bank_mask:0xf\n\t" \
  "s_waitcnt lgkmcnt(4)\n\t" \
  "v_smfmac_f32_16x16x64_bf16 v[76:79], v[64:67], a[144:151], v104\n\t" \
  "ds_read_b128 v[56:59], v101 offset:9216\n\t" \
  "v_max_f32_dpp v42, v42, v42 row_half_mirror row_mask:0xf bank_mask:0xf\n\t" \
  "v_smfmac_f32_16x16x64_bf16 v[80:83], v[64:67], a[152:159], v104\n\t" \
  "ds_read_b128 v[60:63], v101 offset:12288\n\t" \
  "v_max_f32_dpp v42, v42, v42 row_mirror row_mask:0xf bank_mask:0xf\n\t" \
  "v_mul_f32 v42, %[cs], v42\n\t" \
  "s_waitcnt lgkmcnt(3)\n\t" \
  "v_smfmac_f32_16x16x64_bf16 v[76:79], v[48:51], v[16:23], v104\n\t" \
  "ds_read_b128 v[0:3], v103 offset:32768\n\t" \
  "v_fma_f32 v40, v40, %[cs], -v42\n\t" \
  "v_fma_f32 v41, v41, %[cs], -v42\n\t" \
  "v_smfmac_f32_16x16x64_bf16 v[80:83], v[48:51], v[24:31], v104\n\t" \
  "ds_read_b128 v[4:7], v103 offset:33792\n\t" \
  "v_exp_f32 v43, v40\n\t" \
  "v_exp_f32 v44, v41\n\t" \
  "s_waitcnt lgkmcnt(4)\n\t" \
  "v_smfmac_f32_16x16x64_bf16 v[76:79], v[52:55], a[128:135], v104\n\t" \
  "ds_read_b128 v[8:11], v103 offset:34816\n\t" \
  "v_cvt_pk_bf16_f32 v45, v43, v44\n\t" \
  "buffer_store_dword v45, v112, %[rh], s94 offen sc1\n\t" \
  "v_smfmac_f32_16x16x64_bf16 v[80:83], v[52:55], a[136:143], v104\n\t" \
  "ds_read_b128 v[12:15], v103 offset:35840\n\t" \
  "buffer_store_dword v45, v112, %[rh], s95 offen sc1\n\t" \
  "s_waitcnt lgkmcnt(5)\n\t" \
  "v_smfmac_f32_16x16x64_bf16 v[76:79], v[56:59], a[224:231], v104\n\t" \
  "buffer_store_dword v45, v112, %[rh], s96 offen sc1\n\t" \
  "s_waitcnt lgkmcnt(2)\n\t" \
  "v_smfmac_f32_16x16x64_bf16 v[76:79], v[60:63], v[0:7], v104\n\t" \
  "buffer_store_dword v45, v112, %[rh], s47 offen sc1\n\t" \
  "v_smfmac_f32_16x16x64_bf16 v[80:83], v[56:59], a[232:239], v104\n\t" \
  "s_waitcnt lgkmcnt(0)\n\t" \
  "v_smfmac_f32_16x16x64_bf16 v[80:83], v[60:63], v[8:15], v104\n\t" \
  "v_add_f32 v20, v76, v78\n\t" \
  "v_add_f32 v21, v77, v79\n\t" \
  "v_cvt_pk_bf16_f32 v20, v20, v21\n\t" \
  "ds_write_b32 v106, v20\n\t" \
  "v_add_f32 v92, v43, v44\n\t" \
  "v_mov_b32 v25, v115\n\t" \
  "s_nop 1\n\t" \
  "v_add_f32 v23, v80, v82\n\t" \
  "v_add_f32 v21, v81, v83\n\t" \
  "v_cvt_pk_bf16_f32 v21, v23, v21\n\t" \
  "ds_write_b32 v106, v21 offset:256\n\t" \
  "v_add_f32_dpp v92, v92, v92 row_ror:8 row_mask:0xf bank_mask:0xf\n\t" \
  "v_mov_b64 v[0:1], 0\n\t" \
  "v_mov_b64 v[2:3], 0\n\t" \
  "v_mov_b32 v27, v114\n\t" \
  "s_waitcnt lgkmcnt(0)\n\t" \
  "s_barrier\n\t" \
  "ds_read_b64_tr_b16 v[4:5], v108 offset:0\n\t" \
  "ds_read_b64_tr_b16 v[6:7], v108 offset:64\n\t" \
  "ds_read_b64_tr_b16 v[8:9], v108 offset:512\n\t" \
  "ds_read_b64_tr_b16 v[10:11], v108 offset:576\n\t" \
  "ds_read_b64_tr_b16 v[12:13], v108 offset:1024\n\t" \
  "ds_read_b64_tr_b16 v[14:15], v108 offset:1088\n\t" \
  "ds_read_b64_tr_b16 v[16:17], v108 offset:1536\n\t" \
  "ds_read_b64_tr_b16 v[18:19], v108 offset:1600\n\t" \
  "s_waitcnt lgkmcnt(4)\n\t" \
  "v_smfmac_f32_16x16x64_bf16 v[0:3], v[84:87], v[4:11], v104\n\t" \
  "s_waitcnt lgkmcnt(0)\n\t" \
  "v_smfmac_f32_16x16x64_bf16 v[0:3], v[88:91], v[12:19], v104\n\t" \
  "s_nop 7\n\t" \
  "v_add_f32 v96, v0, v2\n\t" \
  "v_fma_f32 v96, v96, %[cs], -v42\n\t" \
  "v_exp_f32 v24, v96\n\t" \
  "s_nop 0\n\t" \
  "v_add_f32 v29, v92, v24\n\t" \
  "s_nop 0\n\t" \
  "v_mov_b32_dpp v25, v24 quad_perm:[1,0,3,2] row_mask:0xf bank_mask:0x3\n\t" \
  "v_add_f32_dpp v29, v29, v29 quad_perm:[1,0,3,2] row_mask:0xf bank_mask:0xf\n\t" \
  "v_mov_b32_dpp v27, v24 quad_perm:[0,1,2,3] row_mask:0xf bank_mask:0x3\n\t" \
  "s_nop 0\n\t" \
  "v_add_f32_dpp v29, v29, v29 quad_perm:[2,3,0,1] row_mask:0xf bank_mask:0xf\n\t" \
  "s_nop 1\n\t" \
  "v_add_f32_dpp v29, v29, v29 row_half_mirror row_mask:0xf bank_mask:0xf\n\t" \
  "v_rcp_f32 v26, v29\n\t" \
  "v_cmpx_gt_f32 vcc, 0x5d5e0b6b, v29\n\t" \
  "v_mul_f32 v24, v27, v26\n\t" \
  "v_mul_f32 v25, v25, v26\n\t" \
  "v_cvt_pk_bf16_f32 v24, v24, v25\n\t" \
  "buffer_store_dword v24, v113, %[rh], s94 offen sc1\n\t" \
  "buffer_store_dword v24, v113, %[rh], s95 offen sc1\n\t" \
  "buffer_store_dword v24, v113, %[rh], s96 offen sc1\n\t" \
  "buffer_store_dword v24, v113, %[rh], s47 offen sc1\n\t" \
  "s_memtime s[76:77]\n\t" \
  "s_mov_b64 exec, -1\n\t" \
  "v_mov_b32 v97, v29\n\t" \
  "s_cmp_eq_u64 vcc, -1\n\t" \
  "s_cselect_b32 s42, 0, 8\n\t" \
  "s_waitcnt lgkmcnt(0)\n\t" \
  "s_mov_b32 s57, s76\n\t" \
  "s_add_i32 s41, s57, 400\n\t" \
  "s_sub_i32 s76, s92, s51\n\t" \
  "s_sub_i32 s77, s93, s53\n\t" \
  "s_cmp_eq_u32 s76, 0\n\t" \
  "s_cbranch_scc1 21f\n\t" \
  "v_cvt_f32_u32 v117, s76\n\t" \
  "v_cvt_f32_u32 v116, s77\n\t" \
  "v_rcp_f32 v117, v117\n\t" \
  "s_mov_b32 s51, s92\n\t" \
  "v_mul_f32 v117, v117, v116\n\t" \
  "v_max_f32 v117, 0x3f028f5c, v117\n\t" \
  "v_min_f32 v117, 0x3ffeb852, v117\n\t" \
  "v_rcp_f32 v116, v117\n\t" \
  "v_mul_f32 v117, 0x4f000000, v117\n\t" \
  "v_mul_f32 v116, 0x47800000, v116\n\t" \
  "v_cvt_u32_f32 v117, v117\n\t" \
  "v_cvt_u32_f32 v116, v116\n\t" \
  "s_mov_b32 s53, s93\n\t" \
  "v_readfirstlane_b32 s46, v117\n\t" \
  "v_readfirstlane_b32 s52, v116\n\t" \
  "21:\n\t" \
  "v_mov_b64 v[0:1], -1\n\t" \
  "v_mov_b64 v[2:3], -1\n\t" \
  "s_sleep 2\n\t" \
  "s_add_i32 s76, s48, 0x0\n\t" \
  "buffer_store_dwordx4 v[0:3], v109, %[rh], s76 offen sc1\n\t" \
  "s_add_i32 s76, s48, 0x1000\n\t" \
  "buffer_store_dwordx4 v[0:3], v109, %[rh], s76 offen sc1\n\t" \
  "s_add_i32 s44, s44, 1\n\t" \
  "s_and_b32 s58, s58, 4\n\t" \
  "s_and_b32 s76, s44, 64\n\t" \
  "s_or_b32 s42, s42, s76\n\t" \
  "s_cbranch_scc0 20b\n\t" \
  "s_branch 99f\n\t" \
  "14:\n\t" \
  "s_sleep 0\n\t" \
  "s_add_u32 s59, s59, 0x10000\n\t" \
  "s_cmp_lt_u32 s59, 0x100000\n\t" \
  "s_cbranch_scc1 13b\n\t" \
  "s_mov_b64 exec, -1\n\t" \
  "s_mov_b32 s42, 16\n\t" \
  "s_branch 99f\n\t" \
  "8:\n\t" \
  "s_bitcmp1_b32 s58, 1\n\t" \
  "s_cbranch_scc1 4b\n\t" \
  "s_bitcmp1_b32 s58, 2\n\t" \
  "s_cbranch_scc1 5b\n\t" \
  "v_max3_u32 v116, v32, v33, v34\n\t" \
  "v_max3_u32 v117, v35, v36, v37\n\t" \
  "v_max3_u32 v116, v116, v38, v39\n\t" \
  "v_max_u32 v116, v116, v117\n\t" \
  "v_cmp_eq_u32 vcc, -1, v116\n\t" \
  "s_cbranch_vccz 5b\n\t" \
  "s_branch 0f\n\t" \
  "9:\n\t" \
  "s_bitcmp1_b32 s58, 2\n\t" \
  "s_cbranch_scc1 6b\n\t" \
  "v_max3_u32 v116, v40, v41, v42\n\t" \
  "v_max3_u32 v117, v43, v44, v45\n\t" \
  "v_max3_u32 v116, v116, v46, v47\n\t" \
  "v_max_u32 v116, v116, v117\n\t" \
  "v_cmp_eq_u32 vcc, -1, v116\n\t" \
  "s_cbranch_vccz 6b\n\t" \
  "s_or_b32 s58, s58, 2\n\t" \
  "0:\n\t" \
  "s_or_b32 s58, s58, 1\n\t" \
  "s_sleep 0\n\t" \
  "11:\n\t" \
  "buffer_load_dwordx4 v[32:35], v100, %[rh], s40 offen sc1\n\t" \
  "buffer_load_dwordx4 v[36:39], v100, %[rh], s76 offen sc1\n\t" \
  "buffer_load_dwordx4 v[40:43], v100, %[rh], s77 offen sc1\n\t" \
  "buffer_load_dwordx4 v[44:47], v100, %[rh], s78 offen sc1\n\t" \
  "s_waitcnt lgkmcnt(0)\n\t" \
  "s_branch 10b\n\t" \
  "17:\n\t" \
  "v_mov_b64 v[68:69], 0\n\t" \
  "v_mov_b64 v[70:71], 0\n\t" \
  "v_mov_b64 v[72:73], 0\n\t" \
  "v_mov_b64 v[74:75], 0\n\t" \
  "v_mov_b64 v[76:77], 0\n\t" \
  "v_mov_b64 v[78:79], 0\n\t" \
  "v_mov_b64 v[80:81], 0\n\t" \
  "v_mov_b64 v[82:83], 0\n\t" \
  "s_add_u32 s59, s59, 1\n\t" \
  "s_bitcmp1_b32 s58, 2\n\t" \
  "s_cbranch_scc1 3b\n\t" \
  "s_waitcnt vmcnt(0)\n\t" \
  "v_max_u32 v116, v32, v33\n\t" \
  "v_max3_u32 v116, v116, v34, v35\n\t" \
  "v_max3_u32 v116, v116, v36, v37\n\t" \
  "v_max3_u32 v116, v116, v38, v39\n\t" \
  "v_max3_u32 v116, v116, v40, v41\n\t" \
  "v_max3_u32 v116, v116, v42, v43\n\t" \
  "v_max3_u32 v116, v116, v44, v45\n\t" \
  "v_max3_u32 v116, v116, v46, v47\n\t" \
  "v_cmp_eq_u32 vcc, -1, v116\n\t" \
  "s_cbranch_vccz 3b\n\t" \
  "s_bitcmp1_b32 s59, 6\n\t" \
  "s_cbranch_scc1 12f\n\t" \
  "buffer_load_dwordx4 v[32:35], v100, %[rh], s40 offen sc1\n\t" \
  "buffer_load_dwordx4 v[36:39], v100, %[rh], s76 offen sc1\n\t" \
  "buffer_load_dwordx4 v[40:43], v100, %[rh], s77 offen sc1\n\t" \
  "buffer_load_dwordx4 v[44:47], v100, %[rh], s78 offen sc1\n\t" \
  "s_branch 17b\n\t" \
  "12:\n\t" \
  "s_mov_b32 s42, s58\n\t" \
  "s_branch 99f\n\t" \
  "91:\n\t" \
  "s_mov_b32 s58, s42\n\t" \
  ".p2align 6\n\t" \
  "20:\n\t" \
  "s_and_b32 s76, s44, 7\n\t" \
  "s_lshl_b32 s76, s76, 15\n\t" \
  "s_add_i32 s40, s54, s76\n\t" \
  "s_and_b32 s76, s44, 3\n\t" \
  "s_lshl_b32 s45, s76, 16\n\t" \
  "s_add_i32 s47, s45, s55\n\t" \
  "s_add_i32 s49, s44, -1\n\t" \
  "s_and_b32 s48, s49, 3\n\t" \
  "s_lshl_b32 s48, s48, 16\n\t" \
  "s_lshl_b32 s50, s53, 8\n\t" \
  "s_mov_b32 s59, 0\n\t" \
  "s_mov_b32 s92, s51\n\t" \
  "s_mov_b32 s93, s53\n\t" \
  "s_xor_b32 s76, s40, 0x1000\n\t" \
  "s_xor_b32 s77, s40, 0x4000\n\t" \
  "s_xor_b32 s78, s40, 0x5000\n\t" \
  "s_cmp_lg_u32 s58, 0\n\t" \
  "s_cbranch_scc1 11f\n\t" \
  "10:\n\t" \
  "ds_read_b128 v[48:51], v102 offset:32768\n\t" \
  "ds_read_b128 v[52:55], v102 offset:33792\n\t" \
  "ds_read_b128 v[56:59], v102 offset:34816\n\t" \
  "ds_read_b128 v[60:63], v102 offset:35840\n\t" \
  "ds_read_b128 v[16:19], v102 offset:49152\n\t" \
  "ds_read_b128 v[20:23], v102 offset:50176\n\t" \
  "ds_read_b128 v[24:27], v102 offset:51200\n\t" \
  "ds_read_b128 v[28:31], v102 offset:52224\n\t" \
  "ds_read_b128 v[84:87], v103 offset:16384\n\t" \
  "ds_read_b128 v[88:91], v103 offset:17408\n\t" \
  "ds_read_b128 v[92:95], v103 offset:18432\n\t" \
  "ds_read_b128 v[96:99], v103 offset:19456\n\t" \
  "s_waitcnt lgkmcnt(4)\n\t" \
  "ds_read_b128 v[0:3], v103 offset:49152\n\t" \
  "ds_read_b128 v[4:7], v103 offset:50176\n\t" \
  "ds_read_b128 v[8:11], v103 offset:51200\n\t" \
  "ds_read_b128 v[12:15], v103 offset:52224\n\t" \
  "s_bitcmp1_b32 s58, 1\n\t" \
  "s_cselect_b64 s[80:81], -1, 0\n\t" \
  "s_cmp_lg_u32 s58, 0\n\t" \
  "s_cbranch_scc1 17f\n\t" \
  "s_mov_b32 exec_hi, 0\n\t" \
  "16:\n\t" \
  "s_memtime vcc\n\t" \
  "s_waitcnt lgkmcnt(0)\n\t" \
  "s_sub_i32 s79, s41, vcc_lo\n\t" \
  "s_cmp_gt_i32 s79, 64\n\t" \
  "s_cbranch_scc1 16b\n\t" \
  "13:\n\t" \
  "buffer_load_dword v116, v110, %[rh], s48 offen sc1\n\t" \
  "s_waitcnt vmcnt(0)\n\t" \
  "15:\n\t" \
  "v_mov_b32 v117, 20000\n\t" \
  "v_cmp_ne_u32_sdwa vcc, v116, s49 src0_sel:BYTE_0 src1_sel:DWORD\n\t" \
  "v_subrev_u32 v116, s50, v116\n\t" \
  "v_ashrrev_i32 v116, 8, v116\n\t" \
  "s_nop 1\n\t" \
  "v_max_i32_dpp v116, v116, v116 quad_perm:[1,0,3,2] row_mask:0xf bank_mask:0xf\n\t" \
  "s_cbranch_vccnz 14f\n\t" \
  "s_nop 0\n\t" \
  "v_max_i32_dpp v116, v116, v116 quad_perm:[2,3,0,1] row_mask:0xf bank_mask:0xf\n\t" \
  "s_nop 1\n\t" \
  "v_max_i32_dpp v116, v116, v116 row_half_mirror row_mask:0xf bank_mask:0xf\n\t" \
  "s_nop 1\n\t" \
  "v_max_i32_dpp v116, v116, v116 row_mirror row_mask:0xf bank_mask:0xf\n\t" \
  "v_med3_i32 v116, v116, 0, v117\n\t" \
  "v_mul_u32_u24 v116, s52, v116\n\t" \
  "v_lshrrev_b32 v116, 16, v116\n\t" \
  "s_mov_b64 exec, -1\n\t" \
  "v_readlane_b32 s84, v116, 0\n\t" \
  "v_readlane_b32 s85, v116, 16\n\t" \
  "s_max_u32 s84, s84, s85\n\t" \
  "s_add_i32 s84, s84, s51\n\t" \
  "v_mov_b64 v[68:69], 0\n\t" \
  "v_mov_b64 v[70:71], 0\n\t" \
  "v_mov_b64 v[72:73], 0\n\t" \
  "v_mov_b64 v[74:75], 0\n\t" \
  "v_mov_b64 v[76:77], 0\n\t" \
  "v_mov_b64 v[78:79], 0\n\t" \
  "v_mov_b64 v[80:81], 0\n\t" \
  "v_mov_b64 v[82:83], 0\n\t" \
  "1:\n\t" \
  "s_memtime vcc\n\t" \
  "s_waitcnt lgkmcnt(0)\n\t" \
  "s_sub_i32 s79, s84, vcc_lo\n\t" \
  "s_cmp_gt_i32 s79, 128\n\t" \
  "s_cbranch_scc1 1b\n\t" \
  "2:\n\t" \
  "s_sub_i32 s79, s79, 33\n\t" \
  "s_cmp_gt_i32 s79, 56\n\t" \
  "s_cbranch_scc1 2b\n\t" \
  "buffer_load_dwordx4 v[32:35], v100, %[rh], s40 offen sc1\n\t" \
  "s_nop 12\n\t" \
  "buffer_load_dwordx4 v[36:39], v100, %[rh], s76 offen sc1\n\t" \
  "s_nop 12\n\t" \
  "buffer_load_dwordx4 v[40:43], v100, %[rh], s77 offen sc1\n\t" \
  "s_nop 12\n\t" \
  "buffer_load_dwordx4 v[44:47], v100, %[rh], s78 offen sc1\n\t" \
  "s_memtime vcc\n\t" \
  "s_memrealtime s[82:83]\n\t" \
  "s_nop 4\n\t" \
  "s_memrealtime s[84:85]\n\t" \
  "s_nop 4\n\t" \
  "s_memrealtime s[94:95]\n\t" \
  "s_nop 4\n\t" \
  "s_memrealtime s[86:87]\n\t" \
  "s_nop 4\n\t" \
  "s_memrealtime s[88:89]\n\t" \
  "s_nop 4\n\t" \
  "s_memrealtime s[90:91]\n\t" \
  "s_mov_b32 s92, 0\n\t" \
  "s_waitcnt lgkmcnt(0)\n\t" \
  "s_cmp_eq_u32 s82, s84\n\t" \
  "s_addc_u32 s92, s92, 0\n\t" \
  "s_cmp_eq_u32 s82, s94\n\t" \
  "s_addc_u32 s92, s92, 0\n\t" \
  "s_cmp_eq_u32 s82, s86\n\t" \
  "s_addc_u32 s92, s92, 0\n\t" \
  "s_cmp_eq_u32 s82, s88\n\t" \
  "s_addc_u32 s92, s92, 0\n\t" \
  "s_cmp_eq_u32 s82, s90\n\t" \
  "s_addc_u32 s92, s92, 0\n\t" \
  "s_mul_i32 s92, s92, 24\n\t" \
  "s_add_i32 s92, s92, vcc_lo\n\t" \
  "s_lshl4_add_u32 s93, s82, 64\n\t" \
  "3:\n\t" \
  "s_waitcnt lgkmcnt(0)\n\t" \
  "s_waitcnt vmcnt(3)\n\t" \
  "v_smfmac_f32_16x16x64_bf16 v[68:71], v[32:35], a[96:103], v104\n\t" \
  "ds_write_b128 v101, v[32:35] offset:4096\n\t" \
  "v_smfmac_f32_16x16x64_bf16 v[72:75], v[32:35], a[104:111], v104\n\t" \
  "v_smfmac_f32_16x16x64_bf16 v[76:79], v[32:35], v[48:55], v104\n\t" \
  "v_smfmac_f32_16x16x64_bf16 v[80:83], v[32:35], v[56:63], v104\n\t" \
  "s_waitcnt vmcnt(2)\n\t" \
  "v_smfmac_f32_16x16x64_bf16 v[68:71], v[36:39], a[112:119], v104\n\t" \
  "ds_write_b128 v101, v[36:39] offset:5120\n\t" \
  "v_smfmac_f32_16x16x64_bf16 v[72:75], v[36:39], a[120:127], v104\n\t" \
  "v_smfmac_f32_16x16x64_bf16 v[76:79], v[36:39], a[128:135], v104\n\t" \
  "v_cmp_u_f32 vcc, v68, v69\n\t" \
  "v_smfmac_f32_16x16x64_bf16 v[80:83], v[36:39], a[136:143], v104\n\t" \
  "v_cmp_u_f32 s[82:83], v70, v71\n\t" \
  "s_waitcnt vmcnt(1)\n\t" \
  "v_smfmac_f32_16x16x64_bf16 v[68:71], v[40:43], v[16:23], v104\n\t" \
  "ds_write_b128 v101, v[40:43] offset:6144\n\t" \
  "v_smfmac_f32_16x16x64_bf16 v[72:75], v[40:43], v[24:31], v104\n\t" \
  "s_or_b64 vcc, vcc, s[82:83]\n\t" \
  "s_or_b64 vcc, vcc, s[80:81]\n\t" \
  "s_waitcnt lgkmcnt(1)\n\t" \
  "v_smfmac_f32_16x16x64_bf16 v[76:79], v[40:43], a[144:151], v104\n\t" \
  "s_cbranch_scc1 8f\n\t" \
  "5:\n\t" \
  "s_barrier\n\t" \
  "4:\n\t" \
  "ds_read_b128 v[48:51], v101 offset:8192\n\t" \
  "v_smfmac_f32_16x16x64_bf16 v[80:83], v[40:43], a[152:159], v104\n\t" \
  "ds_read_b128 v[52:55], v101 offset:9216\n\t" \
  "ds_read_b128 v[56:59], v101 offset:12288\n\t" \
  "s_waitcnt vmcnt(0)\n\t" \
  "v_smfmac_f32_16x16x64_bf16 v[68:71], v[44:47], a[160:167], v104\n\t" \
  "ds_write_b128 v101, v[44:47] offset:7168\n\t" \
  "v_smfmac_f32_16x16x64_bf16 v[72:75], v[44:47], a[168:175], v104\n\t" \
  "v_smfmac_f32_16x16x64_bf16 v[76:79], v[44:47], a[176:183], v104\n\t" \
  "v_cmp_u_f32 vcc, v68, v69\n\t" \
  "v_smfmac_f32_16x16x64_bf16 v[80:83], v[44:47], a[184:191], v104\n\t" \
  "v_cmp_u_f32 s[82:83], v70, v71\n\t" \
  "s_waitcnt lgkmcnt(3)\n\t" \
  "v_smfmac_f32_16x16x64_bf16 v[68:71], v[48:51], a[192:199], v104\n\t" \
  "ds_read_b128 v[60:63], v101 offset:13312\n\t" \
  "ds_read_b128 v[64:67], v101 offset:0\n\t" \
  "v_smfmac_f32_16x16x64_bf16 v[72:75], v[48:51], a[200:207], v104\n\t" \
  "s_or_b64 vcc, vcc, s[82:83]\n\t" \
  "s_waitcnt lgkmcnt(2)\n\t" \
  "v_smfmac_f32_16x16x64_bf16 v[68:71], v[52:55], a[208:215], v104\n\t" \
  "s_cbranch_scc1 9f\n\t" \
  "6:\n\t" \
  "s_barrier\n\t" \
  "v_smfmac_f32_16x16x64_bf16 v[72:75], v[52:55], a[216:223], v104\n\t" \
  "ds_read_b128 v[48:51], v101 offset:1024\n\t" \
  "v_smfmac_f32_16x16x64_bf16 v[68:71], v[56:59], v[152:159], v104\n\t" \
  "ds_read_b128 v[52:55], v101 offset:10240\n\t" \
  "v_smfmac_f32_16x16x64_bf16 v[72:75], v[56:59], v[160:167], v104\n\t" \
  "s_xor_b32 s94, s47, 0x5000\n\t" \
  "s_waitcnt lgkmcnt(3)\n\t" \
  "v_smfmac_f32_16x16x64_bf16 v[68:71], v[60:63], v[168:175], v104\n\t" \
  "ds_read_b128 v[56:59], v101 offset:11264\n\t" \
  "v_smfmac_f32_16x16x64_bf16 v[72:75], v[60:63], v[176:183], v104\n\t" \
  "s_xor_b32 s95, s47, 0x4000\n\t" \
  "s_waitcnt lgkmcnt(3)\n\t" \
  "v_smfmac_f32_16x16x64_bf16 v[68:71], v[64:67], a[0:7], v104\n\t" \
  "ds_read_b128 v[60:63], v101 offset:14336\n\t" \
  "ds_read_b128 v[32:35], v119 offset:0\n\t" \
  "v_smfmac_f32_16x16x64_bf16 v[72:75], v[64:67], a[8:15], v104\n\t" \
  "ds_read_b128 v[16:19], v102 offset:16384\n\t" \
  "s_waitcnt lgkmcnt(5)\n\t" \
  "v_smfmac_f32_16x16x64_bf16 v[68:71], v[48:51], a[16:23], v104\n\t" \
  "ds_read_b128 v[64:67], v101 offset:15360\n\t" \
  "ds_read_b128 v[36:39], v119 offset:1024\n\t" \
  "v_smfmac_f32_16x16x64_bf16 v[72:75], v[48:51], a[24:31], v104\n\t" \
  "ds_read_b128 v[20:23], v102 offset:17408\n\t" \
  "s_waitcnt lgkmcnt(7)\n\t" \
  "v_smfmac_f32_16x16x64_bf16 v[68:71], v[52:55], v[84:91], v104\n\t" \
  "ds_read_b128 v[48:51], v101 offset:3072\n\t" \
  "ds_read_b128 v[40:43], v119 offset:16\n\t" \
  "v_smfmac_f32_16x16x64_bf16 v[72:75], v[52:55], v[92:99], v104\n\t" \
  "ds_read_b128 v[24:27], v102 offset:18432\n\t" \
  "s_waitcnt lgkmcnt(9)\n\t" \
  "v_smfmac_f32_16x16x64_bf16 v[68:71], v[56:59], v[120:127], v104\n\t" \
  "ds_read_b128 v[52:55], v101 offset:2048\n\t" \
  "ds_read_b128 v[44:47], v119 offset:1040\n\t" \
  "v_smfmac_f32_16x16x64_bf16 v[72:75], v[56:59], v[128:135], v104\n\t" \
  "ds_read_b128 v[28:31], v102 offset:19456\n\t" \
  "s_waitcnt lgkmcnt(11)\n\t" \
  "v_smfmac_f32_16x16x64_bf16 v[68:71], v[60:63], v[0:7], v104\n\t" \
  "ds_read_b128 v[92:95], v119 offset:2048\n\t" \
  "v_smfmac_f32_16x16x64_bf16 v[72:75], v[60:63], v[8:15], v104\n\t" \
  "ds_read_b128 v[96:99], v119 offset:3072\n\t" \
  "s_waitcnt lgkmcnt(10)\n\t" \
  "v_smfmac_f32_16x16x64_bf16 v[68:71], v[64:67], v[216:223], v104\n\t" \
  "ds_read_b128 v[0:3], v103 offset:0\n\t" \
  "v_smfmac_f32_16x16x64_bf16 v[72:75], v[64:67], v[224:231], v104\n\t" \
  "ds_read_b128 v[4:7], v103 offset:1024\n\t" \
  "s_waitcnt lgkmcnt(9)\n\t" \
  "v_smfmac_f32_16x16x64_bf16 v[68:71], v[48:51], a[64:71], v104\n\t" \
  "ds_read_b128 v[8:11], v103 offset:2048\n\t" \
  "s_waitcnt lgkmcnt(7)\n\t" \
  "v_smfmac_f32_16x16x64_bf16 v[68:71], v[52:55], v[16:23], v104\n\t" \
  "ds_read_b128 v[12:15], v103 offset:3072\n\t" \
  "v_smfmac_f32_16x16x64_bf16 v[72:75], v[48:51], a[72:79], v104\n\t" \
  "ds_read_b128 v[16:19], v119 offset:2064\n\t" \
  "ds_read_b128 v[20:23], v119 offset:3088\n\t" \
  "s_waitcnt lgkmcnt(8)\n\t" \
  "v_smfmac_f32_16x16x64_bf16 v[72:75], v[52:55], v[24:31], v104\n\t" \
  "v_add_f32 v116, v68, v70\n\t" \
  "v_add_f32 v117, v69, v71\n\t" \
  "v_smfmac_f32_16x16x64_bf16 v[76:79], v[56:59], v[136:143], v104\n\t" \
  "v_cvt_pk_bf16_f32 v116, v116, v117\n\t" \
  "ds_read_b128 v[24:27], v103 offset:34816\n\t" \
  "v_smfmac_f32_16x16x64_bf16 v[80:83], v[56:59], v[144:151], v104\n\t" \
  "v_add_f32 v117, v72, v74\n\t" \
  "v_add_f32 v118, v73, v75\n\t" \
  "v_cvt_pk_bf16_f32 v117, v117, v118\n\t" \
  "ds_write2st64_b32 v105, v116, v117 offset1:1\n\t" \
  "v_smfmac_f32_16x16x64_bf16 v[76:79], v[60:63], v[200:207], v104\n\t" \
  "v_mov_b64 v[68:69], 0\n\t" \
  "s_xor_b32 s96, s47, 0x1000\n\t" \
  "v_smfmac_f32_16x16x64_bf16 v[80:83], v[60:63], v[208:215], v104\n\t" \
  "v_mov_b64 v[70:71], 0\n\t" \
  "v_smfmac_f32_16x16x64_bf16 v[76:79], v[64:67], v[232:239], v104\n\t" \
  "v_mov_b64 v[72:73], 0\n\t" \
  "v_smfmac_f32_16x16x64_bf16 v[80:83], v[64:67], v[240:247], v104\n\t" \
  "s_waitcnt lgkmcnt(0)\n\t" \
  "s_barrier\n\t" \
  "ds_read_b64_tr_b16 v[84:85], v107 offset:0\n\t" \
  "ds_read_b64_tr_b16 v[86:87], v107 offset:128\n\t" \
  "v_smfmac_f32_16x16x64_bf16 v[76:79], v[48:51], a[80:87], v104\n\t" \
  "ds_read_b64_tr_b16 v[88:89], v107 offset:1024\n\t" \
  "ds_read_b64_tr_b16 v[90:91], v107 offset:1152\n\t" \
  "v_smfmac_f32_16x16x64_bf16 v[80:83], v[48:51], a[88:95], v104\n\t" \
  "ds_read_b128 v[56:59], v101 offset:8192\n\t" \
  "v_mov_b64 v[74:75], 0\n\t" \
  "v_smfmac_f32_16x16x64_bf16 v[76:79], v[52:55], a[48:55], v104\n\t" \
  "ds_read_b128 v[60:63], v101 offset:1024\n\t" \
  "ds_read_b128 v[28:31], v103 offset:35840\n\t" \
  "v_smfmac_f32_16x16x64_bf16 v[80:83], v[52:55], a[56:63], v104\n\t" \
  "s_waitcnt lgkmcnt(5)\n\t" \
  "v_smfmac_f32_16x16x64_bf16 v[68:71], v[84:87], v[32:39], v104\n\t" \
  "v_smfmac_f32_16x16x64_bf16 v[72:75], v[84:87], v[40:47], v104\n\t" \
  "ds_read_b128 v[64:67], v101 offset:10240\n\t" \
  "s_waitcnt lgkmcnt(4)\n\t" \
  "v_smfmac_f32_16x16x64_bf16 v[68:71], v[88:91], v[92:99], v104\n\t" \
  "v_smfmac_f32_16x16x64_bf16 v[72:75], v[88:91], v[16:23], v104\n\t" \
  "ds_read_b128 v[48:51], v101 offset:12288\n\t" \
  "s_waitcnt lgkmcnt(4)\n\t" \
  "v_smfmac_f32_16x16x64_bf16 v[76:79], v[56:59], v[0:7], v104\n\t" \
  "ds_read_b128 v[16:19], v103 offset:32768\n\t" \
  "v_add_f32 v40, v69, v71\n\t" \
  "v_smfmac_f32_16x16x64_bf16 v[80:83], v[56:59], v[8:15], v104\n\t" \
  "v_add_f32 v41, v73, v75\n\t" \
  "v_max_f32 v42, v40, v41\n\t" \
  "s_waitcnt lgkmcnt(4)\n\t" \
  "v_smfmac_f32_16x16x64_bf16 v[76:79], v[60:63], a[32:39], v104\n\t" \
  "ds_read_b128 v[20:23], v103 offset:33792\n\t" \
  "v_max_f32_dpp v42, v42, v42 quad_perm:[1,0,3,2] row_mask:0xf bank_mask:0xf\n\t" \
  "v_smfmac_f32_16x16x64_bf16 v[80:83], v[60:63], a[40:47], v104\n\t" \
  "ds_read_b128 v[52:55], v101 offset:9216\n\t" \
  "v_max_f32_dpp v42, v42, v42 quad_perm:[2,3,0,1] row_mask:0xf bank_mask:0xf\n\t" \
  "s_waitcnt lgkmcnt(4)\n\t" \
  "v_smfmac_f32_16x16x64_bf16 v[76:79], v[64:67], a[240:247], v104\n\t" \
  "ds_read_b128 v[56:59], v101 offset:13312\n\t" \
  "v_max_f32_dpp v42, v42, v42 row_half_mirror row_mask:0xf bank_mask:0xf\n\t" \
  "v_smfmac_f32_16x16x64_bf16 v[80:83], v[64:67], a[248:255], v104\n\t" \
  "ds_read_b128 v[60:63], v101 offset:0\n\t" \
  "v_max_f32_dpp v42, v42, v42 row_mirror row_mask:0xf bank_mask:0xf\n\t" \
  "v_mul_f32 v42, %[cs], v42\n\t" \
  "s_waitcnt lgkmcnt(3)\n\t" \
  "v_smfmac_f32_16x16x64_bf16 v[76:79], v[48:51], v[16:23], v104\n\t" \
  "ds_read_b128 v[0:3], v102 offset:0\n\t" \
  "v_fma_f32 v40, v40, %[cs], -v42\n\t" \
  "v_fma_f32 v41, v41, %[cs], -v42\n\t" \
  "v_smfmac_f32_16x16x64_bf16 v[80:83], v[48:51], v[24:31], v104\n\t" \
  "ds_read_b128 v[4:7], v102 offset:1024\n\t" \
  "v_exp_f32 v43, v40\n\t" \
  "v_exp_f32 v44, v41\n\t" \
  "s_waitcnt lgkmcnt(4)\n\t" \
  "v_smfmac_f32_16x16x64_bf16 v[76:79], v[52:55], a[224:231], v104\n\t" \
  "ds_read_b128 v[8:11], v102 offset:2048\n\t" \
  "v_cvt_pk_bf16_f32 v45, v43, v44\n\t" \
  "buffer_store_dword v45, v112, %[rh], s94 offen sc1\n\t" \
  "v_smfmac_f32_16x16x64_bf16 v[80:83], v[52:55], a[232:239], v104\n\t" \
  "ds_read_b128 v[12:15], v102 offset:3072\n\t" \
  "buffer_store_dword v45, v112, %[rh], s95 offen sc1\n\t" \
  "s_waitcnt lgkmcnt(5)\n\t" \
  "v_smfmac_f32_16x16x64_bf16 v[76:79], v[56:59], v[184:191], v104\n\t" \
  "buffer_store_dword v45, v112, %[rh], s96 offen sc1\n\t" \
  "s_waitcnt lgkmcnt(2)\n\t" \
  "v_smfmac_f32_16x16x64_bf16 v[76:79], v[60:63], v[0:7], v104\n\t" \
  "buffer_store_dword v45, v112, %[rh], s47 offen sc1\n\t" \
  "v_smfmac_f32_16x16x64_bf16 v[80:83], v[56:59], v[192:199], v104\n\t" \
  "s_waitcnt lgkmcnt(0)\n\t" \
  "v_smfmac_f32_16x16x64_bf16 v[80:83], v[60:63], v[8:15], v104\n\t" \
  "v_add_f32 v20, v76, v78\n\t" \
  "v_add_f32 v21, v77, v79\n\t" \
  "v_cvt_pk_bf16_f32 v20, v20, v21\n\t" \
  "ds_write_b32 v106, v20\n\t" \
  "v_add_f32 v92, v43, v44\n\t" \
  "v_mov_b32 v25, v115\n\t" \
  "s_nop 1\n\t" \
  "v_add_f32 v23, v80, v82\n\t" \
  "v_add_f32 v21, v81, v83\n\t" \
  "v_cvt_pk_bf16_f32 v21, v23, v21\n\t" \
  "ds_write_b32 v106, v21 offset:256\n\t" \
  "v_add_f32_dpp v92, v92, v92 row_ror:8 row_mask:0xf bank_mask:0xf\n\t" \
  "v_mov_b64 v[0:1], 0\n\t" \
  "v_mov_b64 v[2:3], 0\n\t" \
  "v_mov_b32 v27, v114\n\t" \
  "s_waitcnt lgkmcnt(0)\n\t" \
  "s_barrier\n\t" \
  "ds_read_b64_tr_b16 v[4:5], v108 offset:0\n\t" \
  "ds_read_b64_tr_b16 v[6:7], v108 offset:64\n\t" \
  "ds_read_b64_tr_b16 v[8:9], v108 offset:512\n\t" \
  "ds_read_b64_tr_b16 v[10:11], v108 offset:576\n\t" \
  "ds_read_b64_tr_b16 v[12:13], v108 offset:1024\n\t" \
  "ds_read_b64_tr_b16 v[14:15], v108 offset:1088\n\t" \
  "ds_read_b64_tr_b16 v[16:17], v108 offset:1536\n\t" \
  "ds_read_b64_tr_b16 v[18:19], v108 offset:1600\n\t" \
  "s_waitcnt lgkmcnt(4)\n\t" \
  "v_smfmac_f32_16x16x64_bf16 v[0:3], v[84:87], v[4:11], v104\n\t" \
  "s_waitcnt lgkmcnt(0)\n\t" \
  "v_smfmac_f32_16x16x64_bf16 v[0:3], v[88:91], v[12:19], v104\n\t" \
  "s_nop 7\n\t" \
  "v_add_f32 v96, v1, v3\n\t" \
  "v_fma_f32 v96, v96, %[cs], -v42\n\t" \
  "v_exp_f32 v24, v96\n\t" \
  "s_nop 0\n\t" \
  "v_add_f32 v29, v92, v24\n\t" \
  "s_nop 0\n\t" \
  "v_mov_b32_dpp v25, v24 quad_perm:[1,0,3,2] row_mask:0xf bank_mask:0x3\n\t" \
  "v_add_f32_dpp v29, v29, v29 quad_perm:[1,0,3,2] row_mask:0xf bank_mask:0xf\n\t" \
  "v_mov_b32_dpp v27, v24 quad_perm:[0,1,2,3] row_mask:0xf bank_mask:0x3\n\t" \
  "s_nop 0\n\t" \
  "v_add_f32_dpp v29, v29, v29 quad_perm:[2,3,0,1] row_mask:0xf bank_mask:0xf\n\t" \
  "s_nop 1\n\t" \
  "v_add_f32_dpp v29, v29, v29 row_half_mirror row_mask:0xf bank_mask:0xf\n\t" \
  "v_rcp_f32 v26, v29\n\t" \
  "v_cmpx_gt_f32 vcc, 0x5d5e0b6b, v29\n\t" \
  "v_mul_f32 v24, v27, v26\n\t" \
  "v_mul_f32 v25, v25, v26\n\t" \
  "v_cvt_pk_bf16_f32 v24, v24, v25\n\t" \
  "buffer_store_dword v24, v113, %[rh], s94 offen sc1\n\t" \
  "buffer_store_dword v24, v113, %[rh], s95 offen sc1\n\t" \
  "buffer_store_dword v24, v113, %[rh], s96 offen sc1\n\t" \
  "buffer_store_dword v24, v113, %[rh], s47 offen sc1\n\t" \
  "s_memtime s[76:77]\n\t" \
  "s_mov_b64 exec, -1\n\t" \
  "v_mov_b32 v97, v29\n\t" \
  "s_cmp_eq_u64 vcc, -1\n\t" \
  "s_cselect_b32 s42, 0, 8\n\t" \
  "s_waitcnt lgkmcnt(0)\n\t" \
  "s_mov_b32 s57, s76\n\t" \
  "s_add_i32 s41, s57, 400\n\t" \
  "s_sub_i32 s76, s92, s51\n\t" \
  "s_sub_i32 s77, s93, s53\n\t" \
  "s_cmp_eq_u32 s76, 0\n\t" \
  "s_cbranch_scc1 21f\n\t" \
  "v_cvt_f32_u32 v117, s76\n\t" \
  "v_cvt_f32_u32 v116, s77\n\t" \
  "v_rcp_f32 v117, v117\n\t" \
  "s_mov_b32 s51, s92\n\t" \
  "v_mul_f32 v117, v117, v116\n\t" \
  "v_max_f32 v117, 0x3f028f5c, v117\n\t" \
  "v_min_f32 v117, 0x3ffeb852, v117\n\t" \
  "v_rcp_f32 v116, v117\n\t" \
  "v_mul_f32 v117, 0x4f000000, v117\n\t" \
  "v_mul_f32 v116, 0x47800000, v116\n\t" \
  "v_cvt_u32_f32 v117, v117\n\t" \
  "v_cvt_u32_f32 v116, v116\n\t" \
  "s_mov_b32 s53, s93\n\t" \
  "v_readfirstlane_b32 s46, v117\n\t" \
  "v_readfirstlane_b32 s52, v116\n\t" \
  "21:\n\t" \
  "v_mov_b64 v[0:1], -1\n\t" \
  "v_mov_b64 v[2:3], -1\n\t" \
  "s_sleep 2\n\t" \
  "s_add_i32 s76, s48, 0x4000\n\t" \
  "buffer_store_dwordx4 v[0:3], v109, %[rh], s76 offen sc1\n\t" \
  "s_add_i32 s76, s48, 0x5000\n\t" \
  "buffer_store_dwordx4 v[0:3], v109, %[rh], s76 offen sc1\n\t" \
  "s_add_i32 s44, s44, 1\n\t" \
  "s_and_b32 s58, s58, 4\n\t" \
  "s_and_b32 s76, s44, 64\n\t" \
  "s_or_b32 s42, s42, s76\n\t" \
  "s_cbranch_scc0 20b\n\t" \
  "s_branch 99f\n\t" \
  "14:\n\t" \
  "s_sleep 0\n\t" \
  "s_add_u32 s59, s59, 0x10000\n\t" \
  "s_cmp_lt_u32 s59, 0x100000\n\t" \
  "s_cbranch_scc1 13b\n\t" \
  "s_mov_b64 exec, -1\n\t" \
  "s_mov_b32 s42, 16\n\t" \
  "s_branch 99f\n\t" \
  "8:\n\t" \
  "s_bitcmp1_b32 s58, 1\n\t" \
  "s_cbranch_scc1 4b\n\t" \
  "s_bitcmp1_b32 s58, 2\n\t" \
  "s_cbranch_scc1 5b\n\t" \
  "v_max3_u32 v116, v32, v33, v34\n\t" \
  "v_max3_u32 v117, v35, v36, v37\n\t" \
  "v_max3_u32 v116, v116, v38, v39\n\t" \
  "v_max_u32 v116, v116, v117\n\t" \
  "v_cmp_eq_u32 vcc, -1, v116\n\t" \
  "s_cbranch_vccz 5b\n\t" \
  "s_branch 0f\n\t" \
  "9:\n\t" \
  "s_bitcmp1_b32 s58, 2\n\t" \
  "s_cbranch_scc1 6b\n\t" \
  "v_max3_u32 v116, v40, v41, v42\n\t" \
  "v_max3_u32 v117, v43, v44, v45\n\t" \
  "v_max3_u32 v116, v116, v46, v47\n\t" \
  "v_max_u32 v116, v116, v117\n\t" \
  "v_cmp_eq_u32 vcc, -1, v116\n\t" \
  "s_cbranch_vccz 6b\n\t" \
  "s_or_b32 s58, s58, 2\n\t" \
  "0:\n\t" \
  "s_or_b32 s58, s58, 1\n\t" \
  "s_sleep 0\n\t" \
  "11:\n\t" \
  "buffer_load_dwordx4 v[32:35], v100, %[rh], s40 offen sc1\n\t" \
  "buffer_load_dwordx4 v[36:39], v100, %[rh], s76 offen sc1\n\t" \
  "buffer_load_dwordx4 v[40:43], v100, %[rh], s77 offen sc1\n\t" \
  "buffer_load_dwordx4 v[44:47], v100, %[rh], s78 offen sc1\n\t" \
  "s_waitcnt lgkmcnt(0)\n\t" \
  "s_branch 10b\n\t" \
  "17:\n\t" \
  "v_mov_b64 v[68:69], 0\n\t" \
  "v_mov_b64 v[70:71], 0\n\t" \
  "v_mov_b64 v[72:73], 0\n\t" \
  "v_mov_b64 v[74:75], 0\n\t" \
  "v_mov_b64 v[76:77], 0\n\t" \
  "v_mov_b64 v[78:79], 0\n\t" \
  "v_mov_b64 v[80:81], 0\n\t" \
  "v_mov_b64 v[82:83], 0\n\t" \
  "s_add_u32 s59, s59, 1\n\t" \
  "s_bitcmp1_b32 s58, 2\n\t" \
  "s_cbranch_scc1 3b\n\t" \
  "s_waitcnt vmcnt(0)\n\t" \
  "v_max_u32 v116, v32, v33\n\t" \
  "v_max3_u32 v116, v116, v34, v35\n\t" \
  "v_max3_u32 v116, v116, v36, v37\n\t" \
  "v_max3_u32 v116, v116, v38, v39\n\t" \
  "v_max3_u32 v116, v116, v40, v41\n\t" \
  "v_max3_u32 v116, v116, v42, v43\n\t" \
  "v_max3_u32 v116, v116, v44, v45\n\t" \
  "v_max3_u32 v116, v116, v46, v47\n\t" \
  "v_cmp_eq_u32 vcc, -1, v116\n\t" \
  "s_cbranch_vccz 3b\n\t" \
  "s_bitcmp1_b32 s59, 6\n\t" \
  "s_cbranch_scc1 12f\n\t" \
  "buffer_load_dwordx4 v[32:35], v100, %[rh], s40 offen sc1\n\t" \
  "buffer_load_dwordx4 v[36:39], v100, %[rh], s76 offen sc1\n\t" \
  "buffer_load_dwordx4 v[40:43], v100, %[rh], s77 offen sc1\n\t" \
  "buffer_load_dwordx4 v[44:47], v100, %[rh], s78 offen sc1\n\t" \
  "s_branch 17b\n\t" \
  "12:\n\t" \
  "s_mov_b32 s42, s58\n\t" \
  "s_branch 99f\n\t" \
  "92:\n\t" \
  "s_mov_b32 s58, s42\n\t" \
  ".p2align 6\n\t" \
  "20:\n\t" \
  "s_and_b32 s76, s44, 7\n\t" \
  "s_lshl_b32 s76, s76, 15\n\t" \
  "s_add_i32 s40, s54, s76\n\t" \
  "s_and_b32 s76, s44, 3\n\t" \
  "s_lshl_b32 s45, s76, 16\n\t" \
  "s_add_i32 s47, s44, 0\n\t" \
  "s_min_u32 s47, s47, 63\n\t" \
  "s_lshr_b32 s76, s47, 2\n\t" \
  "s_bitcmp1_b32 %[hm], s76\n\t" \
  "s_cselect_b32 s77, 0x2000, 0\n\t" \
  "s_cmp_eq_u32 s76, 0\n\t" \
  "s_cselect_b32 s77, %[d0], s77\n\t" \
  "s_lshl_b32 s47, s47, 19\n\t" \
  "s_add_i32 s47, s47, s55\n\t" \
  "s_add_i32 s47, s47, s77\n\t" \
  "s_add_i32 s97, s47, 0x1000\n\t" \
  "s_mov_b32 s96, 1\n\t" \
  "s_add_i32 s49, s44, -1\n\t" \
  "s_and_b32 s48, s49, 3\n\t" \
  "s_lshl_b32 s48, s48, 16\n\t" \
  "s_lshl_b32 s50, s53, 8\n\t" \
  "s_mov_b32 s59, 0\n\t" \
  "s_mov_b32 s92, s51\n\t" \
  "s_mov_b32 s93, s53\n\t" \
  "s_xor_b32 s76, s40, 0x1000\n\t" \
  "s_xor_b32 s77, s40, 0x4000\n\t" \
  "s_xor_b32 s78, s40, 0x5000\n\t" \
  "s_cmp_lg_u32 s58, 0\n\t" \
  "s_cbranch_scc1 11f\n\t" \
  "10:\n\t" \
  "ds_read_b128 v[48:51], v103 offset:0\n\t" \
  "ds_read_b128 v[52:55], v103 offset:1024\n\t" \
  "ds_read_b128 v[56:59], v103 offset:2048\n\t" \
  "ds_read_b128 v[60:63], v103 offset:3072\n\t" \
  "ds_read_b128 v[16:19], v103 offset:16384\n\t" \
  "ds_read_b128 v[20:23], v103 offset:17408\n\t" \
  "ds_read_b128 v[24:27], v103 offset:18432\n\t" \
  "ds_read_b128 v[28:31], v103 offset:19456\n\t" \
  "ds_read_b128 v[84:87], v103 offset:49152\n\t" \
  "ds_read_b128 v[88:91], v103 offset:50176\n\t" \
  "ds_read_b128 v[92:95], v103 offset:51200\n\t" \
  "ds_read_b128 v[96:99], v103 offset:52224\n\t" \
  "s_waitcnt lgkmcnt(4)\n\t" \
  "ds_read_b128 v[0:3], v102 offset:16384\n\t" \
  "ds_read_b128 v[4:7], v102 offset:17408\n\t" \
  "ds_read_b128 v[8:11], v102 offset:18432\n\t" \
  "ds_read_b128 v[12:15], v102 offset:19456\n\t" \
  "s_cmp_eq_u32 s96, 0\n\t" \
  "s_cbranch_scc1 22f\n\t" \
  "s_mov_b32 s96, 0\n\t" \
  "v_mov_b32 v116, s49\n\t" \
  "s_waitcnt vmcnt(0)\n\t" \
  "buffer_store_dword v116, v109, %[rh], s48 offen sc1\n\t" \
  "22:\n\t" \
  "s_bitcmp1_b32 s58, 1\n\t" \
  "s_cselect_b64 s[80:81], -1, 0\n\t" \
  "s_cmp_lg_u32 s58, 0\n\t" \
  "s_cbranch_scc1 17f\n\t" \
  "s_mov_b32 exec_hi, 0\n\t" \
  "s_waitcnt vmcnt(2)\n\t" \
  "v_mov_b32 v116, v118\n\t" \
  "s_branch 15f\n\t" \
  "13:\n\t" \
  "buffer_load_dword v116, v110, %[rh], s48 offen sc1\n\t" \
  "s_waitcnt vmcnt(0)\n\t" \
  "15:\n\t" \
  "v_mov_b32 v117, 20000\n\t" \
  "v_cmp_ne_u32_sdwa vcc, v116, s49 src0_sel:BYTE_0 src1_sel:DWORD\n\t" \
  "v_subrev_u32 v116, s50, v116\n\t" \
  "v_ashrrev_i32 v116, 8, v116\n\t" \
  "s_nop 1\n\t" \
  "v_max_i32_dpp v116, v116, v116 quad_perm:[1,0,3,2] row_mask:0xf bank_mask:0xf\n\t" \
  "s_cbranch_vccnz 14f\n\t" \
  "s_nop 0\n\t" \
  "v_max_i32_dpp v116, v116, v116 quad_perm:[2,3,0,1] row_mask:0xf bank_mask:0xf\n\t" \
  "s_nop 1\n\t" \
  "v_max_i32_dpp v116, v116, v116 row_half_mirror row_mask:0xf bank_mask:0xf\n\t" \
  "s_nop 1\n\t" \
  "v_max_i32_dpp v116, v116, v116 row_mirror row_mask:0xf bank_mask:0xf\n\t" \
  "v_med3_i32 v116, v116, 0, v117\n\t" \
  "v_mul_u32_u24 v116, s52, v116\n\t" \
  "v_lshrrev_b32 v116, 16, v116\n\t" \
  "s_mov_b64 exec, -1\n\t" \
  "v_readlane_b32 s84, v116, 0\n\t" \
  "v_readlane_b32 s85, v116, 16\n\t" \
  "s_max_u32 s84, s84, s85\n\t" \
  "s_add_i32 s84, s84, s51\n\t" \
  "v_mov_b64 v[68:69], 0\n\t" \
  "v_mov_b64 v[70:71], 0\n\t" \
  "v_mov_b64 v[72:73], 0\n\t" \
  "v_mov_b64 v[74:75], 0\n\t" \
  "v_mov_b64 v[76:77], 0\n\t" \
  "v_mov_b64 v[78:79], 0\n\t" \
  "v_mov_b64 v[80:81], 0\n\t" \
  "v_mov_b64 v[82:83], 0\n\t" \
  "1:\n\t" \
  "s_memtime vcc\n\t" \
  "s_waitcnt lgkmcnt(0)\n\t" \
  "s_sub_i32 s79, s84, vcc_lo\n\t" \
  "s_cmp_gt_i32 s79, 128\n\t" \
  "s_cbranch_scc1 1b\n\t" \
  "2:\n\t" \
  "s_sub_i32 s79, s79, 33\n\t" \
  "s_cmp_gt_i32 s79, 56\n\t" \
  "s_cbranch_scc1 2b\n\t" \
  "buffer_load_dwordx4 v[32:35], v100, %[rh], s40 offen sc1\n\t" \
  "s_nop 12\n\t" \
  "buffer_load_dwordx4 v[36:39], v100, %[rh], s76 offen sc1\n\t" \
  "s_nop 12\n\t" \
  "buffer_load_dwordx4 v[40:43], v100, %[rh], s77 offen sc1\n\t" \
  "s_nop 12\n\t" \
  "buffer_load_dwordx4 v[44:47], v100, %[rh], s78 offen sc1\n\t" \
  "s_memtime vcc\n\t" \
  "s_memrealtime s[82:83]\n\t" \
  "s_nop 4\n\t" \
  "s_memrealtime s[84:85]\n\t" \
  "s_nop 4\n\t" \
  "s_memrealtime s[94:95]\n\t" \
  "s_nop 4\n\t" \
  "s_memrealtime s[86:87]\n\t" \
  "s_nop 4\n\t" \
  "s_memrealtime s[88:89]\n\t" \
  "s_nop 4\n\t" \
  "s_memrealtime s[90:91]\n\t" \
  "s_mov_b32 s92, 0\n\t" \
  "s_waitcnt lgkmcnt(0)\n\t" \
  "s_cmp_eq_u32 s82, s84\n\t" \
  "s_addc_u32 s92, s92, 0\n\t" \
  "s_cmp_eq_u32 s82, s94\n\t" \
  "s_addc_u32 s92, s92, 0\n\t" \
  "s_cmp_eq_u32 s82, s86\n\t" \
  "s_addc_u32 s92, s92, 0\n\t" \
  "s_cmp_eq_u32 s82, s88\n\t" \
  "s_addc_u32 s92, s92, 0\n\t" \
  "s_cmp_eq_u32 s82, s90\n\t" \
  "s_addc_u32 s92, s92, 0\n\t" \
  "s_mul_i32 s92, s92, 24\n\t" \
  "s_add_i32 s92, s92, vcc_lo\n\t" \
  "s_lshl4_add_u32 s93, s82, 64\n\t" \
  "3:\n\t" \
  "s_waitcnt lgkmcnt(0)\n\t" \
  "s_waitcnt vmcnt(3)\n\t" \
  "v_smfmac_f32_16x16x64_bf16 v[68:71], v[32:35], a[192:199], v104\n\t" \
  "ds_write_b128 v101, v[32:35] offset:8192\n\t" \
  "v_smfmac_f32_16x16x64_bf16 v[72:75], v[32:35], a[200:207], v104\n\t" \
  "v_smfmac_f32_16x16x64_bf16 v[76:79], v[32:35], v[48:55], v104\n\t" \
  "v_smfmac_f32_16x16x64_bf16 v[80:83], v[32:35], v[56:63], v104\n\t" \
  "s_waitcnt vmcnt(2)\n\t" \
  "v_smfmac_f32_16x16x64_bf16 v[68:71], v[36:39], a[208:215], v104\n\t" \
  "ds_write_b128 v101, v[36:39] offset:9216\n\t" \
  "v_smfmac_f32_16x16x64_bf16 v[72:75], v[36:39], a[216:223], v104\n\t" \
  "v_smfmac_f32_16x16x64_bf16 v[76:79], v[36:39], a[224:231], v104\n\t" \
  "v_cmp_u_f32 vcc, v68, v69\n\t" \
  "v_smfmac_f32_16x16x64_bf16 v[80:83], v[36:39], a[232:239], v104\n\t" \
  "v_cmp_u_f32 s[82:83], v70, v71\n\t" \
  "s_waitcnt vmcnt(1)\n\t" \
  "v_smfmac_f32_16x16x64_bf16 v[68:71], v[40:43], v[16:23], v104\n\t" \
  "ds_write_b128 v101, v[40:43] offset:10240\n\t" \
  "v_smfmac_f32_16x16x64_bf16 v[72:75], v[40:43], v[24:31], v104\n\t" \
  "s_or_b64 vcc, vcc, s[82:83]\n\t" \
  "s_or_b64 vcc, vcc, s[80:81]\n\t" \
  "s_waitcnt lgkmcnt(1)\n\t" \
  "v_smfmac_f32_16x16x64_bf16 v[76:79], v[40:43], a[240:247], v104\n\t" \
  "s_cbranch_scc1 8f\n\t" \
  "5:\n\t" \
  "s_barrier\n\t" \
  "4:\n\t" \
  "ds_read_b128 v[48:51], v101 offset:12288\n\t" \
  "v_smfmac_f32_16x16x64_bf16 v[80:83], v[40:43], a[248:255], v104\n\t" \
  "ds_read_b128 v[52:55], v101 offset:13312\n\t" \
  "ds_read_b128 v[56:59], v101 offset:0\n\t" \
  "s_waitcnt vmcnt(0)\n\t" \
  "v_smfmac_f32_16x16x64_bf16 v[68:71], v[44:47], v[120:127], v104\n\t" \
  "v_smfmac_f32_16x16x64_bf16 v[72:75], v[44:47], v[128:135], v104\n\t" \
  "ds_write_b128 v101, v[44:47] offset:11264\n\t" \
  "v_smfmac_f32_16x16x64_bf16 v[76:79], v[44:47], v[136:143], v104\n\t" \
  "v_cmp_u_f32 vcc, v68, v69\n\t" \
  "v_smfmac_f32_16x16x64_bf16 v[80:83], v[44:47], v[144:151], v104\n\t" \
  "v_cmp_u_f32 s[82:83], v70, v71\n\t" \
  "s_waitcnt lgkmcnt(3)\n\t" \
  "v_smfmac_f32_16x16x64_bf16 v[68:71], v[48:51], v[152:159], v104\n\t" \
  "ds_read_b128 v[60:63], v101 offset:1024\n\t" \
  "ds_read_b128 v[64:67], v101 offset:4096\n\t" \
  "v_smfmac_f32_16x16x64_bf16 v[72:75], v[48:51], v[160:167], v104\n\t" \
  "s_or_b64 vcc, vcc, s[82:83]\n\t" \
  "s_waitcnt lgkmcnt(2)\n\t" \
  "v_smfmac_f32_16x16x64_bf16 v[68:71], v[52:55], v[168:175], v104\n\t" \
  "s_cbranch_scc1 9f\n\t" \
  "6:\n\t" \
  "s_barrier\n\t" \
  "v_smfmac_f32_16x16x64_bf16 v[72:75], v[52:55], v[176:183], v104\n\t" \
  "ds_read_b128 v[48:51], v101 offset:5120\n\t" \
  "v_smfmac_f32_16x16x64_bf16 v[68:71], v[56:59], a[0:7], v104\n\t" \
  "ds_read_b128 v[52:55], v101 offset:14336\n\t" \
  "v_smfmac_f32_16x16x64_bf16 v[72:75], v[56:59], a[8:15], v104\n\t" \
  "buffer_load_dwordx4 v[32:35], v100, %[ru], s97 offen offset:0 sc1\n\t" \
  "s_waitcnt lgkmcnt(3)\n\t" \
  "v_smfmac_f32_16x16x64_bf16 v[68:71], v[60:63], a[16:23], v104\n\t" \
  "ds_read_b128 v[56:59], v101 offset:15360\n\t" \
  "v_smfmac_f32_16x16x64_bf16 v[72:75], v[60:63], a[24:31], v104\n\t" \
  "buffer_load_dwordx4 v[36:39], v100, %[ru], s97 offen offset:1024 sc1\n\t" \
  "s_waitcnt lgkmcnt(3)\n\t" \
  "v_smfmac_f32_16x16x64_bf16 v[68:71], v[64:67], a[96:103], v104\n\t" \
  "ds_read_b128 v[60:63], v101 offset:2048\n\t" \
  "buffer_load_dwordx4 v[40:43], v100, %[ru], s97 offen offset:2048 sc1\n\t" \
  "v_smfmac_f32_16x16x64_bf16 v[72:75], v[64:67], a[104:111], v104\n\t" \
  "ds_read_b128 v[16:19], v102 offset:49152\n\t" \
  "s_waitcnt lgkmcnt(4)\n\t" \
  "v_smfmac_f32_16x16x64_bf16 v[68:71], v[48:51], a[112:119], v104\n\t" \
  "ds_read_b128 v[64:67], v101 offset:3072\n\t" \
  "buffer_load_dwordx4 v[44:47], v100, %[ru], s97 offen offset:3072 sc1\n\t" \
  "v_smfmac_f32_16x16x64_bf16 v[72:75], v[48:51], a[120:127], v104\n\t" \
  "ds_read_b128 v[20:23], v102 offset:50176\n\t" \
  "s_waitcnt lgkmcnt(5)\n\t" \
  "v_smfmac_f32_16x16x64_bf16 v[68:71], v[52:55], v[84:91], v104\n\t" \
  "ds_read_b128 v[48:51], v101 offset:7168\n\t" \
  "v_smfmac_f32_16x16x64_bf16 v[72:75], v[52:55], v[92:99], v104\n\t" \
  "ds_read_b128 v[24:27], v102 offset:51200\n\t" \
  "s_waitcnt lgkmcnt(6)\n\t" \
  "v_smfmac_f32_16x16x64_bf16 v[68:71], v[56:59], v[216:223], v104\n\t" \
  "ds_read_b128 v[52:55], v101 offset:6144\n\t" \
  "v_smfmac_f32_16x16x64_bf16 v[72:75], v[56:59], v[224:231], v104\n\t" \
  "ds_read_b128 v[28:31], v102 offset:52224\n\t" \
  "s_waitcnt lgkmcnt(7)\n\t" \
  "v_smfmac_f32_16x16x64_bf16 v[68:71], v[60:63], v[0:7], v104\n\t" \
  "buffer_load_dwordx4 v[84:87], v100, %[ru], s47 offen offset:0 sc1\n\t" \
  "v_smfmac_f32_16x16x64_bf16 v[72:75], v[60:63], v[8:15], v104\n\t" \
  "buffer_load_dwordx4 v[88:91], v100, %[ru], s47 offen offset:1024 sc1\n\t" \
  "s_waitcnt lgkmcnt(5)\n\t" \
  "v_smfmac_f32_16x16x64_bf16 v[68:71], v[64:67], a[64:71], v104\n\t" \
  "ds_read_b128 v[0:3], v103 offset:32768\n\t" \
  "v_smfmac_f32_16x16x64_bf16 v[72:75], v[64:67], a[72:79], v104\n\t" \
  "ds_read_b128 v[4:7], v103 offset:33792\n\t" \
  "s_waitcnt lgkmcnt(5)\n\t" \
  "v_smfmac_f32_16x16x64_bf16 v[68:71], v[48:51], a[160:167], v104\n\t" \
  "ds_read_b128 v[8:11], v103 offset:34816\n\t" \
  "s_waitcnt lgkmcnt(4)\n\t" \
  "v_smfmac_f32_16x16x64_bf16 v[68:71], v[52:55], v[16:23], v104\n\t" \
  "ds_read_b128 v[12:15], v103 offset:35840\n\t" \
  "v_smfmac_f32_16x16x64_bf16 v[72:75], v[48:51], a[168:175], v104\n\t" \
  "buffer_load_dwordx4 v[92:95], v100, %[ru], s47 offen offset:2048 sc1\n\t" \
  "s_waitcnt lgkmcnt(4)\n\t" \
  "v_smfmac_f32_16x16x64_bf16 v[72:75], v[52:55], v[24:31], v104\n\t" \
  "v_add_f32 v116, v68, v70\n\t" \
  "v_add_f32 v117, v69, v71\n\t" \
  "v_smfmac_f32_16x16x64_bf16 v[76:79], v[56:59], v[232:239], v104\n\t" \
  "v_cvt_pk_bf16_f32 v116, v116, v117\n\t" \
  "ds_read_b128 v[24:27], v102 offset:2048\n\t" \
  "v_smfmac_f32_16x16x64_bf16 v[80:83], v[56:59], v[240:247], v104\n\t" \
  "v_add_f32 v117, v72, v74\n\t" \
  "v_add_f32 v118, v73, v75\n\t" \
  "v_cvt_pk_bf16_f32 v117, v117, v118\n\t" \
  "ds_write2st64_b32 v105, v116, v117 offset1:1\n\t" \
  "v_smfmac_f32_16x16x64_bf16 v[76:79], v[60:63], a[48:55], v104\n\t" \
  "buffer_load_dwordx4 v[96:99], v100, %[ru], s47 offen offset:3072 sc1\n\t" \
  "v_smfmac_f32_16x16x64_bf16 v[80:83], v[60:63], a[56:63], v104\n\t" \
  "v_smfmac_f32_16x16x64_bf16 v[76:79], v[64:67], a[80:87], v104\n\t" \
  "buffer_load_dword v117, v107, %[ru], 0 offen sc1\n\t" \
  "v_smfmac_f32_16x16x64_bf16 v[80:83], v[64:67], a[88:95], v104\n\t" \
  "s_waitcnt lgkmcnt(0)\n\t" \
  "s_barrier\n\t" \
  "ds_read_b128 v[56:59], v101 offset:12288\n\t" \
  "v_smfmac_f32_16x16x64_bf16 v[76:79], v[48:51], a[176:183], v104\n\t" \
  "v_smfmac_f32_16x16x64_bf16 v[80:83], v[48:51], a[184:191], v104\n\t" \
  "ds_read_b128 v[60:63], v101 offset:5120\n\t" \
  "v_smfmac_f32_16x16x64_bf16 v[76:79], v[52:55], a[144:151], v104\n\t" \
  "ds_read_b128 v[16:19], v102 offset:0\n\t" \
  "v_smfmac_f32_16x16x64_bf16 v[80:83], v[52:55], a[152:159], v104\n\t" \
  "ds_read_b128 v[64:67], v101 offset:14336\n\t" \
  "s_waitcnt lgkmcnt(3)\n\t" \
  "v_smfmac_f32_16x16x64_bf16 v[76:79], v[56:59], v[0:7], v104\n\t" \
  "ds_read_b128 v[20:23], v102 offset:1024\n\t" \
  "v_smfmac_f32_16x16x64_bf16 v[80:83], v[56:59], v[8:15], v104\n\t" \
  "ds_read_b128 v[48:51], v101 offset:0\n\t" \
  "s_waitcnt lgkmcnt(4)\n\t" \
  "v_smfmac_f32_16x16x64_bf16 v[76:79], v[60:63], a[128:135], v104\n\t" \
  "ds_read_b128 v[28:31], v102 offset:3072\n\t" \
  "v_smfmac_f32_16x16x64_bf16 v[80:83], v[60:63], a[136:143], v104\n\t" \
  "ds_read_b128 v[52:55], v101 offset:13312\n\t" \
  "s_waitcnt lgkmcnt(4)\n\t" \
  "v_smfmac_f32_16x16x64_bf16 v[76:79], v[64:67], v[200:207], v104\n\t" \
  "ds_read_b128 v[0:3], v102 offset:32768\n\t" \
  "v_smfmac_f32_16x16x64_bf16 v[80:83], v[64:67], v[208:215], v104\n\t" \
  "ds_read_b128 v[56:59], v101 offset:1024\n\t" \
  "ds_read_b128 v[4:7], v102 offset:33792\n\t" \
  "s_waitcnt lgkmcnt(5)\n\t" \
  "v_smfmac_f32_16x16x64_bf16 v[76:79], v[48:51], v[16:23], v104\n\t" \
  "ds_read_b128 v[60:63], v101 offset:4096\n\t" \
  "s_waitcnt lgkmcnt(5)\n\t" \
  "v_smfmac_f32_16x16x64_bf16 v[80:83], v[48:51], v[24:31], v104\n\t" \
  "ds_read_b128 v[8:11], v102 offset:34816\n\t" \
  "s_waitcnt lgkmcnt(5)\n\t" \
  "v_smfmac_f32_16x16x64_bf16 v[76:79], v[52:55], v[184:191], v104\n\t" \
  "ds_read_b128 v[12:15], v102 offset:35840\n\t" \
  "v_smfmac_f32_16x16x64_bf16 v[80:83], v[52:55], v[192:199], v104\n\t" \
  "s_waitcnt lgkmcnt(4)\n\t" \
  "v_smfmac_f32_16x16x64_bf16 v[76:79], v[56:59], a[32:39], v104\n\t" \
  "s_waitcnt lgkmcnt(2)\n\t" \
  "v_smfmac_f32_16x16x64_bf16 v[76:79], v[60:63], v[0:7], v104\n\t" \
  "v_smfmac_f32_16x16x64_bf16 v[80:83], v[56:59], a[40:47], v104\n\t" \
  "s_waitcnt lgkmcnt(0)\n\t" \
  "v_smfmac_f32_16x16x64_bf16 v[80:83], v[60:63], v[8:15], v104\n\t" \
  "v_add_f32 v68, v76, v78\n\t" \
  "v_add_f32 v69, v77, v79\n\t" \
  "v_cvt_pk_bf16_f32 v68, v68, v69\n\t" \
  "ds_write_b32 v106, v68\n\t" \
  "ds_read_b128 v[0:3], v101 offset:20480\n\t" \
  "ds_read_b128 v[4:7], v101 offset:21504\n\t" \
  "ds_read_b128 v[8:11], v101 offset:22528\n\t" \
  "ds_read_b128 v[12:15], v101 offset:23552\n\t" \
  "v_add_f32 v70, v80, v82\n\t" \
  "v_add_f32 v71, v81, v83\n\t" \
  "v_cvt_pk_bf16_f32 v70, v70, v71\n\t" \
  "ds_write_b32 v106, v70 offset:256\n\t" \
  "ds_read_b128 v[16:19], v101 offset:24576\n\t" \
  "ds_read_b128 v[20:23], v101 offset:25600\n\t" \
  "ds_read_b128 v[24:27], v101 offset:26624\n\t" \
  "ds_read_b128 v[28:31], v101 offset:27648\n\t" \
  "s_waitcnt lgkmcnt(0)\n\t" \
  "s_barrier\n\t" \
  "s_waitcnt vmcnt(1)\n\t" \
  "v_mfma_f32_16x16x32_bf16 v[64:67], v[0:3], v[32:35], 0\n\t" \
  "v_mfma_f32_16x16x32_bf16 v[68:71], v[16:19], v[32:35], v[112:115]\n\t" \
  "v_mfma_f32_16x16x32_bf16 v[72:75], v[0:3], v[84:87], v[112:115]\n\t" \
  "v_mfma_f32_16x16x32_bf16 v[64:67], v[4:7], v[36:39], v[64:67]\n\t" \
  "v_mfma_f32_16x16x32_bf16 v[68:71], v[20:23], v[36:39], v[68:71]\n\t" \
  "v_mfma_f32_16x16x32_bf16 v[72:75], v[4:7], v[88:91], v[72:75]\n\t" \
  "ds_read_b64_tr_b16 v[48:49], v108 offset:0\n\t" \
  "ds_read_b64_tr_b16 v[50:51], v108 offset:64\n\t" \
  "ds_read_b64_tr_b16 v[52:53], v108 offset:512\n\t" \
  "ds_read_b64_tr_b16 v[54:55], v108 offset:576\n\t" \
  "ds_read_b64_tr_b16 v[56:57], v108 offset:1024\n\t" \
  "ds_read_b64_tr_b16 v[58:59], v108 offset:1088\n\t" \
  "ds_read_b64_tr_b16 v[60:61], v108 offset:1536\n\t" \
  "ds_read_b64_tr_b16 v[62:63], v108 offset:1600\n\t" \
  "v_mfma_f32_16x16x32_bf16 v[64:67], v[8:11], v[40:43], v[64:67]\n\t" \
  "v_mfma_f32_16x16x32_bf16 v[68:71], v[24:27], v[40:43], v[68:71]\n\t" \
  "v_mfma_f32_16x16x32_bf16 v[72:75], v[8:11], v[92:95], v[72:75]\n\t" \
  "v_mfma_f32_16x16x32_bf16 v[64:67], v[12:15], v[44:47], v[64:67]\n\t" \
  "v_mfma_f32_16x16x32_bf16 v[68:71], v[28:31], v[44:47], v[68:71]\n\t" \
  "v_mfma_f32_16x16x32_bf16 v[72:75], v[12:15], v[96:99], v[72:75]\n\t" \
  "s_waitcnt vmcnt(0)\n\t" \
  "v_readfirstlane_b32 s76, v117\n\t" \
  "s_add_i32 s77, s44, 1\n\t" \
  "s_min_i32 s77, s77, 63\n\t" \
  "s_cmp_lt_i32 s76, s77\n\t" \
  "s_cselect_b32 s42, 32, 0\n\t" \
  "s_waitcnt lgkmcnt(0)\n\t" \
  "v_mfma_f32_16x16x32_bf16 v[76:79], v[48:51], v[32:35], 0\n\t" \
  "v_mfma_f32_16x16x32_bf16 v[80:83], v[48:51], v[84:87], 0\n\t" \
  "v_mfma_f32_16x16x32_bf16 v[76:79], v[52:55], v[36:39], v[76:79]\n\t" \
  "v_max3_f32 v1, v64, v65, v66\n\t" \
  "v_mfma_f32_16x16x32_bf16 v[80:83], v[52:55], v[88:91], v[80:83]\n\t" \
  "v_max3_f32 v1, v1, v67, v68\n\t" \
  "v_mfma_f32_16x16x32_bf16 v[76:79], v[56:59], v[40:43], v[76:79]\n\t" \
  "v_max3_f32 v0, v72, v73, v74\n\t" \
  "v_mfma_f32_16x16x32_bf16 v[80:83], v[56:59], v[92:95], v[80:83]\n\t" \
  "v_max3_f32 v1, v1, v69, v70\n\t" \
  "v_mfma_f32_16x16x32_bf16 v[76:79], v[60:63], v[44:47], v[76:79]\n\t" \
  "v_max_f32 v0, v0, v75\n\t" \
  "v_mfma_f32_16x16x32_bf16 v[80:83], v[60:63], v[96:99], v[80:83]\n\t" \
  "v_max_f32 v1, v1, v71\n\t" \
  "s_nop 1\n\t" \
  "v_permlane32_swap_b32 v0, v1\n\t" \
  "s_and_b32 s76, s44, 3\n\t" \
  "v_max_f32 v0, v0, v1\n\t" \
  "s_lshl_b32 s76, s76, 17\n\t" \
  "s_nop 1\n\t" \
  "v_mov_b32_dpp v80, v76 quad_perm:[0,1,2,3] row_mask:0xc bank_mask:0xf\n\t" \
  "v_mov_b32_dpp v81, v77 quad_perm:[0,1,2,3] row_mask:0xc bank_mask:0xf\n\t" \
  "v_mov_b32_dpp v82, v78 quad_perm:[0,1,2,3] row_mask:0xc bank_mask:0xf\n\t" \
  "v_mov_b32_dpp v83, v79 quad_perm:[0,1,2,3] row_mask:0xc bank_mask:0xf\n\t" \
  "v_max3_f32 v0, v0, v80, v81\n\t" \
  "v_max3_f32 v0, v0, v82, v83\n\t" \
  "v_mov_b32 v1, v0\n\t" \
  "s_nop 1\n\t" \
  "v_permlane16_swap_b32 v0, v1\n\t" \
  "s_nop 0\n\t" \
  "v_max_f32 v0, v0, v1\n\t" \
  "v_mul_f32 v0, %[cs], v0\n\t" \
  "v_mov_b32 v1, v0\n\t" \
  "v_mov_b32 v2, v0\n\t" \
  "s_nop 1\n\t" \
  "v_permlane32_swap_b32 v1, v2\n\t" \
  "s_nop 0\n\t" \
  "v_fma_f32 v80, v80, %[cs], -v0\n\t" \
  "v_fma_f32 v81, v81, %[cs], -v0\n\t" \
  "v_fma_f32 v82, v82, %[cs], -v0\n\t" \
  "v_fma_f32 v83, v83, %[cs], -v0\n\t" \
  "v_fma_f32 v72, v72, %[cs], -v1\n\t" \
  "v_fma_f32 v73, v73, %[cs], -v1\n\t" \
  "v_fma_f32 v74, v74, %[cs], -v1\n\t" \
  "v_fma_f32 v75, v75, %[cs], -v1\n\t" \
  "v_fma_f32 v64, v64, %[cs], -v2\n\t" \
  "v_fma_f32 v65, v65, %[cs], -v2\n\t" \
  "v_fma_f32 v66, v66, %[cs], -v2\n\t" \
  "v_fma_f32 v67, v67, %[cs], -v2\n\t" \
  "v_fma_f32 v68, v68, %[cs], -v2\n\t" \
  "v_fma_f32 v69, v69, %[cs], -v2\n\t" \
  "v_fma_f32 v70, v70, %[cs], -v2\n\t" \
  "v_fma_f32 v71, v71, %[cs], -v2\n\t" \
  "v_exp_f32 v80, v80\n\t" \
  "v_exp_f32 v81, v81\n\t" \
  "v_exp_f32 v82, v82\n\t" \
  "v_exp_f32 v83, v83\n\t" \
  "v_exp_f32 v72, v72\n\t" \
  "v_exp_f32 v73, v73\n\t" \
  "v_exp_f32 v74, v74\n\t" \
  "v_exp_f32 v75, v75\n\t" \
  "v_exp_f32 v64, v64\n\t" \
  "v_exp_f32 v65, v65\n\t" \
  "v_exp_f32 v66, v66\n\t" \
  "v_exp_f32 v67, v67\n\t" \
  "v_exp_f32 v68, v68\n\t" \
  "v_exp_f32 v69, v69\n\t" \
  "v_exp_f32 v70, v70\n\t" \
  "v_exp_f32 v71, v71\n\t" \
  "v_add_f32 v3, v80, v81\n\t" \
  "v_add_f32 v0, v72, v73\n\t" \
  "v_add_f32 v1, v64, v65\n\t" \
  "v_add_f32 v2, v68, v69\n\t" \
  "v_add_f32 v3, v3, v82\n\t" \
  "v_add_f32 v0, v0, v74\n\t" \
  "v_add_f32 v1, v1, v66\n\t" \
  "v_add_f32 v2, v2, v70\n\t" \
  "v_add_f32 v0, v0, v75\n\t" \
  "v_add_f32 v1, v1, v67\n\t" \
  "v_add_f32 v2, v2, v71\n\t" \
  "v_add_f32 v1, v1, v2\n\t" \
  "v_add_f32 v3, v3, v83\n\t" \
  "s_nop 0\n\t" \
  "v_permlane32_swap_b32 v0, v1\n\t" \
  "s_nop 0\n\t" \
  "v_add_f32 v0, v0, v1\n\t" \
  "v_add_f32 v0, v0, v3\n\t" \
  "v_mov_b32 v1, v0\n\t" \
  "s_nop 1\n\t" \
  "v_permlane16_swap_b32 v0, v1\n\t" \
  "s_nop 0\n\t" \
  "v_add_f32 v0, v0, v1\n\t" \
  "v_rcp_f32 v0, v0\n\t" \
  "s_mov_b32 exec_hi, 0\n\t" \
  "buffer_load_dword v118, v110, %[rh], s45 offen sc1\n\t" \
  "s_mov_b64 exec, -1\n\t" \
  "v_mov_b32 v1, v0\n\t" \
  "v_mov_b32 v2, v0\n\t" \
  "s_nop 1\n\t" \
  "v_permlane32_swap_b32 v1, v2\n\t" \
  "s_nop 0\n\t" \
  "v_mul_f32 v80, v80, v0\n\t" \
  "v_mul_f32 v81, v81, v0\n\t" \
  "v_mul_f32 v82, v82, v0\n\t" \
  "v_mul_f32 v83, v83, v0\n\t" \
  "v_mul_f32 v72, v72, v1\n\t" \
  "v_mul_f32 v73, v73, v1\n\t" \
  "v_mul_f32 v74, v74, v1\n\t" \
  "v_mul_f32 v75, v75, v1\n\t" \
  "v_mul_f32 v64, v64, v2\n\t" \
  "v_mul_f32 v65, v65, v2\n\t" \
  "v_mul_f32 v66, v66, v2\n\t" \
  "v_mul_f32 v67, v67, v2\n\t" \
  "v_mul_f32 v68, v68, v2\n\t" \
  "v_mul_f32 v69, v69, v2\n\t" \
  "v_mul_f32 v70, v70, v2\n\t" \
  "v_mul_f32 v71, v71, v2\n\t" \
  "v_cvt_pk_bf16_f32 v0, v72, v73\n\t" \
  "v_cvt_pk_bf16_f32 v1, v64, v65\n\t" \
  "v_cvt_pk_bf16_f32 v2, v74, v75\n\t" \
  "v_cvt_pk_bf16_f32 v3, v66, v67\n\t" \
  "v_cvt_pk_bf16_f32 v4, v68, v69\n\t" \
  "v_cvt_pk_bf16_f32 v5, v80, v81\n\t" \
  "v_cvt_pk_bf16_f32 v6, v70, v71\n\t" \
  "v_cvt_pk_bf16_f32 v7, v82, v83\n\t" \
  "buffer_store_dwordx4 v[0:3], v119, %[rh], s76 offen sc1\n\t" \
  "buffer_store_dwordx4 v[4:7], v119, %[rh], s76 offen offset:1024 sc1\n\t" \
  "s_add_i32 s41, s57, 400\n\t" \
  "s_sub_i32 s76, s92, s51\n\t" \
  "s_sub_i32 s77, s93, s53\n\t" \
  "s_cmp_eq_u32 s76, 0\n\t" \
  "s_cbranch_scc1 21f\n\t" \
  "v_cvt_f32_u32 v117, s76\n\t" \
  "v_cvt_f32_u32 v116, s77\n\t" \
  "v_rcp_f32 v117, v117\n\t" \
  "s_mov_b32 s51, s92\n\t" \
  "v_mul_f32 v117, v117, v116\n\t" \
  "v_max_f32 v117, 0x3f028f5c, v117\n\t" \
  "v_min_f32 v117, 0x3ffeb852, v117\n\t" \
  "v_rcp_f32 v116, v117\n\t" \
  "v_mul_f32 v117, 0x4f000000, v117\n\t" \
  "v_mul_f32 v116, 0x47800000, v116\n\t" \
  "v_cvt_u32_f32 v117, v117\n\t" \
  "v_cvt_u32_f32 v116, v116\n\t" \
  "s_mov_b32 s53, s93\n\t" \
  "v_readfirstlane_b32 s46, v117\n\t" \
  "v_readfirstlane_b32 s52, v116\n\t" \
  "21:\n\t" \
  "s_add_i32 s44, s44, 1\n\t" \
  "s_and_b32 s58, s58, 4\n\t" \
  "s_and_b32 s76, s44, 64\n\t" \
  "s_or_b32 s42, s42, s76\n\t" \
  "s_cbranch_scc0 20b\n\t" \
  "s_branch 99f\n\t" \
  "14:\n\t" \
  "s_sleep 0\n\t" \
  "s_add_u32 s59, s59, 0x10000\n\t" \
  "s_cmp_lt_u32 s59, 0x100000\n\t" \
  "s_cbranch_scc1 13b\n\t" \
  "s_mov_b64 exec, -1\n\t" \
  "s_mov_b32 s42, 16\n\t" \
  "s_branch 99f\n\t" \
  "8:\n\t" \
  "s_bitcmp1_b32 s58, 1\n\t" \
  "s_cbranch_scc1 4b\n\t" \
  "s_bitcmp1_b32 s58, 2\n\t" \
  "s_cbranch_scc1 5b\n\t" \
  "v_max3_u32 v116, v32, v33, v34\n\t" \
  "v_max3_u32 v117, v35, v36, v37\n\t" \
  "v_max3_u32 v116, v116, v38, v39\n\t" \
  "v_max_u32 v116, v116, v117\n\t" \
  "v_cmp_eq_u32 vcc, -1, v116\n\t" \
  "s_cbranch_vccz 5b\n\t" \
  "s_branch 0f\n\t" \
  "9:\n\t" \
  "s_bitcmp1_b32 s58, 2\n\t" \
  "s_cbranch_scc1 6b\n\t" \
  "v_max3_u32 v116, v40, v41, v42\n\t" \
  "v_max3_u32 v117, v43, v44, v45\n\t" \
  "v_max3_u32 v116, v116, v46, v47\n\t" \
  "v_max_u32 v116, v116, v117\n\t" \
  "v_cmp_eq_u32 vcc, -1, v116\n\t" \
  "s_cbranch_vccz 6b\n\t" \
  "s_or_b32 s58, s58, 2\n\t" \
  "0:\n\t" \
  "s_or_b32 s58, s58, 1\n\t" \
  "s_sleep 0\n\t" \
  "11:\n\t" \
  "buffer_load_dwordx4 v[32:35], v100, %[rh], s40 offen sc1\n\t" \
  "buffer_load_dwordx4 v[36:39], v100, %[rh], s76 offen sc1\n\t" \
  "buffer_load_dwordx4 v[40:43], v100, %[rh], s77 offen sc1\n\t" \
  "buffer_load_dwordx4 v[44:47], v100, %[rh], s78 offen sc1\n\t" \
  "s_waitcnt lgkmcnt(0)\n\t" \
  "s_branch 10b\n\t" \
  "17:\n\t" \
  "v_mov_b64 v[68:69], 0\n\t" \
  "v_mov_b64 v[70:71], 0\n\t" \
  "v_mov_b64 v[72:73], 0\n\t" \
  "v_mov_b64 v[74:75], 0\n\t" \
  "v_mov_b64 v[76:77], 0\n\t" \
  "v_mov_b64 v[78:79], 0\n\t" \
  "v_mov_b64 v[80:81], 0\n\t" \
  "v_mov_b64 v[82:83], 0\n\t" \
  "s_add_u32 s59, s59, 1\n\t" \
  "s_bitcmp1_b32 s58, 2\n\t" \
  "s_cbranch_scc1 3b\n\t" \
  "s_waitcnt vmcnt(0)\n\t" \
  "v_max_u32 v116, v32, v33\n\t" \
  "v_max3_u32 v116, v116, v34, v35\n\t" \
  "v_max3_u32 v116, v116, v36, v37\n\t" \
  "v_max3_u32 v116, v116, v38, v39\n\t" \
  "v_max3_u32 v116, v116, v40, v41\n\t" \
  "v_max3_u32 v116, v116, v42, v43\n\t" \
  "v_max3_u32 v116, v116, v44, v45\n\t" \
  "v_max3_u32 v116, v116, v46, v47\n\t" \
  "v_cmp_eq_u32 vcc, -1, v116\n\t" \
  "s_cbranch_vccz 3b\n\t" \
  "s_bitcmp1_b32 s59, 6\n\t" \
  "s_cbranch_scc1 12f\n\t" \
  "buffer_load_dwordx4 v[32:35], v100, %[rh], s40 offen sc1\n\t" \
  "buffer_load_dwordx4 v[36:39], v100, %[rh], s76 offen sc1\n\t" \
  "buffer_load_dwordx4 v[40:43], v100, %[rh], s77 offen sc1\n\t" \
  "buffer_load_dwordx4 v[44:47], v100, %[rh], s78 offen sc1\n\t" \
  "s_branch 17b\n\t" \
  "12:\n\t" \
  "s_mov_b32 s42, s58\n\t" \
  "s_branch 99f\n\t" \
  "93:\n\t" \
  "s_mov_b32 s58, s42\n\t" \
  ".p2align 6\n\t" \
  "20:\n\t" \
  "s_and_b32 s76, s44, 7\n\t" \
  "s_lshl_b32 s76, s76, 15\n\t" \
  "s_add_i32 s40, s54, s76\n\t" \
  "s_and_b32 s76, s44, 3\n\t" \
  "s_lshl_b32 s45, s76, 16\n\t" \
  "s_add_i32 s47, s44, 1\n\t" \
  "s_min_u32 s47, s47, 63\n\t" \
  "s_lshr_b32 s76, s47, 2\n\t" \
  "s_bitcmp1_b32 %[hm], s76\n\t" \
  "s_cselect_b32 s77, 0x2000, 0\n\t" \
  "s_cmp_eq_u32 s76, 0\n\t" \
  "s_cselect_b32 s77, %[d0], s77\n\t" \
  "s_lshl_b32 s47, s47, 19\n\t" \
  "s_add_i32 s47, s47, s55\n\t" \
  "s_add_i32 s47, s47, s77\n\t" \
  "s_add_i32 s97, s47, 0x1000\n\t" \
  "s_mov_b32 s96, 1\n\t" \
  "s_add_i32 s49, s44, -1\n\t" \
  "s_and_b32 s48, s49, 3\n\t" \
  "s_lshl_b32 s48, s48, 16\n\t" \
  "s_lshl_b32 s50, s53, 8\n\t" \
  "s_mov_b32 s59, 0\n\t" \
  "s_mov_b32 s92, s51\n\t" \
  "s_mov_b32 s93, s53\n\t" \
  "s_xor_b32 s76, s40, 0x1000\n\t" \
  "s_xor_b32 s77, s40, 0x4000\n\t" \
  "s_xor_b32 s78, s40, 0x5000\n\t" \
  "s_cmp_lg_u32 s58, 0\n\t" \
  "s_cbranch_scc1 11f\n\t" \
  "10:\n\t" \
  "ds_read_b128 v[48:51], v103 offset:32768\n\t" \
  "ds_read_b128 v[52:55], v103 offset:33792\n\t" \
  "ds_read_b128 v[56:59], v103 offset:34816\n\t" \
  "ds_read_b128 v[60:63], v103 offset:35840\n\t" \
  "ds_read_b128 v[16:19], v103 offset:49152\n\t" \
  "ds_read_b128 v[20:23], v103 offset:50176\n\t" \
  "ds_read_b128 v[24:27], v103 offset:51200\n\t" \
  "ds_read_b128 v[28:31], v103 offset:52224\n\t" \
  "ds_read_b128 v[84:87], v102 offset:16384\n\t" \
  "ds_read_b128 v[88:91], v102 offset:17408\n\t" \
  "ds_read_b128 v[92:95], v102 offset:18432\n\t" \
  "ds_read_b128 v[96:99], v102 offset:19456\n\t" \
  "s_waitcnt lgkmcnt(4)\n\t" \
  "ds_read_b128 v[0:3], v102 offset:49152\n\t" \
  "ds_read_b128 v[4:7], v102 offset:50176\n\t" \
  "ds_read_b128 v[8:11], v102 offset:51200\n\t" \
  "ds_read_b128 v[12:15], v102 offset:52224\n\t" \
  "s_bitcmp1_b32 s58, 1\n\t" \
  "s_cselect_b64 s[80:81], -1, 0\n\t" \
  "s_cmp_lg_u32 s58, 0\n\t" \
  "s_cbranch_scc1 17f\n\t" \
  "s_mov_b32 exec_hi, 0\n\t" \
  "s_waitcnt vmcnt(1)\n\t" \
  "v_mov_b32 v116, v118\n\t" \
  "s_branch 15f\n\t" \
  "13:\n\t" \
  "buffer_load_dword v116, v110, %[rh], s48 offen sc1\n\t" \
  "s_waitcnt vmcnt(0)\n\t" \
  "15:\n\t" \
  "v_mov_b32 v117, 20000\n\t" \
  "v_cmp_ne_u32_sdwa vcc, v116, s49 src0_sel:BYTE_0 src1_sel:DWORD\n\t" \
  "v_subrev_u32 v116, s50, v116\n\t" \
  "v_ashrrev_i32 v116, 8, v116\n\t" \
  "s_nop 1\n\t" \
  "v_max_i32_dpp v116, v116, v116 quad_perm:[1,0,3,2] row_mask:0xf bank_mask:0xf\n\t" \
  "s_cbranch_vccnz 14f\n\t" \
  "s_nop 0\n\t" \
  "v_max_i32_dpp v116, v116, v116 quad_perm:[2,3,0,1] row_mask:0xf bank_mask:0xf\n\t" \
  "s_nop 1\n\t" \
  "v_max_i32_dpp v116, v116, v116 row_half_mirror row_mask:0xf bank_mask:0xf\n\t" \
  "s_nop 1\n\t" \
  "v_max_i32_dpp v116, v116, v116 row_mirror row_mask:0xf bank_mask:0xf\n\t" \
  "v_med3_i32 v116, v116, 0, v117\n\t" \
  "v_mul_u32_u24 v116, s52, v116\n\t" \
  "v_lshrrev_b32 v116, 16, v116\n\t" \
  "s_mov_b64 exec, -1\n\t" \
  "v_readlane_b32 s84, v116, 0\n\t" \
  "v_readlane_b32 s85, v116, 16\n\t" \
  "s_max_u32 s84, s84, s85\n\t" \
  "s_add_i32 s84, s84, s51\n\t" \
  "v_mov_b64 v[68:69], 0\n\t" \
  "v_mov_b64 v[70:71], 0\n\t" \
  "v_mov_b64 v[72:73], 0\n\t" \
  "v_mov_b64 v[74:75], 0\n\t" \
  "v_mov_b64 v[76:77], 0\n\t" \
  "v_mov_b64 v[78:79], 0\n\t" \
  "v_mov_b64 v[80:81], 0\n\t" \
  "v_mov_b64 v[82:83], 0\n\t" \
  "1:\n\t" \
  "s_memtime vcc\n\t" \
  "s_waitcnt lgkmcnt(0)\n\t" \
  "s_sub_i32 s79, s84, vcc_lo\n\t" \
  "s_cmp_gt_i32 s79, 128\n\t" \
  "s_cbranch_scc1 1b\n\t" \
  "2:\n\t" \
  "s_sub_i32 s79, s79, 33\n\t" \
  "s_cmp_gt_i32 s79, 56\n\t" \
  "s_cbranch_scc1 2b\n\t" \
  "buffer_load_dwordx4 v[32:35], v100, %[rh], s40 offen sc1\n\t" \
  "s_nop 12\n\t" \
  "buffer_load_dwordx4 v[36:39], v100, %[rh], s76 offen sc1\n\t" \
  "s_nop 12\n\t" \
  "buffer_load_dwordx4 v[40:43], v100, %[rh], s77 offen sc1\n\t" \
  "s_nop 12\n\t" \
  "buffer_load_dwordx4 v[44:47], v100, %[rh], s78 offen sc1\n\t" \
  "s_memtime vcc\n\t" \
  "s_memrealtime s[82:83]\n\t" \
  "s_nop 4\n\t" \
  "s_memrealtime s[84:85]\n\t" \
  "s_nop 4\n\t" \
  "s_memrealtime s[94:95]\n\t" \
  "s_nop 4\n\t" \
  "s_memrealtime s[86:87]\n\t" \
  "s_nop 4\n\t" \
  "s_memrealtime s[88:89]\n\t" \
  "s_nop 4\n\t" \
  "s_memrealtime s[90:91]\n\t" \
  "s_mov_b32 s92, 0\n\t" \
  "s_waitcnt lgkmcnt(0)\n\t" \
  "s_cmp_eq_u32 s82, s84\n\t" \
  "s_addc_u32 s92, s92, 0\n\t" \
  "s_cmp_eq_u32 s82, s94\n\t" \
  "s_addc_u32 s92, s92, 0\n\t" \
  "s_cmp_eq_u32 s82, s86\n\t" \
  "s_addc_u32 s92, s92, 0\n\t" \
  "s_cmp_eq_u32 s82, s88\n\t" \
  "s_addc_u32 s92, s92, 0\n\t" \
  "s_cmp_eq_u32 s82, s90\n\t" \
  "s_addc_u32 s92, s92, 0\n\t" \
  "s_mul_i32 s92, s92, 24\n\t" \
  "s_add_i32 s92, s92, vcc_lo\n\t" \
  "s_lshl4_add_u32 s93, s82, 64\n\t" \
  "3:\n\t" \
  "s_sub_i32 s84, s92, s43\n\t" \
  "s_lshl_b32 s85, s93, 8\n\t" \
  "s_or_b32 s85, s85, s44\n\t" \
  "v_mov_b32 v118, s85\n\t" \
  "s_waitcnt lgkmcnt(0)\n\t" \
  "s_waitcnt vmcnt(3)\n\t" \
  "v_smfmac_f32_16x16x64_bf16 v[68:71], v[32:35], v[152:159], v104\n\t" \
  "ds_write_b128 v101, v[32:35] offset:12288\n\t" \
  "v_smfmac_f32_16x16x64_bf16 v[72:75], v[32:35], v[160:167], v104\n\t" \
  "v_smfmac_f32_16x16x64_bf16 v[76:79], v[32:35], v[48:55], v104\n\t" \
  "v_smfmac_f32_16x16x64_bf16 v[80:83], v[32:35], v[56:63], v104\n\t" \
  "s_waitcnt vmcnt(2)\n\t" \
  "v_smfmac_f32_16x16x64_bf16 v[68:71], v[36:39], v[168:175], v104\n\t" \
  "ds_write_b128 v101, v[36:39] offset:13312\n\t" \
  "v_smfmac_f32_16x16x64_bf16 v[72:75], v[36:39], v[176:183], v104\n\t" \
  "v_smfmac_f32_16x16x64_bf16 v[76:79], v[36:39], v[184:191], v104\n\t" \
  "v_cmp_u_f32 vcc, v68, v69\n\t" \
  "v_smfmac_f32_16x16x64_bf16 v[80:83], v[36:39], v[192:199], v104\n\t" \
  "v_cmp_u_f32 s[82:83], v70, v71\n\t" \
  "s_waitcnt vmcnt(1)\n\t" \
  "v_smfmac_f32_16x16x64_bf16 v[68:71], v[40:43], v[16:23], v104\n\t" \
  "ds_write_b128 v101, v[40:43] offset:14336\n\t" \
  "v_smfmac_f32_16x16x64_bf16 v[72:75], v[40:43], v[24:31], v104\n\t" \
  "s_or_b64 vcc, vcc, s[82:83]\n\t" \
  "s_or_b64 vcc, vcc, s[80:81]\n\t" \
  "s_waitcnt lgkmcnt(1)\n\t" \
  "v_smfmac_f32_16x16x64_bf16 v[76:79], v[40:43], v[200:207], v104\n\t" \
  "s_cbranch_scc1 8f\n\t" \
  "5:\n\t" \
  "s_barrier\n\t" \
  "4:\n\t" \
  "ds_read_b128 v[48:51], v101 offset:0\n\t" \
  "v_smfmac_f32_16x16x64_bf16 v[80:83], v[40:43], v[208:215], v104\n\t" \
  "ds_read_b128 v[52:55], v101 offset:1024\n\t" \
  "ds_read_b128 v[56:59], v101 offset:4096\n\t" \
  "s_waitcnt vmcnt(0)\n\t" \
  "v_smfmac_f32_16x16x64_bf16 v[68:71], v[44:47], v[216:223], v104\n\t" \
  "v_smfmac_f32_16x16x64_bf16 v[72:75], v[44:47], v[224:231], v104\n\t" \
  "ds_write_b128 v101, v[44:47] offset:15360\n\t" \
  "v_smfmac_f32_16x16x64_bf16 v[76:79], v[44:47], v[232:239], v104\n\t" \
  "v_cmp_u_f32 vcc, v68, v69\n\t" \
  "v_smfmac_f32_16x16x64_bf16 v[80:83], v[44:47], v[240:247], v104\n\t" \
  "v_cmp_u_f32 s[82:83], v70, v71\n\t" \
  "s_waitcnt lgkmcnt(3)\n\t" \
  "v_smfmac_f32_16x16x64_bf16 v[68:71], v[48:51], a[0:7], v104\n\t" \
  "ds_read_b128 v[60:63], v101 offset:5120\n\t" \
  "ds_read_b128 v[64:67], v101 offset:8192\n\t" \
  "v_smfmac_f32_16x16x64_bf16 v[72:75], v[48:51], a[8:15], v104\n\t" \
  "s_or_b64 vcc, vcc, s[82:83]\n\t" \
  "s_waitcnt lgkmcnt(2)\n\t" \
  "v_smfmac_f32_16x16x64_bf16 v[68:71], v[52:55], a[16:23], v104\n\t" \
  "s_cbranch_scc1 9f\n\t" \
  "6:\n\t" \
  "s_barrier\n\t" \
  "s_memtime s[94:95]\n\t" \
  "v_smfmac_f32_16x16x64_bf16 v[72:75], v[52:55], a[24:31], v104\n\t" \
  "ds_read_b128 v[48:51], v101 offset:9216\n\t" \
  "v_smfmac_f32_16x16x64_bf16 v[68:71], v[56:59], a[96:103], v104\n\t" \
  "s_waitcnt lgkmcnt(0)\n\t" \
  "ds_read_b128 v[52:55], v101 offset:2048\n\t" \
  "v_smfmac_f32_16x16x64_bf16 v[72:75], v[56:59], a[104:111], v104\n\t" \
  "s_sub_i32 s79, s94, s84\n\t" \
  "s_mul_hi_u32 s79, s79, s46\n\t" \
  "v_smfmac_f32_16x16x64_bf16 v[68:71], v[60:63], a[112:119], v104\n\t" \
  "ds_read_b128 v[56:59], v101 offset:3072\n\t" \
  "v_lshl_add_u32 v116, s79, 9, v118\n\t" \
  "buffer_store_dword v116, v112, %[rh], s45 offen sc1\n\t" \
  "v_smfmac_f32_16x16x64_bf16 v[72:75], v[60:63], a[120:127], v104\n\t" \
  "v_smfmac_f32_16x16x64_bf16 v[68:71], v[64:67], a[192:199], v104\n\t" \
  "ds_read_b128 v[60:63], v101 offset:6144\n\t" \
  "v_smfmac_f32_16x16x64_bf16 v[72:75], v[64:67], a[200:207], v104\n\t" \
  "ds_read_b128 v[16:19], v103 offset:16384\n\t" \
  "v_smfmac_f32_16x16x64_bf16 v[68:71], v[48:51], a[208:215], v104\n\t" \
  "ds_read_b128 v[64:67], v101 offset:7168\n\t" \
  "v_smfmac_f32_16x16x64_bf16 v[72:75], v[48:51], a[216:223], v104\n\t" \
  "ds_read_b128 v[20:23], v103 offset:17408\n\t" \
  "s_waitcnt lgkmcnt(5)\n\t" \
  "v_smfmac_f32_16x16x64_bf16 v[68:71], v[52:55], v[84:91], v104\n\t" \
  "ds_read_b128 v[48:51], v101 offset:11264\n\t" \
  "v_smfmac_f32_16x16x64_bf16 v[72:75], v[52:55], v[92:99], v104\n\t" \
  "ds_read_b128 v[24:27], v103 offset:18432\n\t" \
  "s_waitcnt lgkmcnt(6)\n\t" \
  "v_smfmac_f32_16x16x64_bf16 v[68:71], v[56:59], a[64:71], v104\n\t" \
  "ds_read_b128 v[52:55], v101 offset:10240\n\t" \
  "v_smfmac_f32_16x16x64_bf16 v[72:75], v[56:59], a[72:79], v104\n\t" \
  "ds_read_b128 v[28:31], v103 offset:19456\n\t" \
  "s_waitcnt lgkmcnt(7)\n\t" \
  "v_smfmac_f32_16x16x64_bf16 v[68:71], v[60:63], v[0:7], v104\n\t" \
  "v_smfmac_f32_16x16x64_bf16 v[72:75], v[60:63], v[8:15], v104\n\t" \
  "s_waitcnt lgkmcnt(5)\n\t" \
  "v_smfmac_f32_16x16x64_bf16 v[68:71], v[64:67], a[160:167], v104\n\t" \
  "ds_read_b128 v[0:3], v102 offset:0\n\t" \
  "v_smfmac_f32_16x16x64_bf16 v[72:75], v[64:67], a[168:175], v104\n\t" \
  "ds_read_b128 v[4:7], v102 offset:1024\n\t" \
  "s_waitcnt lgkmcnt(5)\n\t" \
  "v_smfmac_f32_16x16x64_bf16 v[68:71], v[48:51], v[120:127], v104\n\t" \
  "ds_read_b128 v[8:11], v102 offset:2048\n\t" \
  "s_waitcnt lgkmcnt(4)\n\t" \
  "v_smfmac_f32_16x16x64_bf16 v[68:71], v[52:55], v[16:23], v104\n\t" \
  "ds_read_b128 v[12:15], v102 offset:3072\n\t" \
  "v_smfmac_f32_16x16x64_bf16 v[72:75], v[48:51], v[128:135], v104\n\t" \
  "s_waitcnt lgkmcnt(4)\n\t" \
  "v_smfmac_f32_16x16x64_bf16 v[72:75], v[52:55], v[24:31], v104\n\t" \
  "v_add_f32 v116, v68, v70\n\t" \
  "v_add_f32 v117, v69, v71\n\t" \
  "v_smfmac_f32_16x16x64_bf16 v[76:79], v[56:59], a[80:87], v104\n\t" \
  "v_cvt_pk_bf16_f32 v116, v116, v117\n\t" \
  "ds_read_b128 v[24:27], v102 offset:34816\n\t" \
  "v_smfmac_f32_16x16x64_bf16 v[80:83], v[56:59], a[88:95], v104\n\t" \
  "v_add_f32 v117, v72, v74\n\t" \
  "v_add_f32 v118, v73, v75\n\t" \
  "v_cvt_pk_bf16_f32 v117, v117, v118\n\t" \
  "ds_write2st64_b32 v105, v116, v117 offset1:1\n\t" \
  "v_smfmac_f32_16x16x64_bf16 v[76:79], v[60:63], a[144:151], v104\n\t" \
  "v_smfmac_f32_16x16x64_bf16 v[80:83], v[60:63], a[152:159], v104\n\t" \
  "v_smfmac_f32_16x16x64_bf16 v[76:79], v[64:67], a[176:183], v104\n\t" \
  "buffer_load_dword v117, v107, %[ru], 0 offen sc1\n\t" \
  "v_smfmac_f32_16x16x64_bf16 v[80:83], v[64:67], a[184:191], v104\n\t" \
  "s_waitcnt lgkmcnt(0)\n\t" \
  "s_barrier\n\t" \
  "ds_read_b128 v[56:59], v101 offset:0\n\t" \
  "v_smfmac_f32_16x16x64_bf16 v[76:79], v[48:51], v[136:143], v104\n\t" \
  "v_smfmac_f32_16x16x64_bf16 v[80:83], v[48:51], v[144:151], v104\n\t" \
  "ds_read_b128 v[60:63], v101 offset:9216\n\t" \
  "buffer_load_dwordx4 v[84:87], v100, %[ru], s47 offen offset:0 sc1\n\t" \
  "v_smfmac_f32_16x16x64_bf16 v[76:79], v[52:55], a[240:247], v104\n\t" \
  "ds_read_b128 v[16:19], v102 offset:32768\n\t" \
  "v_smfmac_f32_16x16x64_bf16 v[80:83], v[52:55], a[248:255], v104\n\t" \
  "ds_read_b128 v[64:67], v101 offset:2048\n\t" \
  "buffer_load_dwordx4 v[88:91], v100, %[ru], s47 offen offset:1024 sc1\n\t" \
  "s_waitcnt lgkmcnt(3)\n\t" \
  "v_smfmac_f32_16x16x64_bf16 v[76:79], v[56:59], v[0:7], v104\n\t" \
  "ds_read_b128 v[20:23], v102 offset:33792\n\t" \
  "v_smfmac_f32_16x16x64_bf16 v[80:83], v[56:59], v[8:15], v104\n\t" \
  "ds_read_b128 v[48:51], v101 offset:4096\n\t" \
  "buffer_load_dwordx4 v[92:95], v100, %[ru], s47 offen offset:2048 sc1\n\t" \
  "s_waitcnt lgkmcnt(4)\n\t" \
  "v_smfmac_f32_16x16x64_bf16 v[76:79], v[60:63], a[224:231], v104\n\t" \
  "ds_read_b128 v[28:31], v102 offset:35840\n\t" \
  "v_smfmac_f32_16x16x64_bf16 v[80:83], v[60:63], a[232:239], v104\n\t" \
  "ds_read_b128 v[52:55], v101 offset:1024\n\t" \
  "buffer_load_dwordx4 v[96:99], v100, %[ru], s47 offen offset:3072 sc1\n\t" \
  "s_waitcnt lgkmcnt(4)\n\t" \
  "v_smfmac_f32_16x16x64_bf16 v[76:79], v[64:67], a[48:55], v104\n\t" \
  "ds_read_b128 v[0:3], v103 offset:0\n\t" \
  "v_smfmac_f32_16x16x64_bf16 v[80:83], v[64:67], a[56:63], v104\n\t" \
  "ds_read_b128 v[56:59], v101 offset:5120\n\t" \
  "ds_read_b128 v[4:7], v103 offset:1024\n\t" \
  "buffer_load_dwordx4 v[32:35], v100, %[ru], s97 offen offset:0 sc1\n\t" \
  "s_waitcnt lgkmcnt(5)\n\t" \
  "v_smfmac_f32_16x16x64_bf16 v[76:79], v[48:51], v[16:23], v104\n\t" \
  "ds_read_b128 v[60:63], v101 offset:8192\n\t" \
  "s_waitcnt lgkmcnt(5)\n\t" \
  "v_smfmac_f32_16x16x64_bf16 v[80:83], v[48:51], v[24:31], v104\n\t" \
  "ds_read_b128 v[8:11], v103 offset:2048\n\t" \
  "buffer_load_dwordx4 v[36:39], v100, %[ru], s97 offen offset:1024 sc1\n\t" \
  "s_waitcnt lgkmcnt(5)\n\t" \
  "v_smfmac_f32_16x16x64_bf16 v[76:79], v[52:55], a[32:39], v104\n\t" \
  "ds_read_b128 v[12:15], v103 offset:3072\n\t" \
  "v_smfmac_f32_16x16x64_bf16 v[80:83], v[52:55], a[40:47], v104\n\t" \
  "buffer_load_dwordx4 v[40:43], v100, %[ru], s97 offen offset:2048 sc1\n\t" \
  "s_waitcnt lgkmcnt(4)\n\t" \
  "v_smfmac_f32_16x16x64_bf16 v[76:79], v[56:59], a[128:135], v104\n\t" \
  "s_waitcnt lgkmcnt(2)\n\t" \
  "v_smfmac_f32_16x16x64_bf16 v[76:79], v[60:63], v[0:7], v104\n\t" \
  "buffer_load_dwordx4 v[44:47], v100, %[ru], s97 offen offset:3072 sc1\n\t" \
  "v_smfmac_f32_16x16x64_bf16 v[80:83], v[56:59], a[136:143], v104\n\t" \
  "s_waitcnt lgkmcnt(0)\n\t" \
  "v_smfmac_f32_16x16x64_bf16 v[80:83], v[60:63], v[8:15], v104\n\t" \
  "v_add_f32 v68, v76, v78\n\t" \
  "v_add_f32 v69, v77, v79\n\t" \
  "v_cvt_pk_bf16_f32 v68, v68, v69\n\t" \
  "ds_write_b32 v106, v68\n\t" \
  "s_nop 3\n\t" \
  "v_add_f32 v70, v80, v82\n\t" \
  "v_add_f32 v71, v81, v83\n\t" \
  "v_cvt_pk_bf16_f32 v70, v70, v71\n\t" \
  "ds_write_b32 v106, v70 offset:256\n\t" \
  "s_waitcnt lgkmcnt(0)\n\t" \
  "s_barrier\n\t" \
  "s_memtime s[76:77]\n\t" \
  "s_waitcnt lgkmcnt(0)\n\t" \
  "s_add_i32 s57, s76, 240\n\t" \
  "s_sleep 5\n\t" \
  "s_waitcnt vmcnt(8)\n\t" \
  "v_readfirstlane_b32 s76, v117\n\t" \
  "s_add_i32 s77, s44, 2\n\t" \
  "s_min_i32 s77, s77, 63\n\t" \
  "s_cmp_lt_i32 s76, s77\n\t" \
  "s_cselect_b32 s42, 32, 0\n\t" \
  "s_add_i32 s41, s57, 400\n\t" \
  "s_sub_i32 s76, s57, s94\n\t" \
  "s_add_i32 s77, s43, 8\n\t" \
  "s_min_u32 s76, s76, s77\n\t" \
  "s_sub_i32 s43, s43, 4\n\t" \
  "s_max_u32 s43, s43, s76\n\t" \
  "s_sub_i32 s76, s92, s51\n\t" \
  "s_sub_i32 s77, s93, s53\n\t" \
  "s_cmp_eq_u32 s76, 0\n\t" \
  "s_cbranch_scc1 21f\n\t" \
  "v_cvt_f32_u32 v117, s76\n\t" \
  "v_cvt_f32_u32 v116, s77\n\t" \
  "v_rcp_f32 v117, v117\n\t" \
  "s_mov_b32 s51, s92\n\t" \
  "v_mul_f32 v117, v117, v116\n\t" \
  "v_max_f32 v117, 0x3f028f5c, v117\n\t" \
  "v_min_f32 v117, 0x3ffeb852, v117\n\t" \
  "v_rcp_f32 v116, v117\n\t" \
  "v_mul_f32 v117, 0x4f000000, v117\n\t" \
  "v_mul_f32 v116, 0x47800000, v116\n\t" \
  "v_cvt_u32_f32 v117, v117\n\t" \
  "v_cvt_u32_f32 v116, v116\n\t" \
  "s_mov_b32 s53, s93\n\t" \
  "v_readfirstlane_b32 s46, v117\n\t" \
  "v_readfirstlane_b32 s52, v116\n\t" \
  "21:\n\t" \
  "s_waitcnt vmcnt(0)\n\t" \
  "ds_write_b128 v101, v[84:87] offset:20480\n\t" \
  "ds_write_b128 v101, v[88:91] offset:21504\n\t" \
  "ds_write_b128 v101, v[92:95] offset:22528\n\t" \
  "ds_write_b128 v101, v[96:99] offset:23552\n\t" \
  "ds_write_b128 v101, v[32:35] offset:24576\n\t" \
  "ds_write_b128 v101, v[36:39] offset:25600\n\t" \
  "ds_write_b128 v101, v[40:43] offset:26624\n\t" \
  "ds_write_b128 v101, v[44:47] offset:27648\n\t" \
  "v_mov_b32 v0, -1\n\t" \
  "s_add_i32 s76, s45, 0x10000\n\t" \
  "s_and_b32 s76, s76, 0x30000\n\t" \
  "16:\n\t" \
  "s_memtime vcc\n\t" \
  "s_waitcnt lgkmcnt(0)\n\t" \
  "s_sub_i32 s79, s41, vcc_lo\n\t" \
  "s_cmp_gt_i32 s79, 64\n\t" \
  "s_cbranch_scc1 16b\n\t" \
  "s_mov_b32 exec_hi, 0\n\t" \
  "buffer_load_dword v118, v110, %[rh], s45 offen sc1\n\t" \
  "s_mov_b64 exec, -1\n\t" \
  "buffer_store_dword v0, v113, %[rh], s76 offen sc1\n\t" \
  "s_add_i32 s44, s44, 1\n\t" \
  "s_and_b32 s58, s58, 4\n\t" \
  "s_and_b32 s76, s44, 64\n\t" \
  "s_or_b32 s42, s42, s76\n\t" \
  "s_cbranch_scc0 20b\n\t" \
  "s_branch 99f\n\t" \
  "14:\n\t" \
  "s_sleep 0\n\t" \
  "s_add_u32 s59, s59, 0x10000\n\t" \
  "s_cmp_lt_u32 s59, 0x100000\n\t" \
  "s_cbranch_scc1 13b\n\t" \
  "s_mov_b64 exec, -1\n\t" \
  "s_mov_b32 s42, 16\n\t" \
  "s_branch 99f\n\t" \
  "8:\n\t" \
  "s_bitcmp1_b32 s58, 1\n\t" \
  "s_cbranch_scc1 4b\n\t" \
  "s_bitcmp1_b32 s58, 2\n\t" \
  "s_cbranch_scc1 5b\n\t" \
  "v_max3_u32 v116, v32, v33, v34\n\t" \
  "v_max3_u32 v117, v35, v36, v37\n\t" \
  "v_max3_u32 v116, v116, v38, v39\n\t" \
  "v_max_u32 v116, v116, v117\n\t" \
  "v_cmp_eq_u32 vcc, -1, v116\n\t" \
  "s_cbranch_vccz 5b\n\t" \
  "s_branch 0f\n\t" \
  "9:\n\t" \
  "s_bitcmp1_b32 s58, 2\n\t" \
  "s_cbranch_scc1 6b\n\t" \
  "v_max3_u32 v116, v40, v41, v42\n\t" \
  "v_max3_u32 v117, v43, v44, v45\n\t" \
  "v_max3_u32 v116, v116, v46, v47\n\t" \
  "v_max_u32 v116, v116, v117\n\t" \
  "v_cmp_eq_u32 vcc, -1, v116\n\t" \
  "s_cbranch_vccz 6b\n\t" \
  "s_or_b32 s58, s58, 2\n\t" \
  "0:\n\t" \
  "s_or_b32 s58, s58, 1\n\t" \
  "s_sleep 0\n\t" \
  "11:\n\t" \
  "buffer_load_dwordx4 v[32:35], v100, %[rh], s40 offen sc1\n\t" \
  "buffer_load_dwordx4 v[36:39], v100, %[rh], s76 offen sc1\n\t" \
  "buffer_load_dwordx4 v[40:43], v100, %[rh], s77 offen sc1\n\t" \
  "buffer_load_dwordx4 v[44:47], v100, %[rh], s78 offen sc1\n\t" \
  "s_waitcnt lgkmcnt(0)\n\t" \
  "s_branch 10b\n\t" \
  "17:\n\t" \
  "v_mov_b64 v[68:69], 0\n\t" \
  "v_mov_b64 v[70:71], 0\n\t" \
  "v_mov_b64 v[72:73], 0\n\t" \
  "v_mov_b64 v[74:75], 0\n\t" \
  "v_mov_b64 v[76:77], 0\n\t" \
  "v_mov_b64 v[78:79], 0\n\t" \
  "v_mov_b64 v[80:81], 0\n\t" \
  "v_mov_b64 v[82:83], 0\n\t" \
  "s_add_u32 s59, s59, 1\n\t" \
  "s_bitcmp1_b32 s58, 2\n\t" \
  "s_cbranch_scc1 3b\n\t" \
  "s_waitcnt vmcnt(0)\n\t" \
  "v_max_u32 v116, v32, v33\n\t" \
  "v_max3_u32 v116, v116, v34, v35\n\t" \
  "v_max3_u32 v116, v116, v36, v37\n\t" \
  "v_max3_u32 v116, v116, v38, v39\n\t" \
  "v_max3_u32 v116, v116, v40, v41\n\t" \
  "v_max3_u32 v116, v116, v42, v43\n\t" \
  "v_max3_u32 v116, v116, v44, v45\n\t" \
  "v_max3_u32 v116, v116, v46, v47\n\t" \
  "v_cmp_eq_u32 vcc, -1, v116\n\t" \
  "s_cbranch_vccz 3b\n\t" \
  "s_bitcmp1_b32 s59, 6\n\t" \
  "s_cbranch_scc1 12f\n\t" \
  "buffer_load_dwordx4 v[32:35], v100, %[rh], s40 offen sc1\n\t" \
  "buffer_load_dwordx4 v[36:39], v100, %[rh], s76 offen sc1\n\t" \
  "buffer_load_dwordx4 v[40:43], v100, %[rh], s77 offen sc1\n\t" \
  "buffer_load_dwordx4 v[44:47], v100, %[rh], s78 offen sc1\n\t" \
  "s_branch 17b\n\t" \
  "12:\n\t" \
  "s_mov_b32 s42, s58\n\t" \
  "s_branch 99f\n\t" \
  "99:\n\t" \
  "s_waitcnt vmcnt(0) lgkmcnt(0)\n\t" \
  ""
#define VO_BODY \
  "s_cmp_eq_u32 %[wv], 1\n\t" \
  "s_cbranch_scc1 91f\n\t" \
  "s_cmp_eq_u32 %[wv], 2\n\t" \
  "s_cbranch_scc1 92f\n\t" \
  "s_cmp_eq_u32 %[wv], 3\n\t" \
  "s_cbranch_scc1 93f\n\t" \
  "90:\n\t" \
  "s_mov_b32 s58, s42\n\t" \
  ".p2align 6\n\t" \
  "20:\n\t" \
  "s_and_b32 s76, s44, 7\n\t" \
  "s_lshl_b32 s76, s76, 15\n\t" \
  "s_add_i32 s40, s54, s76\n\t" \
  "s_and_b32 s76, s44, 3\n\t" \
  "s_lshl_b32 s45, s76, 16\n\t" \
  "s_and_b32 s47, s44, 3\n\t" \
  "s_lshl_b32 s76, s47, 2\n\t" \
  "s_lshl_b32 s47, s47, 20\n\t" \
  "s_add_i32 s47, s47, s55\n\t" \
  "s_lshr_b32 s76, %[um], s76\n\t" \
  "s_and_b32 s76, s76, 7\n\t" \
  "s_lshl_b32 s76, s76, 12\n\t" \
  "s_xor_b32 s47, s47, s76\n\t" \
  "s_add_i32 s49, s44, -1\n\t" \
  "s_and_b32 s48, s49, 3\n\t" \
  "s_lshl_b32 s48, s48, 16\n\t" \
  "s_lshl_b32 s50, s53, 8\n\t" \
  "s_mov_b32 s59, 0\n\t" \
  "s_mov_b32 s92, s51\n\t" \
  "s_mov_b32 s93, s53\n\t" \
  "s_xor_b32 s76, s40, 0x1000\n\t" \
  "s_xor_b32 s77, s40, 0x4000\n\t" \
  "s_xor_b32 s78, s40, 0x5000\n\t" \
  "s_cmp_lg_u32 s58, 0\n\t" \
  "s_cbranch_scc1 11f\n\t" \
  "10:\n\t" \
  "ds_read_b128 v[48:51], v102 offset:0\n\t" \
  "ds_read_b128 v[52:55], v102 offset:1024\n\t" \
  "ds_read_b128 v[56:59], v102 offset:2048\n\t" \
  "ds_read_b128 v[60:63], v102 offset:3072\n\t" \
  "ds_read_b128 v[0:3], v102 offset:16384\n\t" \
  "ds_read_b128 v[4:7], v102 offset:17408\n\t" \
  "ds_read_b128 v[8:11], v102 offset:18432\n\t" \
  "ds_read_b128 v[12:15], v102 offset:19456\n\t" \
  "ds_read_b128 v[16:19], v102 offset:32768\n\t" \
  "ds_read_b128 v[20:23], v102 offset:33792\n\t" \
  "ds_read_b128 v[24:27], v102 offset:34816\n\t" \
  "ds_read_b128 v[28:31], v102 offset:35840\n\t" \
  "s_waitcnt lgkmcnt(4)\n\t" \
  "ds_read_b128 v[80:83], v102 offset:49152\n\t" \
  "ds_read_b128 v[84:87], v102 offset:50176\n\t" \
  "ds_read_b128 v[88:91], v102 offset:51200\n\t" \
  "ds_read_b128 v[92:95], v102 offset:52224\n\t" \
  "s_bitcmp1_b32 s58, 1\n\t" \
  "s_cselect_b64 s[80:81], -1, 0\n\t" \
  "s_cmp_lg_u32 s58, 0\n\t" \
  "s_cbranch_scc1 17f\n\t" \
  "s_mov_b32 exec_hi, 0\n\t" \
  "16:\n\t" \
  "s_memtime vcc\n\t" \
  "s_waitcnt lgkmcnt(0)\n\t" \
  "s_sub_i32 s79, s41, vcc_lo\n\t" \
  "s_cmp_gt_i32 s79, 64\n\t" \
  "s_cbranch_scc1 16b\n\t" \
  "13:\n\t" \
  "buffer_load_dword v116, v110, %[rh], s48 offen sc1\n\t" \
  "s_waitcnt vmcnt(0)\n\t" \
  "15:\n\t" \
  "v_mov_b32 v117, 20000\n\t" \
  "v_cmp_ne_u32_sdwa vcc, v116, s49 src0_sel:BYTE_0 src1_sel:DWORD\n\t" \
  "v_subrev_u32 v116, s50, v116\n\t" \
  "v_ashrrev_i32 v116, 8, v116\n\t" \
  "s_nop 1\n\t" \
  "v_max_i32_dpp v116, v116, v116 quad_perm:[1,0,3,2] row_mask:0xf bank_mask:0xf\n\t" \
  "s_cbranch_vccnz 14f\n\t" \
  "s_nop 0\n\t" \
  "v_max_i32_dpp v116, v116, v116 quad_perm:[2,3,0,1] row_mask:0xf bank_mask:0xf\n\t" \
  "s_nop 1\n\t" \
  "v_max_i32_dpp v116, v116, v116 row_half_mirror row_mask:0xf bank_mask:0xf\n\t" \
  "s_nop 1\n\t" \
  "v_max_i32_dpp v116, v116, v116 row_mirror row_mask:0xf bank_mask:0xf\n\t" \
  "v_med3_i32 v116, v116, 0, v117\n\t" \
  "v_mul_u32_u24 v116, s52, v116\n\t" \
  "v_lshrrev_b32 v116, 16, v116\n\t" \
  "s_mov_b64 exec, -1\n\t" \
  "v_readlane_b32 s84, v116, 0\n\t" \
  "v_readlane_b32 s85, v116, 16\n\t" \
  "s_max_u32 s84, s84, s85\n\t" \
  "s_add_i32 s84, s84, s51\n\t" \
  "v_mov_b64 v[72:73], 0\n\t" \
  "v_mov_b64 v[74:75], 0\n\t" \
  "v_mov_b64 v[76:77], 0\n\t" \
  "v_mov_b64 v[78:79], 0\n\t" \
  "1:\n\t" \
  "s_memtime vcc\n\t" \
  "s_waitcnt lgkmcnt(0)\n\t" \
  "s_sub_i32 s79, s84, vcc_lo\n\t" \
  "s_cmp_gt_i32 s79, 128\n\t" \
  "s_cbranch_scc1 1b\n\t" \
  "2:\n\t" \
  "s_sub_i32 s79, s79, 33\n\t" \
  "s_cmp_gt_i32 s79, 56\n\t" \
  "s_cbranch_scc1 2b\n\t" \
  "buffer_load_dwordx4 v[32:35], v100, %[rh], s40 offen sc1\n\t" \
  "s_nop 12\n\t" \
  "buffer_load_dwordx4 v[36:39], v100, %[rh], s76 offen sc1\n\t" \
  "s_nop 12\n\t" \
  "buffer_load_dwordx4 v[40:43], v100, %[rh], s77 offen sc1\n\t" \
  "s_nop 12\n\t" \
  "buffer_load_dwordx4 v[44:47], v100, %[rh], s78 offen sc1\n\t" \
  "s_memtime vcc\n\t" \
  "s_memrealtime s[82:83]\n\t" \
  "s_nop 4\n\t" \
  "s_memrealtime s[84:85]\n\t" \
  "s_nop 4\n\t" \
  "s_memrealtime s[94:95]\n\t" \
  "s_nop 4\n\t" \
  "s_memrealtime s[86:87]\n\t" \
  "s_nop 4\n\t" \
  "s_memrealtime s[88:89]\n\t" \
  "s_nop 4\n\t" \
  "s_memrealtime s[90:91]\n\t" \
  "s_mov_b32 s92, 0\n\t" \
  "s_waitcnt lgkmcnt(0)\n\t" \
  "s_cmp_eq_u32 s82, s84\n\t" \
  "s_addc_u32 s92, s92, 0\n\t" \
  "s_cmp_eq_u32 s82, s94\n\t" \
  "s_addc_u32 s92, s92, 0\n\t" \
  "s_cmp_eq_u32 s82, s86\n\t" \
  "s_addc_u32 s92, s92, 0\n\t" \
  "s_cmp_eq_u32 s82, s88\n\t" \
  "s_addc_u32 s92, s92, 0\n\t" \
  "s_cmp_eq_u32 s82, s90\n\t" \
  "s_addc_u32 s92, s92, 0\n\t" \
  "s_mul_i32 s92, s92, 24\n\t" \
  "s_add_i32 s92, s92, vcc_lo\n\t" \
  "s_lshl4_add_u32 s93, s82, 64\n\t" \
  "3:\n\t" \
  "s_waitcnt lgkmcnt(0)\n\t" \
  "s_waitcnt vmcnt(3)\n\t" \
  "v_smfmac_f32_16x16x64_bf16 v[72:75], v[32:35], v[48:55], v104\n\t" \
  "ds_write_b128 v101, v[32:35] offset:0\n\t" \
  "v_smfmac_f32_16x16x64_bf16 v[76:79], v[32:35], v[56:63], v104\n\t" \
  "s_waitcnt vmcnt(2)\n\t" \
  "ds_write_b128 v101, v[36:39] offset:1024\n\t" \
  "v_smfmac_f32_16x16x64_bf16 v[72:75], v[36:39], a[0:7], v104\n\t" \
  "v_smfmac_f32_16x16x64_bf16 v[76:79], v[36:39], a[8:15], v104\n\t" \
  "s_nop 3\n\t" \
  "v_cmp_u_f32 vcc, v72, v73\n\t" \
  "v_cmp_u_f32 s[82:83], v74, v75\n\t" \
  "s_waitcnt vmcnt(1)\n\t" \
  "v_smfmac_f32_16x16x64_bf16 v[72:75], v[40:43], a[16:23], v104\n\t" \
  "s_or_b64 vcc, vcc, s[82:83]\n\t" \
  "s_or_b64 vcc, vcc, s[80:81]\n\t" \
  "v_smfmac_f32_16x16x64_bf16 v[76:79], v[40:43], a[24:31], v104\n\t" \
  "s_waitcnt lgkmcnt(0)\n\t" \
  "s_cbranch_scc1 8f\n\t" \
  "5:\n\t" \
  "s_barrier\n\t" \
  "4:\n\t" \
  "ds_read_b128 v[48:51], v101 offset:4096\n\t" \
  "ds_read_b128 v[52:55], v101 offset:5120\n\t" \
  "ds_read_b128 v[56:59], v101 offset:9216\n\t" \
  "s_waitcnt vmcnt(0)\n\t" \
  "v_smfmac_f32_16x16x64_bf16 v[72:75], v[44:47], a[32:39], v104\n\t" \
  "ds_read_b128 v[60:63], v101 offset:8192\n\t" \
  "ds_read_b128 v[64:67], v101 offset:13312\n\t" \
  "v_smfmac_f32_16x16x64_bf16 v[76:79], v[44:47], a[40:47], v104\n\t" \
  "ds_read_b128 v[68:71], v101 offset:12288\n\t" \
  "ds_write_b128 v101, v[40:43] offset:2048\n\t" \
  "s_nop 1\n\t" \
  "v_cmp_u_f32 vcc, v72, v73\n\t" \
  "v_cmp_u_f32 s[82:83], v74, v75\n\t" \
  "s_waitcnt lgkmcnt(6)\n\t" \
  "v_smfmac_f32_16x16x64_bf16 v[72:75], v[48:51], v[0:7], v104\n\t" \
  "s_or_b64 vcc, vcc, s[82:83]\n\t" \
  "v_smfmac_f32_16x16x64_bf16 v[76:79], v[48:51], v[8:15], v104\n\t" \
  "s_cbranch_scc1 9f\n\t" \
  "6:\n\t" \
  "s_waitcnt lgkmcnt(5)\n\t" \
  "v_smfmac_f32_16x16x64_bf16 v[72:75], v[52:55], a[48:55], v104\n\t" \
  "ds_write_b128 v101, v[44:47] offset:3072\n\t" \
  "v_smfmac_f32_16x16x64_bf16 v[76:79], v[52:55], a[56:63], v104\n\t" \
  "v_mov_b64 v[40:41], 0\n\t" \
  "s_waitcnt lgkmcnt(5)\n\t" \
  "v_smfmac_f32_16x16x64_bf16 v[72:75], v[56:59], a[96:103], v104\n\t" \
  "v_mov_b64 v[42:43], 0\n\t" \
  "v_smfmac_f32_16x16x64_bf16 v[76:79], v[56:59], a[104:111], v104\n\t" \
  "s_waitcnt lgkmcnt(4)\n\t" \
  "v_smfmac_f32_16x16x64_bf16 v[72:75], v[60:63], v[16:23], v104\n\t" \
  "s_waitcnt lgkmcnt(0)\n\t" \
  "s_barrier\n\t" \
  "v_smfmac_f32_16x16x64_bf16 v[76:79], v[60:63], v[24:31], v104\n\t" \
  "ds_read_b128 v[48:51], v101 offset:6144\n\t" \
  "ds_read_b128 v[52:55], v101 offset:10240\n\t" \
  "v_smfmac_f32_16x16x64_bf16 v[72:75], v[64:67], a[144:151], v104\n\t" \
  "ds_read_b128 v[56:59], v101 offset:14336\n\t" \
  "ds_read_b128 v[60:63], v101 offset:7168\n\t" \
  "v_smfmac_f32_16x16x64_bf16 v[76:79], v[64:67], a[152:159], v104\n\t" \
  "ds_read_b128 v[32:35], v101 offset:11264\n\t" \
  "ds_read_b128 v[36:39], v101 offset:15360\n\t" \
  "v_smfmac_f32_16x16x64_bf16 v[72:75], v[68:71], v[80:87], v104\n\t" \
  "ds_read_b128 v[0:3], v103 offset:0\n\t" \
  "v_mov_b64 v[44:45], 0\n\t" \
  "v_smfmac_f32_16x16x64_bf16 v[76:79], v[68:71], v[88:95], v104\n\t" \
  "v_mov_b64 v[46:47], 0\n\t" \
  "s_waitcnt lgkmcnt(5)\n\t" \
  "v_smfmac_f32_16x16x64_bf16 v[72:75], v[48:51], a[64:71], v104\n\t" \
  "ds_read_b128 v[4:7], v103 offset:1024\n\t" \
  "v_mov_b64 v[80:81], 0\n\t" \
  "v_smfmac_f32_16x16x64_bf16 v[72:75], v[52:55], a[112:119], v104\n\t" \
  "v_mov_b64 v[82:83], 0\n\t" \
  "s_waitcnt lgkmcnt(2)\n\t" \
  "v_smfmac_f32_16x16x64_bf16 v[72:75], v[56:59], a[160:167], v104\n\t" \
  "ds_read_b128 v[8:11], v103 offset:2048\n\t" \
  "v_mov_b64 v[84:85], 0\n\t" \
  "v_smfmac_f32_16x16x64_bf16 v[72:75], v[60:63], a[80:87], v104\n\t" \
  "ds_read_b128 v[12:15], v103 offset:3072\n\t" \
  "v_mov_b64 v[86:87], 0\n\t" \
  "v_smfmac_f32_16x16x64_bf16 v[72:75], v[32:35], a[128:135], v104\n\t" \
  "ds_read_b128 v[16:19], v103 offset:16384\n\t" \
  "v_mov_b64 v[88:89], 0\n\t" \
  "v_smfmac_f32_16x16x64_bf16 v[72:75], v[36:39], a[176:183], v104\n\t" \
  "ds_read_b128 v[20:23], v103 offset:17408\n\t" \
  "v_mov_b64 v[90:91], 0\n\t" \
  "v_smfmac_f32_16x16x64_bf16 v[76:79], v[48:51], a[72:79], v104\n\t" \
  "ds_read_b128 v[24:27], v103 offset:18432\n\t" \
  "v_mov_b64 v[92:93], 0\n\t" \
  "v_smfmac_f32_16x16x64_bf16 v[76:79], v[52:55], a[120:127], v104\n\t" \
  "v_add_f32 v96, v72, v74\n\t" \
  "v_add_f32 v97, v73, v75\n\t" \
  "v_cvt_pk_bf16_f32 v96, v96, v97\n\t" \
  "ds_write_b32 v105, v96\n\t" \
  "v_smfmac_f32_16x16x64_bf16 v[76:79], v[56:59], a[168:175], v104\n\t" \
  "ds_read_b128 v[28:31], v103 offset:19456\n\t" \
  "v_mov_b64 v[94:95], 0\n\t" \
  "v_smfmac_f32_16x16x64_bf16 v[76:79], v[60:63], a[88:95], v104\n\t" \
  "v_smfmac_f32_16x16x64_bf16 v[76:79], v[32:35], a[136:143], v104\n\t" \
  "v_smfmac_f32_16x16x64_bf16 v[76:79], v[36:39], a[184:191], v104\n\t" \
  "s_waitcnt lgkmcnt(1)\n\t" \
  "s_barrier\n\t" \
  "ds_read_b64_tr_b16 v[96:97], v107 offset:0\n\t" \
  "ds_read_b64_tr_b16 v[98:99], v107 offset:64\n\t" \
  "ds_read_b64_tr_b16 v[56:57], v107 offset:1024\n\t" \
  "ds_read_b64_tr_b16 v[58:59], v107 offset:1088\n\t" \
  "s_nop 1\n\t" \
  "v_add_f32 v116, v76, v78\n\t" \
  "v_add_f32 v117, v77, v79\n\t" \
  "v_cvt_pk_bf16_f32 v116, v116, v117\n\t" \
  "ds_write_b32 v105, v116 offset:256\n\t" \
  "s_waitcnt lgkmcnt(3)\n\t" \
  "v_smfmac_f32_16x16x64_bf16 v[40:43], v[96:99], a[192:199], v106\n\t" \
  "v_smfmac_f32_16x16x64_bf16 v[44:47], v[96:99], a[200:207], v106\n\t" \
  "s_waitcnt lgkmcnt(0)\n\t" \
  "s_barrier\n\t" \
  "ds_read_b64_tr_b16 v[52:53], v107 offset:256\n\t" \
  "ds_read_b64_tr_b16 v[54:55], v107 offset:320\n\t" \
  "v_smfmac_f32_16x16x64_bf16 v[40:43], v[56:59], a[224:231], v106\n\t" \
  "ds_read_b64_tr_b16 v[60:61], v107 offset:1280\n\t" \
  "ds_read_b64_tr_b16 v[62:63], v107 offset:1344\n\t" \
  "v_smfmac_f32_16x16x64_bf16 v[44:47], v[56:59], a[232:239], v106\n\t" \
  "v_smfmac_f32_16x16x64_bf16 v[80:83], v[96:99], a[240:247], v106\n\t" \
  "v_mov_b64 v[32:33], 0\n\t" \
  "v_smfmac_f32_16x16x64_bf16 v[84:87], v[96:99], a[248:255], v106\n\t" \
  "v_mov_b64 v[34:35], 0\n\t" \
  "v_smfmac_f32_16x16x64_bf16 v[80:83], v[56:59], v[136:143], v106\n\t" \
  "v_mov_b64 v[36:37], 0\n\t" \
  "v_smfmac_f32_16x16x64_bf16 v[84:87], v[56:59], v[144:151], v106\n\t" \
  "s_waitcnt lgkmcnt(2)\n\t" \
  "v_smfmac_f32_16x16x64_bf16 v[40:43], v[52:55], a[208:215], v106\n\t" \
  "v_smfmac_f32_16x16x64_bf16 v[44:47], v[52:55], a[216:223], v106\n\t" \
  "s_xor_b32 s76, s47, 0x40000\n\t" \
  "s_waitcnt lgkmcnt(0)\n\t" \
  "v_smfmac_f32_16x16x64_bf16 v[40:43], v[60:63], v[0:7], v106\n\t" \
  "v_mov_b64 v[38:39], 0\n\t" \
  "s_xor_b32 s77, s47, 0x84000\n\t" \
  "v_smfmac_f32_16x16x64_bf16 v[44:47], v[60:63], v[8:15], v106\n\t" \
  "s_xor_b32 s78, s47, 0xc4000\n\t" \
  "v_smfmac_f32_16x16x64_bf16 v[80:83], v[52:55], v[120:127], v106\n\t" \
  "ds_read_b128 v[0:3], v103 offset:32768\n\t" \
  "v_cvt_pk_bf16_f32 v64, v40, v41\n\t" \
  "v_smfmac_f32_16x16x64_bf16 v[84:87], v[52:55], v[128:135], v106\n\t" \
  "ds_read_b128 v[4:7], v103 offset:33792\n\t" \
  "v_cvt_pk_bf16_f32 v66, v42, v43\n\t" \
  "v_smfmac_f32_16x16x64_bf16 v[80:83], v[60:63], v[16:23], v106\n\t" \
  "ds_read_b128 v[8:11], v103 offset:34816\n\t" \
  "v_cvt_pk_bf16_f32 v65, v44, v45\n\t" \
  "v_smfmac_f32_16x16x64_bf16 v[84:87], v[60:63], v[24:31], v106\n\t" \
  "ds_read_b128 v[12:15], v103 offset:35840\n\t" \
  "v_cvt_pk_bf16_f32 v67, v46, v47\n\t" \
  "v_smfmac_f32_16x16x64_bf16 v[88:91], v[96:99], v[152:159], v106\n\t" \
  "buffer_store_dwordx4 v[64:67], v108, %[ru], s47 offen sc1\n\t" \
  "v_cvt_pk_bf16_f32 v68, v80, v81\n\t" \
  "v_smfmac_f32_16x16x64_bf16 v[92:95], v[96:99], v[160:167], v106\n\t" \
  "v_cvt_pk_bf16_f32 v70, v82, v83\n\t" \
  "v_smfmac_f32_16x16x64_bf16 v[88:91], v[52:55], v[168:175], v106\n\t" \
  "v_cvt_pk_bf16_f32 v69, v84, v85\n\t" \
  "v_smfmac_f32_16x16x64_bf16 v[92:95], v[52:55], v[176:183], v106\n\t" \
  "v_cvt_pk_bf16_f32 v71, v86, v87\n\t" \
  "v_smfmac_f32_16x16x64_bf16 v[88:91], v[56:59], v[184:191], v106\n\t" \
  "ds_read_b128 v[16:19], v103 offset:49152\n\t" \
  "buffer_store_dwordx4 v[68:71], v108, %[ru], s76 offen sc1\n\t" \
  "v_smfmac_f32_16x16x64_bf16 v[92:95], v[56:59], v[192:199], v106\n\t" \
  "ds_read_b128 v[20:23], v103 offset:50176\n\t" \
  "s_waitcnt lgkmcnt(4)\n\t" \
  "v_smfmac_f32_16x16x64_bf16 v[88:91], v[60:63], v[0:7], v106\n\t" \
  "ds_read_b128 v[24:27], v103 offset:51200\n\t" \
  "s_waitcnt lgkmcnt(3)\n\t" \
  "v_smfmac_f32_16x16x64_bf16 v[92:95], v[60:63], v[8:15], v106\n\t" \
  "ds_read_b128 v[28:31], v103 offset:52224\n\t" \
  "v_smfmac_f32_16x16x64_bf16 v[32:35], v[96:99], v[200:207], v106\n\t" \
  "v_cvt_pk_bf16_f32 v72, v88, v89\n\t" \
  "v_smfmac_f32_16x16x64_bf16 v[36:39], v[96:99], v[208:215], v106\n\t" \
  "v_cvt_pk_bf16_f32 v74, v90, v91\n\t" \
  "v_smfmac_f32_16x16x64_bf16 v[32:35], v[52:55], v[216:223], v106\n\t" \
  "v_cvt_pk_bf16_f32 v73, v92, v93\n\t" \
  "v_smfmac_f32_16x16x64_bf16 v[36:39], v[52:55], v[224:231], v106\n\t" \
  "v_cvt_pk_bf16_f32 v75, v94, v95\n\t" \
  "v_smfmac_f32_16x16x64_bf16 v[32:35], v[56:59], v[232:239], v106\n\t" \
  "buffer_store_dwordx4 v[72:75], v108, %[ru], s77 offen sc1\n\t" \
  "s_waitcnt lgkmcnt(2)\n\t" \
  "v_smfmac_f32_16x16x64_bf16 v[32:35], v[60:63], v[16:23], v106\n\t" \
  "v_smfmac_f32_16x16x64_bf16 v[36:39], v[56:59], v[240:247], v106\n\t" \
  "s_waitcnt lgkmcnt(0)\n\t" \
  "v_smfmac_f32_16x16x64_bf16 v[36:39], v[60:63], v[24:31], v106\n\t" \
  "v_cvt_pk_bf16_f32 v76, v32, v33\n\t" \
  "v_cvt_pk_bf16_f32 v78, v34, v35\n\t" \
  "s_nop 5\n\t" \
  "v_cvt_pk_bf16_f32 v77, v36, v37\n\t" \
  "v_cvt_pk_bf16_f32 v79, v38, v39\n\t" \
  "buffer_store_dwordx4 v[76:79], v108, %[ru], s78 offen sc1\n\t" \
  "s_memtime s[76:77]\n\t" \
  "s_mov_b32 s42, 0\n\t" \
  "s_waitcnt lgkmcnt(0)\n\t" \
  "s_mov_b32 s57, s76\n\t" \
  "s_add_i32 s41, s57, 560\n\t" \
  "s_sub_i32 s76, s92, s51\n\t" \
  "s_sub_i32 s77, s93, s53\n\t" \
  "s_cmp_eq_u32 s76, 0\n\t" \
  "s_cbranch_scc1 21f\n\t" \
  "v_cvt_f32_u32 v117, s76\n\t" \
  "v_cvt_f32_u32 v116, s77\n\t" \
  "v_rcp_f32 v117, v117\n\t" \
  "s_mov_b32 s51, s92\n\t" \
  "v_mul_f32 v117, v117, v116\n\t" \
  "v_max_f32 v117, 0x3f028f5c, v117\n\t" \
  "v_min_f32 v117, 0x3ffeb852, v117\n\t" \
  "v_rcp_f32 v116, v117\n\t" \
  "v_mul_f32 v117, 0x4f000000, v117\n\t" \
  "v_mul_f32 v116, 0x47800000, v116\n\t" \
  "v_cvt_u32_f32 v117, v117\n\t" \
  "v_cvt_u32_f32 v116, v116\n\t" \
  "s_mov_b32 s53, s93\n\t" \
  "v_readfirstlane_b32 s46, v117\n\t" \
  "v_readfirstlane_b32 s52, v116\n\t" \
  "21:\n\t" \
  "s_add_i32 s44, s44, 1\n\t" \
  "s_and_b32 s58, s58, 4\n\t" \
  "s_and_b32 s76, s44, 64\n\t" \
  "s_or_b32 s42, s42, s76\n\t" \
  "s_cbranch_scc0 20b\n\t" \
  "s_branch 99f\n\t" \
  "14:\n\t" \
  "s_sleep 0\n\t" \
  "s_add_u32 s59, s59, 0x10000\n\t" \
  "s_cmp_lt_u32 s59, 0x100000\n\t" \
  "s_cbranch_scc1 13b\n\t" \
  "s_mov_b64 exec, -1\n\t" \
  "s_mov_b32 s42, 16\n\t" \
  "s_branch 99f\n\t" \
  "8:\n\t" \
  "s_bitcmp1_b32 s58, 1\n\t" \
  "s_cbranch_scc1 4b\n\t" \
  "s_bitcmp1_b32 s58, 2\n\t" \
  "s_cbranch_scc1 5b\n\t" \
  "v_max3_u32 v116, v32, v33, v34\n\t" \
  "v_max3_u32 v117, v35, v36, v37\n\t" \
  "v_max3_u32 v116, v116, v38, v39\n\t" \
  "v_max_u32 v116, v116, v117\n\t" \
  "v_cmp_eq_u32 vcc, -1, v116\n\t" \
  "s_cbranch_vccz 5b\n\t" \
  "s_branch 0f\n\t" \
  "9:\n\t" \
  "s_bitcmp1_b32 s58, 2\n\t" \
  "s_cbranch_scc1 6b\n\t" \
  "v_max3_u32 v116, v40, v41, v42\n\t" \
  "v_max3_u32 v117, v43, v44, v45\n\t" \
  "v_max3_u32 v116, v116, v46, v47\n\t" \
  "v_max_u32 v116, v116, v117\n\t" \
  "v_cmp_eq_u32 vcc, -1, v116\n\t" \
  "s_cbranch_vccz 6b\n\t" \
  "s_or_b32 s58, s58, 2\n\t" \
  "0:\n\t" \
  "s_or_b32 s58, s58, 1\n\t" \
  "s_sleep 0\n\t" \
  "11:\n\t" \
  "buffer_load_dwordx4 v[32:35], v100, %[rh], s40 offen sc1\n\t" \
  "buffer_load_dwordx4 v[36:39], v100, %[rh], s76 offen sc1\n\t" \
  "buffer_load_dwordx4 v[40:43], v100, %[rh], s77 offen sc1\n\t" \
  "buffer_load_dwordx4 v[44:47], v100, %[rh], s78 offen sc1\n\t" \
  "s_waitcnt lgkmcnt(0)\n\t" \
  "s_branch 10b\n\t" \
  "17:\n\t" \
  "v_mov_b64 v[72:73], 0\n\t" \
  "v_mov_b64 v[74:75], 0\n\t" \
  "v_mov_b64 v[76:77], 0\n\t" \
  "v_mov_b64 v[78:79], 0\n\t" \
  "s_add_u32 s59, s59, 1\n\t" \
  "s_bitcmp1_b32 s58, 2\n\t" \
  "s_cbranch_scc1 3b\n\t" \
  "s_waitcnt vmcnt(0)\n\t" \
  "v_max_u32 v116, v32, v33\n\t" \
  "v_max3_u32 v116, v116, v34, v35\n\t" \
  "v_max3_u32 v116, v116, v36, v37\n\t" \
  "v_max3_u32 v116, v116, v38, v39\n\t" \
  "v_max3_u32 v116, v116, v40, v41\n\t" \
  "v_max3_u32 v116, v116, v42, v43\n\t" \
  "v_max3_u32 v116, v116, v44, v45\n\t" \
  "v_max3_u32 v116, v116, v46, v47\n\t" \
  "v_cmp_eq_u32 vcc, -1, v116\n\t" \
  "s_cbranch_vccz 3b\n\t" \
  "s_bitcmp1_b32 s59, 6\n\t" \
  "s_cbranch_scc1 12f\n\t" \
  "buffer_load_dwordx4 v[32:35], v100, %[rh], s40 offen sc1\n\t" \
  "buffer_load_dwordx4 v[36:39], v100, %[rh], s76 offen sc1\n\t" \
  "buffer_load_dwordx4 v[40:43], v100, %[rh], s77 offen sc1\n\t" \
  "buffer_load_dwordx4 v[44:47], v100, %[rh], s78 offen sc1\n\t" \
  "s_branch 17b\n\t" \
  "12:\n\t" \
  "s_mov_b32 s42, s58\n\t" \
  "s_branch 99f\n\t" \
  "91:\n\t" \
  "s_mov_b32 s58, s42\n\t" \
  ".p2align 6\n\t" \
  "20:\n\t" \
  "s_and_b32 s76, s44, 7\n\t" \
  "s_lshl_b32 s76, s76, 15\n\t" \
  "s_add_i32 s40, s54, s76\n\t" \
  "s_and_b32 s76, s44, 3\n\t" \
  "s_lshl_b32 s45, s76, 16\n\t" \
  "s_and_b32 s47, s44, 3\n\t" \
  "s_lshl_b32 s76, s47, 2\n\t" \
  "s_lshl_b32 s47, s47, 20\n\t" \
  "s_add_i32 s47, s47, s55\n\t" \
  "s_lshr_b32 s76, %[um], s76\n\t" \
  "s_and_b32 s76, s76, 7\n\t" \
  "s_lshl_b32 s76, s76, 12\n\t" \
  "s_xor_b32 s47, s47, s76\n\t" \
  "s_add_i32 s49, s44, -1\n\t" \
  "s_and_b32 s48, s49, 3\n\t" \
  "s_lshl_b32 s48, s48, 16\n\t" \
  "s_lshl_b32 s50, s53, 8\n\t" \
  "s_mov_b32 s59, 0\n\t" \
  "s_mov_b32 s92, s51\n\t" \
  "s_mov_b32 s93, s53\n\t" \
  "s_xor_b32 s76, s40, 0x1000\n\t" \
  "s_xor_b32 s77, s40, 0x4000\n\t" \
  "s_xor_b32 s78, s40, 0x5000\n\t" \
  "s_cmp_lg_u32 s58, 0\n\t" \
  "s_cbranch_scc1 11f\n\t" \
  "10:\n\t" \
  "ds_read_b128 v[48:51], v102 offset:16384\n\t" \
  "ds_read_b128 v[52:55], v102 offset:17408\n\t" \
  "ds_read_b128 v[56:59], v102 offset:18432\n\t" \
  "ds_read_b128 v[60:63], v102 offset:19456\n\t" \
  "ds_read_b128 v[0:3], v102 offset:32768\n\t" \
  "ds_read_b128 v[4:7], v102 offset:33792\n\t" \
  "ds_read_b128 v[8:11], v102 offset:34816\n\t" \
  "ds_read_b128 v[12:15], v102 offset:35840\n\t" \
  "ds_read_b128 v[16:19], v102 offset:49152\n\t" \
  "ds_read_b128 v[20:23], v102 offset:50176\n\t" \
  "ds_read_b128 v[24:27], v102 offset:51200\n\t" \
  "ds_read_b128 v[28:31], v102 offset:52224\n\t" \
  "s_waitcnt lgkmcnt(4)\n\t" \
  "ds_read_b128 v[80:83], v102 offset:0\n\t" \
  "ds_read_b128 v[84:87], v102 offset:1024\n\t" \
  "ds_read_b128 v[88:91], v102 offset:2048\n\t" \
  "ds_read_b128 v[92:95], v102 offset:3072\n\t" \
  "s_bitcmp1_b32 s58, 1\n\t" \
  "s_cselect_b64 s[80:81], -1, 0\n\t" \
  "s_cmp_lg_u32 s58, 0\n\t" \
  "s_cbranch_scc1 17f\n\t" \
  "s_mov_b32 exec_hi, 0\n\t" \
  "16:\n\t" \
  "s_memtime vcc\n\t" \
  "s_waitcnt lgkmcnt(0)\n\t" \
  "s_sub_i32 s79, s41, vcc_lo\n\t" \
  "s_cmp_gt_i32 s79, 64\n\t" \
  "s_cbranch_scc1 16b\n\t" \
  "13:\n\t" \
  "buffer_load_dword v116, v110, %[rh], s48 offen sc1\n\t" \
  "s_waitcnt vmcnt(0)\n\t" \
  "15:\n\t" \
  "v_mov_b32 v117, 20000\n\t" \
  "v_cmp_ne_u32_sdwa vcc, v116, s49 src0_sel:BYTE_0 src1_sel:DWORD\n\t" \
  "v_subrev_u32 v116, s50, v116\n\t" \
  "v_ashrrev_i32 v116, 8, v116\n\t" \
  "s_nop 1\n\t" \
  "v_max_i32_dpp v116, v116, v116 quad_perm:[1,0,3,2] row_mask:0xf bank_mask:0xf\n\t" \
  "s_cbranch_vccnz 14f\n\t" \
  "s_nop 0\n\t" \
  "v_max_i32_dpp v116, v116, v116 quad_perm:[2,3,0,1] row_mask:0xf bank_mask:0xf\n\t" \
  "s_nop 1\n\t" \
  "v_max_i32_dpp v116, v116, v116 row_half_mirror row_mask:0xf bank_mask:0xf\n\t" \
  "s_nop 1\n\t" \
  "v_max_i32_dpp v116, v116, v116 row_mirror row_mask:0xf bank_mask:0xf\n\t" \
  "v_med3_i32 v116, v116, 0, v117\n\t" \
  "v_mul_u32_u24 v116, s52, v116\n\t" \
  "v_lshrrev_b32 v116, 16, v116\n\t" \
  "s_mov_b64 exec, -1\n\t" \
  "v_readlane_b32 s84, v116, 0\n\t" \
  "v_readlane_b32 s85, v116, 16\n\t" \
  "s_max_u32 s84, s84, s85\n\t" \
  "s_add_i32 s84, s84, s51\n\t" \
  "v_mov_b64 v[72:73], 0\n\t" \
  "v_mov_b64 v[74:75], 0\n\t" \
  "v_mov_b64 v[76:77], 0\n\t" \
  "v_mov_b64 v[78:79], 0\n\t" \
  "1:\n\t" \
  "s_memtime vcc\n\t" \
  "s_waitcnt lgkmcnt(0)\n\t" \
  "s_sub_i32 s79, s84, vcc_lo\n\t" \
  "s_cmp_gt_i32 s79, 128\n\t" \
  "s_cbranch_scc1 1b\n\t" \
  "2:\n\t" \
  "s_sub_i32 s79, s79, 33\n\t" \
  "s_cmp_gt_i32 s79, 56\n\t" \
  "s_cbranch_scc1 2b\n\t" \
  "buffer_load_dwordx4 v[32:35], v100, %[rh], s40 offen sc1\n\t" \
  "s_nop 12\n\t" \
  "buffer_load_dwordx4 v[36:39], v100, %[rh], s76 offen sc1\n\t" \
  "s_nop 12\n\t" \
  "buffer_load_dwordx4 v[40:43], v100, %[rh], s77 offen sc1\n\t" \
  "s_nop 12\n\t" \
  "buffer_load_dwordx4 v[44:47], v100, %[rh], s78 offen sc1\n\t" \
  "s_memtime vcc\n\t" \
  "s_memrealtime s[82:83]\n\t" \
  "s_nop 4\n\t" \
  "s_memrealtime s[84:85]\n\t" \
  "s_nop 4\n\t" \
  "s_memrealtime s[94:95]\n\t" \
  "s_nop 4\n\t" \
  "s_memrealtime s[86:87]\n\t" \
  "s_nop 4\n\t" \
  "s_memrealtime s[88:89]\n\t" \
  "s_nop 4\n\t" \
  "s_memrealtime s[90:91]\n\t" \
  "s_mov_b32 s92, 0\n\t" \
  "s_waitcnt lgkmcnt(0)\n\t" \
  "s_cmp_eq_u32 s82, s84\n\t" \
  "s_addc_u32 s92, s92, 0\n\t" \
  "s_cmp_eq_u32 s82, s94\n\t" \
  "s_addc_u32 s92, s92, 0\n\t" \
  "s_cmp_eq_u32 s82, s86\n\t" \
  "s_addc_u32 s92, s92, 0\n\t" \
  "s_cmp_eq_u32 s82, s88\n\t" \
  "s_addc_u32 s92, s92, 0\n\t" \
  "s_cmp_eq_u32 s82, s90\n\t" \
  "s_addc_u32 s92, s92, 0\n\t" \
  "s_mul_i32 s92, s92, 24\n\t" \
  "s_add_i32 s92, s92, vcc_lo\n\t" \
  "s_lshl4_add_u32 s93, s82, 64\n\t" \
  "3:\n\t" \
  "s_waitcnt lgkmcnt(0)\n\t" \
  "s_waitcnt vmcnt(3)\n\t" \
  "v_smfmac_f32_16x16x64_bf16 v[72:75], v[32:35], v[48:55], v104\n\t" \
  "ds_write_b128 v101, v[32:35] offset:4096\n\t" \
  "v_smfmac_f32_16x16x64_bf16 v[76:79], v[32:35], v[56:63], v104\n\t" \
  "s_waitcnt vmcnt(2)\n\t" \
  "ds_write_b128 v101, v[36:39] offset:5120\n\t" \
  "v_smfmac_f32_16x16x64_bf16 v[72:75], v[36:39], a[48:55], v104\n\t" \
  "v_smfmac_f32_16x16x64_bf16 v[76:79], v[36:39], a[56:63], v104\n\t" \
  "s_nop 3\n\t" \
  "v_cmp_u_f32 vcc, v72, v73\n\t" \
  "v_cmp_u_f32 s[82:83], v74, v75\n\t" \
  "s_waitcnt vmcnt(1)\n\t" \
  "v_smfmac_f32_16x16x64_bf16 v[72:75], v[40:43], a[64:71], v104\n\t" \
  "s_or_b64 vcc, vcc, s[82:83]\n\t" \
  "s_or_b64 vcc, vcc, s[80:81]\n\t" \
  "v_smfmac_f32_16x16x64_bf16 v[76:79], v[40:43], a[72:79], v104\n\t" \
  "s_waitcnt lgkmcnt(0)\n\t" \
  "s_cbranch_scc1 8f\n\t" \
  "5:\n\t" \
  "s_barrier\n\t" \
  "4:\n\t" \
  "ds_read_b128 v[48:51], v101 offset:8192\n\t" \
  "ds_read_b128 v[52:55], v101 offset:9216\n\t" \
  "ds_read_b128 v[56:59], v101 offset:13312\n\t" \
  "s_waitcnt vmcnt(0)\n\t" \
  "v_smfmac_f32_16x16x64_bf16 v[72:75], v[44:47], a[80:87], v104\n\t" \
  "ds_read_b128 v[60:63], v101 offset:12288\n\t" \
  "ds_read_b128 v[64:67], v101 offset:1024\n\t" \
  "v_smfmac_f32_16x16x64_bf16 v[76:79], v[44:47], a[88:95], v104\n\t" \
  "ds_read_b128 v[68:71], v101 offset:0\n\t" \
  "ds_write_b128 v101, v[40:43] offset:6144\n\t" \
  "s_nop 1\n\t" \
  "v_cmp_u_f32 vcc, v72, v73\n\t" \
  "v_cmp_u_f32 s[82:83], v74, v75\n\t" \
  "s_waitcnt lgkmcnt(6)\n\t" \
  "v_smfmac_f32_16x16x64_bf16 v[72:75], v[48:51], v[0:7], v104\n\t" \
  "s_or_b64 vcc, vcc, s[82:83]\n\t" \
  "v_smfmac_f32_16x16x64_bf16 v[76:79], v[48:51], v[8:15], v104\n\t" \
  "s_cbranch_scc1 9f\n\t" \
  "6:\n\t" \
  "s_waitcnt lgkmcnt(5)\n\t" \
  "v_smfmac_f32_16x16x64_bf16 v[72:75], v[52:55], a[96:103], v104\n\t" \
  "ds_write_b128 v101, v[44:47] offset:7168\n\t" \
  "v_smfmac_f32_16x16x64_bf16 v[76:79], v[52:55], a[104:111], v104\n\t" \
  "v_mov_b64 v[40:41], 0\n\t" \
  "s_waitcnt lgkmcnt(5)\n\t" \
  "v_smfmac_f32_16x16x64_bf16 v[72:75], v[56:59], a[144:151], v104\n\t" \
  "v_mov_b64 v[42:43], 0\n\t" \
  "v_smfmac_f32_16x16x64_bf16 v[76:79], v[56:59], a[152:159], v104\n\t" \
  "s_waitcnt lgkmcnt(4)\n\t" \
  "v_smfmac_f32_16x16x64_bf16 v[72:75], v[60:63], v[16:23], v104\n\t" \
  "s_waitcnt lgkmcnt(0)\n\t" \
  "s_barrier\n\t" \
  "v_smfmac_f32_16x16x64_bf16 v[76:79], v[60:63], v[24:31], v104\n\t" \
  "ds_read_b128 v[48:51], v101 offset:10240\n\t" \
  "ds_read_b128 v[52:55], v101 offset:14336\n\t" \
  "v_smfmac_f32_16x16x64_bf16 v[72:75], v[64:67], a[0:7], v104\n\t" \
  "ds_read_b128 v[56:59], v101 offset:2048\n\t" \
  "ds_read_b128 v[60:63], v101 offset:11264\n\t" \
  "v_smfmac_f32_16x16x64_bf16 v[76:79], v[64:67], a[8:15], v104\n\t" \
  "ds_read_b128 v[32:35], v101 offset:15360\n\t" \
  "ds_read_b128 v[36:39], v101 offset:3072\n\t" \
  "v_smfmac_f32_16x16x64_bf16 v[72:75], v[68:71], v[80:87], v104\n\t" \
  "ds_read_b128 v[0:3], v103 offset:0\n\t" \
  "v_mov_b64 v[44:45], 0\n\t" \
  "v_smfmac_f32_16x16x64_bf16 v[76:79], v[68:71], v[88:95], v104\n\t" \
  "v_mov_b64 v[46:47], 0\n\t" \
  "s_waitcnt lgkmcnt(5)\n\t" \
  "v_smfmac_f32_16x16x64_bf16 v[72:75], v[48:51], a[112:119], v104\n\t" \
  "ds_read_b128 v[4:7], v103 offset:1024\n\t" \
  "v_mov_b64 v[80:81], 0\n\t" \
  "v_smfmac_f32_16x16x64_bf16 v[72:75], v[52:55], a[160:167], v104\n\t" \
  "v_mov_b64 v[82:83], 0\n\t" \
  "s_waitcnt lgkmcnt(2)\n\t" \
  "v_smfmac_f32_16x16x64_bf16 v[72:75], v[56:59], a[16:23], v104\n\t" \
  "ds_read_b128 v[8:11], v103 offset:2048\n\t" \
  "v_mov_b64 v[84:85], 0\n\t" \
  "v_smfmac_f32_16x16x64_bf16 v[72:75], v[60:63], a[128:135], v104\n\t" \
  "ds_read_b128 v[12:15], v103 offset:3072\n\t" \
  "v_mov_b64 v[86:87], 0\n\t" \
  "v_smfmac_f32_16x16x64_bf16 v[72:75], v[32:35], a[176:183], v104\n\t" \
  "ds_read_b128 v[16:19], v103 offset:16384\n\t" \
  "v_mov_b64 v[88:89], 0\n\t" \
  "v_smfmac_f32_16x16x64_bf16 v[72:75], v[36:39], a[32:39], v104\n\t" \
  "ds_read_b128 v[20:23], v103 offset:17408\n\t" \
  "v_mov_b64 v[90:91], 0\n\t" \
  "v_smfmac_f32_16x16x64_bf16 v[76:79], v[48:51], a[120:127], v104\n\t" \
  "ds_read_b128 v[24:27], v103 offset:18432\n\t" \
  "v_mov_b64 v[92:93], 0\n\t" \
  "v_smfmac_f32_16x16x64_bf16 v[76:79], v[52:55], a[168:175], v104\n\t" \
  "v_add_f32 v96, v72, v74\n\t" \
  "v_add_f32 v97, v73, v75\n\t" \
  "v_cvt_pk_bf16_f32 v96, v96, v97\n\t" \
  "ds_write_b32 v105, v96\n\t" \
  "v_smfmac_f32_16x16x64_bf16 v[76:79], v[56:59], a[24:31], v104\n\t" \
  "ds_read_b128 v[28:31], v103 offset:19456\n\t" \
  "v_mov_b64 v[94:95], 0\n\t" \
  "v_smfmac_f32_16x16x64_bf16 v[76:79], v[60:63], a[136:143], v104\n\t" \
  "v_smfmac_f32_16x16x64_bf16 v[76:79], v[32:35], a[184:191], v104\n\t" \
  "v_smfmac_f32_16x16x64_bf16 v[76:79], v[36:39], a[40:47], v104\n\t" \
  "s_waitcnt lgkmcnt(1)\n\t" \
  "s_barrier\n\t" \
  "ds_read_b64_tr_b16 v[96:97], v107 offset:0\n\t" \
  "ds_read_b64_tr_b16 v[98:99], v107 offset:64\n\t" \
  "ds_read_b64_tr_b16 v[56:57], v107 offset:1024\n\t" \
  "ds_read_b64_tr_b16 v[58:59], v107 offset:1088\n\t" \
  "s_nop 1\n\t" \
  "v_add_f32 v116, v76, v78\n\t" \
  "v_add_f32 v117, v77, v79\n\t" \
  "v_cvt_pk_bf16_f32 v116, v116, v117\n\t" \
  "ds_write_b32 v105, v116 offset:256\n\t" \
  "s_waitcnt lgkmcnt(3)\n\t" \
  "v_smfmac_f32_16x16x64_bf16 v[40:43], v[96:99], a[192:199], v106\n\t" \
  "v_smfmac_f32_16x16x64_bf16 v[44:47], v[96:99], a[200:207], v106\n\t" \
  "s_waitcnt lgkmcnt(0)\n\t" \
  "s_barrier\n\t" \
  "ds_read_b64_tr_b16 v[52:53], v107 offset:256\n\t" \
  "ds_read_b64_tr_b16 v[54:55], v107 offset:320\n\t" \
  "v_smfmac_f32_16x16x64_bf16 v[40:43], v[56:59], a[224:231], v106\n\t" \
  "ds_read_b64_tr_b16 v[60:61], v107 offset:1280\n\t" \
  "ds_read_b64_tr_b16 v[62:63], v107 offset:1344\n\t" \
  "v_smfmac_f32_16x16x64_bf16 v[44:47], v[56:59], a[232:239], v106\n\t" \
  "v_smfmac_f32_16x16x64_bf16 v[80:83], v[96:99], a[240:247], v106\n\t" \
  "v_mov_b64 v[32:33], 0\n\t" \
  "v_smfmac_f32_16x16x64_bf16 v[84:87], v[96:99], a[248:255], v106\n\t" \
  "v_mov_b64 v[34:35], 0\n\t" \
  "v_smfmac_f32_16x16x64_bf16 v[80:83], v[56:59], v[136:143], v106\n\t" \
  "v_mov_b64 v[36:37], 0\n\t" \
  "v_smfmac_f32_16x16x64_bf16 v[84:87], v[56:59], v[144:151], v106\n\t" \
  "s_waitcnt lgkmcnt(2)\n\t" \
  "v_smfmac_f32_16x16x64_bf16 v[40:43], v[52:55], a[208:215], v106\n\t" \
  "v_smfmac_f32_16x16x64_bf16 v[44:47], v[52:55], a[216:223], v106\n\t" \
  "s_xor_b32 s76, s47, 0x40000\n\t" \
  "s_waitcnt lgkmcnt(0)\n\t" \
  "v_smfmac_f32_16x16x64_bf16 v[40:43], v[60:63], v[0:7], v106\n\t" \
  "v_mov_b64 v[38:39], 0\n\t" \
  "s_xor_b32 s77, s47, 0x84000\n\t" \
  "v_smfmac_f32_16x16x64_bf16 v[44:47], v[60:63], v[8:15], v106\n\t" \
  "s_xor_b32 s78, s47, 0xc4000\n\t" \
  "v_smfmac_f32_16x16x64_bf16 v[80:83], v[52:55], v[120:127], v106\n\t" \
  "ds_read_b128 v[0:3], v103 offset:32768\n\t" \
  "v_cvt_pk_bf16_f32 v64, v40, v41\n\t" \
  "v_smfmac_f32_16x16x64_bf16 v[84:87], v[52:55], v[128:135], v106\n\t" \
  "ds_read_b128 v[4:7], v103 offset:33792\n\t" \
  "v_cvt_pk_bf16_f32 v66, v42, v43\n\t" \
  "v_smfmac_f32_16x16x64_bf16 v[80:83], v[60:63], v[16:23], v106\n\t" \
  "ds_read_b128 v[8:11], v103 offset:34816\n\t" \
  "v_cvt_pk_bf16_f32 v65, v44, v45\n\t" \
  "v_smfmac_f32_16x16x64_bf16 v[84:87], v[60:63], v[24:31], v106\n\t" \
  "ds_read_b128 v[12:15], v103 offset:35840\n\t" \
  "v_cvt_pk_bf16_f32 v67, v46, v47\n\t" \
  "v_smfmac_f32_16x16x64_bf16 v[88:91], v[96:99], v[152:159], v106\n\t" \
  "v_cvt_pk_bf16_f32 v68, v80, v81\n\t" \
  "v_smfmac_f32_16x16x64_bf16 v[92:95], v[96:99], v[160:167], v106\n\t" \
  "buffer_store_dwordx4 v[64:67], v108, %[ru], s47 offen sc1\n\t" \
  "v_cvt_pk_bf16_f32 v70, v82, v83\n\t" \
  "v_smfmac_f32_16x16x64_bf16 v[88:91], v[52:55], v[168:175], v106\n\t" \
  "v_cvt_pk_bf16_f32 v69, v84, v85\n\t" \
  "v_smfmac_f32_16x16x64_bf16 v[92:95], v[52:55], v[176:183], v106\n\t" \
  "v_cvt_pk_bf16_f32 v71, v86, v87\n\t" \
  "v_smfmac_f32_16x16x64_bf16 v[88:91], v[56:59], v[184:191], v106\n\t" \
  "ds_read_b128 v[16:19], v103 offset:49152\n\t" \
  "v_smfmac_f32_16x16x64_bf16 v[92:95], v[56:59], v[192:199], v106\n\t" \
  "ds_read_b128 v[20:23], v103 offset:50176\n\t" \
  "buffer_store_dwordx4 v[68:71], v108, %[ru], s76 offen sc1\n\t" \
  "s_waitcnt lgkmcnt(4)\n\t" \
  "v_smfmac_f32_16x16x64_bf16 v[88:91], v[60:63], v[0:7], v106\n\t" \
  "ds_read_b128 v[24:27], v103 offset:51200\n\t" \
  "s_waitcnt lgkmcnt(3)\n\t" \
  "v_smfmac_f32_16x16x64_bf16 v[92:95], v[60:63], v[8:15], v106\n\t" \
  "ds_read_b128 v[28:31], v103 offset:52224\n\t" \
  "v_smfmac_f32_16x16x64_bf16 v[32:35], v[96:99], v[200:207], v106\n\t" \
  "v_cvt_pk_bf16_f32 v72, v88, v89\n\t" \
  "v_smfmac_f32_16x16x64_bf16 v[36:39], v[96:99], v[208:215], v106\n\t" \
  "v_cvt_pk_bf16_f32 v74, v90, v91\n\t" \
  "v_smfmac_f32_16x16x64_bf16 v[32:35], v[52:55], v[216:223], v106\n\t" \
  "v_cvt_pk_bf16_f32 v73, v92, v93\n\t" \
  "v_smfmac_f32_16x16x64_bf16 v[36:39], v[52:55], v[224:231], v106\n\t" \
  "v_cvt_pk_bf16_f32 v75, v94, v95\n\t" \
  "v_smfmac_f32_16x16x64_bf16 v[32:35], v[56:59], v[232:239], v106\n\t" \
  "s_waitcnt lgkmcnt(2)\n\t" \
  "v_smfmac_f32_16x16x64_bf16 v[32:35], v[60:63], v[16:23], v106\n\t" \
  "buffer_store_dwordx4 v[72:75], v108, %[ru], s77 offen sc1\n\t" \
  "v_smfmac_f32_16x16x64_bf16 v[36:39], v[56:59], v[240:247], v106\n\t" \
  "s_waitcnt lgkmcnt(0)\n\t" \
  "v_smfmac_f32_16x16x64_bf16 v[36:39], v[60:63], v[24:31], v106\n\t" \
  "v_cvt_pk_bf16_f32 v76, v32, v33\n\t" \
  "v_cvt_pk_bf16_f32 v78, v34, v35\n\t" \
  "s_nop 5\n\t" \
  "v_cvt_pk_bf16_f32 v77, v36, v37\n\t" \
  "v_cvt_pk_bf16_f32 v79, v38, v39\n\t" \
  "buffer_store_dwordx4 v[76:79], v108, %[ru], s78 offen sc1\n\t" \
  "s_memtime s[76:77]\n\t" \
  "s_mov_b32 s42, 0\n\t" \
  "s_waitcnt lgkmcnt(0)\n\t" \
  "s_mov_b32 s57, s76\n\t" \
  "s_add_i32 s41, s57, 560\n\t" \
  "s_sub_i32 s76, s92, s51\n\t" \
  "s_sub_i32 s77, s93, s53\n\t" \
  "s_cmp_eq_u32 s76, 0\n\t" \
  "s_cbranch_scc1 21f\n\t" \
  "v_cvt_f32_u32 v117, s76\n\t" \
  "v_cvt_f32_u32 v116, s77\n\t" \
  "v_rcp_f32 v117, v117\n\t" \
  "s_mov_b32 s51, s92\n\t" \
  "v_mul_f32 v117, v117, v116\n\t" \
  "v_max_f32 v117, 0x3f028f5c, v117\n\t" \
  "v_min_f32 v117, 0x3ffeb852, v117\n\t" \
  "v_rcp_f32 v116, v117\n\t" \
  "v_mul_f32 v117, 0x4f000000, v117\n\t" \
  "v_mul_f32 v116, 0x47800000, v116\n\t" \
  "v_cvt_u32_f32 v117, v117\n\t" \
  "v_cvt_u32_f32 v116, v116\n\t" \
  "s_mov_b32 s53, s93\n\t" \
  "v_readfirstlane_b32 s46, v117\n\t" \
  "v_readfirstlane_b32 s52, v116\n\t" \
  "21:\n\t" \
  "s_add_i32 s44, s44, 1\n\t" \
  "s_and_b32 s58, s58, 4\n\t" \
  "s_and_b32 s76, s44, 64\n\t" \
  "s_or_b32 s42, s42, s76\n\t" \
  "s_cbranch_scc0 20b\n\t" \
  "s_branch 99f\n\t" \
  "14:\n\t" \
  "s_sleep 0\n\t" \
  "s_add_u32 s59, s59, 0x10000\n\t" \
  "s_cmp_lt_u32 s59, 0x100000\n\t" \
  "s_cbranch_scc1 13b\n\t" \
  "s_mov_b64 exec, -1\n\t" \
  "s_mov_b32 s42, 16\n\t" \
  "s_branch 99f\n\t" \
  "8:\n\t" \
  "s_bitcmp1_b32 s58, 1\n\t" \
  "s_cbranch_scc1 4b\n\t" \
  "s_bitcmp1_b32 s58, 2\n\t" \
  "s_cbranch_scc1 5b\n\t" \
  "v_max3_u32 v116, v32, v33, v34\n\t" \
  "v_max3_u32 v117, v35, v36, v37\n\t" \
  "v_max3_u32 v116, v116, v38, v39\n\t" \
  "v_max_u32 v116, v116, v117\n\t" \
  "v_cmp_eq_u32 vcc, -1, v116\n\t" \
  "s_cbranch_vccz 5b\n\t" \
  "s_branch 0f\n\t" \
  "9:\n\t" \
  "s_bitcmp1_b32 s58, 2\n\t" \
  "s_cbranch_scc1 6b\n\t" \
  "v_max3_u32 v116, v40, v41, v42\n\t" \
  "v_max3_u32 v117, v43, v44, v45\n\t" \
  "v_max3_u32 v116, v116, v46, v47\n\t" \
  "v_max_u32 v116, v116, v117\n\t" \
  "v_cmp_eq_u32 vcc, -1, v116\n\t" \
  "s_cbranch_vccz 6b\n\t" \
  "s_or_b32 s58, s58, 2\n\t" \
  "0:\n\t" \
  "s_or_b32 s58, s58, 1\n\t" \
  "s_sleep 0\n\t" \
  "11:\n\t" \
  "buffer_load_dwordx4 v[32:35], v100, %[rh], s40 offen sc1\n\t" \
  "buffer_load_dwordx4 v[36:39], v100, %[rh], s76 offen sc1\n\t" \
  "buffer_load_dwordx4 v[40:43], v100, %[rh], s77 offen sc1\n\t" \
  "buffer_load_dwordx4 v[44:47], v100, %[rh], s78 offen sc1\n\t" \
  "s_waitcnt lgkmcnt(0)\n\t" \
  "s_branch 10b\n\t" \
  "17:\n\t" \
  "v_mov_b64 v[72:73], 0\n\t" \
  "v_mov_b64 v[74:75], 0\n\t" \
  "v_mov_b64 v[76:77], 0\n\t" \
  "v_mov_b64 v[78:79], 0\n\t" \
  "s_add_u32 s59, s59, 1\n\t" \
  "s_bitcmp1_b32 s58, 2\n\t" \
  "s_cbranch_scc1 3b\n\t" \
  "s_waitcnt vmcnt(0)\n\t" \
  "v_max_u32 v116, v32, v33\n\t" \
  "v_max3_u32 v116, v116, v34, v35\n\t" \
  "v_max3_u32 v116, v116, v36, v37\n\t" \
  "v_max3_u32 v116, v116, v38, v39\n\t" \
  "v_max3_u32 v116, v116, v40, v41\n\t" \
  "v_max3_u32 v116, v116, v42, v43\n\t" \
  "v_max3_u32 v116, v116, v44, v45\n\t" \
  "v_max3_u32 v116, v116, v46, v47\n\t" \
  "v_cmp_eq_u32 vcc, -1, v116\n\t" \
  "s_cbranch_vccz 3b\n\t" \
  "s_bitcmp1_b32 s59, 6\n\t" \
  "s_cbranch_scc1 12f\n\t" \
  "buffer_load_dwordx4 v[32:35], v100, %[rh], s40 offen sc1\n\t" \
  "buffer_load_dwordx4 v[36:39], v100, %[rh], s76 offen sc1\n\t" \
  "buffer_load_dwordx4 v[40:43], v100, %[rh], s77 offen sc1\n\t" \
  "buffer_load_dwordx4 v[44:47], v100, %[rh], s78 offen sc1\n\t" \
  "s_branch 17b\n\t" \
  "12:\n\t" \
  "s_mov_b32 s42, s58\n\t" \
  "s_branch 99f\n\t" \
  "92:\n\t" \
  "s_mov_b32 s58, s42\n\t" \
  ".p2align 6\n\t" \
  "20:\n\t" \
  "s_and_b32 s76, s44, 7\n\t" \
  "s_lshl_b32 s76, s76, 15\n\t" \
  "s_add_i32 s40, s54, s76\n\t" \
  "s_and_b32 s76, s44, 3\n\t" \
  "s_lshl_b32 s45, s76, 16\n\t" \
  "s_and_b32 s47, s44, 3\n\t" \
  "s_lshl_b32 s76, s47, 2\n\t" \
  "s_lshl_b32 s47, s47, 20\n\t" \
  "s_add_i32 s47, s47, s55\n\t" \
  "s_lshr_b32 s76, %[um], s76\n\t" \
  "s_and_b32 s76, s76, 7\n\t" \
  "s_lshl_b32 s76, s76, 12\n\t" \
  "s_xor_b32 s47, s47, s76\n\t" \
  "s_add_i32 s49, s44, -1\n\t" \
  "s_and_b32 s48, s49, 3\n\t" \
  "s_lshl_b32 s48, s48, 16\n\t" \
  "s_lshl_b32 s50, s53, 8\n\t" \
  "s_mov_b32 s59, 0\n\t" \
  "s_mov_b32 s92, s51\n\t" \
  "s_mov_b32 s93, s53\n\t" \
  "s_xor_b32 s76, s40, 0x1000\n\t" \
  "s_xor_b32 s77, s40, 0x4000\n\t" \
  "s_xor_b32 s78, s40, 0x5000\n\t" \
  "s_cmp_lg_u32 s58, 0\n\t" \
  "s_cbranch_scc1 11f\n\t" \
  "10:\n\t" \
  "ds_read_b128 v[48:51], v102 offset:32768\n\t" \
  "ds_read_b128 v[52:55], v102 offset:33792\n\t" \
  "ds_read_b128 v[56:59], v102 offset:34816\n\t" \
  "ds_read_b128 v[60:63], v102 offset:35840\n\t" \
  "ds_read_b128 v[0:3], v102 offset:49152\n\t" \
  "ds_read_b128 v[4:7], v102 offset:50176\n\t" \
  "ds_read_b128 v[8:11], v102 offset:51200\n\t" \
  "ds_read_b128 v[12:15], v102 offset:52224\n\t" \
  "ds_read_b128 v[16:19], v102 offset:0\n\t" \
  "ds_read_b128 v[20:23], v102 offset:1024\n\t" \
  "ds_read_b128 v[24:27], v102 offset:2048\n\t" \
  "ds_read_b128 v[28:31], v102 offset:3072\n\t" \
  "s_waitcnt lgkmcnt(4)\n\t" \
  "ds_read_b128 v[80:83], v102 offset:16384\n\t" \
  "ds_read_b128 v[84:87], v102 offset:17408\n\t" \
  "ds_read_b128 v[88:91], v102 offset:18432\n\t" \
  "ds_read_b128 v[92:95], v102 offset:19456\n\t" \
  "s_bitcmp1_b32 s58, 1\n\t" \
  "s_cselect_b64 s[80:81], -1, 0\n\t" \
  "s_cmp_lg_u32 s58, 0\n\t" \
  "s_cbranch_scc1 17f\n\t" \
  "s_mov_b32 exec_hi, 0\n\t" \
  "16:\n\t" \
  "s_memtime vcc\n\t" \
  "s_waitcnt lgkmcnt(0)\n\t" \
  "s_sub_i32 s79, s41, vcc_lo\n\t" \
  "s_cmp_gt_i32 s79, 64\n\t" \
  "s_cbranch_scc1 16b\n\t" \
  "13:\n\t" \
  "buffer_load_dword v116, v110, %[rh], s48 offen sc1\n\t" \
  "s_waitcnt vmcnt(0)\n\t" \
  "15:\n\t" \
  "v_mov_b32 v117, 20000\n\t" \
  "v_cmp_ne_u32_sdwa vcc, v116, s49 src0_sel:BYTE_0 src1_sel:DWORD\n\t" \
  "v_subrev_u32 v116, s50, v116\n\t" \
  "v_ashrrev_i32 v116, 8, v116\n\t" \
  "s_nop 1\n\t" \
  "v_max_i32_dpp v116, v116, v116 quad_perm:[1,0,3,2] row_mask:0xf bank_mask:0xf\n\t" \
  "s_cbranch_vccnz 14f\n\t" \
  "s_nop 0\n\t" \
  "v_max_i32_dpp v116, v116, v116 quad_perm:[2,3,0,1] row_mask:0xf bank_mask:0xf\n\t" \
  "s_nop 1\n\t" \
  "v_max_i32_dpp v116, v116, v116 row_half_mirror row_mask:0xf bank_mask:0xf\n\t" \
  "s_nop 1\n\t" \
  "v_max_i32_dpp v116, v116, v116 row_mirror row_mask:0xf bank_mask:0xf\n\t" \
  "v_med3_i32 v116, v116, 0, v117\n\t" \
  "v_mul_u32_u24 v116, s52, v116\n\t" \
  "v_lshrrev_b32 v116, 16, v116\n\t" \
  "s_mov_b64 exec, -1\n\t" \
  "v_readlane_b32 s84, v116, 0\n\t" \
  "v_readlane_b32 s85, v116, 16\n\t" \
  "s_max_u32 s84, s84, s85\n\t" \
  "s_add_i32 s84, s84, s51\n\t" \
  "v_mov_b64 v[72:73], 0\n\t" \
  "v_mov_b64 v[74:75], 0\n\t" \
  "v_mov_b64 v[76:77], 0\n\t" \
  "v_mov_b64 v[78:79], 0\n\t" \
  "1:\n\t" \
  "s_memtime vcc\n\t" \
  "s_waitcnt lgkmcnt(0)\n\t" \
  "s_sub_i32 s79, s84, vcc_lo\n\t" \
  "s_cmp_gt_i32 s79, 128\n\t" \
  "s_cbranch_scc1 1b\n\t" \
  "2:\n\t" \
  "s_sub_i32 s79, s79, 33\n\t" \
  "s_cmp_gt_i32 s79, 56\n\t" \
  "s_cbranch_scc1 2b\n\t" \
  "buffer_load_dwordx4 v[32:35], v100, %[rh], s40 offen sc1\n\t" \
  "s_nop 12\n\t" \
  "buffer_load_dwordx4 v[36:39], v100, %[rh], s76 offen sc1\n\t" \
  "s_nop 12\n\t" \
  "buffer_load_dwordx4 v[40:43], v100, %[rh], s77 offen sc1\n\t" \
  "s_nop 12\n\t" \
  "buffer_load_dwordx4 v[44:47], v100, %[rh], s78 offen sc1\n\t" \
  "s_memtime vcc\n\t" \
  "s_memrealtime s[82:83]\n\t" \
  "s_nop 4\n\t" \
  "s_memrealtime s[84:85]\n\t" \
  "s_nop 4\n\t" \
  "s_memrealtime s[94:95]\n\t" \
  "s_nop 4\n\t" \
  "s_memrealtime s[86:87]\n\t" \
  "s_nop 4\n\t" \
  "s_memrealtime s[88:89]\n\t" \
  "s_nop 4\n\t" \
  "s_memrealtime s[90:91]\n\t" \
  "s_mov_b32 s92, 0\n\t" \
  "s_waitcnt lgkmcnt(0)\n\t" \
  "s_cmp_eq_u32 s82, s84\n\t" \
  "s_addc_u32 s92, s92, 0\n\t" \
  "s_cmp_eq_u32 s82, s94\n\t" \
  "s_addc_u32 s92, s92, 0\n\t" \
  "s_cmp_eq_u32 s82, s86\n\t" \
  "s_addc_u32 s92, s92, 0\n\t" \
  "s_cmp_eq_u32 s82, s88\n\t" \
  "s_addc_u32 s92, s92, 0\n\t" \
  "s_cmp_eq_u32 s82, s90\n\t" \
  "s_addc_u32 s92, s92, 0\n\t" \
  "s_mul_i32 s92, s92, 24\n\t" \
  "s_add_i32 s92, s92, vcc_lo\n\t" \
  "s_lshl4_add_u32 s93, s82, 64\n\t" \
  "3:\n\t" \
  "s_waitcnt lgkmcnt(0)\n\t" \
  "s_waitcnt vmcnt(3)\n\t" \
  "v_smfmac_f32_16x16x64_bf16 v[72:75], v[32:35], v[48:55], v104\n\t" \
  "ds_write_b128 v101, v[32:35] offset:8192\n\t" \
  "v_smfmac_f32_16x16x64_bf16 v[76:79], v[32:35], v[56:63], v104\n\t" \
  "s_waitcnt vmcnt(2)\n\t" \
  "ds_write_b128 v101, v[36:39] offset:9216\n\t" \
  "v_smfmac_f32_16x16x64_bf16 v[72:75], v[36:39], a[96:103], v104\n\t" \
  "v_smfmac_f32_16x16x64_bf16 v[76:79], v[36:39], a[104:111], v104\n\t" \
  "s_nop 3\n\t" \
  "v_cmp_u_f32 vcc, v72, v73\n\t" \
  "v_cmp_u_f32 s[82:83], v74, v75\n\t" \
  "s_waitcnt vmcnt(1)\n\t" \
  "v_smfmac_f32_16x16x64_bf16 v[72:75], v[40:43], a[112:119], v104\n\t" \
  "s_or_b64 vcc, vcc, s[82:83]\n\t" \
  "s_or_b64 vcc, vcc, s[80:81]\n\t" \
  "v_smfmac_f32_16x16x64_bf16 v[76:79], v[40:43], a[120:127], v104\n\t" \
  "s_waitcnt lgkmcnt(0)\n\t" \
  "s_cbranch_scc1 8f\n\t" \
  "5:\n\t" \
  "s_barrier\n\t" \
  "4:\n\t" \
  "ds_read_b128 v[48:51], v101 offset:12288\n\t" \
  "ds_read_b128 v[52:55], v101 offset:13312\n\t" \
  "ds_read_b128 v[56:59], v101 offset:1024\n\t" \
  "s_waitcnt vmcnt(0)\n\t" \
  "v_smfmac_f32_16x16x64_bf16 v[72:75], v[44:47], a[128:135], v104\n\t" \
  "ds_read_b128 v[60:63], v101 offset:0\n\t" \
  "ds_read_b128 v[64:67], v101 offset:5120\n\t" \
  "v_smfmac_f32_16x16x64_bf16 v[76:79], v[44:47], a[136:143], v104\n\t" \
  "ds_read_b128 v[68:71], v101 offset:4096\n\t" \
  "ds_write_b128 v101, v[40:43] offset:10240\n\t" \
  "s_nop 1\n\t" \
  "v_cmp_u_f32 vcc, v72, v73\n\t" \
  "v_cmp_u_f32 s[82:83], v74, v75\n\t" \
  "s_waitcnt lgkmcnt(6)\n\t" \
  "v_smfmac_f32_16x16x64_bf16 v[72:75], v[48:51], v[0:7], v104\n\t" \
  "s_or_b64 vcc, vcc, s[82:83]\n\t" \
  "v_smfmac_f32_16x16x64_bf16 v[76:79], v[48:51], v[8:15], v104\n\t" \
  "s_cbranch_scc1 9f\n\t" \
  "6:\n\t" \
  "s_waitcnt lgkmcnt(5)\n\t" \
  "v_smfmac_f32_16x16x64_bf16 v[72:75], v[52:55], a[144:151], v104\n\t" \
  "ds_write_b128 v101, v[44:47] offset:11264\n\t" \
  "v_smfmac_f32_16x16x64_bf16 v[76:79], v[52:55], a[152:159], v104\n\t" \
  "v_mov_b64 v[40:41], 0\n\t" \
  "s_waitcnt lgkmcnt(5)\n\t" \
  "v_smfmac_f32_16x16x64_bf16 v[72:75], v[56:59], a[0:7], v104\n\t" \
  "v_mov_b64 v[42:43], 0\n\t" \
  "v_smfmac_f32_16x16x64_bf16 v[76:79], v[56:59], a[8:15], v104\n\t" \
  "s_waitcnt lgkmcnt(4)\n\t" \
  "v_smfmac_f32_16x16x64_bf16 v[72:75], v[60:63], v[16:23], v104\n\t" \
  "s_waitcnt lgkmcnt(0)\n\t" \
  "s_barrier\n\t" \
  "v_smfmac_f32_16x16x64_bf16 v[76:79], v[60:63], v[24:31], v104\n\t" \
  "ds_read_b128 v[48:51], v101 offset:14336\n\t" \
  "ds_read_b128 v[52:55], v101 offset:2048\n\t" \
  "v_smfmac_f32_16x16x64_bf16 v[72:75], v[64:67], a[48:55], v104\n\t" \
  "ds_read_b128 v[56:59], v101 offset:6144\n\t" \
  "ds_read_b128 v[60:63], v101 offset:15360\n\t" \
  "v_smfmac_f32_16x16x64_bf16 v[76:79], v[64:67], a[56:63], v104\n\t" \
  "ds_read_b128 v[32:35], v101 offset:3072\n\t" \
  "ds_read_b128 v[36:39], v101 offset:7168\n\t" \
  "v_smfmac_f32_16x16x64_bf16 v[72:75], v[68:71], v[80:87], v104\n\t" \
  "ds_read_b128 v[0:3], v103 offset:0\n\t" \
  "v_mov_b64 v[44:45], 0\n\t" \
  "v_smfmac_f32_16x16x64_bf16 v[76:79], v[68:71], v[88:95], v104\n\t" \
  "v_mov_b64 v[46:47], 0\n\t" \
  "s_waitcnt lgkmcnt(5)\n\t" \
  "v_smfmac_f32_16x16x64_bf16 v[72:75], v[48:51], a[160:167], v104\n\t" \
  "ds_read_b128 v[4:7], v103 offset:1024\n\t" \
  "v_mov_b64 v[80:81], 0\n\t" \
  "v_smfmac_f32_16x16x64_bf16 v[72:75], v[52:55], a[16:23], v104\n\t" \
  "v_mov_b64 v[82:83], 0\n\t" \
  "s_waitcnt lgkmcnt(2)\n\t" \
  "v_smfmac_f32_16x16x64_bf16 v[72:75], v[56:59], a[64:71], v104\n\t" \
  "ds_read_b128 v[8:11], v103 offset:2048\n\t" \
  "v_mov_b64 v[84:85], 0\n\t" \
  "v_smfmac_f32_16x16x64_bf16 v[72:75], v[60:63], a[176:183], v104\n\t" \
  "ds_read_b128 v[12:15], v103 offset:3072\n\t" \
  "v_mov_b64 v[86:87], 0\n\t" \
  "v_smfmac_f32_16x16x64_bf16 v[72:75], v[32:35], a[32:39], v104\n\t" \
  "ds_read_b128 v[16:19], v103 offset:16384\n\t" \
  "v_mov_b64 v[88:89], 0\n\t" \
  "v_smfmac_f32_16x16x64_bf16 v[72:75], v[36:39], a[80:87], v104\n\t" \
  "ds_read_b128 v[20:23], v103 offset:17408\n\t" \
  "v_mov_b64 v[90:91], 0\n\t" \
  "v_smfmac_f32_16x16x64_bf16 v[76:79], v[48:51], a[168:175], v104\n\t" \
  "ds_read_b128 v[24:27], v103 offset:18432\n\t" \
  "v_mov_b64 v[92:93], 0\n\t" \
  "v_smfmac_f32_16x16x64_bf16 v[76:79], v[52:55], a[24:31], v104\n\t" \
  "v_add_f32 v96, v72, v74\n\t" \
  "v_add_f32 v97, v73, v75\n\t" \
  "v_cvt_pk_bf16_f32 v96, v96, v97\n\t" \
  "ds_write_b32 v105, v96\n\t" \
  "v_smfmac_f32_16x16x64_bf16 v[76:79], v[56:59], a[72:79], v104\n\t" \
  "ds_read_b128 v[28:31], v103 offset:19456\n\t" \
  "v_mov_b64 v[94:95], 0\n\t" \
  "v_smfmac_f32_16x16x64_bf16 v[76:79], v[60:63], a[184:191], v104\n\t" \
  "v_smfmac_f32_16x16x64_bf16 v[76:79], v[32:35], a[40:47], v104\n\t" \
  "v_smfmac_f32_16x16x64_bf16 v[76:79], v[36:39], a[88:95], v104\n\t" \
  "s_waitcnt lgkmcnt(1)\n\t" \
  "s_barrier\n\t" \
  "ds_read_b64_tr_b16 v[96:97], v107 offset:0\n\t" \
  "ds_read_b64_tr_b16 v[98:99], v107 offset:64\n\t" \
  "ds_read_b64_tr_b16 v[56:57], v107 offset:1024\n\t" \
  "ds_read_b64_tr_b16 v[58:59], v107 offset:1088\n\t" \
  "s_nop 1\n\t" \
  "v_add_f32 v116, v76, v78\n\t" \
  "v_add_f32 v117, v77, v79\n\t" \
  "v_cvt_pk_bf16_f32 v116, v116, v117\n\t" \
  "ds_write_b32 v105, v116 offset:256\n\t" \
  "s_waitcnt lgkmcnt(3)\n\t" \
  "v_smfmac_f32_16x16x64_bf16 v[40:43], v[96:99], a[192:199], v106\n\t" \
  "v_smfmac_f32_16x16x64_bf16 v[44:47], v[96:99], a[200:207], v106\n\t" \
  "s_waitcnt lgkmcnt(0)\n\t" \
  "s_barrier\n\t" \
  "ds_read_b64_tr_b16 v[52:53], v107 offset:256\n\t" \
  "ds_read_b64_tr_b16 v[54:55], v107 offset:320\n\t" \
  "v_smfmac_f32_16x16x64_bf16 v[40:43], v[56:59], a[224:231], v106\n\t" \
  "ds_read_b64_tr_b16 v[60:61], v107 offset:1280\n\t" \
  "ds_read_b64_tr_b16 v[62:63], v107 offset:1344\n\t" \
  "v_smfmac_f32_16x16x64_bf16 v[44:47], v[56:59], a[232:239], v106\n\t" \
  "v_smfmac_f32_16x16x64_bf16 v[80:83], v[96:99], a[240:247], v106\n\t" \
  "v_mov_b64 v[32:33], 0\n\t" \
  "v_smfmac_f32_16x16x64_bf16 v[84:87], v[96:99], a[248:255], v106\n\t" \
  "v_mov_b64 v[34:35], 0\n\t" \
  "v_smfmac_f32_16x16x64_bf16 v[80:83], v[56:59], v[136:143], v106\n\t" \
  "v_mov_b64 v[36:37], 0\n\t" \
  "v_smfmac_f32_16x16x64_bf16 v[84:87], v[56:59], v[144:151], v106\n\t" \
  "s_waitcnt lgkmcnt(2)\n\t" \
  "v_smfmac_f32_16x16x64_bf16 v[40:43], v[52:55], a[208:215], v106\n\t" \
  "v_smfmac_f32_16x16x64_bf16 v[44:47], v[52:55], a[216:223], v106\n\t" \
  "s_xor_b32 s76, s47, 0x40000\n\t" \
  "s_waitcnt lgkmcnt(0)\n\t" \
  "v_smfmac_f32_16x16x64_bf16 v[40:43], v[60:63], v[0:7], v106\n\t" \
  "v_mov_b64 v[38:39], 0\n\t" \
  "s_xor_b32 s77, s47, 0x84000\n\t" \
  "v_smfmac_f32_16x16x64_bf16 v[44:47], v[60:63], v[8:15], v106\n\t" \
  "s_xor_b32 s78, s47, 0xc4000\n\t" \
  "v_smfmac_f32_16x16x64_bf16 v[80:83], v[52:55], v[120:127], v106\n\t" \
  "ds_read_b128 v[0:3], v103 offset:32768\n\t" \
  "v_cvt_pk_bf16_f32 v64, v40, v41\n\t" \
  "v_smfmac_f32_16x16x64_bf16 v[84:87], v[52:55], v[128:135], v106\n\t" \
  "ds_read_b128 v[4:7], v103 offset:33792\n\t" \
  "v_cvt_pk_bf16_f32 v66, v42, v43\n\t" \
  "v_smfmac_f32_16x16x64_bf16 v[80:83], v[60:63], v[16:23], v106\n\t" \
  "ds_read_b128 v[8:11], v103 offset:34816\n\t" \
  "v_cvt_pk_bf16_f32 v65, v44, v45\n\t" \
  "v_smfmac_f32_16x16x64_bf16 v[84:87], v[60:63], v[24:31], v106\n\t" \
  "ds_read_b128 v[12:15], v103 offset:35840\n\t" \
  "v_cvt_pk_bf16_f32 v67, v46, v47\n\t" \
  "v_smfmac_f32_16x16x64_bf16 v[88:91], v[96:99], v[152:159], v106\n\t" \
  "v_cvt_pk_bf16_f32 v68, v80, v81\n\t" \
  "v_smfmac_f32_16x16x64_bf16 v[92:95], v[96:99], v[160:167], v106\n\t" \
  "v_cvt_pk_bf16_f32 v70, v82, v83\n\t" \
  "v_smfmac_f32_16x16x64_bf16 v[88:91], v[52:55], v[168:175], v106\n\t" \
  "buffer_store_dwordx4 v[64:67], v108, %[ru], s47 offen sc1\n\t" \
  "v_cvt_pk_bf16_f32 v69, v84, v85\n\t" \
  "v_smfmac_f32_16x16x64_bf16 v[92:95], v[52:55], v[176:183], v106\n\t" \
  "v_cvt_pk_bf16_f32 v71, v86, v87\n\t" \
  "v_smfmac_f32_16x16x64_bf16 v[88:91], v[56:59], v[184:191], v106\n\t" \
  "ds_read_b128 v[16:19], v103 offset:49152\n\t" \
  "v_smfmac_f32_16x16x64_bf16 v[92:95], v[56:59], v[192:199], v106\n\t" \
  "ds_read_b128 v[20:23], v103 offset:50176\n\t" \
  "s_waitcnt lgkmcnt(4)\n\t" \
  "v_smfmac_f32_16x16x64_bf16 v[88:91], v[60:63], v[0:7], v106\n\t" \
  "ds_read_b128 v[24:27], v103 offset:51200\n\t" \
  "buffer_store_dwordx4 v[68:71], v108, %[ru], s76 offen sc1\n\t" \
  "s_waitcnt lgkmcnt(3)\n\t" \
  "v_smfmac_f32_16x16x64_bf16 v[92:95], v[60:63], v[8:15], v106\n\t" \
  "ds_read_b128 v[28:31], v103 offset:52224\n\t" \
  "v_smfmac_f32_16x16x64_bf16 v[32:35], v[96:99], v[200:207], v106\n\t" \
  "v_cvt_pk_bf16_f32 v72, v88, v89\n\t" \
  "v_smfmac_f32_16x16x64_bf16 v[36:39], v[96:99], v[208:215], v106\n\t" \
  "v_cvt_pk_bf16_f32 v74, v90, v91\n\t" \
  "v_smfmac_f32_16x16x64_bf16 v[32:35], v[52:55], v[216:223], v106\n\t" \
  "v_cvt_pk_bf16_f32 v73, v92, v93\n\t" \
  "v_smfmac_f32_16x16x64_bf16 v[36:39], v[52:55], v[224:231], v106\n\t" \
  "v_cvt_pk_bf16_f32 v75, v94, v95\n\t" \
  "v_smfmac_f32_16x16x64_bf16 v[32:35], v[56:59], v[232:239], v106\n\t" \
  "s_waitcnt lgkmcnt(2)\n\t" \
  "v_smfmac_f32_16x16x64_bf16 v[32:35], v[60:63], v[16:23], v106\n\t" \
  "v_smfmac_f32_16x16x64_bf16 v[36:39], v[56:59], v[240:247], v106\n\t" \
  "buffer_store_dwordx4 v[72:75], v108, %[ru], s77 offen sc1\n\t" \
  "s_waitcnt lgkmcnt(0)\n\t" \
  "v_smfmac_f32_16x16x64_bf16 v[36:39], v[60:63], v[24:31], v106\n\t" \
  "v_cvt_pk_bf16_f32 v76, v32, v33\n\t" \
  "v_cvt_pk_bf16_f32 v78, v34, v35\n\t" \
  "s_nop 5\n\t" \
  "v_cvt_pk_bf16_f32 v77, v36, v37\n\t" \
  "v_cvt_pk_bf16_f32 v79, v38, v39\n\t" \
  "buffer_store_dwordx4 v[76:79], v108, %[ru], s78 offen sc1\n\t" \
  "s_memtime s[76:77]\n\t" \
  "s_mov_b32 s42, 0\n\t" \
  "s_waitcnt lgkmcnt(0)\n\t" \
  "s_mov_b32 s57, s76\n\t" \
  "s_add_i32 s41, s57, 560\n\t" \
  "s_sub_i32 s76, s92, s51\n\t" \
  "s_sub_i32 s77, s93, s53\n\t" \
  "s_cmp_eq_u32 s76, 0\n\t" \
  "s_cbranch_scc1 21f\n\t" \
  "v_cvt_f32_u32 v117, s76\n\t" \
  "v_cvt_f32_u32 v116, s77\n\t" \
  "v_rcp_f32 v117, v117\n\t" \
  "s_mov_b32 s51, s92\n\t" \
  "v_mul_f32 v117, v117, v116\n\t" \
  "v_max_f32 v117, 0x3f028f5c, v117\n\t" \
  "v_min_f32 v117, 0x3ffeb852, v117\n\t" \
  "v_rcp_f32 v116, v117\n\t" \
  "v_mul_f32 v117, 0x4f000000, v117\n\t" \
  "v_mul_f32 v116, 0x47800000, v116\n\t" \
  "v_cvt_u32_f32 v117, v117\n\t" \
  "v_cvt_u32_f32 v116, v116\n\t" \
  "s_mov_b32 s53, s93\n\t" \
  "v_readfirstlane_b32 s46, v117\n\t" \
  "v_readfirstlane_b32 s52, v116\n\t" \
  "21:\n\t" \
  "s_add_i32 s44, s44, 1\n\t" \
  "s_and_b32 s58, s58, 4\n\t" \
  "s_and_b32 s76, s44, 64\n\t" \
  "s_or_b32 s42, s42, s76\n\t" \
  "s_cbranch_scc0 20b\n\t" \
  "s_branch 99f\n\t" \
  "14:\n\t" \
  "s_sleep 0\n\t" \
  "s_add_u32 s59, s59, 0x10000\n\t" \
  "s_cmp_lt_u32 s59, 0x100000\n\t" \
  "s_cbranch_scc1 13b\n\t" \
  "s_mov_b64 exec, -1\n\t" \
  "s_mov_b32 s42, 16\n\t" \
  "s_branch 99f\n\t" \
  "8:\n\t" \
  "s_bitcmp1_b32 s58, 1\n\t" \
  "s_cbranch_scc1 4b\n\t" \
  "s_bitcmp1_b32 s58, 2\n\t" \
  "s_cbranch_scc1 5b\n\t" \
  "v_max3_u32 v116, v32, v33, v34\n\t" \
  "v_max3_u32 v117, v35, v36, v37\n\t" \
  "v_max3_u32 v116, v116, v38, v39\n\t" \
  "v_max_u32 v116, v116, v117\n\t" \
  "v_cmp_eq_u32 vcc, -1, v116\n\t" \
  "s_cbranch_vccz 5b\n\t" \
  "s_branch 0f\n\t" \
  "9:\n\t" \
  "s_bitcmp1_b32 s58, 2\n\t" \
  "s_cbranch_scc1 6b\n\t" \
  "v_max3_u32 v116, v40, v41, v42\n\t" \
  "v_max3_u32 v117, v43, v44, v45\n\t" \
  "v_max3_u32 v116, v116, v46, v47\n\t" \
  "v_max_u32 v116, v116, v117\n\t" \
  "v_cmp_eq_u32 vcc, -1, v116\n\t" \
  "s_cbranch_vccz 6b\n\t" \
  "s_or_b32 s58, s58, 2\n\t" \
  "0:\n\t" \
  "s_or_b32 s58, s58, 1\n\t" \
  "s_sleep 0\n\t" \
  "11:\n\t" \
  "buffer_load_dwordx4 v[32:35], v100, %[rh], s40 offen sc1\n\t" \
  "buffer_load_dwordx4 v[36:39], v100, %[rh], s76 offen sc1\n\t" \
  "buffer_load_dwordx4 v[40:43], v100, %[rh], s77 offen sc1\n\t" \
  "buffer_load_dwordx4 v[44:47], v100, %[rh], s78 offen sc1\n\t" \
  "s_waitcnt lgkmcnt(0)\n\t" \
  "s_branch 10b\n\t" \
  "17:\n\t" \
  "v_mov_b64 v[72:73], 0\n\t" \
  "v_mov_b64 v[74:75], 0\n\t" \
  "v_mov_b64 v[76:77], 0\n\t" \
  "v_mov_b64 v[78:79], 0\n\t" \
  "s_add_u32 s59, s59, 1\n\t" \
  "s_bitcmp1_b32 s58, 2\n\t" \
  "s_cbranch_scc1 3b\n\t" \
  "s_waitcnt vmcnt(0)\n\t" \
  "v_max_u32 v116, v32, v33\n\t" \
  "v_max3_u32 v116, v116, v34, v35\n\t" \
  "v_max3_u32 v116, v116, v36, v37\n\t" \
  "v_max3_u32 v116, v116, v38, v39\n\t" \
  "v_max3_u32 v116, v116, v40, v41\n\t" \
  "v_max3_u32 v116, v116, v42, v43\n\t" \
  "v_max3_u32 v116, v116, v44, v45\n\t" \
  "v_max3_u32 v116, v116, v46, v47\n\t" \
  "v_cmp_eq_u32 vcc, -1, v116\n\t" \
  "s_cbranch_vccz 3b\n\t" \
  "s_bitcmp1_b32 s59, 6\n\t" \
  "s_cbranch_scc1 12f\n\t" \
  "buffer_load_dwordx4 v[32:35], v100, %[rh], s40 offen sc1\n\t" \
  "buffer_load_dwordx4 v[36:39], v100, %[rh], s76 offen sc1\n\t" \
  "buffer_load_dwordx4 v[40:43], v100, %[rh], s77 offen sc1\n\t" \
  "buffer_load_dwordx4 v[44:47], v100, %[rh], s78 offen sc1\n\t" \
  "s_branch 17b\n\t" \
  "12:\n\t" \
  "s_mov_b32 s42, s58\n\t" \
  "s_branch 99f\n\t" \
  "93:\n\t" \
  "s_mov_b32 s58, s42\n\t" \
  ".p2align 6\n\t" \
  "20:\n\t" \
  "s_and_b32 s76, s44, 7\n\t" \
  "s_lshl_b32 s76, s76, 15\n\t" \
  "s_add_i32 s40, s54, s76\n\t" \
  "s_and_b32 s76, s44, 3\n\t" \
  "s_lshl_b32 s45, s76, 16\n\t" \
  "s_and_b32 s47, s44, 3\n\t" \
  "s_lshl_b32 s76, s47, 2\n\t" \
  "s_lshl_b32 s47, s47, 20\n\t" \
  "s_add_i32 s47, s47, s55\n\t" \
  "s_lshr_b32 s76, %[um], s76\n\t" \
  "s_and_b32 s76, s76, 7\n\t" \
  "s_lshl_b32 s76, s76, 12\n\t" \
  "s_xor_b32 s47, s47, s76\n\t" \
  "s_add_i32 s49, s44, -1\n\t" \
  "s_and_b32 s48, s49, 3\n\t" \
  "s_lshl_b32 s48, s48, 16\n\t" \
  "s_lshl_b32 s50, s53, 8\n\t" \
  "s_mov_b32 s59, 0\n\t" \
  "s_mov_b32 s92, s51\n\t" \
  "s_mov_b32 s93, s53\n\t" \
  "s_xor_b32 s76, s40, 0x1000\n\t" \
  "s_xor_b32 s77, s40, 0x4000\n\t" \
  "s_xor_b32 s78, s40, 0x5000\n\t" \
  "s_cmp_lg_u32 s58, 0\n\t" \
  "s_cbranch_scc1 11f\n\t" \
  "10:\n\t" \
  "ds_read_b128 v[48:51], v102 offset:49152\n\t" \
  "ds_read_b128 v[52:55], v102 offset:50176\n\t" \
  "ds_read_b128 v[56:59], v102 offset:51200\n\t" \
  "ds_read_b128 v[60:63], v102 offset:52224\n\t" \
  "ds_read_b128 v[0:3], v102 offset:0\n\t" \
  "ds_read_b128 v[4:7], v102 offset:1024\n\t" \
  "ds_read_b128 v[8:11], v102 offset:2048\n\t" \
  "ds_read_b128 v[12:15], v102 offset:3072\n\t" \
  "ds_read_b128 v[16:19], v102 offset:16384\n\t" \
  "ds_read_b128 v[20:23], v102 offset:17408\n\t" \
  "ds_read_b128 v[24:27], v102 offset:18432\n\t" \
  "ds_read_b128 v[28:31], v102 offset:19456\n\t" \
  "s_waitcnt lgkmcnt(4)\n\t" \
  "ds_read_b128 v[80:83], v102 offset:32768\n\t" \
  "ds_read_b128 v[84:87], v102 offset:33792\n\t" \
  "ds_read_b128 v[88:91], v102 offset:34816\n\t" \
  "ds_read_b128 v[92:95], v102 offset:35840\n\t" \
  "s_bitcmp1_b32 s58, 1\n\t" \
  "s_cselect_b64 s[80:81], -1, 0\n\t" \
  "s_cmp_lg_u32 s58, 0\n\t" \
  "s_cbranch_scc1 17f\n\t" \
  "s_mov_b32 exec_hi, 0\n\t" \
  "16:\n\t" \
  "s_memtime vcc\n\t" \
  "s_waitcnt lgkmcnt(0)\n\t" \
  "s_sub_i32 s79, s41, vcc_lo\n\t" \
  "s_cmp_gt_i32 s79, 64\n\t" \
  "s_cbranch_scc1 16b\n\t" \
  "13:\n\t" \
  "buffer_load_dword v116, v110, %[rh], s48 offen sc1\n\t" \
  "s_waitcnt vmcnt(0)\n\t" \
  "15:\n\t" \
  "v_mov_b32 v117, 20000\n\t" \
  "v_cmp_ne_u32_sdwa vcc, v116, s49 src0_sel:BYTE_0 src1_sel:DWORD\n\t" \
  "v_subrev_u32 v116, s50, v116\n\t" \
  "v_ashrrev_i32 v116, 8, v116\n\t" \
  "s_nop 1\n\t" \
  "v_max_i32_dpp v116, v116, v116 quad_perm:[1,0,3,2] row_mask:0xf bank_mask:0xf\n\t" \
  "s_cbranch_vccnz 14f\n\t" \
  "s_nop 0\n\t" \
  "v_max_i32_dpp v116, v116, v116 quad_perm:[2,3,0,1] row_mask:0xf bank_mask:0xf\n\t" \
  "s_nop 1\n\t" \
  "v_max_i32_dpp v116, v116, v116 row_half_mirror row_mask:0xf bank_mask:0xf\n\t" \
  "s_nop 1\n\t" \
  "v_max_i32_dpp v116, v116, v116 row_mirror row_mask:0xf bank_mask:0xf\n\t" \
  "v_med3_i32 v116, v116, 0, v117\n\t" \
  "v_mul_u32_u24 v116, s52, v116\n\t" \
  "v_lshrrev_b32 v116, 16, v116\n\t" \
  "s_mov_b64 exec, -1\n\t" \
  "v_readlane_b32 s84, v116, 0\n\t" \
  "v_readlane_b32 s85, v116, 16\n\t" \
  "s_max_u32 s84, s84, s85\n\t" \
  "s_add_i32 s84, s84, s51\n\t" \
  "v_mov_b64 v[72:73], 0\n\t" \
  "v_mov_b64 v[74:75], 0\n\t" \
  "v_mov_b64 v[76:77], 0\n\t" \
  "v_mov_b64 v[78:79], 0\n\t" \
  "1:\n\t" \
  "s_memtime vcc\n\t" \
  "s_waitcnt lgkmcnt(0)\n\t" \
  "s_sub_i32 s79, s84, vcc_lo\n\t" \
  "s_cmp_gt_i32 s79, 128\n\t" \
  "s_cbranch_scc1 1b\n\t" \
  "2:\n\t" \
  "s_sub_i32 s79, s79, 33\n\t" \
  "s_cmp_gt_i32 s79, 56\n\t" \
  "s_cbranch_scc1 2b\n\t" \
  "buffer_load_dwordx4 v[32:35], v100, %[rh], s40 offen sc1\n\t" \
  "s_nop 12\n\t" \
  "buffer_load_dwordx4 v[36:39], v100, %[rh], s76 offen sc1\n\t" \
  "s_nop 12\n\t" \
  "buffer_load_dwordx4 v[40:43], v100, %[rh], s77 offen sc1\n\t" \
  "s_nop 12\n\t" \
  "buffer_load_dwordx4 v[44:47], v100, %[rh], s78 offen sc1\n\t" \
  "s_memtime vcc\n\t" \
  "s_memrealtime s[82:83]\n\t" \
  "s_nop 4\n\t" \
  "s_memrealtime s[84:85]\n\t" \
  "s_nop 4\n\t" \
  "s_memrealtime s[94:95]\n\t" \
  "s_nop 4\n\t" \
  "s_memrealtime s[86:87]\n\t" \
  "s_nop 4\n\t" \
  "s_memrealtime s[88:89]\n\t" \
  "s_nop 4\n\t" \
  "s_memrealtime s[90:91]\n\t" \
  "s_mov_b32 s92, 0\n\t" \
  "s_waitcnt lgkmcnt(0)\n\t" \
  "s_cmp_eq_u32 s82, s84\n\t" \
  "s_addc_u32 s92, s92, 0\n\t" \
  "s_cmp_eq_u32 s82, s94\n\t" \
  "s_addc_u32 s92, s92, 0\n\t" \
  "s_cmp_eq_u32 s82, s86\n\t" \
  "s_addc_u32 s92, s92, 0\n\t" \
  "s_cmp_eq_u32 s82, s88\n\t" \
  "s_addc_u32 s92, s92, 0\n\t" \
  "s_cmp_eq_u32 s82, s90\n\t" \
  "s_addc_u32 s92, s92, 0\n\t" \
  "s_mul_i32 s92, s92, 24\n\t" \
  "s_add_i32 s92, s92, vcc_lo\n\t" \
  "s_lshl4_add_u32 s93, s82, 64\n\t" \
  "3:\n\t" \
  "s_waitcnt lgkmcnt(0)\n\t" \
  "s_waitcnt vmcnt(3)\n\t" \
  "v_smfmac_f32_16x16x64_bf16 v[72:75], v[32:35], v[48:55], v104\n\t" \
  "ds_write_b128 v101, v[32:35] offset:12288\n\t" \
  "v_smfmac_f32_16x16x64_bf16 v[76:79], v[32:35], v[56:63], v104\n\t" \
  "s_waitcnt vmcnt(2)\n\t" \
  "ds_write_b128 v101, v[36:39] offset:13312\n\t" \
  "v_smfmac_f32_16x16x64_bf16 v[72:75], v[36:39], a[144:151], v104\n\t" \
  "v_smfmac_f32_16x16x64_bf16 v[76:79], v[36:39], a[152:159], v104\n\t" \
  "s_nop 3\n\t" \
  "v_cmp_u_f32 vcc, v72, v73\n\t" \
  "v_cmp_u_f32 s[82:83], v74, v75\n\t" \
  "s_waitcnt vmcnt(1)\n\t" \
  "v_smfmac_f32_16x16x64_bf16 v[72:75], v[40:43], a[160:167], v104\n\t" \
  "s_or_b64 vcc, vcc, s[82:83]\n\t" \
  "s_or_b64 vcc, vcc, s[80:81]\n\t" \
  "v_smfmac_f32_16x16x64_bf16 v[76:79], v[40:43], a[168:175], v104\n\t" \
  "s_waitcnt lgkmcnt(0)\n\t" \
  "s_cbranch_scc1 8f\n\t" \
  "5:\n\t" \
  "s_barrier\n\t" \
  "4:\n\t" \
  "ds_read_b128 v[48:51], v101 offset:0\n\t" \
  "ds_read_b128 v[52:55], v101 offset:1024\n\t" \
  "ds_read_b128 v[56:59], v101 offset:5120\n\t" \
  "s_waitcnt vmcnt(0)\n\t" \
  "v_smfmac_f32_16x16x64_bf16 v[72:75], v[44:47], a[176:183], v104\n\t" \
  "ds_read_b128 v[60:63], v101 offset:4096\n\t" \
  "ds_read_b128 v[64:67], v101 offset:9216\n\t" \
  "v_smfmac_f32_16x16x64_bf16 v[76:79], v[44:47], a[184:191], v104\n\t" \
  "ds_read_b128 v[68:71], v101 offset:8192\n\t" \
  "ds_write_b128 v101, v[40:43] offset:14336\n\t" \
  "s_nop 1\n\t" \
  "v_cmp_u_f32 vcc, v72, v73\n\t" \
  "v_cmp_u_f32 s[82:83], v74, v75\n\t" \
  "s_waitcnt lgkmcnt(6)\n\t" \
  "v_smfmac_f32_16x16x64_bf16 v[72:75], v[48:51], v[0:7], v104\n\t" \
  "s_or_b64 vcc, vcc, s[82:83]\n\t" \
  "v_smfmac_f32_16x16x64_bf16 v[76:79], v[48:51], v[8:15], v104\n\t" \
  "s_cbranch_scc1 9f\n\t" \
  "6:\n\t" \
  "s_waitcnt lgkmcnt(5)\n\t" \
  "v_smfmac_f32_16x16x64_bf16 v[72:75], v[52:55], a[0:7], v104\n\t" \
  "ds_write_b128 v101, v[44:47] offset:15360\n\t" \
  "v_smfmac_f32_16x16x64_bf16 v[76:79], v[52:55], a[8:15], v104\n\t" \
  "v_mov_b64 v[40:41], 0\n\t" \
  "s_waitcnt lgkmcnt(5)\n\t" \
  "v_smfmac_f32_16x16x64_bf16 v[72:75], v[56:59], a[48:55], v104\n\t" \
  "v_mov_b64 v[42:43], 0\n\t" \
  "v_smfmac_f32_16x16x64_bf16 v[76:79], v[56:59], a[56:63], v104\n\t" \
  "s_waitcnt lgkmcnt(4)\n\t" \
  "v_smfmac_f32_16x16x64_bf16 v[72:75], v[60:63], v[16:23], v104\n\t" \
  "s_waitcnt lgkmcnt(0)\n\t" \
  "s_barrier\n\t" \
  "v_smfmac_f32_16x16x64_bf16 v[76:79], v[60:63], v[24:31], v104\n\t" \
  "ds_read_b128 v[48:51], v101 offset:2048\n\t" \
  "ds_read_b128 v[52:55], v101 offset:6144\n\t" \
  "v_smfmac_f32_16x16x64_bf16 v[72:75], v[64:67], a[96:103], v104\n\t" \
  "ds_read_b128 v[56:59], v101 offset:10240\n\t" \
  "ds_read_b128 v[60:63], v101 offset:3072\n\t" \
  "v_smfmac_f32_16x16x64_bf16 v[76:79], v[64:67], a[104:111], v104\n\t" \
  "ds_read_b128 v[32:35], v101 offset:7168\n\t" \
  "ds_read_b128 v[36:39], v101 offset:11264\n\t" \
  "v_smfmac_f32_16x16x64_bf16 v[72:75], v[68:71], v[80:87], v104\n\t" \
  "ds_read_b128 v[0:3], v103 offset:0\n\t" \
  "v_mov_b64 v[44:45], 0\n\t" \
  "v_smfmac_f32_16x16x64_bf16 v[76:79], v[68:71], v[88:95], v104\n\t" \
  "v_mov_b64 v[46:47], 0\n\t" \
  "s_waitcnt lgkmcnt(5)\n\t" \
  "v_smfmac_f32_16x16x64_bf16 v[72:75], v[48:51], a[16:23], v104\n\t" \
  "ds_read_b128 v[4:7], v103 offset:1024\n\t" \
  "v_mov_b64 v[80:81], 0\n\t" \
  "v_smfmac_f32_16x16x64_bf16 v[72:75], v[52:55], a[64:71], v104\n\t" \
  "v_mov_b64 v[82:83], 0\n\t" \
  "s_waitcnt lgkmcnt(2)\n\t" \
  "v_smfmac_f32_16x16x64_bf16 v[72:75], v[56:59], a[112:119], v104\n\t" \
  "ds_read_b128 v[8:11], v103 offset:2048\n\t" \
  "v_mov_b64 v[84:85], 0\n\t" \
  "v_smfmac_f32_16x16x64_bf16 v[72:75], v[60:63], a[32:39], v104\n\t" \
  "ds_read_b128 v[12:15], v103 offset:3072\n\t" \
  "v_mov_b64 v[86:87], 0\n\t" \
  "v_smfmac_f32_16x16x64_bf16 v[72:75], v[32:35], a[80:87], v104\n\t" \
  "ds_read_b128 v[16:19], v103 offset:16384\n\t" \
  "v_mov_b64 v[88:89], 0\n\t" \
  "v_smfmac_f32_16x16x64_bf16 v[72:75], v[36:39], a[128:135], v104\n\t" \
  "ds_read_b128 v[20:23], v103 offset:17408\n\t" \
  "v_mov_b64 v[90:91], 0\n\t" \
  "v_smfmac_f32_16x16x64_bf16 v[76:79], v[48:51], a[24:31], v104\n\t" \
  "ds_read_b128 v[24:27], v103 offset:18432\n\t" \
  "v_mov_b64 v[92:93], 0\n\t" \
  "v_smfmac_f32_16x16x64_bf16 v[76:79], v[52:55], a[72:79], v104\n\t" \
  "v_add_f32 v96, v72, v74\n\t" \
  "v_add_f32 v97, v73, v75\n\t" \
  "v_cvt_pk_bf16_f32 v96, v96, v97\n\t" \
  "ds_write_b32 v105, v96\n\t" \
  "v_smfmac_f32_16x16x64_bf16 v[76:79], v[56:59], a[120:127], v104\n\t" \
  "ds_read_b128 v[28:31], v103 offset:19456\n\t" \
  "v_mov_b64 v[94:95], 0\n\t" \
  "v_smfmac_f32_16x16x64_bf16 v[76:79], v[60:63], a[40:47], v104\n\t" \
  "v_smfmac_f32_16x16x64_bf16 v[76:79], v[32:35], a[88:95], v104\n\t" \
  "v_smfmac_f32_16x16x64_bf16 v[76:79], v[36:39], a[136:143], v104\n\t" \
  "s_waitcnt lgkmcnt(1)\n\t" \
  "s_barrier\n\t" \
  "ds_read_b64_tr_b16 v[96:97], v107 offset:0\n\t" \
  "ds_read_b64_tr_b16 v[98:99], v107 offset:64\n\t" \
  "ds_read_b64_tr_b16 v[56:57], v107 offset:1024\n\t" \
  "ds_read_b64_tr_b16 v[58:59], v107 offset:1088\n\t" \
  "s_nop 1\n\t" \
  "v_add_f32 v116, v76, v78\n\t" \
  "v_add_f32 v117, v77, v79\n\t" \
  "v_cvt_pk_bf16_f32 v116, v116, v117\n\t" \
  "ds_write_b32 v105, v116 offset:256\n\t" \
  "s_waitcnt lgkmcnt(3)\n\t" \
  "v_smfmac_f32_16x16x64_bf16 v[40:43], v[96:99], a[192:199], v106\n\t" \
  "v_smfmac_f32_16x16x64_bf16 v[44:47], v[96:99], a[200:207], v106\n\t" \
  "s_waitcnt lgkmcnt(0)\n\t" \
  "s_barrier\n\t" \
  "ds_read_b64_tr_b16 v[52:53], v107 offset:256\n\t" \
  "ds_read_b64_tr_b16 v[54:55], v107 offset:320\n\t" \
  "v_smfmac_f32_16x16x64_bf16 v[40:43], v[56:59], a[224:231], v106\n\t" \
  "ds_read_b64_tr_b16 v[60:61], v107 offset:1280\n\t" \
  "ds_read_b64_tr_b16 v[62:63], v107 offset:1344\n\t" \
  "v_smfmac_f32_16x16x64_bf16 v[44:47], v[56:59], a[232:239], v106\n\t" \
  "v_smfmac_f32_16x16x64_bf16 v[80:83], v[96:99], a[240:247], v106\n\t" \
  "v_mov_b64 v[32:33], 0\n\t" \
  "v_smfmac_f32_16x16x64_bf16 v[84:87], v[96:99], a[248:255], v106\n\t" \
  "v_mov_b64 v[34:35], 0\n\t" \
  "v_smfmac_f32_16x16x64_bf16 v[80:83], v[56:59], v[136:143], v106\n\t" \
  "v_mov_b64 v[36:37], 0\n\t" \
  "v_smfmac_f32_16x16x64_bf16 v[84:87], v[56:59], v[144:151], v106\n\t" \
  "s_waitcnt lgkmcnt(2)\n\t" \
  "v_smfmac_f32_16x16x64_bf16 v[40:43], v[52:55], a[208:215], v106\n\t" \
  "v_smfmac_f32_16x16x64_bf16 v[44:47], v[52:55], a[216:223], v106\n\t" \
  "s_xor_b32 s76, s47, 0x40000\n\t" \
  "s_waitcnt lgkmcnt(0)\n\t" \
  "v_smfmac_f32_16x16x64_bf16 v[40:43], v[60:63], v[0:7], v106\n\t" \
  "v_mov_b64 v[38:39], 0\n\t" \
  "s_xor_b32 s77, s47, 0x84000\n\t" \
  "v_smfmac_f32_16x16x64_bf16 v[44:47], v[60:63], v[8:15], v106\n\t" \
  "s_xor_b32 s78, s47, 0xc4000\n\t" \
  "v_smfmac_f32_16x16x64_bf16 v[80:83], v[52:55], v[120:127], v106\n\t" \
  "ds_read_b128 v[0:3], v103 offset:32768\n\t" \
  "v_cvt_pk_bf16_f32 v64, v40, v41\n\t" \
  "v_smfmac_f32_16x16x64_bf16 v[84:87], v[52:55], v[128:135], v106\n\t" \
  "ds_read_b128 v[4:7], v103 offset:33792\n\t" \
  "v_cvt_pk_bf16_f32 v66, v42, v43\n\t" \
  "v_smfmac_f32_16x16x64_bf16 v[80:83], v[60:63], v[16:23], v106\n\t" \
  "ds_read_b128 v[8:11], v103 offset:34816\n\t" \
  "v_cvt_pk_bf16_f32 v65, v44, v45\n\t" \
  "v_smfmac_f32_16x16x64_bf16 v[84:87], v[60:63], v[24:31], v106\n\t" \
  "ds_read_b128 v[12:15], v103 offset:35840\n\t" \
  "v_cvt_pk_bf16_f32 v67, v46, v47\n\t" \
  "v_smfmac_f32_16x16x64_bf16 v[88:91], v[96:99], v[152:159], v106\n\t" \
  "v_cvt_pk_bf16_f32 v68, v80, v81\n\t" \
  "v_smfmac_f32_16x16x64_bf16 v[92:95], v[96:99], v[160:167], v106\n\t" \
  "v_cvt_pk_bf16_f32 v70, v82, v83\n\t" \
  "v_smfmac_f32_16x16x64_bf16 v[88:91], v[52:55], v[168:175], v106\n\t" \
  "v_cvt_pk_bf16_f32 v69, v84, v85\n\t" \
  "v_smfmac_f32_16x16x64_bf16 v[92:95], v[52:55], v[176:183], v106\n\t" \
  "buffer_store_dwordx4 v[64:67], v108, %[ru], s47 offen sc1\n\t" \
  "v_cvt_pk_bf16_f32 v71, v86, v87\n\t" \
  "v_smfmac_f32_16x16x64_bf16 v[88:91], v[56:59], v[184:191], v106\n\t" \
  "ds_read_b128 v[16:19], v103 offset:49152\n\t" \
  "v_smfmac_f32_16x16x64_bf16 v[92:95], v[56:59], v[192:199], v106\n\t" \
  "ds_read_b128 v[20:23], v103 offset:50176\n\t" \
  "s_waitcnt lgkmcnt(4)\n\t" \
  "v_smfmac_f32_16x16x64_bf16 v[88:91], v[60:63], v[0:7], v106\n\t" \
  "ds_read_b128 v[24:27], v103 offset:51200\n\t" \
  "s_waitcnt lgkmcnt(3)\n\t" \
  "v_smfmac_f32_16x16x64_bf16 v[92:95], v[60:63], v[8:15], v106\n\t" \
  "ds_read_b128 v[28:31], v103 offset:52224\n\t" \
  "buffer_store_dwordx4 v[68:71], v108, %[ru], s76 offen sc1\n\t" \
  "v_smfmac_f32_16x16x64_bf16 v[32:35], v[96:99], v[200:207], v106\n\t" \
  "v_cvt_pk_bf16_f32 v72, v88, v89\n\t" \
  "v_smfmac_f32_16x16x64_bf16 v[36:39], v[96:99], v[208:215], v106\n\t" \
  "v_cvt_pk_bf16_f32 v74, v90, v91\n\t" \
  "v_smfmac_f32_16x16x64_bf16 v[32:35], v[52:55], v[216:223], v106\n\t" \
  "v_cvt_pk_bf16_f32 v73, v92, v93\n\t" \
  "v_smfmac_f32_16x16x64_bf16 v[36:39], v[52:55], v[224:231], v106\n\t" \
  "v_cvt_pk_bf16_f32 v75, v94, v95\n\t" \
  "v_smfmac_f32_16x16x64_bf16 v[32:35], v[56:59], v[232:239], v106\n\t" \
  "s_waitcnt lgkmcnt(2)\n\t" \
  "v_smfmac_f32_16x16x64_bf16 v[32:35], v[60:63], v[16:23], v106\n\t" \
  "v_smfmac_f32_16x16x64_bf16 v[36:39], v[56:59], v[240:247], v106\n\t" \
  "s_waitcnt lgkmcnt(0)\n\t" \
  "v_smfmac_f32_16x16x64_bf16 v[36:39], v[60:63], v[24:31], v106\n\t" \
  "buffer_store_dwordx4 v[72:75], v108, %[ru], s77 offen sc1\n\t" \
  "v_cvt_pk_bf16_f32 v76, v32, v33\n\t" \
  "v_cvt_pk_bf16_f32 v78, v34, v35\n\t" \
  "s_nop 5\n\t" \
  "v_cvt_pk_bf16_f32 v77, v36, v37\n\t" \
  "v_cvt_pk_bf16_f32 v79, v38, v39\n\t" \
  "buffer_store_dwordx4 v[76:79], v108, %[ru], s78 offen sc1\n\t" \
  "s_memtime s[76:77]\n\t" \
  "s_mov_b32 s42, 0\n\t" \
  "s_waitcnt lgkmcnt(0)\n\t" \
  "s_mov_b32 s57, s76\n\t" \
  "s_add_i32 s41, s57, 560\n\t" \
  "s_sub_i32 s76, s92, s51\n\t" \
  "s_sub_i32 s77, s93, s53\n\t" \
  "s_cmp_eq_u32 s76, 0\n\t" \
  "s_cbranch_scc1 21f\n\t" \
  "v_cvt_f32_u32 v117, s76\n\t" \
  "v_cvt_f32_u32 v116, s77\n\t" \
  "v_rcp_f32 v117, v117\n\t" \
  "s_mov_b32 s51, s92\n\t" \
  "v_mul_f32 v117, v117, v116\n\t" \
  "v_max_f32 v117, 0x3f028f5c, v117\n\t" \
  "v_min_f32 v117, 0x3ffeb852, v117\n\t" \
  "v_rcp_f32 v116, v117\n\t" \
  "v_mul_f32 v117, 0x4f000000, v117\n\t" \
  "v_mul_f32 v116, 0x47800000, v116\n\t" \
  "v_cvt_u32_f32 v117, v117\n\t" \
  "v_cvt_u32_f32 v116, v116\n\t" \
  "s_mov_b32 s53, s93\n\t" \
  "v_readfirstlane_b32 s46, v117\n\t" \
  "v_readfirstlane_b32 s52, v116\n\t" \
  "21:\n\t" \
  "s_add_i32 s44, s44, 1\n\t" \
  "s_and_b32 s58, s58, 4\n\t" \
  "s_and_b32 s76, s44, 64\n\t" \
  "s_or_b32 s42, s42, s76\n\t" \
  "s_cbranch_scc0 20b\n\t" \
  "s_branch 99f\n\t" \
  "14:\n\t" \
  "s_sleep 0\n\t" \
  "s_add_u32 s59, s59, 0x10000\n\t" \
  "s_cmp_lt_u32 s59, 0x100000\n\t" \
  "s_cbranch_scc1 13b\n\t" \
  "s_mov_b64 exec, -1\n\t" \
  "s_mov_b32 s42, 16\n\t" \
  "s_branch 99f\n\t" \
  "8:\n\t" \
  "s_bitcmp1_b32 s58, 1\n\t" \
  "s_cbranch_scc1 4b\n\t" \
  "s_bitcmp1_b32 s58, 2\n\t" \
  "s_cbranch_scc1 5b\n\t" \
  "v_max3_u32 v116, v32, v33, v34\n\t" \
  "v_max3_u32 v117, v35, v36, v37\n\t" \
  "v_max3_u32 v116, v116, v38, v39\n\t" \
  "v_max_u32 v116, v116, v117\n\t" \
  "v_cmp_eq_u32 vcc, -1, v116\n\t" \
  "s_cbranch_vccz 5b\n\t" \
  "s_branch 0f\n\t" \
  "9:\n\t" \
  "s_bitcmp1_b32 s58, 2\n\t" \
  "s_cbranch_scc1 6b\n\t" \
  "v_max3_u32 v116, v40, v41, v42\n\t" \
  "v_max3_u32 v117, v43, v44, v45\n\t" \
  "v_max3_u32 v116, v116, v46, v47\n\t" \
  "v_max_u32 v116, v116, v117\n\t" \
  "v_cmp_eq_u32 vcc, -1, v116\n\t" \
  "s_cbranch_vccz 6b\n\t" \
  "s_or_b32 s58, s58, 2\n\t" \
  "0:\n\t" \
  "s_or_b32 s58, s58, 1\n\t" \
  "s_sleep 0\n\t" \
  "11:\n\t" \
  "buffer_load_dwordx4 v[32:35], v100, %[rh], s40 offen sc1\n\t" \
  "buffer_load_dwordx4 v[36:39], v100, %[rh], s76 offen sc1\n\t" \
  "buffer_load_dwordx4 v[40:43], v100, %[rh], s77 offen sc1\n\t" \
  "buffer_load_dwordx4 v[44:47], v100, %[rh], s78 offen sc1\n\t" \
  "s_waitcnt lgkmcnt(0)\n\t" \
  "s_branch 10b\n\t" \
  "17:\n\t" \
  "v_mov_b64 v[72:73], 0\n\t" \
  "v_mov_b64 v[74:75], 0\n\t" \
  "v_mov_b64 v[76:77], 0\n\t" \
  "v_mov_b64 v[78:79], 0\n\t" \
  "s_add_u32 s59, s59, 1\n\t" \
  "s_bitcmp1_b32 s58, 2\n\t" \
  "s_cbranch_scc1 3b\n\t" \
  "s_waitcnt vmcnt(0)\n\t" \
  "v_max_u32 v116, v32, v33\n\t" \
  "v_max3_u32 v116, v116, v34, v35\n\t" \
  "v_max3_u32 v116, v116, v36, v37\n\t" \
  "v_max3_u32 v116, v116, v38, v39\n\t" \
  "v_max3_u32 v116, v116, v40, v41\n\t" \
  "v_max3_u32 v116, v116, v42, v43\n\t" \
  "v_max3_u32 v116, v116, v44, v45\n\t" \
  "v_max3_u32 v116, v116, v46, v47\n\t" \
  "v_cmp_eq_u32 vcc, -1, v116\n\t" \
  "s_cbranch_vccz 3b\n\t" \
  "s_bitcmp1_b32 s59, 6\n\t" \
  "s_cbranch_scc1 12f\n\t" \
  "buffer_load_dwordx4 v[32:35], v100, %[rh], s40 offen sc1\n\t" \
  "buffer_load_dwordx4 v[36:39], v100, %[rh], s76 offen sc1\n\t" \
  "buffer_load_dwordx4 v[40:43], v100, %[rh], s77 offen sc1\n\t" \
  "buffer_load_dwordx4 v[44:47], v100, %[rh], s78 offen sc1\n\t" \
  "s_branch 17b\n\t" \
  "12:\n\t" \
  "s_mov_b32 s42, s58\n\t" \
  "s_branch 99f\n\t" \
  "99:\n\t" \
  "s_waitcnt vmcnt(0) lgkmcnt(0)\n\t" \
  ".Lchain_last:\n\t" \
  ""
#define CB_BODY \
  "s_mov_b32 s58, 8\n\t" \
  "s_mov_b32 s59, 0\n\t" \
  "s_sub_i32 s56, s51, 8000\n\t" \
  "s_lshl_b32 s50, s53, 8\n\t" \
  "s_branch 40f\n\t" \
  ".p2align 6\n\t" \
  "20:\n\t" \
  "v_readfirstlane_b32 s101, v211\n\t" \
  "v_readlane_b32 s91, v211, 1\n\t" \
  "s_mov_b64 exec, 0xff\n\t" \
  "s_max_i32 s91, s91, 0\n\t" \
  "s_max_i32 s101, s101, 0\n\t" \
  "s_min_i32 s101, s101, 240\n\t" \
  "s_mov_b32 s88, s51\n\t" \
  "s_add_i32 s85, s44, -1\n\t" \
  "s_add_i32 s79, s44, 2\n\t" \
  "v_cmp_gt_i32 vcc, s79, v210\n\t" \
  "s_and_b32 s96, s79, 15\n\t" \
  "s_lshl_b32 s96, s96, 22\n\t" \
  "s_add_i32 s96, s96, s42\n\t" \
  "s_lshr_b32 s82, s96, 21\n\t" \
  "s_bitcmp0_b32 %[sel], s82\n\t" \
  "s_cselect_b32 s82, 0x2000, 0\n\t" \
  "s_add_i32 s96, s96, s82\n\t" \
  "s_cmp_lt_u32 s79, 64\n\t" \
  "s_cselect_b32 s79, vcc_lo, -1\n\t" \
  "s_cmp_eq_u32 s79, 0\n\t" \
  "s_cselect_b32 s96, s96, 0x7f000000\n\t" \
  "s_cmp_eq_u32 s44, 0\n\t" \
  "s_cbranch_scc1 23f\n\t" \
  "21:\n\t" \
  "v_cmp_ne_u32_sdwa vcc, v208, s85 src0_sel:BYTE_0 src1_sel:DWORD\n\t" \
  "v_subrev_u32 v208, s50, v208\n\t" \
  "v_ashrrev_i32 v208, 8, v208\n\t" \
  "s_nop 1\n\t" \
  "v_max_i32_dpp v208, v208, v208 quad_perm:[1,0,3,2] row_mask:0xf bank_mask:0xf\n\t" \
  "s_cbranch_vccnz 22f\n\t" \
  "24:\n\t" \
  "s_nop 0\n\t" \
  "v_max_i32_dpp v208, v208, v208 quad_perm:[2,3,0,1] row_mask:0xf bank_mask:0xf\n\t" \
  "s_nop 1\n\t" \
  "v_max_i32_dpp v208, v208, v208 row_half_mirror row_mask:0xf bank_mask:0xf\n\t" \
  "v_add_u32 v208, 1056, v208\n\t" \
  "v_med3_i32 v208, v208, 0, v246\n\t" \
  "v_mul_u32_u24 v208, s52, v208\n\t" \
  "v_lshrrev_b32 v208, 16, v208\n\t" \
  "s_nop 0\n\t" \
  "v_readlane_b32 s88, v208, 0\n\t" \
  "s_add_i32 s88, s88, s51\n\t" \
  "s_add_i32 s88, s88, s101\n\t" \
  "23:\n\t" \
  "s_mov_b32 exec_lo, -1\n\t" \
  "v_min_u32 v209, 1520, v209\n\t" \
  "s_nop 1\n\t" \
  "v_max_u32_dpp v209, v209, v209 quad_perm:[1,0,3,2] row_mask:0xf bank_mask:0xf\n\t" \
  "s_nop 1\n\t" \
  "v_max_u32_dpp v209, v209, v209 quad_perm:[2,3,0,1] row_mask:0xf bank_mask:0xf\n\t" \
  "s_nop 1\n\t" \
  "v_max_u32_dpp v209, v209, v209 row_half_mirror row_mask:0xf bank_mask:0xf\n\t" \
  "s_nop 1\n\t" \
  "v_max_u32_dpp v209, v209, v209 row_mirror row_mask:0xf bank_mask:0xf\n\t" \
  "s_nop 1\n\t" \
  "v_readlane_b32 s89, v209, 0\n\t" \
  "v_readlane_b32 s90, v209, 16\n\t" \
  "s_max_u32 s57, s89, s90\n\t" \
  "s_add_i32 s57, s57, 260\n\t" \
  "s_sub_i32 s81, 8, s44\n\t" \
  "s_max_i32 s81, s81, 0\n\t" \
  "s_mul_i32 s81, s81, 20\n\t" \
  "s_add_i32 s57, s57, s81\n\t" \
  "s_and_b32 s81, s91, 255\n\t" \
  "s_add_i32 s57, s57, s81\n\t" \
  "s_and_b32 s81, s44, 3\n\t" \
  "s_lshl_b32 s81, s81, 16\n\t" \
  "s_mov_b64 exec, 0xff\n\t" \
  "1:\n\t" \
  "s_memtime vcc\n\t" \
  "s_waitcnt lgkmcnt(0)\n\t" \
  "s_sub_i32 s79, s88, vcc_lo\n\t" \
  "s_cmp_gt_i32 s79, 128\n\t" \
  "s_cbranch_scc1 1b\n\t" \
  "2:\n\t" \
  "s_sub_i32 s79, s79, 33\n\t" \
  "s_cmp_gt_i32 s79, 56\n\t" \
  "s_cbranch_scc1 2b\n\t" \
  "buffer_load_dword v208, v234, %[rh], s81 offen sc1\n\t" \
  "s_mov_b64 exec, -1\n\t" \
  "v_accvgpr_read_b32 v0, a0\n\t" \
  "v_accvgpr_read_b32 v1, a8\n\t" \
  "v_accvgpr_read_b32 v2, a2\n\t" \
  "v_accvgpr_read_b32 v3, a10\n\t" \
  "v_accvgpr_read_b32 v4, a4\n\t" \
  "v_accvgpr_read_b32 v5, a12\n\t" \
  "v_accvgpr_read_b32 v6, a6\n\t" \
  "v_accvgpr_read_b32 v7, a14\n\t" \
  "v_accvgpr_read_b32 v8, a1\n\t" \
  "v_accvgpr_read_b32 v9, a9\n\t" \
  "v_accvgpr_read_b32 v10, a3\n\t" \
  "v_accvgpr_read_b32 v11, a11\n\t" \
  "v_accvgpr_read_b32 v12, a5\n\t" \
  "v_accvgpr_read_b32 v13, a13\n\t" \
  "v_accvgpr_read_b32 v14, a7\n\t" \
  "v_accvgpr_read_b32 v15, a15\n\t" \
  "v_accvgpr_read_b32 v16, a32\n\t" \
  "v_accvgpr_read_b32 v17, a40\n\t" \
  "v_accvgpr_read_b32 v18, a34\n\t" \
  "v_accvgpr_read_b32 v19, a42\n\t" \
  "v_accvgpr_read_b32 v20, a36\n\t" \
  "v_accvgpr_read_b32 v21, a44\n\t" \
  "v_accvgpr_read_b32 v22, a38\n\t" \
  "v_accvgpr_read_b32 v23, a46\n\t" \
  "v_accvgpr_read_b32 v24, a33\n\t" \
  "v_accvgpr_read_b32 v25, a41\n\t" \
  "v_accvgpr_read_b32 v26, a35\n\t" \
  "v_accvgpr_read_b32 v27, a43\n\t" \
  "v_accvgpr_read_b32 v28, a37\n\t" \
  "v_accvgpr_read_b32 v29, a45\n\t" \
  "v_accvgpr_read_b32 v30, a39\n\t" \
  "v_accvgpr_read_b32 v31, a47\n\t" \
  "v_accvgpr_read_b32 v32, a16\n\t" \
  "v_accvgpr_read_b32 v33, a24\n\t" \
  "v_accvgpr_read_b32 v34, a18\n\t" \
  "v_accvgpr_read_b32 v35, a26\n\t" \
  "v_accvgpr_read_b32 v36, a20\n\t" \
  "v_accvgpr_read_b32 v37, a28\n\t" \
  "v_accvgpr_read_b32 v38, a22\n\t" \
  "v_accvgpr_read_b32 v39, a30\n\t" \
  "v_accvgpr_read_b32 v40, a17\n\t" \
  "v_accvgpr_read_b32 v41, a25\n\t" \
  "v_accvgpr_read_b32 v42, a19\n\t" \
  "v_accvgpr_read_b32 v43, a27\n\t" \
  "v_accvgpr_read_b32 v44, a21\n\t" \
  "v_accvgpr_read_b32 v45, a29\n\t" \
  "v_accvgpr_read_b32 v46, a23\n\t" \
  "v_accvgpr_read_b32 v47, a31\n\t" \
  "v_accvgpr_read_b32 v48, a48\n\t" \
  "v_accvgpr_read_b32 v49, a56\n\t" \
  "v_accvgpr_read_b32 v50, a50\n\t" \
  "v_accvgpr_read_b32 v51, a58\n\t" \
  "v_accvgpr_read_b32 v52, a52\n\t" \
  "v_accvgpr_read_b32 v53, a60\n\t" \
  "v_accvgpr_read_b32 v54, a54\n\t" \
  "v_accvgpr_read_b32 v55, a62\n\t" \
  "v_accvgpr_read_b32 v56, a49\n\t" \
  "v_accvgpr_read_b32 v57, a57\n\t" \
  "v_accvgpr_read_b32 v58, a51\n\t" \
  "v_accvgpr_read_b32 v59, a59\n\t" \
  "v_accvgpr_read_b32 v60, a53\n\t" \
  "v_accvgpr_read_b32 v61, a61\n\t" \
  "v_accvgpr_read_b32 v62, a55\n\t" \
  "v_accvgpr_read_b32 v63, a63\n\t" \
  "s_mov_b64 exec, 0xff\n\t" \
  "s_add_i32 s89, s57, s53\n\t" \
  "s_mov_b32 s84, 600\n\t" \
  "s_mov_b32 s90, 0\n\t" \
  "s_mov_b32 s93, 0\n\t" \
  "13:\n\t" \
  "s_waitcnt vmcnt(0)\n\t" \
  "s_memtime s[86:87]\n\t" \
  "v_cmp_ne_u32_sdwa vcc, v208, s44 src0_sel:BYTE_0 src1_sel:DWORD\n\t" \
  "v_subrev_u32 v208, s50, v208\n\t" \
  "v_ashrrev_i32 v208, 8, v208\n\t" \
  "s_nop 1\n\t" \
  "v_max_i32_dpp v208, v208, v208 quad_perm:[1,0,3,2] row_mask:0xf bank_mask:0xf\n\t" \
  "s_nop 1\n\t" \
  "v_max_i32_dpp v208, v208, v208 quad_perm:[2,3,0,1] row_mask:0xf bank_mask:0xf\n\t" \
  "s_cbranch_vccnz 14f\n\t" \
  "15:\n\t" \
  "s_nop 0\n\t" \
  "v_max_i32_dpp v208, v208, v208 row_half_mirror row_mask:0xf bank_mask:0xf\n\t" \
  "v_add_u32 v209, 255, v208\n\t" \
  "v_med3_i32 v209, v209, 0, v246\n\t" \
  "v_mul_u32_u24 v209, s52, v209\n\t" \
  "v_readlane_b32 s82, v208, 0\n\t" \
  "v_lshrrev_b32 v209, 16, v209\n\t" \
  "s_waitcnt lgkmcnt(0)\n\t" \
  "s_sub_i32 s79, s51, s86\n\t" \
  "v_readlane_b32 s83, v209, 0\n\t" \
  "s_mov_b64 exec, -1\n\t" \
  "s_add_i32 s79, s79, s83\n\t" \
  "s_add_i32 s79, s79, s90\n\t" \
  "s_add_i32 s79, s79, s101\n\t" \
  "s_sub_i32 s85, s84, s79\n\t" \
  "s_max_i32 s85, s85, 0\n\t" \
  "s_add_i32 s85, s85, s93\n\t" \
  "s_add_i32 s79, s79, -582\n\t" \
  "s_mul_hi_u32 s85, s85, s46\n\t" \
  "s_lshl1_add_u32 s85, s85, s82\n\t" \
  "s_add_i32 s85, s85, s89\n\t" \
  "s_lshl_b32 s85, s85, 8\n\t" \
  "s_or_b32 s85, s85, s44\n\t" \
  "v_mov_b32 v209, s85\n\t" \
  "buffer_store_dword v209, v235, %[rh], s81 offen sc1\n\t" \
  "s_add_i32 s85, s53, s82\n\t" \
  "v_mov_b32 v208, s85\n\t" \
  "ds_write_b32 v245, v208\n\t" \
  "3:\n\t" \
  "s_sub_i32 s79, s79, 33\n\t" \
  "s_cmp_gt_i32 s79, 56\n\t" \
  "s_cbranch_scc1 3b\n\t" \
  "buffer_load_dwordx4 v[192:195], v226, %[rh], s49 offen offset:0 sc1\n\t" \
  "s_nop 6\n\t" \
  "buffer_load_dwordx4 v[196:199], v226, %[rh], s49 offen offset:2048 sc1\n\t" \
  "s_nop 6\n\t" \
  "buffer_load_dwordx4 v[200:203], v226, %[rh], s80 offen offset:0 sc1\n\t" \
  "s_nop 6\n\t" \
  "buffer_load_dwordx4 v[204:207], v226, %[rh], s80 offen offset:2048 sc1\n\t" \
  "s_nop 6\n\t" \
  "buffer_load_dwordx4 v[64:67], v225, %[ru], s48 offen offset:0 sc1\n\t" \
  "s_nop 6\n\t" \
  "buffer_load_dwordx4 v[72:75], v225, %[ru], s76 offen offset:0 sc1\n\t" \
  "s_nop 6\n\t" \
  "buffer_load_dwordx4 v[68:71], v225, %[ru], s77 offen offset:0 sc1\n\t" \
  "s_nop 6\n\t" \
  "buffer_load_dwordx4 v[76:79], v225, %[ru], s78 offen offset:0 sc1\n\t" \
  "s_add_i32 s79, s79, 184\n\t" \
  "4:\n\t" \
  "s_sub_i32 s79, s79, 33\n\t" \
  "s_cmp_gt_i32 s79, 56\n\t" \
  "s_cbranch_scc1 4b\n\t" \
  "buffer_load_dwordx4 v[212:215], v228, %[rh], s54 offen offset:0 sc1\n\t" \
  "s_nop 6\n\t" \
  "buffer_load_dwordx4 v[208:211], v227, %[rh], s54 offen offset:0 sc1\n\t" \
  "10:\n\t" \
  "s_waitcnt vmcnt(9)\n\t" \
  "v_smfmac_f32_16x16x64_bf16 v[144:147], v[192:195], v[0:7], v229\n\t" \
  "v_smfmac_f32_16x16x64_bf16 v[148:151], v[192:195], v[8:15], v229\n\t" \
  "s_waitcnt vmcnt(8)\n\t" \
  "v_smfmac_f32_16x16x64_bf16 v[152:155], v[196:199], v[16:23], v229\n\t" \
  "v_cvt_pk_bf16_f32 v176, v144, v145\n\t" \
  "v_cvt_pk_bf16_f32 v177, v146, v147\n\t" \
  "v_smfmac_f32_16x16x64_bf16 v[156:159], v[196:199], v[24:31], v229\n\t" \
  "v_cvt_pk_bf16_f32 v184, v148, v149\n\t" \
  "v_cvt_pk_bf16_f32 v185, v150, v151\n\t" \
  "s_waitcnt vmcnt(7)\n\t" \
  "v_smfmac_f32_16x16x64_bf16 v[160:163], v[200:203], v[32:39], v229\n\t" \
  "v_cvt_pk_bf16_f32 v178, v152, v153\n\t" \
  "v_cvt_pk_bf16_f32 v179, v154, v155\n\t" \
  "v_smfmac_f32_16x16x64_bf16 v[164:167], v[200:203], v[40:47], v229\n\t" \
  "v_cvt_pk_bf16_f32 v186, v156, v157\n\t" \
  "v_cvt_pk_bf16_f32 v187, v158, v159\n\t" \
  "s_waitcnt vmcnt(6)\n\t" \
  "v_smfmac_f32_16x16x64_bf16 v[168:171], v[204:207], v[48:55], v229\n\t" \
  "v_cvt_pk_bf16_f32 v180, v160, v161\n\t" \
  "v_cvt_pk_bf16_f32 v181, v162, v163\n\t" \
  "v_smfmac_f32_16x16x64_bf16 v[172:175], v[204:207], v[56:63], v229\n\t" \
  "v_cvt_pk_bf16_f32 v188, v164, v165\n\t" \
  "v_cvt_pk_bf16_f32 v189, v166, v167\n\t" \
  "s_waitcnt vmcnt(5)\n\t" \
  "s_waitcnt vmcnt(4)\n\t" \
  "v_swap_b32 v65, v72\n\t" \
  "v_swap_b32 v67, v74\n\t" \
  "s_nop 1\n\t" \
  "v_cvt_pk_bf16_f32 v182, v168, v169\n\t" \
  "v_cvt_pk_bf16_f32 v183, v170, v171\n\t" \
  "v_cvt_pk_bf16_f32 v190, v172, v173\n\t" \
  "v_cvt_pk_bf16_f32 v191, v174, v175\n\t" \
  "s_waitcnt vmcnt(1)\n\t" \
  "v_smfmac_f32_16x16x64_bf16 v[216:219], v[212:215], v[176:183], v230\n\t" \
  "v_smfmac_f32_16x16x64_bf16 v[220:223], v[212:215], v[184:191], v230\n\t" \
  "v_swap_b32 v69, v76\n\t" \
  "v_swap_b32 v71, v78\n\t" \
  "s_waitcnt vmcnt(0)\n\t" \
  "s_nop 0\n\t" \
  "v_smfmac_f32_16x16x64_bf16 v[216:219], v[208:211], v[64:71], v229\n\t" \
  "v_smfmac_f32_16x16x64_bf16 v[220:223], v[208:211], v[72:79], v229\n\t" \
  "s_nop 3\n\t" \
  "ds_write2st64_b32 v231, v216, v217 offset0:0 offset1:1\n\t" \
  "ds_write2st64_b32 v231, v218, v219 offset0:2 offset1:3\n\t" \
  "v_cmp_u_f32 vcc, v216, v217\n\t" \
  "v_cmp_u_f32 s[82:83], v218, v219\n\t" \
  "ds_write2st64_b32 v231, v220, v221 offset0:16 offset1:17\n\t" \
  "ds_write2st64_b32 v231, v222, v223 offset0:18 offset1:19\n\t" \
  "v_cmp_u_f32 s[84:85], v220, v222\n\t" \
  "s_or_b64 vcc, vcc, s[82:83]\n\t" \
  "s_or_b64 vcc, vcc, s[84:85]\n\t" \
  "s_cbranch_scc1 8f\n\t" \
  "6:\n\t" \
  "s_waitcnt lgkmcnt(0)\n\t" \
  "s_barrier\n\t" \
  "ds_read_b64 v[136:137], v232 offset:0\n\t" \
  "ds_read_b64 v[138:139], v232 offset:1024\n\t" \
  "ds_read_b64 v[140:141], v232 offset:2048\n\t" \
  "ds_read_b64 v[142:143], v232 offset:3072\n\t" \
  "s_add_i32 s79, s44, 1\n\t" \
  "s_and_b32 s79, s79, 7\n\t" \
  "s_lshl_b32 s79, s79, 15\n\t" \
  "s_waitcnt lgkmcnt(2)\n\t" \
  "v_add_f32 v136, v136, v138\n\t" \
  "v_add_f32 v137, v137, v139\n\t" \
  "s_waitcnt lgkmcnt(0)\n\t" \
  "v_add_f32 v140, v140, v142\n\t" \
  "v_add_f32 v141, v141, v143\n\t" \
  "v_add_f32 v136, v136, v140\n\t" \
  "v_add_f32 v137, v137, v141\n\t" \
  "s_nop 0\n\t" \
  "v_add_f32_dpp v136, v136, v136 quad_perm:[1,0,3,2] row_mask:0xf bank_mask:0xf\n\t" \
  "v_add_f32_dpp v137, v137, v137 quad_perm:[1,0,3,2] row_mask:0xf bank_mask:0xf\n\t" \
  "v_cvt_pk_bf16_f32 v136, v136, v137\n\t" \
  "buffer_store_dword v136, v233, %[rh], s79 offen sc1\n\t" \
  "s_memtime s[56:57]\n\t" \
  "s_cmp_eq_u32 s96, 0x7f000000\n\t" \
  "s_cbranch_scc1 40f\n\t" \
  "s_nop 8\n\t" \
  "39:\n\t" \
  "s_xor_b32 s81, s96, 0x0\n\t" \
  "buffer_load_dwordx4 a[0:3], v224, %[ru], s81 offen offset:0 sc1\n\t" \
  "ds_read_b128 v[208:211], v247\n\t" \
  "v_mov_b64 v[212:213], -1\n\t" \
  "v_mov_b64 v[214:215], -1\n\t" \
  "s_add_i32 s79, s44, -1\n\t" \
  "s_and_b32 s79, s79, 7\n\t" \
  "s_lshl_b32 s79, s79, 15\n\t" \
  "buffer_store_dwordx4 v[212:215], v243, %[rh], s79 offen sc1\n\t" \
  "s_waitcnt lgkmcnt(0)\n\t" \
  "s_sub_i32 s76, s56, s51\n\t" \
  "s_mul_hi_u32 s76, s76, s46\n\t" \
  "s_lshl1_add_u32 s76, s76, s53\n\t" \
  "v_sub_u32 v208, s76, v208\n\t" \
  "v_sub_u32 v209, s76, v209\n\t" \
  "v_sub_u32 v210, s76, v210\n\t" \
  "v_sub_u32 v211, s76, v211\n\t" \
  "v_min3_u32 v208, v208, v209, v210\n\t" \
  "buffer_load_dwordx4 a[4:7], v224, %[ru], s81 offen offset:1024 sc1\n\t" \
  "v_min_u32 v208, v208, v211\n\t" \
  "s_nop 0\n\t" \
  "v_readfirstlane_b32 s76, v208\n\t" \
  "s_bitcmp1_b32 s58, 3\n\t" \
  "s_cselect_b32 s76, s45, s76\n\t" \
  "s_min_u32 s77, s45, s47\n\t" \
  "s_min_u32 s77, s77, s76\n\t" \
  "s_mov_b32 s47, s45\n\t" \
  "s_mov_b32 s45, s76\n\t" \
  "v_mov_b32 v208, s77\n\t" \
  "s_and_b32 s58, s58, 4\n\t" \
  "buffer_store_dword v208, v244, %[rh], s55 offen sc1\n\t" \
  "v_mov_b32 v209, s44\n\t" \
  "buffer_store_dword v209, v238, %[ru], 0 offen sc1\n\t" \
  "s_and_b32 s97, s44, 3\n\t" \
  "s_lshl_b32 s97, s97, 17\n\t" \
  "s_add_i32 s97, s97, s43\n\t" \
  "s_add_i32 s82, s56, 260\n\t" \
  "60:\n\t" \
  "s_memtime vcc\n\t" \
  "s_waitcnt lgkmcnt(0)\n\t" \
  "s_sub_i32 s83, s82, vcc_lo\n\t" \
  "s_cmp_gt_i32 s83, 30\n\t" \
  "s_cbranch_scc1 60b\n\t" \
  "s_xor_b32 s81, s96, 0x20000\n\t" \
  "buffer_load_dwordx4 a[8:11], v224, %[ru], s81 offen offset:0 sc1\n\t" \
  "s_andn2_b32 s84, s48, 0xfff\n\t" \
  "s_cmp_lt_i32 s44, 60\n\t" \
  "s_cselect_b32 s84, s84, 0x7f000000\n\t" \
  "s_add_i32 s79, s44, 2\n\t" \
  "s_and_b32 s48, s79, 3\n\t" \
  "s_lshl_b32 s76, s48, 2\n\t" \
  "s_lshl_b32 s48, s48, 20\n\t" \
  "s_add_i32 s48, s48, s40\n\t" \
  "s_lshr_b32 s76, %[um], s76\n\t" \
  "s_and_b32 s76, s76, 7\n\t" \
  "s_lshl_b32 s76, s76, 12\n\t" \
  "s_xor_b32 s48, s48, s76\n\t" \
  "s_xor_b32 s76, s48, 0x200\n\t" \
  "s_xor_b32 s77, s48, 0x400\n\t" \
  "s_xor_b32 s78, s48, 0x600\n\t" \
  "s_and_b32 s79, s79, 3\n\t" \
  "s_lshl_b32 s54, s79, 16\n\t" \
  "s_add_i32 s82, s56, 390\n\t" \
  "61:\n\t" \
  "s_memtime vcc\n\t" \
  "s_waitcnt lgkmcnt(0)\n\t" \
  "s_sub_i32 s83, s82, vcc_lo\n\t" \
  "s_cmp_gt_i32 s83, 30\n\t" \
  "s_cbranch_scc1 61b\n\t" \
  "buffer_load_dwordx4 a[12:15], v224, %[ru], s81 offen offset:1024 sc1\n\t" \
  "s_add_i32 s49, s54, s41\n\t" \
  "s_xor_b32 s80, s49, 0x400\n\t" \
  "buffer_store_dword v212, v235, %[rh], s54 offen sc1\n\t" \
  "ds_read_b128 v[80:83], v240 offset:0\n\t" \
  "ds_read_b128 v[84:87], v240 offset:4096\n\t" \
  "ds_read_b128 v[88:91], v240 offset:8192\n\t" \
  "ds_read_b128 v[92:95], v240 offset:12288\n\t" \
  "s_add_i32 s85, s44, -2\n\t" \
  "s_lshl_b32 s86, s85, 17\n\t" \
  "s_cmp_lt_i32 s85, 0\n\t" \
  "s_cselect_b32 s86, 0x7f000000, s86\n\t" \
  "s_waitcnt lgkmcnt(0)\n\t" \
  "v_add_f32 v80, v80, v84\n\t" \
  "v_add_f32 v81, v81, v85\n\t" \
  "v_add_f32 v82, v82, v86\n\t" \
  "v_add_f32 v83, v83, v87\n\t" \
  "v_add_f32 v88, v88, v92\n\t" \
  "s_add_i32 s82, s56, 520\n\t" \
  "62:\n\t" \
  "s_memtime vcc\n\t" \
  "s_waitcnt lgkmcnt(0)\n\t" \
  "s_sub_i32 s83, s82, vcc_lo\n\t" \
  "s_cmp_gt_i32 s83, 30\n\t" \
  "s_cbranch_scc1 62b\n\t" \
  "s_xor_b32 s81, s96, 0x40000\n\t" \
  "buffer_load_dwordx4 a[16:19], v224, %[ru], s81 offen offset:0 sc1\n\t" \
  "v_add_f32 v89, v89, v93\n\t" \
  "v_add_f32 v90, v90, v94\n\t" \
  "v_add_f32 v91, v91, v95\n\t" \
  "v_add_f32 v80, v80, v88\n\t" \
  "v_add_f32 v81, v81, v89\n\t" \
  "v_add_f32 v82, v82, v90\n\t" \
  "v_add_f32 v83, v83, v91\n\t" \
  "v_cvt_pk_bf16_f32 v80, v80, v81\n\t" \
  "v_cvt_pk_bf16_f32 v81, v82, v83\n\t" \
  "buffer_store_dwordx2 v[80:81], v241, %[ro], s86 offen sc1\n\t" \
  "s_add_i32 s82, s56, 650\n\t" \
  "63:\n\t" \
  "s_memtime vcc\n\t" \
  "s_waitcnt lgkmcnt(0)\n\t" \
  "s_sub_i32 s83, s82, vcc_lo\n\t" \
  "s_cmp_gt_i32 s83, 30\n\t" \
  "s_cbranch_scc1 63b\n\t" \
  "buffer_load_dwordx4 a[20:23], v224, %[ru], s81 offen offset:1024 sc1\n\t" \
  "s_add_i32 s82, s56, 780\n\t" \
  "64:\n\t" \
  "s_memtime vcc\n\t" \
  "s_waitcnt lgkmcnt(0)\n\t" \
  "s_sub_i32 s83, s82, vcc_lo\n\t" \
  "s_cmp_gt_i32 s83, 30\n\t" \
  "s_cbranch_scc1 64b\n\t" \
  "s_xor_b32 s81, s96, 0x60000\n\t" \
  "buffer_load_dwordx4 a[24:27], v224, %[ru], s81 offen offset:0 sc1\n\t" \
  "s_add_i32 s82, s56, 910\n\t" \
  "65:\n\t" \
  "s_memtime vcc\n\t" \
  "s_waitcnt lgkmcnt(0)\n\t" \
  "s_sub_i32 s83, s82, vcc_lo\n\t" \
  "s_cmp_gt_i32 s83, 30\n\t" \
  "s_cbranch_scc1 65b\n\t" \
  "buffer_load_dwordx4 a[28:31], v224, %[ru], s81 offen offset:1024 sc1\n\t" \
  "buffer_store_dwordx4 v[212:215], v224, %[ru], s84 offen offset:0 sc1\n\t" \
  "s_add_i32 s82, s56, 1040\n\t" \
  "66:\n\t" \
  "s_memtime vcc\n\t" \
  "s_waitcnt lgkmcnt(0)\n\t" \
  "s_sub_i32 s83, s82, vcc_lo\n\t" \
  "s_cmp_gt_i32 s83, 30\n\t" \
  "s_cbranch_scc1 66b\n\t" \
  "s_xor_b32 s81, s96, 0x80000\n\t" \
  "buffer_load_dwordx4 a[32:35], v224, %[ru], s81 offen offset:0 sc1\n\t" \
  "s_add_i32 s82, s56, 1170\n\t" \
  "67:\n\t" \
  "s_memtime vcc\n\t" \
  "s_waitcnt lgkmcnt(0)\n\t" \
  "s_sub_i32 s83, s82, vcc_lo\n\t" \
  "s_cmp_gt_i32 s83, 30\n\t" \
  "s_cbranch_scc1 67b\n\t" \
  "buffer_load_dwordx4 a[36:39], v224, %[ru], s81 offen offset:1024 sc1\n\t" \
  "buffer_store_dwordx4 v[212:215], v224, %[ru], s84 offen offset:1024 sc1\n\t" \
  "s_add_i32 s82, s56, 1300\n\t" \
  "68:\n\t" \
  "s_memtime vcc\n\t" \
  "s_waitcnt lgkmcnt(0)\n\t" \
  "s_sub_i32 s83, s82, vcc_lo\n\t" \
  "s_cmp_gt_i32 s83, 30\n\t" \
  "s_cbranch_scc1 68b\n\t" \
  "s_xor_b32 s81, s96, 0xa0000\n\t" \
  "buffer_load_dwordx4 a[40:43], v224, %[ru], s81 offen offset:0 sc1\n\t" \
  "s_add_i32 s82, s56, 1430\n\t" \
  "69:\n\t" \
  "s_memtime vcc\n\t" \
  "s_waitcnt lgkmcnt(0)\n\t" \
  "s_sub_i32 s83, s82, vcc_lo\n\t" \
  "s_cmp_gt_i32 s83, 30\n\t" \
  "s_cbranch_scc1 69b\n\t" \
  "buffer_load_dwordx4 a[44:47], v224, %[ru], s81 offen offset:1024 sc1\n\t" \
  "buffer_store_dwordx4 v[212:215], v224, %[ru], s84 offen offset:2048 sc1\n\t" \
  "s_add_i32 s82, s56, 1560\n\t" \
  "70:\n\t" \
  "s_memtime vcc\n\t" \
  "s_waitcnt lgkmcnt(0)\n\t" \
  "s_sub_i32 s83, s82, vcc_lo\n\t" \
  "s_cmp_gt_i32 s83, 30\n\t" \
  "s_cbranch_scc1 70b\n\t" \
  "s_xor_b32 s81, s96, 0xc0000\n\t" \
  "buffer_load_dwordx4 a[48:51], v224, %[ru], s81 offen offset:0 sc1\n\t" \
  "s_and_b32 s95, s44, 3\n\t" \
  "s_lshl_b32 s95, s95, 16\n\t" \
  "s_mov_b64 exec, 0xff\n\t" \
  "buffer_load_dword v211, v248, %[rh], s95 offen offset:0 sc1\n\t" \
  "s_mov_b64 exec, -1\n\t" \
  "s_add_i32 s82, s56, 1690\n\t" \
  "71:\n\t" \
  "s_memtime vcc\n\t" \
  "s_waitcnt lgkmcnt(0)\n\t" \
  "s_sub_i32 s83, s82, vcc_lo\n\t" \
  "s_cmp_gt_i32 s83, 30\n\t" \
  "s_cbranch_scc1 71b\n\t" \
  "buffer_load_dwordx4 a[52:55], v224, %[ru], s81 offen offset:1024 sc1\n\t" \
  "buffer_store_dwordx4 v[212:215], v224, %[ru], s84 offen offset:3072 sc1\n\t" \
  "s_add_i32 s82, s56, 1820\n\t" \
  "72:\n\t" \
  "s_memtime vcc\n\t" \
  "s_waitcnt lgkmcnt(0)\n\t" \
  "s_sub_i32 s83, s82, vcc_lo\n\t" \
  "s_cmp_gt_i32 s83, 30\n\t" \
  "s_cbranch_scc1 72b\n\t" \
  "s_xor_b32 s81, s96, 0xe0000\n\t" \
  "buffer_load_dwordx4 a[56:59], v224, %[ru], s81 offen offset:0 sc1\n\t" \
  "s_add_i32 s82, s56, 1950\n\t" \
  "73:\n\t" \
  "s_memtime vcc\n\t" \
  "s_waitcnt lgkmcnt(0)\n\t" \
  "s_sub_i32 s83, s82, vcc_lo\n\t" \
  "s_cmp_gt_i32 s83, 30\n\t" \
  "s_cbranch_scc1 73b\n\t" \
  "buffer_load_dwordx4 a[60:63], v224, %[ru], s81 offen offset:1024 sc1\n\t" \
  "s_cmp_lt_i32 s44, 0\n\t" \
  "s_cbranch_scc1 51f\n\t" \
  "s_mov_b32 s79, s56\n\t" \
  "s_memtime vcc\n\t" \
  "s_memrealtime s[82:83]\n\t" \
  "s_nop 4\n\t" \
  "s_memrealtime s[84:85]\n\t" \
  "s_nop 4\n\t" \
  "s_memrealtime s[56:57]\n\t" \
  "s_nop 4\n\t" \
  "s_memrealtime s[86:87]\n\t" \
  "s_nop 4\n\t" \
  "s_memrealtime s[88:89]\n\t" \
  "s_nop 4\n\t" \
  "s_memrealtime s[90:91]\n\t" \
  "s_mov_b32 s92, 0\n\t" \
  "s_waitcnt lgkmcnt(0)\n\t" \
  "s_cmp_eq_u32 s82, s84\n\t" \
  "s_addc_u32 s92, s92, 0\n\t" \
  "s_cmp_eq_u32 s82, s56\n\t" \
  "s_addc_u32 s92, s92, 0\n\t" \
  "s_cmp_eq_u32 s82, s86\n\t" \
  "s_addc_u32 s92, s92, 0\n\t" \
  "s_cmp_eq_u32 s82, s88\n\t" \
  "s_addc_u32 s92, s92, 0\n\t" \
  "s_cmp_eq_u32 s82, s90\n\t" \
  "s_addc_u32 s92, s92, 0\n\t" \
  "s_mul_i32 s92, s92, 24\n\t" \
  "s_add_i32 s92, s92, vcc_lo\n\t" \
  "s_lshl4_add_u32 s93, s82, 64\n\t" \
  "s_mov_b32 s56, s79\n\t" \
  "s_sub_i32 s82, s92, s51\n\t" \
  "s_sub_i32 s83, s93, s53\n\t" \
  "s_cmp_eq_u32 s82, 0\n\t" \
  "s_cbranch_scc1 41f\n\t" \
  "v_cvt_f32_u32 v208, s82\n\t" \
  "v_cvt_f32_u32 v209, s83\n\t" \
  "v_rcp_f32 v208, v208\n\t" \
  "s_mov_b32 s51, s92\n\t" \
  "v_mul_f32 v208, v208, v209\n\t" \
  "v_max_f32 v208, 0x3f028f5c, v208\n\t" \
  "v_min_f32 v208, 0x3ffeb852, v208\n\t" \
  "v_rcp_f32 v209, v208\n\t" \
  "v_mul_f32 v208, 0x4f000000, v208\n\t" \
  "v_mul_f32 v209, 0x47800000, v209\n\t" \
  "v_cvt_u32_f32 v208, v208\n\t" \
  "v_cvt_u32_f32 v209, v209\n\t" \
  "s_mov_b32 s53, s93\n\t" \
  "v_readfirstlane_b32 s46, v208\n\t" \
  "v_readfirstlane_b32 s52, v209\n\t" \
  "41:\n\t" \
  "s_lshl_b32 s50, s53, 8\n\t" \
  "s_add_i32 s85, s44, 1\n\t" \
  "s_and_b32 s94, s85, 3\n\t" \
  "s_lshl_b32 s94, s94, 16\n\t" \
  "s_add_i32 s82, s56, 3100\n\t" \
  "74:\n\t" \
  "s_memtime vcc\n\t" \
  "s_waitcnt lgkmcnt(0)\n\t" \
  "s_sub_i32 s83, s82, vcc_lo\n\t" \
  "s_cmp_gt_i32 s83, 30\n\t" \
  "s_cbranch_scc1 74b\n\t" \
  "s_waitcnt vmcnt(4)\n\t" \
  "s_mov_b64 exec, 0xff\n\t" \
  "47:\n\t" \
  "v_cmp_ne_u32 vcc, s44, v211\n\t" \
  "s_mov_b64 exec, -1\n\t" \
  "s_cbranch_vccnz 45f\n\t" \
  "46:\n\t" \
  "s_barrier\n\t" \
  "s_xor_b32 s81, s97, 0x0\n\t" \
  "buffer_load_dwordx4 v[80:83], v224, %[rh], s81 offen offset:0 sc1\n\t" \
  "s_xor_b32 s81, s97, 0x800\n\t" \
  "buffer_load_dwordx4 v[84:87], v224, %[rh], s81 offen offset:0 sc1\n\t" \
  "s_xor_b32 s81, s97, 0x0\n\t" \
  "buffer_load_dwordx4 v[88:91], v224, %[rh], s81 offen offset:1024 sc1\n\t" \
  "s_xor_b32 s81, s97, 0x800\n\t" \
  "buffer_load_dwordx4 v[92:95], v224, %[rh], s81 offen offset:1024 sc1\n\t" \
  "s_xor_b32 s81, s97, 0x4000\n\t" \
  "buffer_load_dwordx4 v[96:99], v224, %[rh], s81 offen offset:0 sc1\n\t" \
  "s_xor_b32 s81, s97, 0x4800\n\t" \
  "buffer_load_dwordx4 v[100:103], v224, %[rh], s81 offen offset:0 sc1\n\t" \
  "s_xor_b32 s81, s97, 0x4000\n\t" \
  "buffer_load_dwordx4 v[104:107], v224, %[rh], s81 offen offset:1024 sc1\n\t" \
  "s_xor_b32 s81, s97, 0x4800\n\t" \
  "buffer_load_dwordx4 v[108:111], v224, %[rh], s81 offen offset:1024 sc1\n\t" \
  "s_waitcnt vmcnt(6)\n\t" \
  "v_swap_b32 v81, v84\n\t" \
  "v_swap_b32 v83, v86\n\t" \
  "s_nop 1\n\t" \
  "v_mfma_f32_16x16x32_bf16 v[192:195], v[0:3], v[80:83], 0\n\t" \
  "v_mfma_f32_16x16x32_bf16 v[200:203], v[8:11], v[80:83], 0\n\t" \
  "s_xor_b32 s81, s97, 0x1000\n\t" \
  "buffer_load_dwordx4 v[112:115], v224, %[rh], s81 offen offset:0 sc1\n\t" \
  "v_mfma_f32_16x16x32_bf16 v[196:199], v[0:3], v[84:87], 0\n\t" \
  "v_mfma_f32_16x16x32_bf16 v[204:207], v[8:11], v[84:87], 0\n\t" \
  "s_xor_b32 s81, s97, 0x1800\n\t" \
  "buffer_load_dwordx4 v[116:119], v224, %[rh], s81 offen offset:0 sc1\n\t" \
  "s_waitcnt vmcnt(6)\n\t" \
  "v_swap_b32 v89, v92\n\t" \
  "v_swap_b32 v91, v94\n\t" \
  "s_nop 1\n\t" \
  "v_mfma_f32_16x16x32_bf16 v[196:199], v[4:7], v[88:91], v[196:199]\n\t" \
  "v_mfma_f32_16x16x32_bf16 v[204:207], v[12:15], v[88:91], v[204:207]\n\t" \
  "s_xor_b32 s81, s97, 0x1000\n\t" \
  "buffer_load_dwordx4 v[120:123], v224, %[rh], s81 offen offset:1024 sc1\n\t" \
  "s_xor_b32 s81, s97, 0x1800\n\t" \
  "buffer_load_dwordx4 v[124:127], v224, %[rh], s81 offen offset:1024 sc1\n\t" \
  "s_waitcnt vmcnt(6)\n\t" \
  "v_swap_b32 v97, v100\n\t" \
  "v_swap_b32 v99, v102\n\t" \
  "s_nop 1\n\t" \
  "v_mfma_f32_16x16x32_bf16 v[192:195], v[16:19], v[96:99], v[192:195]\n\t" \
  "v_mfma_f32_16x16x32_bf16 v[200:203], v[24:27], v[96:99], v[200:203]\n\t" \
  "s_xor_b32 s81, s97, 0x5000\n\t" \
  "buffer_load_dwordx4 v[128:131], v224, %[rh], s81 offen offset:0 sc1\n\t" \
  "v_mfma_f32_16x16x32_bf16 v[196:199], v[16:19], v[100:103], v[196:199]\n\t" \
  "v_mfma_f32_16x16x32_bf16 v[204:207], v[24:27], v[100:103], v[204:207]\n\t" \
  "s_xor_b32 s81, s97, 0x5800\n\t" \
  "buffer_load_dwordx4 v[132:135], v224, %[rh], s81 offen offset:0 sc1\n\t" \
  "s_waitcnt vmcnt(6)\n\t" \
  "v_swap_b32 v105, v108\n\t" \
  "v_swap_b32 v107, v110\n\t" \
  "s_nop 1\n\t" \
  "v_mfma_f32_16x16x32_bf16 v[196:199], v[20:23], v[104:107], v[196:199]\n\t" \
  "v_mfma_f32_16x16x32_bf16 v[204:207], v[28:31], v[104:107], v[204:207]\n\t" \
  "s_xor_b32 s81, s97, 0x5000\n\t" \
  "buffer_load_dwordx4 v[136:139], v224, %[rh], s81 offen offset:1024 sc1\n\t" \
  "s_xor_b32 s81, s97, 0x5800\n\t" \
  "buffer_load_dwordx4 v[140:143], v224, %[rh], s81 offen offset:1024 sc1\n\t" \
  "s_mov_b32 exec_hi, 0\n\t" \
  "buffer_load_dword v209, v237, %[rh], s55 offen offset:0 sc1\n\t" \
  "s_mov_b64 exec, 0xff\n\t" \
  "buffer_load_dword v210, v242, %[ru], 0 offen offset:0 sc1\n\t" \
  "buffer_load_dword v208, v236, %[rh], s94 offen offset:0 sc1\n\t" \
  "s_mov_b64 exec, 3\n\t" \
  "buffer_load_dword v211, v224, %[rh], %[mg] offen offset:0 sc1\n\t" \
  "s_mov_b64 exec, -1\n\t" \
  "v_permlane32_swap_b32 v92, v108\n\t" \
  "v_permlane32_swap_b32 v93, v109\n\t" \
  "v_permlane32_swap_b32 v94, v110\n\t" \
  "v_permlane32_swap_b32 v95, v111\n\t" \
  "s_nop 1\n\t" \
  "v_mfma_f32_16x16x32_bf16 v[192:195], v[64:67], v[92:95], v[192:195]\n\t" \
  "v_mfma_f32_16x16x32_bf16 v[200:203], v[72:75], v[92:95], v[200:203]\n\t" \
  "v_mfma_f32_16x16x32_bf16 v[196:199], v[64:67], v[108:111], v[196:199]\n\t" \
  "v_mfma_f32_16x16x32_bf16 v[204:207], v[72:75], v[108:111], v[204:207]\n\t" \
  "s_waitcnt vmcnt(10)\n\t" \
  "v_swap_b32 v113, v116\n\t" \
  "v_swap_b32 v115, v118\n\t" \
  "s_nop 1\n\t" \
  "v_mfma_f32_16x16x32_bf16 v[192:195], v[32:35], v[112:115], v[192:195]\n\t" \
  "v_mfma_f32_16x16x32_bf16 v[200:203], v[40:43], v[112:115], v[200:203]\n\t" \
  "v_mfma_f32_16x16x32_bf16 v[196:199], v[32:35], v[116:119], v[196:199]\n\t" \
  "v_mfma_f32_16x16x32_bf16 v[204:207], v[40:43], v[116:119], v[204:207]\n\t" \
  "s_waitcnt vmcnt(8)\n\t" \
  "v_swap_b32 v121, v124\n\t" \
  "v_swap_b32 v123, v126\n\t" \
  "s_nop 1\n\t" \
  "v_mfma_f32_16x16x32_bf16 v[196:199], v[36:39], v[120:123], v[196:199]\n\t" \
  "v_mfma_f32_16x16x32_bf16 v[204:207], v[44:47], v[120:123], v[204:207]\n\t" \
  "s_waitcnt vmcnt(6)\n\t" \
  "v_swap_b32 v129, v132\n\t" \
  "v_swap_b32 v131, v134\n\t" \
  "s_nop 1\n\t" \
  "v_mfma_f32_16x16x32_bf16 v[192:195], v[48:51], v[128:131], v[192:195]\n\t" \
  "v_mfma_f32_16x16x32_bf16 v[200:203], v[56:59], v[128:131], v[200:203]\n\t" \
  "v_mfma_f32_16x16x32_bf16 v[196:199], v[48:51], v[132:135], v[196:199]\n\t" \
  "v_mfma_f32_16x16x32_bf16 v[204:207], v[56:59], v[132:135], v[204:207]\n\t" \
  "s_waitcnt vmcnt(4)\n\t" \
  "v_swap_b32 v137, v140\n\t" \
  "v_swap_b32 v139, v142\n\t" \
  "s_nop 1\n\t" \
  "v_mfma_f32_16x16x32_bf16 v[196:199], v[52:55], v[136:139], v[196:199]\n\t" \
  "v_mfma_f32_16x16x32_bf16 v[204:207], v[60:63], v[136:139], v[204:207]\n\t" \
  "v_permlane32_swap_b32 v124, v140\n\t" \
  "v_permlane32_swap_b32 v125, v141\n\t" \
  "v_permlane32_swap_b32 v126, v142\n\t" \
  "v_permlane32_swap_b32 v127, v143\n\t" \
  "s_nop 1\n\t" \
  "v_mfma_f32_16x16x32_bf16 v[192:195], v[68:71], v[124:127], v[192:195]\n\t" \
  "v_mfma_f32_16x16x32_bf16 v[200:203], v[76:79], v[124:127], v[200:203]\n\t" \
  "v_mfma_f32_16x16x32_bf16 v[196:199], v[68:71], v[140:143], v[196:199]\n\t" \
  "v_mfma_f32_16x16x32_bf16 v[204:207], v[76:79], v[140:143], v[204:207]\n\t" \
  "v_mov_b64 v[144:145], 0\n\t" \
  "v_mov_b64 v[146:147], 0\n\t" \
  "v_mov_b64 v[148:149], 0\n\t" \
  "v_mov_b64 v[150:151], 0\n\t" \
  "v_mov_b64 v[152:153], 0\n\t" \
  "v_mov_b64 v[154:155], 0\n\t" \
  "v_mov_b64 v[156:157], 0\n\t" \
  "v_mov_b64 v[158:159], 0\n\t" \
  "ds_write_b128 v239, v[192:195] offset:0\n\t" \
  "ds_write_b128 v239, v[200:203] offset:2048\n\t" \
  "ds_write_b128 v239, v[196:199] offset:1024\n\t" \
  "ds_write_b128 v239, v[204:207] offset:3072\n\t" \
  "s_cmp_gt_i32 s44, 61\n\t" \
  "s_cbranch_scc1 97f\n\t" \
  "v_mov_b64 v[160:161], 0\n\t" \
  "v_mov_b64 v[162:163], 0\n\t" \
  "v_mov_b64 v[164:165], 0\n\t" \
  "v_mov_b64 v[166:167], 0\n\t" \
  "v_mov_b64 v[168:169], 0\n\t" \
  "v_mov_b64 v[170:171], 0\n\t" \
  "v_mov_b64 v[172:173], 0\n\t" \
  "v_mov_b64 v[174:175], 0\n\t" \
  "v_mov_b64 v[216:217], 0\n\t" \
  "v_mov_b64 v[218:219], 0\n\t" \
  "v_mov_b64 v[220:221], 0\n\t" \
  "v_mov_b64 v[222:223], 0\n\t" \
  "s_waitcnt vmcnt(0)\n\t" \
  "52:\n\t" \
  "s_add_i32 s44, s44, 2\n\t" \
  "s_branch 20b\n\t" \
  "22:\n\t" \
  "s_bitcmp1_b32 s58, 2\n\t" \
  "s_cbranch_scc1 24b\n\t" \
  "s_sleep 2\n\t" \
  "buffer_load_dword v208, v236, %[rh], s94 offen sc1\n\t" \
  "s_waitcnt vmcnt(0)\n\t" \
  "s_add_u32 s59, s59, 1\n\t" \
  "s_bitcmp0_b32 s59, 20\n\t" \
  "s_cbranch_scc1 21b\n\t" \
  "s_or_b32 s58, s58, 4\n\t" \
  "s_branch 21b\n\t" \
  "14:\n\t" \
  "s_bitcmp1_b32 s58, 2\n\t" \
  "s_cbranch_scc1 15b\n\t" \
  "s_lshr_b32 s84, s91, 8\n\t" \
  "s_add_i32 s84, s84, 8\n\t" \
  "s_cmp_lt_u32 s44, s84\n\t" \
  "s_cbranch_scc1 25f\n\t" \
  "s_and_b32 s91, s91, 255\n\t" \
  "s_add_i32 s91, s91, 20\n\t" \
  "s_min_u32 s91, s91, 150\n\t" \
  "s_lshl_b32 s84, s44, 8\n\t" \
  "s_or_b32 s91, s91, s84\n\t" \
  "v_mov_b32 v136, s91\n\t" \
  "s_mov_b64 exec, 2\n\t" \
  "buffer_store_dword v136, v224, %[rh], %[m2] offen sc1\n\t" \
  "s_mov_b64 exec, 0xff\n\t" \
  "25:\n\t" \
  "s_mov_b32 s84, 600\n\t" \
  "s_mov_b32 s90, 200\n\t" \
  "s_mov_b32 s93, 200\n\t" \
  "s_sleep 1\n\t" \
  "buffer_load_dword v208, v234, %[rh], s81 offen sc1\n\t" \
  "s_add_u32 s59, s59, 1\n\t" \
  "s_bitcmp0_b32 s59, 20\n\t" \
  "s_cbranch_scc1 13b\n\t" \
  "s_or_b32 s58, s58, 4\n\t" \
  "s_branch 13b\n\t" \
  "8:\n\t" \
  "s_mov_b32 s79, 0\n\t" \
  "17:\n\t" \
  "s_bitcmp1_b32 s58, 2\n\t" \
  "s_cbranch_scc1 18f\n\t" \
  "v_max_u32 v136, v64, v65\n\t" \
  "v_max3_u32 v136, v136, v66, v67\n\t" \
  "v_max3_u32 v136, v136, v68, v69\n\t" \
  "v_max3_u32 v136, v136, v70, v71\n\t" \
  "v_max3_u32 v136, v136, v72, v73\n\t" \
  "v_max3_u32 v136, v136, v74, v75\n\t" \
  "v_max3_u32 v136, v136, v76, v77\n\t" \
  "v_max3_u32 v136, v136, v78, v79\n\t" \
  "v_max3_u32 v136, v136, v192, v193\n\t" \
  "v_max3_u32 v136, v136, v194, v195\n\t" \
  "v_max3_u32 v136, v136, v196, v197\n\t" \
  "v_max3_u32 v136, v136, v198, v199\n\t" \
  "v_max3_u32 v136, v136, v200, v201\n\t" \
  "v_max3_u32 v136, v136, v202, v203\n\t" \
  "v_max3_u32 v136, v136, v204, v205\n\t" \
  "v_max3_u32 v136, v136, v206, v207\n\t" \
  "v_max3_u32 v136, v136, v208, v209\n\t" \
  "v_max3_u32 v136, v136, v210, v211\n\t" \
  "v_max3_u32 v136, v136, v212, v213\n\t" \
  "v_max3_u32 v136, v136, v214, v215\n\t" \
  "v_cmp_eq_u32 vcc, -1, v136\n\t" \
  "s_cbranch_vccz 18f\n\t" \
  "s_or_b32 s79, s79, s90\n\t" \
  "s_cbranch_scc1 19f\n\t" \
  "s_cmp_lt_u32 s44, 8\n\t" \
  "s_cbranch_scc1 19f\n\t" \
  "s_add_i32 s79, s101, 30\n\t" \
  "v_mov_b32 v136, s79\n\t" \
  "s_mov_b64 exec, 1\n\t" \
  "buffer_store_dword v136, v224, %[rh], %[m1] offen sc1\n\t" \
  "s_mov_b64 exec, -1\n\t" \
  "19:\n\t" \
  "s_mov_b32 s79, 1\n\t" \
  "buffer_load_dwordx4 v[192:195], v226, %[rh], s49 offen offset:0 sc1\n\t" \
  "buffer_load_dwordx4 v[64:67], v225, %[ru], s48 offen offset:0 sc1\n\t" \
  "buffer_load_dwordx4 v[196:199], v226, %[rh], s49 offen offset:2048 sc1\n\t" \
  "buffer_load_dwordx4 v[72:75], v225, %[ru], s76 offen offset:0 sc1\n\t" \
  "buffer_load_dwordx4 v[200:203], v226, %[rh], s80 offen offset:0 sc1\n\t" \
  "buffer_load_dwordx4 v[68:71], v225, %[ru], s77 offen offset:0 sc1\n\t" \
  "buffer_load_dwordx4 v[204:207], v226, %[rh], s80 offen offset:2048 sc1\n\t" \
  "buffer_load_dwordx4 v[76:79], v225, %[ru], s78 offen offset:0 sc1\n\t" \
  "buffer_load_dwordx4 v[208:211], v227, %[rh], s54 offen offset:0 sc1\n\t" \
  "buffer_load_dwordx4 v[212:215], v228, %[rh], s54 offen offset:0 sc1\n\t" \
  "s_waitcnt vmcnt(0)\n\t" \
  "s_add_u32 s59, s59, 1\n\t" \
  "s_bitcmp0_b32 s59, 20\n\t" \
  "s_cbranch_scc1 17b\n\t" \
  "s_or_b32 s58, s58, 4\n\t" \
  "s_branch 17b\n\t" \
  "18:\n\t" \
  "s_cmp_eq_u32 s79, 0\n\t" \
  "s_cbranch_scc1 6b\n\t" \
  "v_mov_b64 v[144:145], 0\n\t" \
  "v_mov_b64 v[146:147], 0\n\t" \
  "v_mov_b64 v[148:149], 0\n\t" \
  "v_mov_b64 v[150:151], 0\n\t" \
  "v_mov_b64 v[152:153], 0\n\t" \
  "v_mov_b64 v[154:155], 0\n\t" \
  "v_mov_b64 v[156:157], 0\n\t" \
  "v_mov_b64 v[158:159], 0\n\t" \
  "v_mov_b64 v[160:161], 0\n\t" \
  "v_mov_b64 v[162:163], 0\n\t" \
  "v_mov_b64 v[164:165], 0\n\t" \
  "v_mov_b64 v[166:167], 0\n\t" \
  "v_mov_b64 v[168:169], 0\n\t" \
  "v_mov_b64 v[170:171], 0\n\t" \
  "v_mov_b64 v[172:173], 0\n\t" \
  "v_mov_b64 v[174:175], 0\n\t" \
  "v_mov_b64 v[216:217], 0\n\t" \
  "v_mov_b64 v[218:219], 0\n\t" \
  "v_mov_b64 v[220:221], 0\n\t" \
  "v_mov_b64 v[222:223], 0\n\t" \
  "s_branch 10b\n\t" \
  "40:\n\t" \
  "s_add_i32 s79, s44, 2\n\t" \
  "s_cmp_lt_i32 s79, 64\n\t" \
  "s_cbranch_scc0 44f\n\t" \
  "42:\n\t" \
  "s_mov_b64 exec, 0xff\n\t" \
  "buffer_load_dword v210, v242, %[ru], 0 offen sc1\n\t" \
  "s_waitcnt vmcnt(0)\n\t" \
  "v_cmp_gt_i32 vcc, s79, v210\n\t" \
  "s_mov_b64 exec, -1\n\t" \
  "s_cbranch_vccz 43f\n\t" \
  "s_sleep 3\n\t" \
  "s_bitcmp1_b32 s58, 2\n\t" \
  "s_cbranch_scc1 43f\n\t" \
  "s_add_u32 s59, s59, 1\n\t" \
  "s_bitcmp0_b32 s59, 20\n\t" \
  "s_cbranch_scc1 42b\n\t" \
  "s_or_b32 s58, s58, 4\n\t" \
  "s_branch 43f\n\t" \
  "43:\n\t" \
  "s_and_b32 s96, s79, 15\n\t" \
  "s_lshl_b32 s96, s96, 22\n\t" \
  "s_add_i32 s96, s96, s42\n\t" \
  "s_lshr_b32 s82, s96, 21\n\t" \
  "s_bitcmp0_b32 %[sel], s82\n\t" \
  "s_cselect_b32 s82, 0x2000, 0\n\t" \
  "s_add_i32 s96, s96, s82\n\t" \
  "s_branch 39b\n\t" \
  "51:\n\t" \
  "s_memtime vcc\n\t" \
  "s_memrealtime s[82:83]\n\t" \
  "s_nop 4\n\t" \
  "s_memrealtime s[84:85]\n\t" \
  "s_nop 4\n\t" \
  "s_memrealtime s[94:95]\n\t" \
  "s_nop 4\n\t" \
  "s_memrealtime s[86:87]\n\t" \
  "s_nop 4\n\t" \
  "s_memrealtime s[88:89]\n\t" \
  "s_nop 4\n\t" \
  "s_memrealtime s[90:91]\n\t" \
  "s_mov_b32 s92, 0\n\t" \
  "s_waitcnt lgkmcnt(0)\n\t" \
  "s_cmp_eq_u32 s82, s84\n\t" \
  "s_addc_u32 s92, s92, 0\n\t" \
  "s_cmp_eq_u32 s82, s94\n\t" \
  "s_addc_u32 s92, s92, 0\n\t" \
  "s_cmp_eq_u32 s82, s86\n\t" \
  "s_addc_u32 s92, s92, 0\n\t" \
  "s_cmp_eq_u32 s82, s88\n\t" \
  "s_addc_u32 s92, s92, 0\n\t" \
  "s_cmp_eq_u32 s82, s90\n\t" \
  "s_addc_u32 s92, s92, 0\n\t" \
  "s_mul_i32 s92, s92, 24\n\t" \
  "s_add_i32 s92, s92, vcc_lo\n\t" \
  "s_lshl4_add_u32 s93, s82, 64\n\t" \
  "s_sub_i32 s82, s92, s51\n\t" \
  "s_sub_i32 s83, s93, s53\n\t" \
  "s_cmp_eq_u32 s82, 0\n\t" \
  "s_cbranch_scc1 53f\n\t" \
  "v_cvt_f32_u32 v208, s82\n\t" \
  "v_cvt_f32_u32 v209, s83\n\t" \
  "v_rcp_f32 v208, v208\n\t" \
  "s_mov_b32 s51, s92\n\t" \
  "v_mul_f32 v208, v208, v209\n\t" \
  "v_max_f32 v208, 0x3f028f5c, v208\n\t" \
  "v_min_f32 v208, 0x3ffeb852, v208\n\t" \
  "v_rcp_f32 v209, v208\n\t" \
  "v_mul_f32 v208, 0x4f000000, v208\n\t" \
  "v_mul_f32 v209, 0x47800000, v209\n\t" \
  "v_cvt_u32_f32 v208, v208\n\t" \
  "v_cvt_u32_f32 v209, v209\n\t" \
  "s_mov_b32 s53, s93\n\t" \
  "v_readfirstlane_b32 s46, v208\n\t" \
  "v_readfirstlane_b32 s52, v209\n\t" \
  "53:\n\t" \
  "s_lshl_b32 s50, s53, 8\n\t" \
  "s_add_i32 s85, s44, 1\n\t" \
  "s_and_b32 s94, s85, 3\n\t" \
  "s_lshl_b32 s94, s94, 16\n\t" \
  "s_mov_b32 exec_hi, 0\n\t" \
  "buffer_load_dword v209, v237, %[rh], s55 offen offset:0 sc1\n\t" \
  "s_mov_b64 exec, 0xff\n\t" \
  "buffer_load_dword v210, v242, %[ru], 0 offen offset:0 sc1\n\t" \
  "buffer_load_dword v208, v236, %[rh], s94 offen offset:0 sc1\n\t" \
  "s_mov_b64 exec, 3\n\t" \
  "buffer_load_dword v211, v224, %[rh], %[mg] offen offset:0 sc1\n\t" \
  "s_mov_b64 exec, -1\n\t" \
  "v_mov_b64 v[144:145], 0\n\t" \
  "v_mov_b64 v[146:147], 0\n\t" \
  "v_mov_b64 v[148:149], 0\n\t" \
  "v_mov_b64 v[150:151], 0\n\t" \
  "v_mov_b64 v[152:153], 0\n\t" \
  "v_mov_b64 v[154:155], 0\n\t" \
  "v_mov_b64 v[156:157], 0\n\t" \
  "v_mov_b64 v[158:159], 0\n\t" \
  "v_mov_b64 v[160:161], 0\n\t" \
  "v_mov_b64 v[162:163], 0\n\t" \
  "v_mov_b64 v[164:165], 0\n\t" \
  "v_mov_b64 v[166:167], 0\n\t" \
  "v_mov_b64 v[168:169], 0\n\t" \
  "v_mov_b64 v[170:171], 0\n\t" \
  "v_mov_b64 v[172:173], 0\n\t" \
  "v_mov_b64 v[174:175], 0\n\t" \
  "v_mov_b64 v[216:217], 0\n\t" \
  "v_mov_b64 v[218:219], 0\n\t" \
  "v_mov_b64 v[220:221], 0\n\t" \
  "v_mov_b64 v[222:223], 0\n\t" \
  "s_waitcnt vmcnt(0)\n\t" \
  "s_branch 52b\n\t" \
  "97:\n\t" \
  "s_waitcnt lgkmcnt(0)\n\t" \
  "s_barrier\n\t" \
  "ds_read_b128 v[80:83], v240 offset:0\n\t" \
  "ds_read_b128 v[84:87], v240 offset:4096\n\t" \
  "ds_read_b128 v[88:91], v240 offset:8192\n\t" \
  "ds_read_b128 v[92:95], v240 offset:12288\n\t" \
  "s_add_i32 s85, s44, 0\n\t" \
  "s_lshl_b32 s86, s85, 17\n\t" \
  "s_cmp_lt_i32 s85, 0\n\t" \
  "s_cselect_b32 s86, 0x7f000000, s86\n\t" \
  "s_waitcnt lgkmcnt(0)\n\t" \
  "v_add_f32 v80, v80, v84\n\t" \
  "v_add_f32 v81, v81, v85\n\t" \
  "v_add_f32 v82, v82, v86\n\t" \
  "v_add_f32 v83, v83, v87\n\t" \
  "v_add_f32 v88, v88, v92\n\t" \
  "v_add_f32 v89, v89, v93\n\t" \
  "v_add_f32 v90, v90, v94\n\t" \
  "v_add_f32 v91, v91, v95\n\t" \
  "v_add_f32 v80, v80, v88\n\t" \
  "v_add_f32 v81, v81, v89\n\t" \
  "v_add_f32 v82, v82, v90\n\t" \
  "v_add_f32 v83, v83, v91\n\t" \
  "v_cvt_pk_bf16_f32 v80, v80, v81\n\t" \
  "v_cvt_pk_bf16_f32 v81, v82, v83\n\t" \
  "buffer_store_dwordx2 v[80:81], v241, %[ro], s86 offen sc1\n\t" \
  "s_branch 99f\n\t" \
  "44:\n\t" \
  "s_waitcnt lgkmcnt(0)\n\t" \
  "ds_read_b128 v[80:83], v240 offset:0\n\t" \
  "ds_read_b128 v[84:87], v240 offset:4096\n\t" \
  "ds_read_b128 v[88:91], v240 offset:8192\n\t" \
  "ds_read_b128 v[92:95], v240 offset:12288\n\t" \
  "s_add_i32 s85, s44, -2\n\t" \
  "s_lshl_b32 s86, s85, 17\n\t" \
  "s_cmp_lt_i32 s85, 0\n\t" \
  "s_cselect_b32 s86, 0x7f000000, s86\n\t" \
  "s_waitcnt lgkmcnt(0)\n\t" \
  "v_add_f32 v80, v80, v84\n\t" \
  "v_add_f32 v81, v81, v85\n\t" \
  "v_add_f32 v82, v82, v86\n\t" \
  "v_add_f32 v83, v83, v87\n\t" \
  "v_add_f32 v88, v88, v92\n\t" \
  "v_add_f32 v89, v89, v93\n\t" \
  "v_add_f32 v90, v90, v94\n\t" \
  "v_add_f32 v91, v91, v95\n\t" \
  "v_add_f32 v80, v80, v88\n\t" \
  "v_add_f32 v81, v81, v89\n\t" \
  "v_add_f32 v82, v82, v90\n\t" \
  "v_add_f32 v83, v83, v91\n\t" \
  "v_cvt_pk_bf16_f32 v80, v80, v81\n\t" \
  "v_cvt_pk_bf16_f32 v81, v82, v83\n\t" \
  "buffer_store_dwordx2 v[80:81], v241, %[ro], s86 offen sc1\n\t" \
  "s_and_b32 s97, s44, 3\n\t" \
  "s_lshl_b32 s97, s97, 17\n\t" \
  "s_add_i32 s97, s97, s43\n\t" \
  "s_add_i32 s85, s44, 1\n\t" \
  "s_and_b32 s94, s85, 3\n\t" \
  "s_lshl_b32 s94, s94, 16\n\t" \
  "s_and_b32 s95, s44, 3\n\t" \
  "s_lshl_b32 s95, s95, 16\n\t" \
  "s_add_i32 s82, s56, 500\n\t" \
  "48:\n\t" \
  "s_memtime vcc\n\t" \
  "s_waitcnt lgkmcnt(0)\n\t" \
  "s_sub_i32 s83, s82, vcc_lo\n\t" \
  "s_cmp_gt_i32 s83, 30\n\t" \
  "s_cbranch_scc1 48b\n\t" \
  "45:\n\t" \
  "s_bitcmp1_b32 s58, 2\n\t" \
  "s_cbranch_scc1 46b\n\t" \
  "s_sleep 1\n\t" \
  "s_mov_b64 exec, 0xff\n\t" \
  "buffer_load_dword v211, v248, %[rh], s95 offen sc1\n\t" \
  "s_waitcnt vmcnt(0)\n\t" \
  "s_add_u32 s59, s59, 1\n\t" \
  "s_bitcmp0_b32 s59, 20\n\t" \
  "s_cbranch_scc1 47b\n\t" \
  "s_or_b32 s58, s58, 4\n\t" \
  "s_branch 47b\n\t" \
  "99:\n\t" \
  "s_waitcnt vmcnt(0) lgkmcnt(0)\n\t" \
  ""
#define P_BODY \
  "s_cmp_eq_u32 %[wv], 1\n\t" \
  "s_cbranch_scc1 91f\n\t" \
  "s_cmp_eq_u32 %[wv], 2\n\t" \
  "s_cbranch_scc1 92f\n\t" \
  "s_cmp_eq_u32 %[wv], 3\n\t" \
  "s_cbranch_scc1 93f\n\t" \
  "90:\n\t" \
  "ds_read_b128 v[0:3], v164 offset:0\n\t" \
  "ds_read_b128 v[4:7], v164 offset:8192\n\t" \
  "ds_read_b128 v[8:11], v165 offset:0\n\t" \
  "ds_read_b128 v[12:15], v165 offset:8192\n\t" \
  "ds_read_b128 v[16:19], v166 offset:0\n\t" \
  "ds_read_b128 v[20:23], v166 offset:8192\n\t" \
  "ds_read_b128 v[24:27], v167 offset:0\n\t" \
  "ds_read_b128 v[28:31], v167 offset:8192\n\t" \
  "ds_read_b128 v[32:35], v164 offset:256\n\t" \
  "ds_read_b128 v[36:39], v164 offset:8448\n\t" \
  "ds_read_b128 v[40:43], v165 offset:256\n\t" \
  "ds_read_b128 v[44:47], v165 offset:8448\n\t" \
  "ds_read_b128 v[48:51], v166 offset:256\n\t" \
  "ds_read_b128 v[52:55], v166 offset:8448\n\t" \
  "ds_read_b128 v[56:59], v167 offset:256\n\t" \
  "ds_read_b128 v[60:63], v167 offset:8448\n\t" \
  "v_mov_b64 v[152:153], -1\n\t" \
  "v_mov_b64 v[154:155], -1\n\t" \
  "s_cmp_le_u32 s44, 12\n\t" \
  ".p2align 6\n\t" \
  "20:\n\t" \
  "s_waitcnt vmcnt(28) lgkmcnt(0)\n\t" \
  "v_mfma_f32_16x16x32_bf16 v[128:131], a[0:3], v[0:3], 0\n\t" \
  "s_cbranch_scc1 30f\n\t" \
  "40:\n\t" \
  "s_barrier\n\t" \
  "v_cvt_pk_bf16_f32 v148, v136, v137\n\t" \
  "v_cvt_pk_bf16_f32 v149, v138, v139\n\t" \
  "v_mfma_f32_16x16x32_bf16 v[132:135], a[4:7], v[0:3], 0\n\t" \
  "v_cvt_pk_bf16_f32 v150, v140, v141\n\t" \
  "v_cvt_pk_bf16_f32 v151, v142, v143\n\t" \
  "v_mfma_f32_16x16x32_bf16 v[136:139], a[0:3], v[4:7], 0\n\t" \
  "s_add_i32 m0, s41, 0\n\t" \
  "v_mfma_f32_16x16x32_bf16 v[140:143], a[4:7], v[4:7], 0\n\t" \
  "buffer_load_dwordx4 v160, %[rx], s40 offen lds\n\t" \
  "v_mfma_f32_16x16x32_bf16 v[128:131], a[8:11], v[8:11], v[128:131]\n\t" \
  "ds_read_b128 v[64:67], v164 offset:16384\n\t" \
  "v_mfma_f32_16x16x32_bf16 v[132:135], a[12:15], v[8:11], v[132:135]\n\t" \
  "ds_read_b128 v[68:71], v164 offset:24576\n\t" \
  "v_mfma_f32_16x16x32_bf16 v[136:139], a[8:11], v[12:15], v[136:139]\n\t" \
  "ds_read_b128 v[72:75], v165 offset:16384\n\t" \
  "v_mfma_f32_16x16x32_bf16 v[140:143], a[12:15], v[12:15], v[140:143]\n\t" \
  "buffer_store_dwordx4 v[144:147], v168, %[ru], s42 offen sc1\n\t" \
  "v_mfma_f32_16x16x32_bf16 v[128:131], a[16:19], v[16:19], v[128:131]\n\t" \
  "ds_read_b128 v[76:79], v165 offset:24576\n\t" \
  "v_mfma_f32_16x16x32_bf16 v[132:135], a[20:23], v[16:19], v[132:135]\n\t" \
  "ds_read_b128 v[80:83], v166 offset:16384\n\t" \
  "v_mfma_f32_16x16x32_bf16 v[136:139], a[16:19], v[20:23], v[136:139]\n\t" \
  "s_add_i32 m0, s41, 1024\n\t" \
  "v_mfma_f32_16x16x32_bf16 v[140:143], a[20:23], v[20:23], v[140:143]\n\t" \
  "buffer_load_dwordx4 v161, %[rx], s40 offen lds\n\t" \
  "v_mfma_f32_16x16x32_bf16 v[128:131], a[24:27], v[24:27], v[128:131]\n\t" \
  "ds_read_b128 v[84:87], v166 offset:24576\n\t" \
  "v_mfma_f32_16x16x32_bf16 v[132:135], a[28:31], v[24:27], v[132:135]\n\t" \
  "ds_read_b128 v[88:91], v167 offset:16384\n\t" \
  "v_mfma_f32_16x16x32_bf16 v[136:139], a[24:27], v[28:31], v[136:139]\n\t" \
  "ds_read_b128 v[92:95], v167 offset:24576\n\t" \
  "v_mfma_f32_16x16x32_bf16 v[140:143], a[28:31], v[28:31], v[140:143]\n\t" \
  "buffer_store_dwordx4 v[148:151], v169, %[ru], s42 offen sc1\n\t" \
  "v_mfma_f32_16x16x32_bf16 v[128:131], a[32:35], v[32:35], v[128:131]\n\t" \
  "ds_read_b128 v[96:99], v164 offset:16640\n\t" \
  "v_mfma_f32_16x16x32_bf16 v[132:135], a[36:39], v[32:35], v[132:135]\n\t" \
  "ds_read_b128 v[100:103], v164 offset:24832\n\t" \
  "v_mfma_f32_16x16x32_bf16 v[136:139], a[32:35], v[36:39], v[136:139]\n\t" \
  "s_add_i32 m0, s41, 2048\n\t" \
  "v_mfma_f32_16x16x32_bf16 v[140:143], a[36:39], v[36:39], v[140:143]\n\t" \
  "buffer_load_dwordx4 v162, %[rx], s40 offen lds\n\t" \
  "v_mfma_f32_16x16x32_bf16 v[128:131], a[40:43], v[40:43], v[128:131]\n\t" \
  "ds_read_b128 v[104:107], v165 offset:16640\n\t" \
  "v_mfma_f32_16x16x32_bf16 v[132:135], a[44:47], v[40:43], v[132:135]\n\t" \
  "ds_read_b128 v[108:111], v165 offset:24832\n\t" \
  "v_mfma_f32_16x16x32_bf16 v[136:139], a[40:43], v[44:47], v[136:139]\n\t" \
  "s_add_i32 m0, s41, 3072\n\t" \
  "v_mfma_f32_16x16x32_bf16 v[140:143], a[44:47], v[44:47], v[140:143]\n\t" \
  "buffer_load_dwordx4 v163, %[rx], s40 offen lds\n\t" \
  "v_mfma_f32_16x16x32_bf16 v[128:131], a[48:51], v[48:51], v[128:131]\n\t" \
  "ds_read_b128 v[112:115], v166 offset:16640\n\t" \
  "v_mfma_f32_16x16x32_bf16 v[132:135], a[52:55], v[48:51], v[132:135]\n\t" \
  "ds_read_b128 v[116:119], v166 offset:24832\n\t" \
  "v_mfma_f32_16x16x32_bf16 v[136:139], a[48:51], v[52:55], v[136:139]\n\t" \
  "ds_read_b128 v[120:123], v167 offset:16640\n\t" \
  "v_mfma_f32_16x16x32_bf16 v[140:143], a[52:55], v[52:55], v[140:143]\n\t" \
  "ds_read_b128 v[124:127], v167 offset:24832\n\t" \
  "v_mfma_f32_16x16x32_bf16 v[128:131], a[56:59], v[56:59], v[128:131]\n\t" \
  "v_mfma_f32_16x16x32_bf16 v[132:135], a[60:63], v[56:59], v[132:135]\n\t" \
  "v_mfma_f32_16x16x32_bf16 v[136:139], a[56:59], v[60:63], v[136:139]\n\t" \
  "v_mfma_f32_16x16x32_bf16 v[140:143], a[60:63], v[60:63], v[140:143]\n\t" \
  "s_cmp_le_u32 s44, 12\n\t" \
  "s_waitcnt vmcnt(30) lgkmcnt(0)\n\t" \
  "v_mfma_f32_16x16x32_bf16 v[128:131], a[64:67], v[64:67], v[128:131]\n\t" \
  "s_cbranch_scc1 31f\n\t" \
  "41:\n\t" \
  "s_barrier\n\t" \
  "v_mfma_f32_16x16x32_bf16 v[132:135], a[68:71], v[64:67], v[132:135]\n\t" \
  "s_add_i32 s76, s40, 512\n\t" \
  "v_mfma_f32_16x16x32_bf16 v[136:139], a[64:67], v[68:71], v[136:139]\n\t" \
  "s_add_i32 m0, s41, 16384\n\t" \
  "v_mfma_f32_16x16x32_bf16 v[140:143], a[68:71], v[68:71], v[140:143]\n\t" \
  "buffer_load_dwordx4 v160, %[rx], s76 offen lds\n\t" \
  "v_mfma_f32_16x16x32_bf16 v[128:131], a[72:75], v[72:75], v[128:131]\n\t" \
  "ds_read_b128 v[0:3], v164 offset:32768\n\t" \
  "v_mfma_f32_16x16x32_bf16 v[132:135], a[76:79], v[72:75], v[132:135]\n\t" \
  "ds_read_b128 v[4:7], v164 offset:40960\n\t" \
  "v_mfma_f32_16x16x32_bf16 v[136:139], a[72:75], v[76:79], v[136:139]\n\t" \
  "ds_read_b128 v[8:11], v165 offset:32768\n\t" \
  "v_mfma_f32_16x16x32_bf16 v[140:143], a[76:79], v[76:79], v[140:143]\n\t" \
  "v_mfma_f32_16x16x32_bf16 v[128:131], a[80:83], v[80:83], v[128:131]\n\t" \
  "ds_read_b128 v[12:15], v165 offset:40960\n\t" \
  "v_mfma_f32_16x16x32_bf16 v[132:135], a[84:87], v[80:83], v[132:135]\n\t" \
  "ds_read_b128 v[16:19], v166 offset:32768\n\t" \
  "v_mfma_f32_16x16x32_bf16 v[136:139], a[80:83], v[84:87], v[136:139]\n\t" \
  "s_add_i32 m0, s41, 17408\n\t" \
  "v_mfma_f32_16x16x32_bf16 v[140:143], a[84:87], v[84:87], v[140:143]\n\t" \
  "buffer_load_dwordx4 v161, %[rx], s76 offen lds\n\t" \
  "v_mfma_f32_16x16x32_bf16 v[128:131], a[88:91], v[88:91], v[128:131]\n\t" \
  "ds_read_b128 v[20:23], v166 offset:40960\n\t" \
  "v_mfma_f32_16x16x32_bf16 v[132:135], a[92:95], v[88:91], v[132:135]\n\t" \
  "ds_read_b128 v[24:27], v167 offset:32768\n\t" \
  "v_mfma_f32_16x16x32_bf16 v[136:139], a[88:91], v[92:95], v[136:139]\n\t" \
  "ds_read_b128 v[28:31], v167 offset:40960\n\t" \
  "v_mfma_f32_16x16x32_bf16 v[140:143], a[92:95], v[92:95], v[140:143]\n\t" \
  "v_mfma_f32_16x16x32_bf16 v[128:131], a[96:99], v[96:99], v[128:131]\n\t" \
  "ds_read_b128 v[32:35], v164 offset:33024\n\t" \
  "v_mfma_f32_16x16x32_bf16 v[132:135], a[100:103], v[96:99], v[132:135]\n\t" \
  "ds_read_b128 v[36:39], v164 offset:41216\n\t" \
  "v_mfma_f32_16x16x32_bf16 v[136:139], a[96:99], v[100:103], v[136:139]\n\t" \
  "s_add_i32 m0, s41, 18432\n\t" \
  "v_mfma_f32_16x16x32_bf16 v[140:143], a[100:103], v[100:103], v[140:143]\n\t" \
  "buffer_load_dwordx4 v162, %[rx], s76 offen lds\n\t" \
  "v_mfma_f32_16x16x32_bf16 v[128:131], a[104:107], v[104:107], v[128:131]\n\t" \
  "ds_read_b128 v[40:43], v165 offset:33024\n\t" \
  "v_mfma_f32_16x16x32_bf16 v[132:135], a[108:111], v[104:107], v[132:135]\n\t" \
  "ds_read_b128 v[44:47], v165 offset:41216\n\t" \
  "v_mfma_f32_16x16x32_bf16 v[136:139], a[104:107], v[108:111], v[136:139]\n\t" \
  "s_add_i32 m0, s41, 19456\n\t" \
  "v_mfma_f32_16x16x32_bf16 v[140:143], a[108:111], v[108:111], v[140:143]\n\t" \
  "buffer_load_dwordx4 v163, %[rx], s76 offen lds\n\t" \
  "v_mfma_f32_16x16x32_bf16 v[128:131], a[112:115], v[112:115], v[128:131]\n\t" \
  "ds_read_b128 v[48:51], v166 offset:33024\n\t" \
  "v_mfma_f32_16x16x32_bf16 v[132:135], a[116:119], v[112:115], v[132:135]\n\t" \
  "ds_read_b128 v[52:55], v166 offset:41216\n\t" \
  "v_mfma_f32_16x16x32_bf16 v[136:139], a[112:115], v[116:119], v[136:139]\n\t" \
  "ds_read_b128 v[56:59], v167 offset:33024\n\t" \
  "v_mfma_f32_16x16x32_bf16 v[140:143], a[116:119], v[116:119], v[140:143]\n\t" \
  "ds_read_b128 v[60:63], v167 offset:41216\n\t" \
  "v_mfma_f32_16x16x32_bf16 v[128:131], a[120:123], v[120:123], v[128:131]\n\t" \
  "s_lshr_b32 s79, s44, 2\n\t" \
  "v_mfma_f32_16x16x32_bf16 v[132:135], a[124:127], v[120:123], v[132:135]\n\t" \
  "s_bitcmp1_b32 %[hm], s79\n\t" \
  "s_cselect_b32 s79, 0x2000, 0\n\t" \
  "v_mfma_f32_16x16x32_bf16 v[136:139], a[120:123], v[124:127], v[136:139]\n\t" \
  "s_cmp_lt_u32 s44, 4\n\t" \
  "s_cselect_b32 s79, %[d0], s79\n\t" \
  "v_mfma_f32_16x16x32_bf16 v[140:143], a[124:127], v[124:127], v[140:143]\n\t" \
  "s_cmp_le_u32 s44, 12\n\t" \
  "s_waitcnt vmcnt(29) lgkmcnt(0)\n\t" \
  "v_mfma_f32_16x16x32_bf16 v[128:131], a[128:131], v[0:3], v[128:131]\n\t" \
  "s_cbranch_scc1 32f\n\t" \
  "42:\n\t" \
  "s_barrier\n\t" \
  "v_mfma_f32_16x16x32_bf16 v[132:135], a[132:135], v[0:3], v[132:135]\n\t" \
  "s_add_i32 s76, s40, 1024\n\t" \
  "v_mfma_f32_16x16x32_bf16 v[136:139], a[128:131], v[4:7], v[136:139]\n\t" \
  "s_add_i32 m0, s41, 32768\n\t" \
  "v_mfma_f32_16x16x32_bf16 v[140:143], a[132:135], v[4:7], v[140:143]\n\t" \
  "buffer_load_dwordx4 v160, %[rx], s76 offen lds\n\t" \
  "v_mfma_f32_16x16x32_bf16 v[128:131], a[136:139], v[8:11], v[128:131]\n\t" \
  "ds_read_b128 v[64:67], v164 offset:49152\n\t" \
  "v_mfma_f32_16x16x32_bf16 v[132:135], a[140:143], v[8:11], v[132:135]\n\t" \
  "ds_read_b128 v[68:71], v164 offset:57344\n\t" \
  "v_mfma_f32_16x16x32_bf16 v[136:139], a[136:139], v[12:15], v[136:139]\n\t" \
  "ds_read_b128 v[72:75], v165 offset:49152\n\t" \
  "v_mfma_f32_16x16x32_bf16 v[140:143], a[140:143], v[12:15], v[140:143]\n\t" \
  "v_mfma_f32_16x16x32_bf16 v[128:131], a[144:147], v[16:19], v[128:131]\n\t" \
  "ds_read_b128 v[76:79], v165 offset:57344\n\t" \
  "v_mfma_f32_16x16x32_bf16 v[132:135], a[148:151], v[16:19], v[132:135]\n\t" \
  "ds_read_b128 v[80:83], v166 offset:49152\n\t" \
  "v_mfma_f32_16x16x32_bf16 v[136:139], a[144:147], v[20:23], v[136:139]\n\t" \
  "s_add_i32 m0, s41, 33792\n\t" \
  "v_mfma_f32_16x16x32_bf16 v[140:143], a[148:151], v[20:23], v[140:143]\n\t" \
  "buffer_load_dwordx4 v161, %[rx], s76 offen lds\n\t" \
  "v_mfma_f32_16x16x32_bf16 v[128:131], a[152:155], v[24:27], v[128:131]\n\t" \
  "ds_read_b128 v[84:87], v166 offset:57344\n\t" \
  "v_mfma_f32_16x16x32_bf16 v[132:135], a[156:159], v[24:27], v[132:135]\n\t" \
  "ds_read_b128 v[88:91], v167 offset:49152\n\t" \
  "v_mfma_f32_16x16x32_bf16 v[136:139], a[152:155], v[28:31], v[136:139]\n\t" \
  "ds_read_b128 v[92:95], v167 offset:57344\n\t" \
  "v_mfma_f32_16x16x32_bf16 v[140:143], a[156:159], v[28:31], v[140:143]\n\t" \
  "v_mfma_f32_16x16x32_bf16 v[128:131], a[160:163], v[32:35], v[128:131]\n\t" \
  "ds_read_b128 v[96:99], v164 offset:49408\n\t" \
  "v_mfma_f32_16x16x32_bf16 v[132:135], a[164:167], v[32:35], v[132:135]\n\t" \
  "ds_read_b128 v[100:103], v164 offset:57600\n\t" \
  "v_mfma_f32_16x16x32_bf16 v[136:139], a[160:163], v[36:39], v[136:139]\n\t" \
  "s_add_i32 m0, s41, 34816\n\t" \
  "v_mfma_f32_16x16x32_bf16 v[140:143], a[164:167], v[36:39], v[140:143]\n\t" \
  "buffer_load_dwordx4 v162, %[rx], s76 offen lds\n\t" \
  "v_mfma_f32_16x16x32_bf16 v[128:131], a[168:171], v[40:43], v[128:131]\n\t" \
  "ds_read_b128 v[104:107], v165 offset:49408\n\t" \
  "v_mfma_f32_16x16x32_bf16 v[132:135], a[172:175], v[40:43], v[132:135]\n\t" \
  "ds_read_b128 v[108:111], v165 offset:57600\n\t" \
  "v_mfma_f32_16x16x32_bf16 v[136:139], a[168:171], v[44:47], v[136:139]\n\t" \
  "s_add_i32 m0, s41, 35840\n\t" \
  "v_mfma_f32_16x16x32_bf16 v[140:143], a[172:175], v[44:47], v[140:143]\n\t" \
  "buffer_load_dwordx4 v163, %[rx], s76 offen lds\n\t" \
  "v_mfma_f32_16x16x32_bf16 v[128:131], a[176:179], v[48:51], v[128:131]\n\t" \
  "ds_read_b128 v[112:115], v166 offset:49408\n\t" \
  "v_mfma_f32_16x16x32_bf16 v[132:135], a[180:183], v[48:51], v[132:135]\n\t" \
  "ds_read_b128 v[116:119], v166 offset:57600\n\t" \
  "v_mfma_f32_16x16x32_bf16 v[136:139], a[176:179], v[52:55], v[136:139]\n\t" \
  "ds_read_b128 v[120:123], v167 offset:49408\n\t" \
  "v_mfma_f32_16x16x32_bf16 v[140:143], a[180:183], v[52:55], v[140:143]\n\t" \
  "ds_read_b128 v[124:127], v167 offset:57600\n\t" \
  "v_mfma_f32_16x16x32_bf16 v[128:131], a[184:187], v[56:59], v[128:131]\n\t" \
  "s_add_i32 s78, s44, -2\n\t" \
  "v_mfma_f32_16x16x32_bf16 v[132:135], a[188:191], v[56:59], v[132:135]\n\t" \
  "s_max_i32 s78, s78, s46\n\t" \
  "v_mfma_f32_16x16x32_bf16 v[136:139], a[184:187], v[60:63], v[136:139]\n\t" \
  "v_mov_b32 v156, s78\n\t" \
  "v_mfma_f32_16x16x32_bf16 v[140:143], a[188:191], v[60:63], v[140:143]\n\t" \
  "s_cmp_le_u32 s44, 12\n\t" \
  "s_waitcnt vmcnt(27) lgkmcnt(0)\n\t" \
  "v_mfma_f32_16x16x32_bf16 v[128:131], a[192:195], v[64:67], v[128:131]\n\t" \
  "s_cbranch_scc1 33f\n\t" \
  "43:\n\t" \
  "s_barrier\n\t" \
  "v_xor_b32 v164, 0x10000, v164\n\t" \
  "v_mfma_f32_16x16x32_bf16 v[132:135], a[196:199], v[64:67], v[132:135]\n\t" \
  "s_add_i32 s76, s40, 1536\n\t" \
  "buffer_store_dword v156, v171, %[ru], 0 offen sc1\n\t" \
  "v_mfma_f32_16x16x32_bf16 v[136:139], a[192:195], v[68:71], v[136:139]\n\t" \
  "s_add_i32 m0, s41, 49152\n\t" \
  "v_xor_b32 v165, 0x10000, v165\n\t" \
  "v_mfma_f32_16x16x32_bf16 v[140:143], a[196:199], v[68:71], v[140:143]\n\t" \
  "buffer_load_dwordx4 v160, %[rx], s76 offen lds\n\t" \
  "v_xor_b32 v166, 0x10000, v166\n\t" \
  "v_mfma_f32_16x16x32_bf16 v[128:131], a[200:203], v[72:75], v[128:131]\n\t" \
  "v_xor_b32 v167, 0x10000, v167\n\t" \
  "ds_read_b128 v[0:3], v164 offset:0\n\t" \
  "v_mfma_f32_16x16x32_bf16 v[132:135], a[204:207], v[72:75], v[132:135]\n\t" \
  "ds_read_b128 v[4:7], v164 offset:8192\n\t" \
  "v_mfma_f32_16x16x32_bf16 v[136:139], a[200:203], v[76:79], v[136:139]\n\t" \
  "ds_read_b128 v[8:11], v165 offset:0\n\t" \
  "v_mfma_f32_16x16x32_bf16 v[140:143], a[204:207], v[76:79], v[140:143]\n\t" \
  "v_mfma_f32_16x16x32_bf16 v[128:131], a[208:211], v[80:83], v[128:131]\n\t" \
  "ds_read_b128 v[12:15], v165 offset:8192\n\t" \
  "v_mfma_f32_16x16x32_bf16 v[132:135], a[212:215], v[80:83], v[132:135]\n\t" \
  "ds_read_b128 v[16:19], v166 offset:0\n\t" \
  "v_mfma_f32_16x16x32_bf16 v[136:139], a[208:211], v[84:87], v[136:139]\n\t" \
  "s_add_i32 m0, s41, 50176\n\t" \
  "ds_read_b128 v[20:23], v166 offset:8192\n\t" \
  "v_mfma_f32_16x16x32_bf16 v[140:143], a[212:215], v[84:87], v[140:143]\n\t" \
  "buffer_load_dwordx4 v161, %[rx], s76 offen lds\n\t" \
  "v_mfma_f32_16x16x32_bf16 v[128:131], a[216:219], v[88:91], v[128:131]\n\t" \
  "ds_read_b128 v[24:27], v167 offset:0\n\t" \
  "v_mfma_f32_16x16x32_bf16 v[132:135], a[220:223], v[88:91], v[132:135]\n\t" \
  "ds_read_b128 v[28:31], v167 offset:8192\n\t" \
  "v_mfma_f32_16x16x32_bf16 v[136:139], a[216:219], v[92:95], v[136:139]\n\t" \
  "ds_read_b128 v[32:35], v164 offset:256\n\t" \
  "v_mfma_f32_16x16x32_bf16 v[140:143], a[220:223], v[92:95], v[140:143]\n\t" \
  "v_mfma_f32_16x16x32_bf16 v[128:131], a[224:227], v[96:99], v[128:131]\n\t" \
  "ds_read_b128 v[36:39], v164 offset:8448\n\t" \
  "v_mfma_f32_16x16x32_bf16 v[132:135], a[228:231], v[96:99], v[132:135]\n\t" \
  "ds_read_b128 v[40:43], v165 offset:256\n\t" \
  "v_mfma_f32_16x16x32_bf16 v[136:139], a[224:227], v[100:103], v[136:139]\n\t" \
  "s_add_i32 m0, s41, 51200\n\t" \
  "v_mfma_f32_16x16x32_bf16 v[140:143], a[228:231], v[100:103], v[140:143]\n\t" \
  "buffer_load_dwordx4 v162, %[rx], s76 offen lds\n\t" \
  "v_mfma_f32_16x16x32_bf16 v[128:131], a[232:235], v[104:107], v[128:131]\n\t" \
  "ds_read_b128 v[44:47], v165 offset:8448\n\t" \
  "v_mfma_f32_16x16x32_bf16 v[132:135], a[236:239], v[104:107], v[132:135]\n\t" \
  "ds_read_b128 v[48:51], v166 offset:256\n\t" \
  "v_mfma_f32_16x16x32_bf16 v[136:139], a[232:235], v[108:111], v[136:139]\n\t" \
  "s_add_i32 m0, s41, 52224\n\t" \
  "v_mfma_f32_16x16x32_bf16 v[140:143], a[236:239], v[108:111], v[140:143]\n\t" \
  "buffer_load_dwordx4 v163, %[rx], s76 offen lds\n\t" \
  "v_mfma_f32_16x16x32_bf16 v[128:131], a[240:243], v[112:115], v[128:131]\n\t" \
  "ds_read_b128 v[52:55], v166 offset:8448\n\t" \
  "v_mfma_f32_16x16x32_bf16 v[132:135], a[244:247], v[112:115], v[132:135]\n\t" \
  "ds_read_b128 v[56:59], v167 offset:256\n\t" \
  "v_mfma_f32_16x16x32_bf16 v[136:139], a[240:243], v[116:119], v[136:139]\n\t" \
  "ds_read_b128 v[60:63], v167 offset:8448\n\t" \
  "v_mfma_f32_16x16x32_bf16 v[140:143], a[244:247], v[116:119], v[140:143]\n\t" \
  "s_add_i32 s44, s44, 1\n\t" \
  "s_add_i32 s42, s43, s79\n\t" \
  "v_mfma_f32_16x16x32_bf16 v[128:131], a[248:251], v[120:123], v[128:131]\n\t" \
  "s_add_i32 s43, s43, 0x80000\n\t" \
  "s_add_i32 s40, s40, 0x10000\n\t" \
  "v_mfma_f32_16x16x32_bf16 v[132:135], a[252:255], v[120:123], v[132:135]\n\t" \
  "s_xor_b32 s41, s41, 0x10000\n\t" \
  "v_mfma_f32_16x16x32_bf16 v[136:139], a[248:251], v[124:127], v[136:139]\n\t" \
  "v_cvt_pk_bf16_f32 v144, v128, v129\n\t" \
  "v_cvt_pk_bf16_f32 v145, v130, v131\n\t" \
  "v_mfma_f32_16x16x32_bf16 v[140:143], a[252:255], v[124:127], v[140:143]\n\t" \
  "v_cvt_pk_bf16_f32 v146, v132, v133\n\t" \
  "v_cvt_pk_bf16_f32 v147, v134, v135\n\t" \
  "s_cmp_eq_u32 s44, 64\n\t" \
  "s_cbranch_scc1 98f\n\t" \
  "s_cmp_le_u32 s44, 12\n\t" \
  "s_branch 20b\n\t" \
  "30:\n\t" \
  "s_waitcnt vmcnt(24)\n\t" \
  "s_branch 40b\n\t" \
  "31:\n\t" \
  "s_waitcnt vmcnt(24)\n\t" \
  "s_branch 41b\n\t" \
  "32:\n\t" \
  "s_waitcnt vmcnt(6)\n\t" \
  "s_barrier\n\t" \
  "s_add_i32 s46, s44, -1\n\t" \
  "v_mov_b32 v156, s46\n\t" \
  "buffer_store_dword v156, v171, %[ru], 0 offen sc1\n\t" \
  "s_branch 42b\n\t" \
  "33:\n\t" \
  "s_waitcnt vmcnt(24)\n\t" \
  "s_branch 43b\n\t" \
  "91:\n\t" \
  "ds_read_b128 v[0:3], v164 offset:0\n\t" \
  "ds_read_b128 v[4:7], v164 offset:8192\n\t" \
  "ds_read_b128 v[8:11], v165 offset:0\n\t" \
  "ds_read_b128 v[12:15], v165 offset:8192\n\t" \
  "ds_read_b128 v[16:19], v166 offset:0\n\t" \
  "ds_read_b128 v[20:23], v166 offset:8192\n\t" \
  "ds_read_b128 v[24:27], v167 offset:0\n\t" \
  "ds_read_b128 v[28:31], v167 offset:8192\n\t" \
  "ds_read_b128 v[32:35], v164 offset:256\n\t" \
  "ds_read_b128 v[36:39], v164 offset:8448\n\t" \
  "ds_read_b128 v[40:43], v165 offset:256\n\t" \
  "ds_read_b128 v[44:47], v165 offset:8448\n\t" \
  "ds_read_b128 v[48:51], v166 offset:256\n\t" \
  "ds_read_b128 v[52:55], v166 offset:8448\n\t" \
  "ds_read_b128 v[56:59], v167 offset:256\n\t" \
  "ds_read_b128 v[60:63], v167 offset:8448\n\t" \
  "v_mov_b64 v[152:153], -1\n\t" \
  "v_mov_b64 v[154:155], -1\n\t" \
  "s_cmp_le_u32 s44, 12\n\t" \
  ".p2align 6\n\t" \
  "20:\n\t" \
  "s_waitcnt vmcnt(28) lgkmcnt(0)\n\t" \
  "v_mfma_f32_16x16x32_bf16 v[128:131], a[0:3], v[0:3], 0\n\t" \
  "s_cbranch_scc1 30f\n\t" \
  "40:\n\t" \
  "s_barrier\n\t" \
  "v_cvt_pk_bf16_f32 v148, v136, v137\n\t" \
  "v_cvt_pk_bf16_f32 v149, v138, v139\n\t" \
  "v_mfma_f32_16x16x32_bf16 v[132:135], a[4:7], v[0:3], 0\n\t" \
  "v_cvt_pk_bf16_f32 v150, v140, v141\n\t" \
  "v_cvt_pk_bf16_f32 v151, v142, v143\n\t" \
  "v_mfma_f32_16x16x32_bf16 v[136:139], a[0:3], v[4:7], 0\n\t" \
  "ds_read_b128 v[64:67], v164 offset:16384\n\t" \
  "v_mfma_f32_16x16x32_bf16 v[140:143], a[4:7], v[4:7], 0\n\t" \
  "s_add_i32 m0, s41, 0\n\t" \
  "v_mfma_f32_16x16x32_bf16 v[128:131], a[8:11], v[8:11], v[128:131]\n\t" \
  "buffer_load_dwordx4 v160, %[rx], s40 offen lds\n\t" \
  "v_mfma_f32_16x16x32_bf16 v[132:135], a[12:15], v[8:11], v[132:135]\n\t" \
  "ds_read_b128 v[68:71], v164 offset:24576\n\t" \
  "v_mfma_f32_16x16x32_bf16 v[136:139], a[8:11], v[12:15], v[136:139]\n\t" \
  "ds_read_b128 v[72:75], v165 offset:16384\n\t" \
  "v_mfma_f32_16x16x32_bf16 v[140:143], a[12:15], v[12:15], v[140:143]\n\t" \
  "ds_read_b128 v[76:79], v165 offset:24576\n\t" \
  "v_mfma_f32_16x16x32_bf16 v[128:131], a[16:19], v[16:19], v[128:131]\n\t" \
  "buffer_store_dwordx4 v[144:147], v168, %[ru], s42 offen sc1\n\t" \
  "v_mfma_f32_16x16x32_bf16 v[132:135], a[20:23], v[16:19], v[132:135]\n\t" \
  "ds_read_b128 v[80:83], v166 offset:16384\n\t" \
  "v_mfma_f32_16x16x32_bf16 v[136:139], a[16:19], v[20:23], v[136:139]\n\t" \
  "ds_read_b128 v[84:87], v166 offset:24576\n\t" \
  "v_mfma_f32_16x16x32_bf16 v[140:143], a[20:23], v[20:23], v[140:143]\n\t" \
  "s_add_i32 m0, s41, 1024\n\t" \
  "v_mfma_f32_16x16x32_bf16 v[128:131], a[24:27], v[24:27], v[128:131]\n\t" \
  "buffer_load_dwordx4 v161, %[rx], s40 offen lds\n\t" \
  "v_mfma_f32_16x16x32_bf16 v[132:135], a[28:31], v[24:27], v[132:135]\n\t" \
  "ds_read_b128 v[88:91], v167 offset:16384\n\t" \
  "v_mfma_f32_16x16x32_bf16 v[136:139], a[24:27], v[28:31], v[136:139]\n\t" \
  "ds_read_b128 v[92:95], v167 offset:24576\n\t" \
  "v_mfma_f32_16x16x32_bf16 v[140:143], a[28:31], v[28:31], v[140:143]\n\t" \
  "ds_read_b128 v[96:99], v164 offset:16640\n\t" \
  "v_mfma_f32_16x16x32_bf16 v[128:131], a[32:35], v[32:35], v[128:131]\n\t" \
  "buffer_store_dwordx4 v[148:151], v169, %[ru], s42 offen sc1\n\t" \
  "v_mfma_f32_16x16x32_bf16 v[132:135], a[36:39], v[32:35], v[132:135]\n\t" \
  "ds_read_b128 v[100:103], v164 offset:24832\n\t" \
  "v_mfma_f32_16x16x32_bf16 v[136:139], a[32:35], v[36:39], v[136:139]\n\t" \
  "ds_read_b128 v[104:107], v165 offset:16640\n\t" \
  "v_mfma_f32_16x16x32_bf16 v[140:143], a[36:39], v[36:39], v[140:143]\n\t" \
  "s_add_i32 m0, s41, 2048\n\t" \
  "v_mfma_f32_16x16x32_bf16 v[128:131], a[40:43], v[40:43], v[128:131]\n\t" \
  "buffer_load_dwordx4 v162, %[rx], s40 offen lds\n\t" \
  "v_mfma_f32_16x16x32_bf16 v[132:135], a[44:47], v[40:43], v[132:135]\n\t" \
  "ds_read_b128 v[108:111], v165 offset:24832\n\t" \
  "v_mfma_f32_16x16x32_bf16 v[136:139], a[40:43], v[44:47], v[136:139]\n\t" \
  "ds_read_b128 v[112:115], v166 offset:16640\n\t" \
  "v_mfma_f32_16x16x32_bf16 v[140:143], a[44:47], v[44:47], v[140:143]\n\t" \
  "s_add_i32 m0, s41, 3072\n\t" \
  "v_mfma_f32_16x16x32_bf16 v[128:131], a[48:51], v[48:51], v[128:131]\n\t" \
  "buffer_load_dwordx4 v163, %[rx], s40 offen lds\n\t" \
  "v_mfma_f32_16x16x32_bf16 v[132:135], a[52:55], v[48:51], v[132:135]\n\t" \
  "ds_read_b128 v[116:119], v166 offset:24832\n\t" \
  "v_mfma_f32_16x16x32_bf16 v[136:139], a[48:51], v[52:55], v[136:139]\n\t" \
  "ds_read_b128 v[120:123], v167 offset:16640\n\t" \
  "v_mfma_f32_16x16x32_bf16 v[140:143], a[52:55], v[52:55], v[140:143]\n\t" \
  "ds_read_b128 v[124:127], v167 offset:24832\n\t" \
  "v_mfma_f32_16x16x32_bf16 v[128:131], a[56:59], v[56:59], v[128:131]\n\t" \
  "v_mfma_f32_16x16x32_bf16 v[132:135], a[60:63], v[56:59], v[132:135]\n\t" \
  "v_mfma_f32_16x16x32_bf16 v[136:139], a[56:59], v[60:63], v[136:139]\n\t" \
  "v_mfma_f32_16x16x32_bf16 v[140:143], a[60:63], v[60:63], v[140:143]\n\t" \
  "s_cmp_le_u32 s44, 12\n\t" \
  "s_waitcnt vmcnt(30) lgkmcnt(0)\n\t" \
  "v_mfma_f32_16x16x32_bf16 v[128:131], a[64:67], v[64:67], v[128:131]\n\t" \
  "s_cbranch_scc1 31f\n\t" \
  "41:\n\t" \
  "s_barrier\n\t" \
  "v_mfma_f32_16x16x32_bf16 v[132:135], a[68:71], v[64:67], v[132:135]\n\t" \
  "s_add_i32 s76, s40, 512\n\t" \
  "v_mfma_f32_16x16x32_bf16 v[136:139], a[64:67], v[68:71], v[136:139]\n\t" \
  "ds_read_b128 v[0:3], v164 offset:32768\n\t" \
  "v_mfma_f32_16x16x32_bf16 v[140:143], a[68:71], v[68:71], v[140:143]\n\t" \
  "s_add_i32 m0, s41, 16384\n\t" \
  "v_mfma_f32_16x16x32_bf16 v[128:131], a[72:75], v[72:75], v[128:131]\n\t" \
  "buffer_load_dwordx4 v160, %[rx], s76 offen lds\n\t" \
  "v_mfma_f32_16x16x32_bf16 v[132:135], a[76:79], v[72:75], v[132:135]\n\t" \
  "ds_read_b128 v[4:7], v164 offset:40960\n\t" \
  "v_mfma_f32_16x16x32_bf16 v[136:139], a[72:75], v[76:79], v[136:139]\n\t" \
  "ds_read_b128 v[8:11], v165 offset:32768\n\t" \
  "v_mfma_f32_16x16x32_bf16 v[140:143], a[76:79], v[76:79], v[140:143]\n\t" \
  "ds_read_b128 v[12:15], v165 offset:40960\n\t" \
  "v_mfma_f32_16x16x32_bf16 v[128:131], a[80:83], v[80:83], v[128:131]\n\t" \
  "v_mfma_f32_16x16x32_bf16 v[132:135], a[84:87], v[80:83], v[132:135]\n\t" \
  "ds_read_b128 v[16:19], v166 offset:32768\n\t" \
  "v_mfma_f32_16x16x32_bf16 v[136:139], a[80:83], v[84:87], v[136:139]\n\t" \
  "ds_read_b128 v[20:23], v166 offset:40960\n\t" \
  "v_mfma_f32_16x16x32_bf16 v[140:143], a[84:87], v[84:87], v[140:143]\n\t" \
  "s_add_i32 m0, s41, 17408\n\t" \
  "v_mfma_f32_16x16x32_bf16 v[128:131], a[88:91], v[88:91], v[128:131]\n\t" \
  "buffer_load_dwordx4 v161, %[rx], s76 offen lds\n\t" \
  "v_mfma_f32_16x16x32_bf16 v[132:135], a[92:95], v[88:91], v[132:135]\n\t" \
  "ds_read_b128 v[24:27], v167 offset:32768\n\t" \
  "v_mfma_f32_16x16x32_bf16 v[136:139], a[88:91], v[92:95], v[136:139]\n\t" \
  "ds_read_b128 v[28:31], v167 offset:40960\n\t" \
  "v_mfma_f32_16x16x32_bf16 v[140:143], a[92:95], v[92:95], v[140:143]\n\t" \
  "ds_read_b128 v[32:35], v164 offset:33024\n\t" \
  "v_mfma_f32_16x16x32_bf16 v[128:131], a[96:99], v[96:99], v[128:131]\n\t" \
  "v_mfma_f32_16x16x32_bf16 v[132:135], a[100:103], v[96:99], v[132:135]\n\t" \
  "ds_read_b128 v[36:39], v164 offset:41216\n\t" \
  "v_mfma_f32_16x16x32_bf16 v[136:139], a[96:99], v[100:103], v[136:139]\n\t" \
  "ds_read_b128 v[40:43], v165 offset:33024\n\t" \
  "v_mfma_f32_16x16x32_bf16 v[140:143], a[100:103], v[100:103], v[140:143]\n\t" \
  "s_add_i32 m0, s41, 18432\n\t" \
  "v_mfma_f32_16x16x32_bf16 v[128:131], a[104:107], v[104:107], v[128:131]\n\t" \
  "buffer_load_dwordx4 v162, %[rx], s76 offen lds\n\t" \
  "v_mfma_f32_16x16x32_bf16 v[132:135], a[108:111], v[104:107], v[132:135]\n\t" \
  "ds_read_b128 v[44:47], v165 offset:41216\n\t" \
  "v_mfma_f32_16x16x32_bf16 v[136:139], a[104:107], v[108:111], v[136:139]\n\t" \
  "ds_read_b128 v[48:51], v166 offset:33024\n\t" \
  "v_mfma_f32_16x16x32_bf16 v[140:143], a[108:111], v[108:111], v[140:143]\n\t" \
  "s_add_i32 m0, s41, 19456\n\t" \
  "v_mfma_f32_16x16x32_bf16 v[128:131], a[112:115], v[112:115], v[128:131]\n\t" \
  "buffer_load_dwordx4 v163, %[rx], s76 offen lds\n\t" \
  "v_mfma_f32_16x16x32_bf16 v[132:135], a[116:119], v[112:115], v[132:135]\n\t" \
  "ds_read_b128 v[52:55], v166 offset:41216\n\t" \
  "v_mfma_f32_16x16x32_bf16 v[136:139], a[112:115], v[116:119], v[136:139]\n\t" \
  "ds_read_b128 v[56:59], v167 offset:33024\n\t" \
  "v_mfma_f32_16x16x32_bf16 v[140:143], a[116:119], v[116:119], v[140:143]\n\t" \
  "ds_read_b128 v[60:63], v167 offset:41216\n\t" \
  "v_mfma_f32_16x16x32_bf16 v[128:131], a[120:123], v[120:123], v[128:131]\n\t" \
  "s_lshr_b32 s79, s44, 2\n\t" \
  "v_mfma_f32_16x16x32_bf16 v[132:135], a[124:127], v[120:123], v[132:135]\n\t" \
  "s_bitcmp1_b32 %[hm], s79\n\t" \
  "s_cselect_b32 s79, 0x2000, 0\n\t" \
  "v_mfma_f32_16x16x32_bf16 v[136:139], a[120:123], v[124:127], v[136:139]\n\t" \
  "s_cmp_lt_u32 s44, 4\n\t" \
  "s_cselect_b32 s79, %[d0], s79\n\t" \
  "v_mfma_f32_16x16x32_bf16 v[140:143], a[124:127], v[124:127], v[140:143]\n\t" \
  "s_cmp_le_u32 s44, 12\n\t" \
  "s_waitcnt vmcnt(29) lgkmcnt(0)\n\t" \
  "v_mfma_f32_16x16x32_bf16 v[128:131], a[128:131], v[0:3], v[128:131]\n\t" \
  "s_cbranch_scc1 32f\n\t" \
  "42:\n\t" \
  "s_barrier\n\t" \
  "v_mfma_f32_16x16x32_bf16 v[132:135], a[132:135], v[0:3], v[132:135]\n\t" \
  "s_add_i32 s76, s40, 1024\n\t" \
  "v_mfma_f32_16x16x32_bf16 v[136:139], a[128:131], v[4:7], v[136:139]\n\t" \
  "ds_read_b128 v[64:67], v164 offset:49152\n\t" \
  "v_mfma_f32_16x16x32_bf16 v[140:143], a[132:135], v[4:7], v[140:143]\n\t" \
  "s_add_i32 m0, s41, 32768\n\t" \
  "v_mfma_f32_16x16x32_bf16 v[128:131], a[136:139], v[8:11], v[128:131]\n\t" \
  "buffer_load_dwordx4 v160, %[rx], s76 offen lds\n\t" \
  "v_mfma_f32_16x16x32_bf16 v[132:135], a[140:143], v[8:11], v[132:135]\n\t" \
  "ds_read_b128 v[68:71], v164 offset:57344\n\t" \
  "v_mfma_f32_16x16x32_bf16 v[136:139], a[136:139], v[12:15], v[136:139]\n\t" \
  "ds_read_b128 v[72:75], v165 offset:49152\n\t" \
  "v_mfma_f32_16x16x32_bf16 v[140:143], a[140:143], v[12:15], v[140:143]\n\t" \
  "ds_read_b128 v[76:79], v165 offset:57344\n\t" \
  "v_mfma_f32_16x16x32_bf16 v[128:131], a[144:147], v[16:19], v[128:131]\n\t" \
  "v_mfma_f32_16x16x32_bf16 v[132:135], a[148:151], v[16:19], v[132:135]\n\t" \
  "ds_read_b128 v[80:83], v166 offset:49152\n\t" \
  "v_mfma_f32_16x16x32_bf16 v[136:139], a[144:147], v[20:23], v[136:139]\n\t" \
  "ds_read_b128 v[84:87], v166 offset:57344\n\t" \
  "v_mfma_f32_16x16x32_bf16 v[140:143], a[148:151], v[20:23], v[140:143]\n\t" \
  "s_add_i32 m0, s41, 33792\n\t" \
  "v_mfma_f32_16x16x32_bf16 v[128:131], a[152:155], v[24:27], v[128:131]\n\t" \
  "buffer_load_dwordx4 v161, %[rx], s76 offen lds\n\t" \
  "v_mfma_f32_16x16x32_bf16 v[132:135], a[156:159], v[24:27], v[132:135]\n\t" \
  "ds_read_b128 v[88:91], v167 offset:49152\n\t" \
  "v_mfma_f32_16x16x32_bf16 v[136:139], a[152:155], v[28:31], v[136:139]\n\t" \
  "ds_read_b128 v[92:95], v167 offset:57344\n\t" \
  "v_mfma_f32_16x16x32_bf16 v[140:143], a[156:159], v[28:31], v[140:143]\n\t" \
  "ds_read_b128 v[96:99], v164 offset:49408\n\t" \
  "v_mfma_f32_16x16x32_bf16 v[128:131], a[160:163], v[32:35], v[128:131]\n\t" \
  "v_mfma_f32_16x16x32_bf16 v[132:135], a[164:167], v[32:35], v[132:135]\n\t" \
  "ds_read_b128 v[100:103], v164 offset:57600\n\t" \
  "v_mfma_f32_16x16x32_bf16 v[136:139], a[160:163], v[36:39], v[136:139]\n\t" \
  "ds_read_b128 v[104:107], v165 offset:49408\n\t" \
  "v_mfma_f32_16x16x32_bf16 v[140:143], a[164:167], v[36:39], v[140:143]\n\t" \
  "s_add_i32 m0, s41, 34816\n\t" \
  "v_mfma_f32_16x16x32_bf16 v[128:131], a[168:171], v[40:43], v[128:131]\n\t" \
  "buffer_load_dwordx4 v162, %[rx], s76 offen lds\n\t" \
  "v_mfma_f32_16x16x32_bf16 v[132:135], a[172:175], v[40:43], v[132:135]\n\t" \
  "ds_read_b128 v[108:111], v165 offset:57600\n\t" \
  "v_mfma_f32_16x16x32_bf16 v[136:139], a[168:171], v[44:47], v[136:139]\n\t" \
  "ds_read_b128 v[112:115], v166 offset:49408\n\t" \
  "v_mfma_f32_16x16x32_bf16 v[140:143], a[172:175], v[44:47], v[140:143]\n\t" \
  "s_add_i32 m0, s41, 35840\n\t" \
  "v_mfma_f32_16x16x32_bf16 v[128:131], a[176:179], v[48:51], v[128:131]\n\t" \
  "buffer_load_dwordx4 v163, %[rx], s76 offen lds\n\t" \
  "v_mfma_f32_16x16x32_bf16 v[132:135], a[180:183], v[48:51], v[132:135]\n\t" \
  "ds_read_b128 v[116:119], v166 offset:57600\n\t" \
  "v_mfma_f32_16x16x32_bf16 v[136:139], a[176:179], v[52:55], v[136:139]\n\t" \
  "ds_read_b128 v[120:123], v167 offset:49408\n\t" \
  "v_mfma_f32_16x16x32_bf16 v[140:143], a[180:183], v[52:55], v[140:143]\n\t" \
  "ds_read_b128 v[124:127], v167 offset:57600\n\t" \
  "v_mfma_f32_16x16x32_bf16 v[128:131], a[184:187], v[56:59], v[128:131]\n\t" \
  "s_add_i32 s78, s44, -2\n\t" \
  "v_mfma_f32_16x16x32_bf16 v[132:135], a[188:191], v[56:59], v[132:135]\n\t" \
  "s_max_i32 s78, s78, s46\n\t" \
  "v_mfma_f32_16x16x32_bf16 v[136:139], a[184:187], v[60:63], v[136:139]\n\t" \
  "v_mov_b32 v156, s78\n\t" \
  "v_mfma_f32_16x16x32_bf16 v[140:143], a[188:191], v[60:63], v[140:143]\n\t" \
  "s_cmp_le_u32 s44, 12\n\t" \
  "s_waitcnt vmcnt(27) lgkmcnt(0)\n\t" \
  "v_mfma_f32_16x16x32_bf16 v[128:131], a[192:195], v[64:67], v[128:131]\n\t" \
  "s_cbranch_scc1 33f\n\t" \
  "43:\n\t" \
  "s_barrier\n\t" \
  "v_xor_b32 v164, 0x10000, v164\n\t" \
  "v_mfma_f32_16x16x32_bf16 v[132:135], a[196:199], v[64:67], v[132:135]\n\t" \
  "s_add_i32 s76, s40, 1536\n\t" \
  "buffer_store_dword v156, v171, %[ru], 0 offen sc1\n\t" \
  "v_mfma_f32_16x16x32_bf16 v[136:139], a[192:195], v[68:71], v[136:139]\n\t" \
  "v_xor_b32 v165, 0x10000, v165\n\t" \
  "ds_read_b128 v[0:3], v164 offset:0\n\t" \
  "v_mfma_f32_16x16x32_bf16 v[140:143], a[196:199], v[68:71], v[140:143]\n\t" \
  "s_add_i32 m0, s41, 49152\n\t" \
  "v_xor_b32 v166, 0x10000, v166\n\t" \
  "v_mfma_f32_16x16x32_bf16 v[128:131], a[200:203], v[72:75], v[128:131]\n\t" \
  "buffer_load_dwordx4 v160, %[rx], s76 offen lds\n\t" \
  "v_xor_b32 v167, 0x10000, v167\n\t" \
  "v_mfma_f32_16x16x32_bf16 v[132:135], a[204:207], v[72:75], v[132:135]\n\t" \
  "ds_read_b128 v[4:7], v164 offset:8192\n\t" \
  "v_mfma_f32_16x16x32_bf16 v[136:139], a[200:203], v[76:79], v[136:139]\n\t" \
  "ds_read_b128 v[8:11], v165 offset:0\n\t" \
  "v_mfma_f32_16x16x32_bf16 v[140:143], a[204:207], v[76:79], v[140:143]\n\t" \
  "ds_read_b128 v[12:15], v165 offset:8192\n\t" \
  "v_mfma_f32_16x16x32_bf16 v[128:131], a[208:211], v[80:83], v[128:131]\n\t" \
  "v_mfma_f32_16x16x32_bf16 v[132:135], a[212:215], v[80:83], v[132:135]\n\t" \
  "ds_read_b128 v[16:19], v166 offset:0\n\t" \
  "v_mfma_f32_16x16x32_bf16 v[136:139], a[208:211], v[84:87], v[136:139]\n\t" \
  "ds_read_b128 v[20:23], v166 offset:8192\n\t" \
  "v_mfma_f32_16x16x32_bf16 v[140:143], a[212:215], v[84:87], v[140:143]\n\t" \
  "s_add_i32 m0, s41, 50176\n\t" \
  "ds_read_b128 v[24:27], v167 offset:0\n\t" \
  "v_mfma_f32_16x16x32_bf16 v[128:131], a[216:219], v[88:91], v[128:131]\n\t" \
  "buffer_load_dwordx4 v161, %[rx], s76 offen lds\n\t" \
  "v_mfma_f32_16x16x32_bf16 v[132:135], a[220:223], v[88:91], v[132:135]\n\t" \
  "ds_read_b128 v[28:31], v167 offset:8192\n\t" \
  "v_mfma_f32_16x16x32_bf16 v[136:139], a[216:219], v[92:95], v[136:139]\n\t" \
  "ds_read_b128 v[32:35], v164 offset:256\n\t" \
  "v_mfma_f32_16x16x32_bf16 v[140:143], a[220:223], v[92:95], v[140:143]\n\t" \
  "ds_read_b128 v[36:39], v164 offset:8448\n\t" \
  "v_mfma_f32_16x16x32_bf16 v[128:131], a[224:227], v[96:99], v[128:131]\n\t" \
  "v_mfma_f32_16x16x32_bf16 v[132:135], a[228:231], v[96:99], v[132:135]\n\t" \
  "ds_read_b128 v[40:43], v165 offset:256\n\t" \
  "v_mfma_f32_16x16x32_bf16 v[136:139], a[224:227], v[100:103], v[136:139]\n\t" \
  "ds_read_b128 v[44:47], v165 offset:8448\n\t" \
  "v_mfma_f32_16x16x32_bf16 v[140:143], a[228:231], v[100:103], v[140:143]\n\t" \
  "s_add_i32 m0, s41, 51200\n\t" \
  "v_mfma_f32_16x16x32_bf16 v[128:131], a[232:235], v[104:107], v[128:131]\n\t" \
  "buffer_load_dwordx4 v162, %[rx], s76 offen lds\n\t" \
  "v_mfma_f32_16x16x32_bf16 v[132:135], a[236:239], v[104:107], v[132:135]\n\t" \
  "ds_read_b128 v[48:51], v166 offset:256\n\t" \
  "v_mfma_f32_16x16x32_bf16 v[136:139], a[232:235], v[108:111], v[136:139]\n\t" \
  "ds_read_b128 v[52:55], v166 offset:8448\n\t" \
  "v_mfma_f32_16x16x32_bf16 v[140:143], a[236:239], v[108:111], v[140:143]\n\t" \
  "s_add_i32 m0, s41, 52224\n\t" \
  "v_mfma_f32_16x16x32_bf16 v[128:131], a[240:243], v[112:115], v[128:131]\n\t" \
  "buffer_load_dwordx4 v163, %[rx], s76 offen lds\n\t" \
  "v_mfma_f32_16x16x32_bf16 v[132:135], a[244:247], v[112:115], v[132:135]\n\t" \
  "ds_read_b128 v[56:59], v167 offset:256\n\t" \
  "v_mfma_f32_16x16x32_bf16 v[136:139], a[240:243], v[116:119], v[136:139]\n\t" \
  "ds_read_b128 v[60:63], v167 offset:8448\n\t" \
  "v_mfma_f32_16x16x32_bf16 v[140:143], a[244:247], v[116:119], v[140:143]\n\t" \
  "s_add_i32 s44, s44, 1\n\t" \
  "s_add_i32 s42, s43, s79\n\t" \
  "v_mfma_f32_16x16x32_bf16 v[128:131], a[248:251], v[120:123], v[128:131]\n\t" \
  "s_add_i32 s43, s43, 0x80000\n\t" \
  "s_add_i32 s40, s40, 0x10000\n\t" \
  "v_mfma_f32_16x16x32_bf16 v[132:135], a[252:255], v[120:123], v[132:135]\n\t" \
  "s_xor_b32 s41, s41, 0x10000\n\t" \
  "v_mfma_f32_16x16x32_bf16 v[136:139], a[248:251], v[124:127], v[136:139]\n\t" \
  "v_cvt_pk_bf16_f32 v144, v128, v129\n\t" \
  "v_cvt_pk_bf16_f32 v145, v130, v131\n\t" \
  "v_mfma_f32_16x16x32_bf16 v[140:143], a[252:255], v[124:127], v[140:143]\n\t" \
  "v_cvt_pk_bf16_f32 v146, v132, v133\n\t" \
  "v_cvt_pk_bf16_f32 v147, v134, v135\n\t" \
  "s_cmp_eq_u32 s44, 64\n\t" \
  "s_cbranch_scc1 98f\n\t" \
  "s_cmp_le_u32 s44, 12\n\t" \
  "s_branch 20b\n\t" \
  "30:\n\t" \
  "s_waitcnt vmcnt(24)\n\t" \
  "s_branch 40b\n\t" \
  "31:\n\t" \
  "s_waitcnt vmcnt(24)\n\t" \
  "s_branch 41b\n\t" \
  "32:\n\t" \
  "s_waitcnt vmcnt(6)\n\t" \
  "s_barrier\n\t" \
  "s_add_i32 s46, s44, -1\n\t" \
  "v_mov_b32 v156, s46\n\t" \
  "buffer_store_dword v156, v171, %[ru], 0 offen sc1\n\t" \
  "s_branch 42b\n\t" \
  "33:\n\t" \
  "s_waitcnt vmcnt(24)\n\t" \
  "s_branch 43b\n\t" \
  "92:\n\t" \
  "ds_read_b128 v[0:3], v164 offset:0\n\t" \
  "ds_read_b128 v[4:7], v164 offset:8192\n\t" \
  "ds_read_b128 v[8:11], v165 offset:0\n\t" \
  "ds_read_b128 v[12:15], v165 offset:8192\n\t" \
  "ds_read_b128 v[16:19], v166 offset:0\n\t" \
  "ds_read_b128 v[20:23], v166 offset:8192\n\t" \
  "ds_read_b128 v[24:27], v167 offset:0\n\t" \
  "ds_read_b128 v[28:31], v167 offset:8192\n\t" \
  "ds_read_b128 v[32:35], v164 offset:256\n\t" \
  "ds_read_b128 v[36:39], v164 offset:8448\n\t" \
  "ds_read_b128 v[40:43], v165 offset:256\n\t" \
  "ds_read_b128 v[44:47], v165 offset:8448\n\t" \
  "ds_read_b128 v[48:51], v166 offset:256\n\t" \
  "ds_read_b128 v[52:55], v166 offset:8448\n\t" \
  "ds_read_b128 v[56:59], v167 offset:256\n\t" \
  "ds_read_b128 v[60:63], v167 offset:8448\n\t" \
  "v_mov_b64 v[152:153], -1\n\t" \
  "v_mov_b64 v[154:155], -1\n\t" \
  "s_cmp_le_u32 s44, 12\n\t" \
  ".p2align 6\n\t" \
  "20:\n\t" \
  "s_waitcnt vmcnt(28) lgkmcnt(0)\n\t" \
  "v_mfma_f32_16x16x32_bf16 v[128:131], a[0:3], v[0:3], 0\n\t" \
  "s_cbranch_scc1 30f\n\t" \
  "40:\n\t" \
  "s_barrier\n\t" \
  "v_cvt_pk_bf16_f32 v148, v136, v137\n\t" \
  "v_cvt_pk_bf16_f32 v149, v138, v139\n\t" \
  "v_mfma_f32_16x16x32_bf16 v[132:135], a[4:7], v[0:3], 0\n\t" \
  "v_cvt_pk_bf16_f32 v150, v140, v141\n\t" \
  "v_cvt_pk_bf16_f32 v151, v142, v143\n\t" \
  "v_mfma_f32_16x16x32_bf16 v[136:139], a[0:3], v[4:7], 0\n\t" \
  "ds_read_b128 v[64:67], v164 offset:16384\n\t" \
  "v_mfma_f32_16x16x32_bf16 v[140:143], a[4:7], v[4:7], 0\n\t" \
  "ds_read_b128 v[68:71], v164 offset:24576\n\t" \
  "v_mfma_f32_16x16x32_bf16 v[128:131], a[8:11], v[8:11], v[128:131]\n\t" \
  "s_add_i32 m0, s41, 0\n\t" \
  "v_mfma_f32_16x16x32_bf16 v[132:135], a[12:15], v[8:11], v[132:135]\n\t" \
  "buffer_load_dwordx4 v160, %[rx], s40 offen lds\n\t" \
  "v_mfma_f32_16x16x32_bf16 v[136:139], a[8:11], v[12:15], v[136:139]\n\t" \
  "ds_read_b128 v[72:75], v165 offset:16384\n\t" \
  "v_mfma_f32_16x16x32_bf16 v[140:143], a[12:15], v[12:15], v[140:143]\n\t" \
  "ds_read_b128 v[76:79], v165 offset:24576\n\t" \
  "v_mfma_f32_16x16x32_bf16 v[128:131], a[16:19], v[16:19], v[128:131]\n\t" \
  "ds_read_b128 v[80:83], v166 offset:16384\n\t" \
  "v_mfma_f32_16x16x32_bf16 v[132:135], a[20:23], v[16:19], v[132:135]\n\t" \
  "buffer_store_dwordx4 v[144:147], v168, %[ru], s42 offen sc1\n\t" \
  "v_mfma_f32_16x16x32_bf16 v[136:139], a[16:19], v[20:23], v[136:139]\n\t" \
  "ds_read_b128 v[84:87], v166 offset:24576\n\t" \
  "v_mfma_f32_16x16x32_bf16 v[140:143], a[20:23], v[20:23], v[140:143]\n\t" \
  "ds_read_b128 v[88:91], v167 offset:16384\n\t" \
  "v_mfma_f32_16x16x32_bf16 v[128:131], a[24:27], v[24:27], v[128:131]\n\t" \
  "s_add_i32 m0, s41, 1024\n\t" \
  "v_mfma_f32_16x16x32_bf16 v[132:135], a[28:31], v[24:27], v[132:135]\n\t" \
  "buffer_load_dwordx4 v161, %[rx], s40 offen lds\n\t" \
  "v_mfma_f32_16x16x32_bf16 v[136:139], a[24:27], v[28:31], v[136:139]\n\t" \
  "ds_read_b128 v[92:95], v167 offset:24576\n\t" \
  "v_mfma_f32_16x16x32_bf16 v[140:143], a[28:31], v[28:31], v[140:143]\n\t" \
  "ds_read_b128 v[96:99], v164 offset:16640\n\t" \
  "v_mfma_f32_16x16x32_bf16 v[128:131], a[32:35], v[32:35], v[128:131]\n\t" \
  "ds_read_b128 v[100:103], v164 offset:24832\n\t" \
  "v_mfma_f32_16x16x32_bf16 v[132:135], a[36:39], v[32:35], v[132:135]\n\t" \
  "buffer_store_dwordx4 v[148:151], v169, %[ru], s42 offen sc1\n\t" \
  "v_mfma_f32_16x16x32_bf16 v[136:139], a[32:35], v[36:39], v[136:139]\n\t" \
  "ds_read_b128 v[104:107], v165 offset:16640\n\t" \
  "v_mfma_f32_16x16x32_bf16 v[140:143], a[36:39], v[36:39], v[140:143]\n\t" \
  "ds_read_b128 v[108:111], v165 offset:24832\n\t" \
  "v_mfma_f32_16x16x32_bf16 v[128:131], a[40:43], v[40:43], v[128:131]\n\t" \
  "s_add_i32 m0, s41, 2048\n\t" \
  "v_mfma_f32_16x16x32_bf16 v[132:135], a[44:47], v[40:43], v[132:135]\n\t" \
  "buffer_load_dwordx4 v162, %[rx], s40 offen lds\n\t" \
  "v_mfma_f32_16x16x32_bf16 v[136:139], a[40:43], v[44:47], v[136:139]\n\t" \
  "ds_read_b128 v[112:115], v166 offset:16640\n\t" \
  "v_mfma_f32_16x16x32_bf16 v[140:143], a[44:47], v[44:47], v[140:143]\n\t" \
  "ds_read_b128 v[116:119], v166 offset:24832\n\t" \
  "v_mfma_f32_16x16x32_bf16 v[128:131], a[48:51], v[48:51], v[128:131]\n\t" \
  "s_add_i32 m0, s41, 3072\n\t" \
  "v_mfma_f32_16x16x32_bf16 v[132:135], a[52:55], v[48:51], v[132:135]\n\t" \
  "buffer_load_dwordx4 v163, %[rx], s40 offen lds\n\t" \
  "v_mfma_f32_16x16x32_bf16 v[136:139], a[48:51], v[52:55], v[136:139]\n\t" \
  "ds_read_b128 v[120:123], v167 offset:16640\n\t" \
  "v_mfma_f32_16x16x32_bf16 v[140:143], a[52:55], v[52:55], v[140:143]\n\t" \
  "ds_read_b128 v[124:127], v167 offset:24832\n\t" \
  "v_mfma_f32_16x16x32_bf16 v[128:131], a[56:59], v[56:59], v[128:131]\n\t" \
  "v_mfma_f32_16x16x32_bf16 v[132:135], a[60:63], v[56:59], v[132:135]\n\t" \
  "v_mfma_f32_16x16x32_bf16 v[136:139], a[56:59], v[60:63], v[136:139]\n\t" \
  "v_mfma_f32_16x16x32_bf16 v[140:143], a[60:63], v[60:63], v[140:143]\n\t" \
  "s_cmp_le_u32 s44, 12\n\t" \
  "s_waitcnt vmcnt(30) lgkmcnt(0)\n\t" \
  "v_mfma_f32_16x16x32_bf16 v[128:131], a[64:67], v[64:67], v[128:131]\n\t" \
  "s_cbranch_scc1 31f\n\t" \
  "41:\n\t" \
  "s_barrier\n\t" \
  "v_mfma_f32_16x16x32_bf16 v[132:135], a[68:71], v[64:67], v[132:135]\n\t" \
  "s_add_i32 s76, s40, 512\n\t" \
  "v_mfma_f32_16x16x32_bf16 v[136:139], a[64:67], v[68:71], v[136:139]\n\t" \
  "ds_read_b128 v[0:3], v164 offset:32768\n\t" \
  "v_mfma_f32_16x16x32_bf16 v[140:143], a[68:71], v[68:71], v[140:143]\n\t" \
  "ds_read_b128 v[4:7], v164 offset:40960\n\t" \
  "v_mfma_f32_16x16x32_bf16 v[128:131], a[72:75], v[72:75], v[128:131]\n\t" \
  "s_add_i32 m0, s41, 16384\n\t" \
  "v_mfma_f32_16x16x32_bf16 v[132:135], a[76:79], v[72:75], v[132:135]\n\t" \
  "buffer_load_dwordx4 v160, %[rx], s76 offen lds\n\t" \
  "v_mfma_f32_16x16x32_bf16 v[136:139], a[72:75], v[76:79], v[136:139]\n\t" \
  "ds_read_b128 v[8:11], v165 offset:32768\n\t" \
  "v_mfma_f32_16x16x32_bf16 v[140:143], a[76:79], v[76:79], v[140:143]\n\t" \
  "ds_read_b128 v[12:15], v165 offset:40960\n\t" \
  "v_mfma_f32_16x16x32_bf16 v[128:131], a[80:83], v[80:83], v[128:131]\n\t" \
  "ds_read_b128 v[16:19], v166 offset:32768\n\t" \
  "v_mfma_f32_16x16x32_bf16 v[132:135], a[84:87], v[80:83], v[132:135]\n\t" \
  "v_mfma_f32_16x16x32_bf16 v[136:139], a[80:83], v[84:87], v[136:139]\n\t" \
  "ds_read_b128 v[20:23], v166 offset:40960\n\t" \
  "v_mfma_f32_16x16x32_bf16 v[140:143], a[84:87], v[84:87], v[140:143]\n\t" \
  "ds_read_b128 v[24:27], v167 offset:32768\n\t" \
  "v_mfma_f32_16x16x32_bf16 v[128:131], a[88:91], v[88:91], v[128:131]\n\t" \
  "s_add_i32 m0, s41, 17408\n\t" \
  "v_mfma_f32_16x16x32_bf16 v[132:135], a[92:95], v[88:91], v[132:135]\n\t" \
  "buffer_load_dwordx4 v161, %[rx], s76 offen lds\n\t" \
  "v_mfma_f32_16x16x32_bf16 v[136:139], a[88:91], v[92:95], v[136:139]\n\t" \
  "ds_read_b128 v[28:31], v167 offset:40960\n\t" \
  "v_mfma_f32_16x16x32_bf16 v[140:143], a[92:95], v[92:95], v[140:143]\n\t" \
  "ds_read_b128 v[32:35], v164 offset:33024\n\t" \
  "v_mfma_f32_16x16x32_bf16 v[128:131], a[96:99], v[96:99], v[128:131]\n\t" \
  "ds_read_b128 v[36:39], v164 offset:41216\n\t" \
  "v_mfma_f32_16x16x32_bf16 v[132:135], a[100:103], v[96:99], v[132:135]\n\t" \
  "v_mfma_f32_16x16x32_bf16 v[136:139], a[96:99], v[100:103], v[136:139]\n\t" \
  "ds_read_b128 v[40:43], v165 offset:33024\n\t" \
  "v_mfma_f32_16x16x32_bf16 v[140:143], a[100:103], v[100:103], v[140:143]\n\t" \
  "ds_read_b128 v[44:47], v165 offset:41216\n\t" \
  "v_mfma_f32_16x16x32_bf16 v[128:131], a[104:107], v[104:107], v[128:131]\n\t" \
  "s_add_i32 m0, s41, 18432\n\t" \
  "v_mfma_f32_16x16x32_bf16 v[132:135], a[108:111], v[104:107], v[132:135]\n\t" \
  "buffer_load_dwordx4 v162, %[rx], s76 offen lds\n\t" \
  "v_mfma_f32_16x16x32_bf16 v[136:139], a[104:107], v[108:111], v[136:139]\n\t" \
  "ds_read_b128 v[48:51], v166 offset:33024\n\t" \
  "v_mfma_f32_16x16x32_bf16 v[140:143], a[108:111], v[108:111], v[140:143]\n\t" \
  "ds_read_b128 v[52:55], v166 offset:41216\n\t" \
  "v_mfma_f32_16x16x32_bf16 v[128:131], a[112:115], v[112:115], v[128:131]\n\t" \
  "s_add_i32 m0, s41, 19456\n\t" \
  "v_mfma_f32_16x16x32_bf16 v[132:135], a[116:119], v[112:115], v[132:135]\n\t" \
  "buffer_load_dwordx4 v163, %[rx], s76 offen lds\n\t" \
  "v_mfma_f32_16x16x32_bf16 v[136:139], a[112:115], v[116:119], v[136:139]\n\t" \
  "ds_read_b128 v[56:59], v167 offset:33024\n\t" \
  "v_mfma_f32_16x16x32_bf16 v[140:143], a[116:119], v[116:119], v[140:143]\n\t" \
  "ds_read_b128 v[60:63], v167 offset:41216\n\t" \
  "v_mfma_f32_16x16x32_bf16 v[128:131], a[120:123], v[120:123], v[128:131]\n\t" \
  "s_lshr_b32 s79, s44, 2\n\t" \
  "v_mfma_f32_16x16x32_bf16 v[132:135], a[124:127], v[120:123], v[132:135]\n\t" \
  "s_bitcmp1_b32 %[hm], s79\n\t" \
  "s_cselect_b32 s79, 0x2000, 0\n\t" \
  "v_mfma_f32_16x16x32_bf16 v[136:139], a[120:123], v[124:127], v[136:139]\n\t" \
  "s_cmp_lt_u32 s44, 4\n\t" \
  "s_cselect_b32 s79, %[d0], s79\n\t" \
  "v_mfma_f32_16x16x32_bf16 v[140:143], a[124:127], v[124:127], v[140:143]\n\t" \
  "s_cmp_le_u32 s44, 12\n\t" \
  "s_waitcnt vmcnt(29) lgkmcnt(0)\n\t" \
  "v_mfma_f32_16x16x32_bf16 v[128:131], a[128:131], v[0:3], v[128:131]\n\t" \
  "s_cbranch_scc1 32f\n\t" \
  "42:\n\t" \
  "s_barrier\n\t" \
  "v_mfma_f32_16x16x32_bf16 v[132:135], a[132:135], v[0:3], v[132:135]\n\t" \
  "s_add_i32 s76, s40, 1024\n\t" \
  "v_mfma_f32_16x16x32_bf16 v[136:139], a[128:131], v[4:7], v[136:139]\n\t" \
  "ds_read_b128 v[64:67], v164 offset:49152\n\t" \
  "v_mfma_f32_16x16x32_bf16 v[140:143], a[132:135], v[4:7], v[140:143]\n\t" \
  "ds_read_b128 v[68:71], v164 offset:57344\n\t" \
  "v_mfma_f32_16x16x32_bf16 v[128:131], a[136:139], v[8:11], v[128:131]\n\t" \
  "s_add_i32 m0, s41, 32768\n\t" \
  "v_mfma_f32_16x16x32_bf16 v[132:135], a[140:143], v[8:11], v[132:135]\n\t" \
  "buffer_load_dwordx4 v160, %[rx], s76 offen lds\n\t" \
  "v_mfma_f32_16x16x32_bf16 v[136:139], a[136:139], v[12:15], v[136:139]\n\t" \
  "ds_read_b128 v[72:75], v165 offset:49152\n\t" \
  "v_mfma_f32_16x16x32_bf16 v[140:143], a[140:143], v[12:15], v[140:143]\n\t" \
  "ds_read_b128 v[76:79], v165 offset:57344\n\t" \
  "v_mfma_f32_16x16x32_bf16 v[128:131], a[144:147], v[16:19], v[128:131]\n\t" \
  "ds_read_b128 v[80:83], v166 offset:49152\n\t" \
  "v_mfma_f32_16x16x32_bf16 v[132:135], a[148:151], v[16:19], v[132:135]\n\t" \
  "v_mfma_f32_16x16x32_bf16 v[136:139], a[144:147], v[20:23], v[136:139]\n\t" \
  "ds_read_b128 v[84:87], v166 offset:57344\n\t" \
  "v_mfma_f32_16x16x32_bf16 v[140:143], a[148:151], v[20:23], v[140:143]\n\t" \
  "ds_read_b128 v[88:91], v167 offset:49152\n\t" \
  "v_mfma_f32_16x16x32_bf16 v[128:131], a[152:155], v[24:27], v[128:131]\n\t" \
  "s_add_i32 m0, s41, 33792\n\t" \
  "v_mfma_f32_16x16x32_bf16 v[132:135], a[156:159], v[24:27], v[132:135]\n\t" \
  "buffer_load_dwordx4 v161, %[rx], s76 offen lds\n\t" \
  "v_mfma_f32_16x16x32_bf16 v[136:139], a[152:155], v[28:31], v[136:139]\n\t" \
  "ds_read_b128 v[92:95], v167 offset:57344\n\t" \
  "v_mfma_f32_16x16x32_bf16 v[140:143], a[156:159], v[28:31], v[140:143]\n\t" \
  "ds_read_b128 v[96:99], v164 offset:49408\n\t" \
  "v_mfma_f32_16x16x32_bf16 v[128:131], a[160:163], v[32:35], v[128:131]\n\t" \
  "ds_read_b128 v[100:103], v164 offset:57600\n\t" \
  "v_mfma_f32_16x16x32_bf16 v[132:135], a[164:167], v[32:35], v[132:135]\n\t" \
  "v_mfma_f32_16x16x32_bf16 v[136:139], a[160:163], v[36:39], v[136:139]\n\t" \
  "ds_read_b128 v[104:107], v165 offset:49408\n\t" \
  "v_mfma_f32_16x16x32_bf16 v[140:143], a[164:167], v[36:39], v[140:143]\n\t" \
  "ds_read_b128 v[108:111], v165 offset:57600\n\t" \
  "v_mfma_f32_16x16x32_bf16 v[128:131], a[168:171], v[40:43], v[128:131]\n\t" \
  "s_add_i32 m0, s41, 34816\n\t" \
  "v_mfma_f32_16x16x32_bf16 v[132:135], a[172:175], v[40:43], v[132:135]\n\t" \
  "buffer_load_dwordx4 v162, %[rx], s76 offen lds\n\t" \
  "v_mfma_f32_16x16x32_bf16 v[136:139], a[168:171], v[44:47], v[136:139]\n\t" \
  "ds_read_b128 v[112:115], v166 offset:49408\n\t" \
  "v_mfma_f32_16x16x32_bf16 v[140:143], a[172:175], v[44:47], v[140:143]\n\t" \
  "ds_read_b128 v[116:119], v166 offset:57600\n\t" \
  "v_mfma_f32_16x16x32_bf16 v[128:131], a[176:179], v[48:51], v[128:131]\n\t" \
  "s_add_i32 m0, s41, 35840\n\t" \
  "v_mfma_f32_16x16x32_bf16 v[132:135], a[180:183], v[48:51], v[132:135]\n\t" \
  "buffer_load_dwordx4 v163, %[rx], s76 offen lds\n\t" \
  "v_mfma_f32_16x16x32_bf16 v[136:139], a[176:179], v[52:55], v[136:139]\n\t" \
  "ds_read_b128 v[120:123], v167 offset:49408\n\t" \
  "v_mfma_f32_16x16x32_bf16 v[140:143], a[180:183], v[52:55], v[140:143]\n\t" \
  "ds_read_b128 v[124:127], v167 offset:57600\n\t" \
  "v_mfma_f32_16x16x32_bf16 v[128:131], a[184:187], v[56:59], v[128:131]\n\t" \
  "s_add_i32 s78, s44, -2\n\t" \
  "v_mfma_f32_16x16x32_bf16 v[132:135], a[188:191], v[56:59], v[132:135]\n\t" \
  "s_max_i32 s78, s78, s46\n\t" \
  "v_mfma_f32_16x16x32_bf16 v[136:139], a[184:187], v[60:63], v[136:139]\n\t" \
  "v_mov_b32 v156, s78\n\t" \
  "v_mfma_f32_16x16x32_bf16 v[140:143], a[188:191], v[60:63], v[140:143]\n\t" \
  "s_cmp_le_u32 s44, 12\n\t" \
  "s_waitcnt vmcnt(27) lgkmcnt(0)\n\t" \
  "v_mfma_f32_16x16x32_bf16 v[128:131], a[192:195], v[64:67], v[128:131]\n\t" \
  "s_cbranch_scc1 33f\n\t" \
  "43:\n\t" \
  "s_barrier\n\t" \
  "v_xor_b32 v164, 0x10000, v164\n\t" \
  "v_mfma_f32_16x16x32_bf16 v[132:135], a[196:199], v[64:67], v[132:135]\n\t" \
  "s_add_i32 s76, s40, 1536\n\t" \
  "buffer_store_dword v156, v171, %[ru], 0 offen sc1\n\t" \
  "v_mfma_f32_16x16x32_bf16 v[136:139], a[192:195], v[68:71], v[136:139]\n\t" \
  "v_xor_b32 v165, 0x10000, v165\n\t" \
  "ds_read_b128 v[0:3], v164 offset:0\n\t" \
  "v_mfma_f32_16x16x32_bf16 v[140:143], a[196:199], v[68:71], v[140:143]\n\t" \
  "v_xor_b32 v166, 0x10000, v166\n\t" \
  "ds_read_b128 v[4:7], v164 offset:8192\n\t" \
  "v_mfma_f32_16x16x32_bf16 v[128:131], a[200:203], v[72:75], v[128:131]\n\t" \
  "s_add_i32 m0, s41, 49152\n\t" \
  "v_xor_b32 v167, 0x10000, v167\n\t" \
  "v_mfma_f32_16x16x32_bf16 v[132:135], a[204:207], v[72:75], v[132:135]\n\t" \
  "buffer_load_dwordx4 v160, %[rx], s76 offen lds\n\t" \
  "v_mfma_f32_16x16x32_bf16 v[136:139], a[200:203], v[76:79], v[136:139]\n\t" \
  "ds_read_b128 v[8:11], v165 offset:0\n\t" \
  "v_mfma_f32_16x16x32_bf16 v[140:143], a[204:207], v[76:79], v[140:143]\n\t" \
  "ds_read_b128 v[12:15], v165 offset:8192\n\t" \
  "v_mfma_f32_16x16x32_bf16 v[128:131], a[208:211], v[80:83], v[128:131]\n\t" \
  "ds_read_b128 v[16:19], v166 offset:0\n\t" \
  "v_mfma_f32_16x16x32_bf16 v[132:135], a[212:215], v[80:83], v[132:135]\n\t" \
  "v_mfma_f32_16x16x32_bf16 v[136:139], a[208:211], v[84:87], v[136:139]\n\t" \
  "ds_read_b128 v[20:23], v166 offset:8192\n\t" \
  "v_mfma_f32_16x16x32_bf16 v[140:143], a[212:215], v[84:87], v[140:143]\n\t" \
  "ds_read_b128 v[24:27], v167 offset:0\n\t" \
  "v_mfma_f32_16x16x32_bf16 v[128:131], a[216:219], v[88:91], v[128:131]\n\t" \
  "s_add_i32 m0, s41, 50176\n\t" \
  "ds_read_b128 v[28:31], v167 offset:8192\n\t" \
  "v_mfma_f32_16x16x32_bf16 v[132:135], a[220:223], v[88:91], v[132:135]\n\t" \
  "buffer_load_dwordx4 v161, %[rx], s76 offen lds\n\t" \
  "v_mfma_f32_16x16x32_bf16 v[136:139], a[216:219], v[92:95], v[136:139]\n\t" \
  "ds_read_b128 v[32:35], v164 offset:256\n\t" \
  "v_mfma_f32_16x16x32_bf16 v[140:143], a[220:223], v[92:95], v[140:143]\n\t" \
  "ds_read_b128 v[36:39], v164 offset:8448\n\t" \
  "v_mfma_f32_16x16x32_bf16 v[128:131], a[224:227], v[96:99], v[128:131]\n\t" \
  "ds_read_b128 v[40:43], v165 offset:256\n\t" \
  "v_mfma_f32_16x16x32_bf16 v[132:135], a[228:231], v[96:99], v[132:135]\n\t" \
  "v_mfma_f32_16x16x32_bf16 v[136:139], a[224:227], v[100:103], v[136:139]\n\t" \
  "ds_read_b128 v[44:47], v165 offset:8448\n\t" \
  "v_mfma_f32_16x16x32_bf16 v[140:143], a[228:231], v[100:103], v[140:143]\n\t" \
  "ds_read_b128 v[48:51], v166 offset:256\n\t" \
  "v_mfma_f32_16x16x32_bf16 v[128:131], a[232:235], v[104:107], v[128:131]\n\t" \
  "s_add_i32 m0, s41, 51200\n\t" \
  "v_mfma_f32_16x16x32_bf16 v[132:135], a[236:239], v[104:107], v[132:135]\n\t" \
  "buffer_load_dwordx4 v162, %[rx], s76 offen lds\n\t" \
  "v_mfma_f32_16x16x32_bf16 v[136:139], a[232:235], v[108:111], v[136:139]\n\t" \
  "ds_read_b128 v[52:55], v166 offset:8448\n\t" \
  "v_mfma_f32_16x16x32_bf16 v[140:143], a[236:239], v[108:111], v[140:143]\n\t" \
  "ds_read_b128 v[56:59], v167 offset:256\n\t" \
  "v_mfma_f32_16x16x32_bf16 v[128:131], a[240:243], v[112:115], v[128:131]\n\t" \
  "s_add_i32 m0, s41, 52224\n\t" \
  "v_mfma_f32_16x16x32_bf16 v[132:135], a[244:247], v[112:115], v[132:135]\n\t" \
  "buffer_load_dwordx4 v163, %[rx], s76 offen lds\n\t" \
  "v_mfma_f32_16x16x32_bf16 v[136:139], a[240:243], v[116:119], v[136:139]\n\t" \
  "ds_read_b128 v[60:63], v167 offset:8448\n\t" \
  "v_mfma_f32_16x16x32_bf16 v[140:143], a[244:247], v[116:119], v[140:143]\n\t" \
  "s_add_i32 s44, s44, 1\n\t" \
  "s_add_i32 s42, s43, s79\n\t" \
  "v_mfma_f32_16x16x32_bf16 v[128:131], a[248:251], v[120:123], v[128:131]\n\t" \
  "s_add_i32 s43, s43, 0x80000\n\t" \
  "s_add_i32 s40, s40, 0x10000\n\t" \
  "v_mfma_f32_16x16x32_bf16 v[132:135], a[252:255], v[120:123], v[132:135]\n\t" \
  "s_xor_b32 s41, s41, 0x10000\n\t" \
  "v_mfma_f32_16x16x32_bf16 v[136:139], a[248:251], v[124:127], v[136:139]\n\t" \
  "v_cvt_pk_bf16_f32 v144, v128, v129\n\t" \
  "v_cvt_pk_bf16_f32 v145, v130, v131\n\t" \
  "v_mfma_f32_16x16x32_bf16 v[140:143], a[252:255], v[124:127], v[140:143]\n\t" \
  "v_cvt_pk_bf16_f32 v146, v132, v133\n\t" \
  "v_cvt_pk_bf16_f32 v147, v134, v135\n\t" \
  "s_cmp_eq_u32 s44, 64\n\t" \
  "s_cbranch_scc1 98f\n\t" \
  "s_cmp_le_u32 s44, 12\n\t" \
  "s_branch 20b\n\t" \
  "30:\n\t" \
  "s_waitcnt vmcnt(24)\n\t" \
  "s_branch 40b\n\t" \
  "31:\n\t" \
  "s_waitcnt vmcnt(24)\n\t" \
  "s_branch 41b\n\t" \
  "32:\n\t" \
  "s_waitcnt vmcnt(6)\n\t" \
  "s_barrier\n\t" \
  "s_add_i32 s46, s44, -1\n\t" \
  "v_mov_b32 v156, s46\n\t" \
  "buffer_store_dword v156, v171, %[ru], 0 offen sc1\n\t" \
  "s_branch 42b\n\t" \
  "33:\n\t" \
  "s_waitcnt vmcnt(24)\n\t" \
  "s_branch 43b\n\t" \
  "93:\n\t" \
  "ds_read_b128 v[0:3], v164 offset:0\n\t" \
  "ds_read_b128 v[4:7], v164 offset:8192\n\t" \
  "ds_read_b128 v[8:11], v165 offset:0\n\t" \
  "ds_read_b128 v[12:15], v165 offset:8192\n\t" \
  "ds_read_b128 v[16:19], v166 offset:0\n\t" \
  "ds_read_b128 v[20:23], v166 offset:8192\n\t" \
  "ds_read_b128 v[24:27], v167 offset:0\n\t" \
  "ds_read_b128 v[28:31], v167 offset:8192\n\t" \
  "ds_read_b128 v[32:35], v164 offset:256\n\t" \
  "ds_read_b128 v[36:39], v164 offset:8448\n\t" \
  "ds_read_b128 v[40:43], v165 offset:256\n\t" \
  "ds_read_b128 v[44:47], v165 offset:8448\n\t" \
  "ds_read_b128 v[48:51], v166 offset:256\n\t" \
  "ds_read_b128 v[52:55], v166 offset:8448\n\t" \
  "ds_read_b128 v[56:59], v167 offset:256\n\t" \
  "ds_read_b128 v[60:63], v167 offset:8448\n\t" \
  "v_mov_b64 v[152:153], -1\n\t" \
  "v_mov_b64 v[154:155], -1\n\t" \
  "s_cmp_le_u32 s44, 12\n\t" \
  ".p2align 6\n\t" \
  "20:\n\t" \
  "s_waitcnt vmcnt(28) lgkmcnt(0)\n\t" \
  "v_mfma_f32_16x16x32_bf16 v[128:131], a[0:3], v[0:3], 0\n\t" \
  "s_cbranch_scc1 30f\n\t" \
  "40:\n\t" \
  "s_barrier\n\t" \
  "v_cvt_pk_bf16_f32 v148, v136, v137\n\t" \
  "v_cvt_pk_bf16_f32 v149, v138, v139\n\t" \
  "v_mfma_f32_16x16x32_bf16 v[132:135], a[4:7], v[0:3], 0\n\t" \
  "v_cvt_pk_bf16_f32 v150, v140, v141\n\t" \
  "v_cvt_pk_bf16_f32 v151, v142, v143\n\t" \
  "v_mfma_f32_16x16x32_bf16 v[136:139], a[0:3], v[4:7], 0\n\t" \
  "ds_read_b128 v[64:67], v164 offset:16384\n\t" \
  "v_mfma_f32_16x16x32_bf16 v[140:143], a[4:7], v[4:7], 0\n\t" \
  "ds_read_b128 v[68:71], v164 offset:24576\n\t" \
  "v_mfma_f32_16x16x32_bf16 v[128:131], a[8:11], v[8:11], v[128:131]\n\t" \
  "ds_read_b128 v[72:75], v165 offset:16384\n\t" \
  "v_mfma_f32_16x16x32_bf16 v[132:135], a[12:15], v[8:11], v[132:135]\n\t" \
  "s_add_i32 m0, s41, 0\n\t" \
  "v_mfma_f32_16x16x32_bf16 v[136:139], a[8:11], v[12:15], v[136:139]\n\t" \
  "buffer_load_dwordx4 v160, %[rx], s40 offen lds\n\t" \
  "v_mfma_f32_16x16x32_bf16 v[140:143], a[12:15], v[12:15], v[140:143]\n\t" \
  "ds_read_b128 v[76:79], v165 offset:24576\n\t" \
  "v_mfma_f32_16x16x32_bf16 v[128:131], a[16:19], v[16:19], v[128:131]\n\t" \
  "ds_read_b128 v[80:83], v166 offset:16384\n\t" \
  "v_mfma_f32_16x16x32_bf16 v[132:135], a[20:23], v[16:19], v[132:135]\n\t" \
  "ds_read_b128 v[84:87], v166 offset:24576\n\t" \
  "v_mfma_f32_16x16x32_bf16 v[136:139], a[16:19], v[20:23], v[136:139]\n\t" \
  "buffer_store_dwordx4 v[144:147], v168, %[ru], s42 offen sc1\n\t" \
  "v_mfma_f32_16x16x32_bf16 v[140:143], a[20:23], v[20:23], v[140:143]\n\t" \
  "ds_read_b128 v[88:91], v167 offset:16384\n\t" \
  "v_mfma_f32_16x16x32_bf16 v[128:131], a[24:27], v[24:27], v[128:131]\n\t" \
  "ds_read_b128 v[92:95], v167 offset:24576\n\t" \
  "v_mfma_f32_16x16x32_bf16 v[132:135], a[28:31], v[24:27], v[132:135]\n\t" \
  "s_add_i32 m0, s41, 1024\n\t" \
  "v_mfma_f32_16x16x32_bf16 v[136:139], a[24:27], v[28:31], v[136:139]\n\t" \
  "buffer_load_dwordx4 v161, %[rx], s40 offen lds\n\t" \
  "v_mfma_f32_16x16x32_bf16 v[140:143], a[28:31], v[28:31], v[140:143]\n\t" \
  "ds_read_b128 v[96:99], v164 offset:16640\n\t" \
  "v_mfma_f32_16x16x32_bf16 v[128:131], a[32:35], v[32:35], v[128:131]\n\t" \
  "ds_read_b128 v[100:103], v164 offset:24832\n\t" \
  "v_mfma_f32_16x16x32_bf16 v[132:135], a[36:39], v[32:35], v[132:135]\n\t" \
  "ds_read_b128 v[104:107], v165 offset:16640\n\t" \
  "v_mfma_f32_16x16x32_bf16 v[136:139], a[32:35], v[36:39], v[136:139]\n\t" \
  "buffer_store_dwordx4 v[148:151], v169, %[ru], s42 offen sc1\n\t" \
  "v_mfma_f32_16x16x32_bf16 v[140:143], a[36:39], v[36:39], v[140:143]\n\t" \
  "ds_read_b128 v[108:111], v165 offset:24832\n\t" \
  "v_mfma_f32_16x16x32_bf16 v[128:131], a[40:43], v[40:43], v[128:131]\n\t" \
  "ds_read_b128 v[112:115], v166 offset:16640\n\t" \
  "v_mfma_f32_16x16x32_bf16 v[132:135], a[44:47], v[40:43], v[132:135]\n\t" \
  "s_add_i32 m0, s41, 2048\n\t" \
  "v_mfma_f32_16x16x32_bf16 v[136:139], a[40:43], v[44:47], v[136:139]\n\t" \
  "buffer_load_dwordx4 v162, %[rx], s40 offen lds\n\t" \
  "v_mfma_f32_16x16x32_bf16 v[140:143], a[44:47], v[44:47], v[140:143]\n\t" \
  "ds_read_b128 v[116:119], v166 offset:24832\n\t" \
  "v_mfma_f32_16x16x32_bf16 v[128:131], a[48:51], v[48:51], v[128:131]\n\t" \
  "ds_read_b128 v[120:123], v167 offset:16640\n\t" \
  "v_mfma_f32_16x16x32_bf16 v[132:135], a[52:55], v[48:51], v[132:135]\n\t" \
  "s_add_i32 m0, s41, 3072\n\t" \
  "v_mfma_f32_16x16x32_bf16 v[136:139], a[48:51], v[52:55], v[136:139]\n\t" \
  "buffer_load_dwordx4 v163, %[rx], s40 offen lds\n\t" \
  "v_mfma_f32_16x16x32_bf16 v[140:143], a[52:55], v[52:55], v[140:143]\n\t" \
  "ds_read_b128 v[124:127], v167 offset:24832\n\t" \
  "v_mfma_f32_16x16x32_bf16 v[128:131], a[56:59], v[56:59], v[128:131]\n\t" \
  "v_mfma_f32_16x16x32_bf16 v[132:135], a[60:63], v[56:59], v[132:135]\n\t" \
  "v_mfma_f32_16x16x32_bf16 v[136:139], a[56:59], v[60:63], v[136:139]\n\t" \
  "v_mfma_f32_16x16x32_bf16 v[140:143], a[60:63], v[60:63], v[140:143]\n\t" \
  "s_cmp_le_u32 s44, 12\n\t" \
  "s_waitcnt vmcnt(30) lgkmcnt(0)\n\t" \
  "v_mfma_f32_16x16x32_bf16 v[128:131], a[64:67], v[64:67], v[128:131]\n\t" \
  "s_cbranch_scc1 31f\n\t" \
  "41:\n\t" \
  "s_barrier\n\t" \
  "v_mfma_f32_16x16x32_bf16 v[132:135], a[68:71], v[64:67], v[132:135]\n\t" \
  "s_add_i32 s76, s40, 512\n\t" \
  "v_mfma_f32_16x16x32_bf16 v[136:139], a[64:67], v[68:71], v[136:139]\n\t" \
  "ds_read_b128 v[0:3], v164 offset:32768\n\t" \
  "v_mfma_f32_16x16x32_bf16 v[140:143], a[68:71], v[68:71], v[140:143]\n\t" \
  "ds_read_b128 v[4:7], v164 offset:40960\n\t" \
  "v_mfma_f32_16x16x32_bf16 v[128:131], a[72:75], v[72:75], v[128:131]\n\t" \
  "ds_read_b128 v[8:11], v165 offset:32768\n\t" \
  "v_mfma_f32_16x16x32_bf16 v[132:135], a[76:79], v[72:75], v[132:135]\n\t" \
  "s_add_i32 m0, s41, 16384\n\t" \
  "v_mfma_f32_16x16x32_bf16 v[136:139], a[72:75], v[76:79], v[136:139]\n\t" \
  "buffer_load_dwordx4 v160, %[rx], s76 offen lds\n\t" \
  "v_mfma_f32_16x16x32_bf16 v[140:143], a[76:79], v[76:79], v[140:143]\n\t" \
  "ds_read_b128 v[12:15], v165 offset:40960\n\t" \
  "v_mfma_f32_16x16x32_bf16 v[128:131], a[80:83], v[80:83], v[128:131]\n\t" \
  "ds_read_b128 v[16:19], v166 offset:32768\n\t" \
  "v_mfma_f32_16x16x32_bf16 v[132:135], a[84:87], v[80:83], v[132:135]\n\t" \
  "ds_read_b128 v[20:23], v166 offset:40960\n\t" \
  "v_mfma_f32_16x16x32_bf16 v[136:139], a[80:83], v[84:87], v[136:139]\n\t" \
  "v_mfma_f32_16x16x32_bf16 v[140:143], a[84:87], v[84:87], v[140:143]\n\t" \
  "ds_read_b128 v[24:27], v167 offset:32768\n\t" \
  "v_mfma_f32_16x16x32_bf16 v[128:131], a[88:91], v[88:91], v[128:131]\n\t" \
  "ds_read_b128 v[28:31], v167 offset:40960\n\t" \
  "v_mfma_f32_16x16x32_bf16 v[132:135], a[92:95], v[88:91], v[132:135]\n\t" \
  "s_add_i32 m0, s41, 17408\n\t" \
  "v_mfma_f32_16x16x32_bf16 v[136:139], a[88:91], v[92:95], v[136:139]\n\t" \
  "buffer_load_dwordx4 v161, %[rx], s76 offen lds\n\t" \
  "v_mfma_f32_16x16x32_bf16 v[140:143], a[92:95], v[92:95], v[140:143]\n\t" \
  "ds_read_b128 v[32:35], v164 offset:33024\n\t" \
  "v_mfma_f32_16x16x32_bf16 v[128:131], a[96:99], v[96:99], v[128:131]\n\t" \
  "ds_read_b128 v[36:39], v164 offset:41216\n\t" \
  "v_mfma_f32_16x16x32_bf16 v[132:135], a[100:103], v[96:99], v[132:135]\n\t" \
  "ds_read_b128 v[40:43], v165 offset:33024\n\t" \
  "v_mfma_f32_16x16x32_bf16 v[136:139], a[96:99], v[100:103], v[136:139]\n\t" \
  "v_mfma_f32_16x16x32_bf16 v[140:143], a[100:103], v[100:103], v[140:143]\n\t" \
  "ds_read_b128 v[44:47], v165 offset:41216\n\t" \
  "v_mfma_f32_16x16x32_bf16 v[128:131], a[104:107], v[104:107], v[128:131]\n\t" \
  "ds_read_b128 v[48:51], v166 offset:33024\n\t" \
  "v_mfma_f32_16x16x32_bf16 v[132:135], a[108:111], v[104:107], v[132:135]\n\t" \
  "s_add_i32 m0, s41, 18432\n\t" \
  "v_mfma_f32_16x16x32_bf16 v[136:139], a[104:107], v[108:111], v[136:139]\n\t" \
  "buffer_load_dwordx4 v162, %[rx], s76 offen lds\n\t" \
  "v_mfma_f32_16x16x32_bf16 v[140:143], a[108:111], v[108:111], v[140:143]\n\t" \
  "ds_read_b128 v[52:55], v166 offset:41216\n\t" \
  "v_mfma_f32_16x16x32_bf16 v[128:131], a[112:115], v[112:115], v[128:131]\n\t" \
  "ds_read_b128 v[56:59], v167 offset:33024\n\t" \
  "v_mfma_f32_16x16x32_bf16 v[132:135], a[116:119], v[112:115], v[132:135]\n\t" \
  "s_add_i32 m0, s41, 19456\n\t" \
  "v_mfma_f32_16x16x32_bf16 v[136:139], a[112:115], v[116:119], v[136:139]\n\t" \
  "buffer_load_dwordx4 v163, %[rx], s76 offen lds\n\t" \
  "v_mfma_f32_16x16x32_bf16 v[140:143], a[116:119], v[116:119], v[140:143]\n\t" \
  "ds_read_b128 v[60:63], v167 offset:41216\n\t" \
  "v_mfma_f32_16x16x32_bf16 v[128:131], a[120:123], v[120:123], v[128:131]\n\t" \
  "s_lshr_b32 s79, s44, 2\n\t" \
  "v_mfma_f32_16x16x32_bf16 v[132:135], a[124:127], v[120:123], v[132:135]\n\t" \
  "s_bitcmp1_b32 %[hm], s79\n\t" \
  "s_cselect_b32 s79, 0x2000, 0\n\t" \
  "v_mfma_f32_16x16x32_bf16 v[136:139], a[120:123], v[124:127], v[136:139]\n\t" \
  "s_cmp_lt_u32 s44, 4\n\t" \
  "s_cselect_b32 s79, %[d0], s79\n\t" \
  "v_mfma_f32_16x16x32_bf16 v[140:143], a[124:127], v[124:127], v[140:143]\n\t" \
  "s_cmp_le_u32 s44, 12\n\t" \
  "s_waitcnt vmcnt(29) lgkmcnt(0)\n\t" \
  "v_mfma_f32_16x16x32_bf16 v[128:131], a[128:131], v[0:3], v[128:131]\n\t" \
  "s_cbranch_scc1 32f\n\t" \
  "42:\n\t" \
  "s_barrier\n\t" \
  "v_mfma_f32_16x16x32_bf16 v[132:135], a[132:135], v[0:3], v[132:135]\n\t" \
  "s_add_i32 s76, s40, 1024\n\t" \
  "v_mfma_f32_16x16x32_bf16 v[136:139], a[128:131], v[4:7], v[136:139]\n\t" \
  "ds_read_b128 v[64:67], v164 offset:49152\n\t" \
  "v_mfma_f32_16x16x32_bf16 v[140:143], a[132:135], v[4:7], v[140:143]\n\t" \
  "ds_read_b128 v[68:71], v164 offset:57344\n\t" \
  "v_mfma_f32_16x16x32_bf16 v[128:131], a[136:139], v[8:11], v[128:131]\n\t" \
  "ds_read_b128 v[72:75], v165 offset:49152\n\t" \
  "v_mfma_f32_16x16x32_bf16 v[132:135], a[140:143], v[8:11], v[132:135]\n\t" \
  "s_add_i32 m0, s41, 32768\n\t" \
  "v_mfma_f32_16x16x32_bf16 v[136:139], a[136:139], v[12:15], v[136:139]\n\t" \
  "buffer_load_dwordx4 v160, %[rx], s76 offen lds\n\t" \
  "v_mfma_f32_16x16x32_bf16 v[140:143], a[140:143], v[12:15], v[140:143]\n\t" \
  "ds_read_b128 v[76:79], v165 offset:57344\n\t" \
  "v_mfma_f32_16x16x32_bf16 v[128:131], a[144:147], v[16:19], v[128:131]\n\t" \
  "ds_read_b128 v[80:83], v166 offset:49152\n\t" \
  "v_mfma_f32_16x16x32_bf16 v[132:135], a[148:151], v[16:19], v[132:135]\n\t" \
  "ds_read_b128 v[84:87], v166 offset:57344\n\t" \
  "v_mfma_f32_16x16x32_bf16 v[136:139], a[144:147], v[20:23], v[136:139]\n\t" \
  "v_mfma_f32_16x16x32_bf16 v[140:143], a[148:151], v[20:23], v[140:143]\n\t" \
  "ds_read_b128 v[88:91], v167 offset:49152\n\t" \
  "v_mfma_f32_16x16x32_bf16 v[128:131], a[152:155], v[24:27], v[128:131]\n\t" \
  "ds_read_b128 v[92:95], v167 offset:57344\n\t" \
  "v_mfma_f32_16x16x32_bf16 v[132:135], a[156:159], v[24:27], v[132:135]\n\t" \
  "s_add_i32 m0, s41, 33792\n\t" \
  "v_mfma_f32_16x16x32_bf16 v[136:139], a[152:155], v[28:31], v[136:139]\n\t" \
  "buffer_load_dwordx4 v161, %[rx], s76 offen lds\n\t" \
  "v_mfma_f32_16x16x32_bf16 v[140:143], a[156:159], v[28:31], v[140:143]\n\t" \
  "ds_read_b128 v[96:99], v164 offset:49408\n\t" \
  "v_mfma_f32_16x16x32_bf16 v[128:131], a[160:163], v[32:35], v[128:131]\n\t" \
  "ds_read_b128 v[100:103], v164 offset:57600\n\t" \
  "v_mfma_f32_16x16x32_bf16 v[132:135], a[164:167], v[32:35], v[132:135]\n\t" \
  "ds_read_b128 v[104:107], v165 offset:49408\n\t" \
  "v_mfma_f32_16x16x32_bf16 v[136:139], a[160:163], v[36:39], v[136:139]\n\t" \
  "v_mfma_f32_16x16x32_bf16 v[140:143], a[164:167], v[36:39], v[140:143]\n\t" \
  "ds_read_b128 v[108:111], v165 offset:57600\n\t" \
  "v_mfma_f32_16x16x32_bf16 v[128:131], a[168:171], v[40:43], v[128:131]\n\t" \
  "ds_read_b128 v[112:115], v166 offset:49408\n\t" \
  "v_mfma_f32_16x16x32_bf16 v[132:135], a[172:175], v[40:43], v[132:135]\n\t" \
  "s_add_i32 m0, s41, 34816\n\t" \
  "v_mfma_f32_16x16x32_bf16 v[136:139], a[168:171], v[44:47], v[136:139]\n\t" \
  "buffer_load_dwordx4 v162, %[rx], s76 offen lds\n\t" \
  "v_mfma_f32_16x16x32_bf16 v[140:143], a[172:175], v[44:47], v[140:143]\n\t" \
  "ds_read_b128 v[116:119], v166 offset:57600\n\t" \
  "v_mfma_f32_16x16x32_bf16 v[128:131], a[176:179], v[48:51], v[128:131]\n\t" \
  "ds_read_b128 v[120:123], v167 offset:49408\n\t" \
  "v_mfma_f32_16x16x32_bf16 v[132:135], a[180:183], v[48:51], v[132:135]\n\t" \
  "s_add_i32 m0, s41, 35840\n\t" \
  "v_mfma_f32_16x16x32_bf16 v[136:139], a[176:179], v[52:55], v[136:139]\n\t" \
  "buffer_load_dwordx4 v163, %[rx], s76 offen lds\n\t" \
  "v_mfma_f32_16x16x32_bf16 v[140:143], a[180:183], v[52:55], v[140:143]\n\t" \
  "ds_read_b128 v[124:127], v167 offset:57600\n\t" \
  "v_mfma_f32_16x16x32_bf16 v[128:131], a[184:187], v[56:59], v[128:131]\n\t" \
  "s_add_i32 s78, s44, -2\n\t" \
  "v_mfma_f32_16x16x32_bf16 v[132:135], a[188:191], v[56:59], v[132:135]\n\t" \
  "s_max_i32 s78, s78, s46\n\t" \
  "v_mfma_f32_16x16x32_bf16 v[136:139], a[184:187], v[60:63], v[136:139]\n\t" \
  "v_mov_b32 v156, s78\n\t" \
  "v_mfma_f32_16x16x32_bf16 v[140:143], a[188:191], v[60:63], v[140:143]\n\t" \
  "s_cmp_le_u32 s44, 12\n\t" \
  "s_waitcnt vmcnt(27) lgkmcnt(0)\n\t" \
  "v_mfma_f32_16x16x32_bf16 v[128:131], a[192:195], v[64:67], v[128:131]\n\t" \
  "s_cbranch_scc1 33f\n\t" \
  "43:\n\t" \
  "s_barrier\n\t" \
  "v_xor_b32 v164, 0x10000, v164\n\t" \
  "v_mfma_f32_16x16x32_bf16 v[132:135], a[196:199], v[64:67], v[132:135]\n\t" \
  "s_add_i32 s76, s40, 1536\n\t" \
  "buffer_store_dword v156, v171, %[ru], 0 offen sc1\n\t" \
  "v_mfma_f32_16x16x32_bf16 v[136:139], a[192:195], v[68:71], v[136:139]\n\t" \
  "v_xor_b32 v165, 0x10000, v165\n\t" \
  "ds_read_b128 v[0:3], v164 offset:0\n\t" \
  "v_mfma_f32_16x16x32_bf16 v[140:143], a[196:199], v[68:71], v[140:143]\n\t" \
  "v_xor_b32 v166, 0x10000, v166\n\t" \
  "ds_read_b128 v[4:7], v164 offset:8192\n\t" \
  "v_mfma_f32_16x16x32_bf16 v[128:131], a[200:203], v[72:75], v[128:131]\n\t" \
  "v_xor_b32 v167, 0x10000, v167\n\t" \
  "ds_read_b128 v[8:11], v165 offset:0\n\t" \
  "v_mfma_f32_16x16x32_bf16 v[132:135], a[204:207], v[72:75], v[132:135]\n\t" \
  "s_add_i32 m0, s41, 49152\n\t" \
  "ds_read_b128 v[12:15], v165 offset:8192\n\t" \
  "v_mfma_f32_16x16x32_bf16 v[136:139], a[200:203], v[76:79], v[136:139]\n\t" \
  "buffer_load_dwordx4 v160, %[rx], s76 offen lds\n\t" \
  "v_mfma_f32_16x16x32_bf16 v[140:143], a[204:207], v[76:79], v[140:143]\n\t" \
  "ds_read_b128 v[16:19], v166 offset:0\n\t" \
  "v_mfma_f32_16x16x32_bf16 v[128:131], a[208:211], v[80:83], v[128:131]\n\t" \
  "ds_read_b128 v[20:23], v166 offset:8192\n\t" \
  "v_mfma_f32_16x16x32_bf16 v[132:135], a[212:215], v[80:83], v[132:135]\n\t" \
  "ds_read_b128 v[24:27], v167 offset:0\n\t" \
  "v_mfma_f32_16x16x32_bf16 v[136:139], a[208:211], v[84:87], v[136:139]\n\t" \
  "v_mfma_f32_16x16x32_bf16 v[140:143], a[212:215], v[84:87], v[140:143]\n\t" \
  "ds_read_b128 v[28:31], v167 offset:8192\n\t" \
  "v_mfma_f32_16x16x32_bf16 v[128:131], a[216:219], v[88:91], v[128:131]\n\t" \
  "ds_read_b128 v[32:35], v164 offset:256\n\t" \
  "v_mfma_f32_16x16x32_bf16 v[132:135], a[220:223], v[88:91], v[132:135]\n\t" \
  "s_add_i32 m0, s41, 50176\n\t" \
  "v_mfma_f32_16x16x32_bf16 v[136:139], a[216:219], v[92:95], v[136:139]\n\t" \
  "buffer_load_dwordx4 v161, %[rx], s76 offen lds\n\t" \
  "v_mfma_f32_16x16x32_bf16 v[140:143], a[220:223], v[92:95], v[140:143]\n\t" \
  "ds_read_b128 v[36:39], v164 offset:8448\n\t" \
  "v_mfma_f32_16x16x32_bf16 v[128:131], a[224:227], v[96:99], v[128:131]\n\t" \
  "ds_read_b128 v[40:43], v165 offset:256\n\t" \
  "v_mfma_f32_16x16x32_bf16 v[132:135], a[228:231], v[96:99], v[132:135]\n\t" \
  "ds_read_b128 v[44:47], v165 offset:8448\n\t" \
  "v_mfma_f32_16x16x32_bf16 v[136:139], a[224:227], v[100:103], v[136:139]\n\t" \
  "v_mfma_f32_16x16x32_bf16 v[140:143], a[228:231], v[100:103], v[140:143]\n\t" \
  "ds_read_b128 v[48:51], v166 offset:256\n\t" \
  "v_mfma_f32_16x16x32_bf16 v[128:131], a[232:235], v[104:107], v[128:131]\n\t" \
  "ds_read_b128 v[52:55], v166 offset:8448\n\t" \
  "v_mfma_f32_16x16x32_bf16 v[132:135], a[236:239], v[104:107], v[132:135]\n\t" \
  "s_add_i32 m0, s41, 51200\n\t" \
  "v_mfma_f32_16x16x32_bf16 v[136:139], a[232:235], v[108:111], v[136:139]\n\t" \
  "buffer_load_dwordx4 v162, %[rx], s76 offen lds\n\t" \
  "v_mfma_f32_16x16x32_bf16 v[140:143], a[236:239], v[108:111], v[140:143]\n\t" \
  "ds_read_b128 v[56:59], v167 offset:256\n\t" \
  "v_mfma_f32_16x16x32_bf16 v[128:131], a[240:243], v[112:115], v[128:131]\n\t" \
  "ds_read_b128 v[60:63], v167 offset:8448\n\t" \
  "v_mfma_f32_16x16x32_bf16 v[132:135], a[244:247], v[112:115], v[132:135]\n\t" \
  "s_add_i32 m0, s41, 52224\n\t" \
  "v_mfma_f32_16x16x32_bf16 v[136:139], a[240:243], v[116:119], v[136:139]\n\t" \
  "buffer_load_dwordx4 v163, %[rx], s76 offen lds\n\t" \
  "v_mfma_f32_16x16x32_bf16 v[140:143], a[244:247], v[116:119], v[140:143]\n\t" \
  "s_add_i32 s44, s44, 1\n\t" \
  "s_add_i32 s42, s43, s79\n\t" \
  "v_mfma_f32_16x16x32_bf16 v[128:131], a[248:251], v[120:123], v[128:131]\n\t" \
  "s_add_i32 s43, s43, 0x80000\n\t" \
  "s_add_i32 s40, s40, 0x10000\n\t" \
  "v_mfma_f32_16x16x32_bf16 v[132:135], a[252:255], v[120:123], v[132:135]\n\t" \
  "s_xor_b32 s41, s41, 0x10000\n\t" \
  "v_mfma_f32_16x16x32_bf16 v[136:139], a[248:251], v[124:127], v[136:139]\n\t" \
  "v_cvt_pk_bf16_f32 v144, v128, v129\n\t" \
  "v_cvt_pk_bf16_f32 v145, v130, v131\n\t" \
  "v_mfma_f32_16x16x32_bf16 v[140:143], a[252:255], v[124:127], v[140:143]\n\t" \
  "v_cvt_pk_bf16_f32 v146, v132, v133\n\t" \
  "v_cvt_pk_bf16_f32 v147, v134, v135\n\t" \
  "s_cmp_eq_u32 s44, 64\n\t" \
  "s_cbranch_scc1 98f\n\t" \
  "s_cmp_le_u32 s44, 12\n\t" \
  "s_branch 20b\n\t" \
  "30:\n\t" \
  "s_waitcnt vmcnt(24)\n\t" \
  "s_branch 40b\n\t" \
  "31:\n\t" \
  "s_waitcnt vmcnt(24)\n\t" \
  "s_branch 41b\n\t" \
  "32:\n\t" \
  "s_waitcnt vmcnt(6)\n\t" \
  "s_barrier\n\t" \
  "s_add_i32 s46, s44, -1\n\t" \
  "v_mov_b32 v156, s46\n\t" \
  "buffer_store_dword v156, v171, %[ru], 0 offen sc1\n\t" \
  "s_branch 42b\n\t" \
  "33:\n\t" \
  "s_waitcnt vmcnt(24)\n\t" \
  "s_branch 43b\n\t" \
  "98:\n\t" \
  "s_nop 7\n\t" \
  "v_cvt_pk_bf16_f32 v148, v136, v137\n\t" \
  "v_cvt_pk_bf16_f32 v149, v138, v139\n\t" \
  "v_cvt_pk_bf16_f32 v150, v140, v141\n\t" \
  "v_cvt_pk_bf16_f32 v151, v142, v143\n\t" \
  "buffer_store_dwordx4 v[144:147], v168, %[ru], s42 offen sc1\n\t" \
  "buffer_store_dwordx4 v[148:151], v169, %[ru], s42 offen sc1\n\t" \
  "v_mov_b32 v156, 63\n\t" \
  "s_waitcnt vmcnt(0) lgkmcnt(0)\n\t" \
  "s_barrier\n\t" \
  "buffer_store_dword v156, v171, %[ru], 0 offen sc1\n\t" \
  "99:\n\t" \
  ""
#define U_BODY \
  "s_cmp_lg_u32 %[wv], 0\n\t" \
  "s_cbranch_scc1 91f\n\t" \
  "90:\n\t" \
  "ds_read_b128 v[0:3], v81 offset:0\n\t" \
  "ds_read_b128 v[16:19], v81 offset:4096\n\t" \
  "ds_read_b128 v[4:7], v81 offset:1024\n\t" \
  "ds_read_b128 v[20:23], v81 offset:5120\n\t" \
  "ds_read_b128 v[8:11], v81 offset:2048\n\t" \
  "ds_read_b128 v[24:27], v81 offset:6144\n\t" \
  "ds_read_b128 v[12:15], v81 offset:3072\n\t" \
  "ds_read_b128 v[28:31], v81 offset:7168\n\t" \
  ".p2align 6\n\t" \
  "20:\n\t" \
  "s_waitcnt lgkmcnt(7)\n\t" \
  "v_mfma_f32_16x16x32_bf16 v[32:35], v[0:3], a[0:3], 0\n\t" \
  "buffer_store_dwordx4 v[52:55], v80, %[ru], s44 offen offset:1024 sc1\n\t" \
  "s_waitcnt lgkmcnt(6)\n\t" \
  "v_mfma_f32_16x16x32_bf16 v[36:39], v[16:19], a[0:3], 0\n\t" \
  "s_and_b32 s45, s40, 15\n\t" \
  "s_waitcnt lgkmcnt(5)\n\t" \
  "v_mfma_f32_16x16x32_bf16 v[32:35], v[4:7], a[4:7], v[32:35]\n\t" \
  "v_cvt_pk_bf16_f32 v57, v40, v41\n\t" \
  "s_lshl_b32 s45, s45, 22\n\t" \
  "s_waitcnt lgkmcnt(4)\n\t" \
  "v_mfma_f32_16x16x32_bf16 v[36:39], v[20:23], a[4:7], v[36:39]\n\t" \
  "v_cvt_pk_bf16_f32 v59, v42, v43\n\t" \
  "s_add_i32 s45, s45, s42\n\t" \
  "s_waitcnt lgkmcnt(3)\n\t" \
  "v_mfma_f32_16x16x32_bf16 v[32:35], v[8:11], a[8:11], v[32:35]\n\t" \
  "v_cvt_pk_bf16_f32 v61, v44, v45\n\t" \
  "s_lshr_b32 s76, s45, 21\n\t" \
  "s_waitcnt lgkmcnt(2)\n\t" \
  "v_mfma_f32_16x16x32_bf16 v[36:39], v[24:27], a[8:11], v[36:39]\n\t" \
  "v_cvt_pk_bf16_f32 v63, v46, v47\n\t" \
  "s_bitcmp0_b32 %[sel], s76\n\t" \
  "s_cselect_b32 s76, 0x2000, 0\n\t" \
  "s_waitcnt lgkmcnt(1)\n\t" \
  "v_mfma_f32_16x16x32_bf16 v[32:35], v[12:15], a[12:15], v[32:35]\n\t" \
  "s_add_i32 s45, s45, s76\n\t" \
  "s_waitcnt lgkmcnt(0)\n\t" \
  "v_mfma_f32_16x16x32_bf16 v[36:39], v[28:31], a[12:15], v[36:39]\n\t" \
  "s_add_i32 s77, s40, 1\n\t" \
  "v_mfma_f32_16x16x32_bf16 v[40:43], v[0:3], a[16:19], 0\n\t" \
  "buffer_store_dwordx4 v[56:59], v80, %[ru], s44 offen offset:2048 sc1\n\t" \
  "v_mfma_f32_16x16x32_bf16 v[44:47], v[16:19], a[16:19], 0\n\t" \
  "s_min_u32 s77, s77, 63\n\t" \
  "v_mfma_f32_16x16x32_bf16 v[40:43], v[4:7], a[20:23], v[40:43]\n\t" \
  "v_cvt_pk_bf16_f32 v48, v32, v33\n\t" \
  "s_and_b32 s76, s77, 4\n\t" \
  "v_mfma_f32_16x16x32_bf16 v[44:47], v[20:23], a[20:23], v[44:47]\n\t" \
  "v_cvt_pk_bf16_f32 v50, v34, v35\n\t" \
  "s_lshl_b32 s76, s76, 11\n\t" \
  "v_mfma_f32_16x16x32_bf16 v[40:43], v[8:11], a[24:27], v[40:43]\n\t" \
  "v_cvt_pk_bf16_f32 v52, v36, v37\n\t" \
  "s_lshl_b32 s77, s77, 19\n\t" \
  "v_mfma_f32_16x16x32_bf16 v[44:47], v[24:27], a[24:27], v[44:47]\n\t" \
  "v_cvt_pk_bf16_f32 v54, v38, v39\n\t" \
  "s_add_i32 s77, s77, s43\n\t" \
  "v_mfma_f32_16x16x32_bf16 v[40:43], v[12:15], a[28:31], v[40:43]\n\t" \
  "s_xor_b32 s77, s77, s76\n\t" \
  "v_mfma_f32_16x16x32_bf16 v[44:47], v[28:31], a[28:31], v[44:47]\n\t" \
  "s_cmp_lt_u32 s40, 3\n\t" \
  "s_cselect_b32 s76, 0x2000, 0\n\t" \
  "v_mfma_f32_16x16x32_bf16 v[32:35], v[0:3], a[32:35], 0\n\t" \
  "buffer_store_dwordx4 v[60:63], v80, %[ru], s44 offen offset:3072 sc1\n\t" \
  "v_mfma_f32_16x16x32_bf16 v[36:39], v[16:19], a[32:35], 0\n\t" \
  "s_andn2_b32 s77, s77, s76\n\t" \
  "v_mfma_f32_16x16x32_bf16 v[32:35], v[4:7], a[36:39], v[32:35]\n\t" \
  "v_cvt_pk_bf16_f32 v49, v40, v41\n\t" \
  "s_add_i32 s78, s40, 3\n\t" \
  "v_mfma_f32_16x16x32_bf16 v[36:39], v[20:23], a[36:39], v[36:39]\n\t" \
  "v_cvt_pk_bf16_f32 v51, v42, v43\n\t" \
  "s_cmp_lt_i32 s41, s78\n\t" \
  "s_cselect_b32 s78, 0, 0x7f000000\n\t" \
  "v_mfma_f32_16x16x32_bf16 v[32:35], v[8:11], a[40:43], v[32:35]\n\t" \
  "v_cvt_pk_bf16_f32 v53, v44, v45\n\t" \
  "v_mfma_f32_16x16x32_bf16 v[36:39], v[24:27], a[40:43], v[36:39]\n\t" \
  "v_cvt_pk_bf16_f32 v55, v46, v47\n\t" \
  "v_mfma_f32_16x16x32_bf16 v[32:35], v[12:15], a[44:47], v[32:35]\n\t" \
  "v_mfma_f32_16x16x32_bf16 v[36:39], v[28:31], a[44:47], v[36:39]\n\t" \
  "s_mov_b64 exec, 3\n\t" \
  "buffer_load_dword v72, v83, %[ru], s78 offen sc1\n\t" \
  "s_mov_b64 exec, -1\n\t" \
  "v_mfma_f32_16x16x32_bf16 v[40:43], v[0:3], a[48:51], 0\n\t" \
  "buffer_store_dwordx4 v[48:51], v80, %[ru], s45 offen offset:0 sc1\n\t" \
  "v_mfma_f32_16x16x32_bf16 v[44:47], v[16:19], a[48:51], 0\n\t" \
  "v_mfma_f32_16x16x32_bf16 v[40:43], v[4:7], a[52:55], v[40:43]\n\t" \
  "v_cvt_pk_bf16_f32 v56, v32, v33\n\t" \
  "v_mfma_f32_16x16x32_bf16 v[44:47], v[20:23], a[52:55], v[44:47]\n\t" \
  "v_cvt_pk_bf16_f32 v58, v34, v35\n\t" \
  "v_mfma_f32_16x16x32_bf16 v[40:43], v[8:11], a[56:59], v[40:43]\n\t" \
  "v_cvt_pk_bf16_f32 v60, v36, v37\n\t" \
  "v_mfma_f32_16x16x32_bf16 v[44:47], v[24:27], a[56:59], v[44:47]\n\t" \
  "v_cvt_pk_bf16_f32 v62, v38, v39\n\t" \
  "v_mfma_f32_16x16x32_bf16 v[40:43], v[12:15], a[60:63], v[40:43]\n\t" \
  "v_mfma_f32_16x16x32_bf16 v[44:47], v[28:31], a[60:63], v[44:47]\n\t" \
  "v_mfma_f32_16x16x32_bf16 v[32:35], v[0:3], a[64:67], 0\n\t" \
  "buffer_store_dwordx4 v[52:55], v80, %[ru], s45 offen offset:1024 sc1\n\t" \
  "v_mfma_f32_16x16x32_bf16 v[36:39], v[16:19], a[64:67], 0\n\t" \
  "v_mfma_f32_16x16x32_bf16 v[32:35], v[4:7], a[68:71], v[32:35]\n\t" \
  "v_cvt_pk_bf16_f32 v57, v40, v41\n\t" \
  "v_mfma_f32_16x16x32_bf16 v[36:39], v[20:23], a[68:71], v[36:39]\n\t" \
  "v_cvt_pk_bf16_f32 v59, v42, v43\n\t" \
  "v_mfma_f32_16x16x32_bf16 v[32:35], v[8:11], a[72:75], v[32:35]\n\t" \
  "v_cvt_pk_bf16_f32 v61, v44, v45\n\t" \
  "v_mfma_f32_16x16x32_bf16 v[36:39], v[24:27], a[72:75], v[36:39]\n\t" \
  "v_cvt_pk_bf16_f32 v63, v46, v47\n\t" \
  "v_mfma_f32_16x16x32_bf16 v[32:35], v[12:15], a[76:79], v[32:35]\n\t" \
  "v_mfma_f32_16x16x32_bf16 v[36:39], v[28:31], a[76:79], v[36:39]\n\t" \
  "v_mfma_f32_16x16x32_bf16 v[40:43], v[0:3], a[80:83], 0\n\t" \
  "buffer_store_dwordx4 v[56:59], v80, %[ru], s45 offen offset:2048 sc1\n\t" \
  "v_mfma_f32_16x16x32_bf16 v[44:47], v[16:19], a[80:83], 0\n\t" \
  "v_mfma_f32_16x16x32_bf16 v[40:43], v[4:7], a[84:87], v[40:43]\n\t" \
  "v_cvt_pk_bf16_f32 v48, v32, v33\n\t" \
  "v_mfma_f32_16x16x32_bf16 v[44:47], v[20:23], a[84:87], v[44:47]\n\t" \
  "v_cvt_pk_bf16_f32 v50, v34, v35\n\t" \
  "v_mfma_f32_16x16x32_bf16 v[40:43], v[8:11], a[88:91], v[40:43]\n\t" \
  "v_cvt_pk_bf16_f32 v52, v36, v37\n\t" \
  "v_mfma_f32_16x16x32_bf16 v[44:47], v[24:27], a[88:91], v[44:47]\n\t" \
  "v_cvt_pk_bf16_f32 v54, v38, v39\n\t" \
  "v_mfma_f32_16x16x32_bf16 v[40:43], v[12:15], a[92:95], v[40:43]\n\t" \
  "v_mfma_f32_16x16x32_bf16 v[44:47], v[28:31], a[92:95], v[44:47]\n\t" \
  "v_mfma_f32_16x16x32_bf16 v[32:35], v[0:3], a[96:99], 0\n\t" \
  "buffer_store_dwordx4 v[60:63], v80, %[ru], s45 offen offset:3072 sc1\n\t" \
  "v_mfma_f32_16x16x32_bf16 v[36:39], v[16:19], a[96:99], 0\n\t" \
  "v_mfma_f32_16x16x32_bf16 v[32:35], v[4:7], a[100:103], v[32:35]\n\t" \
  "v_cvt_pk_bf16_f32 v49, v40, v41\n\t" \
  "v_mfma_f32_16x16x32_bf16 v[36:39], v[20:23], a[100:103], v[36:39]\n\t" \
  "v_cvt_pk_bf16_f32 v51, v42, v43\n\t" \
  "v_mfma_f32_16x16x32_bf16 v[32:35], v[8:11], a[104:107], v[32:35]\n\t" \
  "v_cvt_pk_bf16_f32 v53, v44, v45\n\t" \
  "v_mfma_f32_16x16x32_bf16 v[36:39], v[24:27], a[104:107], v[36:39]\n\t" \
  "v_cvt_pk_bf16_f32 v55, v46, v47\n\t" \
  "v_mfma_f32_16x16x32_bf16 v[32:35], v[12:15], a[108:111], v[32:35]\n\t" \
  "v_mfma_f32_16x16x32_bf16 v[36:39], v[28:31], a[108:111], v[36:39]\n\t" \
  "s_add_i32 s45, s45, 0x1000\n\t" \
  "v_mfma_f32_16x16x32_bf16 v[40:43], v[0:3], a[112:115], 0\n\t" \
  "buffer_store_dwordx4 v[48:51], v80, %[ru], s45 offen offset:0 sc1\n\t" \
  "v_mfma_f32_16x16x32_bf16 v[44:47], v[16:19], a[112:115], 0\n\t" \
  "v_mfma_f32_16x16x32_bf16 v[40:43], v[4:7], a[116:119], v[40:43]\n\t" \
  "v_cvt_pk_bf16_f32 v56, v32, v33\n\t" \
  "v_mfma_f32_16x16x32_bf16 v[44:47], v[20:23], a[116:119], v[44:47]\n\t" \
  "v_cvt_pk_bf16_f32 v58, v34, v35\n\t" \
  "v_mfma_f32_16x16x32_bf16 v[40:43], v[8:11], a[120:123], v[40:43]\n\t" \
  "v_cvt_pk_bf16_f32 v60, v36, v37\n\t" \
  "v_mfma_f32_16x16x32_bf16 v[44:47], v[24:27], a[120:123], v[44:47]\n\t" \
  "v_cvt_pk_bf16_f32 v62, v38, v39\n\t" \
  "v_mfma_f32_16x16x32_bf16 v[40:43], v[12:15], a[124:127], v[40:43]\n\t" \
  "s_waitcnt vmcnt(5)\n\t" \
  "buffer_load_dwordx4 v[64:67], v80, %[ru], s77 offen sc1\n\t" \
  "v_mfma_f32_16x16x32_bf16 v[44:47], v[28:31], a[124:127], v[44:47]\n\t" \
  "buffer_load_dwordx4 v[68:71], v80, %[ru], s77 offen offset:1024 sc1\n\t" \
  "v_mfma_f32_16x16x32_bf16 v[32:35], v[0:3], a[128:131], 0\n\t" \
  "buffer_store_dwordx4 v[52:55], v80, %[ru], s45 offen offset:1024 sc1\n\t" \
  "v_mfma_f32_16x16x32_bf16 v[36:39], v[16:19], a[128:131], 0\n\t" \
  "v_readlane_b32 s76, v72, 0\n\t" \
  "v_readlane_b32 s79, v72, 1\n\t" \
  "v_mfma_f32_16x16x32_bf16 v[32:35], v[4:7], a[132:135], v[32:35]\n\t" \
  "v_cvt_pk_bf16_f32 v57, v40, v41\n\t" \
  "v_mfma_f32_16x16x32_bf16 v[36:39], v[20:23], a[132:135], v[36:39]\n\t" \
  "v_cvt_pk_bf16_f32 v59, v42, v43\n\t" \
  "v_mfma_f32_16x16x32_bf16 v[32:35], v[8:11], a[136:139], v[32:35]\n\t" \
  "v_cvt_pk_bf16_f32 v61, v44, v45\n\t" \
  "v_mfma_f32_16x16x32_bf16 v[36:39], v[24:27], a[136:139], v[36:39]\n\t" \
  "v_cvt_pk_bf16_f32 v63, v46, v47\n\t" \
  "v_mfma_f32_16x16x32_bf16 v[32:35], v[12:15], a[140:143], v[32:35]\n\t" \
  "s_add_i32 s79, s79, 15\n\t" \
  "s_min_i32 s76, s76, s79\n\t" \
  "v_mfma_f32_16x16x32_bf16 v[36:39], v[28:31], a[140:143], v[36:39]\n\t" \
  "s_max_i32 s41, s41, s76\n\t" \
  "v_mov_b32 v73, s41\n\t" \
  "v_mfma_f32_16x16x32_bf16 v[40:43], v[0:3], a[144:147], 0\n\t" \
  "buffer_store_dwordx4 v[56:59], v80, %[ru], s45 offen offset:2048 sc1\n\t" \
  "v_mfma_f32_16x16x32_bf16 v[44:47], v[16:19], a[144:147], 0\n\t" \
  "v_mfma_f32_16x16x32_bf16 v[40:43], v[4:7], a[148:151], v[40:43]\n\t" \
  "v_cvt_pk_bf16_f32 v48, v32, v33\n\t" \
  "v_mfma_f32_16x16x32_bf16 v[44:47], v[20:23], a[148:151], v[44:47]\n\t" \
  "v_cvt_pk_bf16_f32 v50, v34, v35\n\t" \
  "v_mfma_f32_16x16x32_bf16 v[40:43], v[8:11], a[152:155], v[40:43]\n\t" \
  "v_cvt_pk_bf16_f32 v52, v36, v37\n\t" \
  "v_mfma_f32_16x16x32_bf16 v[44:47], v[24:27], a[152:155], v[44:47]\n\t" \
  "v_cvt_pk_bf16_f32 v54, v38, v39\n\t" \
  "v_mfma_f32_16x16x32_bf16 v[40:43], v[12:15], a[156:159], v[40:43]\n\t" \
  "v_mfma_f32_16x16x32_bf16 v[44:47], v[28:31], a[156:159], v[44:47]\n\t" \
  "v_mfma_f32_16x16x32_bf16 v[32:35], v[0:3], a[160:163], 0\n\t" \
  "buffer_store_dwordx4 v[60:63], v80, %[ru], s45 offen offset:3072 sc1\n\t" \
  "v_mfma_f32_16x16x32_bf16 v[36:39], v[16:19], a[160:163], 0\n\t" \
  "v_mfma_f32_16x16x32_bf16 v[32:35], v[4:7], a[164:167], v[32:35]\n\t" \
  "v_cvt_pk_bf16_f32 v49, v40, v41\n\t" \
  "v_mfma_f32_16x16x32_bf16 v[36:39], v[20:23], a[164:167], v[36:39]\n\t" \
  "v_cvt_pk_bf16_f32 v51, v42, v43\n\t" \
  "v_mfma_f32_16x16x32_bf16 v[32:35], v[8:11], a[168:171], v[32:35]\n\t" \
  "v_cvt_pk_bf16_f32 v53, v44, v45\n\t" \
  "v_mfma_f32_16x16x32_bf16 v[36:39], v[24:27], a[168:171], v[36:39]\n\t" \
  "v_cvt_pk_bf16_f32 v55, v46, v47\n\t" \
  "v_mfma_f32_16x16x32_bf16 v[32:35], v[12:15], a[172:175], v[32:35]\n\t" \
  "v_mfma_f32_16x16x32_bf16 v[36:39], v[28:31], a[172:175], v[36:39]\n\t" \
  "s_add_i32 s45, s45, 0x3000\n\t" \
  "v_mfma_f32_16x16x32_bf16 v[40:43], v[0:3], a[176:179], 0\n\t" \
  "buffer_store_dwordx4 v[48:51], v80, %[ru], s45 offen offset:0 sc1\n\t" \
  "v_mfma_f32_16x16x32_bf16 v[44:47], v[16:19], a[176:179], 0\n\t" \
  "v_mfma_f32_16x16x32_bf16 v[40:43], v[4:7], a[180:183], v[40:43]\n\t" \
  "v_cvt_pk_bf16_f32 v56, v32, v33\n\t" \
  "v_mfma_f32_16x16x32_bf16 v[44:47], v[20:23], a[180:183], v[44:47]\n\t" \
  "v_cvt_pk_bf16_f32 v58, v34, v35\n\t" \
  "v_mfma_f32_16x16x32_bf16 v[40:43], v[8:11], a[184:187], v[40:43]\n\t" \
  "v_cvt_pk_bf16_f32 v60, v36, v37\n\t" \
  "v_mfma_f32_16x16x32_bf16 v[44:47], v[24:27], a[184:187], v[44:47]\n\t" \
  "v_cvt_pk_bf16_f32 v62, v38, v39\n\t" \
  "v_mfma_f32_16x16x32_bf16 v[40:43], v[12:15], a[188:191], v[40:43]\n\t" \
  "v_mfma_f32_16x16x32_bf16 v[44:47], v[28:31], a[188:191], v[44:47]\n\t" \
  "v_xor_b32 v81, 0x2000, v81\n\t" \
  "v_mfma_f32_16x16x32_bf16 v[32:35], v[0:3], a[192:195], 0\n\t" \
  "buffer_store_dwordx4 v[52:55], v80, %[ru], s45 offen offset:1024 sc1\n\t" \
  "v_mfma_f32_16x16x32_bf16 v[36:39], v[16:19], a[192:195], 0\n\t" \
  "v_xor_b32 v82, 0x2000, v82\n\t" \
  "v_mfma_f32_16x16x32_bf16 v[32:35], v[4:7], a[196:199], v[32:35]\n\t" \
  "v_cvt_pk_bf16_f32 v57, v40, v41\n\t" \
  "v_mfma_f32_16x16x32_bf16 v[36:39], v[20:23], a[196:199], v[36:39]\n\t" \
  "v_cvt_pk_bf16_f32 v59, v42, v43\n\t" \
  "v_mfma_f32_16x16x32_bf16 v[32:35], v[8:11], a[200:203], v[32:35]\n\t" \
  "v_cvt_pk_bf16_f32 v61, v44, v45\n\t" \
  "v_mfma_f32_16x16x32_bf16 v[36:39], v[24:27], a[200:203], v[36:39]\n\t" \
  "v_cvt_pk_bf16_f32 v63, v46, v47\n\t" \
  "v_mfma_f32_16x16x32_bf16 v[32:35], v[12:15], a[204:207], v[32:35]\n\t" \
  "s_add_i32 s79, s40, -1\n\t" \
  "v_xor_b32 v85, 16, v85\n\t" \
  "v_mfma_f32_16x16x32_bf16 v[36:39], v[28:31], a[204:207], v[36:39]\n\t" \
  "v_xor_b32 v86, 16, v86\n\t" \
  "ds_write_b32 v85, v73\n\t" \
  "v_mfma_f32_16x16x32_bf16 v[40:43], v[0:3], a[208:211], 0\n\t" \
  "buffer_store_dwordx4 v[56:59], v80, %[ru], s45 offen offset:2048 sc1\n\t" \
  "v_mfma_f32_16x16x32_bf16 v[44:47], v[16:19], a[208:211], 0\n\t" \
  "v_mov_b32 v74, s79\n\t" \
  "v_mfma_f32_16x16x32_bf16 v[40:43], v[4:7], a[212:215], v[40:43]\n\t" \
  "v_cvt_pk_bf16_f32 v48, v32, v33\n\t" \
  "v_mfma_f32_16x16x32_bf16 v[44:47], v[20:23], a[212:215], v[44:47]\n\t" \
  "v_cvt_pk_bf16_f32 v50, v34, v35\n\t" \
  "v_mfma_f32_16x16x32_bf16 v[40:43], v[8:11], a[216:219], v[40:43]\n\t" \
  "v_cvt_pk_bf16_f32 v52, v36, v37\n\t" \
  "v_mfma_f32_16x16x32_bf16 v[44:47], v[24:27], a[216:219], v[44:47]\n\t" \
  "v_cvt_pk_bf16_f32 v54, v38, v39\n\t" \
  "v_mfma_f32_16x16x32_bf16 v[40:43], v[12:15], a[220:223], v[40:43]\n\t" \
  "s_waitcnt vmcnt(6)\n\t" \
  "ds_write_b128 v82, v[64:67]\n\t" \
  "v_mfma_f32_16x16x32_bf16 v[44:47], v[28:31], a[220:223], v[44:47]\n\t" \
  "ds_write_b128 v82, v[68:71] offset:1024\n\t" \
  "v_mfma_f32_16x16x32_bf16 v[32:35], v[0:3], a[224:227], 0\n\t" \
  "buffer_store_dwordx4 v[60:63], v80, %[ru], s45 offen offset:3072 sc1\n\t" \
  "v_mfma_f32_16x16x32_bf16 v[36:39], v[16:19], a[224:227], 0\n\t" \
  "v_mfma_f32_16x16x32_bf16 v[32:35], v[4:7], a[228:231], v[32:35]\n\t" \
  "v_cvt_pk_bf16_f32 v49, v40, v41\n\t" \
  "v_mfma_f32_16x16x32_bf16 v[36:39], v[20:23], a[228:231], v[36:39]\n\t" \
  "v_cvt_pk_bf16_f32 v51, v42, v43\n\t" \
  "v_mfma_f32_16x16x32_bf16 v[32:35], v[8:11], a[232:235], v[32:35]\n\t" \
  "v_cvt_pk_bf16_f32 v53, v44, v45\n\t" \
  "v_mfma_f32_16x16x32_bf16 v[36:39], v[24:27], a[232:235], v[36:39]\n\t" \
  "v_cvt_pk_bf16_f32 v55, v46, v47\n\t" \
  "v_mfma_f32_16x16x32_bf16 v[32:35], v[12:15], a[236:239], v[32:35]\n\t" \
  "s_waitcnt lgkmcnt(0)\n\t" \
  "s_barrier\n\t" \
  "ds_read_b128 v[76:79], v86\n\t" \
  "v_mfma_f32_16x16x32_bf16 v[36:39], v[28:31], a[236:239], v[36:39]\n\t" \
  "s_add_i32 s45, s45, 0x1000\n\t" \
  "buffer_store_dword v74, v84, %[ru], 0 offen sc1\n\t" \
  "v_mfma_f32_16x16x32_bf16 v[40:43], v[0:3], a[240:243], 0\n\t" \
  "buffer_store_dwordx4 v[48:51], v80, %[ru], s45 offen offset:0 sc1\n\t" \
  "ds_read_b128 v[0:3], v81 offset:0\n\t" \
  "v_mfma_f32_16x16x32_bf16 v[44:47], v[16:19], a[240:243], 0\n\t" \
  "ds_read_b128 v[16:19], v81 offset:4096\n\t" \
  "v_mfma_f32_16x16x32_bf16 v[40:43], v[4:7], a[244:247], v[40:43]\n\t" \
  "v_cvt_pk_bf16_f32 v56, v32, v33\n\t" \
  "ds_read_b128 v[4:7], v81 offset:1024\n\t" \
  "v_mfma_f32_16x16x32_bf16 v[44:47], v[20:23], a[244:247], v[44:47]\n\t" \
  "v_cvt_pk_bf16_f32 v58, v34, v35\n\t" \
  "ds_read_b128 v[20:23], v81 offset:5120\n\t" \
  "v_mfma_f32_16x16x32_bf16 v[40:43], v[8:11], a[248:251], v[40:43]\n\t" \
  "v_cvt_pk_bf16_f32 v60, v36, v37\n\t" \
  "ds_read_b128 v[8:11], v81 offset:2048\n\t" \
  "v_mfma_f32_16x16x32_bf16 v[44:47], v[24:27], a[248:251], v[44:47]\n\t" \
  "v_cvt_pk_bf16_f32 v62, v38, v39\n\t" \
  "ds_read_b128 v[24:27], v81 offset:6144\n\t" \
  "v_mfma_f32_16x16x32_bf16 v[40:43], v[12:15], a[252:255], v[40:43]\n\t" \
  "s_waitcnt lgkmcnt(6)\n\t" \
  "v_min3_i32 v76, v76, v77, v78\n\t" \
  "ds_read_b128 v[12:15], v81 offset:3072\n\t" \
  "v_mfma_f32_16x16x32_bf16 v[44:47], v[28:31], a[252:255], v[44:47]\n\t" \
  "v_min3_i32 v76, v76, v79, 63\n\t" \
  "s_add_i32 s40, s40, 1\n\t" \
  "ds_read_b128 v[28:31], v81 offset:7168\n\t" \
  "v_readfirstlane_b32 s46, v76\n\t" \
  "s_mov_b32 s44, s45\n\t" \
  "s_cmp_le_i32 s40, s46\n\t" \
  "s_cbranch_scc1 20b\n\t" \
  "buffer_store_dwordx4 v[52:55], v80, %[ru], s44 offen offset:1024 sc1\n\t" \
  "s_nop 3\n\t" \
  "v_cvt_pk_bf16_f32 v57, v40, v41\n\t" \
  "v_cvt_pk_bf16_f32 v59, v42, v43\n\t" \
  "v_cvt_pk_bf16_f32 v61, v44, v45\n\t" \
  "v_cvt_pk_bf16_f32 v63, v46, v47\n\t" \
  "buffer_store_dwordx4 v[56:59], v80, %[ru], s44 offen offset:2048 sc1\n\t" \
  "buffer_store_dwordx4 v[60:63], v80, %[ru], s44 offen offset:3072 sc1\n\t" \
  "s_branch 99f\n\t" \
  "91:\n\t" \
  "ds_read_b128 v[0:3], v81 offset:0\n\t" \
  "ds_read_b128 v[16:19], v81 offset:4096\n\t" \
  "ds_read_b128 v[4:7], v81 offset:1024\n\t" \
  "ds_read_b128 v[20:23], v81 offset:5120\n\t" \
  "ds_read_b128 v[8:11], v81 offset:2048\n\t" \
  "ds_read_b128 v[24:27], v81 offset:6144\n\t" \
  "ds_read_b128 v[12:15], v81 offset:3072\n\t" \
  "ds_read_b128 v[28:31], v81 offset:7168\n\t" \
  ".p2align 6\n\t" \
  "20:\n\t" \
  "s_waitcnt lgkmcnt(7)\n\t" \
  "v_mfma_f32_16x16x32_bf16 v[32:35], v[0:3], a[0:3], 0\n\t" \
  "buffer_store_dwordx4 v[52:55], v80, %[ru], s44 offen offset:1024 sc1\n\t" \
  "s_waitcnt lgkmcnt(6)\n\t" \
  "v_mfma_f32_16x16x32_bf16 v[36:39], v[16:19], a[0:3], 0\n\t" \
  "s_and_b32 s45, s40, 15\n\t" \
  "s_waitcnt lgkmcnt(5)\n\t" \
  "v_mfma_f32_16x16x32_bf16 v[32:35], v[4:7], a[4:7], v[32:35]\n\t" \
  "v_cvt_pk_bf16_f32 v57, v40, v41\n\t" \
  "s_lshl_b32 s45, s45, 22\n\t" \
  "s_waitcnt lgkmcnt(4)\n\t" \
  "v_mfma_f32_16x16x32_bf16 v[36:39], v[20:23], a[4:7], v[36:39]\n\t" \
  "v_cvt_pk_bf16_f32 v59, v42, v43\n\t" \
  "s_add_i32 s45, s45, s42\n\t" \
  "s_waitcnt lgkmcnt(3)\n\t" \
  "v_mfma_f32_16x16x32_bf16 v[32:35], v[8:11], a[8:11], v[32:35]\n\t" \
  "v_cvt_pk_bf16_f32 v61, v44, v45\n\t" \
  "s_lshr_b32 s76, s45, 21\n\t" \
  "s_waitcnt lgkmcnt(2)\n\t" \
  "v_mfma_f32_16x16x32_bf16 v[36:39], v[24:27], a[8:11], v[36:39]\n\t" \
  "v_cvt_pk_bf16_f32 v63, v46, v47\n\t" \
  "s_bitcmp0_b32 %[sel], s76\n\t" \
  "s_cselect_b32 s76, 0x2000, 0\n\t" \
  "s_waitcnt lgkmcnt(1)\n\t" \
  "v_mfma_f32_16x16x32_bf16 v[32:35], v[12:15], a[12:15], v[32:35]\n\t" \
  "s_add_i32 s45, s45, s76\n\t" \
  "s_waitcnt lgkmcnt(0)\n\t" \
  "v_mfma_f32_16x16x32_bf16 v[36:39], v[28:31], a[12:15], v[36:39]\n\t" \
  "s_add_i32 s77, s40, 1\n\t" \
  "v_mfma_f32_16x16x32_bf16 v[40:43], v[0:3], a[16:19], 0\n\t" \
  "buffer_store_dwordx4 v[56:59], v80, %[ru], s44 offen offset:2048 sc1\n\t" \
  "v_mfma_f32_16x16x32_bf16 v[44:47], v[16:19], a[16:19], 0\n\t" \
  "s_min_u32 s77, s77, 63\n\t" \
  "v_mfma_f32_16x16x32_bf16 v[40:43], v[4:7], a[20:23], v[40:43]\n\t" \
  "v_cvt_pk_bf16_f32 v48, v32, v33\n\t" \
  "s_and_b32 s76, s77, 4\n\t" \
  "v_mfma_f32_16x16x32_bf16 v[44:47], v[20:23], a[20:23], v[44:47]\n\t" \
  "v_cvt_pk_bf16_f32 v50, v34, v35\n\t" \
  "s_lshl_b32 s76, s76, 11\n\t" \
  "v_mfma_f32_16x16x32_bf16 v[40:43], v[8:11], a[24:27], v[40:43]\n\t" \
  "v_cvt_pk_bf16_f32 v52, v36, v37\n\t" \
  "s_lshl_b32 s77, s77, 19\n\t" \
  "v_mfma_f32_16x16x32_bf16 v[44:47], v[24:27], a[24:27], v[44:47]\n\t" \
  "v_cvt_pk_bf16_f32 v54, v38, v39\n\t" \
  "s_add_i32 s77, s77, s43\n\t" \
  "v_mfma_f32_16x16x32_bf16 v[40:43], v[12:15], a[28:31], v[40:43]\n\t" \
  "s_xor_b32 s77, s77, s76\n\t" \
  "v_mfma_f32_16x16x32_bf16 v[44:47], v[28:31], a[28:31], v[44:47]\n\t" \
  "s_cmp_lt_u32 s40, 3\n\t" \
  "s_cselect_b32 s76, 0x2000, 0\n\t" \
  "v_mfma_f32_16x16x32_bf16 v[32:35], v[0:3], a[32:35], 0\n\t" \
  "buffer_store_dwordx4 v[60:63], v80, %[ru], s44 offen offset:3072 sc1\n\t" \
  "v_mfma_f32_16x16x32_bf16 v[36:39], v[16:19], a[32:35], 0\n\t" \
  "s_andn2_b32 s77, s77, s76\n\t" \
  "v_mfma_f32_16x16x32_bf16 v[32:35], v[4:7], a[36:39], v[32:35]\n\t" \
  "v_cvt_pk_bf16_f32 v49, v40, v41\n\t" \
  "s_add_i32 s78, s40, 3\n\t" \
  "v_mfma_f32_16x16x32_bf16 v[36:39], v[20:23], a[36:39], v[36:39]\n\t" \
  "v_cvt_pk_bf16_f32 v51, v42, v43\n\t" \
  "s_cmp_lt_i32 s41, s78\n\t" \
  "s_cselect_b32 s78, 0, 0x7f000000\n\t" \
  "v_mfma_f32_16x16x32_bf16 v[32:35], v[8:11], a[40:43], v[32:35]\n\t" \
  "v_cvt_pk_bf16_f32 v53, v44, v45\n\t" \
  "v_mfma_f32_16x16x32_bf16 v[36:39], v[24:27], a[40:43], v[36:39]\n\t" \
  "v_cvt_pk_bf16_f32 v55, v46, v47\n\t" \
  "v_mfma_f32_16x16x32_bf16 v[32:35], v[12:15], a[44:47], v[32:35]\n\t" \
  "v_mfma_f32_16x16x32_bf16 v[36:39], v[28:31], a[44:47], v[36:39]\n\t" \
  "s_mov_b64 exec, 3\n\t" \
  "buffer_load_dword v72, v83, %[ru], s78 offen sc1\n\t" \
  "s_mov_b64 exec, -1\n\t" \
  "v_mfma_f32_16x16x32_bf16 v[40:43], v[0:3], a[48:51], 0\n\t" \
  "buffer_store_dwordx4 v[48:51], v80, %[ru], s45 offen offset:0 sc1\n\t" \
  "v_mfma_f32_16x16x32_bf16 v[44:47], v[16:19], a[48:51], 0\n\t" \
  "v_mfma_f32_16x16x32_bf16 v[40:43], v[4:7], a[52:55], v[40:43]\n\t" \
  "v_cvt_pk_bf16_f32 v56, v32, v33\n\t" \
  "v_mfma_f32_16x16x32_bf16 v[44:47], v[20:23], a[52:55], v[44:47]\n\t" \
  "v_cvt_pk_bf16_f32 v58, v34, v35\n\t" \
  "v_mfma_f32_16x16x32_bf16 v[40:43], v[8:11], a[56:59], v[40:43]\n\t" \
  "v_cvt_pk_bf16_f32 v60, v36, v37\n\t" \
  "v_mfma_f32_16x16x32_bf16 v[44:47], v[24:27], a[56:59], v[44:47]\n\t" \
  "v_cvt_pk_bf16_f32 v62, v38, v39\n\t" \
  "v_mfma_f32_16x16x32_bf16 v[40:43], v[12:15], a[60:63], v[40:43]\n\t" \
  "v_mfma_f32_16x16x32_bf16 v[44:47], v[28:31], a[60:63], v[44:47]\n\t" \
  "v_mfma_f32_16x16x32_bf16 v[32:35], v[0:3], a[64:67], 0\n\t" \
  "buffer_store_dwordx4 v[52:55], v80, %[ru], s45 offen offset:1024 sc1\n\t" \
  "v_mfma_f32_16x16x32_bf16 v[36:39], v[16:19], a[64:67], 0\n\t" \
  "v_mfma_f32_16x16x32_bf16 v[32:35], v[4:7], a[68:71], v[32:35]\n\t" \
  "v_cvt_pk_bf16_f32 v57, v40, v41\n\t" \
  "v_mfma_f32_16x16x32_bf16 v[36:39], v[20:23], a[68:71], v[36:39]\n\t" \
  "v_cvt_pk_bf16_f32 v59, v42, v43\n\t" \
  "v_mfma_f32_16x16x32_bf16 v[32:35], v[8:11], a[72:75], v[32:35]\n\t" \
  "v_cvt_pk_bf16_f32 v61, v44, v45\n\t" \
  "v_mfma_f32_16x16x32_bf16 v[36:39], v[24:27], a[72:75], v[36:39]\n\t" \
  "v_cvt_pk_bf16_f32 v63, v46, v47\n\t" \
  "v_mfma_f32_16x16x32_bf16 v[32:35], v[12:15], a[76:79], v[32:35]\n\t" \
  "v_mfma_f32_16x16x32_bf16 v[36:39], v[28:31], a[76:79], v[36:39]\n\t" \
  "v_mfma_f32_16x16x32_bf16 v[40:43], v[0:3], a[80:83], 0\n\t" \
  "buffer_store_dwordx4 v[56:59], v80, %[ru], s45 offen offset:2048 sc1\n\t" \
  "v_mfma_f32_16x16x32_bf16 v[44:47], v[16:19], a[80:83], 0\n\t" \
  "v_mfma_f32_16x16x32_bf16 v[40:43], v[4:7], a[84:87], v[40:43]\n\t" \
  "v_cvt_pk_bf16_f32 v48, v32, v33\n\t" \
  "v_mfma_f32_16x16x32_bf16 v[44:47], v[20:23], a[84:87], v[44:47]\n\t" \
  "v_cvt_pk_bf16_f32 v50, v34, v35\n\t" \
  "v_mfma_f32_16x16x32_bf16 v[40:43], v[8:11], a[88:91], v[40:43]\n\t" \
  "v_cvt_pk_bf16_f32 v52, v36, v37\n\t" \
  "v_mfma_f32_16x16x32_bf16 v[44:47], v[24:27], a[88:91], v[44:47]\n\t" \
  "v_cvt_pk_bf16_f32 v54, v38, v39\n\t" \
  "v_mfma_f32_16x16x32_bf16 v[40:43], v[12:15], a[92:95], v[40:43]\n\t" \
  "v_mfma_f32_16x16x32_bf16 v[44:47], v[28:31], a[92:95], v[44:47]\n\t" \
  "v_mfma_f32_16x16x32_bf16 v[32:35], v[0:3], a[96:99], 0\n\t" \
  "buffer_store_dwordx4 v[60:63], v80, %[ru], s45 offen offset:3072 sc1\n\t" \
  "v_mfma_f32_16x16x32_bf16 v[36:39], v[16:19], a[96:99], 0\n\t" \
  "v_mfma_f32_16x16x32_bf16 v[32:35], v[4:7], a[100:103], v[32:35]\n\t" \
  "v_cvt_pk_bf16_f32 v49, v40, v41\n\t" \
  "v_mfma_f32_16x16x32_bf16 v[36:39], v[20:23], a[100:103], v[36:39]\n\t" \
  "v_cvt_pk_bf16_f32 v51, v42, v43\n\t" \
  "v_mfma_f32_16x16x32_bf16 v[32:35], v[8:11], a[104:107], v[32:35]\n\t" \
  "v_cvt_pk_bf16_f32 v53, v44, v45\n\t" \
  "v_mfma_f32_16x16x32_bf16 v[36:39], v[24:27], a[104:107], v[36:39]\n\t" \
  "v_cvt_pk_bf16_f32 v55, v46, v47\n\t" \
  "v_mfma_f32_16x16x32_bf16 v[32:35], v[12:15], a[108:111], v[32:35]\n\t" \
  "v_mfma_f32_16x16x32_bf16 v[36:39], v[28:31], a[108:111], v[36:39]\n\t" \
  "s_add_i32 s45, s45, 0x1000\n\t" \
  "v_mfma_f32_16x16x32_bf16 v[40:43], v[0:3], a[112:115], 0\n\t" \
  "buffer_store_dwordx4 v[48:51], v80, %[ru], s45 offen offset:0 sc1\n\t" \
  "v_mfma_f32_16x16x32_bf16 v[44:47], v[16:19], a[112:115], 0\n\t" \
  "v_mfma_f32_16x16x32_bf16 v[40:43], v[4:7], a[116:119], v[40:43]\n\t" \
  "v_cvt_pk_bf16_f32 v56, v32, v33\n\t" \
  "v_mfma_f32_16x16x32_bf16 v[44:47], v[20:23], a[116:119], v[44:47]\n\t" \
  "v_cvt_pk_bf16_f32 v58, v34, v35\n\t" \
  "v_mfma_f32_16x16x32_bf16 v[40:43], v[8:11], a[120:123], v[40:43]\n\t" \
  "v_cvt_pk_bf16_f32 v60, v36, v37\n\t" \
  "v_mfma_f32_16x16x32_bf16 v[44:47], v[24:27], a[120:123], v[44:47]\n\t" \
  "v_cvt_pk_bf16_f32 v62, v38, v39\n\t" \
  "v_mfma_f32_16x16x32_bf16 v[40:43], v[12:15], a[124:127], v[40:43]\n\t" \
  "s_waitcnt vmcnt(5)\n\t" \
  "buffer_load_dwordx4 v[64:67], v80, %[ru], s77 offen sc1\n\t" \
  "v_mfma_f32_16x16x32_bf16 v[44:47], v[28:31], a[124:127], v[44:47]\n\t" \
  "buffer_load_dwordx4 v[68:71], v80, %[ru], s77 offen offset:1024 sc1\n\t" \
  "v_mfma_f32_16x16x32_bf16 v[32:35], v[0:3], a[128:131], 0\n\t" \
  "buffer_store_dwordx4 v[52:55], v80, %[ru], s45 offen offset:1024 sc1\n\t" \
  "v_mfma_f32_16x16x32_bf16 v[36:39], v[16:19], a[128:131], 0\n\t" \
  "v_readlane_b32 s76, v72, 0\n\t" \
  "v_readlane_b32 s79, v72, 1\n\t" \
  "v_mfma_f32_16x16x32_bf16 v[32:35], v[4:7], a[132:135], v[32:35]\n\t" \
  "v_cvt_pk_bf16_f32 v57, v40, v41\n\t" \
  "v_mfma_f32_16x16x32_bf16 v[36:39], v[20:23], a[132:135], v[36:39]\n\t" \
  "v_cvt_pk_bf16_f32 v59, v42, v43\n\t" \
  "v_mfma_f32_16x16x32_bf16 v[32:35], v[8:11], a[136:139], v[32:35]\n\t" \
  "v_cvt_pk_bf16_f32 v61, v44, v45\n\t" \
  "v_mfma_f32_16x16x32_bf16 v[36:39], v[24:27], a[136:139], v[36:39]\n\t" \
  "v_cvt_pk_bf16_f32 v63, v46, v47\n\t" \
  "v_mfma_f32_16x16x32_bf16 v[32:35], v[12:15], a[140:143], v[32:35]\n\t" \
  "s_add_i32 s79, s79, 15\n\t" \
  "s_min_i32 s76, s76, s79\n\t" \
  "v_mfma_f32_16x16x32_bf16 v[36:39], v[28:31], a[140:143], v[36:39]\n\t" \
  "s_max_i32 s41, s41, s76\n\t" \
  "v_mov_b32 v73, s41\n\t" \
  "v_mfma_f32_16x16x32_bf16 v[40:43], v[0:3], a[144:147], 0\n\t" \
  "buffer_store_dwordx4 v[56:59], v80, %[ru], s45 offen offset:2048 sc1\n\t" \
  "v_mfma_f32_16x16x32_bf16 v[44:47], v[16:19], a[144:147], 0\n\t" \
  "v_mfma_f32_16x16x32_bf16 v[40:43], v[4:7], a[148:151], v[40:43]\n\t" \
  "v_cvt_pk_bf16_f32 v48, v32, v33\n\t" \
  "v_mfma_f32_16x16x32_bf16 v[44:47], v[20:23], a[148:151], v[44:47]\n\t" \
  "v_cvt_pk_bf16_f32 v50, v34, v35\n\t" \
  "v_mfma_f32_16x16x32_bf16 v[40:43], v[8:11], a[152:155], v[40:43]\n\t" \
  "v_cvt_pk_bf16_f32 v52, v36, v37\n\t" \
  "v_mfma_f32_16x16x32_bf16 v[44:47], v[24:27], a[152:155], v[44:47]\n\t" \
  "v_cvt_pk_bf16_f32 v54, v38, v39\n\t" \
  "v_mfma_f32_16x16x32_bf16 v[40:43], v[12:15], a[156:159], v[40:43]\n\t" \
  "v_mfma_f32_16x16x32_bf16 v[44:47], v[28:31], a[156:159], v[44:47]\n\t" \
  "v_mfma_f32_16x16x32_bf16 v[32:35], v[0:3], a[160:163], 0\n\t" \
  "buffer_store_dwordx4 v[60:63], v80, %[ru], s45 offen offset:3072 sc1\n\t" \
  "v_mfma_f32_16x16x32_bf16 v[36:39], v[16:19], a[160:163], 0\n\t" \
  "v_mfma_f32_16x16x32_bf16 v[32:35], v[4:7], a[164:167], v[32:35]\n\t" \
  "v_cvt_pk_bf16_f32 v49, v40, v41\n\t" \
  "v_mfma_f32_16x16x32_bf16 v[36:39], v[20:23], a[164:167], v[36:39]\n\t" \
  "v_cvt_pk_bf16_f32 v51, v42, v43\n\t" \
  "v_mfma_f32_16x16x32_bf16 v[32:35], v[8:11], a[168:171], v[32:35]\n\t" \
  "v_cvt_pk_bf16_f32 v53, v44, v45\n\t" \
  "v_mfma_f32_16x16x32_bf16 v[36:39], v[24:27], a[168:171], v[36:39]\n\t" \
  "v_cvt_pk_bf16_f32 v55, v46, v47\n\t" \
  "v_mfma_f32_16x16x32_bf16 v[32:35], v[12:15], a[172:175], v[32:35]\n\t" \
  "v_mfma_f32_16x16x32_bf16 v[36:39], v[28:31], a[172:175], v[36:39]\n\t" \
  "s_add_i32 s45, s45, 0x3000\n\t" \
  "v_mfma_f32_16x16x32_bf16 v[40:43], v[0:3], a[176:179], 0\n\t" \
  "buffer_store_dwordx4 v[48:51], v80, %[ru], s45 offen offset:0 sc1\n\t" \
  "v_mfma_f32_16x16x32_bf16 v[44:47], v[16:19], a[176:179], 0\n\t" \
  "v_mfma_f32_16x16x32_bf16 v[40:43], v[4:7], a[180:183], v[40:43]\n\t" \
  "v_cvt_pk_bf16_f32 v56, v32, v33\n\t" \
  "v_mfma_f32_16x16x32_bf16 v[44:47], v[20:23], a[180:183], v[44:47]\n\t" \
  "v_cvt_pk_bf16_f32 v58, v34, v35\n\t" \
  "v_mfma_f32_16x16x32_bf16 v[40:43], v[8:11], a[184:187], v[40:43]\n\t" \
  "v_cvt_pk_bf16_f32 v60, v36, v37\n\t" \
  "v_mfma_f32_16x16x32_bf16 v[44:47], v[24:27], a[184:187], v[44:47]\n\t" \
  "v_cvt_pk_bf16_f32 v62, v38, v39\n\t" \
  "v_mfma_f32_16x16x32_bf16 v[40:43], v[12:15], a[188:191], v[40:43]\n\t" \
  "v_mfma_f32_16x16x32_bf16 v[44:47], v[28:31], a[188:191], v[44:47]\n\t" \
  "v_xor_b32 v81, 0x2000, v81\n\t" \
  "v_mfma_f32_16x16x32_bf16 v[32:35], v[0:3], a[192:195], 0\n\t" \
  "buffer_store_dwordx4 v[52:55], v80, %[ru], s45 offen offset:1024 sc1\n\t" \
  "v_mfma_f32_16x16x32_bf16 v[36:39], v[16:19], a[192:195], 0\n\t" \
  "v_xor_b32 v82, 0x2000, v82\n\t" \
  "v_mfma_f32_16x16x32_bf16 v[32:35], v[4:7], a[196:199], v[32:35]\n\t" \
  "v_cvt_pk_bf16_f32 v57, v40, v41\n\t" \
  "v_mfma_f32_16x16x32_bf16 v[36:39], v[20:23], a[196:199], v[36:39]\n\t" \
  "v_cvt_pk_bf16_f32 v59, v42, v43\n\t" \
  "v_mfma_f32_16x16x32_bf16 v[32:35], v[8:11], a[200:203], v[32:35]\n\t" \
  "v_cvt_pk_bf16_f32 v61, v44, v45\n\t" \
  "v_mfma_f32_16x16x32_bf16 v[36:39], v[24:27], a[200:203], v[36:39]\n\t" \
  "v_cvt_pk_bf16_f32 v63, v46, v47\n\t" \
  "v_mfma_f32_16x16x32_bf16 v[32:35], v[12:15], a[204:207], v[32:35]\n\t" \
  "s_add_i32 s79, s40, -1\n\t" \
  "v_xor_b32 v85, 16, v85\n\t" \
  "v_mfma_f32_16x16x32_bf16 v[36:39], v[28:31], a[204:207], v[36:39]\n\t" \
  "v_xor_b32 v86, 16, v86\n\t" \
  "ds_write_b32 v85, v73\n\t" \
  "v_mfma_f32_16x16x32_bf16 v[40:43], v[0:3], a[208:211], 0\n\t" \
  "buffer_store_dwordx4 v[56:59], v80, %[ru], s45 offen offset:2048 sc1\n\t" \
  "v_mfma_f32_16x16x32_bf16 v[44:47], v[16:19], a[208:211], 0\n\t" \
  "v_mov_b32 v74, s79\n\t" \
  "v_mfma_f32_16x16x32_bf16 v[40:43], v[4:7], a[212:215], v[40:43]\n\t" \
  "v_cvt_pk_bf16_f32 v48, v32, v33\n\t" \
  "v_mfma_f32_16x16x32_bf16 v[44:47], v[20:23], a[212:215], v[44:47]\n\t" \
  "v_cvt_pk_bf16_f32 v50, v34, v35\n\t" \
  "v_mfma_f32_16x16x32_bf16 v[40:43], v[8:11], a[216:219], v[40:43]\n\t" \
  "v_cvt_pk_bf16_f32 v52, v36, v37\n\t" \
  "v_mfma_f32_16x16x32_bf16 v[44:47], v[24:27], a[216:219], v[44:47]\n\t" \
  "v_cvt_pk_bf16_f32 v54, v38, v39\n\t" \
  "v_mfma_f32_16x16x32_bf16 v[40:43], v[12:15], a[220:223], v[40:43]\n\t" \
  "s_waitcnt vmcnt(6)\n\t" \
  "ds_write_b128 v82, v[64:67]\n\t" \
  "v_mfma_f32_16x16x32_bf16 v[44:47], v[28:31], a[220:223], v[44:47]\n\t" \
  "ds_write_b128 v82, v[68:71] offset:1024\n\t" \
  "v_mfma_f32_16x16x32_bf16 v[32:35], v[0:3], a[224:227], 0\n\t" \
  "buffer_store_dwordx4 v[60:63], v80, %[ru], s45 offen offset:3072 sc1\n\t" \
  "v_mfma_f32_16x16x32_bf16 v[36:39], v[16:19], a[224:227], 0\n\t" \
  "v_mfma_f32_16x16x32_bf16 v[32:35], v[4:7], a[228:231], v[32:35]\n\t" \
  "v_cvt_pk_bf16_f32 v49, v40, v41\n\t" \
  "v_mfma_f32_16x16x32_bf16 v[36:39], v[20:23], a[228:231], v[36:39]\n\t" \
  "v_cvt_pk_bf16_f32 v51, v42, v43\n\t" \
  "v_mfma_f32_16x16x32_bf16 v[32:35], v[8:11], a[232:235], v[32:35]\n\t" \
  "v_cvt_pk_bf16_f32 v53, v44, v45\n\t" \
  "v_mfma_f32_16x16x32_bf16 v[36:39], v[24:27], a[232:235], v[36:39]\n\t" \
  "v_cvt_pk_bf16_f32 v55, v46, v47\n\t" \
  "v_mfma_f32_16x16x32_bf16 v[32:35], v[12:15], a[236:239], v[32:35]\n\t" \
  "s_waitcnt lgkmcnt(0)\n\t" \
  "s_barrier\n\t" \
  "ds_read_b128 v[76:79], v86\n\t" \
  "v_mfma_f32_16x16x32_bf16 v[36:39], v[28:31], a[236:239], v[36:39]\n\t" \
  "s_add_i32 s45, s45, 0x1000\n\t" \
  "v_mfma_f32_16x16x32_bf16 v[40:43], v[0:3], a[240:243], 0\n\t" \
  "buffer_store_dwordx4 v[48:51], v80, %[ru], s45 offen offset:0 sc1\n\t" \
  "ds_read_b128 v[0:3], v81 offset:0\n\t" \
  "v_mfma_f32_16x16x32_bf16 v[44:47], v[16:19], a[240:243], 0\n\t" \
  "ds_read_b128 v[16:19], v81 offset:4096\n\t" \
  "v_mfma_f32_16x16x32_bf16 v[40:43], v[4:7], a[244:247], v[40:43]\n\t" \
  "v_cvt_pk_bf16_f32 v56, v32, v33\n\t" \
  "ds_read_b128 v[4:7], v81 offset:1024\n\t" \
  "v_mfma_f32_16x16x32_bf16 v[44:47], v[20:23], a[244:247], v[44:47]\n\t" \
  "v_cvt_pk_bf16_f32 v58, v34, v35\n\t" \
  "ds_read_b128 v[20:23], v81 offset:5120\n\t" \
  "v_mfma_f32_16x16x32_bf16 v[40:43], v[8:11], a[248:251], v[40:43]\n\t" \
  "v_cvt_pk_bf16_f32 v60, v36, v37\n\t" \
  "ds_read_b128 v[8:11], v81 offset:2048\n\t" \
  "v_mfma_f32_16x16x32_bf16 v[44:47], v[24:27], a[248:251], v[44:47]\n\t" \
  "v_cvt_pk_bf16_f32 v62, v38, v39\n\t" \
  "ds_read_b128 v[24:27], v81 offset:6144\n\t" \
  "v_mfma_f32_16x16x32_bf16 v[40:43], v[12:15], a[252:255], v[40:43]\n\t" \
  "s_waitcnt lgkmcnt(6)\n\t" \
  "v_min3_i32 v76, v76, v77, v78\n\t" \
  "ds_read_b128 v[12:15], v81 offset:3072\n\t" \
  "v_mfma_f32_16x16x32_bf16 v[44:47], v[28:31], a[252:255], v[44:47]\n\t" \
  "v_min3_i32 v76, v76, v79, 63\n\t" \
  "s_add_i32 s40, s40, 1\n\t" \
  "ds_read_b128 v[28:31], v81 offset:7168\n\t" \
  "v_readfirstlane_b32 s46, v76\n\t" \
  "s_mov_b32 s44, s45\n\t" \
  "s_cmp_le_i32 s40, s46\n\t" \
  "s_cbranch_scc1 20b\n\t" \
  "buffer_store_dwordx4 v[52:55], v80, %[ru], s44 offen offset:1024 sc1\n\t" \
  "s_nop 3\n\t" \
  "v_cvt_pk_bf16_f32 v57, v40, v41\n\t" \
  "v_cvt_pk_bf16_f32 v59, v42, v43\n\t" \
  "v_cvt_pk_bf16_f32 v61, v44, v45\n\t" \
  "v_cvt_pk_bf16_f32 v63, v46, v47\n\t" \
  "buffer_store_dwordx4 v[56:59], v80, %[ru], s44 offen offset:2048 sc1\n\t" \
  "buffer_store_dwordx4 v[60:63], v80, %[ru], s44 offen offset:3072 sc1\n\t" \
  "s_branch 99f\n\t" \
  "99:\n\t" \
  "s_waitcnt vmcnt(0) lgkmcnt(0)\n\t" \
  ""

#define V10(x) "v" #x "0", "v" #x "1", "v" #x "2", "v" #x "3", "v" #x "4", "v" #x "5", "v" #x "6", "v" #x "7", "v" #x "8", "v" #x "9"
#define A10(x) "a" #x "0", "a" #x "1", "a" #x "2", "a" #x "3", "a" #x "4", "a" #x "5", "a" #x "6", "a" #x "7", "a" #x "8", "a" #x "9"
#define V90 V10(), V10(1), V10(2), V10(3), V10(4), V10(5), V10(6), V10(7), V10(8)
#define S22 "s56", "s57", "s58", "s59", "s76", "s77", "s78", "s79", "s80", "s81", "s82", "s83", "s84", "s85", "s86", "s87", "s88", "s89", \
  "s90", "s91", "s92", "s93", "s94", "s95", "s96", "s97"
#define STEP_PP
#define STEP_CL
#define STEP_PV
#define CB_PV , "{v248}"(C24)
#define STEPS(BODY, ...) asm volatile(BODY : "=&{v[92:99]}"(X), "+&{s[40:55]}"(P) STEP_PV \
  : "{v[120:151]}"(W.v[0]), "{v[152:183]}"(W.v[1]), "{v[184:215]}"(W.v[2]), "{v[216:247]}"(W.v[3]), "{v[100:115]}"(C), [rh] "s"(rh), [wv] "s"(w), [ru] "s"(rum) STEP_PP __VA_ARGS__ \
  : "memory", "vcc", "scc", S22, STEP_CL V90, "v90", "v91", "v116", "v117", "v118")
__device__ __forceinline__ void keep_a(const Wts& W) {
#pragma unroll
  for (int i = 0; i < 32; ++i) asm volatile("" :: "a"(W.a[i]));
}

__device__ __forceinline__ void steps_init(u32x16& P, int h, int w, unsigned place, Sel sel) {
#pragma unroll
  for (int i = 0; i < 16; ++i) P[i] = 0;
  P[2] = 1; P[3] = CD0; P[14] = H_MEM + hot_q(RHO(h), sel) + w * 1024; P[15] = place;
}
__device__ __forceinline__ void steps_enter(u32x16& P, Clk& ck, bool dead) {
  ck.sync();
  P[2] = (P[2] & 3u) | (dead ? 4u : 0u); P[6] = ck.c2t << 15; P[11] = ck.ce; P[12] = ck.t2c; P[13] = ck.te;
#pragma unroll
  for (int i = 0; i < 16; ++i) P[i] = __builtin_amdgcn_readfirstlane(P[i]);
}

__device__ __forceinline__ void role_qk(const ChainArgs& a, int h, char* smem, const int w, Gate& G) {
  int lane = opaque_tid(0);
  const int b = h; (void)b;
  u64* prof = a.prof; (void)prof;
  PDECL(L_PRF, 4);
  const float CS = 0.08838834764831845f * 1.4426950408889634f;
  GAS unsigned* abortw = (GAS unsigned*)(a.sync + SY_ABORT);
  bool dead = false;
  Wts W;
  Clk ck; ck.init();
  load_weights<0>(a, h, w, lane, smem, W);
  if (!G.pass(a.sync)) return;
  PZERO;
  const Sel sel = sel_wait(a.sync, abortw, lane, dead, true, CT_PR + h, 0, CT_PR + 32 + h, 1);
  const __amdgpu_buffer_rsrc_t rh = rsrc_hot(a.sync, sel), rum = rsrc_u(a.sync);
#pragma unroll
  for (int i = 0; i < 2; ++i) *(u32x4*)(smem + L_KX + (2 * w + i) * 1024 + lane * 16) = ld16(rum, x_base(1) + x_first(1) + h * CHK + (2 * w + i) * 1024 + lane * 16);
  const unsigned hq = (h >> 3) * 16 * CHK;
  auto pz_o = [&](int l) { return (l & 1) ? OOR : H_PZ + hq + (h & 7) * 256 + w * 128 + (l & 8) * 8 + (l >> 4) * 16 + (l & 6) * 2; };
  auto z_at = [&](int l, int half) { return ((l & 6) >> 1) == 2 * w + ((h >> 2) & 1) && ((l >> 4) & 1) == half ? 1.f : 0.f; };
  u32x16 C, P;
  unsigned C16;
  {
    const int r = lane & 15, g = lane >> 4;
    const unsigned l0 = (unsigned)(uintptr_t)(LAS char*)smem;
    C[0] = lane * 16; C[1] = l0 + L_MEM + lane * 16; C[2] = l0 + L_WTS + w * 4096 + lane * 16; C[3] = C[2] + 65536;
    C[4] = (lane & 2) ? 0xEEEE : 0x4444;
    C[5] = l0 + L_XCH + (16 * w + 2 * (r >> 2) + (r & 1)) * 32 + g * 8 + ((r >> 1) & 1) * 4;
    C[6] = l0 + L_XK + (32 * w + r) * 16 + g * 4;
    C[7] = w < 2 ? l0 + L_XCH + (8 * g + (r >> 2)) * 32 + (r & 3) * 8 : SY_CT + (CT_PR + 32 * (w - 2) + h) * 128;
    C[8] = l0 + L_XK + (8 * g + (r >> 2)) * 16 + (r & 1) * 8;
    C[9] = w == 2 ? (lane == 0 ? H_FLG + h * 128 : OOR) :
           lane < 32 ? H_P + hq + (h & 7) * 512 + lane * 16 : (lane < 48 ? H_PZ + hq + (h & 7) * 256 + (lane - 32) * 16 : OOR);
    C[10] = H_TR + copy_q((lane >> 3) & 3, h >> 3, sel) + (lane & 7) * 128; C[11] = l0 + L_PRF + w * 1024 + (lane >> 6);
    C[12] = H_P + hq + (h & 7) * 512 + w * 256 + (r >> 2) * 64 + g * 16 + (r & 3) * 4; C[13] = pz_o(lane);
    C[14] = __float_as_uint(z_at(lane, 0)); C[15] = __float_as_uint(z_at(lane, 1));
    C16 = l0 + L_KX + (r >> 3) * 4096 + (g * 16 + 2 * (r & 7)) * 16;
    if (w == 3) { C[12] = lane < 4 ? H_ANN + hq + quarter(lane) + (h & 7) * 128 : OOR; C[13] = lane == 4 ? H_FLG + h * 128 : C[12]; }
    if (w == 2) {
#pragma unroll
      for (int i = 0; i < 4; ++i) C[12 + i] = 4 * g + i <= r ? 0u : 0xff800000u;
      C16 = H_PX + (h >> 2) * CHK + (h & 3) * 2048 + lane * 16;
    }
    asm volatile("" : "+v"(C), "+v"(C16));
  }
  SSTAMP(96 + h);
  steps_init(P, h, w, w < 2 ? copy_q(h >> 3, h >> 3, sel) - hq : x_base(w & 1) + h * CHK, sel);
  unsigned spins = 0;
  while (true) {
    u32x8 X; u32x16 pv; (void)pv;
    steps_enter(P, ck, dead);
    STEPS(QK_BODY, , [cs] "s"(CS), "{v119}"(C16), [hm] "s"(x_half(w & 1, sel)), [d0] "s"(x_first(w & 1)));
    keep_a(W);
    const unsigned res = __builtin_amdgcn_readfirstlane(P[2]), s = __builtin_amdgcn_readfirstlane(P[4]);
    lane = opaque_tid(0);
    if (res & 8u) {
      auto sw = [](float v, int k) { return __int_as_float(k == 0 ? __builtin_amdgcn_mov_dpp(__float_as_int(v), 0xB1, 0xf, 0xf, false) :
                                                           k == 1 ? __builtin_amdgcn_mov_dpp(__float_as_int(v), 0x4E, 0xf, 0xf, false) :
                                                                    __builtin_amdgcn_mov_dpp(__float_as_int(v), 0x141, 0xf, 0xf, false)); };
      float d = fmaxf(__uint_as_float(X[4]), 0.f);
      d = fmaxf(d, sw(d, 0)); d = fmaxf(d, sw(d, 1)); d = fmaxf(d, sw(d, 2));
      const float x = __builtin_amdgcn_exp2f(__uint_as_float(X[4]) - d), y = __builtin_amdgcn_exp2f(-d), xn = sw(x, 0);
      float z = fmaf(__uint_as_float(X[0]), y, x);
      z += sw(z, 0); z += sw(z, 1); z += sw(z, 2);
      z = __builtin_amdgcn_rcpf(z);
      const unsigned pm = nosent((lane & 8) ? pk2(z_at(lane, 0) * y * z, z_at(lane, 1) * y * z) : pk2(x * z, xn * z));
      if (!(__uint_as_float(X[5]) < 1.0e18f))
        for (int q = 0; q < 4; ++q) __builtin_amdgcn_raw_buffer_store_b32(pm, rh, pz_o(lane), ((s - 1) & 3) * 4 * CHK + quarter(q), ST_AUX);
    }
    if (res & 64u) break;
    if (res & 32u) ctr_wait(ctr(a, CT_PR + 32 * (w - 2) + h), min(s + w - 1, (unsigned)NSEG), abortw, lane, dead);
    if (res & 17u) {
      spins |= 63u;
      if (dead || poll_fail(spins, abortw, lane)) dead = true;
    }
  }
  if (w == 2 && lane == 0) st4(rh, H_FLG + 12 * CHK + h * 128, NSEG - 1);
  PDUMP(0);
}

__device__ __forceinline__ void role_vo(const ChainArgs& a, int h, char* smem, const int w, Gate& G) {
  int lane = opaque_tid(0);
  const int b = 32 + h; (void)b;
  u64* prof = a.prof; (void)prof;
  PDECL(L_PRF, 4);
  GAS unsigned* abortw = (GAS unsigned*)(a.sync + SY_ABORT);
  bool dead = false;
  Wts W;
  Clk ck; ck.init();
  load_weights<1>(a, h, w, lane, smem, W);
  if (!G.pass(a.sync)) return;
  PZERO;
  const Sel sel = sel_wait(a.sync, abortw, lane, dead, true);
  const __amdgpu_buffer_rsrc_t rh = rsrc_hot(a.sync, sel), rum = rsrc_u(a.sync);
  u32x16 C, P;
  {
    const int r = lane & 15, g = lane >> 4;
    const unsigned l0 = (unsigned)(uintptr_t)(LAS char*)smem;
#pragma unroll
    for (int i = 0; i < 16; ++i) C[i] = H_TR + copy_q((lane >> 3) & 3, h >> 3, sel) + (lane & 7) * 128;
    C[0] = lane * 16; C[1] = l0 + L_MEM + lane * 16; C[2] = l0 + L_WTS + w * 4096 + lane * 16; C[3] = C[2] + 65536;
    C[4] = (lane & 2) ? 0xEEEE : 0x4444;
    C[5] = l0 + L_XCH + (32 * w + r) * 16 + g * 4;
    C[6] = (lane & 8) ? 0xEEEE : 0x4444;
    C[7] = l0 + L_XCH + (32 * (g >> 1) + 8 * (g & 1) + (r >> 2)) * 16 + (r & 1) * 8;
    C[8] = (lane >> 5) * 8 * CHK + (lane & 31) * 16;
    C[11] = l0 + L_PRF + w * 1024 + (lane >> 6);
    asm volatile("" : "+v"(C));
  }
  SSTAMP(128 + h);
  steps_init(P, h, w, um_place(8 * (h & 3) + 2 * w, h >> 3) + (h & 7) * 512, sel);
  unsigned spins = 0;
  while (true) {
    u32x8 X; u32x16 pv; (void)pv;
    steps_enter(P, ck, dead);
    STEPS(VO_BODY, , [um] "s"(sel[1] >> 8));
    keep_a(W);
    lane = opaque_tid(0);
    if (__builtin_amdgcn_readfirstlane(P[2]) & 64u) break;
    spins |= 63u;
    if (dead || poll_fail(spins, abortw, lane)) dead = true;
  }
  PDUMP(0);
}

__device__ __forceinline__ int timed(__amdgpu_buffer_rsrc_t rs, unsigned at) {
  u64 c0, c1;
  asm volatile("s_waitcnt vmcnt(0)\n\ts_memtime %0\n\ts_waitcnt lgkmcnt(0)" : "=s"(c0) :: "memory");
  unsigned v = __builtin_amdgcn_raw_buffer_load_b32(rs, 0, at, LD_AUX);
  asm volatile("s_waitcnt vmcnt(0)\n\ts_memtime %0\n\ts_waitcnt lgkmcnt(0)" : "=s"(c1), "+v"(v) :: "memory");
  const int t = (int)(c1 - c0);
  return t < 320 ? 4000 : t;
}
__device__ __forceinline__ unsigned calibrate(const ChainArgs& a, int cal, u64 t0) {
  const __amdgpu_buffer_rsrc_t rs = mk_rsrc((void*)a.sync, SY_SET);
  int best[2] = {4000, 4000};
  bool sure = false;
  for (int i = 0; i < 32 && !sure && __builtin_amdgcn_s_memrealtime() - t0 < CAL_A; ++i) {
#pragma unroll
    for (int hf = 0; hf < 2; ++hf) best[hf] = min(best[hf], timed(rs, cal * (PAGE + CHK) + 64 * CHK + hf * 8192 + ((5 * cal + i) & 31) * 256));
    sure = min(best[0], best[1]) < 540;
  }
  return ((best[1] < best[0] ? 1u : 0u) ^ die_half()) + (sure ? 2u : 0u);
}
__device__ __forceinline__ unsigned calibrate8(const ChainArgs& a, unsigned at, u64 t0) {
  const __amdgpu_buffer_rsrc_t rs = mk_rsrc((void*)a.sync, SY_SET);
  int hf = 0, t[4] = {4000, 4000, 4000, 4000};
  for (; hf < 32 && timed(rs, at + (hf & 3) * 8192 + 2048 + (((hf >> 2) + (at >> 15)) & 7) * 256) >= 540; ++hf) if (__builtin_amdgcn_s_memrealtime() - t0 >= CAL_T) hf = 32;
  bool ok = false;
  for (int i = 0; i < 8 && hf < 32 && !ok && __builtin_amdgcn_s_memrealtime() - t0 < CAL_T; ++i) {
#pragma unroll
    for (int k = 0; k < 4; ++k) t[k] = min(t[k], timed(rs, at + (hf & 1) * 8192 + quarter(k) + ((i + (at >> 15)) & 7) * 256));
    ok = i && max(max(t[0], t[1]), max(t[2], t[3])) < 540;
  }
  const unsigned xc = (unsigned)__builtin_amdgcn_s_getreg(20 | (3 << 11));
  const int d12 = min(max(t[0] - t[1], -30), 30) + min(max(t[2] - t[3], -30), 30), d14 = min(max(t[0] - t[2], -30), 30) + min(max(t[1] - t[3], -30), 30);
  ok = ok && !(xc & 4);
  return ((hf & 1) ^ die_half()) | (ok ? 2u : 0u) | (64 + (ok ? ((xc & 2) ? -d12 : d12) : 0)) << 8 | (64 + (ok ? ((xc & 1) ? -d14 : d14) : 0)) << 16;
}
__device__ __forceinline__ void bg_drain() {
  asm volatile("s_waitcnt vmcnt(0)" ::: "memory");
  __builtin_amdgcn_s_barrier();
  asm volatile("" ::: "memory");
}

constexpr int LC_SUM = 65536, LC_TQ = LC_SUM + 8192, LC_OUT = 0, LC_PRF = 98304;
__device__ __forceinline__ void role_cb(const ChainArgs& a, int ci, int par, char* smem, const int w, Gate& G) {
  int lane = opaque_tid(0);
  const int b = 64 + 32 * par + ci, j = ci >> 3; (void)b;
  u64* prof = a.prof; (void)prof;
  PDECL(LC_PRF, 8);
  GAS unsigned* abortw = (GAS unsigned*)(a.sync + SY_ABORT);
  bool dead = false;
  const unsigned l0 = (unsigned)(uintptr_t)(LAS char*)smem;
  const int tq = 2 * ci + (w & 1);
  auto fine = [](int n) { return (n % 3 == 2 ? 32 : n % 3) * PAGE + (n / 3 + 1) * 32768; };
  unsigned m0 = 0, hs = 0;
  if (w == 1 || (w == 2 && !par)) {
    const unsigned x = __builtin_amdgcn_raw_buffer_load_b32(mk_rsrc((void*)a.sync, SY_SET), par ? (ci ^ 2) * (PAGE + CHK) + 64 * CHK + (lane & 1) * 8192 + (lane >> 1) * 256 : fine(ci ^ 2) + (lane & 7) * 4096 + (lane >> 3) * 256 + (w - 1) * 2048, 0, 0);
    asm volatile("" :: "v"(x));
  }
  if (w == 3 && !par) {
    const unsigned x = __builtin_amdgcn_raw_buffer_load_b32(mk_rsrc((void*)a.X, 4u << 20), (ci * 64 + lane) * 128, 0, 0);
    asm volatile("" :: "v"(x));
  }
  if (w == 0) hs = par ? calibrate8(a, fine(ci), G.entry) : calibrate(a, ci, G.entry);
  __builtin_amdgcn_s_barrier();
  m0 = nosent(*(const unsigned*)(a.mem0 + (2 * (lane >> 4) + (lane & 1)) * DM + 16 * tq + (lane & 14)));
  Clk ck; ck.init();
  if (!G.pass(a.sync)) return;
  SSTAMP(0 + ci);
  if ((w | lane) == 0) __hip_atomic_store(ctr(a, CT_CAL + b - 64), hs, __ATOMIC_RELAXED, __HIP_MEMORY_SCOPE_AGENT);
  auto init = [&](Sel sl) {
    const unsigned s1v = ~(unsigned)(lane >> 6);
    const u32x4 sv = {s1v, s1v, s1v, s1v};
#pragma unroll
    for (int i = 0; i < INI; ++i) st16(rsrc_hot(a.sync, sl), (ci + 16 * (i & 6)) * CHK + w * 2048 + (i & 1) * 1024 + lane * 16, sv);
#pragma unroll
    for (int i = 0; i < 8; ++i) st16(rsrc_u(a.sync), (4 * ci + w) * CHK + (sl[0] & 1u) * 8192 + i * 1024 + lane * 16, sv);
    bg_drain();
    if (w == 0 && lane == 0) ctr_add(a, CT_INIT, 1u);
  };
  int told = 0;
  if (b == 65 && w == 3) {
    unsigned spins = 0, v;
    int d;
    auto cnt = [](bool x) { return __builtin_popcountll(__builtin_amdgcn_ballot_w64(x)); };
    while (true) {
      v = __hip_atomic_load(ctr(a, CT_CAL + lane), __ATOMIC_RELAXED, __HIP_MEMORY_SCOPE_AGENT);
      const unsigned u = v == SENT ? 0u : (v & 3u) ^ (lane < 32 ? lane & 1 : lane % 3 == 0), f = lane >= 32 && v != SENT && (v & 2u) ? lane % 3 : 3;
      const int n0 = cnt(u == 2u), n1 = cnt(u == 3u);
      d = max(n0, n1) >= 8 + 4 * min(n0, n1) ? n1 - n0 : 0;
      if (d && !told) {
        told = d;
        const u64 m = __builtin_amdgcn_ballot_w64(((lane ^ (d > 0) ^ die_half()) & 1u) != 0 && lane < 33) | 1ull << 62;
        if (lane == 0) __hip_atomic_store((GAS u64*)(a.sync + SY_SEL), m, __ATOMIC_RELAXED, __HIP_MEMORY_SCOPE_AGENT);
        init((Sel){(unsigned)m, (unsigned)(m >> 32)});
      }
      d = told;
      if ((d && min(min(cnt(f == 0), cnt(f == 1)), cnt(f == 2)) >= CAL_N) || __all(v != SENT) || poll_fail(spins, abortw, lane)) break;
      __builtin_amdgcn_s_sleep(4);
    }
    if (v == SENT) v = 0x404000u;
    unsigned key[3] = {};
    int vote[3][2] = {};
    for (int n = 0; n < 32; ++n) {
      const unsigned x = __builtin_amdgcn_readlane(v, 32 + n);
#pragma unroll
      for (int k = 0; k < 3; ++k) if (n % 3 == k) { vote[k][0] += (int)((x >> 8) & 255u) - 64; vote[k][1] += (int)((x >> 16) & 255u) - 64; }
    }
#pragma unroll
    for (int k = 0; k < 3; ++k) key[k] = ((vote[k][0] > 0) + 2 * (vote[k][1] > 0)) ^ ((KEYX >> (2 * k)) & 3);
    const int h32 = cnt((v & 3) == 3 && lane >= 32 && lane % 3 == 1) - cnt((v & 3) == 2 && lane >= 32 && lane % 3 == 1);
    if (lane == 32) v = h32 > 0;
    if (d) v = lane ^ (d > 0);
    u64 m = __builtin_amdgcn_ballot_w64(((v ^ die_half()) & 1u) != 0 && lane < 33);
    m |= (u64)key[2] << 36;
#pragma unroll
    for (int t = 0; t < 4; ++t) m |= (u64)(((key[t >> 1] ^ t) & 1u) | ((m >> (t >> 1)) & 1u) << 1 | (key[t >> 1] & 2u) << 1) << (40 + 4 * t);
    if (lane == 0) __hip_atomic_store((GAS u64*)(a.sync + SY_SEL), m, __ATOMIC_RELAXED, __HIP_MEMORY_SCOPE_AGENT);
  }
  if (!par && !told) init(sel_wait(a.sync, abortw, lane, dead));
  if (w == 0) sel_wait(a.sync, abortw, lane, dead, true);
  lds_barrier();
  const Sel sel = sel_wait(a.sync, abortw, lane, dead, true);
  const __amdgpu_buffer_rsrc_t rh = rsrc_hot(a.sync, sel), rum = rsrc_u(a.sync), ro = mk_rsrc(a.out, 8u << 20);
  const unsigned cq = hot_q((ci >> 1) & 3, sel), wq = copy_q(w, (ci >> 1) & 3, sel);
  if (!par && w < 2) st4(rh, H_MEM + cq + j * 1024 + (tq & 3) * 256 + (4 * (lane >> 4) + (lane & 3)) * 16 + ((lane >> 2) & 3) * 4, m0);
  PZERO;
  SSTAMP(32 + ci);
  u32x16 C, P; u32x8 C2;
  {
    const int i = lane & 15, g = lane >> 4;
    const unsigned mo = H_MEM + cq + j * 1024 + (ci & 1) * 512 + w * 128;
    C[0] = lane * 16; C[1] = (lane >> 5) * 2048 + (lane & 31) * 16;
    C[2] = ((i >> 3) ^ (j & 1)) * 512 + (i & 4) * 64 + g * 64 + (i & 3) * 16;
    C[3] = H_PZ + wq + (((g & 2) + (i >> 3) + 4 * (g & 1)) ^ j) * 256 + (i & 4) * 32 + (i & 3) * 16;
    C[4] = H_PZ + wq + ((g + 4 * (i >> 3)) ^ j) * 256 + (i & 4) * 32 + 64 + (i & 3) * 16;
    C[5] = (lane & 8) ? 0xEEEE : 0x4444; C[6] = (lane & 2) ? 0xEEEE : 0x4444;
    C[7] = l0 + LC_SUM + w * 1024 + lane * 4;
    C[8] = l0 + LC_SUM + (w >> 1) * 4096 + (2 * (w & 1) + (lane >> 5)) * 256 + ((2 * (lane & 1) + ((lane >> 3) & 1)) * 16 + 2 * (lane & 6) + 2 * ((lane >> 4) & 1)) * 4;
    C[9] = (lane & 1) ? OOR : mo + lane * 2;
    C[10] = H_ANN + wq + (lane & 7) * 128;
    C[11] = ((ci & 3) == w && lane < 4) ? H_TR + ((ci >> 1) & 3) * 16 * CHK + quarter(lane) + (2 * j + (ci & 1)) * 128 : OOR;
    C[12] = H_TR + copy_q((lane >> 1) & 3, (ci >> 1) & 3, sel) + (2 * w + (lane & 1)) * 128; C[13] = (ci & 3) == w ? (lane & 31) * 128 : OOR;
    C[14] = (par | lane) == 0 && w == 1 ? SY_CT + (CT_PR + 96 + ci) * 128 : OOR;
    C[15] = l0 + LC_OUT + w * 4096 + (g * 16 + ((i + 4 * g) & 15)) * 16;
    C2[0] = l0 + LC_OUT + (((lane >> 1) & 2) + (w >> 1)) * 1024 + ((lane & 3) * 16 + ((8 * w + (lane >> 3) + 4 * (lane & 3)) & 15)) * 16;
    C2[1] = (8 * w + (lane >> 3)) * OUTW * 2 + DM * 2 + ci * 64 + (lane & 7) * 8;
    C2[2] = SY_PU + (sel[0] & 2u) * 4096 + (8 * w + (lane & 7)) * 128;
    C2[3] = lane < 8 ? mo + lane * 16 : OOR;
    C2[4] = (w | lane) == 0 ? ci * 128 : OOR;
    C2[5] = l0 + LC_TQ + 4 * w + (lane >> 6); C2[6] = 20000 + (lane >> 6); C2[7] = l0 + LC_TQ + (lane >> 6);
  }
#pragma unroll
  for (int i = 0; i < 16; ++i) P[i] = 0;
  P[0] = um_place(ci, w) + j * 512; P[1] = H_P + wq + (j & 2) * 512; P[8] = um_at(par + 2, P[0], sel);
  P[2] = (8 * w + j) * UXH + ux_tile(2 * ci); P[3] = H_PX + 2 * w * CHK + j * 2048; P[4] = par - 2; P[5] = P[7] = KD0 * TU;
  P[6] = ck.c2t << 15; P[11] = ck.ce; P[12] = ck.t2c; P[13] = ck.te; P[15] = H_DLT + par * 4096;
#pragma unroll
  for (int i = 0; i < 16; ++i) P[i] = __builtin_amdgcn_readfirstlane(P[i]);
  u32x16 pv; (void)pv;
  const unsigned C24 = H_FLG + (8 * w + (lane & 7)) * 128;
  asm volatile(CB_BODY : "+&{s[40:55]}"(P) STEP_PV, "+&{v[224:239]}"(C), "+&{v[240:247]}"(C2) : "s"(0) CB_PV, [rh] "s"(rh), [ru] "s"(rum), [ro] "s"(ro), [sel] "s"(sel[0]), [um] "s"(sel[1] >> 8), [mg] "s"(H_FLG + 4096), [m1] "s"((ci & 25) ? 0x7f000000 : H_FLG + 4096), [m2] "s"(ci ? 0x7f000000 : H_FLG + 4096)
               : "memory", "vcc", "scc", S22, V90, V10(9), V10(10), V10(11), V10(12), V10(13), V10(14), V10(15), V10(16), V10(17), V10(18), V10(19), V10(20), V10(21),
                 "v220", "v221", "v222", "v223", "s98", "s99", "s100", "s101", A10(), A10(1), A10(2), A10(3), A10(4), A10(5), "a60", "a61", "a62", "a63");
  lane = opaque_tid(0);
  PDUMP(2);
}

__device__ __forceinline__ void st16g(u16* p, u32x4 v, bool sc1) {
  if (sc1) asm volatile("global_store_dwordx4 %0, %1, off " OUTP "\n\ts_nop 1" :: "v"(p), "v"(v) : "memory");
  else *(u32x4*)p = v;
}
__device__ __forceinline__ u32x4 pk8(f32x4 a, f32x4 b) { return (u32x4){pk2(a[0], a[1]), pk2(a[2], a[3]), pk2(b[0], b[1]), pk2(b[2], b[3])}; }
__device__ __forceinline__ void role_p(const ChainArgs& a, int kind, int h, char* smem, const int w, Gate& G) {
  const int lane = opaque_tid(0), r = lane & 15, g = lane >> 4;
  const u32x4 rw = own_rsrc(kind == 0 ? a.Wq : (kind == 1 ? a.Wk : a.Wv), 8u << 20), rx = own_rsrc(a.X, 4u << 20);
  const unsigned lds0 = (unsigned)(uintptr_t)(LAS char*)smem + w * 4096;
  unsigned vw[4], vx[4];
#pragma unroll
  for (int i = 0; i < 4; ++i) {
    const int row = (w * 4 + i) * 4 + g, xr = 8 * w + 2 * i + (lane >> 5);
    vw[i] = row * HD * 2 + ((r ^ swz_b(row)) * 16);
    vx[i] = xr * DM * 2 + (((lane & 31) ^ (xr & 15)) * 16);
  }
  auto dma = [&](u32x4 rs, unsigned v, unsigned so, unsigned ld, bool nt = false) {
    if (nt) asm volatile("s_mov_b32 m0, %0\n\ts_nop 3\n\tbuffer_load_dwordx4 %1, %2, %3 offen nt lds" :: "s"(ld), "v"(v), "s"(rs), "s"(so) : "m0", "memory");
    else asm volatile("s_mov_b32 m0, %0\n\ts_nop 3\n\tbuffer_load_dwordx4 %1, %2, %3 offen lds" :: "s"(ld), "v"(v), "s"(rs), "s"(so) : "m0", "memory");
  };
  constexpr int SL = 16384 + MS * 4096, NS = CHAIN_LDS / SL, NI = 4 + MS;
  const unsigned vy = (lane >> 3) * DM * 2 + (((lane & 7) ^ (lane >> 4) ^ ((w & 1) * 4)) * 16);
  auto item = [&](int t) {
#pragma unroll
    for (int i = 0; i < 4; ++i) dma(rw, vw[i], (unsigned)((64 * (t ^ PREV) * HD + 128 * h) * 2), lds0 + (t % NS) * SL + i * 1024, (NTW >> 2) & 1);
#pragma unroll
    for (int i = 0; i < MS; ++i) dma(rx, vy, (unsigned)((32 * i + 8 * w) * DM * 2 + (t ^ PREV) * 128), lds0 - w * 3072 + (t % NS) * SL + 16384 + i * 4096);
  };
  Wts W;
#pragma unroll
  for (int i = 0; i < 32; ++i) asm volatile("" : "=a"(W.a[i]));
  if (kind < 2) while (__builtin_amdgcn_s_memrealtime() - G.entry < (kind ? K_DLY : Q_DLY)) __builtin_amdgcn_s_sleep(8);
#pragma unroll
  for (int t = 0; t < NS; ++t) item(t);
  f32x4 acc[2 * MS][2];
  u64 ep = 0;
#pragma unroll
  for (int t = 0; t < 16; ++t) {
    asm volatile("s_waitcnt vmcnt(%0)" :: "n"(t == 0 ? NI * (NS - 1) : (t < 17 - NS ? NI * (NS - 2) : NI * (15 - t) + 1)) : "memory");
    lds_barrier();
    if (t && t < 17 - NS) item(t + NS - 1);
    if (t == 16 - NS) ep = __hip_atomic_load((GAS u64*)(a.sync + SY_EPOCH), __ATOMIC_RELAXED, __HIP_MEMORY_SCOPE_AGENT);
    u32x4 f[4];
#pragma unroll
    for (int n = 0; n < 8; ++n) {
      const int row = 32 * (n >> 2) + 8 * g + 4 * (n & 1) + (r >> 2);
      const u32x2 u = __builtin_bit_cast(u32x2, __builtin_amdgcn_ds_read_tr16_b64_v4i16((LAS bf16x4*)(smem + (t % NS) * SL + row * 256 + (((4 * w + (r & 3)) ^ swz_b(row)) * 16) + 4 * (n & 2))));
      f[n >> 1][2 * (n & 1)] = u[0]; f[n >> 1][2 * (n & 1) + 1] = u[1];
    }
    u32x4 x[4 * MS];
#pragma unroll
    for (int q = 0; q < 4 * MS; ++q) x[q] = *(const u32x4*)(smem + (t % NS) * SL + 16384 + (16 * (q >> 1) + r) * 128 + (((4 * (q & 1) + g) ^ (r >> 1)) * 16));
    SB0;
#pragma unroll
    for (int q = 0; q < 16; ++q) asm volatile("v_accvgpr_write_b32 a[%1], %0" :: "v"(f[q >> 2][q & 3]), "n"(16 * (t ^ PREV) + q));
#pragma unroll
    for (int q = 0; q < 8 * MS; ++q) {
      if (t == 0 && !(q & 2)) asm volatile("v_mfma_f32_16x16x32_bf16 %0, %1, %2, 0" : "=&v"(acc[q >> 2][q & 1]) : "v"(f[q & 3]), "v"(x[q >> 1]));
      else asm volatile("v_mfma_f32_16x16x32_bf16 %0, %1, %2, %0" : "+v"(acc[q >> 2][q & 1]) : "v"(f[q & 3]), "v"(x[q >> 1]));
    }
  }
  asm volatile("s_nop 7" : "+v"(acc[2 * MS - 1][0]), "+v"(acc[2 * MS - 1][1]));
  SSTAMP(160 + h);
  if (!G.look(a.sync, ep)) { G.arrive(a.sync); return; }
  const __amdgpu_buffer_rsrc_t rs = rsrc_u(a.sync);
#pragma unroll
  for (int q = 0; q < 2 * MS; ++q) st16s(pk8(acc[q][0], acc[q][1]), rs, lane * 16 + (q & 1) * 4096, x_base(kind) + x_first(kind) + ((q >> 1) * NH + h) * CHK + w * 1024);
  lds_barrier();
#pragma unroll
  for (int t = 0; t < 32; ++t) {
    dma(rx, vx[t & 3], (unsigned)((MS + (t >> 4)) * 65536 + ((t >> 2) & 3) * 512), lds0 + (t >> 2) * 16384 + (t & 3) * 1024);
    if (t == 11) {
      wait_vm<12>();
      __builtin_amdgcn_s_barrier();
      st4(rs, (w | lane) == 0 ? SY_CT + (CT_PR + 32 * kind + h) * 128 : OOR, MS - 1);
    }
  }
  G.arrive(a.sync);
  bool dead = false;
  const Sel sel = sel_wait(a.sync, (GAS unsigned*)(a.sync + SY_ABORT), lane, dead);
  wait_vm<0>();
  lds_barrier();
  u32x16 C;
#pragma unroll
  for (int i = 0; i < 4; ++i) { C[i] = vx[i]; C[4 + i] = lds0 - w * 4096 + r * 512 + (((4 * i + g) ^ r) * 16); C[12 + i] = 0; }
  C[8] = lane * 16; C[9] = lane * 16 + 4096;
  C[10] = 0;
  C[11] = (w | lane) == 0 ? SY_CT + (CT_PR + 32 * kind + h) * 128 : OOR;
  u32x8 P = {(MS + 2) * 65536u, lds0, 0x7f000000u, x_base(kind) + (MS * NH + h) * CHK + w * 1024, MS, 0u, MS - 1u, 0u};
#pragma unroll
  for (int i = 0; i < 8; ++i) P[i] = __builtin_amdgcn_readfirstlane(P[i]);
  asm volatile(P_BODY : "+&{s[40:47]}"(P), "+&{v[160:175]}"(C) : [rx] "s"(rx), [ru] "s"(rs), [wv] "s"(w), [hm] "s"(x_half(kind, sel)), [d0] "s"(x_first(kind)), [pp] "s"(a.prof + 296000 + (32 * kind + h) * 16), [pq] "s"(a.prof + 300000 + (32 * kind + h) * 48)
               : "memory", "vcc", "scc", "m0", S22, STEP_CL V90, V10(9), V10(10), V10(11), V10(12), V10(13), V10(14), V10(15), "v176", "v177", "v178", "v179", "v180");
  keep_a(W);
}
#define BG_SLP 64
#define A2N 2
constexpr int A2_NCH = A2N;
__device__ __forceinline__ float blo(unsigned d) { return __uint_as_float(d << 16); }
__device__ __forceinline__ float bhi(unsigned d) { return __uint_as_float(d & 0xffff0000u); }
constexpr int A2_CSTR = 4160;
constexpr int A2_UOFF = 8 * A2_CSTR + 16 * 80;
constexpr int A2_WLDS = A2_UOFF + 64 * 80;
static_assert(4 * A2_WLDS + 256 <= CHAIN_LDS - 16, "lds");
__device__ __forceinline__ unsigned kv_row(int d, Sel sel) { return far_blk(d >> 1, sel) + (d & 1) * 4096; }
__device__ __forceinline__ void a2_wave(const ChainArgs& a, int d0, int d1, char* smem, int w, Sel sel) {
  const int lane = opaque_tid(0);
  char* Wb = smem + w * A2_WLDS;
  char* U = Wb + A2_UOFF;
  const __amdgpu_buffer_rsrc_t rf = mk_rsrc((void*)a.Assm, 4u << 20);
  const __amdgpu_buffer_rsrc_t rkv = mk_rsrc((void*)a.kvT, 8u << 20);
  u32x4 hi[4], lo[4], uv[4];
  auto ld_h = [&](int d) {
#pragma unroll
    for (int i = 0; i < 4; ++i) {
      hi[i] = __builtin_amdgcn_raw_buffer_load_b128(rf, (unsigned)((255 - 64 * i - lane) * 16), (unsigned)(d * 4096), 2);
      lo[i] = __builtin_amdgcn_raw_buffer_load_b128(rf, (unsigned)(max(254 - 64 * i - lane, 0) * 16), (unsigned)(d * 4096), 2);
    }
  };
  auto ld_u = [&](int d) {
#pragma unroll
    for (int i = 0; i < 4; ++i) uv[i] = __builtin_amdgcn_raw_buffer_load_b128(rkv, (unsigned)((i * 64 + lane) * 16), kv_row(d, sel), LD_AUX);
  };
  {
    GAS unsigned* abortw = (GAS unsigned*)(a.sync + SY_ABORT);
    bool dead = false;
    ctr_wait<BG_SLP>(ctr(a, CT_A1 + (min(d0, D_A - 1) >> 7)), d0 < d1 ? 8u : 0u, abortw, lane, dead);
  }
  ld_h(d0); ld_u(d0);
  if (lane < 24) *(f32x4*)(Wb + (lane / 3) * A2_CSTR + (256 + lane % 3) * 16) = vzero();
#pragma unroll
  for (int i = 0; i < 2; ++i) if (64 * i + lane < 80) *(f32x4*)(U - 16 * 80 + (64 * i + lane) * 16) = vzero();
  const int r = lane & 15, g = lane >> 4;
#pragma unroll 1
  for (int d = d0; d < d1; ++d) {
#pragma unroll
    for (int i = 0; i < 4; ++i) {
      u32x4 l = lo[i];
      if (i == 3 && lane == 63) l = (u32x4){0u, 0u, 0u, 0u};
      const unsigned src[8] = {l[0], l[1], l[2], l[3], hi[i][0], hi[i][1], hi[i][2], hi[i][3]};
      unsigned f[15];
#pragma unroll
      for (int x = 0; x < 15; ++x) {
        if ((x & 1) == 0) f[x] = __builtin_amdgcn_alignbit(src[7 - x / 2], src[7 - x / 2], 16);
        else f[x] = __builtin_amdgcn_perm(src[7 - (x + 1) / 2], src[7 - (x - 1) / 2], 0x07060100u);
      }
#pragma unroll
      for (int m = 0; m < 8; ++m) *(u32x4*)(Wb + m * A2_CSTR + (64 * i + lane) * 16) = (u32x4){f[7 - m], f[9 - m], f[11 - m], f[13 - m]};
    }
#pragma unroll
    for (int i = 0; i < 4; ++i) {
      const int ch = i * 64 + lane;
      *(u32x4*)(U + (ch >> 2) * 80 + (ch & 3) * 16) = uv[i];
    }
    const float Dd = bf2f(a.AD[d]);
    asm volatile("" ::: "memory");
    ld_h(min(d + 1, d1 - 1)); ld_u(min(d + 1, d1 - 1));
    f32x4 acc[4][2];
#pragma unroll
    for (int i = 0; i < 8; ++i) acc[i >> 1][i & 1] = vzero();
    const char* pa = Wb + (r & 7) * A2_CSTR + 2 * (2040 - (r & 8) + 8 * g) - 3 * 1024 - 32 - 64;
    const char* pb = U + (r - 1) * 80 + 16 * g;
    bf16x8 fa[2][8], fb[2][4];
    auto rd = [&](int b, int n, int k) {
      if (n < 4) fb[b][n] = *(const bf16x8*)(pb + 80 * (16 * n + 1 - k));
      else fa[b][n - 4] = *(const bf16x8*)(pa + 1024 * (3 - ((n - 4) >> 1)) + 32 * (1 - (n & 1)) + 64 * (1 - k));
    };
#pragma unroll
    for (int n = 0; n < 12; ++n) rd(0, n, 0);
#pragma unroll 1
    for (int k = 0; k < 16; k += 2) {
#pragma unroll
      for (int b = 0; b < 2; ++b) {
        int q = 0;
#pragma unroll
        for (int j = 0; j < 4; ++j)
#pragma unroll
          for (int i = j; i < 4; ++i)
#pragma unroll
            for (int mt = 0; mt < 2; ++mt) {
              asm volatile("v_mfma_f32_16x16x32_bf16 %0, %1, %2, %0" : "+v"(acc[i][mt]) : "v"(fa[b][2 * j + mt]), "v"(fb[b][i - j]));
              SB0;
              if (q < 12) rd(b ^ 1, q, 1);
              if (q == 12) { pa -= 64; pb -= 80; }
              ++q;
              SB0;
            }
      }
    }
    asm volatile("s_nop 7" ::: "memory");
#pragma unroll
    for (int i = 0; i < 8; ++i) asm volatile("" : "+v"(acc[i >> 1][i & 1]));
#pragma unroll
    for (int i = 0; i < 8; ++i) {
      char* pz = U + (16 * (i >> 1) + r) * 80 + 32 * (i & 1) + 8 * g;
      const u32x2 kvv = *(const u32x2*)pz;
      const f32x4 y = acc[i >> 1][i & 1];
      *(u32x2*)pz = (u32x2){pk2(fmaf(blo(kvv[0]), Dd, y[0]), fmaf(bhi(kvv[0]), Dd, y[1])), pk2(fmaf(blo(kvv[1]), Dd, y[2]), fmaf(bhi(kvv[1]), Dd, y[3]))};
    }
#pragma unroll
    for (int i = 0; i < 4; ++i) {
      const int ch = i * 64 + lane;
      st16s(*(const u32x4*)(U + (ch >> 2) * 80 + (ch & 3) * 16), rkv, (unsigned)(ch * 16), kv_row(d, sel));
    }
  }
  wait_vm<0>();
  if (lane == 0 && d0 < d1) ctr_add(a, CT_A2 + (d0 >> 7), d1 - d0);
}

__device__ __forceinline__ void task_a1(const ChainArgs& a, int dp, int tq, char* smem, int wv, Sel sel) {
  const int lane = opaque_tid(0);
  const int dg = 2 * dp + (wv & 1), t0 = tq * 256 + (wv >> 1) * 128;
  const int d = dg * 64 + 8 * (lane & 7);
  const int tb = t0 + 8 * (lane >> 3);
  char* T = smem + wv * 16384;
  u32x4 kk[2][11], vv[2][8], sh[4];
#pragma unroll
  for (int i = 0; i < 4; ++i) sh[i] = *(const u32x4*)(a.Ashift + d * 4 + 8 * i);
  SB0;
#pragma unroll
  for (int gq = 0; gq < 2; ++gq) {
#pragma unroll
    for (int i = 0; i < 11; ++i) {
      const int t = tb + 64 * gq - 3 + i;
      kk[gq][i] = LDNT((const u32x4*)(a.Ak + (long)(t < 0 ? 0 : t) * D_A + d));
    }
#pragma unroll
    for (int i = 0; i < 8; ++i) vv[gq][i] = LDNT((const u32x4*)(a.Av + (long)(tb + 64 * gq + i) * D_A + d));
    SB0;
  }
#pragma unroll
  for (int i = 0; i < 3; ++i) if (tb == 0) kk[0][i] = (u32x4){0u, 0u, 0u, 0u};
#pragma unroll
  for (int gq = 0; gq < 2; ++gq) {
#pragma unroll
    for (int c = 0; c < 8; ++c) {
      const unsigned t01 = sh[c >> 1][2 * (c & 1)], t23 = sh[c >> 1][2 * (c & 1) + 1];
      const float s0 = blo(t01), s1 = bhi(t01), s2 = blo(t23), s3 = bhi(t23);
      float kf[11];
#pragma unroll
      for (int i = 0; i < 11; ++i) kf[i] = (c & 1) ? bhi(kk[gq][i][c >> 1]) : blo(kk[gq][i][c >> 1]);
      u32x4 o;
#pragma unroll
      for (int i = 0; i < 8; i += 2) {
        float r[2];
#pragma unroll
        for (int j = 0; j < 2; ++j)
          r[j] = fmaf(kf[i + j + 3], s0, fmaf(kf[i + j + 2], s1, fmaf(kf[i + j + 1], s2, kf[i + j] * s3))) * ((c & 1) ? bhi(vv[gq][i + j][c >> 1]) : blo(vv[gq][i + j][c >> 1]));
        o[i >> 1] = pk2(r[0], r[1]);
      }
      *(u32x4*)(T + (8 * (lane & 7) + c) * 256 + (((gq * 8 + (lane >> 3)) ^ (lane & 7)) * 16)) = o;
    }
    SB0;
  }
  const __amdgpu_buffer_rsrc_t rkv = mk_rsrc((void*)a.kvT, 8u << 20);
#pragma unroll
  for (int i = 0; i < 16; ++i) {
    const int row = 4 * i + (lane >> 4);
    const u32x4 o = *(const u32x4*)(T + row * 256 + (((lane & 15) ^ (row >> 3)) * 16));
    st16(rkv, kv_row(dg * 64 + row, sel) + (t0 + (lane & 15) * 8) * 2, o);
  }
  bg_drain();
  if (opaque_tid(wv) == 0) ctr_add(a, CT_A1 + dp, 1u);
}

__device__ __forceinline__ void task_a3(const ChainArgs& a, int dp, int tq, char* smem, int wv, Sel sel) {
  const int lane = opaque_tid(0);
  const int dg = 2 * dp + (wv & 1), t0 = tq * 256 + (wv >> 1) * 128;
  const int d = dg * 64 + 8 * (lane & 7);
  const int tb = t0 + 8 * (lane >> 3);
  const __amdgpu_buffer_rsrc_t rz = mk_rsrc((void*)a.kvT, 8u << 20);
  u32x4 qq[16], zz[8][2];
#pragma unroll
  for (int i = 0; i < 16; ++i) qq[i] = LDNT((const u32x4*)(a.Aq + (long)(tb + 64 * (i >> 3) + (i & 7)) * D_A + d));
  char* T = smem + wv * 16384;
  {
    u32x4 zr[16];
    bool dead = false;
    ctr_wait<BG_SLP>(ctr(a, CT_A2 + dp), 128u, (GAS unsigned*)(a.sync + SY_ABORT), lane, dead);
#pragma unroll
    for (int i = 0; i < 16; ++i) zr[i] = ld16(rz, kv_row(dg * 64 + 4 * i + (lane >> 4), sel) + (t0 + (lane & 15) * 8) * 2);
#pragma unroll
    for (int i = 0; i < 16; ++i) {
      const int row = 4 * i + (lane >> 4);
      *(u32x4*)(T + row * 256 + (((lane & 15) ^ (row >> 3)) * 16)) = zr[i];
    }
  }
#pragma unroll
  for (int c = 0; c < 8; ++c)
#pragma unroll
    for (int gq = 0; gq < 2; ++gq) zz[c][gq] = *(const u32x4*)(T + (8 * (lane & 7) + c) * 256 + (((gq * 8 + (lane >> 3)) ^ (lane & 7)) * 16));
#pragma unroll
  for (int i = 0; i < 16; ++i) {
    u32x4 o;
#pragma unroll
    for (int c = 0; c < 8; c += 2) {
      const unsigned z0 = zz[c][i >> 3][(i >> 1) & 3], z1 = zz[c + 1][i >> 3][(i >> 1) & 3];
      o[c >> 1] = pk2(blo(qq[i][c >> 1]) * ((i & 1) ? bhi(z0) : blo(z0)), bhi(qq[i][c >> 1]) * ((i & 1) ? bhi(z1) : blo(z1)));
    }
    st16g(a.out + (long)(tb + 64 * (i >> 3) + (i & 7)) * OUTW + d, o, true);
  }
}

__device__ __forceinline__ void role_a(const ChainArgs& a, char* smem, int wv) {
  constexpr int N2 = 256 / A2_NCH;
  bool dead = false;
  const Sel sel = sel_wait(a.sync, (GAS unsigned*)(a.sync + SY_ABORT), opaque_tid(0), dead);
  while (true) {
    asm volatile("" : "+s"(wv));
    int toff = CHAIN_LDS - 16;
    asm volatile("" : "+v"(toff));
    volatile LAS int* tslot = (volatile LAS int*)(smem + toff);
    if (opaque_tid(wv) == 0) *tslot = (int)__hip_atomic_fetch_add(ctr(a, CT_QUEUE), 1u, __ATOMIC_RELAXED, __HIP_MEMORY_SCOPE_AGENT) + 1;
    __syncthreads();
    const int t = __builtin_amdgcn_readfirstlane(*tslot);
    __syncthreads();
    if (t >= 128 + N2) break;
    if (t < 64) task_a1(a, t >> 3, t & 7, smem, wv, sel);
    else if (t < 64 + N2) a2_wave(a, A2_NCH * (4 * (t - 64) + wv), A2_NCH * (4 * (t - 64) + wv + 1), smem, wv, sel);
    else task_a3(a, (t - 64 - N2) >> 3, t & 7, smem, wv, sel);
  }
}
__device__ __forceinline__ char* lds_base(char* smem, int off) { asm volatile("" : "+v"(off)); return smem + off; }
__device__ __forceinline__ void role_u(const ChainArgs& a, int h, char* smem, int w, Gate& G) {
  int lane = opaque_tid(0);
  GAS unsigned* abortw = (GAS unsigned*)(a.sync + SY_ABORT);
  bool dead = false;
  char* sb = smem + w * 32768;
  const __amdgpu_buffer_rsrc_t rwo = mk_rsrc(a.Wo, 8u << 20);
  const unsigned lds0 = (unsigned)(uintptr_t)(LAS char*)sb;
  unsigned wo4[4];
#pragma unroll
  for (int i = 0; i < 4; ++i) { const int row = i * 4 + (lane >> 4); wo4[i] = row * DM * 2 + (((lane & 15) ^ swz_b(row)) * 16); }
  asm volatile("s_nop 4");
  auto stage = [&](int p) {
#pragma unroll
    for (int i = 0; i < 16; ++i)
      dma16(rwo, wo4[i & 3], (unsigned)(((128 * h + 64 * (p & 1) + 16 * (i >> 2)) * DM + 256 * w + 128 * (p >> 1)) * 2), lds0 + (p & 1) * 16384 + i * 1024, (NTW >> 3) & 1);
  };
  __builtin_amdgcn_s_waitcnt(0x0F70);
  Wts W;
#pragma unroll
  for (int i = 0; i < 32; ++i) asm volatile("" : "=a"(W.a[i]));
  stage(0); stage(1);
#pragma unroll
  for (int p = 0; p < 4; ++p) {
    if (p < 3) wait_vm<16>(); else wait_vm<0>();
#pragma unroll
    for (int t = 0; t < 8; ++t) {
      const u32x8 f = tr_frag(sb + (p & 1) * 16384, t, lane);
#pragma unroll
      for (int i = 0; i < 8; ++i) asm volatile("v_accvgpr_write_b32 a[%1], %0" :: "v"(f[i]), "n"(16 * (8 * (p >> 1) + t) + 8 * (p & 1) + i));
    }
    asm volatile("s_waitcnt lgkmcnt(0)" ::: "memory");
    if (p + 2 < 4) stage(p + 2);
  }
  SSTAMP(192 + h);
  {
    while (__builtin_amdgcn_s_memrealtime() - G.entry < XS_T) __builtin_amdgcn_s_sleep(8);
    const __amdgpu_buffer_rsrc_t rx = mk_rsrc((void*)a.X, 4u << 20);
    unsigned x[4];
#pragma unroll
    for (int i = 0; i < 4; ++i) x[i] = __builtin_amdgcn_raw_buffer_load_b32(rx, (h * 256 + w * 64 + lane) * 128, i << 20, 0);
#pragma unroll
    for (int i = 0; i < 4; ++i) asm volatile("" :: "v"(x[i]));
  }
  if (!G.pass(a.sync)) return;
  const Sel sel = sel_wait(a.sync, abortw, lane, dead);
  const __amdgpu_buffer_rsrc_t ru = rsrc_u(a.sync);
  volatile LAS int* fl = (volatile LAS int*)(smem + 16384);
  int s = 0;
  while (true) {
    lane = opaque_tid(0);
    const unsigned lo = SY_CT + (CT_PR + 64 + 32 * (lane & 1) + h) * 128, sg = (w | lane) == 0 ? SY_PU + (sel[0] & 2u) * 4096 + h * 128 : OOR;
    bg_drain();
    st4(ru, sg, s - 1);
    if (s >= NSEG) break;
    if (w == 0) {
      unsigned spins = 0;
      int can;
      while (true) {
        const unsigned cv = ld4(ru, lane < 2 ? lo : OOR);
        can = min(__builtin_amdgcn_readlane(cv, 0), __builtin_amdgcn_readlane(cv, 1) + URING - 1);
        if (can >= min(s + (s < 4 ? U_AH : U_AH2), NSEG - 1)) break;
        if (s < 4) asm volatile("s_sleep 3" ::: "memory"); else asm volatile("s_sleep %0" :: "n"(U_SLP) : "memory");
        if (dead || poll_fail(spins, abortw, lane)) { dead = true; can = NSEG; break; }
      }
      fl[1] = can;
    }
    lds_barrier();
#pragma unroll
    for (int i = 0; i < 2; ++i) *(u32x4*)(smem + w * 2048 + i * 1024 + lane * 16) = ld16(ru, x_base(2) + (far_blk(s * NH + h, sel) & (s < 4 ? ~8192u : ~0u)) + w * 2048 + i * 1024 + lane * 16);
    u32x8 P = {(unsigned)s, (unsigned)fl[1], h * UXH + ux_tile(16) * w, x_base(2) + far_blk(h, sel) + w * 2048, 0x7f000000u, 0u, 0u, 0u};
#pragma unroll
    for (int i = 0; i < 8; ++i) P[i] = __builtin_amdgcn_readfirstlane(P[i]);
    const unsigned l0 = lds0 - w * 32768;
    u32x8 C = {(unsigned)lane * 16, l0 + lane * 16, l0 + w * 2048 + lane * 16, lo, sg, l0 + 16416 + 4 * w + (lane >> 6), l0 + 16416 + (lane >> 6), 0u};
    lds_barrier();
    asm volatile(U_BODY : "+&{s[40:47]}"(P), "+&{v[80:87]}"(C) : [ru] "s"(ru), [sel] "s"(sel[0]), [wv] "s"(w), [pp] "s"(a.prof + 297600 + h * 64 + 0)
                 : "memory", "vcc", "scc", "s76", "s77", "s78", "s79", "s80", "s81", V10(), V10(1), V10(2), V10(3), V10(4), V10(5), V10(6), V10(7));
    keep_a(W);
    s = __builtin_amdgcn_readfirstlane(P[0]);
  }
}

__global__ __launch_bounds__(256, 1) void chain_kernel(ChainArgs a) {
  extern __shared__ __attribute__((aligned(16))) char smem[];
  unsigned again = blockIdx.x;
  asm volatile("" : "+s"(again));
  const int b = again & 255;
  again >>= 8;
  {
    u32x4 rc;
    asm volatile("s_getpc_b64 s[60:61]\n\t.Lchain_here:\n\ts_sub_u32 s60, s60, .Lchain_here-_Z12chain_kernel9ChainArgs\n\ts_subb_u32 s61, s61, 0\n\ts_and_b32 s61, s61, 0xffff\n\t"
                 "s_mov_b32 s62, .Lchain_end-_Z12chain_kernel9ChainArgs\n\ts_mov_b32 s63, 0x20000\n\t"
                 "buffer_load_dwordx4 a[252:255], %1, s[60:63], 0 offen\n\tbuffer_load_dwordx4 a[252:255], %1, s[60:63], s63 offen"
                 : "={s[60:63]}"(rc) : "v"((b >> 3) * 4096 + threadIdx.x * 16) : "scc", "a252", "a253", "a254", "a255");
  }
  int w = __builtin_amdgcn_readfirstlane(threadIdx.x >> 6);
  Gate G;
  G.entry = __builtin_amdgcn_s_memrealtime(); G.open = again != 0; G.arr = w == 0 && !again ? b & 7 : -1;
  G.rot = (again ? again : (unsigned)__builtin_amdgcn_s_getreg(20 | (3 << 11)) - b + WRONG) & 7u;
  {
    volatile LAS unsigned* t = (volatile LAS unsigned*)smem;
    if (w == 0) { t[0] = (unsigned)G.entry; t[1] = (unsigned)(G.entry >> 32); }
    __syncthreads();
    G.entry = (u64)(unsigned)__builtin_amdgcn_readfirstlane(t[0]) | ((u64)(unsigned)__builtin_amdgcn_readfirstlane(t[1]) << 32);
    __syncthreads();
  }
  if (b == 0 && !again) {
    const int lane = opaque_tid(0);
    const __amdgpu_buffer_rsrc_t rs = mk_rsrc(a.sync + SY_ABORT, SY_BYTES - SY_ABORT);
    for (int i = w; i * 1024 < SY_BYTES - SY_ABORT; i += 4) st16(rs, i * 1024 + lane * 16, (u32x4){SENT, SENT, SENT, SENT});
    bg_drain();
    if (w == 0 && lane == 0) __hip_atomic_store((GAS u64*)(a.sync + SY_EPOCH), (__builtin_amdgcn_s_memrealtime() & ~7ull) | G.rot, __ATOMIC_RELAXED, __HIP_MEMORY_SCOPE_AGENT);
  }
  {
    const unsigned rot = G.rot;
    const int x = (b + rot) & 7, j = ((b >> 3) + 3) & 31, i = j >> 2, role = (x & 4) + (j & 3), q = 2 * (x & 1) + (x >> 1);
    if ((role == 3 || role == 4) && w == 2) {
      const int n = opaque_tid(0);
      const unsigned x0 = __builtin_amdgcn_raw_buffer_load_b32(mk_rsrc(a.sync, role == 3 ? 35 * PAGE : WS_USED), n * PAGE + 4096 + j * 128, 0, 2);
      const unsigned x1 = __builtin_amdgcn_raw_buffer_load_b32(mk_rsrc(a.out, 8u << 20), n * PAGE + 4096 + j * 128, 0, 2);
      asm volatile("" :: "v"(x0), "v"(x1));
    }
    if (role == 0) role_qk(a, 8 * q + i, smem, w, G);
    else if (role == 1) role_vo(a, 8 * q + i, smem, w, G);
    else if (role < 4) role_cb(a, 8 * (i >> 1) + 2 * q + (i & 1), role - 2, smem, w, G);
    else if (role < 7) role_p(a, role - 4, 8 * (x - 4) + i, smem, w, G);
    else role_u(a, 8 * (x - 4) + i, smem, w, G);
    if (G.rot != rot) {
      __syncthreads();
      asm volatile("s_getpc_b64 s[60:61]\n\t.Lchain_back:\n\ts_sub_u32 s60, s60, .Lchain_back-_Z12chain_kernel9ChainArgs\n\ts_subb_u32 s61, s61, 0\n\ts_setpc_b64 s[60:61]"
                   :: "{s[0:1]}"(__builtin_amdgcn_kernarg_segment_ptr()), "{s2}"(b | (8u | G.rot) << 8), "{v0}"(opaque_tid(w)) : "s60", "s61", "scc", "memory");
      __builtin_unreachable();
    }
  }
  if ((b + G.rot) & 4) role_a(a, smem, w);
  asm volatile(".Lchain_end:");
}

extern "C" void kernel_launch(void* const* d_in, const int* in_sizes, int n_in,
                              void* d_out, int out_size, void* d_ws, size_t ws_size,
                              hipStream_t stream) {
  char* ws = (char*)(((uintptr_t)d_ws + (2u << 20) - 1) & ~(uintptr_t)((2u << 20) - 1));
  hipFuncSetAttribute((const void*)chain_kernel, hipFuncAttributeMaxDynamicSharedMemorySize, CHAIN_LDS);
  ChainArgs ca;
  ca.prof = (u64*)((char*)d_ws + PROF_AT);
  ca.Aq = (const u16*)d_in[0]; ca.Ak = (const u16*)d_in[1]; ca.Av = (const u16*)d_in[2];
  ca.Ashift = (const u16*)d_in[3]; ca.Assm = (const u16*)d_in[4]; ca.AD = (const u16*)d_in[5];
  ca.X = (const u16*)d_in[6]; ca.mem0 = (const u16*)d_in[7];
  ca.Wq = (const u16*)d_in[8]; ca.Wk = (const u16*)d_in[9]; ca.Wv = (const u16*)d_in[10]; ca.Wo = (const u16*)d_in[11];
  ca.out = (u16*)d_out; ca.sync = ws; ca.kvT = (u16*)(ws + OFF_KVT);
  chain_kernel<<<256, 256, CHAIN_LDS, stream>>>(ca);
}
```
